```python
import torch

def _softmax(x, axis=-1):
    return torch.softmax(x, dim=axis)
def _gelu(x):
    return torch.nn.functional.gelu(x, approximate="tanh")
def _silu(x):
    return torch.nn.functional.silu(x)
def _sigmoid(x):
    return torch.sigmoid(x)
def _layernorm(x, axis=-1, eps=1e-5):
    m = x.mean(dim=axis, keepdim=True)
    v = x.var(dim=axis, keepdim=True, unbiased=False)
    return (x - m) / torch.sqrt(v + eps)
def _scatter_add(tgt, idx, src, axis=0):
    return tgt.clone().index_add_(axis, idx, src)
def _segment_sum(src, seg, n):
    out = torch.zeros((n,) + src.shape[1:], dtype=src.dtype)
    return out.index_add_(0, seg, src)
def _pad(x, pw):
    flat = tuple(v for pair in reversed(pw) for v in pair)
    return torch.nn.functional.pad(x, flat)

kernel_name = 'standalone__attention_interact_autoint__recsys'

def setup_inputs():

    _w = (lambda a: (lambda t: t.to(torch.bfloat16) if t.is_floating_point() else t)(torch.as_tensor(a)))
    B, F = 4096, 100
    D = max(2, (128 // 2) * 2)
    H = 2
    Dh = D // H
    return {
        "e": _w((lambda *s: torch.randn(*s, dtype=torch.bfloat16) * 0.05)(B, F, D).to(float)),
        "wq": _w((lambda *s: torch.randn(*s, dtype=torch.bfloat16) * 1.6)(H, D, Dh).to(float)),
        "wk": _w((lambda *s: torch.randn(*s, dtype=torch.bfloat16) * 1.6)(H, D, Dh).to(float)),
        "wv": _w((lambda *s: torch.randn(*s, dtype=torch.bfloat16) * 0.05)(H, D, Dh).to(float)),
        "wres": _w((lambda *s: torch.randn(*s, dtype=torch.bfloat16) * 0.05)(D, D).to(float)),
    }

def reference(e, wq, wk, wv, wres):
    H, D, Dh = wq.shape[0], wq.shape[1], wq.shape[2]
    B, F = e.shape[0], e.shape[1]
    q = torch.einsum('bfd,hdk->bhfk', e, wq)
    k = torch.einsum('bfd,hdk->bhfk', e, wk)
    v = torch.einsum('bfd,hdk->bhfk', e, wv)
    scale = float(Dh) ** -0.5
    scores = torch.einsum('bhfk,bhgk->bhfg', q, k) * scale
    attn = _softmax(scores, axis=-1)
    attn = (lambda x, dt: x.to(dt))(attn, v.dtype)
    ctx = torch.einsum('bhfg,bhgk->bhfk', attn, v)
    ctx = (lambda x, *d: x.permute(*d))(ctx, 0, 2, 1, 3).reshape(B, F, H * Dh)
    res = torch.einsum('bfd,de->bfe', e, wres)
    out = ctx + res
    mask = (lambda x, dt: x.to(dt))(out > 0, out.dtype)
    return out * mask
```

```cpp
#include <hip/hip_runtime.h>
#include <stdint.h>

__global__ __launch_bounds__(256, 1)
void autoint_kernel(const uint16_t* __restrict__ e, const uint16_t* __restrict__ wq,
                    const uint16_t* __restrict__ wk, const uint16_t* __restrict__ wv,
                    const uint16_t* __restrict__ wres, uint16_t* __restrict__ out) {
  __shared__ __attribute__((aligned(16))) uint8_t lds[163840];
  const int tid = threadIdx.x;
  asm volatile("s_getpc_b64 s[50:51]\n" "L_ai_pc:\n" "v_and_b32 v251, 0x1c0, %0\n" "v_lshlrev_b32 v250, 7, %0\n" "v_lshl_add_u32 v250, v251, 10, v250\n" "v_add_u32 v250, %1, v250\n" "v_min_u32 v250, L_ai_end-L_ai_pc, v250\n" "global_load_dword v237, v250, s[50:51]\n" :: "v"(tid), "s"(((blockIdx.x >> 3) & 3u) << 13) : "v237", "v250", "v251", "s50", "s51", "s52", "memory");
  const unsigned wave = __builtin_amdgcn_readfirstlane(tid >> 6);
  const unsigned g = blockIdx.x;
  const unsigned h = (g >> 3) & 1;
  const unsigned p = (g & 7) | ((g >> 4) << 3);
  const unsigned first_off = (32u * p + wave) * 25600u;
  const unsigned ldsb = __builtin_amdgcn_readfirstlane((unsigned)(size_t)lds);
#define LO(x) ((unsigned)(size_t)(x))
#define HI(x) ((unsigned)((size_t)(x) >> 32))

  asm volatile(
      "v_mbcnt_lo_u32_b32 v240, -1, 0\n"
      "v_mbcnt_hi_u32_b32 v240, -1, v240\n"
      "s_mov_b32 s46, 32768\n"
      "s_mov_b32 s47, 0x20000\n"
      "v_lshrrev_b32 v234, 3, v240\n"
      "v_and_b32 v235, 7, v240\n"
      "v_lshlrev_b32 v233, 4, v235\n"
      "v_lshl_add_u32 v246, v234, 9, v233\n"
      "v_lshl_add_u32 v236, v234, 10, v233\n"
      "s_lshl_b32 s54, %[wave], 12\n"
      "s_lshl_b32 s55, %[h], 14\n"
      "s_add_u32 s54, s54, s55\n"
      "s_lshl_b32 s56, %[wave], 13\n"
      "s_lshl_b32 s55, %[h], 7\n"
      "s_add_u32 s56, s56, s55\n"
      "s_mov_b32 s44, %[wqlo]\n"
      "s_and_b32 s45, %[wqhi], 0xffff\n"
      "buffer_load_dwordx4 v[96:99], v246, s[44:47], s54 offen offset:0\n"
      "buffer_load_dwordx4 v[100:103], v246, s[44:47], s54 offen offset:128\n"
      "buffer_load_dwordx4 v[104:107], v246, s[44:47], s54 offen offset:256\n"
      "buffer_load_dwordx4 v[108:111], v246, s[44:47], s54 offen offset:384\n"
      "s_mov_b32 s44, %[wklo]\n"
      "s_and_b32 s45, %[wkhi], 0xffff\n"
      "buffer_load_dwordx4 v[64:67], v246, s[44:47], s54 offen offset:0\n"
      "buffer_load_dwordx4 v[68:71], v246, s[44:47], s54 offen offset:128\n"
      "buffer_load_dwordx4 v[72:75], v246, s[44:47], s54 offen offset:256\n"
      "buffer_load_dwordx4 v[76:79], v246, s[44:47], s54 offen offset:384\n"
      "s_mov_b32 s44, %[wvlo]\n"
      "s_and_b32 s45, %[wvhi], 0xffff\n"
      "buffer_load_dwordx4 v[80:83], v246, s[44:47], s54 offen offset:0\n"
      "buffer_load_dwordx4 v[84:87], v246, s[44:47], s54 offen offset:128\n"
      "buffer_load_dwordx4 v[88:91], v246, s[44:47], s54 offen offset:256\n"
      "buffer_load_dwordx4 v[92:95], v246, s[44:47], s54 offen offset:384\n"
      "s_mov_b32 s44, %[wrlo]\n"
      "s_and_b32 s45, %[wrhi], 0xffff\n"
      "buffer_load_dwordx4 v[112:115], v236, s[44:47], s56 offen offset:0\n"
      "buffer_load_dwordx4 v[116:119], v236, s[44:47], s56 offen offset:256\n"
      "buffer_load_dwordx4 v[120:123], v236, s[44:47], s56 offen offset:512\n"
      "buffer_load_dwordx4 v[124:127], v236, s[44:47], s56 offen offset:768\n"
      "s_mov_b32 s40, %[elo]\n"
      "s_and_b32 s41, %[ehi], 0xffff\n"
      "s_mov_b32 s42, 104857600\n"
      "s_mov_b32 s43, 0x20000\n"
      "s_mov_b32 s48, %[first]\n"
      "v_and_b32 v241, 15, v240\n"
      "v_lshrrev_b32 v242, 4, v240\n"
      "v_lshrrev_b32 v243, 2, v241\n"
      "v_and_b32 v244, 3, v240\n"
      "v_mul_u32_u24 v247, 102400, v243\n"
      "v_lshl_add_u32 v247, v244, 8, v247\n"
      "v_lshl_add_u32 v247, v242, 4, v247\n"
      "s_mul_i32 s53, %[wave], 24576\n"
      "s_add_u32 s53, s53, %[ldsb]\n"
      "s_add_u32 s53, s53, 65536\n"
      "v_and_b32 v248, 2, v234\n"
      "v_xor_b32 v248, v248, v244\n"
      "v_lshlrev_b32 v248, 4, v248\n"
      "v_lshrrev_b32 v249, 5, v240\n"
      "v_lshl_add_u32 v249, v249, 6, v248\n"
      "v_bfe_u32 v248, v240, 2, 3\n"
      "v_lshl_add_u32 v1, v248, 8, v249\n"
      "s_mul_i32 s55, %[h], 0\n"
      "s_add_u32 s55, s55, 24576\n"
      "s_add_u32 s55, s48, s55\n"
      "buffer_load_dwordx4 v[32:35], v247, s[40:43], s55 offen offset:0\n"
      "buffer_load_dwordx4 v[36:39], v247, s[40:43], s55 offen offset:64\n"
      "buffer_load_dwordx4 v[40:43], v247, s[40:43], s55 offen offset:128\n"
      "buffer_load_dwordx4 v[44:47], v247, s[40:43], s55 offen offset:192\n"
      "s_mov_b32 s51, 0x3e38aa3b\n"
      "s_lshl_b32 s49, %[wave], 10\n"
      "s_add_u32 s49, s49, %[ldsb]\n"
      "v_lshlrev_b32 v255, 8, v242\n"
      "v_lshl_add_u32 v255, v235, 5, v255\n"
      "v_lshrrev_b32 v248, 2, v235\n"
      "v_lshl_add_u32 v255, v248, 4, v255\n"
      "v_and_b32 v248, 1, v234\n"
      "v_lshl_add_u32 v255, v248, 3, v255\n"
      "v_add_u32 v255, s49, v255\n"
      "v_xor_b32 v232, 16, v255\n"
      "v_mov_b32 v238, 0x05040100\n"
      "v_mov_b32 v239, 0x07060302\n"
      "v_xor_b32 v254, v248, v240\n"
      "v_lshlrev_b32 v254, 4, v254\n"
      "v_add_u32 v254, %[ldsb], v254\n"
      "s_waitcnt vmcnt(16)\n"
      "v_lshlrev_b32 v192, 16, v96\n"
      "v_lshlrev_b32 v193, 16, v100\n"
      "v_mul_f32 v192, s51, v192\n"
      "v_mul_f32 v193, s51, v193\n"
      "v_cvt_pk_bf16_f32 v160, v192, v193\n"
      "v_lshlrev_b32 v194, 16, v104\n"
      "v_lshlrev_b32 v195, 16, v108\n"
      "v_mul_f32 v194, s51, v194\n"
      "v_mul_f32 v195, s51, v195\n"
      "v_cvt_pk_bf16_f32 v161, v194, v195\n"
      "ds_write_b64 v255, v[160:161] offset:32768\n"
      "v_and_b32 v192, 0xffff0000, v96\n"
      "v_and_b32 v193, 0xffff0000, v100\n"
      "v_mul_f32 v192, s51, v192\n"
      "v_mul_f32 v193, s51, v193\n"
      "v_cvt_pk_bf16_f32 v162, v192, v193\n"
      "v_and_b32 v194, 0xffff0000, v104\n"
      "v_and_b32 v195, 0xffff0000, v108\n"
      "v_mul_f32 v194, s51, v194\n"
      "v_mul_f32 v195, s51, v195\n"
      "v_cvt_pk_bf16_f32 v163, v194, v195\n"
      "ds_write_b64 v255, v[162:163] offset:36864\n"
      "v_lshlrev_b32 v192, 16, v97\n"
      "v_lshlrev_b32 v193, 16, v101\n"
      "v_mul_f32 v192, s51, v192\n"
      "v_mul_f32 v193, s51, v193\n"
      "v_cvt_pk_bf16_f32 v164, v192, v193\n"
      "v_lshlrev_b32 v194, 16, v105\n"
      "v_lshlrev_b32 v195, 16, v109\n"
      "v_mul_f32 v194, s51, v194\n"
      "v_mul_f32 v195, s51, v195\n"
      "v_cvt_pk_bf16_f32 v165, v194, v195\n"
      "ds_write_b64 v255, v[164:165] offset:40960\n"
      "v_and_b32 v192, 0xffff0000, v97\n"
      "v_and_b32 v193, 0xffff0000, v101\n"
      "v_mul_f32 v192, s51, v192\n"
      "v_mul_f32 v193, s51, v193\n"
      "v_cvt_pk_bf16_f32 v166, v192, v193\n"
      "v_and_b32 v194, 0xffff0000, v105\n"
      "v_and_b32 v195, 0xffff0000, v109\n"
      "v_mul_f32 v194, s51, v194\n"
      "v_mul_f32 v195, s51, v195\n"
      "v_cvt_pk_bf16_f32 v167, v194, v195\n"
      "ds_write_b64 v255, v[166:167] offset:45056\n"
      "v_lshlrev_b32 v192, 16, v98\n"
      "v_lshlrev_b32 v193, 16, v102\n"
      "v_mul_f32 v192, s51, v192\n"
      "v_mul_f32 v193, s51, v193\n"
      "v_cvt_pk_bf16_f32 v168, v192, v193\n"
      "v_lshlrev_b32 v194, 16, v106\n"
      "v_lshlrev_b32 v195, 16, v110\n"
      "v_mul_f32 v194, s51, v194\n"
      "v_mul_f32 v195, s51, v195\n"
      "v_cvt_pk_bf16_f32 v169, v194, v195\n"
      "ds_write_b64 v232, v[168:169] offset:32768\n"
      "v_and_b32 v192, 0xffff0000, v98\n"
      "v_and_b32 v193, 0xffff0000, v102\n"
      "v_mul_f32 v192, s51, v192\n"
      "v_mul_f32 v193, s51, v193\n"
      "v_cvt_pk_bf16_f32 v170, v192, v193\n"
      "v_and_b32 v194, 0xffff0000, v106\n"
      "v_and_b32 v195, 0xffff0000, v110\n"
      "v_mul_f32 v194, s51, v194\n"
      "v_mul_f32 v195, s51, v195\n"
      "v_cvt_pk_bf16_f32 v171, v194, v195\n"
      "ds_write_b64 v232, v[170:171] offset:36864\n"
      "v_lshlrev_b32 v192, 16, v99\n"
      "v_lshlrev_b32 v193, 16, v103\n"
      "v_mul_f32 v192, s51, v192\n"
      "v_mul_f32 v193, s51, v193\n"
      "v_cvt_pk_bf16_f32 v172, v192, v193\n"
      "v_lshlrev_b32 v194, 16, v107\n"
      "v_lshlrev_b32 v195, 16, v111\n"
      "v_mul_f32 v194, s51, v194\n"
      "v_mul_f32 v195, s51, v195\n"
      "v_cvt_pk_bf16_f32 v173, v194, v195\n"
      "ds_write_b64 v232, v[172:173] offset:40960\n"
      "v_and_b32 v192, 0xffff0000, v99\n"
      "v_and_b32 v193, 0xffff0000, v103\n"
      "v_mul_f32 v192, s51, v192\n"
      "v_mul_f32 v193, s51, v193\n"
      "v_cvt_pk_bf16_f32 v174, v192, v193\n"
      "v_and_b32 v194, 0xffff0000, v107\n"
      "v_and_b32 v195, 0xffff0000, v111\n"
      "v_mul_f32 v194, s51, v194\n"
      "v_mul_f32 v195, s51, v195\n"
      "v_cvt_pk_bf16_f32 v175, v194, v195\n"
      "ds_write_b64 v232, v[174:175] offset:45056\n"
      "s_mul_i32 s55, %[h], 0\n"
      "s_add_u32 s55, s55, 434176\n"
      "s_add_u32 s55, s48, s55\n"
      "buffer_load_dwordx4 v[48:51], v247, s[40:43], s55 offen offset:0\n"
      "s_waitcnt vmcnt(13)\n"
      "v_perm_b32 v128, v68, v64, v238\n"
      "v_perm_b32 v129, v76, v72, v238\n"
      "ds_write_b64 v255, v[128:129] offset:0\n"
      "v_perm_b32 v130, v68, v64, v239\n"
      "v_perm_b32 v131, v76, v72, v239\n"
      "ds_write_b64 v255, v[130:131] offset:4096\n"
      "v_perm_b32 v132, v69, v65, v238\n"
      "v_perm_b32 v133, v77, v73, v238\n"
      "ds_write_b64 v255, v[132:133] offset:8192\n"
      "v_perm_b32 v134, v69, v65, v239\n"
      "v_perm_b32 v135, v77, v73, v239\n"
      "ds_write_b64 v255, v[134:135] offset:12288\n"
      "v_perm_b32 v136, v70, v66, v238\n"
      "v_perm_b32 v137, v78, v74, v238\n"
      "ds_write_b64 v232, v[136:137] offset:0\n"
      "v_perm_b32 v138, v70, v66, v239\n"
      "v_perm_b32 v139, v78, v74, v239\n"
      "ds_write_b64 v232, v[138:139] offset:4096\n"
      "v_perm_b32 v140, v71, v67, v238\n"
      "v_perm_b32 v141, v79, v75, v238\n"
      "ds_write_b64 v232, v[140:141] offset:8192\n"
      "v_perm_b32 v142, v71, v67, v239\n"
      "v_perm_b32 v143, v79, v75, v239\n"
      "ds_write_b64 v232, v[142:143] offset:12288\n"
      "buffer_load_dwordx4 v[52:55], v247, s[40:43], s55 offen offset:64\n"
      "s_waitcnt vmcnt(10)\n"
      "v_perm_b32 v144, v84, v80, v238\n"
      "v_perm_b32 v145, v92, v88, v238\n"
      "ds_write_b64 v255, v[144:145] offset:16384\n"
      "v_perm_b32 v146, v84, v80, v239\n"
      "v_perm_b32 v147, v92, v88, v239\n"
      "ds_write_b64 v255, v[146:147] offset:20480\n"
      "v_perm_b32 v148, v85, v81, v238\n"
      "v_perm_b32 v149, v93, v89, v238\n"
      "ds_write_b64 v255, v[148:149] offset:24576\n"
      "v_perm_b32 v150, v85, v81, v239\n"
      "v_perm_b32 v151, v93, v89, v239\n"
      "ds_write_b64 v255, v[150:151] offset:28672\n"
      "v_perm_b32 v152, v86, v82, v238\n"
      "v_perm_b32 v153, v94, v90, v238\n"
      "ds_write_b64 v232, v[152:153] offset:16384\n"
      "v_perm_b32 v154, v86, v82, v239\n"
      "v_perm_b32 v155, v94, v90, v239\n"
      "ds_write_b64 v232, v[154:155] offset:20480\n"
      "v_perm_b32 v156, v87, v83, v238\n"
      "v_perm_b32 v157, v95, v91, v238\n"
      "ds_write_b64 v232, v[156:157] offset:24576\n"
      "v_perm_b32 v158, v87, v83, v239\n"
      "v_perm_b32 v159, v95, v91, v239\n"
      "ds_write_b64 v232, v[158:159] offset:28672\n"
      "buffer_load_dwordx4 v[56:59], v247, s[40:43], s55 offen offset:128\n"
      "s_waitcnt vmcnt(7)\n"
      "v_perm_b32 v176, v116, v112, v238\n"
      "v_perm_b32 v177, v124, v120, v238\n"
      "ds_write_b64 v255, v[176:177] offset:49152\n"
      "v_perm_b32 v178, v116, v112, v239\n"
      "v_perm_b32 v179, v124, v120, v239\n"
      "ds_write_b64 v255, v[178:179] offset:53248\n"
      "v_perm_b32 v180, v117, v113, v238\n"
      "v_perm_b32 v181, v125, v121, v238\n"
      "ds_write_b64 v255, v[180:181] offset:57344\n"
      "v_perm_b32 v182, v117, v113, v239\n"
      "v_perm_b32 v183, v125, v121, v239\n"
      "ds_write_b64 v255, v[182:183] offset:61440\n"
      "v_perm_b32 v184, v118, v114, v238\n"
      "v_perm_b32 v185, v126, v122, v238\n"
      "ds_write_b64 v232, v[184:185] offset:49152\n"
      "v_perm_b32 v186, v118, v114, v239\n"
      "v_perm_b32 v187, v126, v122, v239\n"
      "ds_write_b64 v232, v[186:187] offset:53248\n"
      "v_perm_b32 v188, v119, v115, v238\n"
      "v_perm_b32 v189, v127, v123, v238\n"
      "ds_write_b64 v232, v[188:189] offset:57344\n"
      "v_perm_b32 v190, v119, v115, v239\n"
      "v_perm_b32 v191, v127, v123, v239\n"
      "ds_write_b64 v232, v[190:191] offset:61440\n"
      "buffer_load_dwordx4 v[60:63], v247, s[40:43], s55 offen offset:192\n"
      "s_waitcnt lgkmcnt(0)\n"
      "s_barrier\n"
      "ds_read_b128 a[192:195], v254 offset:49152\n"
      "ds_read_b128 a[196:199], v254 offset:50176\n"
      "ds_read_b128 a[200:203], v254 offset:51200\n"
      "ds_read_b128 a[204:207], v254 offset:52224\n"
      "ds_read_b128 a[208:211], v254 offset:53248\n"
      "ds_read_b128 a[212:215], v254 offset:54272\n"
      "ds_read_b128 a[216:219], v254 offset:55296\n"
      "ds_read_b128 a[220:223], v254 offset:56320\n"
      "ds_read_b128 a[224:227], v254 offset:57344\n"
      "ds_read_b128 a[228:231], v254 offset:58368\n"
      "ds_read_b128 a[232:235], v254 offset:59392\n"
      "ds_read_b128 a[236:239], v254 offset:60416\n"
      "s_mov_b32 s44, %[olo]\n"
      "s_and_b32 s45, %[ohi], 0xffff\n"
      "s_mov_b32 s46, 104857600\n"
      "s_add_u32 s49, %[first], 102400\n"
      "s_mov_b32 s50, 8\n"
      "v_lshlrev_b32 v245, 3, v241\n"
      "v_lshrrev_b32 v248, 3, v241\n"
      "v_lshlrev_b32 v248, 11, v248\n"
      "v_lshl_add_u32 v248, v235, 6, v248\n"
      "v_lshrrev_b32 v249, 1, v241\n"
      "v_and_b32 v249, 2, v249\n"
      "v_xor_b32 v249, v249, v242\n"
      "v_lshl_add_u32 v248, v249, 4, v248\n"
      "v_add_u32 v0, s53, v248\n"
      "s_lshl_b32 s54, %[h], 7\n"
      "v_add_u32 v248, s54, v245\n"
      "v_lshl_add_u32 v2, v242, 10, v248\n"
      "v_lshl_add_u32 v3, v242, 8, v248\n"
      "v_and_b32 v4, 60, v240\n"
      "v_and_b32 v248, 1, v240\n"
      "v_mul_u32_u24 v5, 0xaaaa, v248\n"
      "v_add_u32 v5, 0x4444, v5\n"
      "v_lshlrev_b32 v7, 6, v243\n"
      "v_lshl_add_u32 v7, v248, 3, v7\n"
      "v_and_b32 v249, 1, v242\n"
      "v_lshl_add_u32 v6, v249, 5, v7\n"
      "v_lshrrev_b32 v250, 1, v242\n"
      "v_lshl_add_u32 v6, v250, 2, v6\n"
      "v_lshl_add_u32 v7, v242, 4, v7\n"
      "v_mov_b32 v8, 1.0\n"
      "s_lshl_b32 s54, %[wave], 14\n"
      "s_add_u32 s54, s54, %[ldsb]\n"
      "v_lshlrev_b32 v9, 9, v242\n"
      "v_add_u32 v9, s54, v9\n"
      "v_lshl_add_u32 v251, v241, 4, v9\n"
      "v_lshl_add_u32 v9, v244, 4, v9\n"
      "s_add_u32 s54, s54, 4096\n"
      "v_lshlrev_b32 v10, 10, v242\n"
      "v_lshl_add_u32 v10, v243, 4, v10\n"
      "v_lshl_add_u32 v10, v248, 9, v10\n"
      "v_add_u32 v10, s54, v10\n"
      "v_lshlrev_b32 v252, 10, v250\n"
      "v_lshl_add_u32 v252, v241, 4, v252\n"
      "v_lshl_add_u32 v252, v249, 3, v252\n"
      "v_add_u32 v252, s54, v252\n"
      "s_add_u32 s54, s54, 4096\n"
      "v_lshlrev_b32 v11, 3, v240\n"
      "v_add_u32 v11, s54, v11\n"
      "v_lshl_add_u32 v253, v242, 9, v245\n"
      "v_add_u32 v253, s54, v253\n"
      "v_mov_b32 v12, 0x3f803f80\n"
      "v_mov_b32 v13, 0x3f803f80\n"
      "v_mov_b32 v14, 0x3f803f80\n"
      "v_mov_b32 v15, 0x3f803f80\n"
      "s_mov_b32 vcc_lo, 0x55555555\n"
      "s_mov_b32 vcc_hi, 0x55555555\n"
      "s_lshl_b32 s54, %[wave], 14\n"
      "s_add_u32 s54, s54, %[ldsb]\n"
      "v_lshlrev_b32 v16, 10, v242\n"
      "v_lshrrev_b32 v248, 1, v241\n"
      "v_lshl_add_u32 v16, v248, 4, v16\n"
      "v_and_b32 v248, 1, v240\n"
      "v_lshl_add_u32 v16, v248, 2, v16\n"
      "v_add_u32 v16, s54, v16\n"
      "v_lshrrev_b32 v248, 3, v241\n"
      "v_lshl_add_u32 v16, v248, 20, v16\n"
      "s_waitcnt vmcnt(4)\n"
      "s_waitcnt lgkmcnt(11)\n"
      "v_mfma_f32_16x16x32_bf16 v[128:131], v[32:35], a[192:195], 0\n"
      "s_waitcnt lgkmcnt(10)\n"
      "ds_read_b128 a[240:243], v254 offset:61440\n"
      "v_mfma_f32_16x16x32_bf16 v[128:131], v[36:39], a[196:199], v[128:131]\n"
      "s_waitcnt lgkmcnt(10)\n"
      "ds_read_b128 a[244:247], v254 offset:62464\n"
      "v_mfma_f32_16x16x32_bf16 v[128:131], v[40:43], a[200:203], v[128:131]\n"
      "s_waitcnt lgkmcnt(10)\n"
      "ds_read_b128 a[248:251], v254 offset:63488\n"
      "v_mfma_f32_16x16x32_bf16 v[128:131], v[44:47], a[204:207], v[128:131]\n"
      "s_waitcnt lgkmcnt(10)\n"
      "ds_read_b128 a[252:255], v254 offset:64512\n"
      "v_mfma_f32_16x16x32_bf16 v[132:135], v[32:35], a[208:211], 0\n"
      "s_waitcnt lgkmcnt(10)\n"
      "ds_read_b128 a[128:131], v254 offset:32768\n"
      "s_mov_b32 s57, 8192\n"
      "s_add_u32 s52, s48, s57\n"
      "s_add_u32 s57, s53, s57\n"
      "s_mov_b32 m0, s57\n"
      "s_nop 0\n"
      "buffer_load_dwordx4 v1, s[40:43], s52 offen offset:0 lds\n"
      "v_mfma_f32_16x16x32_bf16 v[132:135], v[36:39], a[212:215], v[132:135]\n"
      "s_waitcnt lgkmcnt(10)\n"
      "ds_read_b128 a[132:135], v254 offset:33792\n"
      "v_mfma_f32_16x16x32_bf16 v[132:135], v[40:43], a[216:219], v[132:135]\n"
      "s_waitcnt lgkmcnt(10)\n"
      "ds_read_b128 a[136:139], v254 offset:34816\n"
      "v_mfma_f32_16x16x32_bf16 v[132:135], v[44:47], a[220:223], v[132:135]\n"
      "s_waitcnt lgkmcnt(10)\n"
      "ds_read_b128 a[140:143], v254 offset:35840\n"
      "v_mfma_f32_16x16x32_bf16 v[136:139], v[32:35], a[224:227], 0\n"
      "s_waitcnt lgkmcnt(10)\n"
      "ds_read_b128 a[144:147], v254 offset:36864\n"
      "v_mfma_f32_16x16x32_bf16 v[136:139], v[36:39], a[228:231], v[136:139]\n"
      "s_waitcnt lgkmcnt(10)\n"
      "ds_read_b128 a[148:151], v254 offset:37888\n"
      "buffer_load_dwordx4 v1, s[40:43], s52 offen offset:2048 lds\n"
      "v_mfma_f32_16x16x32_bf16 v[136:139], v[40:43], a[232:235], v[136:139]\n"
      "s_waitcnt lgkmcnt(10)\n"
      "ds_read_b128 a[152:155], v254 offset:38912\n"
      "v_mfma_f32_16x16x32_bf16 v[136:139], v[44:47], a[236:239], v[136:139]\n"
      "s_waitcnt lgkmcnt(10)\n"
      "ds_read_b128 a[156:159], v254 offset:39936\n"
      "v_mfma_f32_16x16x32_bf16 v[140:143], v[32:35], a[240:243], 0\n"
      "s_waitcnt lgkmcnt(10)\n"
      "ds_read_b128 a[160:163], v254 offset:40960\n"
      "v_mfma_f32_16x16x32_bf16 v[140:143], v[36:39], a[244:247], v[140:143]\n"
      "s_waitcnt lgkmcnt(10)\n"
      "ds_read_b128 a[164:167], v254 offset:41984\n"
      "v_mfma_f32_16x16x32_bf16 v[140:143], v[40:43], a[248:251], v[140:143]\n"
      "s_waitcnt lgkmcnt(10)\n"
      "ds_read_b128 a[168:171], v254 offset:43008\n"
      "s_add_u32 m0, s57, 896\n"
      "s_nop 0\n"
      "buffer_load_dwordx4 v1, s[40:43], s52 offen offset:128 lds\n"
      "v_mfma_f32_16x16x32_bf16 v[140:143], v[44:47], a[252:255], v[140:143]\n"
      "s_waitcnt lgkmcnt(10)\n"
      "ds_read_b128 a[172:175], v254 offset:44032\n"
      "v_cvt_pk_bf16_f32 v112, v128, v132\n"
      "v_mfma_f32_16x16x32_bf16 v[144:147], a[128:131], v[32:35], 0\n"
      "s_waitcnt lgkmcnt(10)\n"
      "ds_read_b128 a[176:179], v254 offset:45056\n"
      "v_cvt_pk_bf16_f32 v114, v129, v133\n"
      "v_mfma_f32_16x16x32_bf16 v[144:147], a[132:135], v[36:39], v[144:147]\n"
      "s_waitcnt lgkmcnt(10)\n"
      "ds_read_b128 a[180:183], v254 offset:46080\n"
      "v_cvt_pk_bf16_f32 v116, v130, v134\n"
      "v_mfma_f32_16x16x32_bf16 v[144:147], a[136:139], v[40:43], v[144:147]\n"
      "s_waitcnt lgkmcnt(10)\n"
      "ds_read_b128 a[184:187], v254 offset:47104\n"
      "v_cvt_pk_bf16_f32 v118, v131, v135\n"
      "v_mfma_f32_16x16x32_bf16 v[144:147], a[140:143], v[44:47], v[144:147]\n"
      "s_waitcnt lgkmcnt(10)\n"
      "ds_read_b128 a[188:191], v254 offset:48128\n"
      "buffer_load_dwordx4 v1, s[40:43], s52 offen offset:2176 lds\n"
      "v_mfma_f32_16x16x32_bf16 v[148:151], a[144:147], v[32:35], 0\n"
      "s_waitcnt lgkmcnt(10)\n"
      "ds_read_b128 a[64:67], v254 offset:16384\n"
      "v_mfma_f32_16x16x32_bf16 v[148:151], a[148:151], v[36:39], v[148:151]\n"
      "s_waitcnt lgkmcnt(10)\n"
      "ds_read_b128 a[68:71], v254 offset:17408\n"
      "v_mfma_f32_16x16x32_bf16 v[148:151], a[152:155], v[40:43], v[148:151]\n"
      "s_waitcnt lgkmcnt(10)\n"
      "ds_read_b128 a[72:75], v254 offset:18432\n"
      "v_mfma_f32_16x16x32_bf16 v[148:151], a[156:159], v[44:47], v[148:151]\n"
      "s_waitcnt lgkmcnt(10)\n"
      "ds_read_b128 a[76:79], v254 offset:19456\n"
      "v_cvt_pk_bf16_f32 v113, v136, v140\n"
      "v_mfma_f32_16x16x32_bf16 v[152:155], a[160:163], v[32:35], 0\n"
      "s_waitcnt lgkmcnt(10)\n"
      "ds_read_b128 a[80:83], v254 offset:20480\n"
      "s_mov_b32 s57, 20480\n"
      "s_add_u32 s52, s48, s57\n"
      "s_add_u32 s57, s53, s57\n"
      "s_mov_b32 m0, s57\n"
      "s_nop 0\n"
      "buffer_load_dwordx4 v1, s[40:43], s52 offen offset:0 lds\n"
      "v_mfma_f32_16x16x32_bf16 v[152:155], a[164:167], v[36:39], v[152:155]\n"
      "s_waitcnt lgkmcnt(10)\n"
      "ds_read_b128 a[84:87], v254 offset:21504\n"
      "v_cvt_pk_bf16_f32 v115, v137, v141\n"
      "v_mfma_f32_16x16x32_bf16 v[152:155], a[168:171], v[40:43], v[152:155]\n"
      "s_waitcnt lgkmcnt(10)\n"
      "ds_read_b128 a[88:91], v254 offset:22528\n"
      "v_cvt_pk_bf16_f32 v117, v138, v142\n"
      "v_mfma_f32_16x16x32_bf16 v[152:155], a[172:175], v[44:47], v[152:155]\n"
      "s_waitcnt lgkmcnt(10)\n"
      "ds_read_b128 a[92:95], v254 offset:23552\n"
      "v_cvt_pk_bf16_f32 v119, v139, v143\n"
      "v_mfma_f32_16x16x32_bf16 v[156:159], a[176:179], v[32:35], 0\n"
      "s_waitcnt lgkmcnt(10)\n"
      "ds_read_b128 a[96:99], v254 offset:24576\n"
      "v_cvt_pk_bf16_f32 v96, v144, v145\n"
      "v_mfma_f32_16x16x32_bf16 v[156:159], a[180:183], v[36:39], v[156:159]\n"
      "s_waitcnt lgkmcnt(10)\n"
      "ds_read_b128 a[100:103], v254 offset:25600\n"
      "buffer_load_dwordx4 v1, s[40:43], s52 offen offset:2048 lds\n"
      "v_mfma_f32_16x16x32_bf16 v[156:159], a[184:187], v[40:43], v[156:159]\n"
      "s_waitcnt lgkmcnt(10)\n"
      "ds_read_b128 a[104:107], v254 offset:26624\n"
      "v_cvt_pk_bf16_f32 v98, v146, v147\n"
      "v_mfma_f32_16x16x32_bf16 v[156:159], a[188:191], v[44:47], v[156:159]\n"
      "s_waitcnt lgkmcnt(10)\n"
      "ds_read_b128 a[108:111], v254 offset:27648\n"
      "v_cvt_pk_bf16_f32 v97, v148, v149\n"
      "v_mfma_f32_16x16x32_bf16 v[160:163], v[32:35], a[64:67], 0\n"
      "s_waitcnt lgkmcnt(10)\n"
      "ds_read_b128 a[112:115], v254 offset:28672\n"
      "v_cvt_pk_bf16_f32 v99, v150, v151\n"
      "v_mfma_f32_16x16x32_bf16 v[160:163], v[36:39], a[68:71], v[160:163]\n"
      "s_waitcnt lgkmcnt(10)\n"
      "ds_read_b128 a[116:119], v254 offset:29696\n"
      "v_mfma_f32_16x16x32_bf16 v[160:163], v[40:43], a[72:75], v[160:163]\n"
      "s_waitcnt lgkmcnt(10)\n"
      "ds_read_b128 a[120:123], v254 offset:30720\n"
      "s_add_u32 m0, s57, 896\n"
      "s_nop 0\n"
      "buffer_load_dwordx4 v1, s[40:43], s52 offen offset:128 lds\n"
      "v_mfma_f32_16x16x32_bf16 v[160:163], v[44:47], a[76:79], v[160:163]\n"
      "s_waitcnt lgkmcnt(10)\n"
      "ds_read_b128 a[124:127], v254 offset:31744\n"
      "v_cvt_pk_bf16_f32 v100, v152, v153\n"
      "v_mfma_f32_16x16x32_bf16 v[164:167], v[32:35], a[80:83], 0\n"
      "s_waitcnt lgkmcnt(10)\n"
      "ds_read_b128 a[0:3], v254 offset:0\n"
      "v_cvt_pk_bf16_f32 v102, v154, v155\n"
      "v_mfma_f32_16x16x32_bf16 v[164:167], v[36:39], a[84:87], v[164:167]\n"
      "s_waitcnt lgkmcnt(10)\n"
      "ds_read_b128 a[4:7], v254 offset:1024\n"
      "v_mfma_f32_16x16x32_bf16 v[164:167], v[40:43], a[88:91], v[164:167]\n"
      "s_waitcnt lgkmcnt(10)\n"
      "ds_read_b128 a[8:11], v254 offset:2048\n"
      "v_mfma_f32_16x16x32_bf16 v[164:167], v[44:47], a[92:95], v[164:167]\n"
      "s_waitcnt lgkmcnt(10)\n"
      "ds_read_b128 a[12:15], v254 offset:3072\n"
      "buffer_load_dwordx4 v1, s[40:43], s52 offen offset:2176 lds\n"
      "v_mfma_f32_16x16x32_bf16 v[168:171], v[32:35], a[96:99], 0\n"
      "s_waitcnt lgkmcnt(10)\n"
      "ds_read_b128 a[16:19], v254 offset:4096\n"
      "v_cvt_pk_bf16_f32 v101, v156, v157\n"
      "v_mfma_f32_16x16x32_bf16 v[168:171], v[36:39], a[100:103], v[168:171]\n"
      "s_waitcnt lgkmcnt(10)\n"
      "ds_read_b128 a[20:23], v254 offset:5120\n"
      "v_cvt_pk_bf16_f32 v103, v158, v159\n"
      "v_mfma_f32_16x16x32_bf16 v[168:171], v[40:43], a[104:107], v[168:171]\n"
      "s_waitcnt lgkmcnt(10)\n"
      "ds_read_b128 a[24:27], v254 offset:6144\n"
      "v_mfma_f32_16x16x32_bf16 v[168:171], v[44:47], a[108:111], v[168:171]\n"
      "s_waitcnt lgkmcnt(10)\n"
      "ds_read_b128 a[28:31], v254 offset:7168\n"
      "v_cvt_pk_bf16_f32 v80, v160, v161\n"
      "v_mfma_f32_16x16x32_bf16 v[172:175], v[32:35], a[112:115], 0\n"
      "s_waitcnt lgkmcnt(10)\n"
      "ds_read_b128 a[32:35], v254 offset:8192\n"
      "s_mov_b32 s57, 12288\n"
      "s_add_u32 s52, s48, s57\n"
      "s_add_u32 s57, s53, s57\n"
      "s_mov_b32 m0, s57\n"
      "s_nop 0\n"
      "buffer_load_dwordx4 v1, s[40:43], s52 offen offset:0 lds\n"
      "v_mfma_f32_16x16x32_bf16 v[172:175], v[36:39], a[116:119], v[172:175]\n"
      "s_waitcnt lgkmcnt(10)\n"
      "ds_read_b128 a[36:39], v254 offset:9216\n"
      "v_cvt_pk_bf16_f32 v81, v162, v163\n"
      "v_mfma_f32_16x16x32_bf16 v[172:175], v[40:43], a[120:123], v[172:175]\n"
      "s_waitcnt lgkmcnt(10)\n"
      "ds_read_b128 a[40:43], v254 offset:10240\n"
      "v_mfma_f32_16x16x32_bf16 v[172:175], v[44:47], a[124:127], v[172:175]\n"
      "s_waitcnt lgkmcnt(10)\n"
      "ds_read_b128 a[44:47], v254 offset:11264\n"
      "v_cvt_pk_bf16_f32 v82, v164, v165\n"
      "v_mfma_f32_16x16x32_bf16 v[176:179], a[0:3], v[32:35], 0\n"
      "s_waitcnt lgkmcnt(10)\n"
      "ds_read_b128 a[48:51], v254 offset:12288\n"
      "v_cvt_pk_bf16_f32 v83, v166, v167\n"
      "v_mfma_f32_16x16x32_bf16 v[176:179], a[4:7], v[36:39], v[176:179]\n"
      "s_waitcnt lgkmcnt(10)\n"
      "ds_read_b128 a[52:55], v254 offset:13312\n"
      "buffer_load_dwordx4 v1, s[40:43], s52 offen offset:2048 lds\n"
      "v_mfma_f32_16x16x32_bf16 v[176:179], a[8:11], v[40:43], v[176:179]\n"
      "s_waitcnt lgkmcnt(10)\n"
      "ds_read_b128 a[56:59], v254 offset:14336\n"
      "v_mfma_f32_16x16x32_bf16 v[176:179], a[12:15], v[44:47], v[176:179]\n"
      "s_waitcnt lgkmcnt(10)\n"
      "ds_read_b128 a[60:63], v254 offset:15360\n"
      "v_cvt_pk_bf16_f32 v84, v168, v169\n"
      "v_mfma_f32_16x16x32_bf16 v[180:183], a[16:19], v[32:35], 0\n"
      "s_waitcnt lgkmcnt(10)\n"
      "v_cvt_pk_bf16_f32 v85, v170, v171\n"
      "v_mfma_f32_16x16x32_bf16 v[180:183], a[20:23], v[36:39], v[180:183]\n"
      "s_waitcnt lgkmcnt(9)\n"
      "v_mfma_f32_16x16x32_bf16 v[180:183], a[24:27], v[40:43], v[180:183]\n"
      "s_waitcnt lgkmcnt(8)\n"
      "s_add_u32 m0, s57, 896\n"
      "s_nop 0\n"
      "buffer_load_dwordx4 v1, s[40:43], s52 offen offset:128 lds\n"
      "v_mfma_f32_16x16x32_bf16 v[180:183], a[28:31], v[44:47], v[180:183]\n"
      "s_waitcnt lgkmcnt(7)\n"
      "v_cvt_pk_bf16_f32 v86, v172, v173\n"
      "v_mfma_f32_16x16x32_bf16 v[184:187], a[32:35], v[32:35], 0\n"
      "s_waitcnt lgkmcnt(6)\n"
      "v_cvt_pk_bf16_f32 v87, v174, v175\n"
      "v_mfma_f32_16x16x32_bf16 v[184:187], a[36:39], v[36:39], v[184:187]\n"
      "s_waitcnt lgkmcnt(5)\n"
      "v_mfma_f32_16x16x32_bf16 v[184:187], a[40:43], v[40:43], v[184:187]\n"
      "s_waitcnt lgkmcnt(4)\n"
      "v_mfma_f32_16x16x32_bf16 v[184:187], a[44:47], v[44:47], v[184:187]\n"
      "s_waitcnt lgkmcnt(3)\n"
      "buffer_load_dwordx4 v1, s[40:43], s52 offen offset:2176 lds\n"
      "v_mfma_f32_16x16x32_bf16 v[188:191], a[48:51], v[32:35], 0\n"
      "s_waitcnt lgkmcnt(2)\n"
      "v_cvt_pk_bf16_f32 v64, v176, v177\n"
      "v_mfma_f32_16x16x32_bf16 v[188:191], a[52:55], v[36:39], v[188:191]\n"
      "s_waitcnt lgkmcnt(1)\n"
      "v_cvt_pk_bf16_f32 v65, v178, v179\n"
      "v_mfma_f32_16x16x32_bf16 v[188:191], a[56:59], v[40:43], v[188:191]\n"
      "s_waitcnt lgkmcnt(0)\n"
      "v_mfma_f32_16x16x32_bf16 v[188:191], a[60:63], v[44:47], v[188:191]\n"
      "v_cvt_pk_bf16_f32 v66, v180, v181\n"
      "s_waitcnt vmcnt(12) lgkmcnt(0)\n"
      "s_barrier\n"
      "v_mfma_f32_16x16x32_bf16 v[128:131], v[48:51], a[192:195], 0\n"
      "s_mov_b32 s57, 16384\n"
      "s_add_u32 s52, s48, s57\n"
      "s_add_u32 s57, s53, s57\n"
      "s_mov_b32 m0, s57\n"
      "s_nop 0\n"
      "buffer_load_dwordx4 v1, s[40:43], s52 offen offset:0 lds\n"
      "v_mfma_f32_16x16x32_bf16 v[128:131], v[52:55], a[196:199], v[128:131]\n"
      "ds_write_b64 v253, v[112:113] offset:4096\n"
      "v_mfma_f32_16x16x32_bf16 v[128:131], v[56:59], a[200:203], v[128:131]\n"
      "v_cvt_pk_bf16_f32 v67, v182, v183\n"
      "s_mov_b32 s73, 123904\n"
      "v_mfma_f32_16x16x32_bf16 v[128:131], v[60:63], a[204:207], v[128:131]\n"
      "ds_write_b64 v253, v[114:115] offset:4224\n"
      "v_mfma_f32_16x16x32_bf16 v[132:135], v[48:51], a[208:211], 0\n"
      "v_cvt_pk_bf16_f32 v68, v184, v185\n"
      "v_cvt_pk_bf16_f32 v69, v186, v187\n"
      "v_mfma_f32_16x16x32_bf16 v[132:135], v[52:55], a[212:215], v[132:135]\n"
      "buffer_load_dwordx4 v1, s[40:43], s52 offen offset:2048 lds\n"
      "v_mfma_f32_16x16x32_bf16 v[132:135], v[56:59], a[216:219], v[132:135]\n"
      "ds_write_b64 v253, v[116:117] offset:4352\n"
      "v_mfma_f32_16x16x32_bf16 v[132:135], v[60:63], a[220:223], v[132:135]\n"
      "v_cvt_pk_bf16_f32 v70, v188, v189\n"
      "v_cvt_pk_bf16_f32 v71, v190, v191\n"
      "v_mfma_f32_16x16x32_bf16 v[136:139], v[48:51], a[224:227], 0\n"
      "ds_write_b64 v253, v[118:119] offset:4480\n"
      "v_mfma_f32_16x16x32_bf16 v[136:139], v[52:55], a[228:231], v[136:139]\n"
      "s_mov_b32 s74, 123008\n"
      "s_mov_b32 s75, 119808\n"
      "v_mfma_f32_16x16x32_bf16 v[136:139], v[56:59], a[232:235], v[136:139]\n"
      "s_add_u32 m0, s57, 896\n"
      "s_nop 0\n"
      "buffer_load_dwordx4 v1, s[40:43], s52 offen offset:128 lds\n"
      "v_mfma_f32_16x16x32_bf16 v[136:139], v[60:63], a[236:239], v[136:139]\n"
      "ds_write_b64 v252, v[96:97] offset:0\n"
      "v_mfma_f32_16x16x32_bf16 v[140:143], v[48:51], a[240:243], 0\n"
      "s_mov_b32 s76, 118912\n"
      "s_mov_b32 s77, 115712\n"
      "v_mfma_f32_16x16x32_bf16 v[140:143], v[52:55], a[244:247], v[140:143]\n"
      "ds_write_b64 v252, v[98:99] offset:512\n"
      "v_mfma_f32_16x16x32_bf16 v[140:143], v[56:59], a[248:251], v[140:143]\n"
      "s_mov_b32 s78, 114816\n"
      "s_mov_b32 s79, 128000\n"
      "v_mfma_f32_16x16x32_bf16 v[140:143], v[60:63], a[252:255], v[140:143]\n"
      "buffer_load_dwordx4 v1, s[40:43], s52 offen offset:2176 lds\n"
      "v_mfma_f32_16x16x32_bf16 v[144:147], a[128:131], v[48:51], 0\n"
      "ds_write_b64 v252, v[100:101] offset:2048\n"
      "v_mfma_f32_16x16x32_bf16 v[144:147], a[132:135], v[52:55], v[144:147]\n"
      "v_cvt_pk_bf16_f32 v120, v128, v132\n"
      "v_cvt_pk_bf16_f32 v122, v129, v133\n"
      "v_mfma_f32_16x16x32_bf16 v[144:147], a[136:139], v[56:59], v[144:147]\n"
      "ds_write_b64 v252, v[102:103] offset:2560\n"
      "v_mfma_f32_16x16x32_bf16 v[144:147], a[140:143], v[60:63], v[144:147]\n"
      "v_cvt_pk_bf16_f32 v124, v130, v134\n"
      "v_cvt_pk_bf16_f32 v126, v131, v135\n"
      "v_mfma_f32_16x16x32_bf16 v[148:151], a[144:147], v[48:51], 0\n"
      "s_mov_b32 s57, 4096\n"
      "s_add_u32 s52, s48, s57\n"
      "s_add_u32 s57, s53, s57\n"
      "s_mov_b32 m0, s57\n"
      "s_nop 0\n"
      "buffer_load_dwordx4 v1, s[40:43], s52 offen offset:0 lds\n"
      "v_mfma_f32_16x16x32_bf16 v[148:151], a[148:151], v[52:55], v[148:151]\n"
      "ds_write_b64 v253, v[80:81] offset:0\n"
      "v_mfma_f32_16x16x32_bf16 v[148:151], a[152:155], v[56:59], v[148:151]\n"
      "s_mov_b32 s90, 111616\n"
      "s_mov_b32 s91, 110720\n"
      "v_mfma_f32_16x16x32_bf16 v[148:151], a[156:159], v[60:63], v[148:151]\n"
      "ds_write_b64 v253, v[82:83] offset:128\n"
      "v_mfma_f32_16x16x32_bf16 v[152:155], a[160:163], v[48:51], 0\n"
      "v_cvt_pk_bf16_f32 v121, v136, v140\n"
      "v_cvt_pk_bf16_f32 v123, v137, v141\n"
      "v_mfma_f32_16x16x32_bf16 v[152:155], a[164:167], v[52:55], v[152:155]\n"
      "buffer_load_dwordx4 v1, s[40:43], s52 offen offset:2048 lds\n"
      "v_mfma_f32_16x16x32_bf16 v[152:155], a[168:171], v[56:59], v[152:155]\n"
      "ds_write_b64 v253, v[84:85] offset:256\n"
      "v_mfma_f32_16x16x32_bf16 v[152:155], a[172:175], v[60:63], v[152:155]\n"
      "v_cvt_pk_bf16_f32 v125, v138, v142\n"
      "v_cvt_pk_bf16_f32 v127, v139, v143\n"
      "v_mfma_f32_16x16x32_bf16 v[156:159], a[176:179], v[48:51], 0\n"
      "ds_write_b64 v253, v[86:87] offset:384\n"
      "v_mfma_f32_16x16x32_bf16 v[156:159], a[180:183], v[52:55], v[156:159]\n"
      "v_cvt_pk_bf16_f32 v104, v144, v145\n"
      "v_cvt_pk_bf16_f32 v106, v146, v147\n"
      "v_mfma_f32_16x16x32_bf16 v[156:159], a[184:187], v[56:59], v[156:159]\n"
      "s_add_u32 m0, s57, 896\n"
      "s_nop 0\n"
      "buffer_load_dwordx4 v1, s[40:43], s52 offen offset:128 lds\n"
      "v_mfma_f32_16x16x32_bf16 v[156:159], a[188:191], v[60:63], v[156:159]\n"
      "ds_write_b128 v251, v[64:67] offset:0\n"
      "v_mfma_f32_16x16x32_bf16 v[160:163], v[48:51], a[64:67], 0\n"
      "v_cvt_pk_bf16_f32 v105, v148, v149\n"
      "v_cvt_pk_bf16_f32 v107, v150, v151\n"
      "v_mfma_f32_16x16x32_bf16 v[160:163], v[52:55], a[68:71], v[160:163]\n"
      "ds_write_b128 v251, v[68:71] offset:2048\n"
      "v_mfma_f32_16x16x32_bf16 v[160:163], v[56:59], a[72:75], v[160:163]\n"
      "s_mov_b32 s92, 107520\n"
      "s_mov_b32 s93, 106624\n"
      "v_mfma_f32_16x16x32_bf16 v[160:163], v[60:63], a[76:79], v[160:163]\n"
      "buffer_load_dwordx4 v1, s[40:43], s52 offen offset:2176 lds\n"
      "v_mfma_f32_16x16x32_bf16 v[164:167], v[48:51], a[80:83], 0\n"
      "ds_write_b64 v253, v[120:121] offset:6144\n"
      "v_mfma_f32_16x16x32_bf16 v[164:167], v[52:55], a[84:87], v[164:167]\n"
      "v_cvt_pk_bf16_f32 v108, v152, v153\n"
      "v_cvt_pk_bf16_f32 v110, v154, v155\n"
      "v_mfma_f32_16x16x32_bf16 v[164:167], v[56:59], a[88:91], v[164:167]\n"
      "ds_write_b64 v253, v[122:123] offset:6272\n"
      "v_mfma_f32_16x16x32_bf16 v[164:167], v[60:63], a[92:95], v[164:167]\n"
      "v_cvt_pk_bf16_f32 v109, v156, v157\n"
      "v_cvt_pk_bf16_f32 v111, v158, v159\n"
      "v_mfma_f32_16x16x32_bf16 v[168:171], v[48:51], a[96:99], 0\n"
      "s_mov_b32 s57, 0\n"
      "s_add_u32 s52, s48, s57\n"
      "s_add_u32 s57, s53, s57\n"
      "s_mov_b32 m0, s57\n"
      "s_nop 0\n"
      "buffer_load_dwordx4 v1, s[40:43], s52 offen offset:0 lds\n"
      "v_mfma_f32_16x16x32_bf16 v[168:171], v[52:55], a[100:103], v[168:171]\n"
      "ds_write_b64 v253, v[124:125] offset:6400\n"
      "v_mfma_f32_16x16x32_bf16 v[168:171], v[56:59], a[104:107], v[168:171]\n"
      "s_mov_b32 s94, 103424\n"
      "s_mov_b32 s95, 102528\n"
      "v_mfma_f32_16x16x32_bf16 v[168:171], v[60:63], a[108:111], v[168:171]\n"
      "ds_write_b64 v253, v[126:127] offset:6528\n"
      "v_mfma_f32_16x16x32_bf16 v[172:175], v[48:51], a[112:115], 0\n"
      "v_cvt_pk_bf16_f32 v88, v160, v161\n"
      "v_cvt_pk_bf16_f32 v89, v162, v163\n"
      "v_mfma_f32_16x16x32_bf16 v[172:175], v[52:55], a[116:119], v[172:175]\n"
      "buffer_load_dwordx4 v1, s[40:43], s52 offen offset:2048 lds\n"
      "v_mfma_f32_16x16x32_bf16 v[172:175], v[56:59], a[120:123], v[172:175]\n"
      "ds_write_b64 v252, v[104:105] offset:256\n"
      "v_mfma_f32_16x16x32_bf16 v[172:175], v[60:63], a[124:127], v[172:175]\n"
      "v_cvt_pk_bf16_f32 v90, v164, v165\n"
      "v_cvt_pk_bf16_f32 v91, v166, v167\n"
      "v_mfma_f32_16x16x32_bf16 v[176:179], a[0:3], v[48:51], 0\n"
      "ds_write_b64 v252, v[106:107] offset:768\n"
      "v_mfma_f32_16x16x32_bf16 v[176:179], a[4:7], v[52:55], v[176:179]\n"
      "s_mov_b32 s100, 13312\n"
      "s_mov_b32 s101, 9216\n"
      "v_mfma_f32_16x16x32_bf16 v[176:179], a[8:11], v[56:59], v[176:179]\n"
      "s_add_u32 m0, s57, 896\n"
      "s_nop 0\n"
      "buffer_load_dwordx4 v1, s[40:43], s52 offen offset:128 lds\n"
      "v_mfma_f32_16x16x32_bf16 v[176:179], a[12:15], v[60:63], v[176:179]\n"
      "ds_write_b64 v252, v[108:109] offset:2304\n"
      "v_mfma_f32_16x16x32_bf16 v[180:183], a[16:19], v[48:51], 0\n"
      "v_cvt_pk_bf16_f32 v92, v168, v169\n"
      "v_cvt_pk_bf16_f32 v93, v170, v171\n"
      "v_mfma_f32_16x16x32_bf16 v[180:183], a[20:23], v[52:55], v[180:183]\n"
      "ds_write_b64 v252, v[110:111] offset:2816\n"
      "v_mfma_f32_16x16x32_bf16 v[180:183], a[24:27], v[56:59], v[180:183]\n"
      "s_mov_b32 s34, 5120\n"
      "s_mov_b32 s35, 1024\n"
      "v_mfma_f32_16x16x32_bf16 v[180:183], a[28:31], v[60:63], v[180:183]\n"
      "buffer_load_dwordx4 v1, s[40:43], s52 offen offset:2176 lds\n"
      "v_mfma_f32_16x16x32_bf16 v[184:187], a[32:35], v[48:51], 0\n"
      "ds_write_b64 v253, v[88:89] offset:2048\n"
      "v_mfma_f32_16x16x32_bf16 v[184:187], a[36:39], v[52:55], v[184:187]\n"
      "v_cvt_pk_bf16_f32 v94, v172, v173\n"
      "v_cvt_pk_bf16_f32 v95, v174, v175\n"
      "v_mfma_f32_16x16x32_bf16 v[184:187], a[40:43], v[56:59], v[184:187]\n"
      "ds_write_b64 v253, v[90:91] offset:2176\n"
      "v_mfma_f32_16x16x32_bf16 v[184:187], a[44:47], v[60:63], v[184:187]\n"
      "v_cvt_pk_bf16_f32 v72, v176, v177\n"
      "v_cvt_pk_bf16_f32 v73, v178, v179\n"
      "v_mfma_f32_16x16x32_bf16 v[188:191], a[48:51], v[48:51], 0\n"
      "ds_write_b64 v253, v[92:93] offset:2304\n"
      "v_mfma_f32_16x16x32_bf16 v[188:191], a[52:55], v[52:55], v[188:191]\n"
      "v_mfma_f32_16x16x32_bf16 v[188:191], a[56:59], v[56:59], v[188:191]\n"
      "ds_write_b64 v253, v[94:95] offset:2432\n"
      "v_mfma_f32_16x16x32_bf16 v[188:191], a[60:63], v[60:63], v[188:191]\n"
      "v_cvt_pk_bf16_f32 v74, v180, v181\n"
      "v_cvt_pk_bf16_f32 v75, v182, v183\n"
      "s_sub_u32 s54, s48, 1024\n"
      "s_ashr_i32 s55, s54, 31\n"
      "s_add_u32 s40, s40, s54\n"
      "s_addc_u32 s41, s41, s55\n"
      "s_mov_b32 s42, 743424\n"
      "s_sub_u32 s54, s54, 102400\n"
      "s_ashr_i32 s55, s54, 31\n"
      "s_add_u32 s44, s44, s54\n"
      "s_addc_u32 s45, s45, s55\n"
      "s_mov_b32 s46, 129024\n"
      "s_mov_b32 s38, 7\n"
      "ds_write_b128 v251, v[72:75] offset:256\n"
      "v_cvt_pk_bf16_f32 v76, v184, v185\n"
      "v_cvt_pk_bf16_f32 v77, v186, v187\n"
      "v_cvt_pk_bf16_f32 v78, v188, v189\n"
      "v_cvt_pk_bf16_f32 v79, v190, v191\n"
      "ds_write_b128 v251, v[76:79] offset:2304\n"
      "s_nop 1\n"
      "s_waitcnt vmcnt(20)\n"
      "ds_read_b128 v[78:81], v0 offset:8192\n"
      "ds_read_b128 v[150:153], v0 offset:9216\n"
      "ds_read_b128 v[138:141], v0 offset:8704\n"
      "ds_read_b128 v[102:105], v0 offset:9728\n"
      "s_waitcnt vmcnt(16)\n"
      "ds_read_b128 v[208:211], v0 offset:20480\n"
      "ds_read_b128 v[66:69], v0 offset:20992\n"
      "s_waitcnt vmcnt(12)\n"
      "ds_read_b128 v[18:21], v0 offset:12288\n"
      "s_waitcnt lgkmcnt(6)\n"
      "v_mfma_f32_16x16x32_bf16 v[110:113], a[48:51], v[78:81], 0\n"
      "s_waitcnt lgkmcnt(4)\n"
      "v_mfma_f32_16x16x32_bf16 v[110:113], a[52:55], v[138:141], v[110:113]\n"
      "ds_read_b128 v[192:195], v10 offset:0\n"
      "ds_read_b128 v[62:65], v0 offset:22016\n"
      "v_mfma_f32_16x16x32_bf16 v[110:113], a[56:59], v[150:153], v[110:113]\n"
      "ds_read_b128 v[82:85], v0 offset:21504\n"
      "s_waitcnt lgkmcnt(6)\n"
      "v_mfma_f32_16x16x32_bf16 v[110:113], a[60:63], v[102:105], v[110:113]\n"
      "v_mfma_f32_16x16x32_bf16 v[182:185], a[0:3], v[78:81], 0\n"
      "v_mfma_f32_16x16x32_bf16 v[182:185], a[4:7], v[138:141], v[182:185]\n"
      "v_mfma_f32_16x16x32_bf16 v[182:185], a[8:11], v[150:153], v[182:185]\n"
      "v_mfma_f32_16x16x32_bf16 v[182:185], a[12:15], v[102:105], v[182:185]\n"
      "v_mfma_f32_16x16x32_bf16 v[156:159], a[32:35], v[78:81], 0\n"
      "v_mfma_f32_16x16x32_bf16 v[156:159], a[36:39], v[138:141], v[156:159]\n"
      "v_mfma_f32_16x16x32_bf16 v[156:159], a[40:43], v[150:153], v[156:159]\n"
      "v_mfma_f32_16x16x32_bf16 v[156:159], a[44:47], v[102:105], v[156:159]\n"
      "s_nop 3\n"
      "v_cvt_pk_bf16_f32 v87, v184, v185\n"
      "v_mfma_f32_16x16x32_bf16 v[184:187], v[78:81], a[64:67], 0\n"
      "v_mfma_f32_16x16x32_bf16 v[184:187], v[138:141], a[68:71], v[184:187]\n"
      "s_add_u32 m0, s53, 20480\n"
      "v_mfma_f32_16x16x32_bf16 v[184:187], v[150:153], a[72:75], v[184:187]\n"
      "v_mfma_f32_16x16x32_bf16 v[184:187], v[102:105], a[76:79], v[184:187]\n"
      "v_mfma_f32_16x16x32_bf16 v[142:145], v[78:81], a[80:83], 0\n"
      "v_mfma_f32_16x16x32_bf16 v[142:145], v[138:141], a[84:87], v[142:145]\n"
      "v_mfma_f32_16x16x32_bf16 v[142:145], v[150:153], a[88:91], v[142:145]\n"
      "v_mfma_f32_16x16x32_bf16 v[142:145], v[102:105], a[92:95], v[142:145]\n"
      "v_mfma_f32_16x16x32_bf16 v[188:191], v[78:81], a[96:99], 0\n"
      "v_mfma_f32_16x16x32_bf16 v[188:191], v[138:141], a[100:103], v[188:191]\n"
      "v_mfma_f32_16x16x32_bf16 v[188:191], v[150:153], a[104:107], v[188:191]\n"
      "s_waitcnt lgkmcnt(0)\n"
      "buffer_load_dwordx4 v1, s[40:43], s74 offen offset:1024 lds\n"
      "v_mfma_f32_16x16x32_bf16 v[188:191], v[102:105], a[108:111], v[188:191]\n"
      "v_cvt_pk_bf16_f32 v91, v158, v159\n"
      "v_mfma_f32_16x16x32_bf16 v[236:239], a[160:163], v[208:211], 0\n"
      "v_mfma_f32_16x16x32_bf16 v[236:239], a[164:167], v[66:69], v[236:239]\n"
      "v_mfma_f32_16x16x32_bf16 v[236:239], a[168:171], v[82:85], v[236:239]\n"
      "v_cvt_pk_bf16_f32 v90, v156, v157\n"
      "v_mfma_f32_16x16x32_bf16 v[236:239], a[172:175], v[62:65], v[236:239]\n"
      "v_mfma_f32_16x16x32_bf16 v[134:137], v[78:81], a[112:115], 0\n"
      "v_mfma_f32_16x16x32_bf16 v[134:137], v[138:141], a[116:119], v[134:137]\n"
      "v_mfma_f32_16x16x32_bf16 v[134:137], v[150:153], a[120:123], v[134:137]\n"
      "v_mfma_f32_16x16x32_bf16 v[134:137], v[102:105], a[124:127], v[134:137]\n"
      "buffer_load_dwordx4 v1, s[40:43], s74 offen offset:3072 lds\n"
      "v_mfma_f32_16x16x32_bf16 v[42:45], a[16:19], v[78:81], 0\n"
      "v_mfma_f32_16x16x32_bf16 v[42:45], a[20:23], v[138:141], v[42:45]\n"
      "v_mfma_f32_16x16x32_bf16 v[42:45], a[24:27], v[150:153], v[42:45]\n"
      "v_mfma_f32_16x16x32_bf16 v[42:45], a[28:31], v[102:105], v[42:45]\n"
      "v_mfma_f32_16x16x32_bf16 v[102:105], a[32:35], v[208:211], 0\n"
      "v_mfma_f32_16x16x32_bf16 v[102:105], a[36:39], v[66:69], v[102:105]\n"
      "ds_read_b128 v[78:81], v0 offset:13824\n"
      "v_mfma_f32_16x16x32_bf16 v[102:105], a[40:43], v[82:85], v[102:105]\n"
      "v_mfma_f32_16x16x32_bf16 v[102:105], a[44:47], v[62:65], v[102:105]\n"
      "v_mfma_f32_16x16x32_bf16 v[46:49], a[128:131], v[208:211], 0\n"
      "v_mfma_f32_16x16x32_bf16 v[46:49], a[132:135], v[66:69], v[46:49]\n"
      "v_mfma_f32_16x16x32_bf16 v[46:49], a[136:139], v[82:85], v[46:49]\n"
      "v_mfma_f32_16x16x32_bf16 v[46:49], a[140:143], v[62:65], v[46:49]\n"
      "v_mfma_f32_16x16x32_bf16 v[106:109], a[48:51], v[208:211], 0\n"
      "s_nop 2\n"
      "v_cvt_pk_bf16_f32 v216, v102, v103\n"
      "v_mfma_f32_16x16x32_bf16 v[106:109], a[52:55], v[66:69], v[106:109]\n"
      "ds_read_b128 v[178:181], v0 offset:13312\n"
      "v_mfma_f32_16x16x32_bf16 v[106:109], a[56:59], v[82:85], v[106:109]\n"
      "v_cvt_pk_bf16_f32 v139, v238, v239\n"
      "v_cvt_pk_bf16_f32 v217, v104, v105\n"
      "v_mfma_f32_16x16x32_bf16 v[106:109], a[60:63], v[62:65], v[106:109]\n"
      "v_mfma_f32_16x16x32_bf16 v[114:117], a[0:3], v[208:211], 0\n"
      "ds_read_b128 v[150:153], v9 offset:0\n"
      "v_mfma_f32_16x16x32_bf16 v[114:117], a[4:7], v[66:69], v[114:117]\n"
      "buffer_load_dwordx4 v1, s[40:43], s73 offen offset:0 lds\n"
      "v_mfma_f32_16x16x32_bf16 v[114:117], a[8:11], v[82:85], v[114:117]\n"
      "v_mfma_f32_16x16x32_bf16 v[114:117], a[12:15], v[62:65], v[114:117]\n"
      "s_waitcnt vmcnt(11)\n"
      "ds_read_b128 v[102:105], v0 offset:16384\n"
      "ds_read_b128 v[154:157], v9 offset:2048\n"
      "v_cvt_pk_bf16_f32 v219, v108, v109\n"
      "ds_read_b128 v[238:241], v0 offset:12800\n"
      "v_mfma_f32_16x16x32_bf16 v[244:247], a[16:19], v[208:211], 0\n"
      "v_mfma_f32_16x16x32_bf16 v[244:247], a[20:23], v[66:69], v[244:247]\n"
      "s_waitcnt vmcnt(7)\n"
      "ds_read_b128 v[22:25], v0 offset:4096\n"
      "v_cvt_pk_bf16_f32 v213, v116, v117\n"
      "v_mfma_f32_16x16x32_bf16 v[244:247], a[24:27], v[82:85], v[244:247]\n"
      "v_mfma_f32_16x16x32_bf16 v[244:247], a[28:31], v[62:65], v[244:247]\n"
      "ds_read_b128 v[158:161], v0 offset:17408\n"
      "v_cvt_pk_bf16_f32 v249, v190, v191\n"
      "v_cvt_pk_bf16_f32 v248, v188, v189\n"
      "v_cvt_pk_bf16_f32 v93, v112, v113\n"
      "v_mov_b64 v[168:169], 0\n"
      "s_waitcnt lgkmcnt(3)\n"
      "s_nop 0\n"
      "v_smfmac_f32_16x16x64_bf16 v[168:171], v[192:195], v[150:157], v5\n"
      "v_cvt_pk_bf16_f32 v215, v246, v247\n"
      "v_mfma_f32_16x16x32_bf16 v[98:101], v[18:21], a[96:99], 0\n"
      "v_cvt_pk_bf16_f32 v55, v144, v145\n"
      "s_waitcnt lgkmcnt(2)\n"
      "v_mfma_f32_16x16x32_bf16 v[98:101], v[238:241], a[100:103], v[98:101]\n"
      "v_mfma_f32_16x16x32_bf16 v[98:101], v[178:181], a[104:107], v[98:101]\n"
      "s_nop 1\n"
      "v_add_f32 v168, v168, v169\n"
      "v_mfma_f32_16x16x32_bf16 v[98:101], v[78:81], a[108:111], v[98:101]\n"
      "buffer_load_dwordx4 v1, s[40:43], s73 offen offset:2048 lds\n"
      "v_mfma_f32_16x16x32_bf16 v[196:199], a[176:179], v[208:211], 0\n"
      "v_cvt_pk_bf16_f32 v214, v244, v245\n"
      "v_mov_b32 v169, 0\n"
      "v_mfma_f32_16x16x32_bf16 v[196:199], a[180:183], v[66:69], v[196:199]\n"
      "v_mfma_f32_16x16x32_bf16 v[196:199], a[184:187], v[82:85], v[196:199]\n"
      "v_cvt_pk_bf16_f32 v88, v42, v43\n"
      "v_cvt_pk_bf16_f32 v212, v114, v115\n"
      "v_mfma_f32_16x16x32_bf16 v[196:199], a[188:191], v[62:65], v[196:199]\n"
      "v_cvt_pk_bf16_f32 v86, v182, v183\n"
      "v_mfma_f32_16x16x32_bf16 v[172:175], a[48:51], v[18:21], 0\n"
      "v_mfma_f32_16x16x32_bf16 v[172:175], a[52:55], v[238:241], v[172:175]\n"
      "ds_read_b128 v[114:117], v0 offset:17920\n"
      "v_mfma_f32_16x16x32_bf16 v[172:175], a[56:59], v[178:181], v[172:175]\n"
      "v_mfma_f32_16x16x32_bf16 v[172:175], a[60:63], v[78:81], v[172:175]\n"
      "v_mov_b64 v[242:243], 0\n"
      "v_mfma_f32_16x16x32_bf16 v[164:167], a[16:19], v[18:21], 0\n"
      "v_cvt_pk_bf16_f32 v54, v142, v143\n"
      "v_cvt_pk_bf16_f32 v250, v98, v99\n"
      "v_mfma_f32_16x16x32_bf16 v[164:167], a[20:23], v[238:241], v[164:167]\n"
      "ds_read_b128 v[154:157], v0 offset:16896\n"
      "v_cvt_pk_bf16_f32 v89, v44, v45\n"
      "v_mfma_f32_16x16x32_bf16 v[164:167], a[24:27], v[178:181], v[164:167]\n"
      "v_mfma_f32_16x16x32_bf16 v[164:167], a[28:31], v[78:81], v[164:167]\n"
      "v_mfma_f32_16x16x32_bf16 v[244:247], a[144:147], v[208:211], 0\n"
      "v_mfma_f32_16x16x32_bf16 v[244:247], a[148:151], v[66:69], v[244:247]\n"
      "v_mfma_f32_16x16x32_bf16 v[244:247], a[152:155], v[82:85], v[244:247]\n"
      "v_mfma_f32_16x16x32_bf16 v[244:247], a[156:159], v[62:65], v[244:247]\n"
      "v_cvt_pk_bf16_f32 v141, v198, v199\n"
      "v_mfma_f32_16x16x32_bf16 v[200:203], a[0:3], v[18:21], 0\n"
      "v_cvt_pk_bf16_f32 v98, v134, v135\n"
      "v_mfma_f32_16x16x32_bf16 v[200:203], a[4:7], v[238:241], v[200:203]\n"
      "v_cvt_pk_bf16_f32 v205, v186, v187\n"
      "v_mfma_f32_16x16x32_bf16 v[200:203], a[8:11], v[178:181], v[200:203]\n"
      "v_cvt_pk_bf16_f32 v44, v164, v165\n"
      "v_mfma_f32_16x16x32_bf16 v[200:203], a[12:15], v[78:81], v[200:203]\n"
      "v_mfma_f32_16x16x32_bf16 v[188:191], a[32:35], v[18:21], 0\n"
      "v_mfma_f32_16x16x32_bf16 v[188:191], a[36:39], v[238:241], v[188:191]\n"
      "ds_read_b128 v[150:153], v0 offset:5632\n"
      "v_mfma_f32_16x16x32_bf16 v[188:191], a[40:43], v[178:181], v[188:191]\n"
      "ds_read_b128 v[94:97], v0 offset:4608\n"
      "v_mfma_f32_16x16x32_bf16 v[188:191], a[44:47], v[78:81], v[188:191]\n"
      "v_mfma_f32_16x16x32_bf16 v[142:145], v[18:21], a[80:83], 0\n"
      "v_cvt_pk_bf16_f32 v92, v110, v111\n"
      "v_cvt_pk_bf16_f32 v140, v196, v197\n"
      "v_mfma_f32_16x16x32_bf16 v[142:145], v[238:241], a[84:87], v[142:145]\n"
      "v_mfma_f32_16x16x32_bf16 v[142:145], v[178:181], a[88:91], v[142:145]\n"
      "v_cvt_pk_bf16_f32 v99, v136, v137\n"
      "v_mov_b64 v[164:165], 0\n"
      "v_mfma_f32_16x16x32_bf16 v[142:145], v[78:81], a[92:95], v[142:145]\n"
      "v_cvt_pk_bf16_f32 v134, v46, v47\n"
      "v_cvt_pk_bf16_f32 v136, v244, v245\n"
      "ds_read_b128 v[34:37], v0 offset:5120\n"
      "s_add_u32 m0, s53, 16384\n"
      "v_cvt_pk_bf16_f32 v137, v246, v247\n"
      "v_cvt_pk_bf16_f32 v135, v48, v49\n"
      "v_exp_f32_dpp v169, v168 quad_perm:[0,1,2,3] row_mask:0xf bank_mask:0x1\n"
      "v_cvt_pk_bf16_f32 v218, v106, v107\n"
      "s_nop 1\n"
      "v_smfmac_f32_16x16x64_bf16 v[242:245], v[192:195], v[212:219], v5\n"
      "v_cvt_pk_bf16_f32 v56, v142, v143\n"
      "s_waitcnt lgkmcnt(6)\n"
      "v_mfma_f32_16x16x32_bf16 v[40:43], a[16:19], v[22:25], 0\n"
      "v_cvt_pk_bf16_f32 v47, v190, v191\n"
      "s_waitcnt lgkmcnt(1)\n"
      "v_mfma_f32_16x16x32_bf16 v[40:43], a[20:23], v[94:97], v[40:43]\n"
      "s_waitcnt lgkmcnt(0)\n"
      "v_mfma_f32_16x16x32_bf16 v[40:43], a[24:27], v[34:37], v[40:43]\n"
      "v_cvt_pk_bf16_f32 v49, v174, v175\n"
      "v_mfma_f32_16x16x32_bf16 v[40:43], a[28:31], v[150:153], v[40:43]\n"
      "v_cvt_pk_bf16_f32 v138, v236, v237\n"
      "v_mfma_f32_16x16x32_bf16 v[130:133], v[86:89], v[134:137], 0\n"
      "buffer_load_dwordx4 v1, s[40:43], s75 offen offset:2048 lds\n"
      "v_mfma_f32_16x16x32_bf16 v[130:133], v[90:93], v[138:141], v[130:133]\n"
      "v_mfma_f32_16x16x32_bf16 v[50:53], v[212:215], v[134:137], 0\n"
      "v_mfma_f32_16x16x32_bf16 v[50:53], v[216:219], v[138:141], v[50:53]\n"
      "v_mov_b32_dpp v168, v169 quad_perm:[1,1,3,3] row_mask:0xf bank_mask:0xf\n"
      "v_mfma_f32_16x16x32_bf16 v[244:247], a[0:3], v[22:25], 0\n"
      "v_mfma_f32_16x16x32_bf16 v[244:247], a[4:7], v[94:97], v[244:247]\n"
      "v_mfma_f32_16x16x32_bf16 v[244:247], a[8:11], v[34:37], v[244:247]\n"
      "v_mfma_f32_16x16x32_bf16 v[244:247], a[12:15], v[150:153], v[244:247]\n"
      "v_cvt_pk_bf16_f32 v48, v172, v173\n"
      "s_nop 1\n"
      "v_exp_f32 v53, v53\n"
      "v_exp_f32 v133, v133\n"
      "v_exp_f32 v52, v52\n"
      "v_cvt_pk_bf16_f32 v173, v42, v43\n"
      "v_cvt_pk_bf16_f32 v61, v52, v53\n"
      "v_cvt_pk_bf16_f32 v42, v200, v201\n"
      "v_cvt_pk_bf16_f32 v168, v169, v168\n"
      "v_exp_f32 v131, v131\n"
      "v_mfma_f32_16x16x32_bf16 v[124:127], a[48:51], v[22:25], 0\n"
      "v_mfma_f32_16x16x32_bf16 v[124:127], a[52:55], v[94:97], v[124:127]\n"
      "v_mfma_f32_16x16x32_bf16 v[124:127], a[56:59], v[34:37], v[124:127]\n"
      "buffer_load_dwordx4 v1, s[40:43], s75 offen offset:0 lds\n"
      "v_mfma_f32_16x16x32_bf16 v[124:127], a[60:63], v[150:153], v[124:127]\n"
      "ds_read_b64 v[182:183], v11 offset:0\n"
      "v_mfma_f32_16x16x32_bf16 v[196:199], v[22:25], a[80:83], 0\n"
      "v_mfma_f32_16x16x32_bf16 v[196:199], v[94:97], a[84:87], v[196:199]\n"
      "v_cvt_pk_bf16_f32 v46, v188, v189\n"
      "v_cvt_pk_bf16_f32 v172, v40, v41\n"
      "v_mfma_f32_16x16x32_bf16 v[196:199], v[34:37], a[88:91], v[196:199]\n"
      "v_cvt_pk_bf16_f32 v45, v166, v167\n"
      "v_mfma_f32_16x16x32_bf16 v[196:199], v[150:153], a[92:95], v[196:199]\n"
      "v_mfma_f32_16x16x32_bf16 v[110:113], a[32:35], v[22:25], 0\n"
      "v_mfma_f32_16x16x32_bf16 v[110:113], a[36:39], v[94:97], v[110:113]\n"
      "v_cvt_pk_bf16_f32 v251, v100, v101\n"
      "v_cvt_pk_bf16_f32 v176, v124, v125\n"
      "v_mfma_f32_16x16x32_bf16 v[110:113], a[40:43], v[34:37], v[110:113]\n"
      "s_waitcnt vmcnt(6)\n"
      "ds_read_b128 v[188:191], v0 offset:0\n"
      "ds_read_b128 v[106:109], v0 offset:1024\n"
      "v_mfma_f32_16x16x32_bf16 v[110:113], a[44:47], v[150:153], v[110:113]\n"
      "v_mfma_f32_16x16x32_bf16 v[120:123], v[22:25], a[96:99], 0\n"
      "v_cvt_pk_bf16_f32 v43, v202, v203\n"
      "v_cvt_pk_bf16_f32 v52, v196, v197\n"
      "v_mfma_f32_16x16x32_bf16 v[120:123], v[94:97], a[100:103], v[120:123]\n"
      "v_cvt_pk_bf16_f32 v177, v126, v127\n"
      "v_mfma_f32_16x16x32_bf16 v[120:123], v[34:37], a[104:107], v[120:123]\n"
      "v_exp_f32 v51, v51\n"
      "v_mfma_f32_16x16x32_bf16 v[120:123], v[150:153], a[108:111], v[120:123]\n"
      "ds_read_b128 v[146:149], v0 offset:1536\n"
      "v_mfma_f32_16x16x32_bf16 v[74:77], v[22:25], a[64:67], 0\n"
      "v_mfma_f32_16x16x32_bf16 v[74:77], v[94:97], a[68:71], v[74:77]\n"
      "v_exp_f32 v50, v50\n"
      "v_mfma_f32_16x16x32_bf16 v[74:77], v[34:37], a[72:75], v[74:77]\n"
      "v_mfma_f32_16x16x32_bf16 v[74:77], v[150:153], a[76:79], v[74:77]\n"
      "v_cvt_pk_bf16_f32 v171, v246, v247\n"
      "v_smfmac_f32_16x16x64_bf16 v[164:167], v[192:195], v[86:93], v5\n"
      "v_mfma_f32_16x16x32_bf16 v[70:73], v[42:45], v[134:137], 0\n"
      "v_mfma_f32_16x16x32_bf16 v[70:73], v[46:49], v[138:141], v[70:73]\n"
      "v_cvt_pk_bf16_f32 v175, v112, v113\n"
      "v_cvt_pk_bf16_f32 v247, v122, v123\n"
      "v_mfma_f32_16x16x32_bf16 v[38:41], v[22:25], a[112:115], 0\n"
      "v_mfma_f32_16x16x32_bf16 v[38:41], v[94:97], a[116:119], v[38:41]\n"
      "ds_read_b128 v[230:233], v0 offset:512\n"
      "v_cvt_pk_bf16_f32 v57, v144, v145\n"
      "v_mfma_f32_16x16x32_bf16 v[38:41], v[34:37], a[120:123], v[38:41]\n"
      "v_mov_b64 v[124:125], 0\n"
      "v_cvt_pk_bf16_f32 v170, v244, v245\n"
      "v_mfma_f32_16x16x32_bf16 v[38:41], v[150:153], a[124:127], v[38:41]\n"
      "v_cvt_pk_bf16_f32 v202, v74, v75\n"
      "v_mfma_f32_16x16x32_bf16 v[126:129], v[18:21], a[112:115], 0\n"
      "v_cvt_pk_bf16_f32 v174, v110, v111\n"
      "v_cvt_pk_bf16_f32 v204, v184, v185\n"
      "v_mfma_f32_16x16x32_bf16 v[126:129], v[238:241], a[116:119], v[126:129]\n"
      "v_mfma_f32_16x16x32_bf16 v[126:129], v[178:181], a[120:123], v[126:129]\n"
      "v_mfma_f32_16x16x32_bf16 v[126:129], v[78:81], a[124:127], v[126:129]\n"
      "v_cvt_pk_bf16_f32 v203, v76, v77\n"
      "v_cvt_pk_bf16_f32 v53, v198, v199\n"
      "v_mfma_f32_16x16x32_bf16 v[110:113], v[170:173], v[134:137], 0\n"
      "buffer_load_dwordx4 v1, s[40:43], s76 offen offset:1024 lds\n"
      "v_mfma_f32_16x16x32_bf16 v[110:113], v[174:177], v[138:141], v[110:113]\n"
      "s_waitcnt lgkmcnt(3)\n"
      "v_mfma_f32_16x16x32_bf16 v[184:187], v[188:191], a[64:67], 0\n"
      "s_nop 0\n"
      "v_cvt_pk_bf16_f32 v101, v128, v129\n"
      "s_waitcnt lgkmcnt(0)\n"
      "v_mfma_f32_16x16x32_bf16 v[184:187], v[230:233], a[68:71], v[184:187]\n"
      "v_add_f32 v164, v164, v165\n"
      "v_mfma_f32_16x16x32_bf16 v[184:187], v[106:109], a[72:75], v[184:187]\n"
      "v_exp_f32 v130, v130\n"
      "v_mfma_f32_16x16x32_bf16 v[184:187], v[146:149], a[76:79], v[184:187]\n"
      "v_exp_f32 v111, v111\n"
      "v_mfma_f32_16x16x32_bf16 v[22:25], a[48:51], v[188:191], 0\n"
      "v_mfma_f32_16x16x32_bf16 v[22:25], a[52:55], v[230:233], v[22:25]\n"
      "v_add_f32 v242, v242, v243\n"
      "v_mfma_f32_16x16x32_bf16 v[22:25], a[56:59], v[106:109], v[22:25]\n"
      "v_mov_b64 v[196:197], 0\n"
      "v_mfma_f32_16x16x32_bf16 v[22:25], a[60:63], v[146:149], v[22:25]\n"
      "s_nop 0\n"
      "v_cvt_pk_bf16_f32 v201, v186, v187\n"
      "v_cvt_pk_bf16_f32 v200, v184, v185\n"
      "v_mfma_f32_16x16x32_bf16 v[94:97], v[188:191], a[96:99], 0\n"
      "ds_bpermute_b32 v144, v7, v168 offset:248\n"
      "v_mfma_f32_16x16x32_bf16 v[94:97], v[230:233], a[100:103], v[94:97]\n"
      "v_exp_f32 v132, v132\n"
      "v_mfma_f32_16x16x32_bf16 v[94:97], v[106:109], a[104:107], v[94:97]\n"
      "v_cvt_pk_bf16_f32 v100, v126, v127\n"
      "v_cvt_pk_bf16_f32 v60, v50, v51\n"
      "v_mfma_f32_16x16x32_bf16 v[94:97], v[146:149], a[108:111], v[94:97]\n"
      "v_exp_f32 v112, v112\n"
      "v_smfmac_f32_16x16x64_bf16 v[124:127], v[192:195], v[42:49], v5\n"
      "v_mfma_f32_16x16x32_bf16 v[184:187], v[188:191], a[112:115], 0\n"
      "v_mfma_f32_16x16x32_bf16 v[184:187], v[230:233], a[116:119], v[184:187]\n"
      "v_mfma_f32_16x16x32_bf16 v[184:187], v[106:109], a[120:123], v[184:187]\n"
      "v_exp_f32 v73, v73\n"
      "v_mfma_f32_16x16x32_bf16 v[184:187], v[146:149], a[124:127], v[184:187]\n"
      "buffer_load_dwordx4 v1, s[40:43], s76 offen offset:3072 lds\n"
      "v_mfma_f32_16x16x32_bf16 v[32:35], v[188:191], a[80:83], 0\n"
      "v_exp_f32 v72, v72\n"
      "v_mfma_f32_16x16x32_bf16 v[32:35], v[230:233], a[84:87], v[32:35]\n"
      "v_cvt_pk_bf16_f32 v244, v94, v95\n"
      "v_cvt_pk_bf16_f32 v245, v96, v97\n"
      "v_mfma_f32_16x16x32_bf16 v[32:35], v[106:109], a[88:91], v[32:35]\n"
      "v_cvt_pk_bf16_f32 v246, v120, v121\n"
      "v_cvt_pk_bf16_f32 v77, v72, v73\n"
      "v_mfma_f32_16x16x32_bf16 v[32:35], v[146:149], a[92:95], v[32:35]\n"
      "v_cvt_pk_bf16_f32 v94, v184, v185\n"
      "v_mfma_f32_16x16x32_bf16 v[120:123], a[0:3], v[188:191], 0\n"
      "v_add_f32 v124, v124, v125\n"
      "v_mfma_f32_16x16x32_bf16 v[120:123], a[4:7], v[230:233], v[120:123]\n"
      "v_exp_f32 v113, v113\n"
      "v_mfma_f32_16x16x32_bf16 v[120:123], a[8:11], v[106:109], v[120:123]\n"
      "v_exp_f32 v164, v164\n"
      "v_mfma_f32_16x16x32_bf16 v[120:123], a[12:15], v[146:149], v[120:123]\n"
      "ds_read2_b32 v[252:253], v16 offset0:0 offset1:2\n"
      "v_mfma_f32_16x16x32_bf16 v[126:129], a[32:35], v[188:191], 0\n"
      "ds_read2_b32 v[254:255], v16 offset0:128 offset1:130\n"
      "v_cvt_pk_bf16_f32 v50, v32, v33\n"
      "v_mfma_f32_16x16x32_bf16 v[126:129], a[36:39], v[230:233], v[126:129]\n"
      "v_exp_f32 v71, v71\n"
      "v_mfma_f32_16x16x32_bf16 v[126:129], a[40:43], v[106:109], v[126:129]\n"
      "v_cvt_pk_bf16_f32 v51, v34, v35\n"
      "v_cvt_pk_bf16_f32 v95, v186, v187\n"
      "v_mfma_f32_16x16x32_bf16 v[126:129], a[44:47], v[146:149], v[126:129]\n"
      "s_add_u32 m0, s53, 12288\n"
      "v_cvt_pk_bf16_f32 v73, v112, v113\n"
      "v_cvt_pk_bf16_f32 v75, v132, v133\n"
      "v_mfma_f32_16x16x32_bf16 v[150:153], a[16:19], v[188:191], 0\n"
      "v_cvt_pk_bf16_f32 v74, v130, v131\n"
      "v_cvt_pk_bf16_f32 v184, v120, v121\n"
      "v_mfma_f32_16x16x32_bf16 v[150:153], a[20:23], v[230:233], v[150:153]\n"
      "v_exp_f32 v110, v110\n"
      "v_mfma_f32_16x16x32_bf16 v[150:153], a[24:27], v[106:109], v[150:153]\n"
      "v_cvt_pk_bf16_f32 v72, v110, v111\n"
      "v_mfma_f32_16x16x32_bf16 v[150:153], a[28:31], v[146:149], v[150:153]\n"
      "v_cvt_pk_bf16_f32 v185, v122, v123\n"
      "v_mfma_f32_16x16x32_bf16 v[120:123], a[32:35], v[102:105], 0\n"
      "ds_bpermute_b32 v145, v7, v168 offset:8\n"
      "v_mfma_f32_16x16x32_bf16 v[120:123], a[36:39], v[154:157], v[120:123]\n"
      "v_exp_f32 v70, v70\n"
      "v_mfma_f32_16x16x32_bf16 v[120:123], a[40:43], v[158:161], v[120:123]\n"
      "v_cvt_pk_bf16_f32 v76, v70, v71\n"
      "v_cvt_pk_bf16_f32 v97, v40, v41\n"
      "v_mfma_f32_16x16x32_bf16 v[120:123], a[44:47], v[114:117], v[120:123]\n"
      "buffer_load_dwordx4 v1, s[40:43], s77 offen offset:0 lds\n"
      "v_smfmac_f32_16x16x64_bf16 v[196:199], v[192:195], v[170:177], v5\n"
      "v_mov_b64 v[228:229], 0\n"
      "v_mfma_f32_16x16x32_bf16 v[146:149], a[0:3], v[102:105], 0\n"
      "v_cvt_pk_bf16_f32 v96, v38, v39\n"
      "v_cvt_pk_bf16_f32 v186, v150, v151\n"
      "v_mfma_f32_16x16x32_bf16 v[146:149], a[4:7], v[154:157], v[146:149]\n"
      "v_exp_f32 v242, v242\n"
      "v_mfma_f32_16x16x32_bf16 v[146:149], a[8:11], v[158:161], v[146:149]\n"
      "v_cvt_pk_bf16_f32 v188, v126, v127\n"
      "v_cvt_pk_bf16_f32 v111, v122, v123\n"
      "v_mfma_f32_16x16x32_bf16 v[146:149], a[12:15], v[114:117], v[146:149]\n"
      "v_cvt_pk_bf16_f32 v187, v152, v153\n"
      "v_mfma_f32_16x16x32_bf16 v[38:41], a[16:19], v[102:105], 0\n"
      "v_exp_f32 v124, v124\n"
      "v_mfma_f32_16x16x32_bf16 v[38:41], a[20:23], v[154:157], v[38:41]\n"
      "s_waitcnt lgkmcnt(2)\n"
      "v_mov_b32_dpp v150, v252 row_ror:8 row_mask:0xf bank_mask:0xf\n"
      "v_cvt_pk_bf16_f32 v191, v24, v25\n"
      "v_mfma_f32_16x16x32_bf16 v[38:41], a[24:27], v[158:161], v[38:41]\n"
      "v_cvt_pk_bf16_f32 v190, v22, v23\n"
      "v_mfma_f32_16x16x32_bf16 v[38:41], a[28:31], v[114:117], v[38:41]\n"
      "s_waitcnt lgkmcnt(1)\n"
      "v_mov_b32_dpp v153, v255 row_ror:8 row_mask:0xf bank_mask:0xf\n"
      "v_mov_b32_dpp v152, v254 row_ror:8 row_mask:0xf bank_mask:0xf\n"
      "v_mfma_f32_16x16x32_bf16 v[22:25], a[48:51], v[102:105], 0\n"
      "v_cvt_pk_bf16_f32 v189, v128, v129\n"
      "v_mfma_f32_16x16x32_bf16 v[22:25], a[52:55], v[154:157], v[22:25]\n"
      "v_add_f32 v196, v196, v197\n"
      "v_mfma_f32_16x16x32_bf16 v[22:25], a[56:59], v[158:161], v[22:25]\n"
      "v_exp_f32 v196, v196\n"
      "v_mfma_f32_16x16x32_bf16 v[22:25], a[60:63], v[114:117], v[22:25]\n"
      "v_cvt_pk_bf16_f32 v107, v148, v149\n"
      "v_mfma_f32_16x16x32_bf16 v[128:131], v[184:187], v[134:137], 0\n"
      "v_cvt_pk_bf16_f32 v110, v120, v121\n"
      "v_cvt_pk_bf16_f32 v108, v38, v39\n"
      "v_mfma_f32_16x16x32_bf16 v[128:131], v[188:191], v[138:141], v[128:131]\n"
      "v_cvt_pk_bf16_f32 v109, v40, v41\n"
      "v_cvt_pk_bf16_f32 v106, v146, v147\n"
      "v_smfmac_f32_16x16x64_bf16 v[228:231], v[192:195], v[184:191], v5\n"
      "buffer_load_dwordx4 v1, s[40:43], s77 offen offset:2048 lds\n"
      "v_mfma_f32_16x16x32_bf16 v[38:41], v[18:21], a[64:67], 0\n"
      "v_cvt_pk_bf16_f32 v113, v24, v25\n"
      "v_cndmask_b32_dpp v197, v124, v196, vcc quad_perm:[0,0,2,2] row_mask:0xf bank_mask:0xf\n"
      "v_mfma_f32_16x16x32_bf16 v[38:41], v[238:241], a[68:71], v[38:41]\n"
      "v_mov_b64 v[32:33], 0\n"
      "v_cvt_pk_bf16_f32 v112, v22, v23\n"
      "v_mfma_f32_16x16x32_bf16 v[38:41], v[178:181], a[72:75], v[38:41]\n"
      "v_exp_f32 v129, v129\n"
      "v_mfma_f32_16x16x32_bf16 v[38:41], v[78:81], a[76:79], v[38:41]\n"
      "v_exp_f32 v131, v131\n"
      "v_smfmac_f32_16x16x64_bf16 v[32:35], v[192:195], v[106:113], v5\n"
      "v_add_f32 v228, v228, v229\n"
      "v_mfma_f32_16x16x32_bf16 v[22:25], v[106:109], v[134:137], 0\n"
      "v_exp_f32 v128, v128\n"
      "v_mfma_f32_16x16x32_bf16 v[22:25], v[110:113], v[138:141], v[22:25]\n"
      "v_exp_f32 v228, v228\n"
      "v_mfma_f32_16x16x32_bf16 v[120:123], a[128:131], v[102:105], 0\n"
      "v_cvt_pk_bf16_f32 v206, v38, v39\n"
      "v_cvt_pk_bf16_f32 v207, v40, v41\n"
      "v_mfma_f32_16x16x32_bf16 v[120:123], a[132:135], v[154:157], v[120:123]\n"
      "v_add_f32 v32, v32, v33\n"
      "v_cvt_pk_bf16_f32 v70, v128, v129\n"
      "v_mfma_f32_16x16x32_bf16 v[120:123], a[136:139], v[158:161], v[120:123]\n"
      "v_exp_f32 v25, v25\n"
      "v_mfma_f32_16x16x32_bf16 v[120:123], a[140:143], v[114:117], v[120:123]\n"
      "v_exp_f32 v32, v32\n"
      "v_mfma_f32_16x16x32_bf16 v[192:195], a[144:147], v[102:105], 0\n"
      "v_exp_f32 v24, v24\n"
      "v_mfma_f32_16x16x32_bf16 v[192:195], a[148:151], v[154:157], v[192:195]\n"
      "v_exp_f32 v130, v130\n"
      "v_mfma_f32_16x16x32_bf16 v[192:195], a[152:155], v[158:161], v[192:195]\n"
      "v_mov_b32_dpp v151, v253 row_ror:8 row_mask:0xf bank_mask:0xf\n"
      "v_mfma_f32_16x16x32_bf16 v[192:195], a[156:159], v[114:117], v[192:195]\n"
      "v_cndmask_b32_dpp v33, v242, v32, vcc quad_perm:[0,0,2,2] row_mask:0xf bank_mask:0xf\n"
      "v_cndmask_b32_dpp v229, v164, v228, vcc quad_perm:[0,0,2,2] row_mask:0xf bank_mask:0xf\n"
      "v_mfma_f32_16x16x32_bf16 v[38:41], a[176:179], v[102:105], 0\n"
      "v_mov_b64 v[128:129], 0\n"
      "v_cvt_pk_bf16_f32 v162, v120, v121\n"
      "v_mfma_f32_16x16x32_bf16 v[38:41], a[180:183], v[154:157], v[38:41]\n"
      "s_not_b64 vcc, vcc\n"
      "v_cndmask_b32_dpp v165, v228, v164, vcc quad_perm:[1,1,3,3] row_mask:0xf bank_mask:0xf\n"
      "v_add_f32 v228, v228, v196\n"
      "v_mfma_f32_16x16x32_bf16 v[38:41], a[184:187], v[158:161], v[38:41]\n"
      "v_cvt_pk_bf16_f32 v71, v130, v131\n"
      "v_cvt_pk_bf16_f32 v229, v229, v165\n"
      "v_mfma_f32_16x16x32_bf16 v[38:41], a[188:191], v[114:117], v[38:41]\n"
      "v_cndmask_b32_dpp v243, v32, v242, vcc quad_perm:[1,1,3,3] row_mask:0xf bank_mask:0xf\n"
      "v_add_f32 v32, v32, v242\n"
      "v_mfma_f32_16x16x32_bf16 v[118:121], a[160:163], v[102:105], 0\n"
      "v_cvt_pk_bf16_f32 v163, v122, v123\n"
      "v_mov_b64 v[126:127], 0\n"
      "v_mfma_f32_16x16x32_bf16 v[118:121], a[164:167], v[154:157], v[118:121]\n"
      "v_add_f32 v32, v32, v169\n"
      "v_cvt_pk_bf16_f32 v165, v194, v195\n"
      "v_mfma_f32_16x16x32_bf16 v[118:121], a[168:171], v[158:161], v[118:121]\n"
      "ds_bpermute_b32 v148, v6, v229 offset:16\n"
      "v_mfma_f32_16x16x32_bf16 v[118:121], a[172:175], v[114:117], v[118:121]\n"
      "v_add_f32 v164, v164, v124\n"
      "v_add_f32 v228, v228, v164\n"
      "v_mfma_f32_16x16x32_bf16 v[26:29], v[208:211], a[112:115], 0\n"
      "v_exp_f32 v22, v22\n"
      "v_mfma_f32_16x16x32_bf16 v[26:29], v[66:69], a[116:119], v[26:29]\n"
      "v_cvt_pk_bf16_f32 v168, v38, v39\n"
      "v_cvt_pk_bf16_f32 v33, v33, v243\n"
      "v_mfma_f32_16x16x32_bf16 v[26:29], v[82:85], a[120:123], v[26:29]\n"
      "v_cvt_pk_bf16_f32 v164, v192, v193\n"
      "v_cvt_pk_bf16_f32 v166, v118, v119\n"
      "v_mfma_f32_16x16x32_bf16 v[26:29], v[62:65], a[124:127], v[26:29]\n"
      "v_cvt_pk_bf16_f32 v167, v120, v121\n"
      "v_cvt_pk_bf16_f32 v169, v40, v41\n"
      "v_mfma_f32_16x16x32_bf16 v[130:133], v[106:109], v[162:165], 0\n"
      "v_cndmask_b32_dpp v125, v196, v124, vcc quad_perm:[1,1,3,3] row_mask:0xf bank_mask:0xf\n"
      "v_cvt_pk_bf16_f32 v197, v197, v125\n"
      "v_mfma_f32_16x16x32_bf16 v[130:133], v[110:113], v[166:169], v[130:133]\n"
      "v_add_f32 v236, v228, v32\n"
      "v_mfma_f32_16x16x32_bf16 v[120:123], v[184:187], v[162:165], 0\n"
      "v_exp_f32 v23, v23\n"
      "v_mfma_f32_16x16x32_bf16 v[120:123], v[188:191], v[166:169], v[120:123]\n"
      "ds_bpermute_b32 v147, v6, v197\n"
      "v_smfmac_f32_16x16x64_bf16 v[126:129], v[252:255], v[134:141], v5\n"
      "ds_bpermute_b32 v146, v6, v229\n"
      "v_smfmac_f32_16x16x64_bf16 v[126:129], v[150:153], v[162:169], v5\n"
      "ds_bpermute_b32 v242, v7, v33\n"
      "v_mfma_f32_16x16x32_bf16 v[228:231], v[170:173], v[162:165], 0\n"
      "v_exp_f32 v132, v132\n"
      "v_mfma_f32_16x16x32_bf16 v[228:231], v[174:177], v[166:169], v[228:231]\n"
      "v_exp_f32 v133, v133\n"
      "v_mfma_f32_16x16x32_bf16 v[140:143], v[102:105], a[64:67], 0\n"
      "v_cvt_pk_bf16_f32 v59, v24, v25\n"
      "v_cvt_pk_bf16_f32 v135, v132, v133\n"
      "v_mfma_f32_16x16x32_bf16 v[140:143], v[154:157], a[68:71], v[140:143]\n"
      "ds_bpermute_b32 v243, v7, v33 offset:4\n"
      "v_mfma_f32_16x16x32_bf16 v[140:143], v[158:161], a[72:75], v[140:143]\n"
      "s_nop 0\n"
      "v_exp_f32 v230, v230\n"
      "v_mfma_f32_16x16x32_bf16 v[140:143], v[114:117], a[76:79], v[140:143]\n"
      "v_exp_f32 v131, v131\n"
      "v_mfma_f32_16x16x32_bf16 v[38:41], v[86:89], v[162:165], 0\n"
      "v_cvt_pk_bf16_f32 v199, v28, v29\n"
      "v_add_f32 v126, v126, v127\n"
      "v_mfma_f32_16x16x32_bf16 v[38:41], v[90:93], v[166:169], v[38:41]\n"
      "v_exp_f32 v122, v122\n"
      "v_mfma_f32_16x16x32_bf16 v[136:139], v[42:45], v[162:165], 0\n"
      "v_cvt_pk_bf16_f32 v198, v26, v27\n"
      "v_add_f32 v128, v128, v129\n"
      "v_mfma_f32_16x16x32_bf16 v[136:139], v[46:49], v[166:169], v[136:139]\n"
      "v_exp_f32 v128, v128\n"
      "v_mfma_f32_16x16x32_bf16 v[26:29], v[212:215], v[162:165], 0\n"
      "v_exp_f32 v130, v130\n"
      "v_mfma_f32_16x16x32_bf16 v[26:29], v[216:219], v[166:169], v[26:29]\n"
      "v_exp_f32 v41, v41\n"
      "v_mfma_f32_16x16x32_bf16 v[32:35], v[208:211], a[64:67], 0\n"
      "v_exp_f32 v121, v121\n"
      "v_mfma_f32_16x16x32_bf16 v[32:35], v[66:69], a[68:71], v[32:35]\n"
      "v_exp_f32 v40, v40\n"
      "v_mfma_f32_16x16x32_bf16 v[32:35], v[82:85], a[72:75], v[32:35]\n"
      "v_exp_f32 v120, v120\n"
      "v_mfma_f32_16x16x32_bf16 v[32:35], v[62:65], a[76:79], v[32:35]\n"
      "v_cvt_pk_bf16_f32 v58, v22, v23\n"
      "v_mfma_f32_16x16x32_bf16 v[162:165], v[102:105], a[96:99], 0\n"
      "v_exp_f32 v137, v137\n"
      "v_mfma_f32_16x16x32_bf16 v[162:165], v[154:157], a[100:103], v[162:165]\n"
      "v_add_f32_dpp v236, v236, v236 row_ror:8 row_mask:0xf bank_mask:0xf\n"
      "v_cvt_pk_bf16_f32 v167, v142, v143\n"
      "v_mfma_f32_16x16x32_bf16 v[162:165], v[158:161], a[104:107], v[162:165]\n"
      "v_exp_f32 v229, v229\n"
      "v_mfma_f32_16x16x32_bf16 v[162:165], v[114:117], a[108:111], v[162:165]\n"
      "v_cvt_pk_bf16_f32 v134, v130, v131\n"
      "v_cvt_pk_bf16_f32 v168, v32, v33\n"
      "v_mfma_f32_16x16x32_bf16 v[130:133], v[102:105], a[112:115], 0\n"
      "v_cvt_pk_bf16_f32 v224, v120, v121\n"
      "v_cvt_pk_bf16_f32 v169, v34, v35\n"
      "v_mfma_f32_16x16x32_bf16 v[130:133], v[154:157], a[116:119], v[130:133]\n"
      "ds_bpermute_b32 v149, v6, v197 offset:16\n"
      "v_mfma_f32_16x16x32_bf16 v[130:133], v[158:161], a[120:123], v[130:133]\n"
      "v_exp_f32 v231, v231\n"
      "v_mfma_f32_16x16x32_bf16 v[130:133], v[114:117], a[124:127], v[130:133]\n"
      "v_cvt_pk_bf16_f32 v23, v164, v165\n"
      "v_cvt_pk_bf16_f32 v227, v230, v231\n"
      "v_mfma_f32_16x16x32_bf16 v[118:121], v[208:211], a[80:83], 0\n"
      "v_exp_f32 v28, v28\n"
      "v_mfma_f32_16x16x32_bf16 v[118:121], v[66:69], a[84:87], v[118:121]\n"
      "v_exp_f32 v136, v136\n"
      "v_mfma_f32_16x16x32_bf16 v[118:121], v[82:85], a[88:91], v[118:121]\n"
      "v_cvt_pk_bf16_f32 v166, v140, v141\n"
      "v_cvt_pk_bf16_f32 v22, v162, v163\n"
      "v_mfma_f32_16x16x32_bf16 v[118:121], v[62:65], a[92:95], v[118:121]\n"
      "v_exp_f32 v228, v228\n"
      "v_mfma_f32_16x16x32_bf16 v[140:143], v[208:211], a[96:99], 0\n"
      "v_cvt_pk_bf16_f32 v197, v132, v133\n"
      "v_cvt_pk_bf16_f32 v196, v130, v131\n"
      "v_mfma_f32_16x16x32_bf16 v[140:143], v[66:69], a[100:103], v[140:143]\n"
      "v_exp_f32 v126, v126\n"
      "v_mfma_f32_16x16x32_bf16 v[140:143], v[82:85], a[104:107], v[140:143]\n"
      "v_exp_f32 v26, v26\n"
      "v_mfma_f32_16x16x32_bf16 v[140:143], v[62:65], a[108:111], v[140:143]\n"
      "v_exp_f32 v139, v139\n"
      "v_mfma_f32_16x16x32_bf16 v[130:133], a[160:163], v[18:21], 0\n"
      "v_cvt_pk_bf16_f32 v226, v228, v229\n"
      "v_cvt_pk_bf16_f32 v165, v120, v121\n"
      "v_mfma_f32_16x16x32_bf16 v[130:133], a[164:167], v[238:241], v[130:133]\n"
      "v_exp_f32 v38, v38\n"
      "v_mfma_f32_16x16x32_bf16 v[130:133], a[168:171], v[178:181], v[130:133]\n"
      "v_exp_f32 v123, v123\n"
      "v_mfma_f32_16x16x32_bf16 v[130:133], a[172:175], v[78:81], v[130:133]\n"
      "v_cvt_pk_bf16_f32 v225, v122, v123\n"
      "v_cvt_pk_bf16_f32 v25, v142, v143\n"
      "v_mfma_f32_16x16x32_bf16 v[30:33], v[102:105], a[80:83], 0\n"
      "v_exp_f32 v39, v39\n"
      "v_mfma_f32_16x16x32_bf16 v[30:33], v[154:157], a[84:87], v[30:33]\n"
      "v_cvt_pk_bf16_f32 v24, v140, v141\n"
      "v_add_f32 v141, v126, v128\n"
      "v_mfma_f32_16x16x32_bf16 v[30:33], v[158:161], a[88:91], v[30:33]\n"
      "v_cvt_pk_bf16_f32 v229, v40, v41\n"
      "v_cvt_pk_bf16_f32 v164, v118, v119\n"
      "v_mfma_f32_16x16x32_bf16 v[30:33], v[114:117], a[92:95], v[30:33]\n"
      "v_cvt_pk_bf16_f32 v228, v38, v39\n"
      "v_mfma_f32_16x16x32_bf16 v[122:125], a[176:179], v[18:21], 0\n"
      "v_add_f32 v140, v126, v128\n"
      "v_cvt_pk_bf16_f32 v34, v130, v131\n"
      "v_mfma_f32_16x16x32_bf16 v[122:125], a[180:183], v[238:241], v[122:125]\n"
      "v_cvt_pk_bf16_f32 v230, v136, v137\n"
      "v_cvt_pk_bf16_f32 v35, v132, v133\n"
      "v_mfma_f32_16x16x32_bf16 v[122:125], a[184:187], v[178:181], v[122:125]\n"
      "v_exp_f32 v27, v27\n"
      "v_mfma_f32_16x16x32_bf16 v[122:125], a[188:191], v[78:81], v[122:125]\n"
      "v_exp_f32 v29, v29\n"
      "v_mfma_f32_16x16x32_bf16 v[118:121], a[144:147], v[18:21], 0\n"
      "v_cvt_pk_bf16_f32 v137, v28, v29\n"
      "v_cvt_pk_bf16_f32 v136, v26, v27\n"
      "v_mfma_f32_16x16x32_bf16 v[118:121], a[148:151], v[238:241], v[118:121]\n"
      "v_permlane16_swap_b32 v140, v141\n"
      "v_mfma_f32_16x16x32_bf16 v[118:121], a[152:155], v[178:181], v[118:121]\n"
      "s_nop 0\n"
      "v_cvt_pk_bf16_f32 v36, v122, v123\n"
      "v_cvt_pk_bf16_f32 v162, v30, v31\n"
      "v_mfma_f32_16x16x32_bf16 v[118:121], a[156:159], v[78:81], v[118:121]\n"
      "v_cvt_pk_bf16_f32 v26, v126, v128\n"
      "v_cvt_pk_bf16_f32 v27, v126, v128\n"
      "v_mfma_f32_16x16x32_bf16 v[128:131], a[128:131], v[18:21], 0\n"
      "v_cvt_pk_bf16_f32 v163, v32, v33\n"
      "v_cvt_pk_bf16_f32 v37, v124, v125\n"
      "v_mfma_f32_16x16x32_bf16 v[128:131], a[132:135], v[238:241], v[128:131]\n"
      "v_permlane16_swap_b32 v26, v27\n"
      "v_mfma_f32_16x16x32_bf16 v[128:131], a[136:139], v[178:181], v[128:131]\n"
      "v_exp_f32 v138, v138\n"
      "v_mfma_f32_16x16x32_bf16 v[128:131], a[140:143], v[78:81], v[128:131]\n"
      "v_cvt_pk_bf16_f32 v231, v138, v139\n"
      "v_mov_b64 v[192:193], v[140:141]\n"
      "v_smfmac_f32_16x16x64_bf16 v[140:143], v[12:15], v[70:77], v5\n"
      "s_waitcnt lgkmcnt(0)\n"
      "s_nop 7\n"
      "s_nop 3\n"
      "s_branch L_join_%=\n"
      "L_task_%=:\n"
      "v_mfma_f32_16x16x32_bf16 v[114:117], v[42:45], v[154:157], 0\n"
      "s_waitcnt vmcnt(14)\n"
      "ds_read_b128 v[78:81], v0 offset:8192\n"
      "ds_read_b128 v[150:153], v0 offset:9216\n"
      "v_mfma_f32_16x16x32_bf16 v[114:117], v[46:49], v[158:161], v[114:117]\n"
      "buffer_store_dwordx2 v[72:73], v2, s[44:47], s101 offen offset:512 nt sc1\n"
      "v_mfma_f32_16x16x32_bf16 v[46:49], v[184:187], v[154:157], 0\n"
      "v_mfma_f32_16x16x32_bf16 v[46:49], v[188:191], v[158:161], v[46:49]\n"
      "ds_read_b128 v[138:141], v0 offset:8704\n"
      "ds_read_b128 v[102:105], v0 offset:9728\n"
      "v_mfma_f32_16x16x32_bf16 v[178:181], v[212:215], v[154:157], 0\n"
      "v_mfma_f32_16x16x32_bf16 v[178:181], v[216:219], v[158:161], v[178:181]\n"
      "v_mfma_f32_16x16x32_bf16 v[214:217], v[170:173], v[154:157], 0\n"
      "v_cvt_pk_bf16_f32 v26, v26, v30\n"
      "v_cvt_pk_bf16_f32 v30, v27, v31\n"
      "v_mfma_f32_16x16x32_bf16 v[214:217], v[174:177], v[158:161], v[214:217]\n"
      "v_exp_f32 v46, v46\n"
      "v_mfma_f32_16x16x32_bf16 v[240:243], v[106:109], v[154:157], 0\n"
      "v_mfma_f32_16x16x32_bf16 v[240:243], v[110:113], v[158:161], v[240:243]\n"
      "ds_read_b128 v[208:211], v0 offset:20480\n"
      "ds_read_b128 v[66:69], v0 offset:20992\n"
      "v_mfma_f32_16x16x32_bf16 v[106:109], v[86:89], v[154:157], 0\n"
      "v_exp_f32 v48, v48\n"
      "v_mfma_f32_16x16x32_bf16 v[106:109], v[90:93], v[158:161], v[106:109]\n"
      "ds_read_b128 v[18:21], v0 offset:12288\n"
      "s_waitcnt lgkmcnt(6)\n"
      "v_mfma_f32_16x16x32_bf16 v[110:113], a[48:51], v[78:81], 0\n"
      "s_waitcnt lgkmcnt(4)\n"
      "v_mfma_f32_16x16x32_bf16 v[110:113], a[52:55], v[138:141], v[110:113]\n"
      "ds_read_b128 v[192:195], v10 offset:0\n"
      "ds_read_b128 v[62:65], v0 offset:22016\n"
      "v_mfma_f32_16x16x32_bf16 v[110:113], a[56:59], v[150:153], v[110:113]\n"
      "ds_read_b128 v[82:85], v0 offset:21504\n"
      "s_waitcnt lgkmcnt(6)\n"
      "v_mfma_f32_16x16x32_bf16 v[110:113], a[60:63], v[102:105], v[110:113]\n"
      "v_exp_f32 v117, v117\n"
      "v_mfma_f32_16x16x32_bf16 v[182:185], a[0:3], v[78:81], 0\n"
      "v_exp_f32 v178, v178\n"
      "v_mfma_f32_16x16x32_bf16 v[182:185], a[4:7], v[138:141], v[182:185]\n"
      "v_mfma_f32_16x16x32_bf16 v[182:185], a[8:11], v[150:153], v[182:185]\n"
      "v_exp_f32 v180, v180\n"
      "v_mfma_f32_16x16x32_bf16 v[182:185], a[12:15], v[102:105], v[182:185]\n"
      "buffer_store_dwordx2 v[128:129], v2, s[44:47], s100 offen offset:512 nt sc1\n"
      "v_mfma_f32_16x16x32_bf16 v[156:159], a[32:35], v[78:81], 0\n"
      "v_exp_f32 v181, v181\n"
      "v_mfma_f32_16x16x32_bf16 v[156:159], a[36:39], v[138:141], v[156:159]\n"
      "v_exp_f32 v179, v179\n"
      "v_mfma_f32_16x16x32_bf16 v[156:159], a[40:43], v[150:153], v[156:159]\n"
      "v_exp_f32 v240, v240\n"
      "v_mfma_f32_16x16x32_bf16 v[156:159], a[44:47], v[102:105], v[156:159]\n"
      "v_cvt_pk_bf16_f32 v87, v184, v185\n"
      "v_mfma_f32_16x16x32_bf16 v[184:187], v[78:81], a[64:67], 0\n"
      "v_exp_f32 v215, v215\n"
      "v_mfma_f32_16x16x32_bf16 v[184:187], v[138:141], a[68:71], v[184:187]\n"
      "s_add_u32 m0, s53, 20480\n"
      "v_exp_f32 v216, v216\n"
      "v_mfma_f32_16x16x32_bf16 v[184:187], v[150:153], a[72:75], v[184:187]\n"
      "v_exp_f32 v214, v214\n"
      "v_mfma_f32_16x16x32_bf16 v[184:187], v[102:105], a[76:79], v[184:187]\n"
      "v_exp_f32 v108, v108\n"
      "v_mfma_f32_16x16x32_bf16 v[142:145], v[78:81], a[80:83], 0\n"
      "v_cvt_pk_bf16_f32 v31, v35, v39\n"
      "v_cvt_pk_bf16_f32 v28, v28, v32\n"
      "v_mfma_f32_16x16x32_bf16 v[142:145], v[138:141], a[84:87], v[142:145]\n"
      "v_mfma_f32_16x16x32_bf16 v[142:145], v[150:153], a[88:91], v[142:145]\n"
      "v_cvt_pk_bf16_f32 v32, v29, v33\n"
      "v_cvt_pk_bf16_f32 v172, v214, v215\n"
      "v_mfma_f32_16x16x32_bf16 v[142:145], v[102:105], a[92:95], v[142:145]\n"
      "v_exp_f32 v116, v116\n"
      "v_mfma_f32_16x16x32_bf16 v[188:191], v[78:81], a[96:99], 0\n"
      "v_exp_f32 v49, v49\n"
      "v_mfma_f32_16x16x32_bf16 v[188:191], v[138:141], a[100:103], v[188:191]\n"
      "v_exp_f32 v114, v114\n"
      "v_mfma_f32_16x16x32_bf16 v[188:191], v[150:153], a[104:107], v[188:191]\n"
      "s_waitcnt lgkmcnt(0)\n"
      "buffer_load_dwordx4 v1, s[40:43], s74 offen offset:1024 lds\n"
      "v_mfma_f32_16x16x32_bf16 v[188:191], v[102:105], a[108:111], v[188:191]\n"
      "v_cvt_pk_bf16_f32 v91, v158, v159\n"
      "v_mfma_f32_16x16x32_bf16 v[236:239], a[160:163], v[208:211], 0\n"
      "v_exp_f32 v115, v115\n"
      "v_mfma_f32_16x16x32_bf16 v[236:239], a[164:167], v[66:69], v[236:239]\n"
      "v_exp_f32 v106, v106\n"
      "v_mfma_f32_16x16x32_bf16 v[236:239], a[168:171], v[82:85], v[236:239]\n"
      "v_cvt_pk_bf16_f32 v90, v156, v157\n"
      "v_cvt_pk_bf16_f32 v73, v180, v181\n"
      "v_mfma_f32_16x16x32_bf16 v[236:239], a[172:175], v[62:65], v[236:239]\n"
      "buffer_store_dwordx2 v[70:71], v2, s[44:47], s101 offen offset:0 nt sc1\n"
      "v_mfma_f32_16x16x32_bf16 v[134:137], v[78:81], a[112:115], 0\n"
      "v_exp_f32 v242, v242\n"
      "v_mfma_f32_16x16x32_bf16 v[134:137], v[138:141], a[116:119], v[134:137]\n"
      "v_exp_f32 v47, v47\n"
      "v_mfma_f32_16x16x32_bf16 v[134:137], v[150:153], a[120:123], v[134:137]\n"
      "v_exp_f32 v217, v217\n"
      "v_mfma_f32_16x16x32_bf16 v[134:137], v[102:105], a[124:127], v[134:137]\n"
      "buffer_load_dwordx4 v1, s[40:43], s74 offen offset:3072 lds\n"
      "v_mfma_f32_16x16x32_bf16 v[42:45], a[16:19], v[78:81], 0\n"
      "v_exp_f32 v107, v107\n"
      "v_mfma_f32_16x16x32_bf16 v[42:45], a[20:23], v[138:141], v[42:45]\n"
      "v_cvt_pk_bf16_f32 v174, v106, v107\n"
      "v_mfma_f32_16x16x32_bf16 v[42:45], a[24:27], v[150:153], v[42:45]\n"
      "v_mfma_f32_16x16x32_bf16 v[42:45], a[28:31], v[102:105], v[42:45]\n"
      "v_cvt_pk_bf16_f32 v176, v114, v115\n"
      "v_cvt_pk_bf16_f32 v171, v48, v49\n"
      "v_mfma_f32_16x16x32_bf16 v[102:105], a[32:35], v[208:211], 0\n"
      "v_exp_f32 v109, v109\n"
      "v_mfma_f32_16x16x32_bf16 v[102:105], a[36:39], v[66:69], v[102:105]\n"
      "ds_read_b128 v[78:81], v0 offset:13824\n"
      "v_mfma_f32_16x16x32_bf16 v[102:105], a[40:43], v[82:85], v[102:105]\n"
      "v_exp_f32 v241, v241\n"
      "v_mfma_f32_16x16x32_bf16 v[102:105], a[44:47], v[62:65], v[102:105]\n"
      "v_cvt_pk_bf16_f32 v175, v108, v109\n"
      "v_cvt_pk_bf16_f32 v170, v46, v47\n"
      "v_mfma_f32_16x16x32_bf16 v[46:49], a[128:131], v[208:211], 0\n"
      "v_mfma_f32_16x16x32_bf16 v[46:49], a[132:135], v[66:69], v[46:49]\n"
      "v_exp_f32 v243, v243\n"
      "v_mfma_f32_16x16x32_bf16 v[46:49], a[136:139], v[82:85], v[46:49]\n"
      "v_cvt_pk_bf16_f32 v72, v178, v179\n"
      "v_mfma_f32_16x16x32_bf16 v[46:49], a[140:143], v[62:65], v[46:49]\n"
      "v_cvt_pk_bf16_f32 v173, v216, v217\n"
      "v_pk_max_i16 v28, v28, 0\n"
      "v_mfma_f32_16x16x32_bf16 v[106:109], a[48:51], v[208:211], 0\n"
      "v_cvt_pk_bf16_f32 v70, v240, v241\n"
      "v_cvt_pk_bf16_f32 v216, v102, v103\n"
      "v_mfma_f32_16x16x32_bf16 v[106:109], a[52:55], v[66:69], v[106:109]\n"
      "ds_read_b128 v[178:181], v0 offset:13312\n"
      "v_cvt_pk_bf16_f32 v177, v116, v117\n"
      "v_mfma_f32_16x16x32_bf16 v[106:109], a[56:59], v[82:85], v[106:109]\n"
      "v_cvt_pk_bf16_f32 v139, v238, v239\n"
      "v_cvt_pk_bf16_f32 v217, v104, v105\n"
      "v_mfma_f32_16x16x32_bf16 v[106:109], a[60:63], v[62:65], v[106:109]\n"
      "v_cvt_pk_bf16_f32 v71, v242, v243\n"
      "v_smfmac_f32_16x16x64_bf16 v[58:61], v[12:15], v[170:177], v5\n"
      "v_mfma_f32_16x16x32_bf16 v[114:117], a[0:3], v[208:211], 0\n"
      "ds_read_b128 v[150:153], v9 offset:0\n"
      "v_mfma_f32_16x16x32_bf16 v[114:117], a[4:7], v[66:69], v[114:117]\n"
      "buffer_load_dwordx4 v1, s[40:43], s73 offen offset:0 lds\n"
      "v_mfma_f32_16x16x32_bf16 v[114:117], a[8:11], v[82:85], v[114:117]\n"
      "v_mfma_f32_16x16x32_bf16 v[114:117], a[12:15], v[62:65], v[114:117]\n"
      "ds_read_b128 v[102:105], v0 offset:16384\n"
      "v_pk_max_i16 v30, v30, 0\n"
      "v_smfmac_f32_16x16x32_bf16 v[58:61], v[12:13], v[70:73], v5\n"
      "buffer_store_dwordx2 v[76:77], v2, s[44:47], s101 offen offset:768 nt sc1\n"
      "v_mfma_f32_16x16x32_bf16 v[220:223], v[170:173], v[200:203], v[220:223]\n"
      "v_mfma_f32_16x16x32_bf16 v[220:223], v[174:177], v[204:207], v[220:223]\n"
      "ds_read_b128 v[154:157], v9 offset:2048\n"
      "v_cvt_pk_bf16_f32 v219, v108, v109\n"
      "v_mfma_f32_16x16x32_bf16 v[220:223], v[70:73], v[166:169], v[220:223]\n"
      "v_cvt_pk_bf16_f32 v33, v37, v41\n"
      "v_mfma_f32_16x16x32_bf16 v[228:231], v[170:173], v[244:247], v[228:231]\n"
      "ds_read_b128 v[238:241], v0 offset:12800\n"
      "v_mfma_f32_16x16x32_bf16 v[228:231], v[174:177], v[248:251], v[228:231]\n"
      "v_mfma_f32_16x16x32_bf16 v[228:231], v[70:73], v[22:25], v[228:231]\n"
      "v_pk_max_i16 v33, v33, 0\n"
      "v_mfma_f32_16x16x32_bf16 v[244:247], a[16:19], v[208:211], 0\n"
      "v_mfma_f32_16x16x32_bf16 v[244:247], a[20:23], v[66:69], v[244:247]\n"
      "s_waitcnt vmcnt(13)\n"
      "ds_read_b128 v[22:25], v0 offset:4096\n"
      "v_cvt_pk_bf16_f32 v213, v116, v117\n"
      "v_mfma_f32_16x16x32_bf16 v[244:247], a[24:27], v[82:85], v[244:247]\n"
      "v_mfma_f32_16x16x32_bf16 v[244:247], a[28:31], v[62:65], v[244:247]\n"
      "ds_read_b128 v[158:161], v0 offset:17408\n"
      "v_add_f32 v58, v58, v59\n"
      "v_mfma_f32_16x16x32_bf16 v[232:235], v[170:173], v[94:97], v[232:235]\n"
      "v_cvt_pk_bf16_f32 v249, v190, v191\n"
      "v_mfma_f32_16x16x32_bf16 v[232:235], v[174:177], v[98:101], v[232:235]\n"
      "v_mfma_f32_16x16x32_bf16 v[232:235], v[70:73], v[196:199], v[232:235]\n"
      "v_cvt_pk_bf16_f32 v248, v188, v189\n"
      "v_cvt_pk_bf16_f32 v93, v112, v113\n"
      "v_mfma_f32_16x16x32_bf16 v[224:227], v[170:173], v[50:53], v[224:227]\n"
      "v_rcp_f32 v59, v58\n"
      "v_mfma_f32_16x16x32_bf16 v[224:227], v[174:177], v[54:57], v[224:227]\n"
      "v_mov_b64 v[168:169], 0\n"
      "v_mfma_f32_16x16x32_bf16 v[224:227], v[70:73], v[162:165], v[224:227]\n"
      "s_waitcnt lgkmcnt(3)\n"
      "v_smfmac_f32_16x16x64_bf16 v[168:171], v[192:195], v[150:157], v5\n"
      "buffer_store_dwordx2 v[130:131], v2, s[44:47], s100 offen offset:256 nt sc1\n"
      "v_cvt_pk_bf16_f32 v215, v246, v247\n"
      "v_mfma_f32_16x16x32_bf16 v[98:101], v[18:21], a[96:99], 0\n"
      "v_cvt_pk_bf16_f32 v55, v144, v145\n"
      "s_waitcnt lgkmcnt(2)\n"
      "v_mfma_f32_16x16x32_bf16 v[98:101], v[238:241], a[100:103], v[98:101]\n"
      "v_mfma_f32_16x16x32_bf16 v[98:101], v[178:181], a[104:107], v[98:101]\n"
      "s_nop 0\n"
      "v_add_f32 v168, v168, v169\n"
      "v_mfma_f32_16x16x32_bf16 v[98:101], v[78:81], a[108:111], v[98:101]\n"
      "buffer_load_dwordx4 v1, s[40:43], s73 offen offset:2048 lds\n"
      "v_mfma_f32_16x16x32_bf16 v[196:199], a[176:179], v[208:211], 0\n"
      "v_cvt_pk_bf16_f32 v214, v244, v245\n"
      "v_mov_b32 v169, 0\n"
      "v_mfma_f32_16x16x32_bf16 v[196:199], a[180:183], v[66:69], v[196:199]\n"
      "ds_bpermute_b32 v60, v4, v59 offset:128\n"
      "v_mfma_f32_16x16x32_bf16 v[196:199], a[184:187], v[82:85], v[196:199]\n"
      "v_cvt_pk_bf16_f32 v88, v42, v43\n"
      "v_cvt_pk_bf16_f32 v212, v114, v115\n"
      "v_mfma_f32_16x16x32_bf16 v[196:199], a[188:191], v[62:65], v[196:199]\n"
      "v_cvt_pk_bf16_f32 v86, v182, v183\n"
      "v_mfma_f32_16x16x32_bf16 v[172:175], a[48:51], v[18:21], 0\n"
      "v_mfma_f32_16x16x32_bf16 v[172:175], a[52:55], v[238:241], v[172:175]\n"
      "ds_read_b128 v[114:117], v0 offset:17920\n"
      "v_mfma_f32_16x16x32_bf16 v[172:175], a[56:59], v[178:181], v[172:175]\n"
      "v_mfma_f32_16x16x32_bf16 v[172:175], a[60:63], v[78:81], v[172:175]\n"
      "v_mov_b64 v[242:243], 0\n"
      "v_mfma_f32_16x16x32_bf16 v[164:167], a[16:19], v[18:21], 0\n"
      "v_cvt_pk_bf16_f32 v54, v142, v143\n"
      "v_cvt_pk_bf16_f32 v250, v98, v99\n"
      "v_mfma_f32_16x16x32_bf16 v[164:167], a[20:23], v[238:241], v[164:167]\n"
      "ds_read_b128 v[154:157], v0 offset:16896\n"
      "v_cvt_pk_bf16_f32 v89, v44, v45\n"
      "v_mfma_f32_16x16x32_bf16 v[164:167], a[24:27], v[178:181], v[164:167]\n"
      "v_mfma_f32_16x16x32_bf16 v[164:167], a[28:31], v[78:81], v[164:167]\n"
      "buffer_store_dwordx2 v[132:133], v2, s[44:47], s100 offen offset:768 nt sc1\n"
      "v_mfma_f32_16x16x32_bf16 v[244:247], a[144:147], v[208:211], 0\n"
      "s_waitcnt lgkmcnt(2)\n"
      "v_mul_f32_dpp v233, v60, v233 row_newbcast:4 row_mask:0xf bank_mask:0xf\n"
      "v_mfma_f32_16x16x32_bf16 v[244:247], a[148:151], v[66:69], v[244:247]\n"
      "v_mul_f32_dpp v229, v60, v229 row_newbcast:4 row_mask:0xf bank_mask:0xf\n"
      "v_mul_f32_dpp v222, v60, v222 row_newbcast:8 row_mask:0xf bank_mask:0xf\n"
      "v_mfma_f32_16x16x32_bf16 v[244:247], a[152:155], v[82:85], v[244:247]\n"
      "v_cvt_pk_bf16_f32 v29, v36, v40\n"
      "v_mul_f32_dpp v230, v60, v230 row_newbcast:8 row_mask:0xf bank_mask:0xf\n"
      "v_mfma_f32_16x16x32_bf16 v[244:247], a[156:159], v[62:65], v[244:247]\n"
      "v_cvt_pk_bf16_f32 v141, v198, v199\n"
      "v_mfma_f32_16x16x32_bf16 v[200:203], a[0:3], v[18:21], 0\n"
      "v_cvt_pk_bf16_f32 v98, v134, v135\n"
      "v_mfma_f32_16x16x32_bf16 v[200:203], a[4:7], v[238:241], v[200:203]\n"
      "v_cvt_pk_bf16_f32 v27, v34, v38\n"
      "v_cvt_pk_bf16_f32 v205, v186, v187\n"
      "v_mfma_f32_16x16x32_bf16 v[200:203], a[8:11], v[178:181], v[200:203]\n"
      "v_mul_f32_dpp v220, v60, v220 row_newbcast:0 row_mask:0xf bank_mask:0xf\n"
      "v_cvt_pk_bf16_f32 v44, v164, v165\n"
      "v_mfma_f32_16x16x32_bf16 v[200:203], a[12:15], v[78:81], v[200:203]\n"
      "v_mul_f32_dpp v225, v60, v225 row_newbcast:4 row_mask:0xf bank_mask:0xf\n"
      "v_mul_f32_dpp v226, v60, v226 row_newbcast:8 row_mask:0xf bank_mask:0xf\n"
      "v_mfma_f32_16x16x32_bf16 v[188:191], a[32:35], v[18:21], 0\n"
      "v_mul_f32_dpp v227, v60, v227 row_newbcast:12 row_mask:0xf bank_mask:0xf\n"
      "v_mul_f32_dpp v235, v60, v235 row_newbcast:12 row_mask:0xf bank_mask:0xf\n"
      "v_mfma_f32_16x16x32_bf16 v[188:191], a[36:39], v[238:241], v[188:191]\n"
      "ds_read_b128 v[150:153], v0 offset:5632\n"
      "v_mfma_f32_16x16x32_bf16 v[188:191], a[40:43], v[178:181], v[188:191]\n"
      "ds_read_b128 v[94:97], v0 offset:4608\n"
      "v_mul_f32_dpp v224, v60, v224 row_newbcast:0 row_mask:0xf bank_mask:0xf\n"
      "v_mfma_f32_16x16x32_bf16 v[188:191], a[44:47], v[78:81], v[188:191]\n"
      "v_mul_f32_dpp v234, v60, v234 row_newbcast:8 row_mask:0xf bank_mask:0xf\n"
      "v_mfma_f32_16x16x32_bf16 v[142:145], v[18:21], a[80:83], 0\n"
      "v_cvt_pk_bf16_f32 v92, v110, v111\n"
      "v_cvt_pk_bf16_f32 v140, v196, v197\n"
      "v_mfma_f32_16x16x32_bf16 v[142:145], v[238:241], a[84:87], v[142:145]\n"
      "v_mul_f32_dpp v223, v60, v223 row_newbcast:12 row_mask:0xf bank_mask:0xf\n"
      "v_mfma_f32_16x16x32_bf16 v[142:145], v[178:181], a[88:91], v[142:145]\n"
      "v_cvt_pk_bf16_f32 v99, v136, v137\n"
      "v_mov_b64 v[164:165], 0\n"
      "v_mfma_f32_16x16x32_bf16 v[142:145], v[78:81], a[92:95], v[142:145]\n"
      "v_cvt_pk_bf16_f32 v134, v46, v47\n"
      "v_cvt_pk_bf16_f32 v136, v244, v245\n"
      "v_mfma_f32_16x16x32_bf16 v[224:227], v[252:255], a[208:211], v[224:227]\n"
      "ds_read_b128 v[34:37], v0 offset:5120\n"
      "v_mul_f32_dpp v221, v60, v221 row_newbcast:4 row_mask:0xf bank_mask:0xf\n"
      "v_mfma_f32_16x16x32_bf16 v[224:227], v[122:125], a[212:215], v[224:227]\n"
      "v_mfma_f32_16x16x32_bf16 v[224:227], v[146:149], a[216:219], v[224:227]\n"
      "v_maximum3_f32 v8, v8, v58, v59\n"
      "s_add_u32 m0, s53, 16384\n"
      "v_mfma_f32_16x16x32_bf16 v[224:227], v[118:121], a[220:223], v[224:227]\n"
      "v_cvt_pk_bf16_f32 v137, v246, v247\n"
      "v_cvt_pk_bf16_f32 v135, v48, v49\n"
      "v_mfma_f32_16x16x32_bf16 v[220:223], v[252:255], a[192:195], v[220:223]\n"
      "v_mfma_f32_16x16x32_bf16 v[220:223], v[122:125], a[196:199], v[220:223]\n"
      "v_exp_f32_dpp v169, v168 quad_perm:[0,1,2,3] row_mask:0xf bank_mask:0x1\n"
      "v_mfma_f32_16x16x32_bf16 v[220:223], v[146:149], a[200:203], v[220:223]\n"
      "v_cvt_pk_bf16_f32 v218, v106, v107\n"
      "v_mfma_f32_16x16x32_bf16 v[220:223], v[118:121], a[204:207], v[220:223]\n"
      "s_nop 0\n"
      "v_smfmac_f32_16x16x64_bf16 v[242:245], v[192:195], v[212:219], v5\n"
      "v_cvt_pk_bf16_f32 v56, v142, v143\n"
      "v_mfma_f32_16x16x32_bf16 v[40:43], a[16:19], v[22:25], 0\n"
      "v_cvt_pk_bf16_f32 v47, v190, v191\n"
      "s_waitcnt lgkmcnt(1)\n"
      "v_mfma_f32_16x16x32_bf16 v[40:43], a[20:23], v[94:97], v[40:43]\n"
      "s_waitcnt lgkmcnt(0)\n"
      "v_mfma_f32_16x16x32_bf16 v[40:43], a[24:27], v[34:37], v[40:43]\n"
      "v_cvt_pk_bf16_f32 v49, v174, v175\n"
      "v_mfma_f32_16x16x32_bf16 v[40:43], a[28:31], v[150:153], v[40:43]\n"
      "v_cvt_pk_bf16_f32 v220, v220, v224\n"
      "v_cvt_pk_bf16_f32 v138, v236, v237\n"
      "v_mfma_f32_16x16x32_bf16 v[130:133], v[86:89], v[134:137], 0\n"
      "buffer_load_dwordx4 v1, s[40:43], s75 offen offset:2048 lds\n"
      "v_mfma_f32_16x16x32_bf16 v[130:133], v[90:93], v[138:141], v[130:133]\n"
      "buffer_store_dwordx2 v[126:127], v2, s[44:47], s100 offen offset:0 nt sc1\n"
      "v_mul_f32_dpp v231, v60, v231 row_newbcast:12 row_mask:0xf bank_mask:0xf\n"
      "v_mfma_f32_16x16x32_bf16 v[50:53], v[212:215], v[134:137], 0\n"
      "v_pk_max_i16 v26, v26, 0\n"
      "v_mfma_f32_16x16x32_bf16 v[50:53], v[216:219], v[138:141], v[50:53]\n"
      "v_mov_b32_dpp v168, v169 quad_perm:[1,1,3,3] row_mask:0xf bank_mask:0xf\n"
      "v_cvt_pk_bf16_f32 v224, v221, v225\n"
      "v_mfma_f32_16x16x32_bf16 v[244:247], a[0:3], v[22:25], 0\n"
      "v_mfma_f32_16x16x32_bf16 v[244:247], a[4:7], v[94:97], v[244:247]\n"
      "v_mfma_f32_16x16x32_bf16 v[244:247], a[8:11], v[34:37], v[244:247]\n"
      "v_mul_f32_dpp v232, v60, v232 row_newbcast:0 row_mask:0xf bank_mask:0xf\n"
      "v_mul_f32_dpp v228, v60, v228 row_newbcast:0 row_mask:0xf bank_mask:0xf\n"
      "v_mfma_f32_16x16x32_bf16 v[244:247], a[12:15], v[150:153], v[244:247]\n"
      "v_cvt_pk_bf16_f32 v222, v222, v226\n"
      "v_cvt_pk_bf16_f32 v48, v172, v173\n"
      "v_mfma_f32_16x16x32_bf16 v[228:231], v[252:255], a[224:227], v[228:231]\n"
      "v_cvt_pk_bf16_f32 v226, v223, v227\n"
      "v_mfma_f32_16x16x32_bf16 v[228:231], v[122:125], a[228:231], v[228:231]\n"
      "v_exp_f32 v53, v53\n"
      "v_mfma_f32_16x16x32_bf16 v[228:231], v[146:149], a[232:235], v[228:231]\n"
      "v_exp_f32 v133, v133\n"
      "v_mfma_f32_16x16x32_bf16 v[228:231], v[118:121], a[236:239], v[228:231]\n"
      "v_exp_f32 v52, v52\n"
      "v_mfma_f32_16x16x32_bf16 v[232:235], v[252:255], a[240:243], v[232:235]\n"
      "v_cvt_pk_bf16_f32 v173, v42, v43\n"
      "v_cvt_pk_bf16_f32 v61, v52, v53\n"
      "v_mfma_f32_16x16x32_bf16 v[232:235], v[122:125], a[244:247], v[232:235]\n"
      "v_cvt_pk_bf16_f32 v42, v200, v201\n"
      "v_cvt_pk_bf16_f32 v168, v169, v168\n"
      "v_mfma_f32_16x16x32_bf16 v[232:235], v[146:149], a[248:251], v[232:235]\n"
      "v_exp_f32 v131, v131\n"
      "v_mfma_f32_16x16x32_bf16 v[232:235], v[118:121], a[252:255], v[232:235]\n"
      "v_pk_max_i16 v222, v222, 0\n"
      "v_mfma_f32_16x16x32_bf16 v[124:127], a[48:51], v[22:25], 0\n"
      "v_mfma_f32_16x16x32_bf16 v[124:127], a[52:55], v[94:97], v[124:127]\n"
      "v_mfma_f32_16x16x32_bf16 v[124:127], a[56:59], v[34:37], v[124:127]\n"
      "buffer_load_dwordx4 v1, s[40:43], s75 offen offset:0 lds\n"
      "v_mfma_f32_16x16x32_bf16 v[124:127], a[60:63], v[150:153], v[124:127]\n"
      "buffer_store_dwordx2 v[74:75], v2, s[44:47], s101 offen offset:256 nt sc1\n"
      "ds_read_b64 v[182:183], v11 offset:0\n"
      "v_mfma_f32_16x16x32_bf16 v[196:199], v[22:25], a[80:83], 0\n"
      "v_mfma_f32_16x16x32_bf16 v[196:199], v[94:97], a[84:87], v[196:199]\n"
      "v_cvt_pk_bf16_f32 v46, v188, v189\n"
      "v_cvt_pk_bf16_f32 v172, v40, v41\n"
      "v_mfma_f32_16x16x32_bf16 v[196:199], v[34:37], a[88:91], v[196:199]\n"
      "v_cvt_pk_bf16_f32 v45, v166, v167\n"
      "v_mfma_f32_16x16x32_bf16 v[196:199], v[150:153], a[92:95], v[196:199]\n"
      "v_cvt_pk_bf16_f32 v227, v231, v235\n"
      "v_mfma_f32_16x16x32_bf16 v[110:113], a[32:35], v[22:25], 0\n"
      "v_pk_max_i16 v220, v220, 0\n"
      "v_pk_max_i16 v227, v227, 0\n"
      "v_mfma_f32_16x16x32_bf16 v[110:113], a[36:39], v[94:97], v[110:113]\n"
      "v_cvt_pk_bf16_f32 v251, v100, v101\n"
      "v_cvt_pk_bf16_f32 v176, v124, v125\n"
      "v_mfma_f32_16x16x32_bf16 v[110:113], a[40:43], v[34:37], v[110:113]\n"
      "s_waitcnt vmcnt(14)\n"
      "ds_read_b128 v[188:191], v0 offset:0\n"
      "ds_read_b128 v[106:109], v0 offset:1024\n"
      "v_mfma_f32_16x16x32_bf16 v[110:113], a[44:47], v[150:153], v[110:113]\n"
      "v_mfma_f32_16x16x32_bf16 v[120:123], v[22:25], a[96:99], 0\n"
      "v_cvt_pk_bf16_f32 v43, v202, v203\n"
      "v_cvt_pk_bf16_f32 v52, v196, v197\n"
      "v_mfma_f32_16x16x32_bf16 v[120:123], v[94:97], a[100:103], v[120:123]\n"
      "v_cvt_pk_bf16_f32 v177, v126, v127\n"
      "v_pk_max_i16 v31, v31, 0\n"
      "v_mfma_f32_16x16x32_bf16 v[120:123], v[34:37], a[104:107], v[120:123]\n"
      "v_exp_f32 v51, v51\n"
      "v_mfma_f32_16x16x32_bf16 v[120:123], v[150:153], a[108:111], v[120:123]\n"
      "ds_read_b128 v[146:149], v0 offset:1536\n"
      "v_cvt_pk_bf16_f32 v223, v230, v234\n"
      "v_mfma_f32_16x16x32_bf16 v[74:77], v[22:25], a[64:67], 0\n"
      "v_mfma_f32_16x16x32_bf16 v[74:77], v[94:97], a[68:71], v[74:77]\n"
      "v_exp_f32 v50, v50\n"
      "v_mfma_f32_16x16x32_bf16 v[74:77], v[34:37], a[72:75], v[74:77]\n"
      "v_pk_max_i16 v223, v223, 0\n"
      "v_mfma_f32_16x16x32_bf16 v[74:77], v[150:153], a[76:79], v[74:77]\n"
      "v_cvt_pk_bf16_f32 v171, v246, v247\n"
      "v_cvt_pk_bf16_f32 v221, v228, v232\n"
      "v_smfmac_f32_16x16x64_bf16 v[164:167], v[192:195], v[86:93], v5\n"
      "v_cvt_pk_bf16_f32 v225, v229, v233\n"
      "v_mfma_f32_16x16x32_bf16 v[70:73], v[42:45], v[134:137], 0\n"
      "v_mfma_f32_16x16x32_bf16 v[70:73], v[46:49], v[138:141], v[70:73]\n"
      "v_cvt_pk_bf16_f32 v175, v112, v113\n"
      "v_cvt_pk_bf16_f32 v247, v122, v123\n"
      "v_mfma_f32_16x16x32_bf16 v[38:41], v[22:25], a[112:115], 0\n"
      "v_pk_max_i16 v32, v32, 0\n"
      "v_mfma_f32_16x16x32_bf16 v[38:41], v[94:97], a[116:119], v[38:41]\n"
      "ds_read_b128 v[230:233], v0 offset:512\n"
      "v_cvt_pk_bf16_f32 v57, v144, v145\n"
      "v_mfma_f32_16x16x32_bf16 v[38:41], v[34:37], a[120:123], v[38:41]\n"
      "v_mov_b64 v[124:125], 0\n"
      "v_cvt_pk_bf16_f32 v170, v244, v245\n"
      "v_mfma_f32_16x16x32_bf16 v[38:41], v[150:153], a[124:127], v[38:41]\n"
      "buffer_store_dwordx2 v[30:31], v2, s[44:47], s34 offen offset:256 nt sc1\n"
      "v_cvt_pk_bf16_f32 v202, v74, v75\n"
      "v_mfma_f32_16x16x32_bf16 v[126:129], v[18:21], a[112:115], 0\n"
      "v_cvt_pk_bf16_f32 v174, v110, v111\n"
      "v_cvt_pk_bf16_f32 v204, v184, v185\n"
      "v_mfma_f32_16x16x32_bf16 v[126:129], v[238:241], a[116:119], v[126:129]\n"
      "v_mfma_f32_16x16x32_bf16 v[126:129], v[178:181], a[120:123], v[126:129]\n"
      "v_mfma_f32_16x16x32_bf16 v[126:129], v[78:81], a[124:127], v[126:129]\n"
      "v_cvt_pk_bf16_f32 v203, v76, v77\n"
      "v_cvt_pk_bf16_f32 v53, v198, v199\n"
      "v_mfma_f32_16x16x32_bf16 v[110:113], v[170:173], v[134:137], 0\n"
      "buffer_load_dwordx4 v1, s[40:43], s76 offen offset:1024 lds\n"
      "v_mfma_f32_16x16x32_bf16 v[110:113], v[174:177], v[138:141], v[110:113]\n"
      "s_waitcnt lgkmcnt(3)\n"
      "v_mfma_f32_16x16x32_bf16 v[184:187], v[188:191], a[64:67], 0\n"
      "s_nop 0\n"
      "v_cvt_pk_bf16_f32 v101, v128, v129\n"
      "s_waitcnt lgkmcnt(0)\n"
      "v_mfma_f32_16x16x32_bf16 v[184:187], v[230:233], a[68:71], v[184:187]\n"
      "v_pk_max_i16 v225, v225, 0\n"
      "v_add_f32 v164, v164, v165\n"
      "v_mfma_f32_16x16x32_bf16 v[184:187], v[106:109], a[72:75], v[184:187]\n"
      "v_exp_f32 v130, v130\n"
      "v_mfma_f32_16x16x32_bf16 v[184:187], v[146:149], a[76:79], v[184:187]\n"
      "v_exp_f32 v111, v111\n"
      "v_mfma_f32_16x16x32_bf16 v[22:25], a[48:51], v[188:191], 0\n"
      "v_mfma_f32_16x16x32_bf16 v[22:25], a[52:55], v[230:233], v[22:25]\n"
      "v_add_f32 v242, v242, v243\n"
      "v_mfma_f32_16x16x32_bf16 v[22:25], a[56:59], v[106:109], v[22:25]\n"
      "v_mov_b64 v[196:197], 0\n"
      "v_mfma_f32_16x16x32_bf16 v[22:25], a[60:63], v[146:149], v[22:25]\n"
      "s_nop 0\n"
      "v_cvt_pk_bf16_f32 v201, v186, v187\n"
      "v_cvt_pk_bf16_f32 v200, v184, v185\n"
      "v_mfma_f32_16x16x32_bf16 v[94:97], v[188:191], a[96:99], 0\n"
      "ds_bpermute_b32 v144, v7, v168 offset:248\n"
      "v_mfma_f32_16x16x32_bf16 v[94:97], v[230:233], a[100:103], v[94:97]\n"
      "v_exp_f32 v132, v132\n"
      "v_mfma_f32_16x16x32_bf16 v[94:97], v[106:109], a[104:107], v[94:97]\n"
      "v_cvt_pk_bf16_f32 v100, v126, v127\n"
      "v_cvt_pk_bf16_f32 v60, v50, v51\n"
      "v_mfma_f32_16x16x32_bf16 v[94:97], v[146:149], a[108:111], v[94:97]\n"
      "v_exp_f32 v112, v112\n"
      "v_smfmac_f32_16x16x64_bf16 v[124:127], v[192:195], v[42:49], v5\n"
      "buffer_store_dwordx2 v[32:33], v2, s[44:47], s34 offen offset:768 nt sc1\n"
      "v_mfma_f32_16x16x32_bf16 v[184:187], v[188:191], a[112:115], 0\n"
      "v_mfma_f32_16x16x32_bf16 v[184:187], v[230:233], a[116:119], v[184:187]\n"
      "v_mfma_f32_16x16x32_bf16 v[184:187], v[106:109], a[120:123], v[184:187]\n"
      "v_exp_f32 v73, v73\n"
      "v_mfma_f32_16x16x32_bf16 v[184:187], v[146:149], a[124:127], v[184:187]\n"
      "buffer_load_dwordx4 v1, s[40:43], s76 offen offset:3072 lds\n"
      "v_mfma_f32_16x16x32_bf16 v[32:35], v[188:191], a[80:83], 0\n"
      "v_exp_f32 v72, v72\n"
      "v_mfma_f32_16x16x32_bf16 v[32:35], v[230:233], a[84:87], v[32:35]\n"
      "v_cvt_pk_bf16_f32 v244, v94, v95\n"
      "v_cvt_pk_bf16_f32 v245, v96, v97\n"
      "v_mfma_f32_16x16x32_bf16 v[32:35], v[106:109], a[88:91], v[32:35]\n"
      "v_cvt_pk_bf16_f32 v246, v120, v121\n"
      "v_cvt_pk_bf16_f32 v77, v72, v73\n"
      "v_mfma_f32_16x16x32_bf16 v[32:35], v[146:149], a[92:95], v[32:35]\n"
      "v_cvt_pk_bf16_f32 v94, v184, v185\n"
      "v_pk_max_i16 v221, v221, 0\n"
      "v_mfma_f32_16x16x32_bf16 v[120:123], a[0:3], v[188:191], 0\n"
      "v_add_f32 v124, v124, v125\n"
      "v_pk_max_i16 v29, v29, 0\n"
      "v_mfma_f32_16x16x32_bf16 v[120:123], a[4:7], v[230:233], v[120:123]\n"
      "v_exp_f32 v113, v113\n"
      "v_mfma_f32_16x16x32_bf16 v[120:123], a[8:11], v[106:109], v[120:123]\n"
      "v_exp_f32 v164, v164\n"
      "v_mfma_f32_16x16x32_bf16 v[120:123], a[12:15], v[146:149], v[120:123]\n"
      "ds_read2_b32 v[252:253], v16 offset0:0 offset1:2\n"
      "v_mfma_f32_16x16x32_bf16 v[126:129], a[32:35], v[188:191], 0\n"
      "ds_read2_b32 v[254:255], v16 offset0:128 offset1:130\n"
      "v_cvt_pk_bf16_f32 v50, v32, v33\n"
      "v_mfma_f32_16x16x32_bf16 v[126:129], a[36:39], v[230:233], v[126:129]\n"
      "v_exp_f32 v71, v71\n"
      "v_mfma_f32_16x16x32_bf16 v[126:129], a[40:43], v[106:109], v[126:129]\n"
      "v_cvt_pk_bf16_f32 v51, v34, v35\n"
      "v_cvt_pk_bf16_f32 v95, v186, v187\n"
      "v_mfma_f32_16x16x32_bf16 v[126:129], a[44:47], v[146:149], v[126:129]\n"
      "s_add_u32 m0, s53, 12288\n"
      "v_cvt_pk_bf16_f32 v73, v112, v113\n"
      "v_cvt_pk_bf16_f32 v75, v132, v133\n"
      "v_mfma_f32_16x16x32_bf16 v[150:153], a[16:19], v[188:191], 0\n"
      "v_cvt_pk_bf16_f32 v74, v130, v131\n"
      "v_cvt_pk_bf16_f32 v184, v120, v121\n"
      "v_mfma_f32_16x16x32_bf16 v[150:153], a[20:23], v[230:233], v[150:153]\n"
      "v_exp_f32 v110, v110\n"
      "v_mfma_f32_16x16x32_bf16 v[150:153], a[24:27], v[106:109], v[150:153]\n"
      "v_cvt_pk_bf16_f32 v72, v110, v111\n"
      "v_mfma_f32_16x16x32_bf16 v[150:153], a[28:31], v[146:149], v[150:153]\n"
      "v_cvt_pk_bf16_f32 v185, v122, v123\n"
      "v_mfma_f32_16x16x32_bf16 v[120:123], a[32:35], v[102:105], 0\n"
      "ds_bpermute_b32 v145, v7, v168 offset:8\n"
      "v_mfma_f32_16x16x32_bf16 v[120:123], a[36:39], v[154:157], v[120:123]\n"
      "v_exp_f32 v70, v70\n"
      "v_mfma_f32_16x16x32_bf16 v[120:123], a[40:43], v[158:161], v[120:123]\n"
      "v_cvt_pk_bf16_f32 v76, v70, v71\n"
      "v_cvt_pk_bf16_f32 v97, v40, v41\n"
      "v_mfma_f32_16x16x32_bf16 v[120:123], a[44:47], v[114:117], v[120:123]\n"
      "buffer_load_dwordx4 v1, s[40:43], s77 offen offset:0 lds\n"
      "v_smfmac_f32_16x16x64_bf16 v[196:199], v[192:195], v[170:177], v5\n"
      "buffer_store_dwordx2 v[28:29], v2, s[44:47], s34 offen offset:512 nt sc1\n"
      "v_mov_b64 v[228:229], 0\n"
      "v_mfma_f32_16x16x32_bf16 v[146:149], a[0:3], v[102:105], 0\n"
      "v_cvt_pk_bf16_f32 v96, v38, v39\n"
      "v_cvt_pk_bf16_f32 v186, v150, v151\n"
      "v_mfma_f32_16x16x32_bf16 v[146:149], a[4:7], v[154:157], v[146:149]\n"
      "v_exp_f32 v242, v242\n"
      "v_mfma_f32_16x16x32_bf16 v[146:149], a[8:11], v[158:161], v[146:149]\n"
      "v_cvt_pk_bf16_f32 v188, v126, v127\n"
      "v_cvt_pk_bf16_f32 v111, v122, v123\n"
      "v_mfma_f32_16x16x32_bf16 v[146:149], a[12:15], v[114:117], v[146:149]\n"
      "v_cvt_pk_bf16_f32 v187, v152, v153\n"
      "v_mfma_f32_16x16x32_bf16 v[38:41], a[16:19], v[102:105], 0\n"
      "v_exp_f32 v124, v124\n"
      "v_mfma_f32_16x16x32_bf16 v[38:41], a[20:23], v[154:157], v[38:41]\n"
      "s_waitcnt lgkmcnt(2)\n"
      "v_mov_b32_dpp v150, v252 row_ror:8 row_mask:0xf bank_mask:0xf\n"
      "v_cvt_pk_bf16_f32 v191, v24, v25\n"
      "v_mfma_f32_16x16x32_bf16 v[38:41], a[24:27], v[158:161], v[38:41]\n"
      "v_cvt_pk_bf16_f32 v190, v22, v23\n"
      "v_mfma_f32_16x16x32_bf16 v[38:41], a[28:31], v[114:117], v[38:41]\n"
      "s_waitcnt lgkmcnt(1)\n"
      "v_mov_b32_dpp v153, v255 row_ror:8 row_mask:0xf bank_mask:0xf\n"
      "v_mov_b32_dpp v152, v254 row_ror:8 row_mask:0xf bank_mask:0xf\n"
      "v_mfma_f32_16x16x32_bf16 v[22:25], a[48:51], v[102:105], 0\n"
      "v_cvt_pk_bf16_f32 v189, v128, v129\n"
      "v_pk_max_i16 v27, v27, 0\n"
      "v_mfma_f32_16x16x32_bf16 v[22:25], a[52:55], v[154:157], v[22:25]\n"
      "v_add_f32 v196, v196, v197\n"
      "v_mfma_f32_16x16x32_bf16 v[22:25], a[56:59], v[158:161], v[22:25]\n"
      "v_exp_f32 v196, v196\n"
      "v_mfma_f32_16x16x32_bf16 v[22:25], a[60:63], v[114:117], v[22:25]\n"
      "v_cvt_pk_bf16_f32 v107, v148, v149\n"
      "v_pk_max_i16 v224, v224, 0\n"
      "v_mfma_f32_16x16x32_bf16 v[128:131], v[184:187], v[134:137], 0\n"
      "v_cvt_pk_bf16_f32 v110, v120, v121\n"
      "v_cvt_pk_bf16_f32 v108, v38, v39\n"
      "v_mfma_f32_16x16x32_bf16 v[128:131], v[188:191], v[138:141], v[128:131]\n"
      "v_cvt_pk_bf16_f32 v109, v40, v41\n"
      "v_cvt_pk_bf16_f32 v106, v146, v147\n"
      "v_smfmac_f32_16x16x64_bf16 v[228:231], v[192:195], v[184:191], v5\n"
      "buffer_load_dwordx4 v1, s[40:43], s77 offen offset:2048 lds\n"
      "v_mfma_f32_16x16x32_bf16 v[38:41], v[18:21], a[64:67], 0\n"
      "v_cvt_pk_bf16_f32 v113, v24, v25\n"
      "v_cndmask_b32_dpp v197, v124, v196, vcc quad_perm:[0,0,2,2] row_mask:0xf bank_mask:0xf\n"
      "v_mfma_f32_16x16x32_bf16 v[38:41], v[238:241], a[68:71], v[38:41]\n"
      "v_mov_b64 v[32:33], 0\n"
      "v_cvt_pk_bf16_f32 v112, v22, v23\n"
      "v_mfma_f32_16x16x32_bf16 v[38:41], v[178:181], a[72:75], v[38:41]\n"
      "v_exp_f32 v129, v129\n"
      "v_mfma_f32_16x16x32_bf16 v[38:41], v[78:81], a[76:79], v[38:41]\n"
      "v_exp_f32 v131, v131\n"
      "v_smfmac_f32_16x16x64_bf16 v[32:35], v[192:195], v[106:113], v5\n"
      "buffer_store_dwordx2 v[26:27], v2, s[44:47], s34 offen offset:0 nt sc1\n"
      "v_add_f32 v228, v228, v229\n"
      "v_mfma_f32_16x16x32_bf16 v[22:25], v[106:109], v[134:137], 0\n"
      "v_exp_f32 v128, v128\n"
      "v_mfma_f32_16x16x32_bf16 v[22:25], v[110:113], v[138:141], v[22:25]\n"
      "v_exp_f32 v228, v228\n"
      "v_mfma_f32_16x16x32_bf16 v[120:123], a[128:131], v[102:105], 0\n"
      "v_cvt_pk_bf16_f32 v206, v38, v39\n"
      "v_cvt_pk_bf16_f32 v207, v40, v41\n"
      "v_mfma_f32_16x16x32_bf16 v[120:123], a[132:135], v[154:157], v[120:123]\n"
      "v_add_f32 v32, v32, v33\n"
      "v_cvt_pk_bf16_f32 v70, v128, v129\n"
      "v_mfma_f32_16x16x32_bf16 v[120:123], a[136:139], v[158:161], v[120:123]\n"
      "v_exp_f32 v25, v25\n"
      "v_mfma_f32_16x16x32_bf16 v[120:123], a[140:143], v[114:117], v[120:123]\n"
      "v_exp_f32 v32, v32\n"
      "v_mfma_f32_16x16x32_bf16 v[192:195], a[144:147], v[102:105], 0\n"
      "v_exp_f32 v24, v24\n"
      "v_mfma_f32_16x16x32_bf16 v[192:195], a[148:151], v[154:157], v[192:195]\n"
      "v_exp_f32 v130, v130\n"
      "v_mfma_f32_16x16x32_bf16 v[192:195], a[152:155], v[158:161], v[192:195]\n"
      "v_mov_b32_dpp v151, v253 row_ror:8 row_mask:0xf bank_mask:0xf\n"
      "v_pk_max_i16 v226, v226, 0\n"
      "v_mfma_f32_16x16x32_bf16 v[192:195], a[156:159], v[114:117], v[192:195]\n"
      "v_cndmask_b32_dpp v33, v242, v32, vcc quad_perm:[0,0,2,2] row_mask:0xf bank_mask:0xf\n"
      "v_cndmask_b32_dpp v229, v164, v228, vcc quad_perm:[0,0,2,2] row_mask:0xf bank_mask:0xf\n"
      "v_mfma_f32_16x16x32_bf16 v[38:41], a[176:179], v[102:105], 0\n"
      "v_mov_b64 v[128:129], 0\n"
      "v_cvt_pk_bf16_f32 v162, v120, v121\n"
      "v_mfma_f32_16x16x32_bf16 v[38:41], a[180:183], v[154:157], v[38:41]\n"
      "s_not_b64 vcc, vcc\n"
      "v_cndmask_b32_dpp v165, v228, v164, vcc quad_perm:[1,1,3,3] row_mask:0xf bank_mask:0xf\n"
      "v_add_f32 v228, v228, v196\n"
      "v_mfma_f32_16x16x32_bf16 v[38:41], a[184:187], v[158:161], v[38:41]\n"
      "v_cvt_pk_bf16_f32 v71, v130, v131\n"
      "v_cvt_pk_bf16_f32 v229, v229, v165\n"
      "v_mfma_f32_16x16x32_bf16 v[38:41], a[188:191], v[114:117], v[38:41]\n"
      "v_cndmask_b32_dpp v243, v32, v242, vcc quad_perm:[1,1,3,3] row_mask:0xf bank_mask:0xf\n"
      "v_add_f32 v32, v32, v242\n"
      "v_mfma_f32_16x16x32_bf16 v[118:121], a[160:163], v[102:105], 0\n"
      "v_cvt_pk_bf16_f32 v163, v122, v123\n"
      "v_mov_b64 v[126:127], 0\n"
      "v_mfma_f32_16x16x32_bf16 v[118:121], a[164:167], v[154:157], v[118:121]\n"
      "v_add_f32 v32, v32, v169\n"
      "v_cvt_pk_bf16_f32 v165, v194, v195\n"
      "v_mfma_f32_16x16x32_bf16 v[118:121], a[168:171], v[158:161], v[118:121]\n"
      "ds_bpermute_b32 v148, v6, v229 offset:16\n"
      "v_mfma_f32_16x16x32_bf16 v[118:121], a[172:175], v[114:117], v[118:121]\n"
      "v_add_f32 v164, v164, v124\n"
      "v_add_f32 v228, v228, v164\n"
      "v_mfma_f32_16x16x32_bf16 v[26:29], v[208:211], a[112:115], 0\n"
      "v_exp_f32 v22, v22\n"
      "v_mfma_f32_16x16x32_bf16 v[26:29], v[66:69], a[116:119], v[26:29]\n"
      "v_cvt_pk_bf16_f32 v168, v38, v39\n"
      "v_cvt_pk_bf16_f32 v33, v33, v243\n"
      "v_mfma_f32_16x16x32_bf16 v[26:29], v[82:85], a[120:123], v[26:29]\n"
      "v_cvt_pk_bf16_f32 v164, v192, v193\n"
      "v_cvt_pk_bf16_f32 v166, v118, v119\n"
      "v_mfma_f32_16x16x32_bf16 v[26:29], v[62:65], a[124:127], v[26:29]\n"
      "v_cvt_pk_bf16_f32 v167, v120, v121\n"
      "v_cvt_pk_bf16_f32 v169, v40, v41\n"
      "v_mfma_f32_16x16x32_bf16 v[130:133], v[106:109], v[162:165], 0\n"
      "v_cndmask_b32_dpp v125, v196, v124, vcc quad_perm:[1,1,3,3] row_mask:0xf bank_mask:0xf\n"
      "v_cvt_pk_bf16_f32 v197, v197, v125\n"
      "v_mfma_f32_16x16x32_bf16 v[130:133], v[110:113], v[166:169], v[130:133]\n"
      "buffer_store_dwordx2 v[226:227], v2, s[44:47], s35 offen offset:768 nt sc1\n"
      "v_add_f32 v236, v228, v32\n"
      "v_mfma_f32_16x16x32_bf16 v[120:123], v[184:187], v[162:165], 0\n"
      "v_exp_f32 v23, v23\n"
      "v_mfma_f32_16x16x32_bf16 v[120:123], v[188:191], v[166:169], v[120:123]\n"
      "ds_bpermute_b32 v147, v6, v197\n"
      "v_smfmac_f32_16x16x64_bf16 v[126:129], v[252:255], v[134:141], v5\n"
      "ds_bpermute_b32 v146, v6, v229\n"
      "v_smfmac_f32_16x16x64_bf16 v[126:129], v[150:153], v[162:169], v5\n"
      "ds_bpermute_b32 v242, v7, v33\n"
      "v_mfma_f32_16x16x32_bf16 v[228:231], v[170:173], v[162:165], 0\n"
      "v_exp_f32 v132, v132\n"
      "v_mfma_f32_16x16x32_bf16 v[228:231], v[174:177], v[166:169], v[228:231]\n"
      "v_exp_f32 v133, v133\n"
      "v_mfma_f32_16x16x32_bf16 v[140:143], v[102:105], a[64:67], 0\n"
      "v_cvt_pk_bf16_f32 v59, v24, v25\n"
      "v_cvt_pk_bf16_f32 v135, v132, v133\n"
      "v_mfma_f32_16x16x32_bf16 v[140:143], v[154:157], a[68:71], v[140:143]\n"
      "ds_bpermute_b32 v243, v7, v33 offset:4\n"
      "v_mfma_f32_16x16x32_bf16 v[140:143], v[158:161], a[72:75], v[140:143]\n"
      "s_nop 0\n"
      "v_exp_f32 v230, v230\n"
      "v_mfma_f32_16x16x32_bf16 v[140:143], v[114:117], a[76:79], v[140:143]\n"
      "v_exp_f32 v131, v131\n"
      "v_mfma_f32_16x16x32_bf16 v[38:41], v[86:89], v[162:165], 0\n"
      "v_cvt_pk_bf16_f32 v199, v28, v29\n"
      "v_add_f32 v126, v126, v127\n"
      "v_mfma_f32_16x16x32_bf16 v[38:41], v[90:93], v[166:169], v[38:41]\n"
      "v_exp_f32 v122, v122\n"
      "v_mfma_f32_16x16x32_bf16 v[136:139], v[42:45], v[162:165], 0\n"
      "v_cvt_pk_bf16_f32 v198, v26, v27\n"
      "v_add_f32 v128, v128, v129\n"
      "v_mfma_f32_16x16x32_bf16 v[136:139], v[46:49], v[166:169], v[136:139]\n"
      "v_exp_f32 v128, v128\n"
      "v_mfma_f32_16x16x32_bf16 v[26:29], v[212:215], v[162:165], 0\n"
      "v_exp_f32 v130, v130\n"
      "v_mfma_f32_16x16x32_bf16 v[26:29], v[216:219], v[166:169], v[26:29]\n"
      "v_exp_f32 v41, v41\n"
      "v_mfma_f32_16x16x32_bf16 v[32:35], v[208:211], a[64:67], 0\n"
      "v_exp_f32 v121, v121\n"
      "v_mfma_f32_16x16x32_bf16 v[32:35], v[66:69], a[68:71], v[32:35]\n"
      "v_exp_f32 v40, v40\n"
      "v_mfma_f32_16x16x32_bf16 v[32:35], v[82:85], a[72:75], v[32:35]\n"
      "v_exp_f32 v120, v120\n"
      "v_mfma_f32_16x16x32_bf16 v[32:35], v[62:65], a[76:79], v[32:35]\n"
      "buffer_store_dwordx2 v[224:225], v2, s[44:47], s35 offen offset:256 nt sc1\n"
      "v_cvt_pk_bf16_f32 v58, v22, v23\n"
      "v_mfma_f32_16x16x32_bf16 v[162:165], v[102:105], a[96:99], 0\n"
      "v_exp_f32 v137, v137\n"
      "v_mfma_f32_16x16x32_bf16 v[162:165], v[154:157], a[100:103], v[162:165]\n"
      "v_add_f32_dpp v236, v236, v236 row_ror:8 row_mask:0xf bank_mask:0xf\n"
      "v_cvt_pk_bf16_f32 v167, v142, v143\n"
      "v_mfma_f32_16x16x32_bf16 v[162:165], v[158:161], a[104:107], v[162:165]\n"
      "v_exp_f32 v229, v229\n"
      "v_mfma_f32_16x16x32_bf16 v[162:165], v[114:117], a[108:111], v[162:165]\n"
      "v_cvt_pk_bf16_f32 v134, v130, v131\n"
      "v_cvt_pk_bf16_f32 v168, v32, v33\n"
      "v_mfma_f32_16x16x32_bf16 v[130:133], v[102:105], a[112:115], 0\n"
      "v_cvt_pk_bf16_f32 v224, v120, v121\n"
      "v_cvt_pk_bf16_f32 v169, v34, v35\n"
      "v_mfma_f32_16x16x32_bf16 v[130:133], v[154:157], a[116:119], v[130:133]\n"
      "ds_bpermute_b32 v149, v6, v197 offset:16\n"
      "v_mfma_f32_16x16x32_bf16 v[130:133], v[158:161], a[120:123], v[130:133]\n"
      "v_exp_f32 v231, v231\n"
      "v_mfma_f32_16x16x32_bf16 v[130:133], v[114:117], a[124:127], v[130:133]\n"
      "v_cvt_pk_bf16_f32 v23, v164, v165\n"
      "v_cvt_pk_bf16_f32 v227, v230, v231\n"
      "v_mfma_f32_16x16x32_bf16 v[118:121], v[208:211], a[80:83], 0\n"
      "v_exp_f32 v28, v28\n"
      "v_mfma_f32_16x16x32_bf16 v[118:121], v[66:69], a[84:87], v[118:121]\n"
      "v_exp_f32 v136, v136\n"
      "v_mfma_f32_16x16x32_bf16 v[118:121], v[82:85], a[88:91], v[118:121]\n"
      "v_cvt_pk_bf16_f32 v166, v140, v141\n"
      "v_cvt_pk_bf16_f32 v22, v162, v163\n"
      "v_mfma_f32_16x16x32_bf16 v[118:121], v[62:65], a[92:95], v[118:121]\n"
      "v_exp_f32 v228, v228\n"
      "v_mfma_f32_16x16x32_bf16 v[140:143], v[208:211], a[96:99], 0\n"
      "v_cvt_pk_bf16_f32 v197, v132, v133\n"
      "v_cvt_pk_bf16_f32 v196, v130, v131\n"
      "v_mfma_f32_16x16x32_bf16 v[140:143], v[66:69], a[100:103], v[140:143]\n"
      "v_exp_f32 v126, v126\n"
      "v_mfma_f32_16x16x32_bf16 v[140:143], v[82:85], a[104:107], v[140:143]\n"
      "v_exp_f32 v26, v26\n"
      "v_mfma_f32_16x16x32_bf16 v[140:143], v[62:65], a[108:111], v[140:143]\n"
      "v_exp_f32 v139, v139\n"
      "v_mfma_f32_16x16x32_bf16 v[130:133], a[160:163], v[18:21], 0\n"
      "v_cvt_pk_bf16_f32 v226, v228, v229\n"
      "v_cvt_pk_bf16_f32 v165, v120, v121\n"
      "v_mfma_f32_16x16x32_bf16 v[130:133], a[164:167], v[238:241], v[130:133]\n"
      "v_exp_f32 v38, v38\n"
      "v_mfma_f32_16x16x32_bf16 v[130:133], a[168:171], v[178:181], v[130:133]\n"
      "v_exp_f32 v123, v123\n"
      "v_mfma_f32_16x16x32_bf16 v[130:133], a[172:175], v[78:81], v[130:133]\n"
      "v_cvt_pk_bf16_f32 v225, v122, v123\n"
      "v_cvt_pk_bf16_f32 v25, v142, v143\n"
      "v_mfma_f32_16x16x32_bf16 v[30:33], v[102:105], a[80:83], 0\n"
      "v_exp_f32 v39, v39\n"
      "v_mfma_f32_16x16x32_bf16 v[30:33], v[154:157], a[84:87], v[30:33]\n"
      "v_cvt_pk_bf16_f32 v24, v140, v141\n"
      "v_add_f32 v141, v126, v128\n"
      "v_mfma_f32_16x16x32_bf16 v[30:33], v[158:161], a[88:91], v[30:33]\n"
      "v_cvt_pk_bf16_f32 v229, v40, v41\n"
      "v_cvt_pk_bf16_f32 v164, v118, v119\n"
      "v_mfma_f32_16x16x32_bf16 v[30:33], v[114:117], a[92:95], v[30:33]\n"
      "buffer_store_dwordx2 v[220:221], v2, s[44:47], s35 offen offset:0 nt sc1\n"
      "v_cvt_pk_bf16_f32 v228, v38, v39\n"
      "v_mfma_f32_16x16x32_bf16 v[122:125], a[176:179], v[18:21], 0\n"
      "v_add_f32 v140, v126, v128\n"
      "v_cvt_pk_bf16_f32 v34, v130, v131\n"
      "v_mfma_f32_16x16x32_bf16 v[122:125], a[180:183], v[238:241], v[122:125]\n"
      "v_cvt_pk_bf16_f32 v230, v136, v137\n"
      "v_cvt_pk_bf16_f32 v35, v132, v133\n"
      "v_mfma_f32_16x16x32_bf16 v[122:125], a[184:187], v[178:181], v[122:125]\n"
      "v_exp_f32 v27, v27\n"
      "v_mfma_f32_16x16x32_bf16 v[122:125], a[188:191], v[78:81], v[122:125]\n"
      "v_exp_f32 v29, v29\n"
      "v_mfma_f32_16x16x32_bf16 v[118:121], a[144:147], v[18:21], 0\n"
      "v_cvt_pk_bf16_f32 v137, v28, v29\n"
      "v_cvt_pk_bf16_f32 v136, v26, v27\n"
      "v_mfma_f32_16x16x32_bf16 v[118:121], a[148:151], v[238:241], v[118:121]\n"
      "v_permlane16_swap_b32 v140, v141\n"
      "v_mfma_f32_16x16x32_bf16 v[118:121], a[152:155], v[178:181], v[118:121]\n"
      "s_nop 0\n"
      "v_cvt_pk_bf16_f32 v36, v122, v123\n"
      "v_cvt_pk_bf16_f32 v162, v30, v31\n"
      "v_mfma_f32_16x16x32_bf16 v[118:121], a[156:159], v[78:81], v[118:121]\n"
      "v_cvt_pk_bf16_f32 v26, v126, v128\n"
      "v_cvt_pk_bf16_f32 v27, v126, v128\n"
      "v_mfma_f32_16x16x32_bf16 v[128:131], a[128:131], v[18:21], 0\n"
      "v_cvt_pk_bf16_f32 v163, v32, v33\n"
      "v_cvt_pk_bf16_f32 v37, v124, v125\n"
      "v_mfma_f32_16x16x32_bf16 v[128:131], a[132:135], v[238:241], v[128:131]\n"
      "v_permlane16_swap_b32 v26, v27\n"
      "v_mfma_f32_16x16x32_bf16 v[128:131], a[136:139], v[178:181], v[128:131]\n"
      "v_exp_f32 v138, v138\n"
      "v_mfma_f32_16x16x32_bf16 v[128:131], a[140:143], v[78:81], v[128:131]\n"
      "v_cvt_pk_bf16_f32 v231, v138, v139\n"
      "v_mov_b64 v[192:193], v[140:141]\n"
      "v_smfmac_f32_16x16x64_bf16 v[140:143], v[12:15], v[70:77], v5\n"
      "buffer_store_dwordx2 v[222:223], v2, s[44:47], s35 offen offset:512 nt sc1\n"
      "L_join_%=:\n"
      "v_cvt_pk_bf16_f32 v32, v118, v119\n"
      "v_smfmac_f32_16x16x64_bf16 v[192:195], v[12:15], v[224:231], v5\n"
      "s_nop 1\n"
      "v_cvt_pk_bf16_f32 v31, v130, v131\n"
      "v_smfmac_f32_16x16x32_bf16 v[140:143], v[12:13], v[58:61], v5\n"
      "ds_read_b64 v[138:139], v11 offset:4096\n"
      "v_cvt_pk_bf16_f32 v30, v128, v129\n"
      "v_cvt_pk_bf16_f32 v33, v120, v121\n"
      "v_mfma_f32_16x16x4_4b_bf16 v[118:133], v[26:27], v[182:183], 0 cbsz:2 abid:0\n"
      "s_nop 3\n"
      "v_add_f32 v140, v140, v141\n"
      "v_rcp_f32 v141, v140\n"
      "v_smfmac_f32_16x16x32_bf16 v[192:195], v[12:13], v[134:137], v5\n"
      "ds_bpermute_b32 v142, v4, v141\n"
      "v_add_f32_dpp v236, v236, v236 row_ror:4 row_mask:0xf bank_mask:0xf\n"
      "s_nop 0\n"
      "v_mfma_f32_16x16x32_bf16 v[118:121], v[70:73], v[200:203], v[118:121]\n"
      "v_mfma_f32_16x16x32_bf16 v[118:121], v[74:77], v[204:207], v[118:121]\n"
      "buffer_load_dwordx4 v1, s[40:43], s78 offen offset:1024 lds\n"
      "v_mfma_f32_16x16x32_bf16 v[118:121], v[58:61], v[166:169], v[118:121]\n"
      "s_nop 0\n"
      "v_add_f32 v192, v192, v193\n"
      "v_add_f32_dpp v236, v236, v236 row_ror:2 row_mask:0xf bank_mask:0xf\n"
      "v_mfma_f32_16x16x32_bf16 v[122:125], v[70:73], v[50:53], v[122:125]\n"
      "v_rcp_f32 v193, v192\n"
      "v_mfma_f32_16x16x32_bf16 v[122:125], v[74:77], v[54:57], v[122:125]\n"
      "v_add_f32_dpp v236, v236, v236 row_ror:1 row_mask:0xf bank_mask:0xf\n"
      "v_add_u32 v9, 64, v9\n"
      "v_mfma_f32_16x16x32_bf16 v[122:125], v[58:61], v[162:165], v[122:125]\n"
      "v_rcp_f32 v237, v236\n"
      "v_mfma_f32_16x16x32_bf16 v[126:129], v[70:73], v[244:247], v[126:129]\n"
      "v_mfma_f32_16x16x32_bf16 v[126:129], v[74:77], v[248:251], v[126:129]\n"
      "buffer_load_dwordx4 v1, s[40:43], s78 offen offset:3072 lds\n"
      "v_mfma_f32_16x16x32_bf16 v[126:129], v[58:61], v[22:25], v[126:129]\n"
      "s_waitcnt lgkmcnt(0)\n"
      "v_mul_f32_dpp v120, v142, v120 row_newbcast:8 row_mask:0xf bank_mask:0xf\n"
      "v_mfma_f32_16x16x32_bf16 v[130:133], v[70:73], v[94:97], v[130:133]\n"
      "v_mul_f32_dpp v119, v142, v119 row_newbcast:4 row_mask:0xf bank_mask:0xf\n"
      "v_mul_f32_dpp v125, v142, v125 row_newbcast:12 row_mask:0xf bank_mask:0xf\n"
      "v_mfma_f32_16x16x32_bf16 v[130:133], v[74:77], v[98:101], v[130:133]\n"
      "v_mul_f32_dpp v121, v142, v121 row_newbcast:12 row_mask:0xf bank_mask:0xf\n"
      "v_mul_f32_dpp v118, v142, v118 row_newbcast:0 row_mask:0xf bank_mask:0xf\n"
      "v_mfma_f32_16x16x32_bf16 v[130:133], v[58:61], v[196:199], v[130:133]\n"
      "v_mul_f32_dpp v123, v142, v123 row_newbcast:4 row_mask:0xf bank_mask:0xf\n"
      "v_mul_f32_dpp v128, v142, v128 row_newbcast:8 row_mask:0xf bank_mask:0xf\n"
      "v_mfma_f32_16x16x32_bf16 v[118:121], v[208:211], a[192:195], v[118:121]\n"
      "v_mul_f32_dpp v124, v142, v124 row_newbcast:8 row_mask:0xf bank_mask:0xf\n"
      "v_mul_f32_dpp v122, v142, v122 row_newbcast:0 row_mask:0xf bank_mask:0xf\n"
      "v_mfma_f32_16x16x32_bf16 v[118:121], v[66:69], a[196:199], v[118:121]\n"
      "s_nop 1\n"
      "v_mul_f32_dpp v132, v142, v132 row_newbcast:8 row_mask:0xf bank_mask:0xf\n"
      "v_mfma_f32_16x16x32_bf16 v[118:121], v[82:85], a[200:203], v[118:121]\n"
      "ds_read_b128 v[38:41], v0 offset:8192\n"
      "v_mul_f32_dpp v131, v142, v131 row_newbcast:4 row_mask:0xf bank_mask:0xf\n"
      "v_mfma_f32_16x16x32_bf16 v[118:121], v[62:65], a[204:207], v[118:121]\n"
      "v_mul_f32_dpp v133, v142, v133 row_newbcast:12 row_mask:0xf bank_mask:0xf\n"
      "v_lshlrev_b32 v76, 16, v139\n"
      "v_mfma_f32_16x16x32_bf16 v[122:125], v[208:211], a[208:211], v[122:125]\n"
      "s_add_u32 m0, s53, 8192\n"
      "v_mul_f32_dpp v129, v142, v129 row_newbcast:12 row_mask:0xf bank_mask:0xf\n"
      "v_mul_f32_dpp v130, v142, v130 row_newbcast:0 row_mask:0xf bank_mask:0xf\n"
      "v_mfma_f32_16x16x32_bf16 v[122:125], v[66:69], a[212:215], v[122:125]\n"
      "ds_bpermute_b32 v194, v4, v193 offset:128\n"
      "v_mfma_f32_16x16x32_bf16 v[122:125], v[82:85], a[216:219], v[122:125]\n"
      "v_mfma_f32_16x16x32_bf16 v[122:125], v[62:65], a[220:223], v[122:125]\n"
      "v_maximum3_f32 v8, v8, v236, v237\n"
      "v_mul_f32_dpp v127, v142, v127 row_newbcast:4 row_mask:0xf bank_mask:0xf\n"
      "v_mfma_f32_16x16x32_bf16 v[130:133], v[208:211], a[240:243], v[130:133]\n"
      "v_and_b32 v75, 0xffff0000, v138\n"
      "v_mfma_f32_16x16x32_bf16 v[130:133], v[66:69], a[244:247], v[130:133]\n"
      "v_mfma_f32_16x16x32_bf16 v[130:133], v[82:85], a[248:251], v[130:133]\n"
      "v_maximum3_f32 v8, v8, v140, v141\n"
      "v_mul_f32_dpp v126, v142, v126 row_newbcast:0 row_mask:0xf bank_mask:0xf\n"
      "v_mfma_f32_16x16x32_bf16 v[130:133], v[62:65], a[252:255], v[130:133]\n"
      "v_cvt_pk_bf16_f32 v118, v118, v122\n"
      "v_mfma_f32_16x16x32_bf16 v[126:129], v[208:211], a[224:227], v[126:129]\n"
      "v_mfma_f32_16x16x32_bf16 v[126:129], v[66:69], a[228:231], v[126:129]\n"
      "v_pk_max_i16 v118, v118, 0\n"
      "v_and_b32 v77, 0xffff0000, v139\n"
      "v_mfma_f32_16x16x32_bf16 v[126:129], v[82:85], a[232:235], v[126:129]\n"
      "v_mfma_f32_16x16x32_bf16 v[126:129], v[62:65], a[236:239], v[126:129]\n"
      "ds_read_b128 v[232:235], v0 offset:8704\n"
      "v_mfma_f32_16x16x4_4b_bf16 v[58:73], v[26:27], v[182:183], 0 cbsz:2 abid:2\n"
      "ds_read_b128 v[220:223], v0 offset:9216\n"
      "v_lshlrev_b32 v74, 16, v138\n"
      "v_cvt_pk_bf16_f32 v120, v120, v124\n"
      "v_cvt_pk_bf16_f32 v124, v121, v125\n"
      "v_pk_max_i16 v124, v124, 0\n"
      "v_cvt_pk_bf16_f32 v122, v119, v123\n"
      "v_cvt_pk_bf16_f32 v119, v126, v130\n"
      "v_cvt_pk_bf16_f32 v125, v129, v133\n"
      "v_mfma_f32_16x16x32_bf16 v[140:143], v[170:173], v[30:33], 0\n"
      "v_pk_max_i16 v125, v125, 0\n"
      "v_cvt_pk_bf16_f32 v121, v128, v132\n"
      "v_mfma_f32_16x16x32_bf16 v[140:143], v[174:177], v[34:37], v[140:143]\n"
      "buffer_store_dwordx2 v[124:125], v2, s[44:47], s73 offen offset:768 nt sc1\n"
      "v_cvt_pk_bf16_f32 v123, v127, v131\n"
      "v_mfma_f32_16x16x32_bf16 v[70:73], v[224:227], v[94:97], v[70:73]\n"
      "ds_read_b128 v[26:29], v0 offset:9728\n"
      "v_mfma_f32_16x16x32_bf16 v[70:73], v[228:231], v[98:101], v[70:73]\n"
      "v_pk_max_i16 v121, v121, 0\n"
      "v_mfma_f32_16x16x32_bf16 v[70:73], v[134:137], v[196:199], v[70:73]\n"
      "s_nop 0\n"
      "v_exp_f32 v143, v143\n"
      "v_mfma_f32_16x16x32_bf16 v[66:69], v[224:227], v[244:247], v[66:69]\n"
      "v_pk_max_i16 v123, v123, 0\n"
      "v_maximum3_f32 v8, v8, v192, v193\n"
      "v_mfma_f32_16x16x32_bf16 v[66:69], v[228:231], v[248:251], v[66:69]\n"
      "v_exp_f32 v140, v140\n"
      "v_mfma_f32_16x16x32_bf16 v[66:69], v[134:137], v[22:25], v[66:69]\n"
      "v_pk_max_i16 v119, v119, 0\n"
      "v_mfma_f32_16x16x32_bf16 v[58:61], v[224:227], v[200:203], v[58:61]\n"
      "v_exp_f32 v141, v141\n"
      "v_mfma_f32_16x16x32_bf16 v[58:61], v[228:231], v[204:207], v[58:61]\n"
      "s_waitcnt lgkmcnt(3)\n"
      "v_mul_f32_dpp v72, v194, v72 row_newbcast:8 row_mask:0xf bank_mask:0xf\n"
      "v_mul_f32_dpp v70, v194, v70 row_newbcast:0 row_mask:0xf bank_mask:0xf\n"
      "v_mfma_f32_16x16x32_bf16 v[58:61], v[134:137], v[166:169], v[58:61]\n"
      "v_exp_f32 v142, v142\n"
      "v_mfma_f32_16x16x32_bf16 v[62:65], v[224:227], v[50:53], v[62:65]\n"
      "v_mfma_f32_16x16x32_bf16 v[62:65], v[228:231], v[54:57], v[62:65]\n"
      "v_mul_f32_dpp v71, v194, v71 row_newbcast:4 row_mask:0xf bank_mask:0xf\n"
      "v_pk_max_i16 v122, v122, 0\n"
      "v_mfma_f32_16x16x32_bf16 v[62:65], v[134:137], v[162:165], v[62:65]\n"
      "s_waitcnt lgkmcnt(0)\n"
      "buffer_load_dwordx4 v1, s[40:43], s91 offen offset:1024 lds\n"
      "v_mfma_f32_16x16x4_4b_bf16 v[124:139], v[144:145], v[182:183], 0 cbsz:2 abid:0\n"
      "buffer_store_dwordx2 v[122:123], v2, s[44:47], s73 offen offset:256 nt sc1\n"
      "v_mul_f32_dpp v67, v194, v67 row_newbcast:4 row_mask:0xf bank_mask:0xf\n"
      "v_mul_f32_dpp v73, v194, v73 row_newbcast:12 row_mask:0xf bank_mask:0xf\n"
      "v_mul_f32_dpp v66, v194, v66 row_newbcast:0 row_mask:0xf bank_mask:0xf\n"
      "v_mul_f32_dpp v59, v194, v59 row_newbcast:4 row_mask:0xf bank_mask:0xf\n"
      "v_mfma_f32_16x16x32_bf16 v[82:85], v[86:89], v[30:33], 0\n"
      "v_mul_f32_dpp v58, v194, v58 row_newbcast:0 row_mask:0xf bank_mask:0xf\n"
      "v_mfma_f32_16x16x32_bf16 v[82:85], v[90:93], v[34:37], v[82:85]\n"
      "v_mul_f32_dpp v64, v194, v64 row_newbcast:8 row_mask:0xf bank_mask:0xf\n"
      "v_mul_f32_dpp v63, v194, v63 row_newbcast:4 row_mask:0xf bank_mask:0xf\n"
      "v_smfmac_f32_16x16x64_bf16 v[136:139], v[146:149], v[94:101], v5\n"
      "v_mul_f32_dpp v61, v194, v61 row_newbcast:12 row_mask:0xf bank_mask:0xf\n"
      "v_smfmac_f32_16x16x64_bf16 v[124:127], v[146:149], v[200:207], v5\n"
      "v_mul_f32_dpp v65, v194, v65 row_newbcast:12 row_mask:0xf bank_mask:0xf\n"
      "v_smfmac_f32_16x16x64_bf16 v[132:135], v[146:149], v[244:251], v5\n"
      "v_mul_f32_dpp v62, v194, v62 row_newbcast:0 row_mask:0xf bank_mask:0xf\n"
      "v_smfmac_f32_16x16x64_bf16 v[128:131], v[146:149], v[50:57], v5\n"
      "v_cvt_pk_bf16_f32 v227, v142, v143\n"
      "v_smfmac_f32_16x16x32_bf16 v[136:139], v[242:243], v[196:199], v5\n"
      "v_mul_f32_dpp v60, v194, v60 row_newbcast:8 row_mask:0xf bank_mask:0xf\n"
      "v_smfmac_f32_16x16x32_bf16 v[124:127], v[242:243], v[166:169], v5\n"
      "v_mul_f32_dpp v69, v194, v69 row_newbcast:12 row_mask:0xf bank_mask:0xf\n"
      "v_smfmac_f32_16x16x32_bf16 v[132:135], v[242:243], v[22:25], v5\n"
      "v_mul_f32_dpp v68, v194, v68 row_newbcast:8 row_mask:0xf bank_mask:0xf\n"
      "v_smfmac_f32_16x16x32_bf16 v[128:131], v[242:243], v[162:165], v5\n"
      "buffer_load_dwordx4 v1, s[40:43], s91 offen offset:3072 lds\n"
      "v_mov_b64 v[194:195], 0\n"
      "v_cvt_pk_bf16_f32 v226, v140, v141\n"
      "v_mfma_f32_16x16x32_bf16 v[144:147], v[212:215], v[30:33], 0\n"
      "v_exp_f32 v82, v82\n"
      "v_mfma_f32_16x16x32_bf16 v[144:147], v[216:219], v[34:37], v[144:147]\n"
      "v_add_f32 v132, v132, v133\n"
      "v_fma_f32 v132, v132, v237, v76\n"
      "v_mfma_f32_16x16x32_bf16 v[140:143], v[106:109], v[30:33], 0\n"
      "v_add_f32 v128, v128, v129\n"
      "v_fma_f32 v128, v128, v237, v75\n"
      "v_mfma_f32_16x16x32_bf16 v[140:143], v[110:113], v[34:37], v[140:143]\n"
      "v_add_f32 v136, v136, v137\n"
      "v_fma_f32 v136, v136, v237, v77\n"
      "v_mfma_f32_16x16x32_bf16 v[62:65], v[102:105], a[208:211], v[62:65]\n"
      "v_exp_f32 v146, v146\n"
      "v_mfma_f32_16x16x32_bf16 v[62:65], v[154:157], a[212:215], v[62:65]\n"
      "v_exp_f32 v145, v145\n"
      "v_mfma_f32_16x16x32_bf16 v[62:65], v[158:161], a[216:219], v[62:65]\n"
      "v_add_f32 v124, v124, v125\n"
      "v_pk_max_i16 v120, v120, 0\n"
      "v_mfma_f32_16x16x32_bf16 v[62:65], v[114:117], a[220:223], v[62:65]\n"
      "v_cvt_pk_bf16_f32 v125, v132, v136\n"
      "v_mov_b64 v[192:193], 0\n"
      "v_mfma_f32_16x16x32_bf16 v[136:139], v[42:45], v[30:33], 0\n"
      "v_pk_max_i16 v125, v125, 0\n"
      "v_mfma_f32_16x16x32_bf16 v[136:139], v[46:49], v[34:37], v[136:139]\n"
      "v_add_u32 v16, 64, v16\n"
      "v_fma_f32 v124, v124, v237, v74\n"
      "v_mfma_f32_16x16x32_bf16 v[208:211], v[184:187], v[30:33], 0\n"
      "v_exp_f32 v83, v83\n"
      "v_mfma_f32_16x16x32_bf16 v[208:211], v[188:191], v[34:37], v[208:211]\n"
      "buffer_store_dwordx2 v[120:121], v2, s[44:47], s73 offen offset:512 nt sc1\n"
      "v_cvt_pk_bf16_f32 v124, v124, v128\n"
      "v_smfmac_f32_16x16x64_bf16 v[192:195], v[252:255], v[30:37], v5\n"
      "buffer_load_dwordx4 v1, s[40:43], s90 offen offset:0 lds\n"
      "v_mfma_f32_16x16x32_bf16 v[58:61], v[102:105], a[192:195], v[58:61]\n"
      "v_exp_f32 v85, v85\n"
      "v_mfma_f32_16x16x32_bf16 v[58:61], v[154:157], a[196:199], v[58:61]\n"
      "v_exp_f32 v141, v141\n"
      "v_mfma_f32_16x16x32_bf16 v[58:61], v[158:161], a[200:203], v[58:61]\n"
      "v_exp_f32 v140, v140\n"
      "v_mfma_f32_16x16x32_bf16 v[58:61], v[114:117], a[204:207], v[58:61]\n"
      "v_exp_f32 v208, v208\n"
      "v_mfma_f32_16x16x32_bf16 v[132:135], a[128:131], v[38:41], 0\n"
      "v_exp_f32 v137, v137\n"
      "v_mfma_f32_16x16x32_bf16 v[132:135], a[132:135], v[232:235], v[132:135]\n"
      "v_exp_f32 v147, v147\n"
      "v_mfma_f32_16x16x32_bf16 v[132:135], a[136:139], v[220:223], v[132:135]\n"
      "v_exp_f32 v142, v142\n"
      "v_mfma_f32_16x16x32_bf16 v[132:135], a[140:143], v[26:29], v[132:135]\n"
      "v_exp_f32 v144, v144\n"
      "v_mfma_f32_16x16x32_bf16 v[120:123], a[144:147], v[38:41], 0\n"
      "v_exp_f32 v84, v84\n"
      "v_mfma_f32_16x16x32_bf16 v[120:123], a[148:151], v[232:235], v[120:123]\n"
      "v_cvt_pk_bf16_f32 v58, v58, v62\n"
      "v_pk_max_i16 v58, v58, 0\n"
      "v_mfma_f32_16x16x32_bf16 v[120:123], a[152:155], v[220:223], v[120:123]\n"
      "v_exp_f32 v143, v143\n"
      "v_mfma_f32_16x16x32_bf16 v[120:123], a[156:159], v[26:29], v[120:123]\n"
      "v_cvt_pk_bf16_f32 v129, v134, v135\n"
      "v_cvt_pk_bf16_f32 v62, v59, v63\n"
      "v_mfma_f32_16x16x32_bf16 v[32:35], a[176:179], v[38:41], 0\n"
      "v_exp_f32 v211, v211\n"
      "v_mfma_f32_16x16x32_bf16 v[32:35], a[180:183], v[232:235], v[32:35]\n"
      "v_exp_f32 v210, v210\n"
      "v_mfma_f32_16x16x32_bf16 v[32:35], a[184:187], v[220:223], v[32:35]\n"
      "v_cvt_pk_bf16_f32 v128, v132, v133\n"
      "v_cvt_pk_bf16_f32 v130, v120, v121\n"
      "v_mfma_f32_16x16x32_bf16 v[32:35], a[188:191], v[26:29], v[32:35]\n"
      "v_exp_f32 v136, v136\n"
      "v_mfma_f32_16x16x32_bf16 v[74:77], a[160:163], v[38:41], 0\n"
      "v_exp_f32 v209, v209\n"
      "v_mfma_f32_16x16x32_bf16 v[74:77], a[164:167], v[232:235], v[74:77]\n"
      "v_cvt_pk_bf16_f32 v225, v210, v211\n"
      "v_cvt_pk_bf16_f32 v131, v122, v123\n"
      "v_mfma_f32_16x16x32_bf16 v[74:77], a[168:171], v[220:223], v[74:77]\n"
      "v_cvt_pk_bf16_f32 v123, v146, v147\n"
      "v_cvt_pk_bf16_f32 v122, v144, v145\n"
      "v_mfma_f32_16x16x32_bf16 v[74:77], a[172:175], v[26:29], v[74:77]\n"
      "v_cvt_pk_bf16_f32 v135, v34, v35\n"
      "v_cvt_pk_bf16_f32 v134, v32, v33\n"
      "v_mfma_f32_16x16x32_bf16 v[70:73], v[102:105], a[240:243], v[70:73]\n"
      "buffer_load_dwordx4 v1, s[40:43], s90 offen offset:2048 lds\n"
      "v_mfma_f32_16x16x32_bf16 v[70:73], v[154:157], a[244:247], v[70:73]\n"
      "v_cvt_pk_bf16_f32 v230, v136, v137\n"
      "v_pk_max_i16 v124, v124, 0\n"
      "v_mfma_f32_16x16x32_bf16 v[70:73], v[158:161], a[248:251], v[70:73]\n"
      "v_cvt_pk_bf16_f32 v60, v60, v64\n"
      "v_cvt_pk_bf16_f32 v64, v61, v65\n"
      "v_mfma_f32_16x16x32_bf16 v[70:73], v[114:117], a[252:255], v[70:73]\n"
      "v_cvt_pk_bf16_f32 v132, v74, v75\n"
      "v_cvt_pk_bf16_f32 v133, v76, v77\n"
      "v_mfma_f32_16x16x32_bf16 v[30:33], v[106:109], v[128:131], 0\n"
      "v_exp_f32 v138, v138\n"
      "v_mfma_f32_16x16x32_bf16 v[30:33], v[110:113], v[132:135], v[30:33]\n"
      "v_exp_f32 v139, v139\n"
      "v_smfmac_f32_16x16x64_bf16 v[192:195], v[150:153], v[128:135], v5\n"
      "buffer_store_dwordx2 v[118:119], v2, s[44:47], s73 offen offset:0 nt sc1\n"
      "v_cvt_pk_bf16_f32 v228, v82, v83\n"
      "v_mfma_f32_16x16x32_bf16 v[66:69], v[102:105], a[224:227], v[66:69]\n"
      "v_cvt_pk_bf16_f32 v229, v84, v85\n"
      "v_cvt_pk_bf16_f32 v224, v208, v209\n"
      "v_mfma_f32_16x16x32_bf16 v[66:69], v[154:157], a[228:231], v[66:69]\n"
      "ds_read_b128 v[102:105], v0 offset:4096\n"
      "v_cvt_pk_bf16_f32 v120, v140, v141\n"
      "v_mfma_f32_16x16x32_bf16 v[66:69], v[158:161], a[232:235], v[66:69]\n"
      "v_add_f32 v194, v194, v195\n"
      "v_add_f32 v192, v192, v193\n"
      "v_mfma_f32_16x16x32_bf16 v[66:69], v[114:117], a[236:239], v[66:69]\n"
      "v_exp_f32 v194, v194\n"
      "v_mfma_f32_16x16x32_bf16 v[158:161], v[170:173], v[128:131], 0\n"
      "v_exp_f32 v192, v192\n"
      "v_mfma_f32_16x16x32_bf16 v[158:161], v[174:177], v[132:135], v[158:161]\n"
      "v_cvt_pk_bf16_f32 v148, v192, v194\n"
      "v_add_f32 v75, v192, v194\n"
      "v_mfma_f32_16x16x32_bf16 v[114:117], v[184:187], v[128:131], 0\n"
      "v_add_f32 v74, v192, v194\n"
      "v_cvt_pk_bf16_f32 v61, v68, v72\n"
      "v_mfma_f32_16x16x32_bf16 v[114:117], v[188:191], v[132:135], v[114:117]\n"
      "v_cvt_pk_bf16_f32 v149, v192, v194\n"
      "v_cvt_pk_bf16_f32 v63, v67, v71\n"
      "v_mfma_f32_16x16x32_bf16 v[34:37], v[42:45], v[128:131], 0\n"
      "v_cvt_pk_bf16_f32 v65, v69, v73\n"
      "v_pk_max_i16 v65, v65, 0\n"
      "v_mfma_f32_16x16x32_bf16 v[34:37], v[46:49], v[132:135], v[34:37]\n"
      "v_permlane16_swap_b32 v148, v149\n"
      "v_mfma_f32_16x16x32_bf16 v[154:157], v[212:215], v[128:131], 0\n"
      "v_permlane16_swap_b32 v74, v75\n"
      "v_mfma_f32_16x16x32_bf16 v[154:157], v[216:219], v[132:135], v[154:157]\n"
      "v_exp_f32 v115, v115\n"
      "v_mfma_f32_16x16x32_bf16 v[82:85], v[86:89], v[128:131], 0\n"
      "v_mov_b64 v[208:209], v[74:75]\n"
      "v_cvt_pk_bf16_f32 v231, v138, v139\n"
      "v_mfma_f32_16x16x32_bf16 v[82:85], v[90:93], v[132:135], v[82:85]\n"
      "v_exp_f32 v34, v34\n"
      "v_smfmac_f32_16x16x64_bf16 v[74:77], v[12:15], v[224:231], v5\n"
      "buffer_store_dwordx2 v[124:125], v3, s[44:47], s79 offen offset:0 nt sc1\n"
      "v_cvt_pk_bf16_f32 v121, v142, v143\n"
      "v_mfma_f32_16x16x4_4b_bf16 v[126:141], v[148:149], v[182:183], 0 cbsz:2 abid:0\n"
      "s_add_u32 m0, s53, 4096\n"
      "v_pk_max_i16 v63, v63, 0\n"
      "v_cvt_pk_bf16_f32 v59, v66, v70\n"
      "v_exp_f32 v36, v36\n"
      "v_exp_f32 v85, v85\n"
      "v_smfmac_f32_16x16x32_bf16 v[74:77], v[12:13], v[120:123], v5\n"
      "ds_read_b128 v[192:195], v0 offset:4608\n"
      "ds_read_b128 v[66:69], v0 offset:5632\n"
      "ds_read_b128 v[142:145], v0 offset:5120\n"
      "v_pk_max_i16 v59, v59, 0\n"
      "v_mfma_f32_16x16x32_bf16 v[134:137], v[224:227], v[244:247], v[134:137]\n"
      "v_exp_f32 v160, v160\n"
      "v_mfma_f32_16x16x32_bf16 v[134:137], v[228:231], v[248:251], v[134:137]\n"
      "v_exp_f32 v33, v33\n"
      "v_mfma_f32_16x16x32_bf16 v[134:137], v[120:123], v[22:25], v[134:137]\n"
      "v_pk_max_i16 v62, v62, 0\n"
      "v_add_f32 v74, v74, v75\n"
      "v_mfma_f32_16x16x32_bf16 v[126:129], v[224:227], v[200:203], v[126:129]\n"
      "v_rcp_f32 v75, v74\n"
      "v_mfma_f32_16x16x32_bf16 v[126:129], v[228:231], v[204:207], v[126:129]\n"
      "v_exp_f32 v161, v161\n"
      "v_mfma_f32_16x16x32_bf16 v[126:129], v[120:123], v[166:169], v[126:129]\n"
      "ds_bpermute_b32 v76, v4, v75\n"
      "v_mfma_f32_16x16x32_bf16 v[130:133], v[224:227], v[50:53], v[130:133]\n"
      "v_exp_f32 v82, v82\n"
      "v_mfma_f32_16x16x32_bf16 v[130:133], v[228:231], v[54:57], v[130:133]\n"
      "v_exp_f32 v156, v156\n"
      "v_mfma_f32_16x16x32_bf16 v[130:133], v[120:123], v[162:165], v[130:133]\n"
      "v_exp_f32 v159, v159\n"
      "v_mfma_f32_16x16x32_bf16 v[138:141], v[224:227], v[94:97], v[138:141]\n"
      "s_waitcnt lgkmcnt(1)\n"
      "buffer_load_dwordx4 v1, s[40:43], s92 offen offset:0 lds\n"
      "v_mfma_f32_16x16x32_bf16 v[138:141], v[228:231], v[98:101], v[138:141]\n"
      "v_exp_f32 v116, v116\n"
      "v_mfma_f32_16x16x32_bf16 v[138:141], v[120:123], v[196:199], v[138:141]\n"
      "v_exp_f32 v30, v30\n"
      "v_mfma_f32_16x16x32_bf16 v[70:73], a[128:131], v[102:105], 0\n"
      "v_exp_f32 v37, v37\n"
      "v_mfma_f32_16x16x32_bf16 v[70:73], a[132:135], v[192:195], v[70:73]\n"
      "v_exp_f32 v158, v158\n"
      "v_mfma_f32_16x16x32_bf16 v[70:73], a[136:139], v[142:145], v[70:73]\n"
      "v_exp_f32 v155, v155\n"
      "v_mfma_f32_16x16x32_bf16 v[70:73], a[140:143], v[66:69], v[70:73]\n"
      "v_exp_f32 v114, v114\n"
      "v_mfma_f32_16x16x32_bf16 v[118:121], a[144:147], v[102:105], 0\n"
      "s_waitcnt lgkmcnt(0)\n"
      "v_mul_f32_dpp v138, v76, v138 row_newbcast:0 row_mask:0xf bank_mask:0xf\n"
      "v_cvt_pk_bf16_f32 v227, v160, v161\n"
      "v_mfma_f32_16x16x32_bf16 v[118:121], a[148:151], v[192:195], v[118:121]\n"
      "v_exp_f32 v117, v117\n"
      "v_mfma_f32_16x16x32_bf16 v[118:121], a[152:155], v[142:145], v[118:121]\n"
      "v_exp_f32 v32, v32\n"
      "v_mfma_f32_16x16x32_bf16 v[118:121], a[156:159], v[66:69], v[118:121]\n"
      "v_cvt_pk_bf16_f32 v226, v158, v159\n"
      "v_mul_f32_dpp v134, v76, v134 row_newbcast:0 row_mask:0xf bank_mask:0xf\n"
      "v_mfma_f32_16x16x32_bf16 v[122:125], a[160:163], v[102:105], 0\n"
      "v_mul_f32_dpp v130, v76, v130 row_newbcast:0 row_mask:0xf bank_mask:0xf\n"
      "v_mul_f32_dpp v126, v76, v126 row_newbcast:0 row_mask:0xf bank_mask:0xf\n"
      "v_mfma_f32_16x16x32_bf16 v[122:125], a[164:167], v[192:195], v[122:125]\n"
      "v_mul_f32_dpp v129, v76, v129 row_newbcast:12 row_mask:0xf bank_mask:0xf\n"
      "v_cvt_pk_bf16_f32 v225, v116, v117\n"
      "v_mfma_f32_16x16x32_bf16 v[122:125], a[168:171], v[142:145], v[122:125]\n"
      "v_mul_f32_dpp v132, v76, v132 row_newbcast:8 row_mask:0xf bank_mask:0xf\n"
      "v_cvt_pk_bf16_f32 v224, v114, v115\n"
      "v_mfma_f32_16x16x32_bf16 v[122:125], a[172:175], v[66:69], v[122:125]\n"
      "v_cvt_pk_bf16_f32 v114, v70, v71\n"
      "v_cvt_pk_bf16_f32 v115, v72, v73\n"
      "v_mfma_f32_16x16x32_bf16 v[70:73], a[176:179], v[102:105], 0\n"
      "buffer_load_dwordx4 v1, s[40:43], s92 offen offset:2048 lds\n"
      "v_mfma_f32_16x16x32_bf16 v[70:73], a[180:183], v[192:195], v[70:73]\n"
      "s_not_b64 vcc, vcc\n"
      "v_mul_f32_dpp v131, v76, v131 row_newbcast:4 row_mask:0xf bank_mask:0xf\n"
      "v_mul_f32_dpp v133, v76, v133 row_newbcast:12 row_mask:0xf bank_mask:0xf\n"
      "v_mfma_f32_16x16x32_bf16 v[70:73], a[184:187], v[142:145], v[70:73]\n"
      "v_mul_f32_dpp v128, v76, v128 row_newbcast:8 row_mask:0xf bank_mask:0xf\n"
      "v_maximum3_f32 v8, v8, v74, v75\n"
      "v_mfma_f32_16x16x32_bf16 v[70:73], a[188:191], v[66:69], v[70:73]\n"
      "v_pk_max_i16 v61, v61, 0\n"
      "v_mul_f32_dpp v137, v76, v137 row_newbcast:12 row_mask:0xf bank_mask:0xf\n"
      "v_mfma_f32_16x16x32_bf16 v[130:133], v[18:21], a[208:211], v[130:133]\n"
      "v_cvt_pk_bf16_f32 v117, v120, v121\n"
      "v_mul_f32_dpp v139, v76, v139 row_newbcast:4 row_mask:0xf bank_mask:0xf\n"
      "v_mfma_f32_16x16x32_bf16 v[130:133], v[238:241], a[212:215], v[130:133]\n"
      "v_mul_f32_dpp v127, v76, v127 row_newbcast:4 row_mask:0xf bank_mask:0xf\n"
      "v_cvt_pk_bf16_f32 v116, v118, v119\n"
      "v_mfma_f32_16x16x32_bf16 v[130:133], v[178:181], a[216:219], v[130:133]\n"
      "v_exp_f32 v84, v84\n"
      "v_mfma_f32_16x16x32_bf16 v[130:133], v[78:81], a[220:223], v[130:133]\n"
      "v_mul_f32_dpp v141, v76, v141 row_newbcast:12 row_mask:0xf bank_mask:0xf\n"
      "v_mul_f32_dpp v136, v76, v136 row_newbcast:8 row_mask:0xf bank_mask:0xf\n"
      "v_mfma_f32_16x16x32_bf16 v[126:129], v[18:21], a[192:195], v[126:129]\n"
      "v_cvt_pk_bf16_f32 v229, v84, v85\n"
      "v_mul_f32_dpp v140, v76, v140 row_newbcast:8 row_mask:0xf bank_mask:0xf\n"
      "v_mfma_f32_16x16x32_bf16 v[126:129], v[238:241], a[196:199], v[126:129]\n"
      "v_exp_f32 v154, v154\n"
      "v_mfma_f32_16x16x32_bf16 v[126:129], v[178:181], a[200:203], v[126:129]\n"
      "v_mul_f32_dpp v135, v76, v135 row_newbcast:4 row_mask:0xf bank_mask:0xf\n"
      "v_cvt_pk_bf16_f32 v120, v70, v71\n"
      "v_mfma_f32_16x16x32_bf16 v[126:129], v[78:81], a[204:207], v[126:129]\n"
      "v_cvt_pk_bf16_f32 v119, v124, v125\n"
      "v_cvt_pk_bf16_f32 v118, v122, v123\n"
      "s_sub_u32 s38, s38, 1\n"
      "s_cbranch_scc1 L_drain_%=\n"
      "v_mfma_f32_16x16x32_bf16 v[134:137], v[18:21], a[224:227], v[134:137]\n"
      "v_exp_f32 v35, v35\n"
      "v_mfma_f32_16x16x32_bf16 v[134:137], v[238:241], a[228:231], v[134:137]\n"
      "v_exp_f32 v157, v157\n"
      "v_mfma_f32_16x16x32_bf16 v[134:137], v[178:181], a[232:235], v[134:137]\n"
      "v_cvt_pk_bf16_f32 v121, v72, v73\n"
      "v_cvt_pk_bf16_f32 v230, v34, v35\n"
      "v_mfma_f32_16x16x32_bf16 v[134:137], v[78:81], a[236:239], v[134:137]\n"
      "buffer_store_dwordx2 v[58:59], v2, s[44:47], s75 offen offset:0 nt sc1\n"
      "v_cvt_pk_bf16_f32 v161, v156, v157\n"
      "v_mfma_f32_16x16x32_bf16 v[138:141], v[18:21], a[240:243], v[138:141]\n"
      "v_cvt_pk_bf16_f32 v126, v126, v130\n"
      "v_cvt_pk_bf16_f32 v160, v154, v155\n"
      "v_mfma_f32_16x16x32_bf16 v[138:141], v[238:241], a[244:247], v[138:141]\n"
      "v_exp_f32 v31, v31\n"
      "v_mfma_f32_16x16x32_bf16 v[138:141], v[178:181], a[248:251], v[138:141]\n"
      "v_exp_f32 v83, v83\n"
      "v_mfma_f32_16x16x32_bf16 v[138:141], v[78:81], a[252:255], v[138:141]\n"
      "v_cvt_pk_bf16_f32 v228, v82, v83\n"
      "v_cvt_pk_bf16_f32 v130, v127, v131\n"
      "v_mfma_f32_16x16x32_bf16 v[154:157], v[184:187], v[114:117], 0\n"
      "v_pk_max_i16 v60, v60, 0\n"
      "v_cvt_pk_bf16_f32 v128, v128, v132\n"
      "v_mfma_f32_16x16x32_bf16 v[154:157], v[188:191], v[118:121], v[154:157]\n"
      "v_cvt_pk_bf16_f32 v159, v32, v33\n"
      "v_cvt_pk_bf16_f32 v132, v129, v133\n"
      "v_mfma_f32_16x16x32_bf16 v[18:21], v[106:109], v[114:117], 0\n"
      "buffer_load_dwordx4 v1, s[40:43], s93 offen offset:3072 lds\n"
      "v_mfma_f32_16x16x32_bf16 v[18:21], v[110:113], v[118:121], v[18:21]\n"
      "v_cvt_pk_bf16_f32 v127, v134, v138\n"
      "v_cvt_pk_bf16_f32 v158, v30, v31\n"
      "v_mfma_f32_16x16x32_bf16 v[240:243], v[170:173], v[114:117], 0\n"
      "v_exp_f32 v156, v156\n"
      "v_mfma_f32_16x16x32_bf16 v[240:243], v[174:177], v[118:121], v[240:243]\n"
      "v_cvt_pk_bf16_f32 v231, v36, v37\n"
      "v_cvt_pk_bf16_f32 v131, v135, v139\n"
      "v_mfma_f32_16x16x32_bf16 v[178:181], v[212:215], v[114:117], 0\n"
      "v_exp_f32 v21, v21\n"
      "v_mfma_f32_16x16x32_bf16 v[178:181], v[216:219], v[118:121], v[178:181]\n"
      "v_mov_b64 v[32:33], 0\n"
      "v_mov_b64 v[30:31], 0\n"
      "v_smfmac_f32_16x16x64_bf16 v[208:211], v[12:15], v[224:231], v5\n"
      "buffer_store_dwordx2 v[60:61], v2, s[44:47], s75 offen offset:512 nt sc1\n"
      "v_cvt_pk_bf16_f32 v129, v136, v140\n"
      "v_mfma_f32_16x16x4_4b_bf16 v[70:85], v[148:149], v[182:183], 0 cbsz:2 abid:2\n"
      "v_exp_f32 v242, v242\n"
      "v_exp_f32 v154, v154\n"
      "v_exp_f32 v243, v243\n"
      "v_smfmac_f32_16x16x32_bf16 v[208:211], v[12:13], v[158:161], v5\n"
      "buffer_load_dwordx4 v1, s[40:43], s93 offen offset:1024 lds\n"
      "v_exp_f32 v155, v155\n"
      "v_smfmac_f32_16x16x64_bf16 v[30:33], v[252:255], v[114:121], v5\n"
      "ds_read_b128 v[122:125], v0 offset:512\n"
      "v_cvt_pk_bf16_f32 v133, v137, v141\n"
      "v_mfma_f32_16x16x32_bf16 v[58:61], v[42:45], v[114:117], 0\n"
      "v_exp_f32 v157, v157\n"
      "v_mfma_f32_16x16x32_bf16 v[58:61], v[46:49], v[118:121], v[58:61]\n"
      "v_add_f32 v208, v208, v209\n"
      "v_cvt_pk_bf16_f32 v237, v156, v157\n"
      "v_mfma_f32_16x16x32_bf16 v[134:137], v[86:89], v[114:117], 0\n"
      "v_rcp_f32 v209, v208\n"
      "v_mfma_f32_16x16x32_bf16 v[134:137], v[90:93], v[118:121], v[134:137]\n"
      "ds_bpermute_b32 v210, v4, v209 offset:128\n"
      "v_mfma_f32_16x16x32_bf16 v[78:81], v[224:227], v[244:247], v[78:81]\n"
      "ds_read_b128 v[146:149], v0 offset:1024\n"
      "v_cvt_pk_bf16_f32 v236, v154, v155\n"
      "v_mfma_f32_16x16x32_bf16 v[78:81], v[228:231], v[248:251], v[78:81]\n"
      "v_exp_f32 v19, v19\n"
      "v_mfma_f32_16x16x32_bf16 v[78:81], v[158:161], v[22:25], v[78:81]\n"
      "ds_read_b128 v[118:121], v0 offset:1536\n"
      "ds_read_b128 v[252:255], v0 offset:0\n"
      "v_mfma_f32_16x16x32_bf16 v[70:73], v[224:227], v[200:203], v[70:73]\n"
      "v_exp_f32 v137, v137\n"
      "v_mfma_f32_16x16x32_bf16 v[70:73], v[228:231], v[204:207], v[70:73]\n"
      "v_exp_f32 v240, v240\n"
      "v_mfma_f32_16x16x32_bf16 v[70:73], v[158:161], v[166:169], v[70:73]\n"
      "v_exp_f32 v134, v134\n"
      "v_mfma_f32_16x16x32_bf16 v[82:85], v[224:227], v[94:97], v[82:85]\n"
      "v_exp_f32 v179, v179\n"
      "v_mfma_f32_16x16x32_bf16 v[82:85], v[228:231], v[98:101], v[82:85]\n"
      "v_exp_f32 v181, v181\n"
      "v_mfma_f32_16x16x32_bf16 v[82:85], v[158:161], v[196:199], v[82:85]\n"
      "v_exp_f32 v241, v241\n"
      "v_mfma_f32_16x16x32_bf16 v[74:77], v[224:227], v[50:53], v[74:77]\n"
      "v_exp_f32 v180, v180\n"
      "v_mfma_f32_16x16x32_bf16 v[74:77], v[228:231], v[54:57], v[74:77]\n"
      "v_exp_f32 v58, v58\n"
      "v_mfma_f32_16x16x32_bf16 v[74:77], v[158:161], v[162:165], v[74:77]\n"
      "s_waitcnt lgkmcnt(3)\n"
      "v_mul_f32_dpp v73, v210, v73 row_newbcast:12 row_mask:0xf bank_mask:0xf\n"
      "s_waitcnt lgkmcnt(0)\n"
      "v_mfma_f32_16x16x32_bf16 v[114:117], a[160:163], v[252:255], 0\n"
      "v_exp_f32 v20, v20\n"
      "v_mfma_f32_16x16x32_bf16 v[114:117], a[164:167], v[122:125], v[114:117]\n"
      "v_exp_f32 v18, v18\n"
      "v_mfma_f32_16x16x32_bf16 v[114:117], a[168:171], v[146:149], v[114:117]\n"
      "s_add_u32 m0, s53, 0\n"
      "v_mul_f32_dpp v81, v210, v81 row_newbcast:12 row_mask:0xf bank_mask:0xf\n"
      "v_mul_f32_dpp v85, v210, v85 row_newbcast:12 row_mask:0xf bank_mask:0xf\n"
      "v_mfma_f32_16x16x32_bf16 v[114:117], a[172:175], v[118:121], v[114:117]\n"
      "v_exp_f32 v178, v178\n"
      "v_mfma_f32_16x16x32_bf16 v[228:231], a[176:179], v[252:255], 0\n"
      "v_mul_f32_dpp v71, v210, v71 row_newbcast:4 row_mask:0xf bank_mask:0xf\n"
      "v_mul_f32_dpp v74, v210, v74 row_newbcast:0 row_mask:0xf bank_mask:0xf\n"
      "v_mfma_f32_16x16x32_bf16 v[228:231], a[180:183], v[122:125], v[228:231]\n"
      "v_cvt_pk_bf16_f32 v238, v240, v241\n"
      "v_mul_f32_dpp v78, v210, v78 row_newbcast:0 row_mask:0xf bank_mask:0xf\n"
      "v_mfma_f32_16x16x32_bf16 v[228:231], a[184:187], v[146:149], v[228:231]\n"
      "v_cvt_pk_bf16_f32 v239, v242, v243\n"
      "v_mul_f32_dpp v84, v210, v84 row_newbcast:8 row_mask:0xf bank_mask:0xf\n"
      "v_mfma_f32_16x16x32_bf16 v[228:231], a[188:191], v[118:121], v[228:231]\n"
      "v_exp_f32 v136, v136\n"
      "v_mfma_f32_16x16x32_bf16 v[240:243], a[128:131], v[252:255], 0\n"
      "buffer_load_dwordx4 v1, s[40:43], s94 offen offset:2048 lds\n"
      "v_mfma_f32_16x16x32_bf16 v[240:243], a[132:135], v[122:125], v[240:243]\n"
      "v_cvt_pk_bf16_f32 v159, v116, v117\n"
      "v_mul_f32_dpp v76, v210, v76 row_newbcast:8 row_mask:0xf bank_mask:0xf\n"
      "v_mfma_f32_16x16x32_bf16 v[240:243], a[136:139], v[146:149], v[240:243]\n"
      "v_mul_f32_dpp v70, v210, v70 row_newbcast:0 row_mask:0xf bank_mask:0xf\n"
      "v_mul_f32_dpp v72, v210, v72 row_newbcast:8 row_mask:0xf bank_mask:0xf\n"
      "v_mfma_f32_16x16x32_bf16 v[240:243], a[140:143], v[118:121], v[240:243]\n"
      "v_cvt_pk_bf16_f32 v160, v228, v229\n"
      "v_cvt_pk_bf16_f32 v158, v114, v115\n"
      "v_mfma_f32_16x16x32_bf16 v[114:117], a[144:147], v[252:255], 0\n"
      "v_exp_f32 v59, v59\n"
      "v_mfma_f32_16x16x32_bf16 v[114:117], a[148:151], v[122:125], v[114:117]\n"
      "v_mul_f32_dpp v77, v210, v77 row_newbcast:12 row_mask:0xf bank_mask:0xf\n"
      "v_mul_f32_dpp v79, v210, v79 row_newbcast:4 row_mask:0xf bank_mask:0xf\n"
      "v_mfma_f32_16x16x32_bf16 v[114:117], a[152:155], v[146:149], v[114:117]\n"
      "v_exp_f32 v60, v60\n"
      "v_mfma_f32_16x16x32_bf16 v[114:117], a[156:159], v[118:121], v[114:117]\n"
      "v_mul_f32_dpp v75, v210, v75 row_newbcast:4 row_mask:0xf bank_mask:0xf\n"
      "v_cvt_pk_bf16_f32 v161, v230, v231\n"
      "v_mfma_f32_16x16x32_bf16 v[70:73], v[38:41], a[192:195], v[70:73]\n"
      "v_mul_f32_dpp v80, v210, v80 row_newbcast:8 row_mask:0xf bank_mask:0xf\n"
      "v_cvt_pk_bf16_f32 v154, v240, v241\n"
      "v_mfma_f32_16x16x32_bf16 v[70:73], v[232:235], a[196:199], v[70:73]\n"
      "v_cvt_pk_bf16_f32 v155, v242, v243\n"
      "v_cvt_pk_bf16_f32 v141, v180, v181\n"
      "v_mfma_f32_16x16x32_bf16 v[70:73], v[220:223], a[200:203], v[70:73]\n"
      "v_cvt_pk_bf16_f32 v156, v114, v115\n"
      "v_cvt_pk_bf16_f32 v157, v116, v117\n"
      "v_mfma_f32_16x16x32_bf16 v[70:73], v[26:29], a[204:207], v[70:73]\n"
      "v_exp_f32 v61, v61\n"
      "v_smfmac_f32_16x16x64_bf16 v[30:33], v[150:153], v[154:161], v5\n"
      "buffer_store_dwordx2 v[62:63], v2, s[44:47], s75 offen offset:256 nt sc1\n"
      "v_cvt_pk_bf16_f32 v138, v18, v19\n"
      "v_mfma_f32_16x16x32_bf16 v[74:77], v[38:41], a[208:211], v[74:77]\n"
      "v_mul_f32_dpp v82, v210, v82 row_newbcast:0 row_mask:0xf bank_mask:0xf\n"
      "v_mul_f32_dpp v83, v210, v83 row_newbcast:4 row_mask:0xf bank_mask:0xf\n"
      "v_mfma_f32_16x16x32_bf16 v[74:77], v[232:235], a[212:215], v[74:77]\n"
      "v_exp_f32 v135, v135\n"
      "v_mfma_f32_16x16x32_bf16 v[74:77], v[220:223], a[216:219], v[74:77]\n"
      "v_add_f32 v32, v32, v33\n"
      "v_cvt_pk_bf16_f32 v242, v58, v59\n"
      "v_mfma_f32_16x16x32_bf16 v[74:77], v[26:29], a[220:223], v[74:77]\n"
      "v_cvt_pk_bf16_f32 v243, v60, v61\n"
      "v_add_f32 v30, v30, v31\n"
      "v_mfma_f32_16x16x32_bf16 v[82:85], v[38:41], a[240:243], v[82:85]\n"
      "v_exp_f32 v30, v30\n"
      "v_mfma_f32_16x16x32_bf16 v[82:85], v[232:235], a[244:247], v[82:85]\n"
      "v_exp_f32 v32, v32\n"
      "v_mfma_f32_16x16x32_bf16 v[78:81], v[38:41], a[224:227], v[78:81]\n"
      "v_add_f32 v114, v30, v32\n"
      "v_add_f32 v115, v30, v32\n"
      "v_mfma_f32_16x16x32_bf16 v[78:81], v[232:235], a[228:231], v[78:81]\n"
      "v_cvt_pk_bf16_f32 v241, v136, v137\n"
      "v_cvt_pk_bf16_f32 v240, v134, v135\n"
      "v_mfma_f32_16x16x32_bf16 v[78:81], v[220:223], a[232:235], v[78:81]\n"
      "v_permlane16_swap_b32 v114, v115\n"
      "v_mfma_f32_16x16x32_bf16 v[78:81], v[26:29], a[236:239], v[78:81]\n"
      "v_cvt_pk_bf16_f32 v135, v30, v32\n"
      "v_mov_b64 v[58:59], v[114:115]\n"
      "v_smfmac_f32_16x16x64_bf16 v[114:117], v[12:15], v[236:243], v5\n"
      "v_cvt_pk_bf16_f32 v134, v30, v32\n"
      "v_mfma_f32_16x16x32_bf16 v[82:85], v[220:223], a[248:251], v[82:85]\n"
      "v_cvt_pk_bf16_f32 v140, v178, v179\n"
      "v_cvt_pk_bf16_f32 v139, v20, v21\n"
      "v_mfma_f32_16x16x32_bf16 v[82:85], v[26:29], a[252:255], v[82:85]\n"
      "v_permlane16_swap_b32 v134, v135\n"
      "v_smfmac_f32_16x16x32_bf16 v[114:117], v[12:13], v[138:141], v5\n"
      "v_pk_max_i16 v131, v131, 0\n"
      "v_mfma_f32_16x16x4_4b_bf16 v[26:41], v[134:135], v[182:183], 0 cbsz:2 abid:0\n"
      "buffer_load_dwordx4 v1, s[40:43], s95 offen offset:1024 lds\n"
      "v_pk_max_i16 v64, v64, 0\n"
      "v_cvt_pk_bf16_f32 v72, v72, v76\n"
      "v_pk_max_i16 v129, v129, 0\n"
      "v_mfma_f32_16x16x4_4b_bf16 v[220:235], v[134:135], v[182:183], 0 cbsz:2 abid:2\n"
      "s_nop 0\n"
      "v_add_f32 v114, v114, v115\n"
      "v_pk_max_i16 v126, v126, 0\n"
      "v_rcp_f32 v115, v114\n"
      "v_cvt_pk_bf16_f32 v76, v73, v77\n"
      "v_maximum3_f32 v8, v8, v208, v209\n"
      "v_mfma_f32_16x16x32_bf16 v[26:29], v[236:239], v[200:203], v[26:29]\n"
      "ds_bpermute_b32 v116, v4, v115\n"
      "v_mfma_f32_16x16x32_bf16 v[26:29], v[240:243], v[204:207], v[26:29]\n"
      "v_pk_max_i16 v130, v130, 0\n"
      "v_cvt_pk_bf16_f32 v77, v81, v85\n"
      "v_mfma_f32_16x16x32_bf16 v[26:29], v[138:141], v[166:169], v[26:29]\n"
      "v_cvt_pk_bf16_f32 v73, v80, v84\n"
      "v_mfma_f32_16x16x32_bf16 v[34:37], v[236:239], v[244:247], v[34:37]\n"
      "v_mfma_f32_16x16x32_bf16 v[34:37], v[240:243], v[248:251], v[34:37]\n"
      "buffer_load_dwordx4 v1, s[40:43], s95 offen offset:3072 lds\n"
      "v_mfma_f32_16x16x32_bf16 v[34:37], v[138:141], v[22:25], v[34:37]\n"
      "buffer_store_dwordx2 v[64:65], v2, s[44:47], s75 offen offset:768 nt sc1\n"
      "v_pk_max_i16 v132, v132, 0\n"
      "v_mfma_f32_16x16x32_bf16 v[38:41], v[236:239], v[94:97], v[38:41]\n"
      "v_pk_max_i16 v72, v72, 0\n"
      "v_mfma_f32_16x16x32_bf16 v[38:41], v[240:243], v[98:101], v[38:41]\n"
      "v_add_u32 v10, 64, v10\n"
      "v_mfma_f32_16x16x32_bf16 v[38:41], v[138:141], v[196:199], v[38:41]\n"
      "v_cvt_pk_bf16_f32 v70, v70, v74\n"
      "v_pk_max_i16 v70, v70, 0\n"
      "v_mfma_f32_16x16x32_bf16 v[30:33], v[236:239], v[50:53], v[30:33]\n"
      "s_waitcnt lgkmcnt(0)\n"
      "v_mul_f32_dpp v36, v116, v36 row_newbcast:8 row_mask:0xf bank_mask:0xf\n"
      "v_mul_f32_dpp v34, v116, v34 row_newbcast:0 row_mask:0xf bank_mask:0xf\n"
      "v_mfma_f32_16x16x32_bf16 v[30:33], v[240:243], v[54:57], v[30:33]\n"
      "v_mul_f32_dpp v35, v116, v35 row_newbcast:4 row_mask:0xf bank_mask:0xf\n"
      "v_mul_f32_dpp v37, v116, v37 row_newbcast:12 row_mask:0xf bank_mask:0xf\n"
      "v_mfma_f32_16x16x32_bf16 v[30:33], v[138:141], v[162:165], v[30:33]\n"
      "v_mul_f32_dpp v28, v116, v28 row_newbcast:8 row_mask:0xf bank_mask:0xf\n"
      "v_mul_f32_dpp v39, v116, v39 row_newbcast:4 row_mask:0xf bank_mask:0xf\n"
      "v_mfma_f32_16x16x32_bf16 v[34:37], v[102:105], a[224:227], v[34:37]\n"
      "v_pk_max_i16 v73, v73, 0\n"
      "v_mul_f32_dpp v27, v116, v27 row_newbcast:4 row_mask:0xf bank_mask:0xf\n"
      "v_mfma_f32_16x16x32_bf16 v[34:37], v[192:195], a[228:231], v[34:37]\n"
      "v_mul_f32_dpp v26, v116, v26 row_newbcast:0 row_mask:0xf bank_mask:0xf\n"
      "v_mul_f32_dpp v41, v116, v41 row_newbcast:12 row_mask:0xf bank_mask:0xf\n"
      "v_mfma_f32_16x16x32_bf16 v[34:37], v[142:145], a[232:235], v[34:37]\n"
      "v_mul_f32_dpp v32, v116, v32 row_newbcast:8 row_mask:0xf bank_mask:0xf\n"
      "v_mul_f32_dpp v29, v116, v29 row_newbcast:12 row_mask:0xf bank_mask:0xf\n"
      "v_mfma_f32_16x16x32_bf16 v[34:37], v[66:69], a[236:239], v[34:37]\n"
      "v_mul_f32_dpp v33, v116, v33 row_newbcast:12 row_mask:0xf bank_mask:0xf\n"
      "v_pk_max_i16 v77, v77, 0\n"
      "v_mfma_f32_16x16x32_bf16 v[26:29], v[102:105], a[192:195], v[26:29]\n"
      "v_cvt_pk_bf16_f32 v74, v71, v75\n"
      "v_mul_f32_dpp v31, v116, v31 row_newbcast:4 row_mask:0xf bank_mask:0xf\n"
      "v_mfma_f32_16x16x32_bf16 v[26:29], v[192:195], a[196:199], v[26:29]\n"
      "v_pk_max_i16 v74, v74, 0\n"
      "v_mul_f32_dpp v30, v116, v30 row_newbcast:0 row_mask:0xf bank_mask:0xf\n"
      "v_mfma_f32_16x16x32_bf16 v[26:29], v[142:145], a[200:203], v[26:29]\n"
      "v_mul_f32_dpp v40, v116, v40 row_newbcast:8 row_mask:0xf bank_mask:0xf\n"
      "v_mfma_f32_16x16x32_bf16 v[26:29], v[66:69], a[204:207], v[26:29]\n"
      "v_mul_f32_dpp v38, v116, v38 row_newbcast:0 row_mask:0xf bank_mask:0xf\n"
      "v_cvt_pk_bf16_f32 v71, v78, v82\n"
      "v_mfma_f32_16x16x32_bf16 v[30:33], v[102:105], a[208:211], v[30:33]\n"
      "v_pk_max_i16 v133, v133, 0\n"
      "v_pk_max_i16 v128, v128, 0\n"
      "v_mfma_f32_16x16x32_bf16 v[30:33], v[192:195], a[212:215], v[30:33]\n"
      "v_cvt_pk_bf16_f32 v75, v79, v83\n"
      "v_pk_max_i16 v71, v71, 0\n"
      "v_mfma_f32_16x16x32_bf16 v[30:33], v[142:145], a[216:219], v[30:33]\n"
      "s_add_u32 s44, s44, 102400\n s_addc_u32 s45, s45, 0\n"
      "v_pk_max_i16 v76, v76, 0\n"
      "v_mfma_f32_16x16x32_bf16 v[30:33], v[66:69], a[220:223], v[30:33]\n"
      "v_maximum3_f32 v8, v8, v114, v115\n"
      "v_add_u32 v11, 0x200, v11\n"
      "v_mfma_f32_16x16x32_bf16 v[38:41], v[102:105], a[240:243], v[38:41]\n"
      "v_pk_max_i16 v127, v127, 0\n"
      "v_mfma_f32_16x16x32_bf16 v[38:41], v[192:195], a[244:247], v[38:41]\n"
      "buffer_load_dwordx4 v1, s[40:43], s94 offen offset:0 lds\n"
      "v_mfma_f32_16x16x32_bf16 v[38:41], v[142:145], a[248:251], v[38:41]\n"
      "s_add_u32 s40, s40, 102400\n s_addc_u32 s41, s41, 0\n"
      "v_pk_max_i16 v75, v75, 0\n"
      "v_mfma_f32_16x16x32_bf16 v[38:41], v[66:69], a[252:255], v[38:41]\n"
      "s_sub_u32 s42, s42, 102400\n"
      "s_branch L_task_%=\n"
      "L_drain_%=:\n"
      "v_mfma_f32_16x16x32_bf16 v[138:141], v[18:21], a[240:243], v[138:141]\n"
      "v_exp_f32 v35, v35\n"
      "v_mfma_f32_16x16x32_bf16 v[138:141], v[238:241], a[244:247], v[138:141]\n"
      "v_exp_f32 v31, v31\n"
      "v_mfma_f32_16x16x32_bf16 v[138:141], v[178:181], a[248:251], v[138:141]\n"
      "v_cvt_pk_bf16_f32 v158, v30, v31\n"
      "v_cvt_pk_bf16_f32 v121, v72, v73\n"
      "v_mfma_f32_16x16x32_bf16 v[138:141], v[78:81], a[252:255], v[138:141]\n"
      "v_exp_f32 v83, v83\n"
      "v_mfma_f32_16x16x32_bf16 v[134:137], v[18:21], a[224:227], v[134:137]\n"
      "v_cvt_pk_bf16_f32 v231, v36, v37\n"
      "v_pk_max_i16 v60, v60, 0\n"
      "v_mfma_f32_16x16x32_bf16 v[134:137], v[238:241], a[228:231], v[134:137]\n"
      "v_exp_f32 v157, v157\n"
      "v_mfma_f32_16x16x32_bf16 v[134:137], v[178:181], a[232:235], v[134:137]\n"
      "v_cvt_pk_bf16_f32 v228, v82, v83\n"
      "v_pk_max_i16 v64, v64, 0\n"
      "v_mfma_f32_16x16x32_bf16 v[134:137], v[78:81], a[236:239], v[134:137]\n"
      "v_cvt_pk_bf16_f32 v128, v128, v132\n"
      "v_cvt_pk_bf16_f32 v160, v154, v155\n"
      "v_mfma_f32_16x16x32_bf16 v[178:181], v[212:215], v[114:117], 0\n"
      "v_cvt_pk_bf16_f32 v132, v129, v133\n"
      "v_cvt_pk_bf16_f32 v126, v126, v130\n"
      "v_mfma_f32_16x16x32_bf16 v[178:181], v[216:219], v[118:121], v[178:181]\n"
      "buffer_store_dwordx2 v[58:59], v2, s[44:47], s75 offen offset:0 nt sc1\n"
      "v_cvt_pk_bf16_f32 v161, v156, v157\n"
      "v_mfma_f32_16x16x32_bf16 v[154:157], v[184:187], v[114:117], 0\n"
      "ds_read_b128 v[122:125], v0 offset:512\n"
      "v_cvt_pk_bf16_f32 v130, v127, v131\n"
      "v_mfma_f32_16x16x32_bf16 v[154:157], v[188:191], v[118:121], v[154:157]\n"
      "v_cvt_pk_bf16_f32 v159, v32, v33\n"
      "v_mov_b64 v[32:33], 0\n"
      "v_mfma_f32_16x16x32_bf16 v[18:21], v[106:109], v[114:117], 0\n"
      "v_mov_b64 v[30:31], 0\n"
      "v_pk_max_i16 v132, v132, 0\n"
      "v_mfma_f32_16x16x32_bf16 v[18:21], v[110:113], v[118:121], v[18:21]\n"
      "v_pk_max_i16 v130, v130, 0\n"
      "v_cvt_pk_bf16_f32 v230, v34, v35\n"
      "v_mfma_f32_16x16x32_bf16 v[240:243], v[170:173], v[114:117], 0\n"
      "v_exp_f32 v178, v178\n"
      "v_mfma_f32_16x16x32_bf16 v[240:243], v[174:177], v[118:121], v[240:243]\n"
      "v_cvt_pk_bf16_f32 v127, v134, v138\n"
      "v_cvt_pk_bf16_f32 v129, v136, v140\n"
      "v_smfmac_f32_16x16x64_bf16 v[208:211], v[12:15], v[224:231], v5\n"
      "buffer_store_dwordx2 v[62:63], v2, s[44:47], s75 offen offset:256 nt sc1\n"
      "v_cvt_pk_bf16_f32 v133, v137, v141\n"
      "v_mfma_f32_16x16x4_4b_bf16 v[70:85], v[148:149], v[182:183], 0 cbsz:2 abid:2\n"
      "v_pk_max_i16 v133, v133, 0\n"
      "v_exp_f32 v180, v180\n"
      "v_exp_f32 v19, v19\n"
      "v_cvt_pk_bf16_f32 v131, v135, v139\n"
      "v_smfmac_f32_16x16x32_bf16 v[208:211], v[12:13], v[158:161], v5\n"
      "ds_read_b128 v[146:149], v0 offset:1024\n"
      "v_pk_max_i16 v131, v131, 0\n"
      "v_exp_f32 v243, v243\n"
      "v_smfmac_f32_16x16x64_bf16 v[30:33], v[252:255], v[114:121], v5\n"
      "buffer_store_dwordx2 v[60:61], v2, s[44:47], s75 offen offset:512 nt sc1\n"
      "ds_read_b128 v[252:255], v0 offset:0\n"
      "v_mfma_f32_16x16x32_bf16 v[134:137], v[86:89], v[114:117], 0\n"
      "v_exp_f32 v156, v156\n"
      "v_mfma_f32_16x16x32_bf16 v[134:137], v[90:93], v[118:121], v[134:137]\n"
      "v_add_f32 v208, v208, v209\n"
      "v_pk_max_i16 v129, v129, 0\n"
      "v_mfma_f32_16x16x32_bf16 v[74:77], v[224:227], v[50:53], v[74:77]\n"
      "v_exp_f32 v18, v18\n"
      "v_mfma_f32_16x16x32_bf16 v[74:77], v[228:231], v[54:57], v[74:77]\n"
      "v_exp_f32 v179, v179\n"
      "v_mfma_f32_16x16x32_bf16 v[74:77], v[158:161], v[162:165], v[74:77]\n"
      "v_rcp_f32 v209, v208\n"
      "v_mfma_f32_16x16x32_bf16 v[58:61], v[42:45], v[114:117], 0\n"
      "v_exp_f32 v21, v21\n"
      "v_mfma_f32_16x16x32_bf16 v[58:61], v[46:49], v[118:121], v[58:61]\n"
      "ds_read_b128 v[118:121], v0 offset:1536\n"
      "ds_bpermute_b32 v210, v4, v209 offset:128\n"
      "v_pk_max_i16 v127, v127, 0\n"
      "v_mfma_f32_16x16x32_bf16 v[82:85], v[224:227], v[94:97], v[82:85]\n"
      "v_exp_f32 v240, v240\n"
      "v_mfma_f32_16x16x32_bf16 v[82:85], v[228:231], v[98:101], v[82:85]\n"
      "v_exp_f32 v137, v137\n"
      "v_mfma_f32_16x16x32_bf16 v[82:85], v[158:161], v[196:199], v[82:85]\n"
      "v_exp_f32 v136, v136\n"
      "v_mfma_f32_16x16x32_bf16 v[70:73], v[224:227], v[200:203], v[70:73]\n"
      "v_exp_f32 v154, v154\n"
      "v_mfma_f32_16x16x32_bf16 v[70:73], v[228:231], v[204:207], v[70:73]\n"
      "v_exp_f32 v241, v241\n"
      "v_mfma_f32_16x16x32_bf16 v[70:73], v[158:161], v[166:169], v[70:73]\n"
      "v_exp_f32 v61, v61\n"
      "v_mfma_f32_16x16x32_bf16 v[78:81], v[224:227], v[244:247], v[78:81]\n"
      "v_exp_f32 v20, v20\n"
      "v_mfma_f32_16x16x32_bf16 v[78:81], v[228:231], v[248:251], v[78:81]\n"
      "v_exp_f32 v58, v58\n"
      "v_mfma_f32_16x16x32_bf16 v[78:81], v[158:161], v[22:25], v[78:81]\n"
      "s_waitcnt lgkmcnt(0)\n"
      "v_mul_f32_dpp v82, v210, v82 row_newbcast:0 row_mask:0xf bank_mask:0xf\n"
      "v_mul_f32_dpp v75, v210, v75 row_newbcast:4 row_mask:0xf bank_mask:0xf\n"
      "v_mfma_f32_16x16x32_bf16 v[114:117], a[160:163], v[252:255], 0\n"
      "v_exp_f32 v60, v60\n"
      "v_mfma_f32_16x16x32_bf16 v[114:117], a[164:167], v[122:125], v[114:117]\n"
      "v_exp_f32 v59, v59\n"
      "v_mfma_f32_16x16x32_bf16 v[114:117], a[168:171], v[146:149], v[114:117]\n"
      "v_mul_f32_dpp v76, v210, v76 row_newbcast:8 row_mask:0xf bank_mask:0xf\n"
      "v_mul_f32_dpp v83, v210, v83 row_newbcast:4 row_mask:0xf bank_mask:0xf\n"
      "v_mfma_f32_16x16x32_bf16 v[114:117], a[172:175], v[118:121], v[114:117]\n"
      "v_exp_f32 v181, v181\n"
      "v_mfma_f32_16x16x32_bf16 v[228:231], a[176:179], v[252:255], 0\n"
      "v_mul_f32_dpp v79, v210, v79 row_newbcast:4 row_mask:0xf bank_mask:0xf\n"
      "v_cvt_pk_bf16_f32 v238, v240, v241\n"
      "v_mfma_f32_16x16x32_bf16 v[228:231], a[180:183], v[122:125], v[228:231]\n"
      "v_exp_f32 v157, v157\n"
      "v_mfma_f32_16x16x32_bf16 v[228:231], a[184:187], v[146:149], v[228:231]\n"
      "v_mul_f32_dpp v77, v210, v77 row_newbcast:12 row_mask:0xf bank_mask:0xf\n"
      "v_cvt_pk_bf16_f32 v158, v114, v115\n"
      "v_mfma_f32_16x16x32_bf16 v[228:231], a[188:191], v[118:121], v[228:231]\n"
      "v_mul_f32_dpp v72, v210, v72 row_newbcast:8 row_mask:0xf bank_mask:0xf\n"
      "v_cvt_pk_bf16_f32 v159, v116, v117\n"
      "v_mfma_f32_16x16x32_bf16 v[114:117], a[144:147], v[252:255], 0\n"
      "v_exp_f32 v155, v155\n"
      "v_mfma_f32_16x16x32_bf16 v[114:117], a[148:151], v[122:125], v[114:117]\n"
      "v_exp_f32 v242, v242\n"
      "v_mfma_f32_16x16x32_bf16 v[114:117], a[152:155], v[146:149], v[114:117]\n"
      "s_nop 0\n"
      "v_cvt_pk_bf16_f32 v161, v230, v231\n"
      "v_cvt_pk_bf16_f32 v237, v156, v157\n"
      "v_mfma_f32_16x16x32_bf16 v[114:117], a[156:159], v[118:121], v[114:117]\n"
      "v_cvt_pk_bf16_f32 v239, v242, v243\n"
      "v_mul_f32_dpp v73, v210, v73 row_newbcast:12 row_mask:0xf bank_mask:0xf\n"
      "v_mfma_f32_16x16x32_bf16 v[240:243], a[128:131], v[252:255], 0\n"
      "v_cvt_pk_bf16_f32 v236, v154, v155\n"
      "v_mul_f32_dpp v85, v210, v85 row_newbcast:12 row_mask:0xf bank_mask:0xf\n"
      "v_mfma_f32_16x16x32_bf16 v[240:243], a[132:135], v[122:125], v[240:243]\n"
      "v_exp_f32 v134, v134\n"
      "v_mfma_f32_16x16x32_bf16 v[240:243], a[136:139], v[146:149], v[240:243]\n"
      "v_mul_f32_dpp v74, v210, v74 row_newbcast:0 row_mask:0xf bank_mask:0xf\n"
      "v_cvt_pk_bf16_f32 v157, v116, v117\n"
      "v_mfma_f32_16x16x32_bf16 v[240:243], a[140:143], v[118:121], v[240:243]\n"
      "v_cvt_pk_bf16_f32 v160, v228, v229\n"
      "v_mul_f32_dpp v70, v210, v70 row_newbcast:0 row_mask:0xf bank_mask:0xf\n"
      "v_mfma_f32_16x16x32_bf16 v[74:77], v[38:41], a[208:211], v[74:77]\n"
      "v_mul_f32_dpp v81, v210, v81 row_newbcast:12 row_mask:0xf bank_mask:0xf\n"
      "v_cvt_pk_bf16_f32 v141, v180, v181\n"
      "v_mfma_f32_16x16x32_bf16 v[74:77], v[232:235], a[212:215], v[74:77]\n"
      "v_cvt_pk_bf16_f32 v156, v114, v115\n"
      "v_mul_f32_dpp v84, v210, v84 row_newbcast:8 row_mask:0xf bank_mask:0xf\n"
      "v_mfma_f32_16x16x32_bf16 v[74:77], v[220:223], a[216:219], v[74:77]\n"
      "v_cvt_pk_bf16_f32 v154, v240, v241\n"
      "v_cvt_pk_bf16_f32 v155, v242, v243\n"
      "v_mfma_f32_16x16x32_bf16 v[74:77], v[26:29], a[220:223], v[74:77]\n"
      "v_exp_f32 v135, v135\n"
      "v_smfmac_f32_16x16x64_bf16 v[30:33], v[150:153], v[154:161], v5\n"
      "buffer_store_dwordx2 v[64:65], v2, s[44:47], s75 offen offset:768 nt sc1\n"
      "v_mul_f32_dpp v71, v210, v71 row_newbcast:4 row_mask:0xf bank_mask:0xf\n"
      "v_mfma_f32_16x16x32_bf16 v[82:85], v[38:41], a[240:243], v[82:85]\n"
      "s_add_u32 s44, s44, 102400\n s_addc_u32 s45, s45, 0\n"
      "v_mul_f32_dpp v80, v210, v80 row_newbcast:8 row_mask:0xf bank_mask:0xf\n"
      "v_mfma_f32_16x16x32_bf16 v[82:85], v[232:235], a[244:247], v[82:85]\n"
      "v_pk_max_i16 v126, v126, 0\n"
      "v_cvt_pk_bf16_f32 v138, v18, v19\n"
      "v_mfma_f32_16x16x32_bf16 v[70:73], v[38:41], a[192:195], v[70:73]\n"
      "v_cvt_pk_bf16_f32 v240, v134, v135\n"
      "v_cvt_pk_bf16_f32 v242, v58, v59\n"
      "v_mfma_f32_16x16x32_bf16 v[70:73], v[232:235], a[196:199], v[70:73]\n"
      "v_cvt_pk_bf16_f32 v241, v136, v137\n"
      "v_add_f32 v30, v30, v31\n"
      "v_mfma_f32_16x16x32_bf16 v[70:73], v[220:223], a[200:203], v[70:73]\n"
      "v_exp_f32 v30, v30\n"
      "v_mfma_f32_16x16x32_bf16 v[70:73], v[26:29], a[204:207], v[70:73]\n"
      "buffer_store_dwordx2 v[126:127], v2, s[44:47], s100 offen offset:0 nt sc1\n"
      "v_add_f32 v32, v32, v33\n"
      "v_cvt_pk_bf16_f32 v140, v178, v179\n"
      "v_mfma_f32_16x16x32_bf16 v[178:181], v[212:215], v[154:157], 0\n"
      "v_exp_f32 v32, v32\n"
      "v_mfma_f32_16x16x32_bf16 v[178:181], v[216:219], v[158:161], v[178:181]\n"
      "v_add_f32 v114, v30, v32\n"
      "v_cvt_pk_bf16_f32 v134, v30, v32\n"
      "v_cvt_pk_bf16_f32 v243, v60, v61\n"
      "v_mfma_f32_16x16x32_bf16 v[214:217], v[170:173], v[154:157], 0\n"
      "v_cvt_pk_bf16_f32 v135, v30, v32\n"
      "v_mul_f32_dpp v78, v210, v78 row_newbcast:0 row_mask:0xf bank_mask:0xf\n"
      "v_mfma_f32_16x16x32_bf16 v[214:217], v[174:177], v[158:161], v[214:217]\n"
      "buffer_store_dwordx2 v[132:133], v2, s[44:47], s100 offen offset:768 nt sc1\n"
      "v_add_f32 v115, v30, v32\n"
      "v_mfma_f32_16x16x32_bf16 v[82:85], v[220:223], a[248:251], v[82:85]\n"
      "s_nop 0\n"
      "v_permlane16_swap_b32 v114, v115\n"
      "v_mfma_f32_16x16x32_bf16 v[82:85], v[26:29], a[252:255], v[82:85]\n"
      "v_exp_f32 v180, v180\n"
      "v_mfma_f32_16x16x32_bf16 v[78:81], v[38:41], a[224:227], v[78:81]\n"
      "v_exp_f32 v179, v179\n"
      "v_mfma_f32_16x16x32_bf16 v[78:81], v[232:235], a[228:231], v[78:81]\n"
      "v_permlane16_swap_b32 v134, v135\n"
      "v_mfma_f32_16x16x32_bf16 v[78:81], v[220:223], a[232:235], v[78:81]\n"
      "v_mov_b64 v[58:59], v[114:115]\n"
      "v_pk_max_i16 v128, v128, 0\n"
      "v_mfma_f32_16x16x32_bf16 v[78:81], v[26:29], a[236:239], v[78:81]\n"
      "v_cvt_pk_bf16_f32 v72, v72, v76\n"
      "v_cvt_pk_bf16_f32 v76, v73, v77\n"
      "v_smfmac_f32_16x16x64_bf16 v[114:117], v[12:15], v[236:243], v5\n"
      "buffer_store_dwordx2 v[128:129], v2, s[44:47], s100 offen offset:512 nt sc1\n"
      "v_exp_f32 v181, v181\n"
      "v_cvt_pk_bf16_f32 v70, v70, v74\n"
      "v_mfma_f32_16x16x4_4b_bf16 v[26:41], v[134:135], v[182:183], 0 cbsz:2 abid:0\n"
      "v_cvt_pk_bf16_f32 v74, v71, v75\n"
      "v_cvt_pk_bf16_f32 v71, v78, v82\n"
      "v_pk_max_i16 v70, v70, 0\n"
      "v_pk_max_i16 v72, v72, 0\n"
      "v_cvt_pk_bf16_f32 v139, v20, v21\n"
      "v_exp_f32 v214, v214\n"
      "v_pk_max_i16 v76, v76, 0\n"
      "v_smfmac_f32_16x16x32_bf16 v[114:117], v[12:13], v[138:141], v5\n"
      "v_maximum3_f32 v8, v8, v208, v209\n"
      "v_exp_f32 v178, v178\n"
      "v_mfma_f32_16x16x32_bf16 v[26:29], v[236:239], v[200:203], v[26:29]\n"
      "v_pk_max_i16 v74, v74, 0\n"
      "v_cvt_pk_bf16_f32 v77, v81, v85\n"
      "v_mfma_f32_16x16x32_bf16 v[26:29], v[240:243], v[204:207], v[26:29]\n"
      "v_pk_max_i16 v71, v71, 0\n"
      "v_cvt_pk_bf16_f32 v73, v80, v84\n"
      "v_mfma_f32_16x16x32_bf16 v[26:29], v[138:141], v[166:169], v[26:29]\n"
      "v_pk_max_i16 v77, v77, 0\n"
      "v_add_f32 v114, v114, v115\n"
      "v_mfma_f32_16x16x32_bf16 v[34:37], v[236:239], v[244:247], v[34:37]\n"
      "v_rcp_f32 v115, v114\n"
      "v_mfma_f32_16x16x32_bf16 v[34:37], v[240:243], v[248:251], v[34:37]\n"
      "ds_bpermute_b32 v116, v4, v115\n"
      "v_mfma_f32_16x16x32_bf16 v[34:37], v[138:141], v[22:25], v[34:37]\n"
      "buffer_store_dwordx2 v[76:77], v2, s[44:47], s101 offen offset:768 nt sc1\n"
      "v_pk_max_i16 v73, v73, 0\n"
      "v_mfma_f32_16x16x32_bf16 v[30:33], v[236:239], v[50:53], v[30:33]\n"
      "v_exp_f32 v217, v217\n"
      "v_mfma_f32_16x16x32_bf16 v[30:33], v[240:243], v[54:57], v[30:33]\n"
      "v_cvt_pk_bf16_f32 v75, v79, v83\n"
      "v_pk_max_i16 v75, v75, 0\n"
      "v_mfma_f32_16x16x32_bf16 v[30:33], v[138:141], v[162:165], v[30:33]\n"
      "buffer_store_dwordx2 v[72:73], v2, s[44:47], s101 offen offset:512 nt sc1\n"
      "buffer_store_dwordx2 v[130:131], v2, s[44:47], s100 offen offset:256 nt sc1\n"
      "v_exp_f32 v216, v216\n"
      "v_mfma_f32_16x16x32_bf16 v[38:41], v[236:239], v[94:97], v[38:41]\n"
      "v_exp_f32 v215, v215\n"
      "v_mfma_f32_16x16x32_bf16 v[38:41], v[240:243], v[98:101], v[38:41]\n"
      "v_cvt_pk_bf16_f32 v72, v178, v179\n"
      "v_cvt_pk_bf16_f32 v172, v214, v215\n"
      "v_mfma_f32_16x16x32_bf16 v[38:41], v[138:141], v[196:199], v[38:41]\n"
      "buffer_store_dwordx2 v[74:75], v2, s[44:47], s101 offen offset:256 nt sc1\n"
      "v_maximum3_f32 v8, v8, v114, v115\n"
      "v_mfma_f32_16x16x32_bf16 v[240:243], v[106:109], v[154:157], 0\n"
      "s_waitcnt lgkmcnt(0)\n"
      "v_mul_f32_dpp v33, v116, v33 row_newbcast:12 row_mask:0xf bank_mask:0xf\n"
      "v_mul_f32_dpp v26, v116, v26 row_newbcast:0 row_mask:0xf bank_mask:0xf\n"
      "v_mfma_f32_16x16x32_bf16 v[240:243], v[110:113], v[158:161], v[240:243]\n"
      "v_mul_f32_dpp v27, v116, v27 row_newbcast:4 row_mask:0xf bank_mask:0xf\n"
      "v_mul_f32_dpp v28, v116, v28 row_newbcast:8 row_mask:0xf bank_mask:0xf\n"
      "v_mfma_f32_16x16x32_bf16 v[106:109], v[86:89], v[154:157], 0\n"
      "v_mul_f32_dpp v37, v116, v37 row_newbcast:12 row_mask:0xf bank_mask:0xf\n"
      "v_mul_f32_dpp v29, v116, v29 row_newbcast:12 row_mask:0xf bank_mask:0xf\n"
      "v_mfma_f32_16x16x32_bf16 v[106:109], v[90:93], v[158:161], v[106:109]\n"
      "v_mul_f32_dpp v31, v116, v31 row_newbcast:4 row_mask:0xf bank_mask:0xf\n"
      "v_mul_f32_dpp v36, v116, v36 row_newbcast:8 row_mask:0xf bank_mask:0xf\n"
      "v_mfma_f32_16x16x32_bf16 v[26:29], v[102:105], a[192:195], v[26:29]\n"
      "v_mul_f32_dpp v40, v116, v40 row_newbcast:8 row_mask:0xf bank_mask:0xf\n"
      "v_mul_f32_dpp v38, v116, v38 row_newbcast:0 row_mask:0xf bank_mask:0xf\n"
      "v_mfma_f32_16x16x32_bf16 v[26:29], v[192:195], a[196:199], v[26:29]\n"
      "v_mul_f32_dpp v35, v116, v35 row_newbcast:4 row_mask:0xf bank_mask:0xf\n"
      "v_mul_f32_dpp v39, v116, v39 row_newbcast:4 row_mask:0xf bank_mask:0xf\n"
      "v_mfma_f32_16x16x32_bf16 v[26:29], v[142:145], a[200:203], v[26:29]\n"
      "v_mul_f32_dpp v41, v116, v41 row_newbcast:12 row_mask:0xf bank_mask:0xf\n"
      "v_mul_f32_dpp v32, v116, v32 row_newbcast:8 row_mask:0xf bank_mask:0xf\n"
      "v_mfma_f32_16x16x32_bf16 v[26:29], v[66:69], a[204:207], v[26:29]\n"
      "v_mul_f32_dpp v30, v116, v30 row_newbcast:0 row_mask:0xf bank_mask:0xf\n"
      "v_mul_f32_dpp v34, v116, v34 row_newbcast:0 row_mask:0xf bank_mask:0xf\n"
      "v_mfma_f32_16x16x32_bf16 v[114:117], v[42:45], v[154:157], 0\n"
      "v_exp_f32 v109, v109\n"
      "v_mfma_f32_16x16x32_bf16 v[114:117], v[46:49], v[158:161], v[114:117]\n"
      "v_exp_f32 v240, v240\n"
      "v_mfma_f32_16x16x32_bf16 v[46:49], v[184:187], v[154:157], 0\n"
      "v_exp_f32 v107, v107\n"
      "v_mfma_f32_16x16x32_bf16 v[46:49], v[188:191], v[158:161], v[46:49]\n"
      "v_exp_f32 v242, v242\n"
      "v_mfma_f32_16x16x32_bf16 v[34:37], v[102:105], a[224:227], v[34:37]\n"
      "v_exp_f32 v106, v106\n"
      "v_mfma_f32_16x16x32_bf16 v[34:37], v[192:195], a[228:231], v[34:37]\n"
      "v_exp_f32 v108, v108\n"
      "v_mfma_f32_16x16x32_bf16 v[34:37], v[142:145], a[232:235], v[34:37]\n"
      "buffer_store_dwordx2 v[70:71], v2, s[44:47], s101 offen offset:0 nt sc1\n"
      "v_cvt_pk_bf16_f32 v175, v108, v109\n"
      "v_mfma_f32_16x16x32_bf16 v[30:33], v[102:105], a[208:211], v[30:33]\n"
      "v_exp_f32 v48, v48\n"
      "v_mfma_f32_16x16x32_bf16 v[30:33], v[192:195], a[212:215], v[30:33]\n"
      "v_exp_f32 v46, v46\n"
      "v_mfma_f32_16x16x32_bf16 v[30:33], v[142:145], a[216:219], v[30:33]\n"
      "v_exp_f32 v116, v116\n"
      "v_mfma_f32_16x16x32_bf16 v[30:33], v[66:69], a[220:223], v[30:33]\n"
      "v_exp_f32 v49, v49\n"
      "v_mfma_f32_16x16x4_4b_bf16 v[220:235], v[134:135], v[182:183], 0 cbsz:2 abid:2\n"
      "v_exp_f32 v117, v117\n"
      "v_exp_f32 v114, v114\n"
      "v_exp_f32 v47, v47\n"
      "v_cvt_pk_bf16_f32 v174, v106, v107\n"
      "v_cvt_pk_bf16_f32 v170, v46, v47\n"
      "v_mfma_f32_16x16x32_bf16 v[38:41], v[102:105], a[240:243], v[38:41]\n"
      "v_exp_f32 v115, v115\n"
      "v_mfma_f32_16x16x32_bf16 v[38:41], v[192:195], a[244:247], v[38:41]\n"
      "v_cvt_pk_bf16_f32 v173, v216, v217\n"
      "v_cvt_pk_bf16_f32 v177, v116, v117\n"
      "v_mfma_f32_16x16x32_bf16 v[38:41], v[142:145], a[248:251], v[38:41]\n"
      "v_cvt_pk_bf16_f32 v176, v114, v115\n"
      "v_cvt_pk_bf16_f32 v171, v48, v49\n"
      "v_mfma_f32_16x16x32_bf16 v[38:41], v[66:69], a[252:255], v[38:41]\n"
      "v_exp_f32 v243, v243\n"
      "v_smfmac_f32_16x16x64_bf16 v[58:61], v[12:15], v[170:177], v5\n"
      "v_exp_f32 v241, v241\n"
      "v_mfma_f32_16x16x32_bf16 v[34:37], v[66:69], a[236:239], v[34:37]\n"
      "v_cvt_pk_bf16_f32 v71, v242, v243\n"
      "v_cvt_pk_bf16_f32 v70, v240, v241\n"
      "v_cvt_pk_bf16_f32 v73, v180, v181\n"
      "v_cvt_pk_bf16_f32 v28, v28, v32\n"
      "v_cvt_pk_bf16_f32 v32, v29, v33\n"
      "v_smfmac_f32_16x16x32_bf16 v[58:61], v[12:13], v[70:73], v5\n"
      "s_nop 1\n"
      "v_cvt_pk_bf16_f32 v33, v37, v41\n"
      "v_pk_max_i16 v33, v33, 0\n"
      "v_pk_max_i16 v32, v32, 0\n"
      "v_mfma_f32_16x16x32_bf16 v[224:227], v[170:173], v[50:53], v[224:227]\n"
      "buffer_store_dwordx2 v[32:33], v2, s[44:47], s34 offen offset:768 nt sc1\n"
      "s_nop 0\n"
      "v_add_f32 v58, v58, v59\n"
      "v_rcp_f32 v59, v58\n"
      "v_mfma_f32_16x16x32_bf16 v[232:235], v[170:173], v[94:97], v[232:235]\n"
      "ds_bpermute_b32 v60, v4, v59 offset:128\n"
      "v_mfma_f32_16x16x32_bf16 v[232:235], v[174:177], v[98:101], v[232:235]\n"
      "v_cvt_pk_bf16_f32 v26, v26, v30\n"
      "v_mfma_f32_16x16x32_bf16 v[232:235], v[70:73], v[196:199], v[232:235]\n"
      "v_cvt_pk_bf16_f32 v30, v27, v31\n"
      "v_pk_max_i16 v30, v30, 0\n"
      "v_mfma_f32_16x16x32_bf16 v[224:227], v[174:177], v[54:57], v[224:227]\n"
      "v_pk_max_i16 v28, v28, 0\n"
      "v_cvt_pk_bf16_f32 v27, v34, v38\n"
      "v_mfma_f32_16x16x32_bf16 v[224:227], v[70:73], v[162:165], v[224:227]\n"
      "v_cvt_pk_bf16_f32 v31, v35, v39\n"
      "v_mfma_f32_16x16x32_bf16 v[220:223], v[170:173], v[200:203], v[220:223]\n"
      "v_pk_max_i16 v26, v26, 0\n"
      "v_pk_max_i16 v27, v27, 0\n"
      "v_mfma_f32_16x16x32_bf16 v[220:223], v[174:177], v[204:207], v[220:223]\n"
      "v_mfma_f32_16x16x32_bf16 v[220:223], v[70:73], v[166:169], v[220:223]\n"
      "buffer_store_dwordx2 v[26:27], v2, s[44:47], s34 offen offset:0 nt sc1\n"
      "v_pk_max_i16 v31, v31, 0\n"
      "v_mfma_f32_16x16x32_bf16 v[228:231], v[170:173], v[244:247], v[228:231]\n"
      "v_mfma_f32_16x16x32_bf16 v[228:231], v[174:177], v[248:251], v[228:231]\n"
      "s_waitcnt lgkmcnt(0)\n"
      "v_mul_f32_dpp v227, v60, v227 row_newbcast:12 row_mask:0xf bank_mask:0xf\n"
      "v_mul_f32_dpp v226, v60, v226 row_newbcast:8 row_mask:0xf bank_mask:0xf\n"
      "v_mfma_f32_16x16x32_bf16 v[228:231], v[70:73], v[22:25], v[228:231]\n"
      "v_mul_f32_dpp v225, v60, v225 row_newbcast:4 row_mask:0xf bank_mask:0xf\n"
      "v_mul_f32_dpp v224, v60, v224 row_newbcast:0 row_mask:0xf bank_mask:0xf\n"
      "v_mul_f32_dpp v220, v60, v220 row_newbcast:0 row_mask:0xf bank_mask:0xf\n"
      "v_cvt_pk_bf16_f32 v29, v36, v40\n"
      "v_mfma_f32_16x16x32_bf16 v[224:227], v[252:255], a[208:211], v[224:227]\n"
      "v_mul_f32_dpp v221, v60, v221 row_newbcast:4 row_mask:0xf bank_mask:0xf\n"
      "v_mul_f32_dpp v222, v60, v222 row_newbcast:8 row_mask:0xf bank_mask:0xf\n"
      "v_mfma_f32_16x16x32_bf16 v[224:227], v[122:125], a[212:215], v[224:227]\n"
      "v_pk_max_i16 v29, v29, 0\n"
      "v_mul_f32_dpp v230, v60, v230 row_newbcast:8 row_mask:0xf bank_mask:0xf\n"
      "v_mfma_f32_16x16x32_bf16 v[224:227], v[146:149], a[216:219], v[224:227]\n"
      "v_mul_f32_dpp v223, v60, v223 row_newbcast:12 row_mask:0xf bank_mask:0xf\n"
      "v_mfma_f32_16x16x32_bf16 v[224:227], v[118:121], a[220:223], v[224:227]\n"
      "buffer_store_dwordx2 v[28:29], v2, s[44:47], s34 offen offset:512 nt sc1\n"
      "v_mul_f32_dpp v232, v60, v232 row_newbcast:0 row_mask:0xf bank_mask:0xf\n"
      "v_mfma_f32_16x16x32_bf16 v[220:223], v[252:255], a[192:195], v[220:223]\n"
      "v_mul_f32_dpp v235, v60, v235 row_newbcast:12 row_mask:0xf bank_mask:0xf\n"
      "v_mul_f32_dpp v231, v60, v231 row_newbcast:12 row_mask:0xf bank_mask:0xf\n"
      "v_mfma_f32_16x16x32_bf16 v[220:223], v[122:125], a[196:199], v[220:223]\n"
      "v_mfma_f32_16x16x32_bf16 v[220:223], v[146:149], a[200:203], v[220:223]\n"
      "v_mul_f32_dpp v234, v60, v234 row_newbcast:8 row_mask:0xf bank_mask:0xf\n"
      "v_mul_f32_dpp v233, v60, v233 row_newbcast:4 row_mask:0xf bank_mask:0xf\n"
      "v_mfma_f32_16x16x32_bf16 v[220:223], v[118:121], a[204:207], v[220:223]\n"
      "buffer_store_dwordx2 v[30:31], v2, s[44:47], s34 offen offset:256 nt sc1\n"
      "v_mfma_f32_16x16x32_bf16 v[232:235], v[252:255], a[240:243], v[232:235]\n"
      "v_mul_f32_dpp v229, v60, v229 row_newbcast:4 row_mask:0xf bank_mask:0xf\n"
      "v_mfma_f32_16x16x32_bf16 v[232:235], v[122:125], a[244:247], v[232:235]\n"
      "v_mul_f32_dpp v228, v60, v228 row_newbcast:0 row_mask:0xf bank_mask:0xf\n"
      "v_mfma_f32_16x16x32_bf16 v[232:235], v[146:149], a[248:251], v[232:235]\n"
      "s_nop 1\n"
      "v_cvt_pk_bf16_f32 v222, v222, v226\n"
      "v_mfma_f32_16x16x32_bf16 v[232:235], v[118:121], a[252:255], v[232:235]\n"
      "v_cvt_pk_bf16_f32 v220, v220, v224\n"
      "v_pk_max_i16 v220, v220, 0\n"
      "v_mfma_f32_16x16x32_bf16 v[228:231], v[252:255], a[224:227], v[228:231]\n"
      "v_mfma_f32_16x16x32_bf16 v[228:231], v[122:125], a[228:231], v[228:231]\n"
      "v_cvt_pk_bf16_f32 v226, v223, v227\n"
      "v_mfma_f32_16x16x32_bf16 v[228:231], v[146:149], a[232:235], v[228:231]\n"
      "v_add_u32 v11, 0x200, v11\n"
      "v_cvt_pk_bf16_f32 v224, v221, v225\n"
      "v_mfma_f32_16x16x32_bf16 v[228:231], v[118:121], a[236:239], v[228:231]\n"
      "s_nop 7\n"
      "v_cvt_pk_bf16_f32 v221, v228, v232\n"
      "v_pk_max_i16 v221, v221, 0\n"
      "buffer_store_dwordx2 v[220:221], v2, s[44:47], s35 offen offset:0 nt sc1\n"
      "v_cvt_pk_bf16_f32 v225, v229, v233\n"
      "v_pk_max_i16 v225, v225, 0\n"
      "v_pk_max_i16 v222, v222, 0\n"
      "v_cvt_pk_bf16_f32 v227, v231, v235\n"
      "v_pk_max_i16 v227, v227, 0\n"
      "v_cvt_pk_bf16_f32 v223, v230, v234\n"
      "v_add_u32 v10, 64, v10\n"
      "v_pk_max_i16 v223, v223, 0\n"
      "v_pk_max_i16 v224, v224, 0\n"
      "v_pk_max_i16 v226, v226, 0\n"
      "buffer_store_dwordx2 v[222:223], v2, s[44:47], s35 offen offset:512 nt sc1\n"
      "buffer_store_dwordx2 v[224:225], v2, s[44:47], s35 offen offset:256 nt sc1\n"
      "buffer_store_dwordx2 v[226:227], v2, s[44:47], s35 offen offset:768 nt sc1\n"
      "v_maximum3_f32 v8, v8, v58, v59\n"
      "s_waitcnt vmcnt(0) lgkmcnt(0)\n"
      "v_cmp_nge_f32 vcc, 0x53800000, v8\n"
      "s_nop 4\n"
      "s_cbranch_vccnz L_ai_end\n"
      "s_endpgm\n"
      "L_ai_end:\n"
      "s_mov_b32 s40, %[elo]\n"
      "s_and_b32 s41, %[ehi], 0xffff\n"
      "s_mov_b32 s42, 104857600\n"
      "s_mov_b32 s44, %[olo]\n"
      "s_and_b32 s45, %[ohi], 0xffff\n"
      "s_mov_b32 s46, 104857600\n"
      "s_mov_b32 s48, %[first]\n"
      "s_add_u32 s49, %[first], 102400\n"
      "s_mov_b32 s50, 8\n"
      "s_mov_b32 vcc_lo, 0x55555555\n"
      "s_mov_b32 vcc_hi, 0x55555555\n"
      "v_subrev_u32 v9, 512, v9\n"
      "v_subrev_u32 v10, 512, v10\n"
      "v_subrev_u32 v11, 4096, v11\n"
      "s_mov_b32 s57, 8192\n"
      "s_add_u32 s52, s48, s57\n"
      "s_add_u32 s57, s53, s57\n"
      "s_mov_b32 m0, s57\n"
      "s_nop 0\n"
      "buffer_load_dwordx4 v1, s[40:43], s52 offen offset:0 lds\n"
      "buffer_load_dwordx4 v1, s[40:43], s52 offen offset:2048 lds\n"
      "s_add_u32 m0, s57, 896\n"
      "s_nop 0\n"
      "buffer_load_dwordx4 v1, s[40:43], s52 offen offset:128 lds\n"
      "buffer_load_dwordx4 v1, s[40:43], s52 offen offset:2176 lds\n"
      "s_mov_b32 s57, 20480\n"
      "s_add_u32 s52, s48, s57\n"
      "s_add_u32 s57, s53, s57\n"
      "s_mov_b32 m0, s57\n"
      "s_nop 0\n"
      "buffer_load_dwordx4 v1, s[40:43], s52 offen offset:0 lds\n"
      "buffer_load_dwordx4 v1, s[40:43], s52 offen offset:2048 lds\n"
      "s_add_u32 m0, s57, 896\n"
      "s_nop 0\n"
      "buffer_load_dwordx4 v1, s[40:43], s52 offen offset:128 lds\n"
      "buffer_load_dwordx4 v1, s[40:43], s52 offen offset:2176 lds\n"
      "s_mov_b32 s57, 12288\n"
      "s_add_u32 s52, s48, s57\n"
      "s_add_u32 s57, s53, s57\n"
      "s_mov_b32 m0, s57\n"
      "s_nop 0\n"
      "buffer_load_dwordx4 v1, s[40:43], s52 offen offset:0 lds\n"
      "buffer_load_dwordx4 v1, s[40:43], s52 offen offset:2048 lds\n"
      "s_add_u32 m0, s57, 896\n"
      "s_nop 0\n"
      "buffer_load_dwordx4 v1, s[40:43], s52 offen offset:128 lds\n"
      "buffer_load_dwordx4 v1, s[40:43], s52 offen offset:2176 lds\n"
      "s_mov_b32 s57, 16384\n"
      "s_add_u32 s52, s48, s57\n"
      "s_add_u32 s57, s53, s57\n"
      "s_mov_b32 m0, s57\n"
      "s_nop 0\n"
      "buffer_load_dwordx4 v1, s[40:43], s52 offen offset:0 lds\n"
      "buffer_load_dwordx4 v1, s[40:43], s52 offen offset:2048 lds\n"
      "s_add_u32 m0, s57, 896\n"
      "s_nop 0\n"
      "buffer_load_dwordx4 v1, s[40:43], s52 offen offset:128 lds\n"
      "buffer_load_dwordx4 v1, s[40:43], s52 offen offset:2176 lds\n"
      "s_mov_b32 s57, 4096\n"
      "s_add_u32 s52, s48, s57\n"
      "s_add_u32 s57, s53, s57\n"
      "s_mov_b32 m0, s57\n"
      "s_nop 0\n"
      "buffer_load_dwordx4 v1, s[40:43], s52 offen offset:0 lds\n"
      "buffer_load_dwordx4 v1, s[40:43], s52 offen offset:2048 lds\n"
      "s_add_u32 m0, s57, 896\n"
      "s_nop 0\n"
      "buffer_load_dwordx4 v1, s[40:43], s52 offen offset:128 lds\n"
      "buffer_load_dwordx4 v1, s[40:43], s52 offen offset:2176 lds\n"
      "s_mov_b32 s57, 0\n"
      "s_add_u32 s52, s48, s57\n"
      "s_add_u32 s57, s53, s57\n"
      "s_mov_b32 m0, s57\n"
      "s_nop 0\n"
      "buffer_load_dwordx4 v1, s[40:43], s52 offen offset:0 lds\n"
      "buffer_load_dwordx4 v1, s[40:43], s52 offen offset:2048 lds\n"
      "s_add_u32 m0, s57, 896\n"
      "s_nop 0\n"
      "buffer_load_dwordx4 v1, s[40:43], s52 offen offset:128 lds\n"
      "buffer_load_dwordx4 v1, s[40:43], s52 offen offset:2176 lds\n"
      "s_waitcnt vmcnt(0)\n"
      "L_safe_%=:\n"
      "s_waitcnt vmcnt(45)\n"
      "ds_read_b128 v[148:151], v0 offset:12288\n"
      "ds_read_b128 v[136:139], v0 offset:12800\n"
      "ds_read_b128 v[140:143], v0 offset:13312\n"
      "ds_read_b128 v[144:147], v0 offset:13824\n"
      "s_waitcnt vmcnt(41)\n"
      "ds_read_b128 v[252:255], v0 offset:16384\n"
      "ds_read_b128 v[48:51], v0 offset:16896\n"
      "ds_read_b128 v[124:127], v0 offset:17408\n"
      "ds_read_b128 v[116:119], v0 offset:17920\n"
      "s_waitcnt lgkmcnt(7)\n"
      "v_mfma_f32_16x16x32_bf16 v[216:219], a[0:3], v[148:151], 0\n"
      "s_waitcnt lgkmcnt(6)\n"
      "v_mfma_f32_16x16x32_bf16 v[216:219], a[4:7], v[136:139], v[216:219]\n"
      "s_waitcnt lgkmcnt(5)\n"
      "v_mfma_f32_16x16x32_bf16 v[216:219], a[8:11], v[140:143], v[216:219]\n"
      "s_waitcnt lgkmcnt(4)\n"
      "v_mfma_f32_16x16x32_bf16 v[216:219], a[12:15], v[144:147], v[216:219]\n"
      "v_mfma_f32_16x16x32_bf16 v[96:99], a[16:19], v[148:151], 0\n"
      "v_mfma_f32_16x16x32_bf16 v[96:99], a[20:23], v[136:139], v[96:99]\n"
      "v_mfma_f32_16x16x32_bf16 v[96:99], a[24:27], v[140:143], v[96:99]\n"
      "v_mfma_f32_16x16x32_bf16 v[96:99], a[28:31], v[144:147], v[96:99]\n"
      "v_mfma_f32_16x16x32_bf16 v[24:27], a[32:35], v[148:151], 0\n"
      "v_mfma_f32_16x16x32_bf16 v[24:27], a[36:39], v[136:139], v[24:27]\n"
      "v_mfma_f32_16x16x32_bf16 v[24:27], a[40:43], v[140:143], v[24:27]\n"
      "v_mfma_f32_16x16x32_bf16 v[24:27], a[44:47], v[144:147], v[24:27]\n"
      "v_cvt_pk_bf16_f32 v92, v216, v217\n"
      "v_cvt_pk_bf16_f32 v93, v218, v219\n"
      "v_mfma_f32_16x16x32_bf16 v[216:219], a[48:51], v[148:151], 0\n"
      "v_mfma_f32_16x16x32_bf16 v[216:219], a[52:55], v[136:139], v[216:219]\n"
      "v_cvt_pk_bf16_f32 v94, v96, v97\n"
      "v_cvt_pk_bf16_f32 v95, v98, v99\n"
      "v_mfma_f32_16x16x32_bf16 v[216:219], a[56:59], v[140:143], v[216:219]\n"
      "v_mfma_f32_16x16x32_bf16 v[216:219], a[60:63], v[144:147], v[216:219]\n"
      "v_cvt_pk_bf16_f32 v96, v24, v25\n"
      "v_cvt_pk_bf16_f32 v97, v26, v27\n"
      "v_mfma_f32_16x16x32_bf16 v[248:251], v[148:151], a[64:67], 0\n"
      "v_mfma_f32_16x16x32_bf16 v[248:251], v[136:139], a[68:71], v[248:251]\n"
      "v_mfma_f32_16x16x32_bf16 v[248:251], v[140:143], a[72:75], v[248:251]\n"
      "v_mfma_f32_16x16x32_bf16 v[248:251], v[144:147], a[76:79], v[248:251]\n"
      "v_mfma_f32_16x16x32_bf16 v[24:27], v[148:151], a[80:83], 0\n"
      "v_mfma_f32_16x16x32_bf16 v[24:27], v[136:139], a[84:87], v[24:27]\n"
      "v_cvt_pk_bf16_f32 v98, v216, v217\n"
      "v_cvt_pk_bf16_f32 v99, v218, v219\n"
      "v_mfma_f32_16x16x32_bf16 v[24:27], v[140:143], a[88:91], v[24:27]\n"
      "v_mfma_f32_16x16x32_bf16 v[24:27], v[144:147], a[92:95], v[24:27]\n"
      "v_mfma_f32_16x16x32_bf16 v[216:219], v[148:151], a[96:99], 0\n"
      "v_mfma_f32_16x16x32_bf16 v[216:219], v[136:139], a[100:103], v[216:219]\n"
      "v_cvt_pk_bf16_f32 v58, v248, v249\n"
      "v_cvt_pk_bf16_f32 v59, v250, v251\n"
      "v_mfma_f32_16x16x32_bf16 v[216:219], v[140:143], a[104:107], v[216:219]\n"
      "v_mfma_f32_16x16x32_bf16 v[216:219], v[144:147], a[108:111], v[216:219]\n"
      "v_mfma_f32_16x16x32_bf16 v[248:251], v[148:151], a[112:115], 0\n"
      "v_mfma_f32_16x16x32_bf16 v[248:251], v[136:139], a[116:119], v[248:251]\n"
      "v_cvt_pk_bf16_f32 v134, v24, v25\n"
      "v_cvt_pk_bf16_f32 v135, v26, v27\n"
      "v_mfma_f32_16x16x32_bf16 v[248:251], v[140:143], a[120:123], v[248:251]\n"
      "v_mfma_f32_16x16x32_bf16 v[248:251], v[144:147], a[124:127], v[248:251]\n"
      "s_waitcnt vmcnt(37)\n"
      "ds_read_b128 v[24:27], v0 offset:20480\n"
      "ds_read_b128 v[152:155], v0 offset:20992\n"
      "ds_read_b128 v[20:23], v0 offset:21504\n"
      "ds_read_b128 v[156:159], v0 offset:22016\n"
      "v_cvt_pk_bf16_f32 v106, v216, v217\n"
      "v_cvt_pk_bf16_f32 v107, v218, v219\n"
      "s_waitcnt lgkmcnt(7)\n"
      "v_mfma_f32_16x16x32_bf16 v[220:223], a[0:3], v[252:255], 0\n"
      "s_waitcnt lgkmcnt(6)\n"
      "v_mfma_f32_16x16x32_bf16 v[220:223], a[4:7], v[48:51], v[220:223]\n"
      "v_cvt_pk_bf16_f32 v198, v248, v249\n"
      "v_cvt_pk_bf16_f32 v199, v250, v251\n"
      "s_waitcnt lgkmcnt(5)\n"
      "v_mfma_f32_16x16x32_bf16 v[220:223], a[8:11], v[124:127], v[220:223]\n"
      "s_waitcnt lgkmcnt(4)\n"
      "v_mfma_f32_16x16x32_bf16 v[220:223], a[12:15], v[116:119], v[220:223]\n"
      "v_mfma_f32_16x16x32_bf16 v[52:55], a[16:19], v[252:255], 0\n"
      "v_mfma_f32_16x16x32_bf16 v[52:55], a[20:23], v[48:51], v[52:55]\n"
      "v_mfma_f32_16x16x32_bf16 v[52:55], a[24:27], v[124:127], v[52:55]\n"
      "v_mfma_f32_16x16x32_bf16 v[52:55], a[28:31], v[116:119], v[52:55]\n"
      "v_mfma_f32_16x16x32_bf16 v[244:247], a[32:35], v[252:255], 0\n"
      "v_mfma_f32_16x16x32_bf16 v[244:247], a[36:39], v[48:51], v[244:247]\n"
      "v_mfma_f32_16x16x32_bf16 v[244:247], a[40:43], v[124:127], v[244:247]\n"
      "v_mfma_f32_16x16x32_bf16 v[244:247], a[44:47], v[116:119], v[244:247]\n"
      "v_cvt_pk_bf16_f32 v216, v220, v221\n"
      "v_cvt_pk_bf16_f32 v217, v222, v223\n"
      "v_mfma_f32_16x16x32_bf16 v[248:251], a[48:51], v[252:255], 0\n"
      "v_mfma_f32_16x16x32_bf16 v[248:251], a[52:55], v[48:51], v[248:251]\n"
      "v_cvt_pk_bf16_f32 v218, v52, v53\n"
      "v_cvt_pk_bf16_f32 v219, v54, v55\n"
      "v_mfma_f32_16x16x32_bf16 v[248:251], a[56:59], v[124:127], v[248:251]\n"
      "v_mfma_f32_16x16x32_bf16 v[248:251], a[60:63], v[116:119], v[248:251]\n"
      "v_cvt_pk_bf16_f32 v220, v244, v245\n"
      "v_cvt_pk_bf16_f32 v221, v246, v247\n"
      "v_mfma_f32_16x16x32_bf16 v[244:247], v[252:255], a[64:67], 0\n"
      "v_mfma_f32_16x16x32_bf16 v[244:247], v[48:51], a[68:71], v[244:247]\n"
      "v_mfma_f32_16x16x32_bf16 v[244:247], v[124:127], a[72:75], v[244:247]\n"
      "v_mfma_f32_16x16x32_bf16 v[244:247], v[116:119], a[76:79], v[244:247]\n"
      "v_mfma_f32_16x16x32_bf16 v[52:55], v[252:255], a[80:83], 0\n"
      "v_mfma_f32_16x16x32_bf16 v[52:55], v[48:51], a[84:87], v[52:55]\n"
      "v_cvt_pk_bf16_f32 v222, v248, v249\n"
      "v_cvt_pk_bf16_f32 v223, v250, v251\n"
      "v_mfma_f32_16x16x32_bf16 v[52:55], v[124:127], a[88:91], v[52:55]\n"
      "v_mfma_f32_16x16x32_bf16 v[52:55], v[116:119], a[92:95], v[52:55]\n"
      "v_mfma_f32_16x16x32_bf16 v[248:251], v[252:255], a[96:99], 0\n"
      "v_mfma_f32_16x16x32_bf16 v[248:251], v[48:51], a[100:103], v[248:251]\n"
      "v_cvt_pk_bf16_f32 v120, v244, v245\n"
      "v_cvt_pk_bf16_f32 v121, v246, v247\n"
      "v_mfma_f32_16x16x32_bf16 v[248:251], v[124:127], a[104:107], v[248:251]\n"
      "v_mfma_f32_16x16x32_bf16 v[248:251], v[116:119], a[108:111], v[248:251]\n"
      "v_mfma_f32_16x16x32_bf16 v[244:247], v[252:255], a[112:115], 0\n"
      "v_mfma_f32_16x16x32_bf16 v[244:247], v[48:51], a[116:119], v[244:247]\n"
      "v_cvt_pk_bf16_f32 v68, v52, v53\n"
      "v_cvt_pk_bf16_f32 v69, v54, v55\n"
      "v_mfma_f32_16x16x32_bf16 v[244:247], v[124:127], a[120:123], v[244:247]\n"
      "v_mfma_f32_16x16x32_bf16 v[244:247], v[116:119], a[124:127], v[244:247]\n"
      "s_waitcnt vmcnt(25)\n"
      "ds_read_b128 v[52:55], v0 offset:8192\n"
      "ds_read_b128 v[128:131], v0 offset:8704\n"
      "ds_read_b128 v[168:171], v0 offset:9216\n"
      "ds_read_b128 v[164:167], v0 offset:9728\n"
      "v_cvt_pk_bf16_f32 v36, v248, v249\n"
      "v_cvt_pk_bf16_f32 v37, v250, v251\n"
      "s_waitcnt lgkmcnt(7)\n"
      "v_mfma_f32_16x16x32_bf16 v[248:251], a[0:3], v[24:27], 0\n"
      "s_add_u32 m0, s53, 12288\n"
      "s_add_u32 s54, s49, 12288\n"
      "buffer_load_dwordx4 v1, s[40:43], s54 offen offset:0 lds\n"
      "v_cvt_pk_bf16_f32 v240, v244, v245\n"
      "v_cvt_pk_bf16_f32 v241, v246, v247\n"
      "s_waitcnt lgkmcnt(6)\n"
      "v_mfma_f32_16x16x32_bf16 v[248:251], a[4:7], v[152:155], v[248:251]\n"
      "s_waitcnt lgkmcnt(5)\n"
      "v_mfma_f32_16x16x32_bf16 v[248:251], a[8:11], v[20:23], v[248:251]\n"
      "buffer_load_dwordx4 v1, s[40:43], s54 offen offset:2048 lds\n"
      "s_add_u32 m0, s53, 13184\n"
      "s_waitcnt lgkmcnt(4)\n"
      "v_mfma_f32_16x16x32_bf16 v[248:251], a[12:15], v[156:159], v[248:251]\n"
      "v_mfma_f32_16x16x32_bf16 v[40:43], a[16:19], v[24:27], 0\n"
      "buffer_load_dwordx4 v1, s[40:43], s54 offen offset:128 lds\n"
      "v_mfma_f32_16x16x32_bf16 v[40:43], a[20:23], v[152:155], v[40:43]\n"
      "v_mfma_f32_16x16x32_bf16 v[40:43], a[24:27], v[20:23], v[40:43]\n"
      "buffer_load_dwordx4 v1, s[40:43], s54 offen offset:2176 lds\n"
      "v_mfma_f32_16x16x32_bf16 v[40:43], a[28:31], v[156:159], v[40:43]\n"
      "v_mfma_f32_16x16x32_bf16 v[100:103], a[32:35], v[24:27], 0\n"
      "v_mfma_f32_16x16x32_bf16 v[100:103], a[36:39], v[152:155], v[100:103]\n"
      "v_cvt_pk_bf16_f32 v244, v248, v249\n"
      "v_cvt_pk_bf16_f32 v245, v250, v251\n"
      "v_mfma_f32_16x16x32_bf16 v[100:103], a[40:43], v[20:23], v[100:103]\n"
      "v_mfma_f32_16x16x32_bf16 v[100:103], a[44:47], v[156:159], v[100:103]\n"
      "v_mfma_f32_16x16x32_bf16 v[44:47], a[48:51], v[24:27], 0\n"
      "v_mfma_f32_16x16x32_bf16 v[44:47], a[52:55], v[152:155], v[44:47]\n"
      "v_cvt_pk_bf16_f32 v246, v40, v41\n"
      "v_cvt_pk_bf16_f32 v247, v42, v43\n"
      "v_mfma_f32_16x16x32_bf16 v[44:47], a[56:59], v[20:23], v[44:47]\n"
      "v_mfma_f32_16x16x32_bf16 v[44:47], a[60:63], v[156:159], v[44:47]\n"
      "v_mfma_f32_16x16x32_bf16 v[40:43], v[24:27], a[64:67], 0\n"
      "v_mfma_f32_16x16x32_bf16 v[40:43], v[152:155], a[68:71], v[40:43]\n"
      "v_cvt_pk_bf16_f32 v248, v100, v101\n"
      "v_cvt_pk_bf16_f32 v249, v102, v103\n"
      "v_mfma_f32_16x16x32_bf16 v[40:43], v[20:23], a[72:75], v[40:43]\n"
      "v_mfma_f32_16x16x32_bf16 v[40:43], v[156:159], a[76:79], v[40:43]\n"
      "v_mfma_f32_16x16x32_bf16 v[100:103], v[24:27], a[80:83], 0\n"
      "v_mfma_f32_16x16x32_bf16 v[100:103], v[152:155], a[84:87], v[100:103]\n"
      "v_cvt_pk_bf16_f32 v250, v44, v45\n"
      "v_cvt_pk_bf16_f32 v251, v46, v47\n"
      "v_mfma_f32_16x16x32_bf16 v[100:103], v[20:23], a[88:91], v[100:103]\n"
      "v_mfma_f32_16x16x32_bf16 v[100:103], v[156:159], a[92:95], v[100:103]\n"
      "v_mfma_f32_16x16x32_bf16 v[44:47], v[24:27], a[96:99], 0\n"
      "v_mfma_f32_16x16x32_bf16 v[44:47], v[152:155], a[100:103], v[44:47]\n"
      "v_cvt_pk_bf16_f32 v122, v40, v41\n"
      "v_cvt_pk_bf16_f32 v123, v42, v43\n"
      "v_mfma_f32_16x16x32_bf16 v[44:47], v[20:23], a[104:107], v[44:47]\n"
      "v_mfma_f32_16x16x32_bf16 v[44:47], v[156:159], a[108:111], v[44:47]\n"
      "v_mfma_f32_16x16x32_bf16 v[40:43], v[24:27], a[112:115], 0\n"
      "v_mfma_f32_16x16x32_bf16 v[40:43], v[152:155], a[116:119], v[40:43]\n"
      "v_cvt_pk_bf16_f32 v70, v100, v101\n"
      "v_cvt_pk_bf16_f32 v71, v102, v103\n"
      "v_mfma_f32_16x16x32_bf16 v[40:43], v[20:23], a[120:123], v[40:43]\n"
      "v_mfma_f32_16x16x32_bf16 v[40:43], v[156:159], a[124:127], v[40:43]\n"
      "s_waitcnt vmcnt(21)\n"
      "ds_read_b128 v[100:103], v0 offset:4096\n"
      "ds_read_b128 v[108:111], v0 offset:4608\n"
      "ds_read_b128 v[176:179], v0 offset:5120\n"
      "ds_read_b128 v[172:175], v0 offset:5632\n"
      "v_cvt_pk_bf16_f32 v38, v44, v45\n"
      "v_cvt_pk_bf16_f32 v39, v46, v47\n"
      "s_waitcnt lgkmcnt(7)\n"
      "v_mfma_f32_16x16x32_bf16 v[44:47], a[0:3], v[52:55], 0\n"
      "s_add_u32 m0, s53, 16384\n"
      "s_add_u32 s54, s49, 16384\n"
      "buffer_load_dwordx4 v1, s[40:43], s54 offen offset:0 lds\n"
      "v_cvt_pk_bf16_f32 v242, v40, v41\n"
      "v_cvt_pk_bf16_f32 v243, v42, v43\n"
      "s_waitcnt lgkmcnt(6)\n"
      "v_mfma_f32_16x16x32_bf16 v[44:47], a[4:7], v[128:131], v[44:47]\n"
      "s_waitcnt lgkmcnt(5)\n"
      "v_mfma_f32_16x16x32_bf16 v[44:47], a[8:11], v[168:171], v[44:47]\n"
      "buffer_load_dwordx4 v1, s[40:43], s54 offen offset:2048 lds\n"
      "s_add_u32 m0, s53, 17280\n"
      "s_waitcnt lgkmcnt(4)\n"
      "v_mfma_f32_16x16x32_bf16 v[44:47], a[12:15], v[164:167], v[44:47]\n"
      "v_mfma_f32_16x16x32_bf16 v[112:115], a[16:19], v[52:55], 0\n"
      "buffer_load_dwordx4 v1, s[40:43], s54 offen offset:128 lds\n"
      "v_mfma_f32_16x16x32_bf16 v[112:115], a[20:23], v[128:131], v[112:115]\n"
      "v_mfma_f32_16x16x32_bf16 v[112:115], a[24:27], v[168:171], v[112:115]\n"
      "buffer_load_dwordx4 v1, s[40:43], s54 offen offset:2176 lds\n"
      "v_mfma_f32_16x16x32_bf16 v[112:115], a[28:31], v[164:167], v[112:115]\n"
      "v_mfma_f32_16x16x32_bf16 v[192:195], a[32:35], v[52:55], 0\n"
      "v_mfma_f32_16x16x32_bf16 v[192:195], a[36:39], v[128:131], v[192:195]\n"
      "v_cvt_pk_bf16_f32 v40, v44, v45\n"
      "v_cvt_pk_bf16_f32 v41, v46, v47\n"
      "v_mfma_f32_16x16x32_bf16 v[192:195], a[40:43], v[168:171], v[192:195]\n"
      "v_mfma_f32_16x16x32_bf16 v[192:195], a[44:47], v[164:167], v[192:195]\n"
      "v_mfma_f32_16x16x32_bf16 v[224:227], a[48:51], v[52:55], 0\n"
      "v_mfma_f32_16x16x32_bf16 v[224:227], a[52:55], v[128:131], v[224:227]\n"
      "v_cvt_pk_bf16_f32 v42, v112, v113\n"
      "v_cvt_pk_bf16_f32 v43, v114, v115\n"
      "v_mfma_f32_16x16x32_bf16 v[224:227], a[56:59], v[168:171], v[224:227]\n"
      "v_mfma_f32_16x16x32_bf16 v[224:227], a[60:63], v[164:167], v[224:227]\n"
      "v_mfma_f32_16x16x32_bf16 v[32:35], v[52:55], a[64:67], 0\n"
      "v_mfma_f32_16x16x32_bf16 v[32:35], v[128:131], a[68:71], v[32:35]\n"
      "v_cvt_pk_bf16_f32 v44, v192, v193\n"
      "v_cvt_pk_bf16_f32 v45, v194, v195\n"
      "v_mfma_f32_16x16x32_bf16 v[32:35], v[168:171], a[72:75], v[32:35]\n"
      "v_mfma_f32_16x16x32_bf16 v[32:35], v[164:167], a[76:79], v[32:35]\n"
      "v_mfma_f32_16x16x32_bf16 v[160:163], v[52:55], a[80:83], 0\n"
      "v_mfma_f32_16x16x32_bf16 v[160:163], v[128:131], a[84:87], v[160:163]\n"
      "v_cvt_pk_bf16_f32 v46, v224, v225\n"
      "v_cvt_pk_bf16_f32 v47, v226, v227\n"
      "v_mfma_f32_16x16x32_bf16 v[160:163], v[168:171], a[88:91], v[160:163]\n"
      "v_mfma_f32_16x16x32_bf16 v[160:163], v[164:167], a[92:95], v[160:163]\n"
      "v_mfma_f32_16x16x32_bf16 v[112:115], v[52:55], a[96:99], 0\n"
      "v_mfma_f32_16x16x32_bf16 v[112:115], v[128:131], a[100:103], v[112:115]\n"
      "v_cvt_pk_bf16_f32 v56, v32, v33\n"
      "v_cvt_pk_bf16_f32 v57, v34, v35\n"
      "v_mfma_f32_16x16x32_bf16 v[112:115], v[168:171], a[104:107], v[112:115]\n"
      "v_mfma_f32_16x16x32_bf16 v[112:115], v[164:167], a[108:111], v[112:115]\n"
      "v_mfma_f32_16x16x32_bf16 v[192:195], v[52:55], a[112:115], 0\n"
      "v_mfma_f32_16x16x32_bf16 v[192:195], v[128:131], a[116:119], v[192:195]\n"
      "v_cvt_pk_bf16_f32 v132, v160, v161\n"
      "v_cvt_pk_bf16_f32 v133, v162, v163\n"
      "v_mfma_f32_16x16x32_bf16 v[192:195], v[168:171], a[120:123], v[192:195]\n"
      "v_mfma_f32_16x16x32_bf16 v[192:195], v[164:167], a[124:127], v[192:195]\n"
      "s_waitcnt vmcnt(17)\n"
      "ds_read_b128 v[224:227], v0 offset:0\n"
      "ds_read_b128 v[32:35], v0 offset:512\n"
      "ds_read_b128 v[160:163], v0 offset:1024\n"
      "ds_read_b128 v[28:31], v0 offset:1536\n"
      "v_cvt_pk_bf16_f32 v104, v112, v113\n"
      "v_cvt_pk_bf16_f32 v105, v114, v115\n"
      "s_waitcnt lgkmcnt(7)\n"
      "v_mfma_f32_16x16x32_bf16 v[168:171], a[0:3], v[100:103], 0\n"
      "s_add_u32 m0, s53, 20480\n"
      "s_add_u32 s54, s49, 20480\n"
      "buffer_load_dwordx4 v1, s[40:43], s54 offen offset:0 lds\n"
      "v_cvt_pk_bf16_f32 v196, v192, v193\n"
      "v_cvt_pk_bf16_f32 v197, v194, v195\n"
      "s_waitcnt lgkmcnt(6)\n"
      "v_mfma_f32_16x16x32_bf16 v[168:171], a[4:7], v[108:111], v[168:171]\n"
      "s_waitcnt lgkmcnt(5)\n"
      "v_mfma_f32_16x16x32_bf16 v[168:171], a[8:11], v[176:179], v[168:171]\n"
      "buffer_load_dwordx4 v1, s[40:43], s54 offen offset:2048 lds\n"
      "s_add_u32 m0, s53, 21376\n"
      "s_waitcnt lgkmcnt(4)\n"
      "v_mfma_f32_16x16x32_bf16 v[168:171], a[12:15], v[172:175], v[168:171]\n"
      "v_mfma_f32_16x16x32_bf16 v[112:115], a[16:19], v[100:103], 0\n"
      "buffer_load_dwordx4 v1, s[40:43], s54 offen offset:128 lds\n"
      "v_mfma_f32_16x16x32_bf16 v[112:115], a[20:23], v[108:111], v[112:115]\n"
      "v_mfma_f32_16x16x32_bf16 v[112:115], a[24:27], v[176:179], v[112:115]\n"
      "buffer_load_dwordx4 v1, s[40:43], s54 offen offset:2176 lds\n"
      "v_mfma_f32_16x16x32_bf16 v[112:115], a[28:31], v[172:175], v[112:115]\n"
      "v_mfma_f32_16x16x32_bf16 v[192:195], a[32:35], v[100:103], 0\n"
      "v_mfma_f32_16x16x32_bf16 v[192:195], a[36:39], v[108:111], v[192:195]\n"
      "v_cvt_pk_bf16_f32 v164, v168, v169\n"
      "v_cvt_pk_bf16_f32 v165, v170, v171\n"
      "v_mfma_f32_16x16x32_bf16 v[192:195], a[40:43], v[176:179], v[192:195]\n"
      "v_mfma_f32_16x16x32_bf16 v[192:195], a[44:47], v[172:175], v[192:195]\n"
      "v_mfma_f32_16x16x32_bf16 v[52:55], a[48:51], v[100:103], 0\n"
      "v_mfma_f32_16x16x32_bf16 v[52:55], a[52:55], v[108:111], v[52:55]\n"
      "v_cvt_pk_bf16_f32 v166, v112, v113\n"
      "v_cvt_pk_bf16_f32 v167, v114, v115\n"
      "v_mfma_f32_16x16x32_bf16 v[52:55], a[56:59], v[176:179], v[52:55]\n"
      "v_mfma_f32_16x16x32_bf16 v[52:55], a[60:63], v[172:175], v[52:55]\n"
      "v_mfma_f32_16x16x32_bf16 v[112:115], v[100:103], a[64:67], 0\n"
      "v_mfma_f32_16x16x32_bf16 v[112:115], v[108:111], a[68:71], v[112:115]\n"
      "v_cvt_pk_bf16_f32 v168, v192, v193\n"
      "v_cvt_pk_bf16_f32 v169, v194, v195\n"
      "v_mfma_f32_16x16x32_bf16 v[112:115], v[176:179], a[72:75], v[112:115]\n"
      "v_mfma_f32_16x16x32_bf16 v[112:115], v[172:175], a[76:79], v[112:115]\n"
      "v_mfma_f32_16x16x32_bf16 v[192:195], v[100:103], a[80:83], 0\n"
      "v_mfma_f32_16x16x32_bf16 v[192:195], v[108:111], a[84:87], v[192:195]\n"
      "v_cvt_pk_bf16_f32 v170, v52, v53\n"
      "v_cvt_pk_bf16_f32 v171, v54, v55\n"
      "v_mfma_f32_16x16x32_bf16 v[192:195], v[176:179], a[88:91], v[192:195]\n"
      "v_mfma_f32_16x16x32_bf16 v[192:195], v[172:175], a[92:95], v[192:195]\n"
      "v_mfma_f32_16x16x32_bf16 v[88:91], v[100:103], a[96:99], 0\n"
      "v_mfma_f32_16x16x32_bf16 v[88:91], v[108:111], a[100:103], v[88:91]\n"
      "v_cvt_pk_bf16_f32 v54, v112, v113\n"
      "v_cvt_pk_bf16_f32 v55, v114, v115\n"
      "v_mfma_f32_16x16x32_bf16 v[88:91], v[176:179], a[104:107], v[88:91]\n"
      "v_mfma_f32_16x16x32_bf16 v[88:91], v[172:175], a[108:111], v[88:91]\n"
      "v_mfma_f32_16x16x32_bf16 v[112:115], v[100:103], a[112:115], 0\n"
      "v_mfma_f32_16x16x32_bf16 v[112:115], v[108:111], a[116:119], v[112:115]\n"
      "v_cvt_pk_bf16_f32 v130, v192, v193\n"
      "v_cvt_pk_bf16_f32 v131, v194, v195\n"
      "v_mfma_f32_16x16x32_bf16 v[112:115], v[176:179], a[120:123], v[112:115]\n"
      "v_mfma_f32_16x16x32_bf16 v[112:115], v[172:175], a[124:127], v[112:115]\n"
      "ds_read_b128 v[172:175], v9 offset:0\n"
      "ds_read_b128 v[176:179], v9 offset:2048\n"
      "v_cvt_pk_bf16_f32 v102, v88, v89\n"
      "v_cvt_pk_bf16_f32 v103, v90, v91\n"
      "s_waitcnt lgkmcnt(5)\n"
      "v_mfma_f32_16x16x32_bf16 v[88:91], a[0:3], v[224:227], 0\n"
      "s_waitcnt lgkmcnt(4)\n"
      "v_mfma_f32_16x16x32_bf16 v[88:91], a[4:7], v[32:35], v[88:91]\n"
      "s_waitcnt lgkmcnt(3)\n"
      "v_mfma_f32_16x16x32_bf16 v[88:91], a[8:11], v[160:163], v[88:91]\n"
      "s_waitcnt lgkmcnt(2)\n"
      "v_mfma_f32_16x16x32_bf16 v[88:91], a[12:15], v[28:31], v[88:91]\n"
      "v_cvt_pk_bf16_f32 v194, v112, v113\n"
      "v_cvt_pk_bf16_f32 v195, v114, v115\n"
      "v_mfma_f32_16x16x32_bf16 v[112:115], a[16:19], v[224:227], 0\n"
      "v_mfma_f32_16x16x32_bf16 v[112:115], a[20:23], v[32:35], v[112:115]\n"
      "v_mfma_f32_16x16x32_bf16 v[112:115], a[24:27], v[160:163], v[112:115]\n"
      "v_mfma_f32_16x16x32_bf16 v[112:115], a[28:31], v[28:31], v[112:115]\n"
      "v_mfma_f32_16x16x32_bf16 v[190:193], a[32:35], v[224:227], 0\n"
      "v_mfma_f32_16x16x32_bf16 v[190:193], a[36:39], v[32:35], v[190:193]\n"
      "v_cvt_pk_bf16_f32 v108, v88, v89\n"
      "v_cvt_pk_bf16_f32 v109, v90, v91\n"
      "v_mfma_f32_16x16x32_bf16 v[190:193], a[40:43], v[160:163], v[190:193]\n"
      "v_mfma_f32_16x16x32_bf16 v[190:193], a[44:47], v[28:31], v[190:193]\n"
      "v_mfma_f32_16x16x32_bf16 v[88:91], a[48:51], v[224:227], 0\n"
      "v_mfma_f32_16x16x32_bf16 v[88:91], a[52:55], v[32:35], v[88:91]\n"
      "v_cvt_pk_bf16_f32 v110, v112, v113\n"
      "v_cvt_pk_bf16_f32 v111, v114, v115\n"
      "v_mfma_f32_16x16x32_bf16 v[88:91], a[56:59], v[160:163], v[88:91]\n"
      "v_mfma_f32_16x16x32_bf16 v[88:91], a[60:63], v[28:31], v[88:91]\n"
      "v_add_u32 v9, 64, v9\n"
      "v_mfma_f32_16x16x32_bf16 v[228:231], v[224:227], a[64:67], 0\n"
      "v_cvt_pk_bf16_f32 v112, v190, v191\n"
      "v_cvt_pk_bf16_f32 v113, v192, v193\n"
      "v_mfma_f32_16x16x32_bf16 v[228:231], v[32:35], a[68:71], v[228:231]\n"
      "v_mfma_f32_16x16x32_bf16 v[228:231], v[160:163], a[72:75], v[228:231]\n"
      "v_mfma_f32_16x16x32_bf16 v[228:231], v[28:31], a[76:79], v[228:231]\n"
      "v_mfma_f32_16x16x32_bf16 v[190:193], v[224:227], a[80:83], 0\n"
      "v_cvt_pk_bf16_f32 v114, v88, v89\n"
      "v_cvt_pk_bf16_f32 v115, v90, v91\n"
      "v_mfma_f32_16x16x32_bf16 v[190:193], v[32:35], a[84:87], v[190:193]\n"
      "v_mfma_f32_16x16x32_bf16 v[190:193], v[160:163], a[88:91], v[190:193]\n"
      "v_mfma_f32_16x16x32_bf16 v[190:193], v[28:31], a[92:95], v[190:193]\n"
      "v_mfma_f32_16x16x32_bf16 v[88:91], v[224:227], a[96:99], 0\n"
      "v_mfma_f32_16x16x32_bf16 v[88:91], v[32:35], a[100:103], v[88:91]\n"
      "v_cvt_pk_bf16_f32 v52, v228, v229\n"
      "v_cvt_pk_bf16_f32 v53, v230, v231\n"
      "v_mfma_f32_16x16x32_bf16 v[88:91], v[160:163], a[104:107], v[88:91]\n"
      "v_mfma_f32_16x16x32_bf16 v[88:91], v[28:31], a[108:111], v[88:91]\n"
      "v_mfma_f32_16x16x32_bf16 v[228:231], v[224:227], a[112:115], 0\n"
      "v_mfma_f32_16x16x32_bf16 v[228:231], v[32:35], a[116:119], v[228:231]\n"
      "v_cvt_pk_bf16_f32 v128, v190, v191\n"
      "v_cvt_pk_bf16_f32 v129, v192, v193\n"
      "v_mfma_f32_16x16x32_bf16 v[228:231], v[160:163], a[120:123], v[228:231]\n"
      "v_mfma_f32_16x16x32_bf16 v[228:231], v[28:31], a[124:127], v[228:231]\n"
      "ds_read_b64 v[18:19], v11 offset:0\n"
      "v_mfma_f32_16x16x32_bf16 v[28:31], a[128:131], v[24:27], 0\n"
      "v_cvt_pk_bf16_f32 v100, v88, v89\n"
      "v_cvt_pk_bf16_f32 v101, v90, v91\n"
      "v_mfma_f32_16x16x32_bf16 v[28:31], a[132:135], v[152:155], v[28:31]\n"
      "v_mfma_f32_16x16x32_bf16 v[28:31], a[136:139], v[20:23], v[28:31]\n"
      "v_mfma_f32_16x16x32_bf16 v[28:31], a[140:143], v[156:159], v[28:31]\n"
      "v_mfma_f32_16x16x32_bf16 v[88:91], a[144:147], v[24:27], 0\n"
      "v_cvt_pk_bf16_f32 v192, v228, v229\n"
      "v_cvt_pk_bf16_f32 v193, v230, v231\n"
      "v_mfma_f32_16x16x32_bf16 v[88:91], a[148:151], v[152:155], v[88:91]\n"
      "v_mfma_f32_16x16x32_bf16 v[88:91], a[152:155], v[20:23], v[88:91]\n"
      "v_mfma_f32_16x16x32_bf16 v[88:91], a[156:159], v[156:159], v[88:91]\n"
      "v_mfma_f32_16x16x32_bf16 v[228:231], a[160:163], v[24:27], 0\n"
      "v_mfma_f32_16x16x32_bf16 v[228:231], a[164:167], v[152:155], v[228:231]\n"
      "v_cvt_pk_bf16_f32 v160, v28, v29\n"
      "v_cvt_pk_bf16_f32 v161, v30, v31\n"
      "v_mfma_f32_16x16x32_bf16 v[228:231], a[168:171], v[20:23], v[228:231]\n"
      "v_mfma_f32_16x16x32_bf16 v[228:231], a[172:175], v[156:159], v[228:231]\n"
      "v_mfma_f32_16x16x32_bf16 v[32:35], a[176:179], v[24:27], 0\n"
      "v_mfma_f32_16x16x32_bf16 v[32:35], a[180:183], v[152:155], v[32:35]\n"
      "v_cvt_pk_bf16_f32 v162, v88, v89\n"
      "v_cvt_pk_bf16_f32 v163, v90, v91\n"
      "v_mfma_f32_16x16x32_bf16 v[32:35], a[184:187], v[20:23], v[32:35]\n"
      "v_mfma_f32_16x16x32_bf16 v[32:35], a[188:191], v[156:159], v[32:35]\n"
      "s_waitcnt lgkmcnt(2)\n"
      "v_mfma_f32_16x16x32_bf16 v[28:31], v[172:175], v[160:163], 0\n"
      "v_cvt_pk_bf16_f32 v88, v228, v229\n"
      "v_cvt_pk_bf16_f32 v89, v230, v231\n"
      "s_nop 3\n"
      "v_cvt_pk_bf16_f32 v90, v32, v33\n"
      "v_cvt_pk_bf16_f32 v91, v34, v35\n"
      "s_waitcnt lgkmcnt(1)\n"
      "s_nop 0\n"
      "v_mfma_f32_16x16x32_bf16 v[28:31], v[176:179], v[88:91], v[28:31]\n"
      "v_mfma_f32_16x16x32_bf16 v[228:231], v[108:111], v[160:163], 0\n"
      "v_mfma_f32_16x16x32_bf16 v[228:231], v[112:115], v[88:91], v[228:231]\n"
      "v_mfma_f32_16x16x32_bf16 v[32:35], v[164:167], v[160:163], 0\n"
      "v_mfma_f32_16x16x32_bf16 v[32:35], v[168:171], v[88:91], v[32:35]\n"
      "v_mfma_f32_16x16x32_bf16 v[224:227], v[40:43], v[160:163], 0\n"
      "v_mfma_f32_16x16x32_bf16 v[224:227], v[44:47], v[88:91], v[224:227]\n"
      "v_mfma_f32_16x16x32_bf16 v[200:203], v[92:95], v[160:163], 0\n"
      "v_mfma_f32_16x16x32_bf16 v[200:203], v[96:99], v[88:91], v[200:203]\n"
      "v_max_f32 v190, v28, v29\n"
      "v_max3_f32 v190, v190, v30, v31\n"
      "v_max3_f32 v190, v190, v228, v229\n"
      "v_max3_f32 v190, v190, v230, v231\n"
      "v_max3_f32 v190, v190, v32, v33\n"
      "v_mfma_f32_16x16x32_bf16 v[180:183], v[216:219], v[160:163], 0\n"
      "v_max3_f32 v190, v190, v34, v35\n"
      "v_max3_f32 v190, v190, v224, v225\n"
      "v_max3_f32 v190, v190, v226, v227\n"
      "v_max3_f32 v190, v190, v200, v201\n"
      "v_max3_f32 v190, v190, v202, v203\n"
      "v_mfma_f32_16x16x32_bf16 v[180:183], v[220:223], v[88:91], v[180:183]\n"
      "v_mfma_f32_16x16x32_bf16 v[208:211], v[244:247], v[160:163], 0\n"
      "v_mfma_f32_16x16x32_bf16 v[208:211], v[248:251], v[88:91], v[208:211]\n"
      "v_mfma_f32_16x16x32_bf16 v[184:187], a[128:131], v[252:255], 0\n"
      "v_mfma_f32_16x16x32_bf16 v[184:187], a[132:135], v[48:51], v[184:187]\n"
      "v_mfma_f32_16x16x32_bf16 v[184:187], a[136:139], v[124:127], v[184:187]\n"
      "v_mfma_f32_16x16x32_bf16 v[184:187], a[140:143], v[116:119], v[184:187]\n"
      "v_mfma_f32_16x16x32_bf16 v[88:91], a[144:147], v[252:255], 0\n"
      "v_mfma_f32_16x16x32_bf16 v[88:91], a[148:151], v[48:51], v[88:91]\n"
      "v_max3_f32 v190, v190, v180, v181\n"
      "v_max3_f32 v190, v190, v182, v183\n"
      "v_max3_f32 v190, v190, v208, v209\n"
      "v_max3_f32 v190, v190, v210, v211\n"
      "v_mov_b32 v162, v190\n"
      "v_mfma_f32_16x16x32_bf16 v[88:91], a[152:155], v[124:127], v[88:91]\n"
      "v_cvt_pk_bf16_f32 v160, v184, v185\n"
      "v_permlane16_swap_b32 v190, v162\n"
      "v_max_f32 v190, v190, v162\n"
      "v_mov_b32 v162, v190\n"
      "v_cvt_pk_bf16_f32 v161, v186, v187\n"
      "v_mfma_f32_16x16x32_bf16 v[88:91], a[156:159], v[116:119], v[88:91]\n"
      "v_permlane32_swap_b32 v190, v162\n"
      "v_max_f32 v190, v190, v162\n"
      "v_sub_f32 v28, v28, v190\n"
      "v_sub_f32 v29, v29, v190\n"
      "v_sub_f32 v30, v30, v190\n"
      "v_sub_f32 v31, v31, v190\n"
      "v_exp_f32 v28, v28\n"
      "v_mfma_f32_16x16x32_bf16 v[184:187], a[160:163], v[252:255], 0\n"
      "v_exp_f32 v29, v29\n"
      "v_exp_f32 v30, v30\n"
      "v_exp_f32 v31, v31\n"
      "v_cvt_pk_bf16_f32 v162, v28, v29\n"
      "v_cvt_pk_bf16_f32 v163, v30, v31\n"
      "v_mfma_f32_16x16x32_bf16 v[184:187], a[164:167], v[48:51], v[184:187]\n"
      "v_sub_f32 v228, v228, v190\n"
      "v_sub_f32 v229, v229, v190\n"
      "v_sub_f32 v230, v230, v190\n"
      "v_sub_f32 v231, v231, v190\n"
      "v_sub_f32 v32, v32, v190\n"
      "v_sub_f32 v33, v33, v190\n"
      "v_sub_f32 v34, v34, v190\n"
      "v_sub_f32 v35, v35, v190\n"
      "v_mfma_f32_16x16x32_bf16 v[184:187], a[168:171], v[124:127], v[184:187]\n"
      "v_sub_f32 v224, v224, v190\n"
      "v_sub_f32 v225, v225, v190\n"
      "v_sub_f32 v226, v226, v190\n"
      "v_sub_f32 v227, v227, v190\n"
      "v_sub_f32 v200, v200, v190\n"
      "v_sub_f32 v201, v201, v190\n"
      "v_sub_f32 v202, v202, v190\n"
      "v_sub_f32 v203, v203, v190\n"
      "v_mfma_f32_16x16x32_bf16 v[184:187], a[172:175], v[116:119], v[184:187]\n"
      "v_add_f32 v188, v28, v29\n"
      "v_add_f32 v189, v30, v31\n"
      "v_exp_f32 v228, v228\n"
      "v_exp_f32 v229, v229\n"
      "v_exp_f32 v230, v230\n"
      "v_cvt_pk_bf16_f32 v28, v228, v229\n"
      "v_mfma_f32_16x16x32_bf16 v[84:87], a[176:179], v[252:255], 0\n"
      "v_exp_f32 v231, v231\n"
      "v_exp_f32 v32, v32\n"
      "v_cvt_pk_bf16_f32 v29, v230, v231\n"
      "v_exp_f32 v33, v33\n"
      "v_mfma_f32_16x16x32_bf16 v[84:87], a[180:183], v[48:51], v[84:87]\n"
      "v_exp_f32 v34, v34\n"
      "v_exp_f32 v35, v35\n"
      "v_cvt_pk_bf16_f32 v30, v32, v33\n"
      "v_cvt_pk_bf16_f32 v31, v34, v35\n"
      "v_exp_f32 v224, v224\n"
      "v_sub_f32 v180, v180, v190\n"
      "v_mfma_f32_16x16x32_bf16 v[84:87], a[184:187], v[124:127], v[84:87]\n"
      "v_exp_f32 v225, v225\n"
      "v_exp_f32 v226, v226\n"
      "v_exp_f32 v227, v227\n"
      "v_cvt_pk_bf16_f32 v32, v224, v225\n"
      "v_cvt_pk_bf16_f32 v33, v226, v227\n"
      "v_mfma_f32_16x16x32_bf16 v[84:87], a[188:191], v[116:119], v[84:87]\n"
      "v_exp_f32 v200, v200\n"
      "v_exp_f32 v201, v201\n"
      "v_exp_f32 v202, v202\n"
      "v_exp_f32 v203, v203\n"
      "s_waitcnt lgkmcnt(0)\n"
      "v_mfma_f32_16x16x4_4b_bf16 v[224:239], v[162:163], v[18:19], 0 cbsz:2 abid:0\n"
      "v_cvt_pk_bf16_f32 v34, v200, v201\n"
      "v_cvt_pk_bf16_f32 v35, v202, v203\n"
      "v_sub_f32 v181, v181, v190\n"
      "v_sub_f32 v182, v182, v190\n"
      "v_sub_f32 v183, v183, v190\n"
      "v_sub_f32 v208, v208, v190\n"
      "v_sub_f32 v209, v209, v190\n"
      "v_sub_f32 v210, v210, v190\n"
      "v_sub_f32 v211, v211, v190\n"
      "v_exp_f32 v180, v180\n"
      "v_exp_f32 v181, v181\n"
      "v_exp_f32 v182, v182\n"
      "v_exp_f32 v183, v183\n"
      "v_cvt_pk_bf16_f32 v200, v180, v181\n"
      "v_cvt_pk_bf16_f32 v201, v182, v183\n"
      "v_exp_f32 v208, v208\n"
      "v_exp_f32 v209, v209\n"
      "v_smfmac_f32_16x16x64_bf16 v[188:191], v[12:15], v[28:35], v5\n"
      "v_exp_f32 v210, v210\n"
      "v_exp_f32 v211, v211\n"
      "v_mfma_f32_16x16x32_bf16 v[224:227], v[28:31], v[52:55], v[224:227]\n"
      "v_cvt_pk_bf16_f32 v202, v208, v209\n"
      "v_cvt_pk_bf16_f32 v203, v210, v211\n"
      "v_cvt_pk_bf16_f32 v162, v88, v89\n"
      "v_cvt_pk_bf16_f32 v163, v90, v91\n"
      "v_mfma_f32_16x16x32_bf16 v[224:227], v[32:35], v[56:59], v[224:227]\n"
      "v_cvt_pk_bf16_f32 v88, v184, v185\n"
      "v_cvt_pk_bf16_f32 v89, v186, v187\n"
      "v_cvt_pk_bf16_f32 v90, v84, v85\n"
      "v_mfma_f32_16x16x32_bf16 v[224:227], v[200:203], v[120:123], v[224:227]\n"
      "v_cvt_pk_bf16_f32 v91, v86, v87\n"
      "v_smfmac_f32_16x16x32_bf16 v[188:191], v[12:13], v[200:203], v5\n"
      "v_mfma_f32_16x16x32_bf16 v[228:231], v[28:31], v[128:131], v[228:231]\n"
      "v_mfma_f32_16x16x32_bf16 v[228:231], v[32:35], v[132:135], v[228:231]\n"
      "v_mfma_f32_16x16x32_bf16 v[228:231], v[200:203], v[68:71], v[228:231]\n"
      "v_mfma_f32_16x16x32_bf16 v[232:235], v[28:31], v[100:103], v[232:235]\n"
      "v_mfma_f32_16x16x32_bf16 v[232:235], v[32:35], v[104:107], v[232:235]\n"
      "v_mfma_f32_16x16x32_bf16 v[232:235], v[200:203], v[36:39], v[232:235]\n"
      "v_mfma_f32_16x16x32_bf16 v[236:239], v[28:31], v[192:195], v[236:239]\n"
      "v_mfma_f32_16x16x32_bf16 v[236:239], v[32:35], v[196:199], v[236:239]\n"
      "v_add_f32 v188, v188, v189\n"
      "v_rcp_f32 v189, v188\n"
      "v_mfma_f32_16x16x32_bf16 v[236:239], v[200:203], v[240:243], v[236:239]\n"
      "ds_bpermute_b32 v190, v4, v189\n"
      "s_waitcnt lgkmcnt(0)\n"
      "v_mul_f32_dpp v224, v190, v224 row_newbcast:0 row_mask:0xf bank_mask:0xf\n"
      "v_mul_f32_dpp v225, v190, v225 row_newbcast:4 row_mask:0xf bank_mask:0xf\n"
      "v_mul_f32_dpp v226, v190, v226 row_newbcast:8 row_mask:0xf bank_mask:0xf\n"
      "v_mul_f32_dpp v227, v190, v227 row_newbcast:12 row_mask:0xf bank_mask:0xf\n"
      "v_mul_f32_dpp v228, v190, v228 row_newbcast:0 row_mask:0xf bank_mask:0xf\n"
      "v_mul_f32_dpp v229, v190, v229 row_newbcast:4 row_mask:0xf bank_mask:0xf\n"
      "v_mfma_f32_16x16x32_bf16 v[224:227], v[24:27], a[192:195], v[224:227]\n"
      "v_mul_f32_dpp v230, v190, v230 row_newbcast:8 row_mask:0xf bank_mask:0xf\n"
      "v_mul_f32_dpp v231, v190, v231 row_newbcast:12 row_mask:0xf bank_mask:0xf\n"
      "v_mfma_f32_16x16x32_bf16 v[224:227], v[152:155], a[196:199], v[224:227]\n"
      "v_mul_f32_dpp v232, v190, v232 row_newbcast:0 row_mask:0xf bank_mask:0xf\n"
      "v_mul_f32_dpp v233, v190, v233 row_newbcast:4 row_mask:0xf bank_mask:0xf\n"
      "v_mul_f32_dpp v234, v190, v234 row_newbcast:8 row_mask:0xf bank_mask:0xf\n"
      "v_mfma_f32_16x16x32_bf16 v[224:227], v[20:23], a[200:203], v[224:227]\n"
      "v_mul_f32_dpp v235, v190, v235 row_newbcast:12 row_mask:0xf bank_mask:0xf\n"
      "v_mul_f32_dpp v236, v190, v236 row_newbcast:0 row_mask:0xf bank_mask:0xf\n"
      "v_mul_f32_dpp v237, v190, v237 row_newbcast:4 row_mask:0xf bank_mask:0xf\n"
      "v_mfma_f32_16x16x32_bf16 v[224:227], v[156:159], a[204:207], v[224:227]\n"
      "v_mul_f32_dpp v238, v190, v238 row_newbcast:8 row_mask:0xf bank_mask:0xf\n"
      "v_mul_f32_dpp v239, v190, v239 row_newbcast:12 row_mask:0xf bank_mask:0xf\n"
      "v_mfma_f32_16x16x32_bf16 v[228:231], v[24:27], a[208:211], v[228:231]\n"
      "v_mfma_f32_16x16x32_bf16 v[228:231], v[152:155], a[212:215], v[228:231]\n"
      "v_mfma_f32_16x16x32_bf16 v[228:231], v[20:23], a[216:219], v[228:231]\n"
      "v_mfma_f32_16x16x32_bf16 v[228:231], v[156:159], a[220:223], v[228:231]\n"
      "v_mfma_f32_16x16x32_bf16 v[232:235], v[24:27], a[224:227], v[232:235]\n"
      "v_mfma_f32_16x16x32_bf16 v[232:235], v[152:155], a[228:231], v[232:235]\n"
      "v_mfma_f32_16x16x32_bf16 v[232:235], v[20:23], a[232:235], v[232:235]\n"
      "v_mfma_f32_16x16x32_bf16 v[232:235], v[156:159], a[236:239], v[232:235]\n"
      "v_mfma_f32_16x16x32_bf16 v[236:239], v[24:27], a[240:243], v[236:239]\n"
      "v_mfma_f32_16x16x32_bf16 v[236:239], v[152:155], a[244:247], v[236:239]\n"
      "v_mfma_f32_16x16x32_bf16 v[236:239], v[20:23], a[248:251], v[236:239]\n"
      "v_mfma_f32_16x16x32_bf16 v[236:239], v[156:159], a[252:255], v[236:239]\n"
      "v_cvt_pk_bf16_f32 v224, v224, v228\n"
      "v_cvt_pk_bf16_f32 v228, v225, v229\n"
      "v_pk_max_i16 v224, v224, 0\n"
      "v_pk_max_i16 v228, v228, 0\n"
      "v_mfma_f32_16x16x32_bf16 v[20:23], v[172:175], v[160:163], 0\n"
      "v_cvt_pk_bf16_f32 v226, v226, v230\n"
      "v_cvt_pk_bf16_f32 v230, v227, v231\n"
      "v_pk_max_i16 v226, v226, 0\n"
      "v_cvt_pk_bf16_f32 v225, v232, v236\n"
      "v_mfma_f32_16x16x32_bf16 v[20:23], v[176:179], v[88:91], v[20:23]\n"
      "v_cvt_pk_bf16_f32 v229, v233, v237\n"
      "v_pk_max_i16 v225, v225, 0\n"
      "v_pk_max_i16 v229, v229, 0\n"
      "v_cvt_pk_bf16_f32 v227, v234, v238\n"
      "v_mfma_f32_16x16x32_bf16 v[24:27], v[108:111], v[160:163], 0\n"
      "v_cvt_pk_bf16_f32 v231, v235, v239\n"
      "v_pk_max_i16 v227, v227, 0\n"
      "v_pk_max_i16 v230, v230, 0\n"
      "v_max_f32 v186, v20, v21\n"
      "v_mfma_f32_16x16x32_bf16 v[24:27], v[112:115], v[88:91], v[24:27]\n"
      "v_max3_f32 v186, v186, v22, v23\n"
      "v_pk_max_i16 v231, v231, 0\n"
      "v_mfma_f32_16x16x32_bf16 v[28:31], v[164:167], v[160:163], 0\n"
      "v_mfma_f32_16x16x32_bf16 v[28:31], v[168:171], v[88:91], v[28:31]\n"
      "v_mfma_f32_16x16x32_bf16 v[236:239], v[40:43], v[160:163], 0\n"
      "v_mfma_f32_16x16x32_bf16 v[236:239], v[44:47], v[88:91], v[236:239]\n"
      "v_mfma_f32_16x16x32_bf16 v[232:235], v[92:95], v[160:163], 0\n"
      "v_mfma_f32_16x16x32_bf16 v[232:235], v[96:99], v[88:91], v[232:235]\n"
      "v_max3_f32 v186, v186, v24, v25\n"
      "v_max3_f32 v186, v186, v26, v27\n"
      "v_mfma_f32_16x16x32_bf16 v[156:159], v[216:219], v[160:163], 0\n"
      "v_mfma_f32_16x16x32_bf16 v[156:159], v[220:223], v[88:91], v[156:159]\n"
      "v_max3_f32 v186, v186, v28, v29\n"
      "v_max3_f32 v186, v186, v30, v31\n"
      "v_max3_f32 v186, v186, v236, v237\n"
      "v_max3_f32 v186, v186, v238, v239\n"
      "v_max3_f32 v186, v186, v232, v233\n"
      "v_max3_f32 v186, v186, v234, v235\n"
      "v_mfma_f32_16x16x32_bf16 v[152:155], v[244:247], v[160:163], 0\n"
      "v_mfma_f32_16x16x32_bf16 v[152:155], v[248:251], v[88:91], v[152:155]\n"
      "v_max3_f32 v186, v186, v156, v157\n"
      "v_max3_f32 v186, v186, v158, v159\n"
      "v_mfma_f32_16x16x32_bf16 v[160:163], a[128:131], v[148:151], 0\n"
      "v_mfma_f32_16x16x32_bf16 v[160:163], a[132:135], v[136:139], v[160:163]\n"
      "s_add_u32 s56, s48, 20480\n"
      "buffer_store_dwordx2 v[224:225], v2, s[44:47], s56 offen offset:0 nt sc1\n"
      "buffer_store_dwordx2 v[228:229], v2, s[44:47], s56 offen offset:256 nt sc1\n"
      "v_mfma_f32_16x16x32_bf16 v[160:163], a[136:139], v[140:143], v[160:163]\n"
      "v_max3_f32 v186, v186, v152, v153\n"
      "v_max3_f32 v186, v186, v154, v155\n"
      "v_mov_b32 v88, v186\n"
      "buffer_store_dwordx2 v[226:227], v2, s[44:47], s56 offen offset:512 nt sc1\n"
      "v_mfma_f32_16x16x32_bf16 v[160:163], a[140:143], v[144:147], v[160:163]\n"
      "v_permlane16_swap_b32 v186, v88\n"
      "v_max_f32 v186, v186, v88\n"
      "v_mfma_f32_16x16x32_bf16 v[32:35], a[144:147], v[148:151], 0\n"
      "v_mov_b32 v88, v186\n"
      "buffer_store_dwordx2 v[230:231], v2, s[44:47], s56 offen offset:768 nt sc1\n"
      "v_mfma_f32_16x16x32_bf16 v[32:35], a[148:151], v[136:139], v[32:35]\n"
      "v_permlane32_swap_b32 v186, v88\n"
      "v_max_f32 v186, v186, v88\n"
      "v_sub_f32 v20, v20, v186\n"
      "v_sub_f32 v21, v21, v186\n"
      "v_sub_f32 v22, v22, v186\n"
      "v_sub_f32 v23, v23, v186\n"
      "v_sub_f32 v24, v24, v186\n"
      "v_mfma_f32_16x16x32_bf16 v[32:35], a[152:155], v[140:143], v[32:35]\n"
      "v_exp_f32 v20, v20\n"
      "v_exp_f32 v21, v21\n"
      "v_exp_f32 v22, v22\n"
      "v_cvt_pk_bf16_f32 v190, v20, v21\n"
      "v_mfma_f32_16x16x32_bf16 v[32:35], a[156:159], v[144:147], v[32:35]\n"
      "v_exp_f32 v23, v23\n"
      "v_sub_f32 v25, v25, v186\n"
      "v_cvt_pk_bf16_f32 v191, v22, v23\n"
      "v_sub_f32 v26, v26, v186\n"
      "v_sub_f32 v27, v27, v186\n"
      "v_sub_f32 v28, v28, v186\n"
      "v_mfma_f32_16x16x32_bf16 v[88:91], a[160:163], v[148:151], 0\n"
      "v_sub_f32 v29, v29, v186\n"
      "v_sub_f32 v30, v30, v186\n"
      "v_sub_f32 v31, v31, v186\n"
      "v_sub_f32 v236, v236, v186\n"
      "v_sub_f32 v237, v237, v186\n"
      "v_sub_f32 v238, v238, v186\n"
      "v_sub_f32 v239, v239, v186\n"
      "v_mfma_f32_16x16x32_bf16 v[88:91], a[164:167], v[136:139], v[88:91]\n"
      "v_sub_f32 v232, v232, v186\n"
      "v_sub_f32 v233, v233, v186\n"
      "v_sub_f32 v234, v234, v186\n"
      "v_sub_f32 v235, v235, v186\n"
      "v_add_f32 v184, v20, v21\n"
      "v_add_f32 v185, v22, v23\n"
      "v_mfma_f32_16x16x32_bf16 v[88:91], a[168:171], v[140:143], v[88:91]\n"
      "v_exp_f32 v24, v24\n"
      "v_exp_f32 v25, v25\n"
      "v_exp_f32 v26, v26\n"
      "v_exp_f32 v27, v27\n"
      "v_mfma_f32_16x16x32_bf16 v[88:91], a[172:175], v[144:147], v[88:91]\n"
      "v_cvt_pk_bf16_f32 v20, v24, v25\n"
      "v_cvt_pk_bf16_f32 v21, v26, v27\n"
      "v_exp_f32 v28, v28\n"
      "v_exp_f32 v29, v29\n"
      "v_mfma_f32_16x16x32_bf16 v[208:211], a[176:179], v[148:151], 0\n"
      "v_exp_f32 v30, v30\n"
      "v_exp_f32 v31, v31\n"
      "v_cvt_pk_bf16_f32 v22, v28, v29\n"
      "v_cvt_pk_bf16_f32 v23, v30, v31\n"
      "v_exp_f32 v236, v236\n"
      "v_mfma_f32_16x16x32_bf16 v[208:211], a[180:183], v[136:139], v[208:211]\n"
      "v_exp_f32 v237, v237\n"
      "v_exp_f32 v238, v238\n"
      "v_exp_f32 v239, v239\n"
      "v_cvt_pk_bf16_f32 v24, v236, v237\n"
      "v_mfma_f32_16x16x32_bf16 v[208:211], a[184:187], v[140:143], v[208:211]\n"
      "v_cvt_pk_bf16_f32 v25, v238, v239\n"
      "v_exp_f32 v232, v232\n"
      "v_exp_f32 v233, v233\n"
      "v_exp_f32 v234, v234\n"
      "v_mfma_f32_16x16x32_bf16 v[208:211], a[188:191], v[144:147], v[208:211]\n"
      "v_exp_f32 v235, v235\n"
      "v_cvt_pk_bf16_f32 v26, v232, v233\n"
      "v_cvt_pk_bf16_f32 v27, v234, v235\n"
      "v_sub_f32 v156, v156, v186\n"
      "v_sub_f32 v157, v157, v186\n"
      "v_sub_f32 v158, v158, v186\n"
      "v_mfma_f32_16x16x4_4b_bf16 v[224:239], v[190:191], v[18:19], 0 cbsz:2 abid:0\n"
      "v_sub_f32 v159, v159, v186\n"
      "v_sub_f32 v152, v152, v186\n"
      "v_sub_f32 v153, v153, v186\n"
      "v_sub_f32 v154, v154, v186\n"
      "v_sub_f32 v155, v155, v186\n"
      "v_exp_f32 v156, v156\n"
      "v_exp_f32 v157, v157\n"
      "v_exp_f32 v158, v158\n"
      "v_exp_f32 v159, v159\n"
      "v_cvt_pk_bf16_f32 v28, v156, v157\n"
      "v_cvt_pk_bf16_f32 v29, v158, v159\n"
      "v_exp_f32 v152, v152\n"
      "v_exp_f32 v153, v153\n"
      "v_cvt_pk_bf16_f32 v156, v160, v161\n"
      "v_smfmac_f32_16x16x64_bf16 v[184:187], v[12:15], v[20:27], v5\n"
      "v_exp_f32 v154, v154\n"
      "v_exp_f32 v155, v155\n"
      "v_mfma_f32_16x16x32_bf16 v[224:227], v[20:23], v[52:55], v[224:227]\n"
      "v_cvt_pk_bf16_f32 v30, v152, v153\n"
      "v_cvt_pk_bf16_f32 v31, v154, v155\n"
      "v_cvt_pk_bf16_f32 v157, v162, v163\n"
      "v_mfma_f32_16x16x32_bf16 v[224:227], v[24:27], v[56:59], v[224:227]\n"
      "v_cvt_pk_bf16_f32 v158, v32, v33\n"
      "v_cvt_pk_bf16_f32 v159, v34, v35\n"
      "v_cvt_pk_bf16_f32 v32, v88, v89\n"
      "v_mfma_f32_16x16x32_bf16 v[224:227], v[28:31], v[120:123], v[224:227]\n"
      "v_cvt_pk_bf16_f32 v33, v90, v91\n"
      "v_cvt_pk_bf16_f32 v34, v208, v209\n"
      "v_cvt_pk_bf16_f32 v35, v210, v211\n"
      "v_smfmac_f32_16x16x32_bf16 v[184:187], v[12:13], v[28:31], v5\n"
      "v_mfma_f32_16x16x32_bf16 v[228:231], v[20:23], v[128:131], v[228:231]\n"
      "v_mfma_f32_16x16x32_bf16 v[228:231], v[24:27], v[132:135], v[228:231]\n"
      "v_mfma_f32_16x16x32_bf16 v[228:231], v[28:31], v[68:71], v[228:231]\n"
      "v_mfma_f32_16x16x32_bf16 v[232:235], v[20:23], v[100:103], v[232:235]\n"
      "v_mfma_f32_16x16x32_bf16 v[232:235], v[24:27], v[104:107], v[232:235]\n"
      "v_mfma_f32_16x16x32_bf16 v[232:235], v[28:31], v[36:39], v[232:235]\n"
      "v_mfma_f32_16x16x32_bf16 v[236:239], v[20:23], v[192:195], v[236:239]\n"
      "v_mfma_f32_16x16x32_bf16 v[236:239], v[24:27], v[196:199], v[236:239]\n"
      "v_add_f32 v184, v184, v185\n"
      "v_rcp_f32 v185, v184\n"
      "v_mfma_f32_16x16x32_bf16 v[236:239], v[28:31], v[240:243], v[236:239]\n"
      "ds_bpermute_b32 v186, v4, v185\n"
      "s_waitcnt lgkmcnt(0)\n"
      "v_mul_f32_dpp v224, v186, v224 row_newbcast:0 row_mask:0xf bank_mask:0xf\n"
      "v_mul_f32_dpp v225, v186, v225 row_newbcast:4 row_mask:0xf bank_mask:0xf\n"
      "v_mul_f32_dpp v226, v186, v226 row_newbcast:8 row_mask:0xf bank_mask:0xf\n"
      "v_mul_f32_dpp v227, v186, v227 row_newbcast:12 row_mask:0xf bank_mask:0xf\n"
      "v_mul_f32_dpp v228, v186, v228 row_newbcast:0 row_mask:0xf bank_mask:0xf\n"
      "v_mul_f32_dpp v229, v186, v229 row_newbcast:4 row_mask:0xf bank_mask:0xf\n"
      "v_mfma_f32_16x16x32_bf16 v[224:227], v[252:255], a[192:195], v[224:227]\n"
      "v_mul_f32_dpp v230, v186, v230 row_newbcast:8 row_mask:0xf bank_mask:0xf\n"
      "v_mul_f32_dpp v231, v186, v231 row_newbcast:12 row_mask:0xf bank_mask:0xf\n"
      "v_mfma_f32_16x16x32_bf16 v[224:227], v[48:51], a[196:199], v[224:227]\n"
      "v_mul_f32_dpp v232, v186, v232 row_newbcast:0 row_mask:0xf bank_mask:0xf\n"
      "v_mul_f32_dpp v233, v186, v233 row_newbcast:4 row_mask:0xf bank_mask:0xf\n"
      "v_mul_f32_dpp v234, v186, v234 row_newbcast:8 row_mask:0xf bank_mask:0xf\n"
      "v_mfma_f32_16x16x32_bf16 v[224:227], v[124:127], a[200:203], v[224:227]\n"
      "v_mul_f32_dpp v235, v186, v235 row_newbcast:12 row_mask:0xf bank_mask:0xf\n"
      "v_mul_f32_dpp v236, v186, v236 row_newbcast:0 row_mask:0xf bank_mask:0xf\n"
      "v_mul_f32_dpp v237, v186, v237 row_newbcast:4 row_mask:0xf bank_mask:0xf\n"
      "v_mul_f32_dpp v238, v186, v238 row_newbcast:8 row_mask:0xf bank_mask:0xf\n"
      "v_mfma_f32_16x16x32_bf16 v[224:227], v[116:119], a[204:207], v[224:227]\n"
      "v_mul_f32_dpp v239, v186, v239 row_newbcast:12 row_mask:0xf bank_mask:0xf\n"
      "v_mfma_f32_16x16x32_bf16 v[228:231], v[252:255], a[208:211], v[228:231]\n"
      "v_mfma_f32_16x16x32_bf16 v[228:231], v[48:51], a[212:215], v[228:231]\n"
      "v_mfma_f32_16x16x32_bf16 v[228:231], v[124:127], a[216:219], v[228:231]\n"
      "v_mfma_f32_16x16x32_bf16 v[228:231], v[116:119], a[220:223], v[228:231]\n"
      "v_mfma_f32_16x16x32_bf16 v[232:235], v[252:255], a[224:227], v[232:235]\n"
      "v_mfma_f32_16x16x32_bf16 v[232:235], v[48:51], a[228:231], v[232:235]\n"
      "v_mfma_f32_16x16x32_bf16 v[232:235], v[124:127], a[232:235], v[232:235]\n"
      "v_mfma_f32_16x16x32_bf16 v[232:235], v[116:119], a[236:239], v[232:235]\n"
      "v_mfma_f32_16x16x32_bf16 v[236:239], v[252:255], a[240:243], v[236:239]\n"
      "v_mfma_f32_16x16x32_bf16 v[236:239], v[48:51], a[244:247], v[236:239]\n"
      "v_mfma_f32_16x16x32_bf16 v[236:239], v[124:127], a[248:251], v[236:239]\n"
      "v_mfma_f32_16x16x32_bf16 v[236:239], v[116:119], a[252:255], v[236:239]\n"
      "v_cvt_pk_bf16_f32 v224, v224, v228\n"
      "v_cvt_pk_bf16_f32 v228, v225, v229\n"
      "v_pk_max_i16 v224, v224, 0\n"
      "v_pk_max_i16 v228, v228, 0\n"
      "v_cvt_pk_bf16_f32 v226, v226, v230\n"
      "v_mfma_f32_16x16x32_bf16 v[20:23], v[172:175], v[156:159], 0\n"
      "v_cvt_pk_bf16_f32 v230, v227, v231\n"
      "v_pk_max_i16 v226, v226, 0\n"
      "v_cvt_pk_bf16_f32 v225, v232, v236\n"
      "v_cvt_pk_bf16_f32 v229, v233, v237\n"
      "v_mfma_f32_16x16x32_bf16 v[20:23], v[176:179], v[32:35], v[20:23]\n"
      "v_pk_max_i16 v225, v225, 0\n"
      "v_pk_max_i16 v229, v229, 0\n"
      "v_cvt_pk_bf16_f32 v227, v234, v238\n"
      "v_cvt_pk_bf16_f32 v231, v235, v239\n"
      "v_pk_max_i16 v227, v227, 0\n"
      "v_mfma_f32_16x16x32_bf16 v[88:91], v[108:111], v[156:159], 0\n"
      "v_pk_max_i16 v230, v230, 0\n"
      "v_pk_max_i16 v231, v231, 0\n"
      "v_max_f32 v190, v20, v21\n"
      "v_max3_f32 v190, v190, v22, v23\n"
      "v_mfma_f32_16x16x32_bf16 v[88:91], v[112:115], v[32:35], v[88:91]\n"
      "v_mfma_f32_16x16x32_bf16 v[24:27], v[164:167], v[156:159], 0\n"
      "v_mfma_f32_16x16x32_bf16 v[24:27], v[168:171], v[32:35], v[24:27]\n"
      "v_mfma_f32_16x16x32_bf16 v[236:239], v[40:43], v[156:159], 0\n"
      "v_mfma_f32_16x16x32_bf16 v[236:239], v[44:47], v[32:35], v[236:239]\n"
      "v_mfma_f32_16x16x32_bf16 v[232:235], v[92:95], v[156:159], 0\n"
      "v_mfma_f32_16x16x32_bf16 v[232:235], v[96:99], v[32:35], v[232:235]\n"
      "v_mfma_f32_16x16x32_bf16 v[160:163], v[216:219], v[156:159], 0\n"
      "v_mfma_f32_16x16x32_bf16 v[160:163], v[220:223], v[32:35], v[160:163]\n"
      "v_max3_f32 v190, v190, v88, v89\n"
      "v_max3_f32 v190, v190, v90, v91\n"
      "v_max3_f32 v190, v190, v24, v25\n"
      "v_max3_f32 v190, v190, v26, v27\n"
      "v_max3_f32 v190, v190, v236, v237\n"
      "v_max3_f32 v190, v190, v238, v239\n"
      "v_max3_f32 v190, v190, v232, v233\n"
      "v_mfma_f32_16x16x32_bf16 v[152:155], v[244:247], v[156:159], 0\n"
      "v_max3_f32 v190, v190, v234, v235\n"
      "v_max3_f32 v190, v190, v160, v161\n"
      "v_max3_f32 v190, v190, v162, v163\n"
      "v_mfma_f32_16x16x32_bf16 v[152:155], v[248:251], v[32:35], v[152:155]\n"
      "ds_read_b128 v[116:119], v0 offset:8192\n"
      "ds_read_b128 v[124:127], v0 offset:8704\n"
      "ds_read_b128 v[48:51], v0 offset:9216\n"
      "ds_read_b128 v[252:255], v0 offset:9728\n"
      "s_add_u32 s56, s48, 16384\n"
      "buffer_store_dwordx2 v[224:225], v2, s[44:47], s56 offen offset:0 nt sc1\n"
      "buffer_store_dwordx2 v[228:229], v2, s[44:47], s56 offen offset:256 nt sc1\n"
      "buffer_store_dwordx2 v[226:227], v2, s[44:47], s56 offen offset:512 nt sc1\n"
      "v_max3_f32 v190, v190, v152, v153\n"
      "v_max3_f32 v190, v190, v154, v155\n"
      "v_mov_b32 v32, v190\n"
      "buffer_store_dwordx2 v[230:231], v2, s[44:47], s56 offen offset:768 nt sc1\n"
      "s_nop 0\n"
      "v_permlane16_swap_b32 v190, v32\n"
      "v_max_f32 v190, v190, v32\n"
      "v_mov_b32 v32, v190\n"
      "s_nop 1\n"
      "v_permlane32_swap_b32 v190, v32\n"
      "v_max_f32 v190, v190, v32\n"
      "v_sub_f32 v20, v20, v190\n"
      "v_sub_f32 v21, v21, v190\n"
      "v_sub_f32 v22, v22, v190\n"
      "s_waitcnt lgkmcnt(3)\n"
      "v_mfma_f32_16x16x32_bf16 v[32:35], a[128:131], v[116:119], 0\n"
      "v_sub_f32 v23, v23, v190\n"
      "v_sub_f32 v88, v88, v190\n"
      "s_waitcnt lgkmcnt(2)\n"
      "v_mfma_f32_16x16x32_bf16 v[32:35], a[132:135], v[124:127], v[32:35]\n"
      "v_exp_f32 v20, v20\n"
      "s_waitcnt lgkmcnt(1)\n"
      "v_mfma_f32_16x16x32_bf16 v[32:35], a[136:139], v[48:51], v[32:35]\n"
      "v_exp_f32 v21, v21\n"
      "s_waitcnt lgkmcnt(0)\n"
      "v_mfma_f32_16x16x32_bf16 v[32:35], a[140:143], v[252:255], v[32:35]\n"
      "v_exp_f32 v22, v22\n"
      "v_mfma_f32_16x16x32_bf16 v[28:31], a[144:147], v[116:119], 0\n"
      "v_exp_f32 v23, v23\n"
      "v_mfma_f32_16x16x32_bf16 v[28:31], a[148:151], v[124:127], v[28:31]\n"
      "v_cvt_pk_bf16_f32 v156, v20, v21\n"
      "v_cvt_pk_bf16_f32 v157, v22, v23\n"
      "v_mfma_f32_16x16x32_bf16 v[28:31], a[152:155], v[48:51], v[28:31]\n"
      "v_sub_f32 v89, v89, v190\n"
      "v_sub_f32 v90, v90, v190\n"
      "v_sub_f32 v91, v91, v190\n"
      "v_sub_f32 v24, v24, v190\n"
      "v_mfma_f32_16x16x32_bf16 v[28:31], a[156:159], v[252:255], v[28:31]\n"
      "v_sub_f32 v25, v25, v190\n"
      "v_sub_f32 v26, v26, v190\n"
      "v_sub_f32 v27, v27, v190\n"
      "v_sub_f32 v236, v236, v190\n"
      "v_sub_f32 v237, v237, v190\n"
      "v_sub_f32 v238, v238, v190\n"
      "v_mfma_f32_16x16x32_bf16 v[184:187], a[160:163], v[116:119], 0\n"
      "v_sub_f32 v239, v239, v190\n"
      "v_sub_f32 v232, v232, v190\n"
      "v_sub_f32 v233, v233, v190\n"
      "v_sub_f32 v234, v234, v190\n"
      "v_sub_f32 v235, v235, v190\n"
      "v_add_f32 v86, v20, v21\n"
      "v_add_f32 v87, v22, v23\n"
      "v_mfma_f32_16x16x32_bf16 v[184:187], a[164:167], v[124:127], v[184:187]\n"
      "v_exp_f32 v88, v88\n"
      "v_exp_f32 v89, v89\n"
      "v_exp_f32 v90, v90\n"
      "v_mfma_f32_16x16x32_bf16 v[184:187], a[168:171], v[48:51], v[184:187]\n"
      "v_exp_f32 v91, v91\n"
      "v_cvt_pk_bf16_f32 v20, v88, v89\n"
      "v_cvt_pk_bf16_f32 v21, v90, v91\n"
      "v_exp_f32 v24, v24\n"
      "v_mfma_f32_16x16x32_bf16 v[184:187], a[172:175], v[252:255], v[184:187]\n"
      "v_exp_f32 v25, v25\n"
      "v_exp_f32 v26, v26\n"
      "v_exp_f32 v27, v27\n"
      "v_cvt_pk_bf16_f32 v22, v24, v25\n"
      "v_mfma_f32_16x16x32_bf16 v[208:211], a[176:179], v[116:119], 0\n"
      "v_cvt_pk_bf16_f32 v23, v26, v27\n"
      "v_exp_f32 v236, v236\n"
      "v_exp_f32 v237, v237\n"
      "v_exp_f32 v238, v238\n"
      "v_mfma_f32_16x16x32_bf16 v[208:211], a[180:183], v[124:127], v[208:211]\n"
      "v_exp_f32 v239, v239\n"
      "v_cvt_pk_bf16_f32 v24, v236, v237\n"
      "v_cvt_pk_bf16_f32 v25, v238, v239\n"
      "v_exp_f32 v232, v232\n"
      "v_mfma_f32_16x16x32_bf16 v[208:211], a[184:187], v[48:51], v[208:211]\n"
      "v_exp_f32 v233, v233\n"
      "v_exp_f32 v234, v234\n"
      "v_exp_f32 v235, v235\n"
      "v_cvt_pk_bf16_f32 v26, v232, v233\n"
      "v_mfma_f32_16x16x32_bf16 v[208:211], a[188:191], v[252:255], v[208:211]\n"
      "v_cvt_pk_bf16_f32 v27, v234, v235\n"
      "v_sub_f32 v160, v160, v190\n"
      "v_sub_f32 v161, v161, v190\n"
      "v_sub_f32 v162, v162, v190\n"
      "v_sub_f32 v163, v163, v190\n"
      "v_sub_f32 v152, v152, v190\n"
      "v_mfma_f32_16x16x4_4b_bf16 v[224:239], v[156:157], v[18:19], 0 cbsz:2 abid:0\n"
      "s_add_u32 m0, s53, 8192\n"
      "s_add_u32 s54, s49, 8192\n"
      "buffer_load_dwordx4 v1, s[40:43], s54 offen offset:0 lds\n"
      "v_sub_f32 v153, v153, v190\n"
      "v_sub_f32 v154, v154, v190\n"
      "v_sub_f32 v155, v155, v190\n"
      "v_exp_f32 v160, v160\n"
      "v_exp_f32 v161, v161\n"
      "v_exp_f32 v162, v162\n"
      "v_exp_f32 v163, v163\n"
      "v_cvt_pk_bf16_f32 v188, v160, v161\n"
      "v_cvt_pk_bf16_f32 v189, v162, v163\n"
      "v_exp_f32 v152, v152\n"
      "v_exp_f32 v153, v153\n"
      "v_exp_f32 v154, v154\n"
      "v_smfmac_f32_16x16x64_bf16 v[86:89], v[12:15], v[20:27], v5\n"
      "v_exp_f32 v155, v155\n"
      "v_cvt_pk_bf16_f32 v190, v152, v153\n"
      "v_mfma_f32_16x16x32_bf16 v[224:227], v[20:23], v[52:55], v[224:227]\n"
      "buffer_load_dwordx4 v1, s[40:43], s54 offen offset:2048 lds\n"
      "s_add_u32 m0, s53, 9088\n"
      "v_cvt_pk_bf16_f32 v191, v154, v155\n"
      "v_cvt_pk_bf16_f32 v156, v32, v33\n"
      "v_cvt_pk_bf16_f32 v157, v34, v35\n"
      "v_mfma_f32_16x16x32_bf16 v[224:227], v[24:27], v[56:59], v[224:227]\n"
      "v_cvt_pk_bf16_f32 v158, v28, v29\n"
      "v_cvt_pk_bf16_f32 v159, v30, v31\n"
      "v_cvt_pk_bf16_f32 v32, v184, v185\n"
      "v_mfma_f32_16x16x32_bf16 v[224:227], v[188:191], v[120:123], v[224:227]\n"
      "buffer_load_dwordx4 v1, s[40:43], s54 offen offset:128 lds\n"
      "v_cvt_pk_bf16_f32 v33, v186, v187\n"
      "v_cvt_pk_bf16_f32 v34, v208, v209\n"
      "v_cvt_pk_bf16_f32 v35, v210, v211\n"
      "v_smfmac_f32_16x16x32_bf16 v[86:89], v[12:13], v[188:191], v5\n"
      "v_mfma_f32_16x16x32_bf16 v[228:231], v[20:23], v[128:131], v[228:231]\n"
      "buffer_load_dwordx4 v1, s[40:43], s54 offen offset:2176 lds\n"
      "v_mfma_f32_16x16x32_bf16 v[228:231], v[24:27], v[132:135], v[228:231]\n"
      "v_mfma_f32_16x16x32_bf16 v[228:231], v[188:191], v[68:71], v[228:231]\n"
      "v_mfma_f32_16x16x32_bf16 v[232:235], v[20:23], v[100:103], v[232:235]\n"
      "v_mfma_f32_16x16x32_bf16 v[232:235], v[24:27], v[104:107], v[232:235]\n"
      "v_mfma_f32_16x16x32_bf16 v[232:235], v[188:191], v[36:39], v[232:235]\n"
      "v_mfma_f32_16x16x32_bf16 v[236:239], v[20:23], v[192:195], v[236:239]\n"
      "v_add_f32 v86, v86, v87\n"
      "v_rcp_f32 v87, v86\n"
      "v_mfma_f32_16x16x32_bf16 v[236:239], v[24:27], v[196:199], v[236:239]\n"
      "ds_bpermute_b32 v88, v4, v87\n"
      "v_mfma_f32_16x16x32_bf16 v[236:239], v[188:191], v[240:243], v[236:239]\n"
      "s_waitcnt lgkmcnt(0)\n"
      "v_mul_f32_dpp v224, v88, v224 row_newbcast:0 row_mask:0xf bank_mask:0xf\n"
      "v_mul_f32_dpp v225, v88, v225 row_newbcast:4 row_mask:0xf bank_mask:0xf\n"
      "v_mul_f32_dpp v226, v88, v226 row_newbcast:8 row_mask:0xf bank_mask:0xf\n"
      "v_mul_f32_dpp v227, v88, v227 row_newbcast:12 row_mask:0xf bank_mask:0xf\n"
      "v_mul_f32_dpp v228, v88, v228 row_newbcast:0 row_mask:0xf bank_mask:0xf\n"
      "v_mul_f32_dpp v229, v88, v229 row_newbcast:4 row_mask:0xf bank_mask:0xf\n"
      "v_mfma_f32_16x16x32_bf16 v[224:227], v[148:151], a[192:195], v[224:227]\n"
      "v_mul_f32_dpp v230, v88, v230 row_newbcast:8 row_mask:0xf bank_mask:0xf\n"
      "v_mul_f32_dpp v231, v88, v231 row_newbcast:12 row_mask:0xf bank_mask:0xf\n"
      "v_mfma_f32_16x16x32_bf16 v[224:227], v[136:139], a[196:199], v[224:227]\n"
      "v_mul_f32_dpp v232, v88, v232 row_newbcast:0 row_mask:0xf bank_mask:0xf\n"
      "v_mul_f32_dpp v233, v88, v233 row_newbcast:4 row_mask:0xf bank_mask:0xf\n"
      "v_mul_f32_dpp v234, v88, v234 row_newbcast:8 row_mask:0xf bank_mask:0xf\n"
      "v_mfma_f32_16x16x32_bf16 v[224:227], v[140:143], a[200:203], v[224:227]\n"
      "v_mul_f32_dpp v235, v88, v235 row_newbcast:12 row_mask:0xf bank_mask:0xf\n"
      "v_mul_f32_dpp v236, v88, v236 row_newbcast:0 row_mask:0xf bank_mask:0xf\n"
      "v_mul_f32_dpp v237, v88, v237 row_newbcast:4 row_mask:0xf bank_mask:0xf\n"
      "v_mul_f32_dpp v238, v88, v238 row_newbcast:8 row_mask:0xf bank_mask:0xf\n"
      "v_mfma_f32_16x16x32_bf16 v[224:227], v[144:147], a[204:207], v[224:227]\n"
      "v_mul_f32_dpp v239, v88, v239 row_newbcast:12 row_mask:0xf bank_mask:0xf\n"
      "v_mfma_f32_16x16x32_bf16 v[228:231], v[148:151], a[208:211], v[228:231]\n"
      "v_mfma_f32_16x16x32_bf16 v[228:231], v[136:139], a[212:215], v[228:231]\n"
      "v_mfma_f32_16x16x32_bf16 v[228:231], v[140:143], a[216:219], v[228:231]\n"
      "v_mfma_f32_16x16x32_bf16 v[228:231], v[144:147], a[220:223], v[228:231]\n"
      "v_mfma_f32_16x16x32_bf16 v[232:235], v[148:151], a[224:227], v[232:235]\n"
      "v_mfma_f32_16x16x32_bf16 v[232:235], v[136:139], a[228:231], v[232:235]\n"
      "v_mfma_f32_16x16x32_bf16 v[232:235], v[140:143], a[232:235], v[232:235]\n"
      "v_mfma_f32_16x16x32_bf16 v[232:235], v[144:147], a[236:239], v[232:235]\n"
      "v_mfma_f32_16x16x32_bf16 v[236:239], v[148:151], a[240:243], v[236:239]\n"
      "v_mfma_f32_16x16x32_bf16 v[236:239], v[136:139], a[244:247], v[236:239]\n"
      "v_mfma_f32_16x16x32_bf16 v[236:239], v[140:143], a[248:251], v[236:239]\n"
      "v_mfma_f32_16x16x32_bf16 v[236:239], v[144:147], a[252:255], v[236:239]\n"
      "v_cvt_pk_bf16_f32 v224, v224, v228\n"
      "v_cvt_pk_bf16_f32 v228, v225, v229\n"
      "v_pk_max_i16 v224, v224, 0\n"
      "v_pk_max_i16 v228, v228, 0\n"
      "v_cvt_pk_bf16_f32 v226, v226, v230\n"
      "v_mfma_f32_16x16x32_bf16 v[140:143], v[172:175], v[156:159], 0\n"
      "v_cvt_pk_bf16_f32 v230, v227, v231\n"
      "v_pk_max_i16 v226, v226, 0\n"
      "v_cvt_pk_bf16_f32 v225, v232, v236\n"
      "v_cvt_pk_bf16_f32 v229, v233, v237\n"
      "v_mfma_f32_16x16x32_bf16 v[140:143], v[176:179], v[32:35], v[140:143]\n"
      "v_pk_max_i16 v225, v225, 0\n"
      "v_pk_max_i16 v229, v229, 0\n"
      "v_cvt_pk_bf16_f32 v227, v234, v238\n"
      "v_cvt_pk_bf16_f32 v231, v235, v239\n"
      "v_pk_max_i16 v227, v227, 0\n"
      "v_mfma_f32_16x16x32_bf16 v[88:91], v[108:111], v[156:159], 0\n"
      "v_pk_max_i16 v230, v230, 0\n"
      "v_pk_max_i16 v231, v231, 0\n"
      "v_max_f32 v188, v140, v141\n"
      "v_max3_f32 v188, v188, v142, v143\n"
      "v_mfma_f32_16x16x32_bf16 v[88:91], v[112:115], v[32:35], v[88:91]\n"
      "v_mfma_f32_16x16x32_bf16 v[144:147], v[164:167], v[156:159], 0\n"
      "v_mfma_f32_16x16x32_bf16 v[144:147], v[168:171], v[32:35], v[144:147]\n"
      "v_mfma_f32_16x16x32_bf16 v[236:239], v[40:43], v[156:159], 0\n"
      "v_mfma_f32_16x16x32_bf16 v[236:239], v[44:47], v[32:35], v[236:239]\n"
      "v_mfma_f32_16x16x32_bf16 v[232:235], v[92:95], v[156:159], 0\n"
      "v_mfma_f32_16x16x32_bf16 v[232:235], v[96:99], v[32:35], v[232:235]\n"
      "v_mfma_f32_16x16x32_bf16 v[160:163], v[216:219], v[156:159], 0\n"
      "v_mfma_f32_16x16x32_bf16 v[160:163], v[220:223], v[32:35], v[160:163]\n"
      "v_max3_f32 v188, v188, v88, v89\n"
      "v_max3_f32 v188, v188, v90, v91\n"
      "v_max3_f32 v188, v188, v144, v145\n"
      "v_max3_f32 v188, v188, v146, v147\n"
      "v_max3_f32 v188, v188, v236, v237\n"
      "v_max3_f32 v188, v188, v238, v239\n"
      "v_max3_f32 v188, v188, v232, v233\n"
      "v_mfma_f32_16x16x32_bf16 v[152:155], v[244:247], v[156:159], 0\n"
      "v_max3_f32 v188, v188, v234, v235\n"
      "v_max3_f32 v188, v188, v160, v161\n"
      "v_max3_f32 v188, v188, v162, v163\n"
      "v_mfma_f32_16x16x32_bf16 v[152:155], v[248:251], v[32:35], v[152:155]\n"
      "ds_read_b128 v[136:139], v0 offset:4096\n"
      "ds_read_b128 v[148:151], v0 offset:4608\n"
      "ds_read_b128 v[24:27], v0 offset:5120\n"
      "ds_read_b128 v[20:23], v0 offset:5632\n"
      "s_add_u32 s56, s48, 12288\n"
      "buffer_store_dwordx2 v[224:225], v2, s[44:47], s56 offen offset:0 nt sc1\n"
      "buffer_store_dwordx2 v[228:229], v2, s[44:47], s56 offen offset:256 nt sc1\n"
      "buffer_store_dwordx2 v[226:227], v2, s[44:47], s56 offen offset:512 nt sc1\n"
      "v_max3_f32 v188, v188, v152, v153\n"
      "v_max3_f32 v188, v188, v154, v155\n"
      "v_mov_b32 v32, v188\n"
      "buffer_store_dwordx2 v[230:231], v2, s[44:47], s56 offen offset:768 nt sc1\n"
      "s_nop 0\n"
      "v_permlane16_swap_b32 v188, v32\n"
      "v_max_f32 v188, v188, v32\n"
      "v_mov_b32 v32, v188\n"
      "s_nop 1\n"
      "v_permlane32_swap_b32 v188, v32\n"
      "v_max_f32 v188, v188, v32\n"
      "v_sub_f32 v140, v140, v188\n"
      "v_sub_f32 v141, v141, v188\n"
      "v_sub_f32 v142, v142, v188\n"
      "s_waitcnt lgkmcnt(3)\n"
      "v_mfma_f32_16x16x32_bf16 v[32:35], a[128:131], v[136:139], 0\n"
      "v_sub_f32 v143, v143, v188\n"
      "v_sub_f32 v88, v88, v188\n"
      "s_waitcnt lgkmcnt(2)\n"
      "v_mfma_f32_16x16x32_bf16 v[32:35], a[132:135], v[148:151], v[32:35]\n"
      "v_exp_f32 v140, v140\n"
      "s_waitcnt lgkmcnt(1)\n"
      "v_mfma_f32_16x16x32_bf16 v[32:35], a[136:139], v[24:27], v[32:35]\n"
      "v_exp_f32 v141, v141\n"
      "s_waitcnt lgkmcnt(0)\n"
      "v_mfma_f32_16x16x32_bf16 v[32:35], a[140:143], v[20:23], v[32:35]\n"
      "v_exp_f32 v142, v142\n"
      "v_mfma_f32_16x16x32_bf16 v[28:31], a[144:147], v[136:139], 0\n"
      "v_exp_f32 v143, v143\n"
      "v_mfma_f32_16x16x32_bf16 v[28:31], a[148:151], v[148:151], v[28:31]\n"
      "v_cvt_pk_bf16_f32 v156, v140, v141\n"
      "v_cvt_pk_bf16_f32 v157, v142, v143\n"
      "v_mfma_f32_16x16x32_bf16 v[28:31], a[152:155], v[24:27], v[28:31]\n"
      "v_sub_f32 v89, v89, v188\n"
      "v_sub_f32 v90, v90, v188\n"
      "v_sub_f32 v91, v91, v188\n"
      "v_sub_f32 v144, v144, v188\n"
      "v_mfma_f32_16x16x32_bf16 v[28:31], a[156:159], v[20:23], v[28:31]\n"
      "v_sub_f32 v145, v145, v188\n"
      "v_sub_f32 v146, v146, v188\n"
      "v_sub_f32 v147, v147, v188\n"
      "v_sub_f32 v236, v236, v188\n"
      "v_sub_f32 v237, v237, v188\n"
      "v_sub_f32 v238, v238, v188\n"
      "v_mfma_f32_16x16x32_bf16 v[184:187], a[160:163], v[136:139], 0\n"
      "v_sub_f32 v239, v239, v188\n"
      "v_sub_f32 v232, v232, v188\n"
      "v_sub_f32 v233, v233, v188\n"
      "v_sub_f32 v234, v234, v188\n"
      "v_sub_f32 v235, v235, v188\n"
      "v_add_f32 v86, v140, v141\n"
      "v_add_f32 v87, v142, v143\n"
      "v_mfma_f32_16x16x32_bf16 v[184:187], a[164:167], v[148:151], v[184:187]\n"
      "v_exp_f32 v88, v88\n"
      "v_exp_f32 v89, v89\n"
      "v_exp_f32 v90, v90\n"
      "v_mfma_f32_16x16x32_bf16 v[184:187], a[168:171], v[24:27], v[184:187]\n"
      "v_exp_f32 v91, v91\n"
      "v_cvt_pk_bf16_f32 v140, v88, v89\n"
      "v_cvt_pk_bf16_f32 v141, v90, v91\n"
      "v_exp_f32 v144, v144\n"
      "v_mfma_f32_16x16x32_bf16 v[184:187], a[172:175], v[20:23], v[184:187]\n"
      "v_exp_f32 v145, v145\n"
      "v_exp_f32 v146, v146\n"
      "v_exp_f32 v147, v147\n"
      "v_cvt_pk_bf16_f32 v142, v144, v145\n"
      "v_mfma_f32_16x16x32_bf16 v[208:211], a[176:179], v[136:139], 0\n"
      "v_cvt_pk_bf16_f32 v143, v146, v147\n"
      "v_exp_f32 v236, v236\n"
      "v_exp_f32 v237, v237\n"
      "v_exp_f32 v238, v238\n"
      "v_mfma_f32_16x16x32_bf16 v[208:211], a[180:183], v[148:151], v[208:211]\n"
      "v_exp_f32 v239, v239\n"
      "v_cvt_pk_bf16_f32 v144, v236, v237\n"
      "v_cvt_pk_bf16_f32 v145, v238, v239\n"
      "v_exp_f32 v232, v232\n"
      "v_mfma_f32_16x16x32_bf16 v[208:211], a[184:187], v[24:27], v[208:211]\n"
      "v_exp_f32 v233, v233\n"
      "v_exp_f32 v234, v234\n"
      "v_exp_f32 v235, v235\n"
      "v_cvt_pk_bf16_f32 v146, v232, v233\n"
      "v_mfma_f32_16x16x32_bf16 v[208:211], a[188:191], v[20:23], v[208:211]\n"
      "v_cvt_pk_bf16_f32 v147, v234, v235\n"
      "v_sub_f32 v160, v160, v188\n"
      "v_sub_f32 v161, v161, v188\n"
      "v_sub_f32 v162, v162, v188\n"
      "v_sub_f32 v163, v163, v188\n"
      "v_sub_f32 v152, v152, v188\n"
      "v_mfma_f32_16x16x4_4b_bf16 v[224:239], v[156:157], v[18:19], 0 cbsz:2 abid:0\n"
      "s_add_u32 m0, s53, 4096\n"
      "s_add_u32 s54, s49, 4096\n"
      "buffer_load_dwordx4 v1, s[40:43], s54 offen offset:0 lds\n"
      "v_sub_f32 v153, v153, v188\n"
      "v_sub_f32 v154, v154, v188\n"
      "v_sub_f32 v155, v155, v188\n"
      "v_exp_f32 v160, v160\n"
      "v_exp_f32 v161, v161\n"
      "v_exp_f32 v162, v162\n"
      "v_exp_f32 v163, v163\n"
      "v_cvt_pk_bf16_f32 v188, v160, v161\n"
      "v_cvt_pk_bf16_f32 v189, v162, v163\n"
      "v_exp_f32 v152, v152\n"
      "v_exp_f32 v153, v153\n"
      "v_exp_f32 v154, v154\n"
      "v_smfmac_f32_16x16x64_bf16 v[86:89], v[12:15], v[140:147], v5\n"
      "v_exp_f32 v155, v155\n"
      "v_cvt_pk_bf16_f32 v190, v152, v153\n"
      "v_mfma_f32_16x16x32_bf16 v[224:227], v[140:143], v[52:55], v[224:227]\n"
      "buffer_load_dwordx4 v1, s[40:43], s54 offen offset:2048 lds\n"
      "s_add_u32 m0, s53, 4992\n"
      "v_cvt_pk_bf16_f32 v191, v154, v155\n"
      "v_cvt_pk_bf16_f32 v156, v32, v33\n"
      "v_cvt_pk_bf16_f32 v157, v34, v35\n"
      "v_mfma_f32_16x16x32_bf16 v[224:227], v[144:147], v[56:59], v[224:227]\n"
      "v_cvt_pk_bf16_f32 v158, v28, v29\n"
      "v_cvt_pk_bf16_f32 v159, v30, v31\n"
      "v_cvt_pk_bf16_f32 v32, v184, v185\n"
      "v_cvt_pk_bf16_f32 v33, v186, v187\n"
      "v_mfma_f32_16x16x32_bf16 v[224:227], v[188:191], v[120:123], v[224:227]\n"
      "buffer_load_dwordx4 v1, s[40:43], s54 offen offset:128 lds\n"
      "v_cvt_pk_bf16_f32 v34, v208, v209\n"
      "v_cvt_pk_bf16_f32 v35, v210, v211\n"
      "v_smfmac_f32_16x16x32_bf16 v[86:89], v[12:13], v[188:191], v5\n"
      "v_mfma_f32_16x16x32_bf16 v[228:231], v[140:143], v[128:131], v[228:231]\n"
      "buffer_load_dwordx4 v1, s[40:43], s54 offen offset:2176 lds\n"
      "v_mfma_f32_16x16x32_bf16 v[228:231], v[144:147], v[132:135], v[228:231]\n"
      "v_mfma_f32_16x16x32_bf16 v[228:231], v[188:191], v[68:71], v[228:231]\n"
      "v_mfma_f32_16x16x32_bf16 v[232:235], v[140:143], v[100:103], v[232:235]\n"
      "v_mfma_f32_16x16x32_bf16 v[232:235], v[144:147], v[104:107], v[232:235]\n"
      "v_mfma_f32_16x16x32_bf16 v[232:235], v[188:191], v[36:39], v[232:235]\n"
      "v_mfma_f32_16x16x32_bf16 v[236:239], v[140:143], v[192:195], v[236:239]\n"
      "v_add_f32 v86, v86, v87\n"
      "v_rcp_f32 v87, v86\n"
      "v_mfma_f32_16x16x32_bf16 v[236:239], v[144:147], v[196:199], v[236:239]\n"
      "ds_bpermute_b32 v88, v4, v87\n"
      "v_mfma_f32_16x16x32_bf16 v[236:239], v[188:191], v[240:243], v[236:239]\n"
      "s_waitcnt lgkmcnt(0)\n"
      "v_mul_f32_dpp v224, v88, v224 row_newbcast:0 row_mask:0xf bank_mask:0xf\n"
      "v_mul_f32_dpp v225, v88, v225 row_newbcast:4 row_mask:0xf bank_mask:0xf\n"
      "v_mul_f32_dpp v226, v88, v226 row_newbcast:8 row_mask:0xf bank_mask:0xf\n"
      "v_mul_f32_dpp v227, v88, v227 row_newbcast:12 row_mask:0xf bank_mask:0xf\n"
      "v_mul_f32_dpp v228, v88, v228 row_newbcast:0 row_mask:0xf bank_mask:0xf\n"
      "v_mul_f32_dpp v229, v88, v229 row_newbcast:4 row_mask:0xf bank_mask:0xf\n"
      "v_mfma_f32_16x16x32_bf16 v[224:227], v[116:119], a[192:195], v[224:227]\n"
      "v_mul_f32_dpp v230, v88, v230 row_newbcast:8 row_mask:0xf bank_mask:0xf\n"
      "v_mul_f32_dpp v231, v88, v231 row_newbcast:12 row_mask:0xf bank_mask:0xf\n"
      "v_mfma_f32_16x16x32_bf16 v[224:227], v[124:127], a[196:199], v[224:227]\n"
      "v_mul_f32_dpp v232, v88, v232 row_newbcast:0 row_mask:0xf bank_mask:0xf\n"
      "v_mul_f32_dpp v233, v88, v233 row_newbcast:4 row_mask:0xf bank_mask:0xf\n"
      "v_mul_f32_dpp v234, v88, v234 row_newbcast:8 row_mask:0xf bank_mask:0xf\n"
      "v_mul_f32_dpp v235, v88, v235 row_newbcast:12 row_mask:0xf bank_mask:0xf\n"
      "v_mfma_f32_16x16x32_bf16 v[224:227], v[48:51], a[200:203], v[224:227]\n"
      "v_mul_f32_dpp v236, v88, v236 row_newbcast:0 row_mask:0xf bank_mask:0xf\n"
      "v_mul_f32_dpp v237, v88, v237 row_newbcast:4 row_mask:0xf bank_mask:0xf\n"
      "v_mul_f32_dpp v238, v88, v238 row_newbcast:8 row_mask:0xf bank_mask:0xf\n"
      "v_mfma_f32_16x16x32_bf16 v[224:227], v[252:255], a[204:207], v[224:227]\n"
      "v_mul_f32_dpp v239, v88, v239 row_newbcast:12 row_mask:0xf bank_mask:0xf\n"
      "v_mfma_f32_16x16x32_bf16 v[228:231], v[116:119], a[208:211], v[228:231]\n"
      "v_mfma_f32_16x16x32_bf16 v[228:231], v[124:127], a[212:215], v[228:231]\n"
      "v_mfma_f32_16x16x32_bf16 v[228:231], v[48:51], a[216:219], v[228:231]\n"
      "v_mfma_f32_16x16x32_bf16 v[228:231], v[252:255], a[220:223], v[228:231]\n"
      "v_mfma_f32_16x16x32_bf16 v[232:235], v[116:119], a[224:227], v[232:235]\n"
      "v_mfma_f32_16x16x32_bf16 v[232:235], v[124:127], a[228:231], v[232:235]\n"
      "v_mfma_f32_16x16x32_bf16 v[232:235], v[48:51], a[232:235], v[232:235]\n"
      "v_mfma_f32_16x16x32_bf16 v[232:235], v[252:255], a[236:239], v[232:235]\n"
      "v_mfma_f32_16x16x32_bf16 v[236:239], v[116:119], a[240:243], v[236:239]\n"
      "v_mfma_f32_16x16x32_bf16 v[236:239], v[124:127], a[244:247], v[236:239]\n"
      "v_mfma_f32_16x16x32_bf16 v[236:239], v[48:51], a[248:251], v[236:239]\n"
      "v_mfma_f32_16x16x32_bf16 v[236:239], v[252:255], a[252:255], v[236:239]\n"
      "v_cvt_pk_bf16_f32 v224, v224, v228\n"
      "v_cvt_pk_bf16_f32 v228, v225, v229\n"
      "v_pk_max_i16 v224, v224, 0\n"
      "v_pk_max_i16 v228, v228, 0\n"
      "v_cvt_pk_bf16_f32 v226, v226, v230\n"
      "v_mfma_f32_16x16x32_bf16 v[140:143], v[172:175], v[156:159], 0\n"
      "v_cvt_pk_bf16_f32 v230, v227, v231\n"
      "v_pk_max_i16 v226, v226, 0\n"
      "v_cvt_pk_bf16_f32 v225, v232, v236\n"
      "v_cvt_pk_bf16_f32 v229, v233, v237\n"
      "v_mfma_f32_16x16x32_bf16 v[140:143], v[176:179], v[32:35], v[140:143]\n"
      "v_pk_max_i16 v225, v225, 0\n"
      "v_pk_max_i16 v229, v229, 0\n"
      "v_cvt_pk_bf16_f32 v227, v234, v238\n"
      "v_cvt_pk_bf16_f32 v231, v235, v239\n"
      "v_pk_max_i16 v227, v227, 0\n"
      "v_mfma_f32_16x16x32_bf16 v[88:91], v[108:111], v[156:159], 0\n"
      "v_pk_max_i16 v230, v230, 0\n"
      "v_pk_max_i16 v231, v231, 0\n"
      "v_max_f32 v188, v140, v141\n"
      "v_max3_f32 v188, v188, v142, v143\n"
      "v_mfma_f32_16x16x32_bf16 v[88:91], v[112:115], v[32:35], v[88:91]\n"
      "v_mfma_f32_16x16x32_bf16 v[144:147], v[164:167], v[156:159], 0\n"
      "v_mfma_f32_16x16x32_bf16 v[144:147], v[168:171], v[32:35], v[144:147]\n"
      "v_mfma_f32_16x16x32_bf16 v[236:239], v[40:43], v[156:159], 0\n"
      "v_mfma_f32_16x16x32_bf16 v[236:239], v[44:47], v[32:35], v[236:239]\n"
      "v_mfma_f32_16x16x32_bf16 v[232:235], v[92:95], v[156:159], 0\n"
      "v_mfma_f32_16x16x32_bf16 v[232:235], v[96:99], v[32:35], v[232:235]\n"
      "v_mfma_f32_16x16x32_bf16 v[160:163], v[216:219], v[156:159], 0\n"
      "v_mfma_f32_16x16x32_bf16 v[160:163], v[220:223], v[32:35], v[160:163]\n"
      "v_max3_f32 v188, v188, v88, v89\n"
      "v_max3_f32 v188, v188, v90, v91\n"
      "v_max3_f32 v188, v188, v144, v145\n"
      "v_max3_f32 v188, v188, v146, v147\n"
      "v_max3_f32 v188, v188, v236, v237\n"
      "v_max3_f32 v188, v188, v238, v239\n"
      "v_max3_f32 v188, v188, v232, v233\n"
      "v_mfma_f32_16x16x32_bf16 v[152:155], v[244:247], v[156:159], 0\n"
      "v_max3_f32 v188, v188, v234, v235\n"
      "v_max3_f32 v188, v188, v160, v161\n"
      "v_max3_f32 v188, v188, v162, v163\n"
      "v_mfma_f32_16x16x32_bf16 v[152:155], v[248:251], v[32:35], v[152:155]\n"
      "ds_read_b128 v[124:127], v0 offset:0\n"
      "ds_read_b128 v[48:51], v0 offset:512\n"
      "ds_read_b128 v[116:119], v0 offset:1024\n"
      "ds_read_b128 v[252:255], v0 offset:1536\n"
      "s_add_u32 s56, s48, 8192\n"
      "buffer_store_dwordx2 v[224:225], v2, s[44:47], s56 offen offset:0 nt sc1\n"
      "buffer_store_dwordx2 v[228:229], v2, s[44:47], s56 offen offset:256 nt sc1\n"
      "buffer_store_dwordx2 v[226:227], v2, s[44:47], s56 offen offset:512 nt sc1\n"
      "v_max3_f32 v188, v188, v152, v153\n"
      "v_max3_f32 v188, v188, v154, v155\n"
      "v_mov_b32 v32, v188\n"
      "buffer_store_dwordx2 v[230:231], v2, s[44:47], s56 offen offset:768 nt sc1\n"
      "s_nop 0\n"
      "v_permlane16_swap_b32 v188, v32\n"
      "v_max_f32 v188, v188, v32\n"
      "v_mov_b32 v32, v188\n"
      "s_nop 1\n"
      "v_permlane32_swap_b32 v188, v32\n"
      "v_max_f32 v188, v188, v32\n"
      "v_sub_f32 v140, v140, v188\n"
      "v_sub_f32 v141, v141, v188\n"
      "v_sub_f32 v142, v142, v188\n"
      "s_waitcnt lgkmcnt(3)\n"
      "v_mfma_f32_16x16x32_bf16 v[32:35], a[128:131], v[124:127], 0\n"
      "v_sub_f32 v143, v143, v188\n"
      "v_sub_f32 v88, v88, v188\n"
      "s_waitcnt lgkmcnt(2)\n"
      "v_mfma_f32_16x16x32_bf16 v[32:35], a[132:135], v[48:51], v[32:35]\n"
      "v_exp_f32 v140, v140\n"
      "s_waitcnt lgkmcnt(1)\n"
      "v_mfma_f32_16x16x32_bf16 v[32:35], a[136:139], v[116:119], v[32:35]\n"
      "v_exp_f32 v141, v141\n"
      "s_waitcnt lgkmcnt(0)\n"
      "v_mfma_f32_16x16x32_bf16 v[32:35], a[140:143], v[252:255], v[32:35]\n"
      "v_exp_f32 v142, v142\n"
      "v_mfma_f32_16x16x32_bf16 v[28:31], a[144:147], v[124:127], 0\n"
      "v_exp_f32 v143, v143\n"
      "v_mfma_f32_16x16x32_bf16 v[28:31], a[148:151], v[48:51], v[28:31]\n"
      "v_cvt_pk_bf16_f32 v190, v140, v141\n"
      "v_cvt_pk_bf16_f32 v191, v142, v143\n"
      "v_mfma_f32_16x16x32_bf16 v[28:31], a[152:155], v[116:119], v[28:31]\n"
      "v_sub_f32 v89, v89, v188\n"
      "v_sub_f32 v90, v90, v188\n"
      "v_sub_f32 v91, v91, v188\n"
      "v_sub_f32 v144, v144, v188\n"
      "v_mfma_f32_16x16x32_bf16 v[28:31], a[156:159], v[252:255], v[28:31]\n"
      "v_sub_f32 v145, v145, v188\n"
      "v_sub_f32 v146, v146, v188\n"
      "v_sub_f32 v147, v147, v188\n"
      "v_sub_f32 v236, v236, v188\n"
      "v_sub_f32 v237, v237, v188\n"
      "v_sub_f32 v238, v238, v188\n"
      "v_mfma_f32_16x16x32_bf16 v[184:187], a[160:163], v[124:127], 0\n"
      "v_sub_f32 v239, v239, v188\n"
      "v_sub_f32 v232, v232, v188\n"
      "v_sub_f32 v233, v233, v188\n"
      "v_sub_f32 v234, v234, v188\n"
      "v_sub_f32 v235, v235, v188\n"
      "v_add_f32 v86, v140, v141\n"
      "v_add_f32 v87, v142, v143\n"
      "v_mfma_f32_16x16x32_bf16 v[184:187], a[164:167], v[48:51], v[184:187]\n"
      "v_exp_f32 v88, v88\n"
      "v_exp_f32 v89, v89\n"
      "v_exp_f32 v90, v90\n"
      "v_mfma_f32_16x16x32_bf16 v[184:187], a[168:171], v[116:119], v[184:187]\n"
      "v_exp_f32 v91, v91\n"
      "v_cvt_pk_bf16_f32 v140, v88, v89\n"
      "v_cvt_pk_bf16_f32 v141, v90, v91\n"
      "v_exp_f32 v144, v144\n"
      "v_mfma_f32_16x16x32_bf16 v[184:187], a[172:175], v[252:255], v[184:187]\n"
      "v_exp_f32 v145, v145\n"
      "v_exp_f32 v146, v146\n"
      "v_exp_f32 v147, v147\n"
      "v_cvt_pk_bf16_f32 v142, v144, v145\n"
      "v_mfma_f32_16x16x32_bf16 v[208:211], a[176:179], v[124:127], 0\n"
      "v_cvt_pk_bf16_f32 v143, v146, v147\n"
      "v_exp_f32 v236, v236\n"
      "v_exp_f32 v237, v237\n"
      "v_exp_f32 v238, v238\n"
      "v_mfma_f32_16x16x32_bf16 v[208:211], a[180:183], v[48:51], v[208:211]\n"
      "v_exp_f32 v239, v239\n"
      "v_cvt_pk_bf16_f32 v144, v236, v237\n"
      "v_cvt_pk_bf16_f32 v145, v238, v239\n"
      "v_exp_f32 v232, v232\n"
      "v_mfma_f32_16x16x32_bf16 v[208:211], a[184:187], v[116:119], v[208:211]\n"
      "v_exp_f32 v233, v233\n"
      "v_exp_f32 v234, v234\n"
      "v_exp_f32 v235, v235\n"
      "v_cvt_pk_bf16_f32 v146, v232, v233\n"
      "v_mfma_f32_16x16x32_bf16 v[208:211], a[188:191], v[252:255], v[208:211]\n"
      "ds_read_b128 v[156:159], v10 offset:0\n"
      "v_cvt_pk_bf16_f32 v147, v234, v235\n"
      "v_sub_f32 v160, v160, v188\n"
      "v_sub_f32 v161, v161, v188\n"
      "v_sub_f32 v162, v162, v188\n"
      "v_sub_f32 v163, v163, v188\n"
      "v_sub_f32 v152, v152, v188\n"
      "v_mfma_f32_16x16x4_4b_bf16 v[224:239], v[190:191], v[18:19], 0 cbsz:2 abid:0\n"
      "s_add_u32 m0, s53, 0\n"
      "s_add_u32 s54, s49, 0\n"
      "buffer_load_dwordx4 v1, s[40:43], s54 offen offset:0 lds\n"
      "v_sub_f32 v153, v153, v188\n"
      "v_sub_f32 v154, v154, v188\n"
      "v_sub_f32 v155, v155, v188\n"
      "v_exp_f32 v160, v160\n"
      "v_exp_f32 v161, v161\n"
      "v_exp_f32 v162, v162\n"
      "v_exp_f32 v163, v163\n"
      "v_cvt_pk_bf16_f32 v188, v160, v161\n"
      "v_cvt_pk_bf16_f32 v189, v162, v163\n"
      "v_exp_f32 v152, v152\n"
      "v_exp_f32 v153, v153\n"
      "v_exp_f32 v154, v154\n"
      "v_smfmac_f32_16x16x64_bf16 v[86:89], v[12:15], v[140:147], v5\n"
      "v_exp_f32 v155, v155\n"
      "v_cvt_pk_bf16_f32 v190, v152, v153\n"
      "v_mfma_f32_16x16x32_bf16 v[224:227], v[140:143], v[52:55], v[224:227]\n"
      "buffer_load_dwordx4 v1, s[40:43], s54 offen offset:2048 lds\n"
      "s_add_u32 m0, s53, 896\n"
      "v_cvt_pk_bf16_f32 v191, v154, v155\n"
      "v_cvt_pk_bf16_f32 v152, v32, v33\n"
      "v_cvt_pk_bf16_f32 v153, v34, v35\n"
      "v_cvt_pk_bf16_f32 v154, v28, v29\n"
      "v_mfma_f32_16x16x32_bf16 v[224:227], v[144:147], v[56:59], v[224:227]\n"
      "v_cvt_pk_bf16_f32 v155, v30, v31\n"
      "v_cvt_pk_bf16_f32 v32, v184, v185\n"
      "v_cvt_pk_bf16_f32 v33, v186, v187\n"
      "v_cvt_pk_bf16_f32 v34, v208, v209\n"
      "v_mfma_f32_16x16x32_bf16 v[224:227], v[188:191], v[120:123], v[224:227]\n"
      "buffer_load_dwordx4 v1, s[40:43], s54 offen offset:128 lds\n"
      "v_cvt_pk_bf16_f32 v35, v210, v211\n"
      "v_smfmac_f32_16x16x32_bf16 v[86:89], v[12:13], v[188:191], v5\n"
      "v_mfma_f32_16x16x32_bf16 v[228:231], v[140:143], v[128:131], v[228:231]\n"
      "buffer_load_dwordx4 v1, s[40:43], s54 offen offset:2176 lds\n"
      "v_mfma_f32_16x16x32_bf16 v[228:231], v[144:147], v[132:135], v[228:231]\n"
      "v_mfma_f32_16x16x32_bf16 v[228:231], v[188:191], v[68:71], v[228:231]\n"
      "v_add_u32 v10, 64, v10\n"
      "v_mfma_f32_16x16x32_bf16 v[232:235], v[140:143], v[100:103], v[232:235]\n"
      "v_mfma_f32_16x16x32_bf16 v[232:235], v[144:147], v[104:107], v[232:235]\n"
      "v_mfma_f32_16x16x32_bf16 v[232:235], v[188:191], v[36:39], v[232:235]\n"
      "v_add_f32 v86, v86, v87\n"
      "v_rcp_f32 v87, v86\n"
      "v_mfma_f32_16x16x32_bf16 v[236:239], v[140:143], v[192:195], v[236:239]\n"
      "ds_bpermute_b32 v88, v4, v87\n"
      "v_mfma_f32_16x16x32_bf16 v[236:239], v[144:147], v[196:199], v[236:239]\n"
      "v_mfma_f32_16x16x32_bf16 v[236:239], v[188:191], v[240:243], v[236:239]\n"
      "s_waitcnt lgkmcnt(0)\n"
      "v_mul_f32_dpp v224, v88, v224 row_newbcast:0 row_mask:0xf bank_mask:0xf\n"
      "v_mul_f32_dpp v225, v88, v225 row_newbcast:4 row_mask:0xf bank_mask:0xf\n"
      "v_mul_f32_dpp v226, v88, v226 row_newbcast:8 row_mask:0xf bank_mask:0xf\n"
      "v_mul_f32_dpp v227, v88, v227 row_newbcast:12 row_mask:0xf bank_mask:0xf\n"
      "v_mul_f32_dpp v228, v88, v228 row_newbcast:0 row_mask:0xf bank_mask:0xf\n"
      "v_mul_f32_dpp v229, v88, v229 row_newbcast:4 row_mask:0xf bank_mask:0xf\n"
      "v_mfma_f32_16x16x32_bf16 v[224:227], v[136:139], a[192:195], v[224:227]\n"
      "v_mul_f32_dpp v230, v88, v230 row_newbcast:8 row_mask:0xf bank_mask:0xf\n"
      "v_mul_f32_dpp v231, v88, v231 row_newbcast:12 row_mask:0xf bank_mask:0xf\n"
      "v_mul_f32_dpp v232, v88, v232 row_newbcast:0 row_mask:0xf bank_mask:0xf\n"
      "v_mfma_f32_16x16x32_bf16 v[224:227], v[148:151], a[196:199], v[224:227]\n"
      "v_mul_f32_dpp v233, v88, v233 row_newbcast:4 row_mask:0xf bank_mask:0xf\n"
      "v_mul_f32_dpp v234, v88, v234 row_newbcast:8 row_mask:0xf bank_mask:0xf\n"
      "v_mul_f32_dpp v235, v88, v235 row_newbcast:12 row_mask:0xf bank_mask:0xf\n"
      "v_mul_f32_dpp v236, v88, v236 row_newbcast:0 row_mask:0xf bank_mask:0xf\n"
      "v_mfma_f32_16x16x32_bf16 v[224:227], v[24:27], a[200:203], v[224:227]\n"
      "v_mul_f32_dpp v237, v88, v237 row_newbcast:4 row_mask:0xf bank_mask:0xf\n"
      "v_mul_f32_dpp v238, v88, v238 row_newbcast:8 row_mask:0xf bank_mask:0xf\n"
      "v_mul_f32_dpp v239, v88, v239 row_newbcast:12 row_mask:0xf bank_mask:0xf\n"
      "v_mfma_f32_16x16x32_bf16 v[224:227], v[20:23], a[204:207], v[224:227]\n"
      "v_mfma_f32_16x16x32_bf16 v[228:231], v[136:139], a[208:211], v[228:231]\n"
      "v_mfma_f32_16x16x32_bf16 v[228:231], v[148:151], a[212:215], v[228:231]\n"
      "v_mfma_f32_16x16x32_bf16 v[228:231], v[24:27], a[216:219], v[228:231]\n"
      "v_mfma_f32_16x16x32_bf16 v[228:231], v[20:23], a[220:223], v[228:231]\n"
      "v_mfma_f32_16x16x32_bf16 v[232:235], v[136:139], a[224:227], v[232:235]\n"
      "v_mfma_f32_16x16x32_bf16 v[232:235], v[148:151], a[228:231], v[232:235]\n"
      "v_mfma_f32_16x16x32_bf16 v[232:235], v[24:27], a[232:235], v[232:235]\n"
      "v_mfma_f32_16x16x32_bf16 v[232:235], v[20:23], a[236:239], v[232:235]\n"
      "v_mfma_f32_16x16x32_bf16 v[236:239], v[136:139], a[240:243], v[236:239]\n"
      "v_mfma_f32_16x16x32_bf16 v[236:239], v[148:151], a[244:247], v[236:239]\n"
      "v_mfma_f32_16x16x32_bf16 v[236:239], v[24:27], a[248:251], v[236:239]\n"
      "v_mfma_f32_16x16x32_bf16 v[236:239], v[20:23], a[252:255], v[236:239]\n"
      "v_cvt_pk_bf16_f32 v224, v224, v228\n"
      "v_cvt_pk_bf16_f32 v228, v225, v229\n"
      "v_pk_max_i16 v224, v224, 0\n"
      "v_pk_max_i16 v228, v228, 0\n"
      "v_cvt_pk_bf16_f32 v226, v226, v230\n"
      "v_cvt_pk_bf16_f32 v230, v227, v231\n"
      "v_pk_max_i16 v226, v226, 0\n"
      "v_mfma_f32_16x16x32_bf16 v[148:151], v[172:175], v[152:155], 0\n"
      "v_cvt_pk_bf16_f32 v225, v232, v236\n"
      "v_cvt_pk_bf16_f32 v229, v233, v237\n"
      "v_pk_max_i16 v225, v225, 0\n"
      "v_pk_max_i16 v229, v229, 0\n"
      "v_cvt_pk_bf16_f32 v227, v234, v238\n"
      "v_cvt_pk_bf16_f32 v231, v235, v239\n"
      "v_pk_max_i16 v227, v227, 0\n"
      "v_mfma_f32_16x16x32_bf16 v[148:151], v[176:179], v[32:35], v[148:151]\n"
      "v_pk_max_i16 v230, v230, 0\n"
      "v_pk_max_i16 v231, v231, 0\n"
      "v_mfma_f32_16x16x32_bf16 v[136:139], v[108:111], v[152:155], 0\n"
      "v_mfma_f32_16x16x32_bf16 v[136:139], v[112:115], v[32:35], v[136:139]\n"
      "v_mfma_f32_16x16x32_bf16 v[236:239], v[164:167], v[152:155], 0\n"
      "v_mfma_f32_16x16x32_bf16 v[236:239], v[168:171], v[32:35], v[236:239]\n"
      "v_mfma_f32_16x16x32_bf16 v[160:163], v[40:43], v[152:155], 0\n"
      "v_mfma_f32_16x16x32_bf16 v[160:163], v[44:47], v[32:35], v[160:163]\n"
      "v_max_f32 v208, v148, v149\n"
      "v_max3_f32 v208, v208, v150, v151\n"
      "v_mfma_f32_16x16x32_bf16 v[28:31], v[92:95], v[152:155], 0\n"
      "v_mfma_f32_16x16x32_bf16 v[28:31], v[96:99], v[32:35], v[28:31]\n"
      "v_max3_f32 v208, v208, v136, v137\n"
      "v_max3_f32 v208, v208, v138, v139\n"
      "v_max3_f32 v208, v208, v236, v237\n"
      "v_max3_f32 v208, v208, v238, v239\n"
      "v_max3_f32 v208, v208, v160, v161\n"
      "v_max3_f32 v208, v208, v162, v163\n"
      "v_mfma_f32_16x16x32_bf16 v[24:27], v[216:219], v[152:155], 0\n"
      "v_mfma_f32_16x16x32_bf16 v[24:27], v[220:223], v[32:35], v[24:27]\n"
      "v_max3_f32 v208, v208, v28, v29\n"
      "v_max3_f32 v208, v208, v30, v31\n"
      "v_mfma_f32_16x16x32_bf16 v[20:23], v[244:247], v[152:155], 0\n"
      "v_mfma_f32_16x16x32_bf16 v[20:23], v[248:251], v[32:35], v[20:23]\n"
      "ds_read_b64 v[152:153], v11 offset:4096\n"
      "s_nop 2\n"
      "v_max3_f32 v208, v208, v24, v25\n"
      "v_max3_f32 v208, v208, v26, v27\n"
      "s_nop 1\n"
      "v_max3_f32 v208, v208, v20, v21\n"
      "v_max3_f32 v208, v208, v22, v23\n"
      "v_mov_b32 v32, v208\n"
      "s_nop 1\n"
      "v_permlane16_swap_b32 v208, v32\n"
      "v_max_f32 v208, v208, v32\n"
      "v_mov_b32 v32, v208\n"
      "s_nop 1\n"
      "v_permlane32_swap_b32 v208, v32\n"
      "v_max_f32 v208, v208, v32\n"
      "v_sub_f32 v148, v148, v208\n"
      "v_sub_f32 v149, v149, v208\n"
      "v_sub_f32 v150, v150, v208\n"
      "v_sub_f32 v151, v151, v208\n"
      "v_exp_f32 v148, v148\n"
      "v_exp_f32 v149, v149\n"
      "v_exp_f32 v150, v150\n"
      "v_exp_f32 v151, v151\n"
      "v_cvt_pk_bf16_f32 v234, v148, v149\n"
      "v_cvt_pk_bf16_f32 v235, v150, v151\n"
      "v_sub_f32 v136, v136, v208\n"
      "v_sub_f32 v137, v137, v208\n"
      "v_sub_f32 v138, v138, v208\n"
      "v_sub_f32 v139, v139, v208\n"
      "v_sub_f32 v236, v236, v208\n"
      "v_sub_f32 v237, v237, v208\n"
      "v_sub_f32 v238, v238, v208\n"
      "v_sub_f32 v239, v239, v208\n"
      "v_sub_f32 v160, v160, v208\n"
      "v_sub_f32 v161, v161, v208\n"
      "v_sub_f32 v162, v162, v208\n"
      "v_sub_f32 v163, v163, v208\n"
      "v_sub_f32 v28, v28, v208\n"
      "v_sub_f32 v29, v29, v208\n"
      "v_sub_f32 v30, v30, v208\n"
      "v_sub_f32 v31, v31, v208\n"
      "v_add_f32 v32, v148, v149\n"
      "v_add_f32 v33, v150, v151\n"
      "v_exp_f32 v136, v136\n"
      "v_exp_f32 v137, v137\n"
      "v_exp_f32 v138, v138\n"
      "v_exp_f32 v139, v139\n"
      "v_cvt_pk_bf16_f32 v232, v136, v137\n"
      "v_cvt_pk_bf16_f32 v233, v138, v139\n"
      "v_exp_f32 v236, v236\n"
      "v_mfma_f32_16x16x4_4b_bf16 v[136:151], v[234:235], v[18:19], 0 cbsz:2 abid:0\n"
      "v_exp_f32 v237, v237\n"
      "v_exp_f32 v238, v238\n"
      "v_exp_f32 v239, v239\n"
      "v_cvt_pk_bf16_f32 v234, v236, v237\n"
      "v_cvt_pk_bf16_f32 v235, v238, v239\n"
      "v_exp_f32 v160, v160\n"
      "v_exp_f32 v161, v161\n"
      "v_exp_f32 v162, v162\n"
      "v_exp_f32 v163, v163\n"
      "v_cvt_pk_bf16_f32 v236, v160, v161\n"
      "v_cvt_pk_bf16_f32 v237, v162, v163\n"
      "v_exp_f32 v28, v28\n"
      "v_exp_f32 v29, v29\n"
      "v_exp_f32 v30, v30\n"
      "v_exp_f32 v31, v31\n"
      "v_cvt_pk_bf16_f32 v238, v28, v29\n"
      "v_cvt_pk_bf16_f32 v239, v30, v31\n"
      "v_sub_f32 v24, v24, v208\n"
      "v_sub_f32 v25, v25, v208\n"
      "v_sub_f32 v26, v26, v208\n"
      "v_sub_f32 v27, v27, v208\n"
      "v_sub_f32 v20, v20, v208\n"
      "v_sub_f32 v21, v21, v208\n"
      "v_sub_f32 v22, v22, v208\n"
      "v_sub_f32 v23, v23, v208\n"
      "v_exp_f32 v24, v24\n"
      "v_exp_f32 v25, v25\n"
      "v_exp_f32 v26, v26\n"
      "v_exp_f32 v27, v27\n"
      "v_cvt_pk_bf16_f32 v208, v24, v25\n"
      "v_cvt_pk_bf16_f32 v209, v26, v27\n"
      "v_exp_f32 v20, v20\n"
      "v_exp_f32 v21, v21\n"
      "v_exp_f32 v22, v22\n"
      "v_exp_f32 v23, v23\n"
      "v_cvt_pk_bf16_f32 v210, v20, v21\n"
      "v_cvt_pk_bf16_f32 v211, v22, v23\n"
      "v_smfmac_f32_16x16x64_bf16 v[32:35], v[12:15], v[232:239], v5\n"
      "s_add_u32 s56, s48, 4096\n"
      "buffer_store_dwordx2 v[224:225], v2, s[44:47], s56 offen offset:0 nt sc1\n"
      "v_mfma_f32_16x16x32_bf16 v[136:139], v[232:235], v[52:55], v[136:139]\n"
      "buffer_store_dwordx2 v[228:229], v2, s[44:47], s56 offen offset:256 nt sc1\n"
      "v_mfma_f32_16x16x32_bf16 v[136:139], v[236:239], v[56:59], v[136:139]\n"
      "buffer_store_dwordx2 v[226:227], v2, s[44:47], s56 offen offset:512 nt sc1\n"
      "v_mfma_f32_16x16x32_bf16 v[136:139], v[208:211], v[120:123], v[136:139]\n"
      "buffer_store_dwordx2 v[230:231], v2, s[44:47], s56 offen offset:768 nt sc1\n"
      "v_smfmac_f32_16x16x32_bf16 v[32:35], v[12:13], v[208:211], v5\n"
      "s_waitcnt lgkmcnt(0)\n"
      "v_lshlrev_b32 v228, 16, v152\n"
      "v_and_b32 v229, 0xffff0000, v152\n"
      "v_mfma_f32_16x16x32_bf16 v[140:143], v[232:235], v[128:131], v[140:143]\n"
      "v_mov_b64 v[224:225], 0\n"
      "v_lshlrev_b32 v230, 16, v153\n"
      "v_and_b32 v231, 0xffff0000, v153\n"
      "v_mfma_f32_16x16x32_bf16 v[140:143], v[236:239], v[132:135], v[140:143]\n"
      "v_mov_b64 v[20:21], 0\n"
      "v_add_f32 v32, v32, v33\n"
      "v_rcp_f32 v33, v32\n"
      "v_mfma_f32_16x16x32_bf16 v[140:143], v[208:211], v[68:71], v[140:143]\n"
      "ds_bpermute_b32 v34, v4, v33\n"
      "v_mov_b64 v[28:29], 0\n"
      "v_mfma_f32_16x16x32_bf16 v[144:147], v[232:235], v[100:103], v[144:147]\n"
      "v_mov_b64 v[160:161], 0\n"
      "v_mfma_f32_16x16x32_bf16 v[144:147], v[236:239], v[104:107], v[144:147]\n"
      "v_mov_b64 v[24:25], 0\n"
      "v_mfma_f32_16x16x32_bf16 v[144:147], v[208:211], v[36:39], v[144:147]\n"
      "v_mov_b64 v[32:33], 0\n"
      "v_mfma_f32_16x16x32_bf16 v[148:151], v[232:235], v[192:195], v[148:151]\n"
      "v_mov_b64 v[232:233], 0\n"
      "v_mfma_f32_16x16x32_bf16 v[148:151], v[236:239], v[196:199], v[148:151]\n"
      "v_mfma_f32_16x16x32_bf16 v[148:151], v[208:211], v[240:243], v[148:151]\n"
      "s_waitcnt lgkmcnt(0)\n"
      "v_mul_f32_dpp v136, v34, v136 row_newbcast:0 row_mask:0xf bank_mask:0xf\n"
      "v_mul_f32_dpp v137, v34, v137 row_newbcast:4 row_mask:0xf bank_mask:0xf\n"
      "v_mul_f32_dpp v138, v34, v138 row_newbcast:8 row_mask:0xf bank_mask:0xf\n"
      "v_mul_f32_dpp v139, v34, v139 row_newbcast:12 row_mask:0xf bank_mask:0xf\n"
      "v_mul_f32_dpp v140, v34, v140 row_newbcast:0 row_mask:0xf bank_mask:0xf\n"
      "v_mul_f32_dpp v141, v34, v141 row_newbcast:4 row_mask:0xf bank_mask:0xf\n"
      "v_mfma_f32_16x16x32_bf16 v[136:139], v[124:127], a[192:195], v[136:139]\n"
      "v_mul_f32_dpp v142, v34, v142 row_newbcast:8 row_mask:0xf bank_mask:0xf\n"
      "v_mul_f32_dpp v143, v34, v143 row_newbcast:12 row_mask:0xf bank_mask:0xf\n"
      "v_mul_f32_dpp v144, v34, v144 row_newbcast:0 row_mask:0xf bank_mask:0xf\n"
      "v_mfma_f32_16x16x32_bf16 v[136:139], v[48:51], a[196:199], v[136:139]\n"
      "v_mul_f32_dpp v145, v34, v145 row_newbcast:4 row_mask:0xf bank_mask:0xf\n"
      "v_mul_f32_dpp v146, v34, v146 row_newbcast:8 row_mask:0xf bank_mask:0xf\n"
      "v_mul_f32_dpp v147, v34, v147 row_newbcast:12 row_mask:0xf bank_mask:0xf\n"
      "v_mul_f32_dpp v148, v34, v148 row_newbcast:0 row_mask:0xf bank_mask:0xf\n"
      "v_mfma_f32_16x16x32_bf16 v[136:139], v[116:119], a[200:203], v[136:139]\n"
      "v_mul_f32_dpp v149, v34, v149 row_newbcast:4 row_mask:0xf bank_mask:0xf\n"
      "v_mul_f32_dpp v150, v34, v150 row_newbcast:8 row_mask:0xf bank_mask:0xf\n"
      "v_mul_f32_dpp v151, v34, v151 row_newbcast:12 row_mask:0xf bank_mask:0xf\n"
      "v_add_u32 v11, 0x200, v11\n"
      "v_mfma_f32_16x16x32_bf16 v[136:139], v[252:255], a[204:207], v[136:139]\n"
      "v_mfma_f32_16x16x32_bf16 v[140:143], v[124:127], a[208:211], v[140:143]\n"
      "v_mfma_f32_16x16x32_bf16 v[140:143], v[48:51], a[212:215], v[140:143]\n"
      "v_mfma_f32_16x16x32_bf16 v[140:143], v[116:119], a[216:219], v[140:143]\n"
      "v_mfma_f32_16x16x32_bf16 v[140:143], v[252:255], a[220:223], v[140:143]\n"
      "s_add_u32 s56, s48, 0\n"
      "v_mfma_f32_16x16x32_bf16 v[144:147], v[124:127], a[224:227], v[144:147]\n"
      "v_mfma_f32_16x16x32_bf16 v[144:147], v[48:51], a[228:231], v[144:147]\n"
      "v_mfma_f32_16x16x32_bf16 v[144:147], v[116:119], a[232:235], v[144:147]\n"
      "v_mfma_f32_16x16x32_bf16 v[144:147], v[252:255], a[236:239], v[144:147]\n"
      "v_mfma_f32_16x16x32_bf16 v[148:151], v[124:127], a[240:243], v[148:151]\n"
      "v_mfma_f32_16x16x32_bf16 v[148:151], v[48:51], a[244:247], v[148:151]\n"
      "v_mfma_f32_16x16x32_bf16 v[148:151], v[116:119], a[248:251], v[148:151]\n"
      "v_cvt_pk_bf16_f32 v136, v136, v140\n"
      "v_cvt_pk_bf16_f32 v140, v137, v141\n"
      "v_pk_max_i16 v136, v136, 0\n"
      "v_pk_max_i16 v140, v140, 0\n"
      "v_cvt_pk_bf16_f32 v138, v138, v142\n"
      "v_cvt_pk_bf16_f32 v142, v139, v143\n"
      "v_pk_max_i16 v138, v138, 0\n"
      "v_mfma_f32_16x16x32_bf16 v[148:151], v[252:255], a[252:255], v[148:151]\n"
      "v_pk_max_i16 v142, v142, 0\n"
      "v_smfmac_f32_16x16x64_bf16 v[224:227], v[156:159], v[108:115], v5\n"
      "v_smfmac_f32_16x16x64_bf16 v[20:23], v[156:159], v[164:171], v5\n"
      "v_smfmac_f32_16x16x64_bf16 v[28:31], v[156:159], v[40:47], v5\n"
      "v_smfmac_f32_16x16x64_bf16 v[160:163], v[156:159], v[92:99], v5\n"
      "v_smfmac_f32_16x16x64_bf16 v[24:27], v[156:159], v[216:223], v5\n"
      "v_smfmac_f32_16x16x64_bf16 v[32:35], v[156:159], v[244:251], v5\n"
      "v_smfmac_f32_16x16x64_bf16 v[232:235], v[156:159], v[172:179], v5\n"
      "v_cvt_pk_bf16_f32 v137, v144, v148\n"
      "v_cvt_pk_bf16_f32 v141, v145, v149\n"
      "v_add_f32 v224, v224, v225\n"
      "v_add_f32 v20, v20, v21\n"
      "v_add_f32 v28, v28, v29\n"
      "v_add_f32 v160, v160, v161\n"
      "v_add_f32 v24, v24, v25\n"
      "v_add_f32 v32, v32, v33\n"
      "v_add_f32 v232, v232, v233\n"
      "v_max3_f32 v148, v224, v20, v28\n"
      "v_max3_f32 v148, v148, v160, v24\n"
      "v_max3_f32 v148, v148, v32, v232\n"
      "v_mov_b32 v233, 0\n"
      "v_pk_max_i16 v137, v137, 0\n"
      "v_max_f32_dpp v148, v148, v148 row_ror:8 row_mask:0xf bank_mask:0xf\n"
      "v_pk_max_i16 v141, v141, 0\n"
      "v_cvt_pk_bf16_f32 v139, v146, v150\n"
      "v_max_f32_dpp v148, v148, v148 row_ror:4 row_mask:0xf bank_mask:0xf\n"
      "v_cvt_pk_bf16_f32 v143, v147, v151\n"
      "v_pk_max_i16 v139, v139, 0\n"
      "v_max_f32_dpp v148, v148, v148 row_ror:2 row_mask:0xf bank_mask:0xf\n"
      "v_pk_max_i16 v143, v143, 0\n"
      "buffer_store_dwordx2 v[136:137], v2, s[44:47], s56 offen offset:0 nt sc1\n"
      "v_max_f32_dpp v148, v148, v148 row_ror:1 row_mask:0xf bank_mask:0xf\n"
      "v_sub_f32 v232, v232, v148\n"
      "v_sub_f32 v224, v224, v148\n"
      "v_sub_f32 v20, v20, v148\n"
      "v_exp_f32_dpp v233, v232 quad_perm:[0,1,2,3] row_mask:0xf bank_mask:0x1\n"
      "v_sub_f32 v28, v28, v148\n"
      "v_sub_f32 v160, v160, v148\n"
      "v_mov_b32_dpp v232, v233 quad_perm:[1,1,3,3] row_mask:0xf bank_mask:0xf\n"
      "v_cvt_pk_bf16_f32 v232, v233, v232\n"
      "ds_bpermute_b32 v136, v7, v232 offset:248\n"
      "ds_bpermute_b32 v137, v7, v232 offset:8\n"
      "v_sub_f32 v24, v24, v148\n"
      "v_sub_f32 v32, v32, v148\n"
      "v_exp_f32 v224, v224\n"
      "v_exp_f32 v20, v20\n"
      "v_exp_f32 v28, v28\n"
      "v_exp_f32 v160, v160\n"
      "v_exp_f32 v24, v24\n"
      "v_exp_f32 v32, v32\n"
      "v_cndmask_b32_dpp v225, v28, v224, vcc quad_perm:[0,0,2,2] row_mask:0xf bank_mask:0xf\n"
      "v_cndmask_b32_dpp v21, v160, v20, vcc quad_perm:[0,0,2,2] row_mask:0xf bank_mask:0xf\n"
      "v_cndmask_b32_dpp v25, v32, v24, vcc quad_perm:[0,0,2,2] row_mask:0xf bank_mask:0xf\n"
      "s_not_b64 vcc, vcc\n"
      "v_cndmask_b32_dpp v29, v224, v28, vcc quad_perm:[1,1,3,3] row_mask:0xf bank_mask:0xf\n"
      "v_cndmask_b32_dpp v161, v20, v160, vcc quad_perm:[1,1,3,3] row_mask:0xf bank_mask:0xf\n"
      "v_cvt_pk_bf16_f32 v225, v225, v29\n"
      "v_cvt_pk_bf16_f32 v21, v21, v161\n"
      "ds_bpermute_b32 v148, v6, v225\n"
      "ds_bpermute_b32 v149, v6, v21\n"
      "ds_bpermute_b32 v150, v6, v225 offset:16\n"
      "ds_bpermute_b32 v151, v6, v21 offset:16\n"
      "v_cndmask_b32_dpp v33, v24, v32, vcc quad_perm:[1,1,3,3] row_mask:0xf bank_mask:0xf\n"
      "v_cvt_pk_bf16_f32 v25, v25, v33\n"
      "ds_bpermute_b32 v146, v7, v25\n"
      "ds_bpermute_b32 v147, v7, v25 offset:4\n"
      "buffer_store_dwordx2 v[140:141], v2, s[44:47], s56 offen offset:256 nt sc1\n"
      "buffer_store_dwordx2 v[138:139], v2, s[44:47], s56 offen offset:512 nt sc1\n"
      "s_waitcnt lgkmcnt(6)\n"
      "v_mfma_f32_16x16x4_4b_bf16 v[164:179], v[136:137], v[18:19], 0 cbsz:2 abid:0\n"
      "buffer_store_dwordx2 v[142:143], v2, s[44:47], s56 offen offset:768 nt sc1\n"
      "s_not_b64 vcc, vcc\n"
      "v_add_f32 v224, v224, v20\n"
      "v_add_f32 v28, v28, v160\n"
      "v_add_f32 v24, v24, v32\n"
      "v_add_f32 v224, v224, v28\n"
      "v_add_f32 v24, v24, v233\n"
      "v_add_f32 v142, v224, v24\n"
      "s_add_u32 s56, s48, 24576\n"
      "s_nop 0\n"
      "v_add_f32_dpp v142, v142, v142 row_ror:8 row_mask:0xf bank_mask:0xf\n"
      "s_nop 1\n"
      "v_add_f32_dpp v142, v142, v142 row_ror:4 row_mask:0xf bank_mask:0xf\n"
      "s_nop 1\n"
      "v_add_f32_dpp v142, v142, v142 row_ror:2 row_mask:0xf bank_mask:0xf\n"
      "s_waitcnt lgkmcnt(2)\n"
      "v_smfmac_f32_16x16x64_bf16 v[164:167], v[148:151], v[52:59], v5\n"
      "v_smfmac_f32_16x16x64_bf16 v[168:171], v[148:151], v[128:135], v5\n"
      "v_add_f32_dpp v142, v142, v142 row_ror:1 row_mask:0xf bank_mask:0xf\n"
      "v_smfmac_f32_16x16x64_bf16 v[172:175], v[148:151], v[100:107], v5\n"
      "v_smfmac_f32_16x16x64_bf16 v[176:179], v[148:151], v[192:199], v5\n"
      "v_rcp_f32 v143, v142\n"
      "s_waitcnt lgkmcnt(0)\n"
      "v_smfmac_f32_16x16x32_bf16 v[164:167], v[146:147], v[120:123], v5\n"
      "v_smfmac_f32_16x16x32_bf16 v[168:171], v[146:147], v[68:71], v5\n"
      "s_nop 0\n"
      "v_smfmac_f32_16x16x32_bf16 v[172:175], v[146:147], v[36:39], v5\n"
      "v_smfmac_f32_16x16x32_bf16 v[176:179], v[146:147], v[240:243], v5\n"
      "s_nop 3\n"
      "v_add_f32 v164, v164, v165\n"
      "v_fma_f32 v164, v164, v143, v228\n"
      "v_add_f32 v168, v168, v169\n"
      "v_fma_f32 v168, v168, v143, v229\n"
      "v_cvt_pk_bf16_f32 v164, v164, v168\n"
      "v_pk_max_i16 v164, v164, 0\n"
      "v_add_f32 v172, v172, v173\n"
      "v_fma_f32 v172, v172, v143, v230\n"
      "v_add_f32 v176, v176, v177\n"
      "v_fma_f32 v176, v176, v143, v231\n"
      "v_cvt_pk_bf16_f32 v165, v172, v176\n"
      "v_pk_max_i16 v165, v165, 0\n"
      "buffer_store_dwordx2 v[164:165], v3, s[44:47], s56 offen offset:0 nt sc1\n"
      "s_mov_b32 s48, s49\n"
      "s_add_u32 s49, s49, 102400\n"
      "s_sub_u32 s50, s50, 1\n"
      "s_cmp_eq_u32 s50, 1\n"
      "s_cselect_b32 s49, 0x80000000, s49\n"
      "s_cmp_lg_u32 s50, 0\n"
      "s_cbranch_scc1 L_safe_%=\n"
      "s_waitcnt vmcnt(0) lgkmcnt(0)\n"
      "L_done_%=:\n"
      :
      : [wave] "s"(wave), [first] "s"(first_off), [h] "s"(h), [ldsb] "s"(ldsb), [elo] "s"(LO(e)), [ehi] "s"(HI(e)), [olo] "s"(LO(out)), [ohi] "s"(HI(out)),
        [wqlo] "s"(LO(wq)), [wqhi] "s"(HI(wq)), [wklo] "s"(LO(wk)), [wkhi] "s"(HI(wk)), [wvlo] "s"(LO(wv)), [wvhi] "s"(HI(wv)), [wrlo] "s"(LO(wres)), [wrhi] "s"(HI(wres))
      : "v0", "v1", "v2", "v3", "v4", "v5", "v6", "v7", "v8", "v9", "v10", "v11", "v12", "v13", "v14", "v15", "v16", "v17", "v18", "v19", "v20", "v21", "v22", "v23", "v24", "v25", "v26", "v27", "v28", "v29", "v30", "v31", "v32", "v33", "v34", "v35", "v36", "v37", "v38", "v39", "v40", "v41", "v42", "v43", "v44", "v45", "v46", "v47", "v48", "v49", "v50", "v51", "v52", "v53", "v54", "v55", "v56", "v57", "v58", "v59", "v60", "v61", "v62", "v63", "v64", "v65", "v66", "v67", "v68", "v69", "v70", "v71", "v72", "v73", "v74", "v75", "v76", "v77", "v78", "v79", "v80", "v81", "v82", "v83", "v84", "v85", "v86", "v87", "v88", "v89", "v90", "v91", "v92", "v93", "v94", "v95", "v96", "v97", "v98", "v99", "v100", "v101", "v102", "v103", "v104", "v105", "v106", "v107", "v108", "v109", "v110", "v111", "v112", "v113", "v114", "v115", "v116", "v117", "v118", "v119", "v120", "v121", "v122", "v123", "v124", "v125", "v126", "v127", "v128", "v129", "v130", "v131", "v132", "v133", "v134", "v135", "v136", "v137", "v138", "v139", "v140", "v141", "v142", "v143", "v144", "v145", "v146", "v147", "v148", "v149", "v150", "v151", "v152", "v153", "v154", "v155", "v156", "v157", "v158", "v159", "v160", "v161", "v162", "v163", "v164", "v165", "v166", "v167", "v168", "v169", "v170", "v171", "v172", "v173", "v174", "v175", "v176", "v177", "v178", "v179", "v180", "v181", "v182", "v183", "v184", "v185", "v186", "v187", "v188", "v189", "v190", "v191", "v192", "v193", "v194", "v195", "v196", "v197", "v198", "v199", "v200", "v201", "v202", "v203", "v204", "v205", "v206", "v207", "v208", "v209", "v210", "v211", "v212", "v213", "v214", "v215", "v216", "v217", "v218", "v219", "v220", "v221", "v222", "v223", "v224", "v225", "v226", "v227", "v228", "v229", "v230", "v231", "v232", "v233", "v234", "v235", "v236", "v237", "v238", "v239", "v240", "v241", "v242", "v243", "v244", "v245", "v246", "v247", "v248", "v249", "v250", "v251", "v252", "v253", "v254", "v255", "a0", "a1", "a2", "a3", "a4", "a5", "a6", "a7", "a8", "a9", "a10", "a11", "a12", "a13", "a14", "a15", "a16", "a17", "a18", "a19", "a20", "a21", "a22", "a23", "a24", "a25", "a26", "a27", "a28", "a29", "a30", "a31", "a32", "a33", "a34", "a35", "a36", "a37", "a38", "a39", "a40", "a41", "a42", "a43", "a44", "a45", "a46", "a47", "a48", "a49", "a50", "a51", "a52", "a53", "a54", "a55", "a56", "a57", "a58", "a59", "a60", "a61", "a62", "a63", "a64", "a65", "a66", "a67", "a68", "a69", "a70", "a71", "a72", "a73", "a74", "a75", "a76", "a77", "a78", "a79", "a80", "a81", "a82", "a83", "a84", "a85", "a86", "a87", "a88", "a89", "a90", "a91", "a92", "a93", "a94", "a95", "a96", "a97", "a98", "a99", "a100", "a101", "a102", "a103", "a104", "a105", "a106", "a107", "a108", "a109", "a110", "a111", "a112", "a113", "a114", "a115", "a116", "a117", "a118", "a119", "a120", "a121", "a122", "a123", "a124", "a125", "a126", "a127", "a128", "a129", "a130", "a131", "a132", "a133", "a134", "a135", "a136", "a137", "a138", "a139", "a140", "a141", "a142", "a143", "a144", "a145", "a146", "a147", "a148", "a149", "a150", "a151", "a152", "a153", "a154", "a155", "a156", "a157", "a158", "a159", "a160", "a161", "a162", "a163", "a164", "a165", "a166", "a167", "a168", "a169", "a170", "a171", "a172", "a173", "a174", "a175", "a176", "a177", "a178", "a179", "a180", "a181", "a182", "a183", "a184", "a185", "a186", "a187", "a188", "a189", "a190", "a191", "a192", "a193", "a194", "a195", "a196", "a197", "a198", "a199", "a200", "a201", "a202", "a203", "a204", "a205", "a206", "a207", "a208", "a209", "a210", "a211", "a212", "a213", "a214", "a215", "a216", "a217", "a218", "a219", "a220", "a221", "a222", "a223", "a224", "a225", "a226", "a227", "a228", "a229", "a230", "a231", "a232", "a233", "a234", "a235", "a236", "a237", "a238", "a239", "a240", "a241", "a242", "a243", "a244", "a245", "a246", "a247", "a248", "a249", "a250", "a251", "a252", "a253", "a254", "a255", "s38", "s39", "s40", "s41", "s42", "s43", "s44", "s45", "s46", "s47", "s48", "s49", "s50", "s51", "s52", "s53", "s54", "s55", "s56", "s57", "s73", "s74", "s75", "s76", "s77", "s78", "s79", "s90", "s91", "s92", "s93", "s94", "s95", "s100", "s101", "s34", "s35", "vcc", "scc", "m0", "memory");
}

extern "C" void kernel_launch(void* const* d_in, const int* in_sizes, int n_in,
                              void* d_out, int out_size, void* d_ws, size_t ws_size,
                              hipStream_t stream) {
  const uint16_t* e = (const uint16_t*)d_in[0];
  const uint16_t* wq = (const uint16_t*)d_in[1];
  const uint16_t* wk = (const uint16_t*)d_in[2];
  const uint16_t* wv = (const uint16_t*)d_in[3];
  const uint16_t* wres = (const uint16_t*)d_in[4];
  uint16_t* out = (uint16_t*)d_out;
  autoint_kernel<<<256, 256, 0, stream>>>(e, wq, wk, wv, wres, out);
}
```

```cpp
#include <hip/hip_runtime.h>
#include <stdint.h>

__global__ __launch_bounds__(256, 1)
void autoint_kernel(const uint16_t* __restrict__ e, const uint16_t* __restrict__ wq,
                    const uint16_t* __restrict__ wk, const uint16_t* __restrict__ wv,
                    const uint16_t* __restrict__ wres, uint16_t* out, uint8_t* ws) {
  __shared__ __attribute__((aligned(16))) uint8_t lds[163840];
  const unsigned wave = __builtin_amdgcn_readfirstlane(threadIdx.x >> 6);
  const unsigned ldsb = __builtin_amdgcn_readfirstlane((unsigned)(size_t)lds);
  asm volatile(
      "s_load_dwordx8 s[4:11], %[ka], 0x0\n"
      "s_load_dwordx4 s[12:15], %[ka], 0x20\n"
      "s_load_dwordx2 s[16:17], %[ka], 0x30\n"
      "s_lshl_b32 s22, %[wave], 3\n"
      "s_add_u32 s22, s22, 8\n"
      "s_load_dwordx2 s[20:21], %[ka], s22\n"
      "v_mbcnt_lo_u32_b32 v240, -1, 0\n"
      "v_mbcnt_hi_u32_b32 v240, -1, v240\n"
      "s_getpc_b64 s[50:51]\n"
      "L_ai_pc:\n"
      "s_bfe_u32 s54, %[g], 0x20003\n"
      "s_lshl2_add_u32 s54, s54, %[wave]\n"
      "s_lshl_b32 s54, s54, 13\n"
      "v_lshlrev_b32 v250, 7, v240\n"
      "v_add_u32 v250, s54, v250\n"
      "v_min_u32 v250, L_ai_end-L_ai_pc, v250\n"
      "global_load_dword v17, v250, s[50:51]\n"
      "s_bfe_u32 s19, %[g], 0x10003\n"
      "s_and_b32 s18, %[g], 7\n"
      "s_lshr_b32 s54, %[g], 4\n"
      "s_lshl3_add_u32 s18, s54, s18\n"
      "s_lshl_b32 s18, s18, 5\n"
      "s_add_u32 s18, s18, %[wave]\n"
      "s_mul_i32 s18, s18, 25600\n"
      "s_mov_b32 s46, 32768\n"
      "s_mov_b32 s47, 0x20000\n"
      "v_lshrrev_b32 v234, 3, v240\n"
      "v_and_b32 v235, 7, v240\n"
      "v_lshlrev_b32 v233, 4, v235\n"
      "v_lshl_add_u32 v246, v234, 9, v233\n"
      "v_lshl_add_u32 v236, v234, 10, v233\n"
      "s_lshl_b32 s52, %[wave], 12\n"
      "s_lshl_b32 s55, s19, 14\n"
      "s_add_u32 s52, s52, s55\n"
      "s_lshl_b32 s57, %[wave], 13\n"
      "s_lshl_b32 s55, s19, 7\n"
      "s_add_u32 s57, s57, s55\n"
      "v_lshlrev_b32 v230, 7, v235\n"
      "s_lshr_b32 s23, %[g], 3\n"
      "s_lshl_b32 s23, s23, 10\n"
      "s_sub_u32 s49, s18, 103424\n"
      "s_ashr_i32 s50, s49, 31\n"
      "s_mov_b32 s42, 845824\n"
      "s_mov_b32 s43, 0x20000\n"
      "v_and_b32 v241, 15, v240\n"
      "v_lshrrev_b32 v242, 4, v240\n"
      "v_lshrrev_b32 v243, 2, v241\n"
      "v_and_b32 v244, 3, v240\n"
      "s_mul_i32 s53, %[wave], 24576\n"
      "s_add_u32 s53, s53, %[ldsb]\n"
      "s_add_u32 s53, s53, 65536\n"
      "v_and_b32 v248, 2, v234\n"
      "v_xor_b32 v248, v248, v244\n"
      "v_lshlrev_b32 v248, 4, v248\n"
      "v_lshrrev_b32 v249, 5, v240\n"
      "v_lshl_add_u32 v249, v249, 6, v248\n"
      "v_bfe_u32 v248, v240, 2, 3\n"
      "v_lshl_add_u32 v1, v248, 8, v249\n"
      "v_and_b32 v248, 1, v242\n"
      "v_mul_u32_u24 v247, 101376, v248\n"
      "v_add_u32 v247, v247, v1\n"
      "s_mov_b32 s73, 123904\n"
      "s_mov_b32 s74, 123008\n"
      "s_mov_b32 s75, 119808\n"
      "s_mov_b32 s76, 118912\n"
      "s_mov_b32 s77, 115712\n"
      "s_mov_b32 s78, 114816\n"
      "s_mov_b32 s79, 128000\n"
      "s_mov_b32 s90, 111616\n"
      "s_mov_b32 s91, 110720\n"
      "s_mov_b32 s92, 107520\n"
      "s_mov_b32 s93, 106624\n"
      "s_mov_b32 s94, 103424\n"
      "s_mov_b32 s95, 102528\n"
      "s_mov_b32 s100, 13312\n"
      "s_mov_b32 s101, 9216\n"
      "s_mov_b32 s34, 5120\n"
      "s_mov_b32 s35, 1024\n"
      "s_mov_b32 s51, 0x3e38aa3b\n"
      "s_lshl_b32 s54, %[wave], 10\n"
      "s_add_u32 s54, s54, %[ldsb]\n"
      "v_lshlrev_b32 v255, 8, v242\n"
      "s_waitcnt lgkmcnt(0)\n"
      "s_mov_b32 s44, s20\n"
      "s_and_b32 s45, s21, 0xffff\n"
      "buffer_load_dword v17, v230, s[44:47], s23 offen\n"
      "s_mov_b32 s44, s8\n s_and_b32 s45, s9, 0xffff\n"
      "buffer_load_dwordx4 v[64:67], v246, s[44:47], s52 offen offset:0\n"
      "s_add_u32 s40, s4, s49\n"
      "s_addc_u32 s41, s5, s50\n"
      "s_and_b32 s41, s41, 0xffff\n"
      "buffer_load_dwordx4 v[68:71], v246, s[44:47], s52 offen offset:128\n"
      "buffer_load_dwordx4 v[72:75], v246, s[44:47], s52 offen offset:256\n"
      "buffer_load_dwordx4 v[76:79], v246, s[44:47], s52 offen offset:384\n"
      "s_mov_b32 s44, s10\n s_and_b32 s45, s11, 0xffff\n"
      "buffer_load_dwordx4 v[80:83], v246, s[44:47], s52 offen offset:0\n"
      "buffer_load_dwordx4 v[84:87], v246, s[44:47], s52 offen offset:128\n"
      "buffer_load_dwordx4 v[88:91], v246, s[44:47], s52 offen offset:256\n"
      "buffer_load_dwordx4 v[92:95], v246, s[44:47], s52 offen offset:384\n"
      "s_add_u32 m0, s53, 0\n s_mov_b32 s30, 128000\n"
      "s_nop 0\n"
      "buffer_load_dwordx4 v247, s[40:43], s30 offen offset:0 lds\n"
      "v_lshl_add_u32 v255, v235, 5, v255\n"
      "v_lshrrev_b32 v248, 2, v235\n"
      "v_lshl_add_u32 v255, v248, 4, v255\n"
      "v_and_b32 v248, 1, v234\n"
      "v_lshl_add_u32 v255, v248, 3, v255\n"
      "v_add_u32 v255, s54, v255\n"
      "v_xor_b32 v232, 16, v255\n"
      "v_mov_b32 v238, 0x05040100\n"
      "v_mov_b32 v239, 0x07060302\n"
      "v_xor_b32 v254, v248, v240\n"
      "s_mov_b32 s30, 330752\n"
      "s_nop 0\n"
      "buffer_load_dwordx4 v247, s[40:43], s30 offen offset:2048 lds\n"
      "v_lshlrev_b32 v254, 4, v254\n"
      "v_add_u32 v254, %[ldsb], v254\n"
      "v_lshlrev_b32 v245, 3, v241\n"
      "v_lshrrev_b32 v248, 3, v241\n"
      "v_lshlrev_b32 v248, 11, v248\n"
      "v_lshl_add_u32 v248, v235, 6, v248\n"
      "v_lshrrev_b32 v249, 1, v241\n"
      "v_and_b32 v249, 2, v249\n"
      "v_xor_b32 v249, v249, v242\n"
      "v_lshl_add_u32 v248, v249, 4, v248\n"
      "s_mov_b32 s30, 127104\n"
      "s_nop 0\n"
      "buffer_load_dwordx4 v247, s[40:43], s30 offen offset:1024 lds\n"
      "v_add_u32 v0, s53, v248\n"
      "s_lshl_b32 s54, s19, 7\n"
      "v_add_u32 v248, s54, v245\n"
      "v_lshl_add_u32 v2, v242, 10, v248\n"
      "v_lshl_add_u32 v3, v242, 8, v248\n"
      "v_and_b32 v4, 60, v240\n"
      "v_and_b32 v248, 1, v240\n"
      "v_mul_u32_u24 v5, 0xaaaa, v248\n"
      "v_add_u32 v5, 0x4444, v5\n"
      "v_lshlrev_b32 v7, 6, v243\n"
      "s_mov_b32 s30, 329856\n"
      "s_nop 0\n"
      "buffer_load_dwordx4 v247, s[40:43], s30 offen offset:3072 lds\n"
      "v_lshl_add_u32 v7, v248, 3, v7\n"
      "v_and_b32 v249, 1, v242\n"
      "v_lshl_add_u32 v6, v249, 5, v7\n"
      "v_lshrrev_b32 v250, 1, v242\n"
      "v_lshl_add_u32 v6, v250, 2, v6\n"
      "v_lshl_add_u32 v7, v242, 4, v7\n"
      "v_mov_b32 v8, 1.0\n"
      "s_lshl_b32 s54, %[wave], 14\n"
      "s_add_u32 s54, s54, %[ldsb]\n"
      "v_lshlrev_b32 v16, 10, v242\n"
      "v_lshrrev_b32 v231, 1, v241\n"
      "v_lshl_add_u32 v16, v231, 4, v16\n"
      "v_lshl_add_u32 v16, v248, 2, v16\n"
      "v_add_u32 v16, s54, v16\n"
      "v_lshrrev_b32 v231, 3, v241\n"
      "v_lshl_add_u32 v16, v231, 20, v16\n"
      "v_lshlrev_b32 v9, 9, v242\n"
      "v_add_u32 v9, s54, v9\n"
      "v_lshl_add_u32 v251, v241, 4, v9\n"
      "v_lshl_add_u32 v9, v244, 4, v9\n"
      "s_add_u32 s54, s54, 4096\n"
      "v_lshlrev_b32 v10, 10, v242\n"
      "v_lshl_add_u32 v10, v243, 4, v10\n"
      "v_lshl_add_u32 v10, v248, 9, v10\n"
      "v_add_u32 v10, s54, v10\n"
      "v_lshlrev_b32 v252, 10, v250\n"
      "v_lshl_add_u32 v252, v241, 4, v252\n"
      "v_lshl_add_u32 v252, v249, 3, v252\n"
      "v_add_u32 v252, s54, v252\n"
      "s_add_u32 s54, s54, 4096\n"
      "v_lshlrev_b32 v11, 3, v240\n"
      "v_add_u32 v11, s54, v11\n"
      "v_lshl_add_u32 v253, v242, 9, v245\n"
      "v_add_u32 v253, s54, v253\n"
      "v_mov_b32 v12, 0x3f803f80\n"
      "v_mov_b32 v13, 0x3f803f80\n"
      "v_mov_b32 v14, 0x3f803f80\n"
      "v_mov_b32 v15, 0x3f803f80\n"
      "s_mov_b32 vcc_lo, 0x55555555\n"
      "s_mov_b32 vcc_hi, 0x55555555\n"
      "s_mov_b32 s26, 16384\n"
      "s_mov_b32 s27, 0x20000\n"
      "s_branch L_hs_%=\n"
      "L_hr_%=:\n"
      "s_setpc_b64 vcc\n"
      "L_hs_%=:\n"
      "s_mov_b32 s38, 0xc000000\n"
      "s_getpc_b64 s[28:29]\n"
      "L_pce5_%=:\n"
      "s_add_u32 s28, s28, L_e5_%=-L_pce5_%=\n"
      "s_addc_u32 s29, s29, 0\n"
      "s_mov_b64 exec, 0\n"
      "s_cmp_eq_u32 %[wave], 0\n"
      "s_cbranch_scc0 L_hn1o0_%=\n"
      "s_call_b64 vcc, L_h0_%=\n"
      "L_hn1o0_%=:\n"
      "s_cmp_eq_u32 %[wave], 1\n"
      "s_cbranch_scc0 L_hn1o1_%=\n"
      "s_call_b64 vcc, L_h1_%=\n"
      "L_hn1o1_%=:\n"
      "s_cmp_eq_u32 %[wave], 2\n"
      "s_cbranch_scc0 L_hn1o2_%=\n"
      "s_call_b64 vcc, L_h2_%=\n"
      "L_hn1o2_%=:\n"
      "s_cmp_eq_u32 %[wave], 3\n"
      "s_cbranch_scc0 L_hn1o3_%=\n"
      "s_nop 0\n"
      "L_hn1o3_%=:\n"
      "s_mov_b64 exec, -1\n"
      "s_mov_b32 vcc_lo, 0x55555555\n"
      "s_mov_b32 vcc_hi, 0x55555555\n"
      "s_add_u32 m0, s53, 20480\n"
      "s_nop 0\n"
      "buffer_load_dwordx4 v1, s[40:43], s73 offen offset:0 lds\n"
      "buffer_load_dwordx4 v1, s[40:43], s73 offen offset:2048 lds\n"
      "buffer_load_dwordx4 v1, s[40:43], s74 offen offset:1024 lds\n"
      "s_waitcnt vmcnt(13)\n"
      "v_perm_b32 v228, v68, v64, v238\n"
      "s_waitcnt vmcnt(11)\n"
      "v_perm_b32 v225, v78, v74, v238\n"
      "v_perm_b32 v224, v70, v66, v238\n"
      "v_perm_b32 v133, v76, v72, v239\n"
      "buffer_load_dwordx4 v1, s[40:43], s74 offen offset:3072 lds\n"
      "v_perm_b32 v25, v79, v75, v238\n"
      "s_mov_b64 exec, 0\n"
      "s_cmp_eq_u32 %[wave], 0\n"
      "s_cbranch_scc0 L_hn2o0_%=\n"
      "s_call_b64 vcc, L_h3_%=\n"
      "s_call_b64 vcc, L_h7_%=\n"
      "s_nop 0\n"
      "L_hn2o0_%=:\n"
      "s_cmp_eq_u32 %[wave], 1\n"
      "s_cbranch_scc0 L_hn2o1_%=\n"
      "s_call_b64 vcc, L_h4_%=\n"
      "s_call_b64 vcc, L_h8_%=\n"
      "s_nop 0\n"
      "L_hn2o1_%=:\n"
      "s_cmp_eq_u32 %[wave], 2\n"
      "s_cbranch_scc0 L_hn2o2_%=\n"
      "s_call_b64 vcc, L_h5_%=\n"
      "s_call_b64 vcc, L_h9_%=\n"
      "s_nop 0\n"
      "L_hn2o2_%=:\n"
      "s_cmp_eq_u32 %[wave], 3\n"
      "s_cbranch_scc0 L_hn2o3_%=\n"
      "s_call_b64 vcc, L_h6_%=\n"
      "s_nop 0\n"
      "s_nop 0\n"
      "L_hn2o3_%=:\n"
      "s_mov_b64 exec, -1\n"
      "s_mov_b32 vcc_lo, 0x55555555\n"
      "s_mov_b32 vcc_hi, 0x55555555\n"
      "v_perm_b32 v24, v71, v67, v238\n"
      "s_waitcnt vmcnt(10)\n"
      "v_perm_b32 v26, v84, v80, v238\n"
      "v_perm_b32 v208, v87, v83, v238\n"
      "v_perm_b32 v61, v77, v73, v239\n"
      "v_perm_b32 v210, v70, v66, v239\n"
      "s_mov_b32 s44, s12\n s_and_b32 s45, s13, 0xffff\n"
      "v_perm_b32 v211, v78, v74, v239\n"
      "s_waitcnt vmcnt(8)\n"
      "v_perm_b32 v101, v95, v91, v239\n"
      "v_perm_b32 v215, v93, v89, v238\n"
      "ds_write_b64 v232, v[224:225] offset:0\n"
      "v_perm_b32 v231, v79, v75, v239\n"
      "v_perm_b32 v100, v87, v83, v239\n"
      "v_perm_b32 v213, v94, v90, v238\n"
      "buffer_load_dwordx4 v[224:227], v236, s[44:47], s57 offen offset:0\n"
      "ds_write_b64 v232, v[210:211] offset:4096\n"
      "v_perm_b32 v62, v85, v81, v239\n"
      "v_perm_b32 v135, v94, v90, v239\n"
      "v_perm_b32 v209, v95, v91, v238\n"
      "s_mov_b32 s30, 537600\n"
      "ds_write_b64 v232, v[208:209] offset:24576\n"
      "v_perm_b32 v27, v92, v88, v238\n"
      "ds_write_b64 v232, v[100:101] offset:28672\n"
      "buffer_load_dwordx4 v[208:211], v236, s[44:47], s57 offen offset:256\n"
      "v_perm_b32 v212, v86, v82, v238\n"
      "buffer_load_dwordx4 v[100:103], v236, s[44:47], s57 offen offset:512\n"
      "v_perm_b32 v63, v93, v89, v239\n"
      "buffer_load_dwordx4 v[184:187], v236, s[44:47], s57 offen offset:768\n"
      "ds_write_b64 v255, v[62:63] offset:28672\n"
      "v_perm_b32 v60, v69, v65, v239\n"
      "ds_write_b64 v255, v[60:61] offset:12288\n"
      "v_perm_b32 v132, v68, v64, v239\n"
      "v_perm_b32 v214, v85, v81, v238\n"
      "v_perm_b32 v230, v71, v67, v239\n"
      "v_perm_b32 v61, v92, v88, v239\n"
      "ds_write_b64 v255, v[132:133] offset:4096\n"
      "ds_write_b64 v255, v[214:215] offset:24576\n"
      "ds_write_b64 v232, v[212:213] offset:16384\n"
      "v_perm_b32 v132, v69, v65, v238\n"
      "v_perm_b32 v134, v86, v82, v239\n"
      "v_perm_b32 v133, v77, v73, v238\n"
      "ds_write_b64 v255, v[132:133] offset:8192\n"
      "ds_write_b64 v255, v[26:27] offset:16384\n"
      "v_perm_b32 v229, v76, v72, v238\n"
      "ds_write_b64 v232, v[230:231] offset:12288\n"
      "v_perm_b32 v60, v84, v80, v239\n"
      "ds_write_b64 v255, v[60:61] offset:20480\n"
      "ds_write_b64 v232, v[134:135] offset:20480\n"
      "ds_write_b64 v255, v[228:229] offset:0\n"
      "s_mov_b32 s44, s6\n s_and_b32 s45, s7, 0xffff\n"
      "ds_write_b64 v232, v[24:25] offset:8192\n"
      "s_waitcnt lgkmcnt(0)\n"
      "s_barrier\n"
      "ds_read_b128 a[8:11], v254 offset:2048\n"
      "ds_read_b128 a[48:51], v254 offset:12288\n"
      "ds_read_b128 a[112:115], v254 offset:28672\n"
      "buffer_load_dwordx4 v[24:27], v246, s[44:47], s52 offen offset:0\n"
      "ds_read_b128 a[96:99], v254 offset:24576\n"
      "ds_read_b128 a[40:43], v254 offset:10240\n"
      "s_waitcnt vmcnt(9)\n"
      "ds_read_b128 v[228:231], v0 offset:0\n"
      "ds_read_b128 a[60:63], v254 offset:15360\n"
      "ds_read_b128 a[20:23], v254 offset:5120\n"
      "ds_read_b128 a[108:111], v254 offset:27648\n"
      "ds_read_b128 a[72:75], v254 offset:18432\n"
      "ds_read_b128 v[18:21], v0 offset:1024\n"
      "ds_read_b128 a[104:107], v254 offset:26624\n"
      "ds_read_b128 v[132:135], v0 offset:512\n"
      "ds_read_b128 a[100:103], v254 offset:25600\n"
      "buffer_load_dwordx4 v[60:63], v246, s[44:47], s52 offen offset:128\n"
      "ds_read_b128 a[44:47], v254 offset:11264\n"
      "buffer_load_dwordx4 v[192:195], v246, s[44:47], s52 offen offset:256\n"
      "buffer_load_dwordx4 v[120:123], v246, s[44:47], s52 offen offset:384\n"
      "ds_read_b128 a[32:35], v254 offset:8192\n"
      "ds_read_b128 v[64:67], v0 offset:1536\n"
      "ds_read_b128 a[124:127], v254 offset:31744\n"
      "s_add_u32 m0, s53, 4096\n"
      "ds_read_b128 a[80:83], v254 offset:20480\n"
      "ds_read_b128 a[36:39], v254 offset:9216\n"
      "ds_read_b128 a[16:19], v254 offset:4096\n"
      "ds_read_b128 a[24:27], v254 offset:6144\n"
      "ds_read_b128 a[84:87], v254 offset:21504\n"
      "ds_read_b128 a[28:31], v254 offset:7168\n"
      "buffer_load_dwordx4 v247, s[40:43], s30 offen offset:0 lds\n"
      "ds_read_b128 a[56:59], v254 offset:14336\n"
      "ds_read_b128 a[92:95], v254 offset:23552\n"
      "s_add_u32 s44, s14, s49\n s_addc_u32 s45, s15, s50\n s_and_b32 s45, s45, 0xffff\n s_mov_b32 s46, 129024\n"
      "ds_read_b128 a[64:67], v254 offset:16384\n"
      "ds_read_b128 a[116:119], v254 offset:29696\n"
      "ds_read_b128 a[120:123], v254 offset:30720\n"
      "ds_read_b128 a[88:91], v254 offset:22528\n"
      "s_waitcnt lgkmcnt(14)\n"
      "v_mfma_f32_16x16x32_bf16 v[216:219], a[32:35], v[228:231], 0\n"
      "s_waitcnt lgkmcnt(10)\n"
      "v_mfma_f32_16x16x32_bf16 v[216:219], a[36:39], v[132:135], v[216:219]\n"
      "v_mfma_f32_16x16x32_bf16 v[216:219], a[40:43], v[18:21], v[216:219]\n"
      "v_mfma_f32_16x16x32_bf16 v[216:219], a[44:47], v[64:67], v[216:219]\n"
      "s_mov_b32 s30, 740352\n"
      "buffer_load_dwordx4 v247, s[40:43], s30 offen offset:2048 lds\n"
      "s_mov_b32 s30, 536704\n"
      "ds_read_b128 a[0:3], v254 offset:0\n"
      "v_mfma_f32_16x16x32_bf16 v[220:223], v[228:231], a[112:115], 0\n"
      "s_waitcnt lgkmcnt(3)\n"
      "v_mfma_f32_16x16x32_bf16 v[220:223], v[132:135], a[116:119], v[220:223]\n"
      "ds_read_b128 a[52:55], v254 offset:13312\n"
      "s_waitcnt lgkmcnt(3)\n"
      "v_mfma_f32_16x16x32_bf16 v[220:223], v[18:21], a[120:123], v[220:223]\n"
      "v_mfma_f32_16x16x32_bf16 v[220:223], v[64:67], a[124:127], v[220:223]\n"
      "ds_read_b128 a[12:15], v254 offset:3072\n"
      "v_mfma_f32_16x16x32_bf16 v[84:87], v[228:231], a[96:99], 0\n"
      "v_mfma_f32_16x16x32_bf16 v[84:87], v[132:135], a[100:103], v[84:87]\n"
      "v_mfma_f32_16x16x32_bf16 v[84:87], v[18:21], a[104:107], v[84:87]\n"
      "v_mfma_f32_16x16x32_bf16 v[84:87], v[64:67], a[108:111], v[84:87]\n"
      "ds_read_b128 a[68:71], v254 offset:17408\n"
      "ds_read_b128 a[4:7], v254 offset:1024\n"
      "buffer_load_dwordx4 v247, s[40:43], s30 offen offset:1024 lds\n"
      "v_mfma_f32_16x16x32_bf16 v[152:155], v[228:231], a[80:83], 0\n"
      "ds_read_b128 a[76:79], v254 offset:19456\n"
      "v_mfma_f32_16x16x32_bf16 v[152:155], v[132:135], a[84:87], v[152:155]\n"
      "s_waitcnt lgkmcnt(6)\n"
      "v_mfma_f32_16x16x32_bf16 v[152:155], v[18:21], a[88:91], v[152:155]\n"
      "v_mfma_f32_16x16x32_bf16 v[152:155], v[64:67], a[92:95], v[152:155]\n"
      "v_mfma_f32_16x16x32_bf16 v[52:55], a[48:51], v[228:231], 0\n"
      "s_waitcnt lgkmcnt(4)\n"
      "v_mfma_f32_16x16x32_bf16 v[52:55], a[52:55], v[132:135], v[52:55]\n"
      "s_mov_b32 s30, 739456\n"
      "v_mfma_f32_16x16x32_bf16 v[52:55], a[56:59], v[18:21], v[52:55]\n"
      "v_mfma_f32_16x16x32_bf16 v[52:55], a[60:63], v[64:67], v[52:55]\n"
      "v_mfma_f32_16x16x32_bf16 v[188:191], a[16:19], v[228:231], 0\n"
      "v_mfma_f32_16x16x32_bf16 v[188:191], a[20:23], v[132:135], v[188:191]\n"
      "v_mfma_f32_16x16x32_bf16 v[188:191], a[24:27], v[18:21], v[188:191]\n"
      "v_mfma_f32_16x16x32_bf16 v[188:191], a[28:31], v[64:67], v[188:191]\n"
      "buffer_load_dwordx4 v247, s[40:43], s30 offen offset:3072 lds\n"
      "s_waitcnt vmcnt(12)\n"
      "ds_read_b128 v[92:95], v0 offset:20480\n"
      "ds_read_b128 v[244:247], v0 offset:22016\n"
      "ds_read_b128 v[124:127], v0 offset:20992\n"
      "ds_read_b128 v[176:179], v0 offset:21504\n"
      "v_mfma_f32_16x16x32_bf16 v[128:131], v[228:231], a[64:67], 0\n"
      "s_waitcnt lgkmcnt(6)\n"
      "v_mfma_f32_16x16x32_bf16 v[128:131], v[132:135], a[68:71], v[128:131]\n"
      "s_add_u32 m0, s53, 16384\n"
      "v_mfma_f32_16x16x32_bf16 v[128:131], v[18:21], a[72:75], v[128:131]\n"
      "s_waitcnt lgkmcnt(4)\n"
      "v_mfma_f32_16x16x32_bf16 v[128:131], v[64:67], a[76:79], v[128:131]\n"
      "buffer_load_dwordx4 v1, s[40:43], s75 offen offset:0 lds\n"
      "v_mfma_f32_16x16x32_bf16 v[80:83], a[0:3], v[228:231], 0\n"
      "v_mfma_f32_16x16x32_bf16 v[80:83], a[4:7], v[132:135], v[80:83]\n"
      "v_mfma_f32_16x16x32_bf16 v[80:83], a[8:11], v[18:21], v[80:83]\n"
      "v_mfma_f32_16x16x32_bf16 v[80:83], a[12:15], v[64:67], v[80:83]\n"
      "s_waitcnt vmcnt(11)\n"
      "v_perm_b32 v88, v208, v224, v238\n"
      "s_waitcnt lgkmcnt(3)\n"
      "v_mfma_f32_16x16x32_bf16 v[164:167], a[0:3], v[92:95], 0\n"
      "v_cvt_pk_bf16_f32 v107, v190, v191\n"
      "s_waitcnt lgkmcnt(1)\n"
      "v_mfma_f32_16x16x32_bf16 v[164:167], a[4:7], v[124:127], v[164:167]\n"
      "v_perm_b32 v68, v209, v225, v238\n"
      "s_waitcnt lgkmcnt(0)\n"
      "v_mfma_f32_16x16x32_bf16 v[164:167], a[8:11], v[176:179], v[164:167]\n"
      "v_perm_b32 v190, v210, v226, v239\n"
      "v_mfma_f32_16x16x32_bf16 v[164:167], a[12:15], v[244:247], v[164:167]\n"
      "v_perm_b32 v118, v208, v224, v239\n"
      "v_mfma_f32_16x16x32_bf16 v[76:79], a[48:51], v[92:95], 0\n"
      "buffer_load_dwordx4 v1, s[40:43], s75 offen offset:2048 lds\n"
      "v_mfma_f32_16x16x32_bf16 v[76:79], a[52:55], v[124:127], v[76:79]\n"
      "buffer_load_dwordx4 v1, s[40:43], s76 offen offset:1024 lds\n"
      "v_mfma_f32_16x16x32_bf16 v[76:79], a[56:59], v[176:179], v[76:79]\n"
      "v_mfma_f32_16x16x32_bf16 v[76:79], a[60:63], v[244:247], v[76:79]\n"
      "s_waitcnt vmcnt(11)\n"
      "v_perm_b32 v69, v185, v101, v238\n"
      "v_cvt_pk_bf16_f32 v75, v54, v55\n"
      "ds_write_b64 v255, v[68:69] offset:57344\n"
      "v_perm_b32 v191, v186, v102, v239\n"
      "v_perm_b32 v54, v209, v225, v239\n"
      "v_perm_b32 v55, v185, v101, v239\n"
      "ds_write_b64 v255, v[54:55] offset:61440\n"
      "v_perm_b32 v206, v211, v227, v238\n"
      "v_perm_b32 v151, v187, v103, v239\n"
      "buffer_load_dwordx4 v1, s[40:43], s76 offen offset:3072 lds\n"
      "ds_write_b64 v232, v[190:191] offset:53248\n"
      "v_perm_b32 v204, v210, v226, v238\n"
      "s_waitcnt vmcnt(11)\n"
      "v_lshlrev_b32 v54, 16, v24\n"
      "v_and_b32 v208, 0xffff0000, v24\n"
      "v_and_b32 v24, 0xffff0000, v27\n"
      "v_mul_f32 v24, s51, v24\n"
      "v_mfma_f32_16x16x32_bf16 v[68:71], v[92:95], a[96:99], 0\n"
      "s_waitcnt vmcnt(10)\n"
      "v_lshlrev_b32 v55, 16, v60\n"
      "v_and_b32 v117, 0xffff0000, v61\n"
      "v_mfma_f32_16x16x32_bf16 v[68:71], v[124:127], a[100:103], v[68:71]\n"
      "v_perm_b32 v150, v211, v227, v239\n"
      "v_mul_f32 v54, s51, v54\n"
      "v_mfma_f32_16x16x32_bf16 v[68:71], v[176:179], a[104:107], v[68:71]\n"
      "v_and_b32 v209, 0xffff0000, v60\n"
      "v_mfma_f32_16x16x32_bf16 v[68:71], v[244:247], a[108:111], v[68:71]\n"
      "v_and_b32 v116, 0xffff0000, v25\n"
      "v_mul_f32 v116, s51, v116\n"
      "v_lshlrev_b32 v91, 16, v63\n"
      "v_and_b32 v210, 0xffff0000, v26\n"
      "v_mfma_f32_16x16x32_bf16 v[224:227], a[16:19], v[92:95], 0\n"
      "v_mul_f32 v209, s51, v209\n"
      "v_mfma_f32_16x16x32_bf16 v[224:227], a[20:23], v[124:127], v[224:227]\n"
      "v_mul_f32 v55, s51, v55\n"
      "v_mul_f32 v210, s51, v210\n"
      "v_mfma_f32_16x16x32_bf16 v[224:227], a[24:27], v[176:179], v[224:227]\n"
      "s_waitcnt vmcnt(9)\n"
      "v_and_b32 v190, 0xffff0000, v192\n"
      "v_mul_f32 v190, s51, v190\n"
      "v_mfma_f32_16x16x32_bf16 v[224:227], a[28:31], v[244:247], v[224:227]\n"
      "s_waitcnt vmcnt(8)\n"
      "v_and_b32 v191, 0xffff0000, v120\n"
      "ds_write_b64 v232, v[150:151] offset:61440\n"
      "v_mul_f32 v191, s51, v191\n"
      "v_lshlrev_b32 v101, 16, v122\n"
      "v_perm_b32 v207, v187, v103, v238\n"
      "v_lshlrev_b32 v151, 16, v120\n"
      "v_lshlrev_b32 v60, 16, v26\n"
      "v_mul_f32 v117, s51, v117\n"
      "v_and_b32 v103, 0xffff0000, v121\n"
      "v_mul_f32 v60, s51, v60\n"
      "v_lshlrev_b32 v26, 16, v195\n"
      "v_and_b32 v120, 0xffff0000, v195\n"
      "v_perm_b32 v205, v186, v102, v238\n"
      "v_and_b32 v197, 0xffff0000, v122\n"
      "v_lshlrev_b32 v90, 16, v27\n"
      "v_perm_b32 v119, v184, v100, v239\n"
      "v_lshlrev_b32 v27, 16, v123\n"
      "v_and_b32 v211, 0xffff0000, v62\n"
      "v_lshlrev_b32 v150, 16, v192\n"
      "v_mul_f32 v150, s51, v150\n"
      "v_lshlrev_b32 v187, 16, v121\n"
      "v_lshlrev_b32 v105, 16, v61\n"
      "v_and_b32 v196, 0xffff0000, v194\n"
      "v_mul_f32 v151, s51, v151\n"
      "v_mul_f32 v208, s51, v208\n"
      "s_add_u32 m0, s53, 12288\n"
      "ds_write_b64 v232, v[206:207] offset:57344\n"
      "v_lshlrev_b32 v104, 16, v25\n"
      "v_mul_f32 v197, s51, v197\n"
      "v_cvt_pk_bf16_f32 v206, v54, v55\n"
      "ds_write_b64 v255, v[118:119] offset:53248\n"
      "v_cvt_pk_bf16_f32 v192, v116, v117\n"
      "v_mul_f32 v211, s51, v211\n"
      "v_mul_f32 v187, s51, v187\n"
      "v_cvt_pk_bf16_f32 v54, v208, v209\n"
      "v_perm_b32 v89, v184, v100, v238\n"
      "ds_write_b64 v232, v[204:205] offset:49152\n"
      "v_and_b32 v102, 0xffff0000, v193\n"
      "v_mul_f32 v102, s51, v102\n"
      "v_lshlrev_b32 v186, 16, v193\n"
      "v_mul_f32 v186, s51, v186\n"
      "v_cvt_pk_bf16_f32 v207, v150, v151\n"
      "v_mul_f32 v196, s51, v196\n"
      "v_cvt_pk_bf16_f32 v119, v196, v197\n"
      "v_lshlrev_b32 v61, 16, v62\n"
      "v_lshlrev_b32 v100, 16, v194\n"
      "v_mul_f32 v91, s51, v91\n"
      "v_mul_f32 v104, s51, v104\n"
      "ds_write_b64 v255, v[206:207] offset:32768\n"
      "v_cvt_pk_bf16_f32 v55, v190, v191\n"
      "v_mul_f32 v90, s51, v90\n"
      "v_cvt_pk_bf16_f32 v116, v90, v91\n"
      "ds_write_b64 v255, v[54:55] offset:36864\n"
      "v_and_b32 v25, 0xffff0000, v63\n"
      "v_mul_f32 v25, s51, v25\n"
      "v_mul_f32 v61, s51, v61\n"
      "v_mul_f32 v105, s51, v105\n"
      "ds_write_b64 v255, v[88:89] offset:49152\n"
      "v_mul_f32 v100, s51, v100\n"
      "v_mul_f32 v101, s51, v101\n"
      "v_cvt_pk_bf16_f32 v206, v104, v105\n"
      "v_mul_f32 v26, s51, v26\n"
      "v_and_b32 v121, 0xffff0000, v123\n"
      "v_mul_f32 v103, s51, v103\n"
      "v_cvt_pk_bf16_f32 v118, v210, v211\n"
      "ds_write_b64 v232, v[118:119] offset:36864\n"
      "v_cvt_pk_bf16_f32 v207, v186, v187\n"
      "ds_write_b64 v255, v[206:207] offset:40960\n"
      "v_cvt_pk_bf16_f32 v118, v24, v25\n"
      "v_cvt_pk_bf16_f32 v104, v60, v61\n"
      "v_cvt_pk_bf16_f32 v105, v100, v101\n"
      "v_cvt_pk_bf16_f32 v193, v102, v103\n"
      "ds_write_b64 v255, v[192:193] offset:45056\n"
      "v_cvt_pk_bf16_f32 v114, v128, v129\n"
      "v_mul_f32 v27, s51, v27\n"
      "v_cvt_pk_bf16_f32 v117, v26, v27\n"
      "ds_write_b64 v232, v[104:105] offset:32768\n"
      "buffer_load_dwordx4 v1, s[40:43], s77 offen offset:0 lds\n"
      "v_mul_f32 v121, s51, v121\n"
      "ds_write_b64 v232, v[116:117] offset:40960\n"
      "v_mul_f32 v120, s51, v120\n"
      "v_cvt_pk_bf16_f32 v119, v120, v121\n"
      "v_cvt_pk_bf16_f32 v104, v80, v81\n"
      "ds_write_b64 v232, v[118:119] offset:45056\n"
      "v_mfma_f32_16x16x32_bf16 v[148:151], v[92:95], a[112:115], 0\n"
      "buffer_load_dwordx4 v1, s[40:43], s77 offen offset:2048 lds\n"
      "v_mfma_f32_16x16x32_bf16 v[148:151], v[124:127], a[116:119], v[148:151]\n"
      "buffer_load_dwordx4 v1, s[40:43], s78 offen offset:1024 lds\n"
      "v_mfma_f32_16x16x32_bf16 v[148:151], v[176:179], a[120:123], v[148:151]\n"
      "v_mfma_f32_16x16x32_bf16 v[148:151], v[244:247], a[124:127], v[148:151]\n"
      "v_cvt_pk_bf16_f32 v116, v152, v153\n"
      "v_cvt_pk_bf16_f32 v117, v154, v155\n"
      "v_mfma_f32_16x16x32_bf16 v[118:121], v[92:95], a[80:83], 0\n"
      "v_mfma_f32_16x16x32_bf16 v[118:121], v[124:127], a[84:87], v[118:121]\n"
      "v_mfma_f32_16x16x32_bf16 v[118:121], v[176:179], a[88:91], v[118:121]\n"
      "v_mfma_f32_16x16x32_bf16 v[118:121], v[244:247], a[92:95], v[118:121]\n"
      "s_waitcnt lgkmcnt(0)\n"
      "s_barrier\n"
      "ds_read_b128 a[144:147], v254 offset:36864\n"
      "buffer_load_dwordx4 v1, s[40:43], s78 offen offset:3072 lds\n"
      "ds_read_b128 a[148:151], v254 offset:37888\n"
      "v_mfma_f32_16x16x32_bf16 v[192:195], a[32:35], v[92:95], 0\n"
      "ds_read_b128 a[152:155], v254 offset:38912\n"
      "v_mfma_f32_16x16x32_bf16 v[192:195], a[36:39], v[124:127], v[192:195]\n"
      "ds_read_b128 a[156:159], v254 offset:39936\n"
      "v_mfma_f32_16x16x32_bf16 v[192:195], a[40:43], v[176:179], v[192:195]\n"
      "s_add_u32 m0, s53, 8192\n"
      "ds_read_b128 a[132:135], v254 offset:33792\n"
      "v_mfma_f32_16x16x32_bf16 v[192:195], a[44:47], v[244:247], v[192:195]\n"
      "ds_read_b128 a[164:167], v254 offset:41984\n"
      "v_mfma_f32_16x16x32_bf16 v[204:207], v[92:95], a[64:67], 0\n"
      "v_cvt_pk_bf16_f32 v122, v118, v119\n"
      "v_mfma_f32_16x16x32_bf16 v[204:207], v[124:127], a[68:71], v[204:207]\n"
      "ds_read_b128 a[128:131], v254 offset:32768\n"
      "v_cvt_pk_bf16_f32 v142, v76, v77\n"
      "v_mfma_f32_16x16x32_bf16 v[204:207], v[176:179], a[72:75], v[204:207]\n"
      "ds_read_b128 a[172:175], v254 offset:44032\n"
      "v_mfma_f32_16x16x32_bf16 v[204:207], v[244:247], a[76:79], v[204:207]\n"
      "ds_read_b128 a[140:143], v254 offset:35840\n"
      "s_waitcnt lgkmcnt(8)\n"
      "v_mfma_f32_16x16x32_bf16 v[88:91], a[144:147], v[228:231], 0\n"
      "ds_read_b128 a[136:139], v254 offset:34816\n"
      "s_waitcnt lgkmcnt(8)\n"
      "v_mfma_f32_16x16x32_bf16 v[88:91], a[148:151], v[132:135], v[88:91]\n"
      "ds_read_b128 a[212:215], v254 offset:54272\n"
      "s_waitcnt lgkmcnt(8)\n"
      "v_mfma_f32_16x16x32_bf16 v[88:91], a[152:155], v[18:21], v[88:91]\n"
      "ds_read_b128 a[168:171], v254 offset:43008\n"
      "s_waitcnt lgkmcnt(8)\n"
      "v_mfma_f32_16x16x32_bf16 v[88:91], a[156:159], v[64:67], v[88:91]\n"
      "ds_read_b128 a[252:255], v254 offset:64512\n"
      "v_mfma_f32_16x16x32_bf16 v[158:161], a[144:147], v[92:95], 0\n"
      "v_mfma_f32_16x16x32_bf16 v[158:161], a[148:151], v[124:127], v[158:161]\n"
      "buffer_load_dwordx4 v1, s[40:43], s90 offen offset:0 lds\n"
      "v_mfma_f32_16x16x32_bf16 v[158:161], a[152:155], v[176:179], v[158:161]\n"
      "v_mfma_f32_16x16x32_bf16 v[158:161], a[156:159], v[244:247], v[158:161]\n"
      "ds_read_b128 a[160:163], v254 offset:40960\n"
      "s_waitcnt lgkmcnt(7)\n"
      "v_mfma_f32_16x16x32_bf16 v[210:213], a[128:131], v[92:95], 0\n"
      "ds_read_b128 a[184:187], v254 offset:47104\n"
      "v_mfma_f32_16x16x32_bf16 v[210:213], a[132:135], v[124:127], v[210:213]\n"
      "ds_read_b128 a[188:191], v254 offset:48128\n"
      "s_waitcnt lgkmcnt(6)\n"
      "v_mfma_f32_16x16x32_bf16 v[210:213], a[136:139], v[176:179], v[210:213]\n"
      "ds_read_b128 a[208:211], v254 offset:53248\n"
      "v_mfma_f32_16x16x32_bf16 v[210:213], a[140:143], v[244:247], v[210:213]\n"
      "ds_read_b128 a[180:183], v254 offset:46080\n"
      "v_mfma_f32_16x16x32_bf16 v[28:31], a[128:131], v[228:231], 0\n"
      "ds_read_b128 a[176:179], v254 offset:45056\n"
      "v_mfma_f32_16x16x32_bf16 v[28:31], a[132:135], v[132:135], v[28:31]\n"
      "v_mfma_f32_16x16x32_bf16 v[28:31], a[136:139], v[18:21], v[28:31]\n"
      "ds_read_b128 a[240:243], v254 offset:61440\n"
      "v_mfma_f32_16x16x32_bf16 v[28:31], a[140:143], v[64:67], v[28:31]\n"
      "buffer_load_dwordx4 v1, s[40:43], s90 offen offset:2048 lds\n"
      "ds_read_b128 a[248:251], v254 offset:63488\n"
      "s_waitcnt lgkmcnt(7)\n"
      "v_mfma_f32_16x16x32_bf16 v[152:155], a[160:163], v[228:231], 0\n"
      "L_h0_%=:\n"
      "s_cbranch_execz L_hr_%=\n"
      "v_mfma_f32_16x16x32_bf16 v[152:155], a[164:167], v[132:135], v[152:155]\n"
      "ds_read_b128 a[232:235], v254 offset:59392\n"
      "v_mfma_f32_16x16x32_bf16 v[152:155], a[168:171], v[18:21], v[152:155]\n"
      "ds_read_b128 a[216:219], v254 offset:55296\n"
      "v_cvt_pk_bf16_f32 v96, v210, v211\n"
      "v_mfma_f32_16x16x32_bf16 v[152:155], a[172:175], v[64:67], v[152:155]\n"
      "v_cvt_pk_bf16_f32 v105, v82, v83\n"
      "v_mfma_f32_16x16x32_bf16 v[40:43], a[160:163], v[92:95], 0\n"
      "v_mfma_f32_16x16x32_bf16 v[40:43], a[164:167], v[124:127], v[40:43]\n"
      "ds_read_b128 a[192:195], v254 offset:49152\n"
      "v_mfma_f32_16x16x32_bf16 v[40:43], a[168:171], v[176:179], v[40:43]\n"
      "v_cvt_pk_bf16_f32 v110, v220, v221\n"
      "v_cvt_pk_bf16_f32 v72, v216, v217\n"
      "v_mfma_f32_16x16x32_bf16 v[40:43], a[172:175], v[244:247], v[40:43]\n"
      "ds_read_b128 a[220:223], v254 offset:56320\n"
      "s_waitcnt lgkmcnt(6)\n"
      "v_mfma_f32_16x16x32_bf16 v[180:183], a[176:179], v[92:95], 0\n"
      "ds_read_b128 a[244:247], v254 offset:62464\n"
      "v_mfma_f32_16x16x32_bf16 v[180:183], a[180:183], v[124:127], v[180:183]\n"
      "v_mfma_f32_16x16x32_bf16 v[180:183], a[184:187], v[176:179], v[180:183]\n"
      "v_cvt_pk_bf16_f32 v216, v30, v31\n"
      "v_mfma_f32_16x16x32_bf16 v[180:183], a[188:191], v[244:247], v[180:183]\n"
      "v_cvt_pk_bf16_f32 v54, v204, v205\n"
      "v_mfma_f32_16x16x32_bf16 v[144:147], a[176:179], v[228:231], 0\n"
      "v_mfma_f32_16x16x32_bf16 v[144:147], a[180:183], v[132:135], v[144:147]\n"
      "s_waitcnt vmcnt(10)\n"
      "ds_read_b128 v[232:235], v0 offset:4096\n"
      "ds_read_b128 v[80:83], v0 offset:5632\n"
      "v_mfma_f32_16x16x32_bf16 v[144:147], a[184:187], v[18:21], v[144:147]\n"
      "ds_read_b128 v[184:187], v0 offset:4608\n"
      "ds_read_b128 v[60:63], v0 offset:5120\n"
      "v_mfma_f32_16x16x32_bf16 v[144:147], a[188:191], v[64:67], v[144:147]\n"
      "buffer_load_dwordx4 v1, s[40:43], s91 offen offset:1024 lds\n"
      "v_mfma_f32_16x16x32_bf16 v[24:27], v[228:231], a[208:211], 0\n"
      "v_cvt_pk_bf16_f32 v139, v226, v227\n"
      "v_mfma_f32_16x16x32_bf16 v[24:27], v[132:135], a[212:215], v[24:27]\n"
      "ds_read_b128 a[236:239], v254 offset:60416\n"
      "s_waitcnt lgkmcnt(8)\n"
      "v_mfma_f32_16x16x32_bf16 v[24:27], v[18:21], a[216:219], v[24:27]\n"
      "s_waitcnt lgkmcnt(6)\n"
      "v_mfma_f32_16x16x32_bf16 v[24:27], v[64:67], a[220:223], v[24:27]\n"
      "buffer_load_dwordx4 v1, s[40:43], s91 offen offset:3072 lds\n"
      "v_mfma_f32_16x16x32_bf16 v[208:211], v[228:231], a[240:243], 0\n"
      "s_waitcnt lgkmcnt(5)\n"
      "v_mfma_f32_16x16x32_bf16 v[208:211], v[132:135], a[244:247], v[208:211]\n"
      "v_cvt_pk_bf16_f32 v23, v88, v89\n"
      "v_mfma_f32_16x16x32_bf16 v[208:211], v[18:21], a[248:251], v[208:211]\n"
      "s_add_u32 m0, s53, 4096\n"
      "ds_read_b128 a[200:203], v254 offset:51200\n"
      "v_cvt_pk_bf16_f32 v47, v86, v87\n"
      "v_mfma_f32_16x16x32_bf16 v[208:211], v[64:67], a[252:255], v[208:211]\n"
      "ds_read_b128 a[204:207], v254 offset:52224\n"
      "s_waitcnt lgkmcnt(6)\n"
      "v_mfma_f32_16x16x32_bf16 v[236:239], v[232:235], a[80:83], 0\n"
      "v_cvt_pk_bf16_f32 v255, v144, v145\n"
      "s_waitcnt lgkmcnt(4)\n"
      "v_mfma_f32_16x16x32_bf16 v[236:239], v[184:187], a[84:87], v[236:239]\n"
      "v_cvt_pk_bf16_f32 v99, v160, v161\n"
      "s_waitcnt lgkmcnt(3)\n"
      "v_mfma_f32_16x16x32_bf16 v[236:239], v[60:63], a[88:91], v[236:239]\n"
      "v_mfma_f32_16x16x32_bf16 v[236:239], v[80:83], a[92:95], v[236:239]\n"
      "v_cvt_pk_bf16_f32 v106, v188, v189\n"
      "v_mfma_f32_16x16x32_bf16 v[36:39], a[176:179], v[232:235], 0\n"
      "v_mfma_f32_16x16x32_bf16 v[36:39], a[180:183], v[184:187], v[36:39]\n"
      "v_mfma_f32_16x16x32_bf16 v[36:39], a[184:187], v[60:63], v[36:39]\n"
      "ds_read_b128 a[196:199], v254 offset:50176\n"
      "v_mfma_f32_16x16x32_bf16 v[36:39], a[188:191], v[80:83], v[36:39]\n"
      "v_cvt_pk_bf16_f32 v217, v90, v91\n"
      "v_cvt_pk_bf16_f32 v137, v166, v167\n"
      "v_mfma_f32_16x16x32_bf16 v[166:169], v[232:235], a[96:99], 0\n"
      "v_mfma_f32_16x16x32_bf16 v[166:169], v[184:187], a[100:103], v[166:169]\n"
      "v_cvt_pk_bf16_f32 v46, v84, v85\n"
      "v_mfma_f32_16x16x32_bf16 v[166:169], v[60:63], a[104:107], v[166:169]\n"
      "ds_read_b128 a[224:227], v254 offset:57344\n"
      "v_mfma_f32_16x16x32_bf16 v[166:169], v[80:83], a[108:111], v[166:169]\n"
      "v_cvt_pk_bf16_f32 v115, v130, v131\n"
      "v_mfma_f32_16x16x32_bf16 v[160:163], a[32:35], v[232:235], 0\n"
      "v_mfma_f32_16x16x32_bf16 v[160:163], a[36:39], v[184:187], v[160:163]\n"
      "v_mfma_f32_16x16x32_bf16 v[160:163], a[40:43], v[60:63], v[160:163]\n"
      "ds_read_b128 a[228:231], v254 offset:58368\n"
      "v_mfma_f32_16x16x32_bf16 v[160:163], a[44:47], v[80:83], v[160:163]\n"
      "v_mfma_f32_16x16x32_bf16 v[240:243], v[232:235], a[112:115], 0\n"
      "v_cvt_pk_bf16_f32 v254, v152, v153\n"
      "v_mfma_f32_16x16x32_bf16 v[240:243], v[184:187], a[116:119], v[240:243]\n"
      "v_mfma_f32_16x16x32_bf16 v[240:243], v[60:63], a[120:123], v[240:243]\n"
      "v_cvt_pk_bf16_f32 v111, v222, v223\n"
      "v_cvt_pk_bf16_f32 v204, v166, v167\n"
      "v_mfma_f32_16x16x32_bf16 v[240:243], v[80:83], a[124:127], v[240:243]\n"
      "v_mfma_f32_16x16x32_bf16 v[188:191], v[232:235], a[192:195], 0\n"
      "v_cvt_pk_bf16_f32 v100, v40, v41\n"
      "s_waitcnt lgkmcnt(2)\n"
      "v_mfma_f32_16x16x32_bf16 v[188:191], v[184:187], a[196:199], v[188:191]\n"
      "buffer_load_dwordx4 v1, s[40:43], s92 offen offset:0 lds\n"
      "v_mfma_f32_16x16x32_bf16 v[188:191], v[60:63], a[200:203], v[188:191]\n"
      "buffer_load_dwordx4 v1, s[40:43], s92 offen offset:2048 lds\n"
      "v_mfma_f32_16x16x32_bf16 v[188:191], v[80:83], a[204:207], v[188:191]\n"
      "v_mfma_f32_16x16x32_bf16 v[56:59], a[48:51], v[232:235], 0\n"
      "v_cvt_pk_bf16_f32 v138, v224, v225\n"
      "v_mfma_f32_16x16x32_bf16 v[56:59], a[52:55], v[184:187], v[56:59]\n"
      "v_cvt_pk_bf16_f32 v22, v28, v29\n"
      "v_mfma_f32_16x16x32_bf16 v[56:59], a[56:59], v[60:63], v[56:59]\n"
      "buffer_load_dwordx4 v1, s[40:43], s93 offen offset:1024 lds\n"
      "v_mfma_f32_16x16x32_bf16 v[56:59], a[60:63], v[80:83], v[56:59]\n"
      "v_cvt_pk_bf16_f32 v205, v168, v169\n"
      "v_mfma_f32_16x16x32_bf16 v[166:169], a[128:131], v[232:235], 0\n"
      "v_mfma_f32_16x16x32_bf16 v[166:169], a[132:135], v[184:187], v[166:169]\n"
      "v_mfma_f32_16x16x32_bf16 v[166:169], a[136:139], v[60:63], v[166:169]\n"
      "v_mfma_f32_16x16x32_bf16 v[166:169], a[140:143], v[80:83], v[166:169]\n"
      "v_cvt_pk_bf16_f32 v141, v194, v195\n"
      "s_waitcnt lgkmcnt(1)\n"
      "v_mfma_f32_16x16x32_bf16 v[88:91], v[228:231], a[224:227], 0\n"
      "s_waitcnt lgkmcnt(0)\n"
      "v_mfma_f32_16x16x32_bf16 v[88:91], v[132:135], a[228:231], v[88:91]\n"
      "v_mfma_f32_16x16x32_bf16 v[88:91], v[18:21], a[232:235], v[88:91]\n"
      "v_mfma_f32_16x16x32_bf16 v[88:91], v[64:67], a[236:239], v[88:91]\n"
      "s_barrier\n"
      "v_mfma_f32_16x16x32_bf16 v[220:223], v[232:235], a[64:67], 0\n"
      "v_mfma_f32_16x16x32_bf16 v[220:223], v[184:187], a[68:71], v[220:223]\n"
      "v_cvt_pk_bf16_f32 v118, v68, v69\n"
      "v_mfma_f32_16x16x32_bf16 v[220:223], v[60:63], a[72:75], v[220:223]\n"
      "v_mfma_f32_16x16x32_bf16 v[220:223], v[80:83], a[76:79], v[220:223]\n"
      "s_waitcnt vmcnt(11)\n"
      "ds_read_b128 v[196:199], v0 offset:16384\n"
      "v_mfma_f32_16x16x32_bf16 v[224:227], a[16:19], v[232:235], 0\n"
      "v_mfma_f32_16x16x32_bf16 v[224:227], a[20:23], v[184:187], v[224:227]\n"
      "buffer_load_dwordx4 v1, s[40:43], s93 offen offset:3072 lds\n"
      "v_mfma_f32_16x16x32_bf16 v[224:227], a[24:27], v[60:63], v[224:227]\n"
      "v_cvt_pk_bf16_f32 v112, v154, v155\n"
      "v_cvt_pk_bf16_f32 v140, v192, v193\n"
      "v_mfma_f32_16x16x32_bf16 v[224:227], a[28:31], v[80:83], v[224:227]\n"
      "v_cvt_pk_bf16_f32 v102, v180, v181\n"
      "v_mfma_f32_16x16x32_bf16 v[48:51], a[144:147], v[232:235], 0\n"
      "ds_read_b128 v[200:203], v0 offset:17920\n"
      "v_cvt_pk_bf16_f32 v69, v222, v223\n"
      "v_mfma_f32_16x16x32_bf16 v[48:51], a[148:151], v[184:187], v[48:51]\n"
      "ds_read_b128 v[152:155], v0 offset:17408\n"
      "v_cvt_pk_bf16_f32 v55, v206, v207\n"
      "v_mfma_f32_16x16x32_bf16 v[48:51], a[152:155], v[60:63], v[48:51]\n"
      "ds_read_b128 v[84:87], v0 offset:16896\n"
      "v_mfma_f32_16x16x32_bf16 v[48:51], a[156:159], v[80:83], v[48:51]\n"
      "ds_write_b128 v251, v[104:107] offset:0\n"
      "v_cvt_pk_bf16_f32 v143, v78, v79\n"
      "v_mfma_f32_16x16x32_bf16 v[32:35], v[228:231], a[192:195], 0\n"
      "v_mfma_f32_16x16x32_bf16 v[32:35], v[132:135], a[196:199], v[32:35]\n"
      "v_cvt_pk_bf16_f32 v45, v226, v227\n"
      "v_mfma_f32_16x16x32_bf16 v[32:35], v[18:21], a[200:203], v[32:35]\n"
      "v_mfma_f32_16x16x32_bf16 v[32:35], v[64:67], a[204:207], v[32:35]\n"
      "s_add_u32 m0, s53, 0\n"
      "ds_write_b64 v253, v[204:205] offset:2304\n"
      "v_cvt_pk_bf16_f32 v106, v160, v161\n"
      "v_mfma_f32_16x16x32_bf16 v[170:173], v[232:235], a[208:211], 0\n"
      "v_mfma_f32_16x16x32_bf16 v[170:173], v[184:187], a[212:215], v[170:173]\n"
      "v_cvt_pk_bf16_f32 v108, v56, v57\n"
      "v_mfma_f32_16x16x32_bf16 v[170:173], v[60:63], a[216:219], v[170:173]\n"
      "v_mov_b64 v[248:249], 0\n"
      "v_cvt_pk_bf16_f32 v57, v89, v209\n"
      "v_mfma_f32_16x16x32_bf16 v[170:173], v[80:83], a[220:223], v[170:173]\n"
      "s_waitcnt lgkmcnt(5)\n"
      "v_mfma_f32_16x16x32_bf16 v[64:67], v[196:199], a[112:115], 0\n"
      "s_waitcnt lgkmcnt(2)\n"
      "v_mfma_f32_16x16x32_bf16 v[64:67], v[84:87], a[116:119], v[64:67]\n"
      "v_mfma_f32_16x16x32_bf16 v[64:67], v[152:155], a[120:123], v[64:67]\n"
      "v_cvt_pk_bf16_f32 v56, v33, v25\n"
      "v_cvt_pk_bf16_f32 v107, v162, v163\n"
      "v_mfma_f32_16x16x32_bf16 v[64:67], v[200:203], a[124:127], v[64:67]\n"
      "buffer_load_dwordx4 v1, s[40:43], s94 offen offset:0 lds\n"
      "v_cvt_pk_bf16_f32 v68, v220, v221\n"
      "v_mfma_f32_16x16x32_bf16 v[18:21], a[0:3], v[196:199], 0\n"
      "v_mfma_f32_16x16x32_bf16 v[18:21], a[4:7], v[84:87], v[18:21]\n"
      "v_cvt_pk_bf16_f32 v204, v168, v169\n"
      "v_mfma_f32_16x16x32_bf16 v[18:21], a[8:11], v[152:155], v[18:21]\n"
      "v_cvt_pk_bf16_f32 v156, v188, v170\n"
      "v_mfma_f32_16x16x32_bf16 v[18:21], a[12:15], v[200:203], v[18:21]\n"
      "ds_write_b64 v253, v[116:117] offset:128\n"
      "v_cvt_pk_bf16_f32 v119, v70, v71\n"
      "v_mfma_f32_16x16x32_bf16 v[76:79], a[160:163], v[232:235], 0\n"
      "v_cvt_pk_bf16_f32 v103, v182, v183\n"
      "v_cvt_pk_bf16_f32 v73, v218, v219\n"
      "v_mfma_f32_16x16x32_bf16 v[76:79], a[164:167], v[184:187], v[76:79]\n"
      "v_cvt_pk_bf16_f32 v218, v34, v26\n"
      "v_mfma_f32_16x16x32_bf16 v[76:79], a[168:171], v[60:63], v[76:79]\n"
      "v_mfma_f32_16x16x32_bf16 v[76:79], a[172:175], v[80:83], v[76:79]\n"
      "ds_write_b64 v252, v[216:217] offset:512\n"
      "v_mfma_f32_16x16x32_bf16 v[160:163], a[160:163], v[196:199], 0\n"
      "v_cvt_pk_bf16_f32 v136, v164, v165\n"
      "v_mfma_f32_16x16x32_bf16 v[160:163], a[164:167], v[84:87], v[160:163]\n"
      "v_cvt_pk_bf16_f32 v230, v148, v149\n"
      "v_mfma_f32_16x16x32_bf16 v[160:163], a[168:171], v[152:155], v[160:163]\n"
      "v_mfma_f32_16x16x32_bf16 v[160:163], a[172:175], v[200:203], v[160:163]\n"
      "ds_write_b64 v253, v[68:69] offset:2048\n"
      "v_cvt_pk_bf16_f32 v33, v48, v49\n"
      "v_mfma_f32_16x16x32_bf16 v[180:183], v[232:235], a[240:243], 0\n"
      "v_cvt_pk_bf16_f32 v195, v88, v208\n"
      "v_cvt_pk_bf16_f32 v44, v224, v225\n"
      "v_mfma_f32_16x16x32_bf16 v[180:183], v[184:187], a[244:247], v[180:183]\n"
      "v_cvt_pk_bf16_f32 v113, v146, v147\n"
      "v_mfma_f32_16x16x32_bf16 v[180:183], v[60:63], a[248:251], v[180:183]\n"
      "buffer_load_dwordx4 v1, s[40:43], s94 offen offset:2048 lds\n"
      "v_mfma_f32_16x16x32_bf16 v[180:183], v[80:83], a[252:255], v[180:183]\n"
      "v_cvt_pk_bf16_f32 v101, v42, v43\n"
      "v_cvt_pk_bf16_f32 v105, v91, v211\n"
      "v_mfma_f32_16x16x32_bf16 v[220:223], a[144:147], v[196:199], 0\n"
      "buffer_load_dwordx4 v1, s[40:43], s95 offen offset:1024 lds\n"
      "v_mfma_f32_16x16x32_bf16 v[220:223], a[148:151], v[84:87], v[220:223]\n"
      "v_cvt_pk_bf16_f32 v231, v150, v151\n"
      "v_cvt_pk_bf16_f32 v193, v162, v163\n"
      "v_mfma_f32_16x16x32_bf16 v[220:223], a[152:155], v[152:155], v[220:223]\n"
      "v_mfma_f32_16x16x32_bf16 v[220:223], a[156:159], v[200:203], v[220:223]\n"
      "v_cvt_pk_bf16_f32 v104, v35, v27\n"
      "v_cvt_pk_bf16_f32 v97, v212, v213\n"
      "v_mfma_f32_16x16x32_bf16 v[68:71], v[196:199], a[96:99], 0\n"
      "buffer_load_dwordx4 v1, s[40:43], s95 offen offset:3072 lds\n"
      "v_mfma_f32_16x16x32_bf16 v[68:71], v[84:87], a[100:103], v[68:71]\n"
      "v_mfma_f32_16x16x32_bf16 v[68:71], v[152:155], a[104:107], v[68:71]\n"
      "v_cvt_pk_bf16_f32 v28, v189, v171\n"
      "v_mfma_f32_16x16x32_bf16 v[68:71], v[200:203], a[108:111], v[68:71]\n"
      "v_cvt_pk_bf16_f32 v219, v90, v210\n"
      "v_mfma_f32_16x16x32_bf16 v[208:211], a[128:131], v[196:199], 0\n"
      "v_mfma_f32_16x16x32_bf16 v[208:211], a[132:135], v[84:87], v[208:211]\n"
      "v_cvt_pk_bf16_f32 v194, v32, v24\n"
      "v_mfma_f32_16x16x32_bf16 v[208:211], a[136:139], v[152:155], v[208:211]\n"
      "v_mfma_f32_16x16x32_bf16 v[208:211], a[140:143], v[200:203], v[208:211]\n"
      "ds_write_b64 v253, v[110:111] offset:384\n"
      "v_cvt_pk_bf16_f32 v48, v190, v172\n"
      "v_mfma_f32_16x16x32_bf16 v[24:27], a[0:3], v[232:235], 0\n"
      "v_cvt_pk_bf16_f32 v32, v166, v167\n"
      "v_cvt_pk_bf16_f32 v116, v68, v69\n"
      "v_mfma_f32_16x16x32_bf16 v[24:27], a[4:7], v[184:187], v[24:27]\n"
      "v_mfma_f32_16x16x32_bf16 v[24:27], a[8:11], v[60:63], v[24:27]\n"
      "s_waitcnt vmcnt(12)\n"
      "ds_read_b128 v[128:131], v0 offset:12288\n"
      "ds_read_b128 v[224:227], v0 offset:13824\n"
      "v_mfma_f32_16x16x32_bf16 v[24:27], a[12:15], v[80:83], v[24:27]\n"
      "ds_write_b64 v253, v[218:219] offset:4352\n"
      "v_cvt_pk_bf16_f32 v31, v38, v39\n"
      "v_cvt_pk_bf16_f32 v228, v64, v65\n"
      "v_mfma_f32_16x16x32_bf16 v[88:91], a[16:19], v[196:199], 0\n"
      "ds_read_b128 v[132:135], v0 offset:13312\n"
      "v_mfma_f32_16x16x32_bf16 v[88:91], a[20:23], v[84:87], v[88:91]\n"
      "v_cvt_pk_bf16_f32 v74, v52, v53\n"
      "s_add_u32 m0, s53, 20480\n"
      "v_mfma_f32_16x16x32_bf16 v[88:91], a[24:27], v[152:155], v[88:91]\n"
      "v_cvt_pk_bf16_f32 v109, v58, v59\n"
      "v_mfma_f32_16x16x32_bf16 v[88:91], a[28:31], v[200:203], v[88:91]\n"
      "ds_write_b64 v252, v[32:33] offset:256\n"
      "v_mfma_f32_16x16x32_bf16 v[216:219], v[232:235], a[224:227], 0\n"
      "ds_read_b128 v[144:147], v0 offset:12800\n"
      "v_cvt_pk_bf16_f32 v168, v236, v237\n"
      "v_mfma_f32_16x16x32_bf16 v[216:219], v[184:187], a[228:231], v[216:219]\n"
      "v_cvt_pk_bf16_f32 v150, v240, v241\n"
      "v_mfma_f32_16x16x32_bf16 v[216:219], v[60:63], a[232:235], v[216:219]\n"
      "v_cvt_pk_bf16_f32 v229, v66, v67\n"
      "v_mfma_f32_16x16x32_bf16 v[216:219], v[80:83], a[236:239], v[216:219]\n"
      "ds_write_b64 v253, v[104:105] offset:4480\n"
      "v_cvt_pk_bf16_f32 v190, v220, v221\n"
      "v_mfma_f32_16x16x32_bf16 v[80:83], a[32:35], v[196:199], 0\n"
      "v_mfma_f32_16x16x32_bf16 v[80:83], a[36:39], v[84:87], v[80:83]\n"
      "v_cvt_pk_bf16_f32 v66, v88, v89\n"
      "v_cvt_pk_bf16_f32 v162, v191, v173\n"
      "v_mfma_f32_16x16x32_bf16 v[80:83], a[40:43], v[152:155], v[80:83]\n"
      "s_add_u32 s40, s40, 102400\n s_addc_u32 s41, s41, 0\n"
      "v_mfma_f32_16x16x32_bf16 v[80:83], a[44:47], v[200:203], v[80:83]\n"
      "ds_write_b128 v251, v[106:109] offset:2304\n"
      "v_cvt_pk_bf16_f32 v65, v20, v21\n"
      "v_cvt_pk_bf16_f32 v191, v222, v223\n"
      "s_waitcnt lgkmcnt(7)\n"
      "v_mfma_f32_16x16x32_bf16 v[164:167], a[16:19], v[128:131], 0\n"
      "s_waitcnt lgkmcnt(2)\n"
      "v_mfma_f32_16x16x32_bf16 v[164:167], a[20:23], v[144:147], v[164:167]\n"
      "v_mfma_f32_16x16x32_bf16 v[164:167], a[24:27], v[132:135], v[164:167]\n"
      "v_mfma_f32_16x16x32_bf16 v[164:167], a[28:31], v[224:227], v[164:167]\n"
      "buffer_load_dwordx4 v1, s[40:43], s73 offen offset:0 lds\n"
      "v_mfma_f32_16x16x32_bf16 v[60:63], a[48:51], v[128:131], 0\n"
      "v_cvt_pk_bf16_f32 v69, v82, v83\n"
      "v_mfma_f32_16x16x32_bf16 v[60:63], a[52:55], v[144:147], v[60:63]\n"
      "v_cvt_pk_bf16_f32 v117, v70, v71\n"
      "v_mfma_f32_16x16x32_bf16 v[60:63], a[56:59], v[132:135], v[60:63]\n"
      "v_cvt_pk_bf16_f32 v188, v208, v209\n"
      "v_mov_b64 v[40:41], 0\n"
      "v_mfma_f32_16x16x32_bf16 v[60:63], a[60:63], v[224:227], v[60:63]\n"
      "ds_write_b64 v253, v[56:57] offset:4224\n"
      "v_mfma_f32_16x16x32_bf16 v[184:187], a[0:3], v[128:131], 0\n"
      "v_cvt_pk_bf16_f32 v67, v90, v91\n"
      "v_cvt_pk_bf16_f32 v98, v158, v159\n"
      "v_mfma_f32_16x16x32_bf16 v[184:187], a[4:7], v[144:147], v[184:187]\n"
      "v_mfma_f32_16x16x32_bf16 v[184:187], a[8:11], v[132:135], v[184:187]\n"
      "v_cvt_pk_bf16_f32 v157, v216, v180\n"
      "v_mfma_f32_16x16x32_bf16 v[184:187], a[12:15], v[224:227], v[184:187]\n"
      "ds_write_b64 v252, v[112:113] offset:2560\n"
      "v_cvt_pk_bf16_f32 v83, v62, v63\n"
      "v_mfma_f32_16x16x32_bf16 v[88:91], v[196:199], a[64:67], 0\n"
      "v_mfma_f32_16x16x32_bf16 v[88:91], v[84:87], a[68:71], v[88:91]\n"
      "buffer_load_dwordx4 v1, s[40:43], s73 offen offset:2048 lds\n"
      "v_mfma_f32_16x16x32_bf16 v[88:91], v[152:155], a[72:75], v[88:91]\n"
      "v_mfma_f32_16x16x32_bf16 v[88:91], v[200:203], a[76:79], v[88:91]\n"
      "ds_write_b64 v253, v[194:195] offset:4096\n"
      "v_mfma_f32_16x16x32_bf16 v[232:235], v[136:139], v[96:99], 0\n"
      "v_cvt_pk_bf16_f32 v68, v80, v81\n"
      "v_cvt_pk_bf16_f32 v195, v36, v37\n"
      "v_mfma_f32_16x16x32_bf16 v[232:235], v[140:143], v[100:103], v[232:235]\n"
      "v_cvt_pk_bf16_f32 v194, v76, v77\n"
      "v_mfma_f32_16x16x32_bf16 v[206:209], a[48:51], v[196:199], 0\n"
      "v_mfma_f32_16x16x32_bf16 v[206:209], a[52:55], v[84:87], v[206:209]\n"
      "v_cvt_pk_bf16_f32 v151, v242, v243\n"
      "v_cvt_pk_bf16_f32 v49, v218, v182\n"
      "v_mfma_f32_16x16x32_bf16 v[206:209], a[56:59], v[152:155], v[206:209]\n"
      "v_cvt_pk_bf16_f32 v29, v217, v181\n"
      "v_mfma_f32_16x16x32_bf16 v[206:209], a[60:63], v[200:203], v[206:209]\n"
      "ds_write_b64 v253, v[156:157] offset:6144\n"
      "s_waitcnt vmcnt(10)\n"
      "ds_read_b128 v[212:215], v0 offset:8192\n"
      "v_mfma_f32_16x16x32_bf16 v[220:223], v[128:131], a[80:83], 0\n"
      "v_cvt_pk_bf16_f32 v30, v78, v79\n"
      "v_cvt_pk_bf16_f32 v123, v120, v121\n"
      "v_mfma_f32_16x16x32_bf16 v[220:223], v[144:147], a[84:87], v[220:223]\n"
      "v_cvt_pk_bf16_f32 v205, v50, v51\n"
      "v_cvt_pk_bf16_f32 v169, v238, v239\n"
      "v_mfma_f32_16x16x32_bf16 v[220:223], v[132:135], a[88:91], v[220:223]\n"
      "ds_read_b128 v[56:59], v0 offset:8704\n"
      "ds_read_b128 v[106:109], v0 offset:9728\n"
      "v_mfma_f32_16x16x32_bf16 v[220:223], v[224:227], a[92:95], v[220:223]\n"
      "ds_write_b64 v253, v[48:49] offset:6400\n"
      "ds_read_b128 v[36:39], v0 offset:9216\n"
      "v_mfma_f32_16x16x32_bf16 v[156:159], v[196:199], a[80:83], 0\n"
      "v_mfma_f32_16x16x32_bf16 v[156:159], v[84:87], a[84:87], v[156:159]\n"
      "v_mfma_f32_16x16x32_bf16 v[156:159], v[152:155], a[88:91], v[156:159]\n"
      "v_cvt_pk_bf16_f32 v71, v208, v209\n"
      "v_cvt_pk_bf16_f32 v70, v206, v207\n"
      "v_mfma_f32_16x16x32_bf16 v[156:159], v[200:203], a[92:95], v[156:159]\n"
      "v_cvt_pk_bf16_f32 v77, v186, v187\n"
      "v_mfma_f32_16x16x32_bf16 v[236:239], v[128:131], a[96:99], 0\n"
      "v_cvt_pk_bf16_f32 v64, v18, v19\n"
      "v_mfma_f32_16x16x32_bf16 v[236:239], v[144:147], a[100:103], v[236:239]\n"
      "v_mfma_f32_16x16x32_bf16 v[236:239], v[132:135], a[104:107], v[236:239]\n"
      "buffer_load_dwordx4 v1, s[40:43], s74 offen offset:1024 lds\n"
      "v_mfma_f32_16x16x32_bf16 v[236:239], v[224:227], a[108:111], v[236:239]\n"
      "ds_write_b64 v253, v[114:115] offset:0\n"
      "v_cvt_pk_bf16_f32 v78, v164, v165\n"
      "v_mfma_f32_16x16x32_bf16 v[32:35], v[64:67], v[96:99], 0\n"
      "v_cvt_pk_bf16_f32 v120, v156, v157\n"
      "v_cvt_pk_bf16_f32 v192, v160, v161\n"
      "v_mfma_f32_16x16x32_bf16 v[32:35], v[68:71], v[100:103], v[32:35]\n"
      "s_waitcnt lgkmcnt(5)\n"
      "v_mfma_f32_16x16x32_bf16 v[206:209], a[48:51], v[212:215], 0\n"
      "s_waitcnt lgkmcnt(4)\n"
      "v_mfma_f32_16x16x32_bf16 v[206:209], a[52:55], v[56:59], v[206:209]\n"
      "v_cvt_pk_bf16_f32 v163, v219, v183\n"
      "s_waitcnt lgkmcnt(1)\n"
      "v_mfma_f32_16x16x32_bf16 v[206:209], a[56:59], v[36:39], v[206:209]\n"
      "v_cvt_pk_bf16_f32 v62, v236, v237\n"
      "v_mfma_f32_16x16x32_bf16 v[206:209], a[60:63], v[106:109], v[206:209]\n"
      "ds_write_b64 v252, v[22:23] offset:0\n"
      "v_cvt_pk_bf16_f32 v53, v90, v91\n"
      "v_mfma_f32_16x16x32_bf16 v[110:113], a[32:35], v[128:131], 0\n"
      "v_exp_f32 v35, v35\n"
      "v_mfma_f32_16x16x32_bf16 v[110:113], a[36:39], v[144:147], v[110:113]\n"
      "v_cvt_pk_bf16_f32 v42, v24, v25\n"
      "v_mfma_f32_16x16x32_bf16 v[110:113], a[40:43], v[132:135], v[110:113]\n"
      "v_exp_f32 v32, v32\n"
      "v_mfma_f32_16x16x32_bf16 v[110:113], a[44:47], v[224:227], v[110:113]\n"
      "ds_write_b64 v253, v[150:151] offset:2432\n"
      "v_cvt_pk_bf16_f32 v186, v220, v221\n"
      "v_mfma_f32_16x16x32_bf16 v[216:219], v[212:215], a[80:83], 0\n"
      "v_cvt_pk_bf16_f32 v79, v166, v167\n"
      "v_mfma_f32_16x16x32_bf16 v[216:219], v[56:59], a[84:87], v[216:219]\n"
      "v_mfma_f32_16x16x32_bf16 v[216:219], v[36:39], a[88:91], v[216:219]\n"
      "v_mfma_f32_16x16x32_bf16 v[216:219], v[106:109], a[92:95], v[216:219]\n"
      "v_exp_f32 v233, v233\n"
      "v_mfma_f32_16x16x32_bf16 v[240:243], v[212:215], a[96:99], 0\n"
      "v_cvt_pk_bf16_f32 v82, v60, v61\n"
      "v_mfma_f32_16x16x32_bf16 v[240:243], v[56:59], a[100:103], v[240:243]\n"
      "v_mfma_f32_16x16x32_bf16 v[240:243], v[36:39], a[104:107], v[240:243]\n"
      "v_mfma_f32_16x16x32_bf16 v[240:243], v[106:109], a[108:111], v[240:243]\n"
      "ds_write_b64 v252, v[254:255] offset:2048\n"
      "v_mfma_f32_16x16x32_bf16 v[48:51], a[32:35], v[212:215], 0\n"
      "v_mfma_f32_16x16x32_bf16 v[48:51], a[36:39], v[56:59], v[48:51]\n"
      "v_mfma_f32_16x16x32_bf16 v[48:51], a[40:43], v[36:39], v[48:51]\n"
      "v_cvt_pk_bf16_f32 v121, v158, v159\n"
      "v_mfma_f32_16x16x32_bf16 v[48:51], a[44:47], v[106:109], v[48:51]\n"
      "ds_write_b64 v253, v[46:47] offset:256\n"
      "v_mfma_f32_16x16x32_bf16 v[22:25], v[128:131], a[112:115], 0\n"
      "v_exp_f32 v235, v235\n"
      "v_mfma_f32_16x16x32_bf16 v[22:25], v[144:147], a[116:119], v[22:25]\n"
      "v_cvt_pk_bf16_f32 v61, v242, v243\n"
      "v_mfma_f32_16x16x32_bf16 v[22:25], v[132:135], a[120:123], v[22:25]\n"
      "v_exp_f32 v33, v33\n"
      "v_mfma_f32_16x16x32_bf16 v[22:25], v[224:227], a[124:127], v[22:25]\n"
      "ds_write_b64 v253, v[162:163] offset:6528\n"
      "v_cvt_pk_bf16_f32 v187, v222, v223\n"
      "v_cvt_pk_bf16_f32 v189, v210, v211\n"
      "v_mfma_f32_16x16x32_bf16 v[156:159], v[212:215], a[64:67], 0\n"
      "buffer_load_dwordx4 v1, s[40:43], s74 offen offset:3072 lds\n"
      "v_mfma_f32_16x16x32_bf16 v[156:159], v[56:59], a[68:71], v[156:159]\n"
      "v_mfma_f32_16x16x32_bf16 v[156:159], v[36:39], a[72:75], v[156:159]\n"
      "v_exp_f32 v34, v34\n"
      "v_mfma_f32_16x16x32_bf16 v[156:159], v[106:109], a[76:79], v[156:159]\n"
      "ds_write_b64 v253, v[28:29] offset:6272\n"
      "v_mfma_f32_16x16x32_bf16 v[148:151], v[212:215], a[112:115], 0\n"
      "v_cvt_pk_bf16_f32 v43, v26, v27\n"
      "v_cvt_pk_bf16_f32 v63, v238, v239\n"
      "v_mfma_f32_16x16x32_bf16 v[148:151], v[56:59], a[116:119], v[148:151]\n"
      "v_cvt_pk_bf16_f32 v239, v24, v25\n"
      "v_mfma_f32_16x16x32_bf16 v[148:151], v[36:39], a[120:123], v[148:151]\n"
      "s_waitcnt vmcnt(8)\n"
      "ds_read_b128 v[164:167], v0 offset:4096\n"
      "ds_read_b128 v[180:183], v0 offset:5120\n"
      "v_mfma_f32_16x16x32_bf16 v[148:151], v[106:109], a[124:127], v[148:151]\n"
      "v_mfma_f32_16x16x32_bf16 v[18:21], a[16:19], v[212:215], 0\n"
      "v_mfma_f32_16x16x32_bf16 v[18:21], a[20:23], v[56:59], v[18:21]\n"
      "v_exp_f32 v232, v232\n"
      "v_mfma_f32_16x16x32_bf16 v[18:21], a[24:27], v[36:39], v[18:21]\n"
      "v_exp_f32 v234, v234\n"
      "v_mfma_f32_16x16x32_bf16 v[18:21], a[28:31], v[106:109], v[18:21]\n"
      "s_add_u32 m0, s53, 16384\n"
      "ds_write_b128 v251, v[72:75] offset:2048\n"
      "ds_read_b128 v[170:173], v0 offset:4608\n"
      "v_cvt_pk_bf16_f32 v76, v184, v185\n"
      "v_mfma_f32_16x16x32_bf16 v[160:163], a[0:3], v[212:215], 0\n"
      "ds_read_b128 v[220:223], v10 offset:0\n"
      "ds_read_b128 v[212:215], v0 offset:5632\n"
      "v_mfma_f32_16x16x32_bf16 v[160:163], a[4:7], v[56:59], v[160:163]\n"
      "v_cvt_pk_bf16_f32 v185, v218, v219\n"
      "v_mfma_f32_16x16x32_bf16 v[160:163], a[8:11], v[36:39], v[160:163]\n"
      "v_cvt_pk_bf16_f32 v184, v216, v217\n"
      "v_cvt_pk_bf16_f32 v238, v22, v23\n"
      "v_mfma_f32_16x16x32_bf16 v[160:163], a[12:15], v[106:109], v[160:163]\n"
      "v_cvt_pk_bf16_f32 v80, v110, v111\n"
      "v_mfma_f32_16x16x32_bf16 v[216:219], v[128:131], a[64:67], 0\n"
      "v_mfma_f32_16x16x32_bf16 v[216:219], v[144:147], a[68:71], v[216:219]\n"
      "buffer_load_dwordx4 v1, s[40:43], s75 offen offset:0 lds\n"
      "v_mfma_f32_16x16x32_bf16 v[216:219], v[132:135], a[72:75], v[216:219]\n"
      "v_cvt_pk_bf16_f32 v60, v240, v241\n"
      "v_cvt_pk_bf16_f32 v23, v20, v21\n"
      "v_mfma_f32_16x16x32_bf16 v[216:219], v[224:227], a[76:79], v[216:219]\n"
      "ds_write_b128 v251, v[42:45] offset:256\n"
      "ds_read2_b32 v[72:73], v16 offset0:0 offset1:2\n"
      "v_cvt_pk_bf16_f32 v236, v148, v149\n"
      "s_waitcnt lgkmcnt(7)\n"
      "v_mfma_f32_16x16x32_bf16 v[240:243], a[32:35], v[164:167], 0\n"
      "v_cvt_pk_bf16_f32 v149, v34, v35\n"
      "s_waitcnt lgkmcnt(4)\n"
      "v_mfma_f32_16x16x32_bf16 v[240:243], a[36:39], v[170:173], v[240:243]\n"
      "v_cvt_pk_bf16_f32 v115, v218, v219\n"
      "v_mfma_f32_16x16x32_bf16 v[240:243], a[40:43], v[180:183], v[240:243]\n"
      "s_waitcnt lgkmcnt(2)\n"
      "v_mfma_f32_16x16x32_bf16 v[240:243], a[44:47], v[212:215], v[240:243]\n"
      "ds_write_b64 v252, v[204:205] offset:768\n"
      "v_mfma_f32_16x16x32_bf16 v[104:107], a[48:51], v[164:167], 0\n"
      "v_mfma_f32_16x16x32_bf16 v[104:107], a[52:55], v[170:173], v[104:107]\n"
      "v_cvt_pk_bf16_f32 v237, v150, v151\n"
      "v_mfma_f32_16x16x32_bf16 v[104:107], a[56:59], v[180:183], v[104:107]\n"
      "v_cvt_pk_bf16_f32 v114, v216, v217\n"
      "v_cvt_pk_bf16_f32 v81, v112, v113\n"
      "v_mfma_f32_16x16x32_bf16 v[104:107], a[60:63], v[212:215], v[104:107]\n"
      "v_cvt_pk_bf16_f32 v25, v50, v51\n"
      "v_mfma_f32_16x16x32_bf16 v[216:219], v[164:167], a[96:99], 0\n"
      "buffer_load_dwordx4 v1, s[40:43], s75 offen offset:2048 lds\n"
      "v_mfma_f32_16x16x32_bf16 v[216:219], v[170:173], a[100:103], v[216:219]\n"
      "v_mfma_f32_16x16x32_bf16 v[216:219], v[180:183], a[104:107], v[216:219]\n"
      "v_mfma_f32_16x16x32_bf16 v[216:219], v[212:215], a[108:111], v[216:219]\n"
      "s_nop 1\n"
      "v_cvt_pk_bf16_f32 v211, v106, v107\n"
      "v_mfma_f32_16x16x32_bf16 v[108:111], v[76:79], v[96:99], 0\n"
      "v_cvt_pk_bf16_f32 v52, v88, v89\n"
      "v_mfma_f32_16x16x32_bf16 v[108:111], v[80:83], v[100:103], v[108:111]\n"
      "v_cvt_pk_bf16_f32 v150, v232, v233\n"
      "v_cvt_pk_bf16_f32 v27, v208, v209\n"
      "v_mfma_f32_16x16x32_bf16 v[56:59], a[0:3], v[164:167], 0\n"
      "v_cvt_pk_bf16_f32 v151, v234, v235\n"
      "v_mfma_f32_16x16x32_bf16 v[56:59], a[4:7], v[170:173], v[56:59]\n"
      "v_cvt_pk_bf16_f32 v112, v156, v157\n"
      "v_cvt_pk_bf16_f32 v208, v240, v241\n"
      "v_mfma_f32_16x16x32_bf16 v[56:59], a[8:11], v[180:183], v[56:59]\n"
      "v_exp_f32 v111, v111\n"
      "v_mfma_f32_16x16x32_bf16 v[56:59], a[12:15], v[212:215], v[56:59]\n"
      "v_cvt_pk_bf16_f32 v20, v160, v161\n"
      "v_mfma_f32_16x16x32_bf16 v[88:91], v[164:167], a[64:67], 0\n"
      "ds_read2_b32 v[74:75], v16 offset0:128 offset1:130\n"
      "v_cvt_pk_bf16_f32 v24, v48, v49\n"
      "v_mfma_f32_16x16x32_bf16 v[88:91], v[170:173], a[68:71], v[88:91]\n"
      "v_exp_f32 v108, v108\n"
      "v_mfma_f32_16x16x32_bf16 v[88:91], v[180:183], a[72:75], v[88:91]\n"
      "v_mfma_f32_16x16x32_bf16 v[88:91], v[212:215], a[76:79], v[88:91]\n"
      "v_cvt_pk_bf16_f32 v210, v104, v105\n"
      "v_cvt_pk_bf16_f32 v22, v18, v19\n"
      "v_smfmac_f32_16x16x64_bf16 v[248:251], v[220:223], v[76:83], v5\n"
      "ds_write_b64 v252, v[30:31] offset:2816\n"
      "v_mfma_f32_16x16x32_bf16 v[232:235], a[16:19], v[164:167], 0\n"
      "v_mfma_f32_16x16x32_bf16 v[232:235], a[20:23], v[170:173], v[232:235]\n"
      "v_mfma_f32_16x16x32_bf16 v[232:235], a[24:27], v[180:183], v[232:235]\n"
      "v_cvt_pk_bf16_f32 v26, v206, v207\n"
      "v_mov_b64 v[28:29], 0\n"
      "v_mfma_f32_16x16x32_bf16 v[232:235], a[28:31], v[212:215], v[232:235]\n"
      "s_nop 0\n"
      "v_add_f32 v248, v248, v249\n"
      "v_cvt_pk_bf16_f32 v21, v162, v163\n"
      "v_mfma_f32_16x16x32_bf16 v[48:51], v[164:167], a[112:115], 0\n"
      "v_mov_b64 v[156:157], 0\n"
      "v_cvt_pk_bf16_f32 v209, v242, v243\n"
      "v_mfma_f32_16x16x32_bf16 v[48:51], v[170:173], a[116:119], v[48:51]\n"
      "buffer_load_dwordx4 v1, s[40:43], s76 offen offset:3072 lds\n"
      "v_mfma_f32_16x16x32_bf16 v[48:51], v[180:183], a[120:123], v[48:51]\n"
      "v_add_u32 v16, 64, v16\n"
      "v_mfma_f32_16x16x32_bf16 v[48:51], v[212:215], a[124:127], v[48:51]\n"
      "v_cvt_pk_bf16_f32 v148, v32, v33\n"
      "v_cvt_pk_bf16_f32 v207, v234, v235\n"
      "v_mfma_f32_16x16x32_bf16 v[104:107], v[164:167], a[80:83], 0\n"
      "s_waitcnt vmcnt(7)\n"
      "ds_read_b128 v[240:243], v0 offset:0\n"
      "v_mfma_f32_16x16x32_bf16 v[104:107], v[170:173], a[84:87], v[104:107]\n"
      "ds_read_b128 v[160:163], v0 offset:512\n"
      "ds_read_b128 v[170:173], v0 offset:1024\n"
      "v_mfma_f32_16x16x32_bf16 v[104:107], v[180:183], a[88:91], v[104:107]\n"
      "v_cvt_pk_bf16_f32 v113, v158, v159\n"
      "v_cvt_pk_bf16_f32 v206, v232, v233\n"
      "v_mfma_f32_16x16x32_bf16 v[104:107], v[212:215], a[92:95], v[104:107]\n"
      "ds_read_b128 v[32:35], v0 offset:1536\n"
      "v_cvt_pk_bf16_f32 v205, v58, v59\n"
      "v_cvt_pk_bf16_f32 v235, v50, v51\n"
      "v_cvt_pk_bf16_f32 v204, v56, v57\n"
      "v_mfma_f32_16x16x32_bf16 v[164:167], v[20:23], v[96:99], 0\n"
      "v_mfma_f32_16x16x32_bf16 v[164:167], v[24:27], v[100:103], v[164:167]\n"
      "v_exp_f32 v110, v110\n"
      "v_smfmac_f32_16x16x64_bf16 v[28:31], v[220:223], v[136:143], v5\n"
      "ds_write_b64 v252, v[194:195] offset:2304\n"
      "v_smfmac_f32_16x16x64_bf16 v[40:43], v[220:223], v[64:71], v5\n"
      "v_smfmac_f32_16x16x64_bf16 v[156:159], v[220:223], v[204:211], v5\n"
      "ds_write_b64 v253, v[168:169] offset:2176\n"
      "s_waitcnt lgkmcnt(5)\n"
      "v_mfma_f32_16x16x32_bf16 v[212:215], v[240:243], a[64:67], 0\n"
      "v_cvt_pk_bf16_f32 v182, v104, v105\n"
      "s_waitcnt lgkmcnt(4)\n"
      "v_mfma_f32_16x16x32_bf16 v[212:215], v[160:163], a[68:71], v[212:215]\n"
      "s_waitcnt lgkmcnt(3)\n"
      "v_mfma_f32_16x16x32_bf16 v[212:215], v[170:173], a[72:75], v[212:215]\n"
      "L_h1_%=:\n"
      "s_cbranch_execz L_hr_%=\n"
      "v_cvt_pk_bf16_f32 v183, v106, v107\n"
      "s_waitcnt lgkmcnt(2)\n"
      "v_mfma_f32_16x16x32_bf16 v[212:215], v[32:35], a[76:79], v[212:215]\n"
      "v_cvt_pk_bf16_f32 v234, v48, v49\n"
      "v_cvt_pk_bf16_f32 v107, v110, v111\n"
      "v_mfma_f32_16x16x32_bf16 v[48:51], v[240:243], a[112:115], 0\n"
      "v_exp_f32 v165, v165\n"
      "v_mfma_f32_16x16x32_bf16 v[48:51], v[160:163], a[116:119], v[48:51]\n"
      "v_add_f32 v156, v156, v157\n"
      "v_add_f32 v40, v40, v41\n"
      "v_mfma_f32_16x16x32_bf16 v[48:51], v[170:173], a[120:123], v[48:51]\n"
      "v_exp_f32 v156, v156\n"
      "v_mfma_f32_16x16x32_bf16 v[48:51], v[32:35], a[124:127], v[48:51]\n"
      "v_exp_f32 v248, v248\n"
      "v_mfma_f32_16x16x32_bf16 v[252:255], v[240:243], a[80:83], 0\n"
      "v_add_f32 v28, v28, v29\n"
      "v_mfma_f32_16x16x32_bf16 v[252:255], v[160:163], a[84:87], v[252:255]\n"
      "v_exp_f32 v167, v167\n"
      "v_mfma_f32_16x16x32_bf16 v[252:255], v[170:173], a[88:91], v[252:255]\n"
      "v_cvt_pk_bf16_f32 v59, v218, v219\n"
      "v_cvt_pk_bf16_f32 v58, v216, v217\n"
      "v_mfma_f32_16x16x32_bf16 v[252:255], v[32:35], a[92:95], v[252:255]\n"
      "v_exp_f32 v109, v109\n"
      "v_mfma_f32_16x16x32_bf16 v[42:45], a[0:3], v[240:243], 0\n"
      "v_exp_f32 v166, v166\n"
      "v_mfma_f32_16x16x32_bf16 v[42:45], a[4:7], v[160:163], v[42:45]\n"
      "v_cvt_pk_bf16_f32 v106, v108, v109\n"
      "v_cvt_pk_bf16_f32 v233, v50, v51\n"
      "v_mfma_f32_16x16x32_bf16 v[42:45], a[8:11], v[170:173], v[42:45]\n"
      "v_cvt_pk_bf16_f32 v232, v48, v49\n"
      "v_cvt_pk_bf16_f32 v105, v166, v167\n"
      "v_mfma_f32_16x16x32_bf16 v[42:45], a[12:15], v[32:35], v[42:45]\n"
      "v_cndmask_b32_dpp v157, v248, v156, vcc quad_perm:[0,0,2,2] row_mask:0xf bank_mask:0xf\n"
      "v_cvt_pk_bf16_f32 v108, v212, v213\n"
      "v_mfma_f32_16x16x32_bf16 v[216:219], a[16:19], v[240:243], 0\n"
      "v_exp_f32 v164, v164\n"
      "v_mfma_f32_16x16x32_bf16 v[216:219], a[20:23], v[160:163], v[216:219]\n"
      "v_cvt_pk_bf16_f32 v111, v90, v91\n"
      "v_cvt_pk_bf16_f32 v109, v214, v215\n"
      "v_mfma_f32_16x16x32_bf16 v[216:219], a[24:27], v[170:173], v[216:219]\n"
      "v_cvt_pk_bf16_f32 v110, v88, v89\n"
      "v_mfma_f32_16x16x32_bf16 v[216:219], a[28:31], v[32:35], v[216:219]\n"
      "v_exp_f32 v28, v28\n"
      "v_mfma_f32_16x16x32_bf16 v[36:39], a[48:51], v[240:243], 0\n"
      "buffer_load_dwordx4 v1, s[40:43], s76 offen offset:1024 lds\n"
      "v_mfma_f32_16x16x32_bf16 v[36:39], a[52:55], v[160:163], v[36:39]\n"
      "s_add_u32 m0, s53, 12288\n"
      "v_cvt_pk_bf16_f32 v104, v164, v165\n"
      "v_mov_b32_dpp v50, v74 row_ror:8 row_mask:0xf bank_mask:0xf\n"
      "v_mfma_f32_16x16x32_bf16 v[36:39], a[56:59], v[170:173], v[36:39]\n"
      "v_cvt_pk_bf16_f32 v165, v44, v45\n"
      "v_cvt_pk_bf16_f32 v166, v216, v217\n"
      "v_mfma_f32_16x16x32_bf16 v[36:39], a[60:63], v[32:35], v[36:39]\n"
      "v_cvt_pk_bf16_f32 v180, v252, v253\n"
      "v_cvt_pk_bf16_f32 v167, v218, v219\n"
      "v_mfma_f32_16x16x32_bf16 v[88:91], a[32:35], v[240:243], 0\n"
      "v_exp_f32 v40, v40\n"
      "v_mfma_f32_16x16x32_bf16 v[88:91], a[36:39], v[160:163], v[88:91]\n"
      "v_mov_b32_dpp v51, v75 row_ror:8 row_mask:0xf bank_mask:0xf\n"
      "v_cvt_pk_bf16_f32 v164, v42, v43\n"
      "v_mfma_f32_16x16x32_bf16 v[88:91], a[40:43], v[170:173], v[88:91]\n"
      "ds_read_b128 v[212:215], v9 offset:0\n"
      "v_mfma_f32_16x16x32_bf16 v[88:91], a[44:47], v[32:35], v[88:91]\n"
      "v_cvt_pk_bf16_f32 v181, v254, v255\n"
      "v_mfma_f32_16x16x32_bf16 v[252:255], v[240:243], a[96:99], 0\n"
      "ds_read_b128 v[216:219], v9 offset:2048\n"
      "v_cndmask_b32_dpp v41, v28, v40, vcc quad_perm:[0,0,2,2] row_mask:0xf bank_mask:0xf\n"
      "v_mfma_f32_16x16x32_bf16 v[252:255], v[160:163], a[100:103], v[252:255]\n"
      "buffer_load_dwordx4 v1, s[40:43], s78 offen offset:3072 lds\n"
      "v_mfma_f32_16x16x32_bf16 v[252:255], v[170:173], a[104:107], v[252:255]\n"
      "v_cvt_pk_bf16_f32 v170, v36, v37\n"
      "v_mov_b64 v[30:31], 0\n"
      "v_mfma_f32_16x16x32_bf16 v[252:255], v[32:35], a[108:111], v[252:255]\n"
      "v_cvt_pk_bf16_f32 v168, v88, v89\n"
      "v_cvt_pk_bf16_f32 v171, v38, v39\n"
      "v_smfmac_f32_16x16x64_bf16 v[30:33], v[220:223], v[20:27], v5\n"
      "v_mov_b32_dpp v49, v73 row_ror:8 row_mask:0xf bank_mask:0xf\n"
      "v_mfma_f32_16x16x32_bf16 v[42:45], a[176:179], v[196:199], 0\n"
      "v_cvt_pk_bf16_f32 v169, v90, v91\n"
      "v_mov_b64 v[250:251], 0\n"
      "v_mfma_f32_16x16x32_bf16 v[42:45], a[180:183], v[84:87], v[42:45]\n"
      "v_mov_b32_dpp v48, v72 row_ror:8 row_mask:0xf bank_mask:0xf\n"
      "v_mfma_f32_16x16x32_bf16 v[42:45], a[184:187], v[152:155], v[42:45]\n"
      "v_mov_b64 v[158:159], 0\n"
      "v_cvt_pk_bf16_f32 v56, v252, v253\n"
      "v_mfma_f32_16x16x32_bf16 v[42:45], a[188:191], v[200:203], v[42:45]\n"
      "v_cvt_pk_bf16_f32 v57, v254, v255\n"
      "v_add_f32 v30, v30, v31\n"
      "v_smfmac_f32_16x16x64_bf16 v[250:253], v[220:223], v[164:171], v5\n"
      "ds_read_b64 v[18:19], v11 offset:0\n"
      "s_waitcnt lgkmcnt(1)\n"
      "v_smfmac_f32_16x16x64_bf16 v[158:161], v[220:223], v[212:219], v5\n"
      "s_nop 1\n"
      "v_cvt_pk_bf16_f32 v194, v42, v43\n"
      "v_mfma_f32_16x16x32_bf16 v[32:35], v[164:167], v[96:99], 0\n"
      "v_exp_f32 v30, v30\n"
      "v_mfma_f32_16x16x32_bf16 v[32:35], v[168:171], v[100:103], v[32:35]\n"
      "v_add_f32 v250, v250, v251\n"
      "v_exp_f32 v250, v250\n"
      "v_mfma_f32_16x16x32_bf16 v[36:39], v[204:207], v[96:99], 0\n"
      "s_nop 0\n"
      "v_cndmask_b32_dpp v251, v30, v250, vcc quad_perm:[0,0,2,2] row_mask:0xf bank_mask:0xf\n"
      "v_mov_b64 v[214:215], 0\n"
      "v_mfma_f32_16x16x32_bf16 v[36:39], v[208:211], v[100:103], v[36:39]\n"
      "s_not_b64 vcc, vcc\n"
      "v_cndmask_b32_dpp v29, v40, v28, vcc quad_perm:[1,1,3,3] row_mask:0xf bank_mask:0xf\n"
      "v_cvt_pk_bf16_f32 v41, v41, v29\n"
      "v_cvt_pk_bf16_f32 v195, v44, v45\n"
      "v_add_f32 v40, v40, v28\n"
      "v_mov_b64 v[212:213], 0\n"
      "v_cndmask_b32_dpp v249, v156, v248, vcc quad_perm:[1,1,3,3] row_mask:0xf bank_mask:0xf\n"
      "v_mfma_f32_16x16x32_bf16 v[160:163], v[136:139], v[188:191], 0\n"
      "v_cndmask_b32_dpp v31, v250, v30, vcc quad_perm:[1,1,3,3] row_mask:0xf bank_mask:0xf\n"
      "v_add_f32 v250, v250, v156\n"
      "v_mfma_f32_16x16x32_bf16 v[160:163], v[140:143], v[192:195], v[160:163]\n"
      "v_cvt_pk_bf16_f32 v157, v157, v249\n"
      "v_add_f32 v158, v158, v159\n"
      "v_add_f32 v30, v30, v248\n"
      "v_mov_b32 v159, 0\n"
      "v_smfmac_f32_16x16x64_bf16 v[212:215], v[72:75], v[96:103], v5\n"
      "ds_bpermute_b32 v29, v7, v41 offset:4\n"
      "v_smfmac_f32_16x16x64_bf16 v[212:215], v[48:51], v[188:195], v5\n"
      "ds_bpermute_b32 v223, v6, v157 offset:16\n"
      "v_add_f32 v250, v250, v30\n"
      "v_exp_f32 v36, v36\n"
      "v_mfma_f32_16x16x32_bf16 v[88:91], v[76:79], v[188:191], 0\n"
      "v_exp_f32 v38, v38\n"
      "v_mfma_f32_16x16x32_bf16 v[88:91], v[80:83], v[192:195], v[88:91]\n"
      "ds_bpermute_b32 v221, v6, v157\n"
      "ds_bpermute_b32 v28, v7, v41\n"
      "v_exp_f32_dpp v159, v158 quad_perm:[0,1,2,3] row_mask:0xf bank_mask:0x1\n"
      "v_exp_f32 v160, v160\n"
      "v_mfma_f32_16x16x32_bf16 v[240:243], v[20:23], v[188:191], 0\n"
      "v_exp_f32 v37, v37\n"
      "v_mfma_f32_16x16x32_bf16 v[240:243], v[24:27], v[192:195], v[240:243]\n"
      "v_exp_f32 v163, v163\n"
      "v_cvt_pk_bf16_f32 v251, v251, v31\n"
      "v_add_f32 v214, v214, v215\n"
      "v_mfma_f32_16x16x32_bf16 v[96:99], v[164:167], v[188:191], 0\n"
      "v_exp_f32 v32, v32\n"
      "v_mfma_f32_16x16x32_bf16 v[96:99], v[168:171], v[192:195], v[96:99]\n"
      "ds_bpermute_b32 v220, v6, v251\n"
      "v_mfma_f32_16x16x32_bf16 v[252:255], v[64:67], v[188:191], 0\n"
      "v_mov_b32_dpp v158, v159 quad_perm:[1,1,3,3] row_mask:0xf bank_mask:0xf\n"
      "v_cvt_pk_bf16_f32 v158, v159, v158\n"
      "v_mfma_f32_16x16x32_bf16 v[252:255], v[68:71], v[192:195], v[252:255]\n"
      "ds_bpermute_b32 v222, v6, v251 offset:16\n"
      "v_mfma_f32_16x16x32_bf16 v[42:45], v[204:207], v[188:191], 0\n"
      "v_exp_f32 v34, v34\n"
      "v_mfma_f32_16x16x32_bf16 v[42:45], v[208:211], v[192:195], v[42:45]\n"
      "ds_bpermute_b32 v193, v7, v158 offset:8\n"
      "v_exp_f32 v33, v33\n"
      "v_mfma_f32_16x16x32_bf16 v[188:191], a[160:163], v[128:131], 0\n"
      "v_exp_f32 v243, v243\n"
      "v_mfma_f32_16x16x32_bf16 v[188:191], a[164:167], v[144:147], v[188:191]\n"
      "v_exp_f32 v253, v253\n"
      "v_mfma_f32_16x16x32_bf16 v[188:191], a[168:171], v[132:135], v[188:191]\n"
      "s_nop 0\n"
      "v_exp_f32 v43, v43\n"
      "v_mfma_f32_16x16x32_bf16 v[188:191], a[172:175], v[224:227], v[188:191]\n"
      "v_add_f32 v40, v40, v159\n"
      "v_add_f32 v46, v250, v40\n"
      "v_exp_f32 v35, v35\n"
      "v_exp_f32 v255, v255\n"
      "v_exp_f32 v162, v162\n"
      "v_exp_f32 v91, v91\n"
      "v_exp_f32 v241, v241\n"
      "v_add_f32 v212, v212, v213\n"
      "v_cvt_pk_bf16_f32 v216, v188, v189\n"
      "v_exp_f32 v39, v39\n"
      "v_cvt_pk_bf16_f32 v102, v36, v37\n"
      "v_exp_f32 v89, v89\n"
      "v_cvt_pk_bf16_f32 v103, v38, v39\n"
      "v_exp_f32 v42, v42\n"
      "v_exp_f32 v254, v254\n"
      "v_cvt_pk_bf16_f32 v217, v190, v191\n"
      "v_cvt_pk_bf16_f32 v101, v34, v35\n"
      "v_exp_f32 v99, v99\n"
      "v_exp_f32 v97, v97\n"
      "v_cvt_pk_bf16_f32 v100, v32, v33\n"
      "v_exp_f32 v212, v212\n"
      "v_mfma_f32_16x16x32_bf16 v[32:35], a[176:179], v[128:131], 0\n"
      "v_exp_f32 v161, v161\n"
      "v_mfma_f32_16x16x32_bf16 v[32:35], a[180:183], v[144:147], v[32:35]\n"
      "v_exp_f32 v214, v214\n"
      "v_mfma_f32_16x16x32_bf16 v[32:35], a[184:187], v[132:135], v[32:35]\n"
      "ds_bpermute_b32 v192, v7, v158 offset:248\n"
      "v_mfma_f32_16x16x32_bf16 v[32:35], a[188:191], v[224:227], v[32:35]\n"
      "v_exp_f32 v96, v96\n"
      "v_exp_f32 v44, v44\n"
      "v_add_f32 v172, v212, v214\n"
      "v_cvt_pk_bf16_f32 v190, v160, v161\n"
      "v_exp_f32 v45, v45\n"
      "v_cvt_pk_bf16_f32 v195, v212, v214\n"
      "v_cvt_pk_bf16_f32 v191, v162, v163\n"
      "v_cvt_pk_bf16_f32 v156, v96, v97\n"
      "v_cvt_pk_bf16_f32 v159, v44, v45\n"
      "v_cvt_pk_bf16_f32 v158, v42, v43\n"
      "v_exp_f32 v88, v88\n"
      "v_add_f32 v173, v212, v214\n"
      "v_mfma_f32_16x16x32_bf16 v[36:39], a[128:131], v[128:131], 0\n"
      "v_exp_f32 v98, v98\n"
      "v_mfma_f32_16x16x32_bf16 v[36:39], a[132:135], v[144:147], v[36:39]\n"
      "v_cvt_pk_bf16_f32 v157, v98, v99\n"
      "v_cvt_pk_bf16_f32 v219, v34, v35\n"
      "v_mfma_f32_16x16x32_bf16 v[36:39], a[136:139], v[132:135], v[36:39]\n"
      "v_exp_f32 v252, v252\n"
      "v_mfma_f32_16x16x32_bf16 v[36:39], a[140:143], v[224:227], v[36:39]\n"
      "v_cvt_pk_bf16_f32 v218, v32, v33\n"
      "v_cvt_pk_bf16_f32 v162, v88, v89\n"
      "v_mfma_f32_16x16x32_bf16 v[32:35], a[144:147], v[128:131], 0\n"
      "v_exp_f32 v240, v240\n"
      "v_mfma_f32_16x16x32_bf16 v[32:35], a[148:151], v[144:147], v[32:35]\n"
      "v_exp_f32 v90, v90\n"
      "v_mfma_f32_16x16x32_bf16 v[32:35], a[152:155], v[132:135], v[32:35]\n"
      "v_exp_f32 v242, v242\n"
      "v_mfma_f32_16x16x32_bf16 v[32:35], a[156:159], v[224:227], v[32:35]\n"
      "v_cvt_pk_bf16_f32 v194, v212, v214\n"
      "v_permlane16_swap_b32 v172, v173\n"
      "v_mov_b64 v[88:89], v[172:173]\n"
      "v_cvt_pk_bf16_f32 v160, v240, v241\n"
      "v_cvt_pk_bf16_f32 v163, v90, v91\n"
      "v_smfmac_f32_16x16x64_bf16 v[172:175], v[12:15], v[100:107], v5\n"
      "s_nop 1\n"
      "v_cvt_pk_bf16_f32 v215, v34, v35\n"
      "v_permlane16_swap_b32 v194, v195\n"
      "v_cvt_pk_bf16_f32 v161, v242, v243\n"
      "v_cvt_pk_bf16_f32 v212, v36, v37\n"
      "v_cvt_pk_bf16_f32 v188, v252, v253\n"
      "v_smfmac_f32_16x16x32_bf16 v[172:175], v[12:13], v[148:151], v5\n"
      "v_cvt_pk_bf16_f32 v189, v254, v255\n"
      "v_cvt_pk_bf16_f32 v214, v32, v33\n"
      "v_cvt_pk_bf16_f32 v213, v38, v39\n"
      "v_smfmac_f32_16x16x64_bf16 v[88:91], v[12:15], v[156:163], v5\n"
      "buffer_load_dwordx4 v1, s[40:43], s77 offen offset:0 lds\n"
      "s_waitcnt lgkmcnt(8)\n"
      "v_mfma_f32_16x16x4_4b_bf16 v[30:45], v[194:195], v[18:19], 0 cbsz:2 abid:0\n"
      "s_nop 0\n"
      "v_add_f32 v172, v172, v173\n"
      "v_add_f32_dpp v46, v46, v46 row_ror:8 row_mask:0xf bank_mask:0xf\n"
      "v_rcp_f32 v173, v172\n"
      "s_nop 0\n"
      "v_add_f32_dpp v46, v46, v46 row_ror:4 row_mask:0xf bank_mask:0xf\n"
      "v_smfmac_f32_16x16x32_bf16 v[88:91], v[12:13], v[188:191], v5\n"
      "ds_bpermute_b32 v174, v4, v173\n"
      "v_add_f32_dpp v46, v46, v46 row_ror:2 row_mask:0xf bank_mask:0xf\n"
      "s_nop 0\n"
      "v_mfma_f32_16x16x32_bf16 v[42:45], v[100:103], v[232:235], v[42:45]\n"
      "v_mfma_f32_16x16x32_bf16 v[42:45], v[104:107], v[236:239], v[42:45]\n"
      "v_mfma_f32_16x16x32_bf16 v[42:45], v[148:151], v[228:231], v[42:45]\n"
      "s_nop 1\n"
      "v_add_f32 v88, v88, v89\n"
      "v_mfma_f32_16x16x32_bf16 v[34:37], v[100:103], v[180:183], v[34:37]\n"
      "v_add_f32_dpp v46, v46, v46 row_ror:1 row_mask:0xf bank_mask:0xf\n"
      "v_mfma_f32_16x16x32_bf16 v[34:37], v[104:107], v[184:187], v[34:37]\n"
      "v_rcp_f32 v47, v46\n"
      "v_mfma_f32_16x16x32_bf16 v[34:37], v[148:151], v[120:123], v[34:37]\n"
      "s_not_b64 vcc, vcc\n"
      "v_rcp_f32 v89, v88\n"
      "v_mfma_f32_16x16x32_bf16 v[30:33], v[100:103], v[108:111], v[30:33]\n"
      "ds_read_b64 v[252:253], v11 offset:4096\n"
      "v_mfma_f32_16x16x32_bf16 v[30:33], v[104:107], v[112:115], v[30:33]\n"
      "v_mfma_f32_16x16x32_bf16 v[30:33], v[148:151], v[52:55], v[30:33]\n"
      "s_waitcnt lgkmcnt(1)\n"
      "v_mul_f32_dpp v44, v174, v44 row_newbcast:8 row_mask:0xf bank_mask:0xf\n"
      "v_mul_f32_dpp v35, v174, v35 row_newbcast:4 row_mask:0xf bank_mask:0xf\n"
      "v_mfma_f32_16x16x32_bf16 v[38:41], v[100:103], v[56:59], v[38:41]\n"
      "v_mul_f32_dpp v37, v174, v37 row_newbcast:12 row_mask:0xf bank_mask:0xf\n"
      "v_mfma_f32_16x16x32_bf16 v[38:41], v[104:107], v[60:63], v[38:41]\n"
      "v_maximum3_f32 v8, v8, v46, v47\n"
      "v_mfma_f32_16x16x32_bf16 v[38:41], v[148:151], v[116:119], v[38:41]\n"
      "v_mul_f32_dpp v43, v174, v43 row_newbcast:4 row_mask:0xf bank_mask:0xf\n"
      "v_mul_f32_dpp v34, v174, v34 row_newbcast:0 row_mask:0xf bank_mask:0xf\n"
      "v_mfma_f32_16x16x32_bf16 v[148:151], v[76:79], v[212:215], 0\n"
      "v_mul_f32_dpp v36, v174, v36 row_newbcast:8 row_mask:0xf bank_mask:0xf\n"
      "v_mul_f32_dpp v31, v174, v31 row_newbcast:4 row_mask:0xf bank_mask:0xf\n"
      "v_mfma_f32_16x16x32_bf16 v[148:151], v[80:83], v[216:219], v[148:151]\n"
      "v_mul_f32_dpp v33, v174, v33 row_newbcast:12 row_mask:0xf bank_mask:0xf\n"
      "v_mfma_f32_16x16x32_bf16 v[34:37], v[92:95], a[208:211], v[34:37]\n"
      "v_mul_f32_dpp v32, v174, v32 row_newbcast:8 row_mask:0xf bank_mask:0xf\n"
      "v_mul_f32_dpp v45, v174, v45 row_newbcast:12 row_mask:0xf bank_mask:0xf\n"
      "v_mfma_f32_16x16x32_bf16 v[34:37], v[124:127], a[212:215], v[34:37]\n"
      "buffer_load_dwordx4 v1, s[40:43], s77 offen offset:2048 lds\n"
      "v_mfma_f32_16x16x32_bf16 v[34:37], v[176:179], a[216:219], v[34:37]\n"
      "v_mul_f32_dpp v40, v174, v40 row_newbcast:8 row_mask:0xf bank_mask:0xf\n"
      "v_mul_f32_dpp v30, v174, v30 row_newbcast:0 row_mask:0xf bank_mask:0xf\n"
      "v_mfma_f32_16x16x32_bf16 v[34:37], v[244:247], a[220:223], v[34:37]\n"
      "v_exp_f32 v151, v151\n"
      "v_mfma_f32_16x16x32_bf16 v[30:33], v[92:95], a[192:195], v[30:33]\n"
      "v_exp_f32 v149, v149\n"
      "v_mfma_f32_16x16x32_bf16 v[30:33], v[124:127], a[196:199], v[30:33]\n"
      "v_mul_f32_dpp v42, v174, v42 row_newbcast:0 row_mask:0xf bank_mask:0xf\n"
      "v_mfma_f32_16x16x32_bf16 v[30:33], v[176:179], a[200:203], v[30:33]\n"
      "v_mfma_f32_16x16x32_bf16 v[30:33], v[244:247], a[204:207], v[30:33]\n"
      "v_mul_f32_dpp v38, v174, v38 row_newbcast:0 row_mask:0xf bank_mask:0xf\n"
      "v_mul_f32_dpp v41, v174, v41 row_newbcast:12 row_mask:0xf bank_mask:0xf\n"
      "v_mfma_f32_16x16x32_bf16 v[42:45], v[92:95], a[240:243], v[42:45]\n"
      "v_exp_f32 v148, v148\n"
      "v_mfma_f32_16x16x32_bf16 v[42:45], v[124:127], a[244:247], v[42:45]\n"
      "ds_bpermute_b32 v90, v4, v89 offset:128\n"
      "v_mfma_f32_16x16x32_bf16 v[42:45], v[176:179], a[248:251], v[42:45]\n"
      "v_mul_f32_dpp v39, v174, v39 row_newbcast:4 row_mask:0xf bank_mask:0xf\n"
      "v_cvt_pk_bf16_f32 v32, v32, v36\n"
      "v_mfma_f32_16x16x32_bf16 v[42:45], v[244:247], a[252:255], v[42:45]\n"
      "s_waitcnt lgkmcnt(1)\n"
      "v_and_b32 v175, 0xffff0000, v253\n"
      "v_pk_max_i16 v32, v32, 0\n"
      "v_mfma_f32_16x16x32_bf16 v[38:41], v[92:95], a[224:227], v[38:41]\n"
      "buffer_load_dwordx4 v1, s[40:43], s78 offen offset:1024 lds\n"
      "v_mfma_f32_16x16x32_bf16 v[38:41], v[124:127], a[228:231], v[38:41]\n"
      "v_cvt_pk_bf16_f32 v36, v33, v37\n"
      "v_mfma_f32_16x16x32_bf16 v[38:41], v[176:179], a[232:235], v[38:41]\n"
      "v_cvt_pk_bf16_f32 v30, v30, v34\n"
      "v_pk_max_i16 v30, v30, 0\n"
      "v_mfma_f32_16x16x32_bf16 v[38:41], v[244:247], a[236:239], v[38:41]\n"
      "v_pk_max_i16 v36, v36, 0\n"
      "v_maximum3_f32 v8, v8, v172, v173\n"
      "v_mfma_f32_16x16x4_4b_bf16 v[92:107], v[194:195], v[18:19], 0 cbsz:2 abid:2\n"
      "v_lshlrev_b32 v174, 16, v253\n"
      "v_cvt_pk_bf16_f32 v194, v148, v149\n"
      "v_and_b32 v173, 0xffff0000, v252\n"
      "v_cvt_pk_bf16_f32 v34, v31, v35\n"
      "s_nop 0\n"
      "v_cvt_pk_bf16_f32 v33, v40, v44\n"
      "v_pk_max_i16 v33, v33, 0\n"
      "v_cvt_pk_bf16_f32 v37, v41, v45\n"
      "v_mfma_f32_16x16x32_bf16 v[176:179], v[20:23], v[212:215], 0\n"
      "v_lshlrev_b32 v172, 16, v252\n"
      "v_mfma_f32_16x16x32_bf16 v[176:179], v[24:27], v[216:219], v[176:179]\n"
      "buffer_store_dwordx2 v[32:33], v2, s[44:47], s73 offen offset:512 nt sc1\n"
      "v_mfma_f32_16x16x32_bf16 v[104:107], v[156:159], v[232:235], v[104:107]\n"
      "v_exp_f32 v150, v150\n"
      "v_mfma_f32_16x16x32_bf16 v[104:107], v[160:163], v[236:239], v[104:107]\n"
      "v_pk_max_i16 v37, v37, 0\n"
      "v_cvt_pk_bf16_f32 v195, v150, v151\n"
      "v_mfma_f32_16x16x32_bf16 v[104:107], v[188:191], v[228:231], v[104:107]\n"
      "v_cvt_pk_bf16_f32 v35, v39, v43\n"
      "v_mfma_f32_16x16x32_bf16 v[92:95], v[156:159], v[108:111], v[92:95]\n"
      "v_cvt_pk_bf16_f32 v31, v38, v42\n"
      "v_mfma_f32_16x16x32_bf16 v[92:95], v[160:163], v[112:115], v[92:95]\n"
      "v_exp_f32 v177, v177\n"
      "v_mfma_f32_16x16x32_bf16 v[92:95], v[188:191], v[52:55], v[92:95]\n"
      "s_waitcnt lgkmcnt(0)\n"
      "s_nop 0\n"
      "v_mul_f32_dpp v105, v90, v105 row_newbcast:4 row_mask:0xf bank_mask:0xf\n"
      "v_mfma_f32_16x16x4_4b_bf16 v[240:255], v[192:193], v[18:19], 0 cbsz:2 abid:0\n"
      "v_mul_f32_dpp v107, v90, v107 row_newbcast:12 row_mask:0xf bank_mask:0xf\n"
      "v_exp_f32 v176, v176\n"
      "v_exp_f32 v178, v178\n"
      "v_exp_f32 v179, v179\n"
      "v_mfma_f32_16x16x32_bf16 v[96:99], v[156:159], v[180:183], v[96:99]\n"
      "s_add_u32 m0, s53, 8192\n"
      "v_mul_f32_dpp v104, v90, v104 row_newbcast:0 row_mask:0xf bank_mask:0xf\n"
      "v_mfma_f32_16x16x32_bf16 v[96:99], v[160:163], v[184:187], v[96:99]\n"
      "v_mfma_f32_16x16x32_bf16 v[96:99], v[188:191], v[120:123], v[96:99]\n"
      "v_cvt_pk_bf16_f32 v192, v176, v177\n"
      "v_cvt_pk_bf16_f32 v193, v178, v179\n"
      "v_mfma_f32_16x16x32_bf16 v[100:103], v[156:159], v[56:59], v[100:103]\n"
      "ds_read_b128 v[148:151], v0 offset:9216\n"
      "v_mfma_f32_16x16x32_bf16 v[100:103], v[160:163], v[60:63], v[100:103]\n"
      "ds_read_b128 v[176:179], v0 offset:8704\n"
      "ds_read_b128 v[124:127], v0 offset:9728\n"
      "v_mfma_f32_16x16x32_bf16 v[100:103], v[188:191], v[116:119], v[100:103]\n"
      "ds_read_b128 v[160:163], v0 offset:8192\n"
      "v_mul_f32_dpp v93, v90, v93 row_newbcast:4 row_mask:0xf bank_mask:0xf\n"
      "v_smfmac_f32_16x16x64_bf16 v[240:243], v[220:223], v[108:115], v5\n"
      "buffer_store_dwordx2 v[36:37], v2, s[44:47], s73 offen offset:768 nt sc1\n"
      "v_pk_max_i16 v34, v34, 0\n"
      "v_smfmac_f32_16x16x64_bf16 v[244:247], v[220:223], v[180:187], v5\n"
      "v_smfmac_f32_16x16x64_bf16 v[252:255], v[220:223], v[232:239], v5\n"
      "v_mul_f32_dpp v97, v90, v97 row_newbcast:4 row_mask:0xf bank_mask:0xf\n"
      "v_smfmac_f32_16x16x64_bf16 v[248:251], v[220:223], v[56:63], v5\n"
      "v_pk_max_i16 v31, v31, 0\n"
      "v_mfma_f32_16x16x32_bf16 v[36:39], v[164:167], v[212:215], 0\n"
      "v_mul_f32_dpp v101, v90, v101 row_newbcast:4 row_mask:0xf bank_mask:0xf\n"
      "v_mul_f32_dpp v92, v90, v92 row_newbcast:0 row_mask:0xf bank_mask:0xf\n"
      "v_mfma_f32_16x16x32_bf16 v[36:39], v[168:171], v[216:219], v[36:39]\n"
      "v_mul_f32_dpp v98, v90, v98 row_newbcast:8 row_mask:0xf bank_mask:0xf\n"
      "v_mul_f32_dpp v94, v90, v94 row_newbcast:8 row_mask:0xf bank_mask:0xf\n"
      "v_smfmac_f32_16x16x32_bf16 v[240:243], v[28:29], v[52:55], v5\n"
      "v_mul_f32_dpp v103, v90, v103 row_newbcast:12 row_mask:0xf bank_mask:0xf\n"
      "v_smfmac_f32_16x16x32_bf16 v[248:251], v[28:29], v[116:119], v5\n"
      "v_mul_f32_dpp v95, v90, v95 row_newbcast:12 row_mask:0xf bank_mask:0xf\n"
      "v_smfmac_f32_16x16x32_bf16 v[244:247], v[28:29], v[120:123], v5\n"
      "s_waitcnt lgkmcnt(0)\n"
      "buffer_load_dwordx4 v1, s[40:43], s91 offen offset:3072 lds\n"
      "v_smfmac_f32_16x16x32_bf16 v[252:255], v[28:29], v[228:231], v5\n"
      "s_nop 0\n"
      "v_add_f32 v240, v240, v241\n"
      "v_mul_f32_dpp v99, v90, v99 row_newbcast:12 row_mask:0xf bank_mask:0xf\n"
      "v_mfma_f32_16x16x32_bf16 v[156:159], v[204:207], v[212:215], 0\n"
      "v_fma_f32 v240, v240, v47, v172\n"
      "v_maximum3_f32 v8, v8, v88, v89\n"
      "v_mfma_f32_16x16x32_bf16 v[156:159], v[208:211], v[216:219], v[156:159]\n"
      "v_exp_f32 v36, v36\n"
      "v_mfma_f32_16x16x32_bf16 v[92:95], v[196:199], a[192:195], v[92:95]\n"
      "v_mul_f32_dpp v100, v90, v100 row_newbcast:0 row_mask:0xf bank_mask:0xf\n"
      "v_mul_f32_dpp v106, v90, v106 row_newbcast:8 row_mask:0xf bank_mask:0xf\n"
      "v_mfma_f32_16x16x32_bf16 v[92:95], v[84:87], a[196:199], v[92:95]\n"
      "buffer_load_dwordx4 v1, s[40:43], s90 offen offset:2048 lds\n"
      "v_mfma_f32_16x16x32_bf16 v[92:95], v[152:155], a[200:203], v[92:95]\n"
      "v_mul_f32_dpp v96, v90, v96 row_newbcast:0 row_mask:0xf bank_mask:0xf\n"
      "v_mfma_f32_16x16x32_bf16 v[92:95], v[200:203], a[204:207], v[92:95]\n"
      "v_add_f32 v244, v244, v245\n"
      "v_fma_f32 v244, v244, v47, v173\n"
      "v_mfma_f32_16x16x32_bf16 v[96:99], v[196:199], a[208:211], v[96:99]\n"
      "v_mul_f32_dpp v102, v90, v102 row_newbcast:8 row_mask:0xf bank_mask:0xf\n"
      "v_mfma_f32_16x16x32_bf16 v[96:99], v[84:87], a[212:215], v[96:99]\n"
      "v_pk_max_i16 v35, v35, 0\n"
      "v_mfma_f32_16x16x32_bf16 v[96:99], v[152:155], a[216:219], v[96:99]\n"
      "v_exp_f32 v37, v37\n"
      "v_mfma_f32_16x16x32_bf16 v[96:99], v[200:203], a[220:223], v[96:99]\n"
      "v_exp_f32 v157, v157\n"
      "v_mfma_f32_16x16x32_bf16 v[100:103], v[196:199], a[224:227], v[100:103]\n"
      "v_mfma_f32_16x16x32_bf16 v[100:103], v[84:87], a[228:231], v[100:103]\n"
      "v_exp_f32 v38, v38\n"
      "v_mfma_f32_16x16x32_bf16 v[100:103], v[152:155], a[232:235], v[100:103]\n"
      "v_exp_f32 v156, v156\n"
      "v_mfma_f32_16x16x32_bf16 v[100:103], v[200:203], a[236:239], v[100:103]\n"
      "v_add_f32 v252, v252, v253\n"
      "v_mov_b64 v[28:29], 0\n"
      "v_mfma_f32_16x16x32_bf16 v[104:107], v[196:199], a[240:243], v[104:107]\n"
      "v_cvt_pk_bf16_f32 v188, v36, v37\n"
      "v_cvt_pk_bf16_f32 v240, v240, v244\n"
      "v_mfma_f32_16x16x32_bf16 v[104:107], v[84:87], a[244:247], v[104:107]\n"
      "v_cvt_pk_bf16_f32 v92, v92, v96\n"
      "v_mfma_f32_16x16x32_bf16 v[104:107], v[152:155], a[248:251], v[104:107]\n"
      "v_add_f32 v248, v248, v249\n"
      "v_cvt_pk_bf16_f32 v94, v94, v98\n"
      "v_mfma_f32_16x16x32_bf16 v[104:107], v[200:203], a[252:255], v[104:107]\n"
      "buffer_store_dwordx2 v[30:31], v2, s[44:47], s73 offen offset:0 nt sc1\n"
      "v_fma_f32 v252, v252, v47, v175\n"
      "v_mfma_f32_16x16x32_bf16 v[152:155], a[160:163], v[160:163], 0\n"
      "v_exp_f32 v39, v39\n"
      "v_mfma_f32_16x16x32_bf16 v[152:155], a[164:167], v[176:179], v[152:155]\n"
      "v_pk_max_i16 v240, v240, 0\n"
      "v_cvt_pk_bf16_f32 v96, v93, v97\n"
      "v_mfma_f32_16x16x32_bf16 v[152:155], a[168:171], v[148:151], v[152:155]\n"
      "v_cvt_pk_bf16_f32 v189, v38, v39\n"
      "v_cvt_pk_bf16_f32 v98, v95, v99\n"
      "v_mfma_f32_16x16x32_bf16 v[152:155], a[172:175], v[124:127], v[152:155]\n"
      "v_cvt_pk_bf16_f32 v95, v102, v106\n"
      "v_mfma_f32_16x16x32_bf16 v[200:203], a[128:131], v[160:163], 0\n"
      "ds_read_b128 v[84:87], v0 offset:5120\n"
      "v_mfma_f32_16x16x32_bf16 v[200:203], a[132:135], v[176:179], v[200:203]\n"
      "v_pk_max_i16 v92, v92, 0\n"
      "v_cvt_pk_bf16_f32 v190, v156, v157\n"
      "v_mfma_f32_16x16x32_bf16 v[200:203], a[136:139], v[148:151], v[200:203]\n"
      "v_exp_f32 v158, v158\n"
      "v_mfma_f32_16x16x32_bf16 v[200:203], a[140:143], v[124:127], v[200:203]\n"
      "v_cvt_pk_bf16_f32 v99, v103, v107\n"
      "v_fma_f32 v248, v248, v47, v174\n"
      "v_mfma_f32_16x16x32_bf16 v[88:91], a[176:179], v[160:163], 0\n"
      "buffer_load_dwordx4 v1, s[40:43], s91 offen offset:1024 lds\n"
      "v_mfma_f32_16x16x32_bf16 v[88:91], a[180:183], v[176:179], v[88:91]\n"
      "v_exp_f32 v159, v159\n"
      "v_mfma_f32_16x16x32_bf16 v[88:91], a[184:187], v[148:151], v[88:91]\n"
      "v_cvt_pk_bf16_f32 v191, v158, v159\n"
      "v_cvt_pk_bf16_f32 v97, v101, v105\n"
      "v_mfma_f32_16x16x32_bf16 v[88:91], a[188:191], v[124:127], v[88:91]\n"
      "v_cvt_pk_bf16_f32 v40, v152, v153\n"
      "v_pk_max_i16 v94, v94, 0\n"
      "v_mfma_f32_16x16x32_bf16 v[44:47], a[144:147], v[160:163], 0\n"
      "v_cvt_pk_bf16_f32 v241, v248, v252\n"
      "v_cvt_pk_bf16_f32 v41, v154, v155\n"
      "v_mfma_f32_16x16x32_bf16 v[44:47], a[148:151], v[176:179], v[44:47]\n"
      "ds_read_b128 v[152:155], v0 offset:5632\n"
      "v_mfma_f32_16x16x32_bf16 v[44:47], a[152:155], v[148:151], v[44:47]\n"
      "v_cvt_pk_bf16_f32 v93, v100, v104\n"
      "v_pk_max_i16 v241, v241, 0\n"
      "v_mfma_f32_16x16x32_bf16 v[44:47], a[156:159], v[124:127], v[44:47]\n"
      "v_cvt_pk_bf16_f32 v42, v88, v89\n"
      "v_cvt_pk_bf16_f32 v43, v90, v91\n"
      "v_pk_max_i16 v95, v95, 0\n"
      "v_mfma_f32_16x16x32_bf16 v[156:159], v[136:139], v[212:215], 0\n"
      "v_mov_b64 v[30:31], 0\n"
      "v_cvt_pk_bf16_f32 v36, v200, v201\n"
      "v_mfma_f32_16x16x32_bf16 v[156:159], v[140:143], v[216:219], v[156:159]\n"
      "s_nop 0\n"
      "v_cvt_pk_bf16_f32 v38, v44, v45\n"
      "v_cvt_pk_bf16_f32 v37, v202, v203\n"
      "v_cvt_pk_bf16_f32 v39, v46, v47\n"
      "v_smfmac_f32_16x16x64_bf16 v[28:31], v[72:75], v[212:219], v5\n"
      "v_pk_max_i16 v98, v98, 0\n"
      "v_smfmac_f32_16x16x64_bf16 v[28:31], v[48:51], v[36:43], v5\n"
      "buffer_store_dwordx2 v[34:35], v2, s[44:47], s73 offen offset:256 nt sc1\n"
      "v_exp_f32 v156, v156\n"
      "v_mfma_f32_16x16x32_bf16 v[88:91], v[64:67], v[212:215], 0\n"
      "v_exp_f32 v159, v159\n"
      "v_mfma_f32_16x16x32_bf16 v[88:91], v[68:71], v[216:219], v[88:91]\n"
      "v_exp_f32 v158, v158\n"
      "v_mfma_f32_16x16x32_bf16 v[200:203], v[20:23], v[36:39], 0\n"
      "s_nop 0\n"
      "v_add_f32 v30, v30, v31\n"
      "v_add_f32 v28, v28, v29\n"
      "v_mfma_f32_16x16x32_bf16 v[200:203], v[24:27], v[40:43], v[200:203]\n"
      "v_exp_f32 v28, v28\n"
      "v_mfma_f32_16x16x32_bf16 v[172:175], v[204:207], v[36:39], 0\n"
      "v_exp_f32 v89, v89\n"
      "v_mfma_f32_16x16x32_bf16 v[172:175], v[208:211], v[40:43], v[172:175]\n"
      "v_exp_f32 v30, v30\n"
      "v_mfma_f32_16x16x32_bf16 v[104:107], v[136:139], v[36:39], 0\n"
      "buffer_load_dwordx4 v1, s[40:43], s90 offen offset:0 lds\n"
      "v_mfma_f32_16x16x32_bf16 v[104:107], v[140:143], v[40:43], v[104:107]\n"
      "v_add_f32 v216, v28, v30\n"
      "v_cvt_pk_bf16_f32 v100, v28, v30\n"
      "v_mfma_f32_16x16x32_bf16 v[248:251], v[164:167], v[36:39], 0\n"
      "v_exp_f32 v91, v91\n"
      "v_mfma_f32_16x16x32_bf16 v[248:251], v[168:171], v[40:43], v[248:251]\n"
      "v_exp_f32 v90, v90\n"
      "v_add_f32 v217, v28, v30\n"
      "v_exp_f32 v157, v157\n"
      "v_mfma_f32_16x16x32_bf16 v[242:245], v[76:79], v[36:39], 0\n"
      "v_permlane16_swap_b32 v216, v217\n"
      "v_mfma_f32_16x16x32_bf16 v[242:245], v[80:83], v[40:43], v[242:245]\n"
      "v_cvt_pk_bf16_f32 v214, v156, v157\n"
      "v_mov_b64 v[220:221], v[216:217]\n"
      "v_smfmac_f32_16x16x64_bf16 v[216:219], v[12:15], v[188:195], v5\n"
      "buffer_store_dwordx2 v[240:241], v3, s[44:47], s79 offen offset:0 nt sc1\n"
      "v_cvt_pk_bf16_f32 v101, v28, v30\n"
      "v_mfma_f32_16x16x32_bf16 v[44:47], v[64:67], v[36:39], 0\n"
      "s_nop 0\n"
      "v_permlane16_swap_b32 v100, v101\n"
      "v_mfma_f32_16x16x32_bf16 v[44:47], v[68:71], v[40:43], v[44:47]\n"
      "v_exp_f32 v88, v88\n"
      "v_mfma_f32_16x16x4_4b_bf16 v[28:43], v[100:101], v[18:19], 0 cbsz:2 abid:0\n"
      "v_cvt_pk_bf16_f32 v215, v158, v159\n"
      "v_cvt_pk_bf16_f32 v213, v90, v91\n"
      "v_cvt_pk_bf16_f32 v212, v88, v89\n"
      "v_exp_f32 v107, v107\n"
      "s_nop 0\n"
      "v_smfmac_f32_16x16x32_bf16 v[216:219], v[12:13], v[212:215], v5\n"
      "s_add_u32 m0, s53, 4096\n"
      "ds_read_b128 v[88:91], v0 offset:4608\n"
      "ds_read_b128 v[156:159], v0 offset:4096\n"
      "v_exp_f32 v249, v249\n"
      "v_mfma_f32_16x16x32_bf16 v[32:35], v[188:191], v[180:183], v[32:35]\n"
      "v_exp_f32 v172, v172\n"
      "v_mfma_f32_16x16x32_bf16 v[32:35], v[192:195], v[184:187], v[32:35]\n"
      "v_exp_f32 v202, v202\n"
      "v_mfma_f32_16x16x32_bf16 v[32:35], v[212:215], v[120:123], v[32:35]\n"
      "v_add_f32 v216, v216, v217\n"
      "v_rcp_f32 v217, v216\n"
      "v_mfma_f32_16x16x32_bf16 v[36:39], v[188:191], v[56:59], v[36:39]\n"
      "ds_bpermute_b32 v218, v4, v217\n"
      "v_mfma_f32_16x16x32_bf16 v[36:39], v[192:195], v[60:63], v[36:39]\n"
      "v_exp_f32 v173, v173\n"
      "v_mfma_f32_16x16x32_bf16 v[36:39], v[212:215], v[116:119], v[36:39]\n"
      "v_exp_f32 v175, v175\n"
      "v_mfma_f32_16x16x32_bf16 v[28:31], v[188:191], v[108:111], v[28:31]\n"
      "v_exp_f32 v245, v245\n"
      "v_mfma_f32_16x16x32_bf16 v[28:31], v[192:195], v[112:115], v[28:31]\n"
      "v_exp_f32 v248, v248\n"
      "v_mfma_f32_16x16x32_bf16 v[28:31], v[212:215], v[52:55], v[28:31]\n"
      "s_waitcnt lgkmcnt(1)\n"
      "buffer_load_dwordx4 v1, s[40:43], s93 offen offset:1024 lds\n"
      "v_mfma_f32_16x16x32_bf16 v[40:43], v[188:191], v[232:235], v[40:43]\n"
      "v_exp_f32 v174, v174\n"
      "v_mfma_f32_16x16x32_bf16 v[40:43], v[192:195], v[236:239], v[40:43]\n"
      "v_exp_f32 v106, v106\n"
      "v_mfma_f32_16x16x32_bf16 v[40:43], v[212:215], v[228:231], v[40:43]\n"
      "v_exp_f32 v251, v251\n"
      "v_mfma_f32_16x16x32_bf16 v[188:191], a[160:163], v[156:159], 0\n"
      "v_exp_f32 v243, v243\n"
      "v_mfma_f32_16x16x32_bf16 v[188:191], a[164:167], v[88:91], v[188:191]\n"
      "s_waitcnt lgkmcnt(0)\n"
      "v_mul_f32_dpp v28, v218, v28 row_newbcast:0 row_mask:0xf bank_mask:0xf\n"
      "v_mul_f32_dpp v32, v218, v32 row_newbcast:0 row_mask:0xf bank_mask:0xf\n"
      "v_mfma_f32_16x16x32_bf16 v[188:191], a[168:171], v[84:87], v[188:191]\n"
      "v_mul_f32_dpp v31, v218, v31 row_newbcast:12 row_mask:0xf bank_mask:0xf\n"
      "v_mul_f32_dpp v41, v218, v41 row_newbcast:4 row_mask:0xf bank_mask:0xf\n"
      "v_mfma_f32_16x16x32_bf16 v[188:191], a[172:175], v[152:155], v[188:191]\n"
      "v_mul_f32_dpp v37, v218, v37 row_newbcast:4 row_mask:0xf bank_mask:0xf\n"
      "v_mul_f32_dpp v29, v218, v29 row_newbcast:4 row_mask:0xf bank_mask:0xf\n"
      "v_mfma_f32_16x16x32_bf16 v[252:255], a[176:179], v[156:159], 0\n"
      "v_pk_max_i16 v93, v93, 0\n"
      "v_mul_f32_dpp v30, v218, v30 row_newbcast:8 row_mask:0xf bank_mask:0xf\n"
      "v_mfma_f32_16x16x32_bf16 v[252:255], a[180:183], v[88:91], v[252:255]\n"
      "v_exp_f32 v203, v203\n"
      "v_mfma_f32_16x16x32_bf16 v[252:255], a[184:187], v[84:87], v[252:255]\n"
      "v_mul_f32_dpp v39, v218, v39 row_newbcast:12 row_mask:0xf bank_mask:0xf\n"
      "v_mul_f32_dpp v40, v218, v40 row_newbcast:0 row_mask:0xf bank_mask:0xf\n"
      "v_mfma_f32_16x16x32_bf16 v[252:255], a[188:191], v[152:155], v[252:255]\n"
      "v_mul_f32_dpp v42, v218, v42 row_newbcast:8 row_mask:0xf bank_mask:0xf\n"
      "v_cvt_pk_bf16_f32 v215, v174, v175\n"
      "v_mfma_f32_16x16x32_bf16 v[28:31], v[128:131], a[192:195], v[28:31]\n"
      "v_exp_f32 v244, v244\n"
      "v_mfma_f32_16x16x32_bf16 v[28:31], v[144:147], a[196:199], v[28:31]\n"
      "v_cvt_pk_bf16_f32 v214, v172, v173\n"
      "v_cvt_pk_bf16_f32 v247, v190, v191\n"
      "v_mfma_f32_16x16x32_bf16 v[28:31], v[132:135], a[200:203], v[28:31]\n"
      "L_h2_%=:\n"
      "s_cbranch_execz L_hr_%=\n"
      "v_mul_f32_dpp v43, v218, v43 row_newbcast:12 row_mask:0xf bank_mask:0xf\n"
      "v_mul_f32_dpp v34, v218, v34 row_newbcast:8 row_mask:0xf bank_mask:0xf\n"
      "v_mfma_f32_16x16x32_bf16 v[28:31], v[224:227], a[204:207], v[28:31]\n"
      "buffer_store_dwordx2 v[94:95], v2, s[44:47], s75 offen offset:512 nt sc1\n"
      "v_mul_f32_dpp v33, v218, v33 row_newbcast:4 row_mask:0xf bank_mask:0xf\n"
      "v_mfma_f32_16x16x32_bf16 v[40:43], v[128:131], a[240:243], v[40:43]\n"
      "v_exp_f32 v104, v104\n"
      "v_mfma_f32_16x16x32_bf16 v[40:43], v[144:147], a[244:247], v[40:43]\n"
      "v_mul_f32_dpp v38, v218, v38 row_newbcast:8 row_mask:0xf bank_mask:0xf\n"
      "v_mul_f32_dpp v35, v218, v35 row_newbcast:12 row_mask:0xf bank_mask:0xf\n"
      "v_mfma_f32_16x16x32_bf16 v[40:43], v[132:135], a[248:251], v[40:43]\n"
      "v_exp_f32 v45, v45\n"
      "v_mfma_f32_16x16x32_bf16 v[40:43], v[224:227], a[252:255], v[40:43]\n"
      "v_exp_f32 v47, v47\n"
      "v_mfma_f32_16x16x32_bf16 v[32:35], v[128:131], a[208:211], v[32:35]\n"
      "v_exp_f32 v250, v250\n"
      "v_mfma_f32_16x16x32_bf16 v[32:35], v[144:147], a[212:215], v[32:35]\n"
      "v_mul_f32_dpp v36, v218, v36 row_newbcast:0 row_mask:0xf bank_mask:0xf\n"
      "v_mfma_f32_16x16x32_bf16 v[32:35], v[132:135], a[216:219], v[32:35]\n"
      "v_maximum3_f32 v8, v8, v216, v217\n"
      "v_cvt_pk_bf16_f32 v219, v244, v245\n"
      "v_mfma_f32_16x16x32_bf16 v[32:35], v[224:227], a[220:223], v[32:35]\n"
      "buffer_load_dwordx4 v1, s[40:43], s93 offen offset:3072 lds\n"
      "v_mfma_f32_16x16x32_bf16 v[36:39], v[128:131], a[224:227], v[36:39]\n"
      "v_exp_f32 v242, v242\n"
      "v_mfma_f32_16x16x32_bf16 v[36:39], v[144:147], a[228:231], v[36:39]\n"
      "v_exp_f32 v44, v44\n"
      "v_mfma_f32_16x16x32_bf16 v[36:39], v[132:135], a[232:235], v[36:39]\n"
      "v_exp_f32 v105, v105\n"
      "v_mfma_f32_16x16x32_bf16 v[36:39], v[224:227], a[236:239], v[36:39]\n"
      "v_exp_f32 v200, v200\n"
      "v_mfma_f32_16x16x32_bf16 v[128:131], a[144:147], v[156:159], 0\n"
      "v_cvt_pk_bf16_f32 v217, v202, v203\n"
      "v_cvt_pk_bf16_f32 v246, v188, v189\n"
      "v_mfma_f32_16x16x32_bf16 v[128:131], a[148:151], v[88:91], v[128:131]\n"
      "v_exp_f32 v201, v201\n"
      "v_mfma_f32_16x16x32_bf16 v[128:131], a[152:155], v[84:87], v[128:131]\n"
      "v_cvt_pk_bf16_f32 v224, v44, v45\n"
      "v_mfma_f32_16x16x32_bf16 v[128:131], a[156:159], v[152:155], v[128:131]\n"
      "v_cvt_pk_bf16_f32 v213, v250, v251\n"
      "v_cvt_pk_bf16_f32 v212, v248, v249\n"
      "v_mfma_f32_16x16x32_bf16 v[132:135], a[128:131], v[156:159], 0\n"
      "ds_read_b128 v[172:175], v0 offset:1024\n"
      "v_cvt_pk_bf16_f32 v218, v242, v243\n"
      "v_mfma_f32_16x16x32_bf16 v[132:135], a[132:135], v[88:91], v[132:135]\n"
      "v_exp_f32 v46, v46\n"
      "v_mfma_f32_16x16x32_bf16 v[132:135], a[136:139], v[84:87], v[132:135]\n"
      "v_cvt_pk_bf16_f32 v216, v200, v201\n"
      "v_cvt_pk_bf16_f32 v226, v104, v105\n"
      "v_mfma_f32_16x16x32_bf16 v[132:135], a[140:143], v[152:155], v[132:135]\n"
      "buffer_store_dwordx2 v[92:93], v2, s[44:47], s75 offen offset:0 nt sc1\n"
      "v_cvt_pk_bf16_f32 v225, v46, v47\n"
      "v_smfmac_f32_16x16x64_bf16 v[220:223], v[12:15], v[212:219], v5\n"
      "ds_read_b128 v[144:147], v0 offset:0\n"
      "v_cvt_pk_bf16_f32 v28, v28, v32\n"
      "v_mfma_f32_16x16x4_4b_bf16 v[188:203], v[100:101], v[18:19], 0 cbsz:2 abid:2\n"
      "v_pk_max_i16 v96, v96, 0\n"
      "v_cvt_pk_bf16_f32 v227, v106, v107\n"
      "v_cvt_pk_bf16_f32 v245, v130, v131\n"
      "v_cvt_pk_bf16_f32 v243, v134, v135\n"
      "v_cvt_pk_bf16_f32 v30, v30, v34\n"
      "v_cvt_pk_bf16_f32 v34, v31, v35\n"
      "v_smfmac_f32_16x16x32_bf16 v[220:223], v[12:13], v[224:227], v5\n"
      "v_cvt_pk_bf16_f32 v32, v29, v33\n"
      "v_cvt_pk_bf16_f32 v29, v36, v40\n"
      "v_mov_b64 v[92:93], 0\n"
      "v_mfma_f32_16x16x32_bf16 v[188:191], v[212:215], v[108:111], v[188:191]\n"
      "v_cvt_pk_bf16_f32 v249, v254, v255\n"
      "v_cvt_pk_bf16_f32 v242, v132, v133\n"
      "v_mfma_f32_16x16x32_bf16 v[188:191], v[216:219], v[112:115], v[188:191]\n"
      "v_cvt_pk_bf16_f32 v244, v128, v129\n"
      "v_cvt_pk_bf16_f32 v35, v39, v43\n"
      "v_mfma_f32_16x16x32_bf16 v[188:191], v[224:227], v[52:55], v[188:191]\n"
      "v_cvt_pk_bf16_f32 v31, v38, v42\n"
      "v_cvt_pk_bf16_f32 v248, v252, v253\n"
      "v_mfma_f32_16x16x32_bf16 v[252:255], v[76:79], v[242:245], 0\n"
      "v_cvt_pk_bf16_f32 v33, v37, v41\n"
      "v_add_f32 v220, v220, v221\n"
      "v_mfma_f32_16x16x32_bf16 v[252:255], v[80:83], v[246:249], v[252:255]\n"
      "v_mov_b64 v[94:95], 0\n"
      "v_pk_max_i16 v33, v33, 0\n"
      "v_mfma_f32_16x16x32_bf16 v[40:43], v[20:23], v[242:245], 0\n"
      "v_rcp_f32 v221, v220\n"
      "v_mfma_f32_16x16x32_bf16 v[40:43], v[24:27], v[246:249], v[40:43]\n"
      "ds_read_b128 v[132:135], v0 offset:1536\n"
      "ds_read_b128 v[104:107], v0 offset:512\n"
      "v_mfma_f32_16x16x32_bf16 v[36:39], v[204:207], v[242:245], 0\n"
      "v_add_u32 v11, 0x200, v11\n"
      "v_pk_max_i16 v32, v32, 0\n"
      "v_mfma_f32_16x16x32_bf16 v[36:39], v[208:211], v[246:249], v[36:39]\n"
      "v_exp_f32 v254, v254\n"
      "v_smfmac_f32_16x16x64_bf16 v[92:95], v[72:75], v[242:249], v5\n"
      "ds_bpermute_b32 v222, v4, v221 offset:128\n"
      "v_mfma_f32_16x16x32_bf16 v[200:203], v[212:215], v[232:235], v[200:203]\n"
      "buffer_load_dwordx4 v1, s[40:43], s92 offen offset:2048 lds\n"
      "v_mfma_f32_16x16x32_bf16 v[200:203], v[216:219], v[236:239], v[200:203]\n"
      "v_exp_f32 v43, v43\n"
      "v_mfma_f32_16x16x32_bf16 v[200:203], v[224:227], v[228:231], v[200:203]\n"
      "v_exp_f32 v37, v37\n"
      "v_mfma_f32_16x16x32_bf16 v[192:195], v[212:215], v[180:183], v[192:195]\n"
      "v_exp_f32 v42, v42\n"
      "v_mfma_f32_16x16x32_bf16 v[192:195], v[216:219], v[184:187], v[192:195]\n"
      "v_exp_f32 v41, v41\n"
      "v_mfma_f32_16x16x32_bf16 v[192:195], v[224:227], v[120:123], v[192:195]\n"
      "v_exp_f32 v38, v38\n"
      "v_mfma_f32_16x16x32_bf16 v[196:199], v[212:215], v[56:59], v[196:199]\n"
      "v_exp_f32 v40, v40\n"
      "v_mfma_f32_16x16x32_bf16 v[196:199], v[216:219], v[60:63], v[196:199]\n"
      "buffer_load_dwordx4 v1, s[40:43], s92 offen offset:0 lds\n"
      "v_mfma_f32_16x16x32_bf16 v[196:199], v[224:227], v[116:119], v[196:199]\n"
      "v_exp_f32 v253, v253\n"
      "v_pk_max_i16 v31, v31, 0\n"
      "v_mfma_f32_16x16x32_bf16 v[72:75], v[164:167], v[242:245], 0\n"
      "s_waitcnt lgkmcnt(0)\n"
      "v_mul_f32_dpp v203, v222, v203 row_newbcast:12 row_mask:0xf bank_mask:0xf\n"
      "v_mul_f32_dpp v202, v222, v202 row_newbcast:8 row_mask:0xf bank_mask:0xf\n"
      "v_mfma_f32_16x16x32_bf16 v[72:75], v[168:171], v[246:249], v[72:75]\n"
      "v_exp_f32 v255, v255\n"
      "v_mfma_f32_16x16x32_bf16 v[128:131], v[136:139], v[242:245], 0\n"
      "v_mul_f32_dpp v191, v222, v191 row_newbcast:12 row_mask:0xf bank_mask:0xf\n"
      "v_cvt_pk_bf16_f32 v216, v40, v41\n"
      "v_mfma_f32_16x16x32_bf16 v[128:131], v[140:143], v[246:249], v[128:131]\n"
      "v_mul_f32_dpp v194, v222, v194 row_newbcast:8 row_mask:0xf bank_mask:0xf\n"
      "v_mul_f32_dpp v190, v222, v190 row_newbcast:8 row_mask:0xf bank_mask:0xf\n"
      "v_mul_f32_dpp v188, v222, v188 row_newbcast:0 row_mask:0xf bank_mask:0xf\n"
      "v_mfma_f32_16x16x32_bf16 v[224:227], v[64:67], v[242:245], 0\n"
      "v_cvt_pk_bf16_f32 v217, v42, v43\n"
      "v_mul_f32_dpp v196, v222, v196 row_newbcast:0 row_mask:0xf bank_mask:0xf\n"
      "v_mfma_f32_16x16x32_bf16 v[224:227], v[68:71], v[246:249], v[224:227]\n"
      "v_cvt_pk_bf16_f32 v219, v254, v255\n"
      "v_mul_f32_dpp v192, v222, v192 row_newbcast:0 row_mask:0xf bank_mask:0xf\n"
      "v_mfma_f32_16x16x32_bf16 v[244:247], a[176:179], v[144:147], 0\n"
      "v_exp_f32 v130, v130\n"
      "v_mfma_f32_16x16x32_bf16 v[244:247], a[180:183], v[104:107], v[244:247]\n"
      "v_mul_f32_dpp v198, v222, v198 row_newbcast:8 row_mask:0xf bank_mask:0xf\n"
      "v_mul_f32_dpp v200, v222, v200 row_newbcast:0 row_mask:0xf bank_mask:0xf\n"
      "v_mfma_f32_16x16x32_bf16 v[244:247], a[184:187], v[172:175], v[244:247]\n"
      "v_exp_f32 v131, v131\n"
      "v_mfma_f32_16x16x32_bf16 v[244:247], a[188:191], v[132:135], v[244:247]\n"
      "v_exp_f32 v226, v226\n"
      "v_mfma_f32_16x16x32_bf16 v[212:215], a[160:163], v[144:147], 0\n"
      "s_add_u32 m0, s53, 0\n"
      "v_exp_f32 v75, v75\n"
      "v_mfma_f32_16x16x32_bf16 v[212:215], a[164:167], v[104:107], v[212:215]\n"
      "v_exp_f32 v252, v252\n"
      "v_mfma_f32_16x16x32_bf16 v[212:215], a[168:171], v[172:175], v[212:215]\n"
      "v_exp_f32 v39, v39\n"
      "v_mfma_f32_16x16x32_bf16 v[212:215], a[172:175], v[132:135], v[212:215]\n"
      "v_cvt_pk_bf16_f32 v218, v252, v253\n"
      "v_cvt_pk_bf16_f32 v252, v244, v245\n"
      "v_mfma_f32_16x16x32_bf16 v[40:43], a[128:131], v[144:147], 0\n"
      "v_mul_f32_dpp v189, v222, v189 row_newbcast:4 row_mask:0xf bank_mask:0xf\n"
      "v_cvt_pk_bf16_f32 v253, v246, v247\n"
      "v_mfma_f32_16x16x32_bf16 v[40:43], a[132:135], v[104:107], v[40:43]\n"
      "v_exp_f32 v129, v129\n"
      "v_mfma_f32_16x16x32_bf16 v[40:43], a[136:139], v[172:175], v[40:43]\n"
      "v_exp_f32 v227, v227\n"
      "v_mfma_f32_16x16x32_bf16 v[40:43], a[140:143], v[132:135], v[40:43]\n"
      "v_exp_f32 v72, v72\n"
      "v_mfma_f32_16x16x32_bf16 v[244:247], a[144:147], v[144:147], 0\n"
      "v_exp_f32 v74, v74\n"
      "v_mfma_f32_16x16x32_bf16 v[244:247], a[148:151], v[104:107], v[244:247]\n"
      "v_mul_f32_dpp v199, v222, v199 row_newbcast:12 row_mask:0xf bank_mask:0xf\n"
      "v_mul_f32_dpp v195, v222, v195 row_newbcast:12 row_mask:0xf bank_mask:0xf\n"
      "v_mfma_f32_16x16x32_bf16 v[244:247], a[152:155], v[172:175], v[244:247]\n"
      "v_exp_f32 v73, v73\n"
      "v_mfma_f32_16x16x32_bf16 v[244:247], a[156:159], v[132:135], v[244:247]\n"
      "v_cvt_pk_bf16_f32 v251, v214, v215\n"
      "v_mul_f32_dpp v201, v222, v201 row_newbcast:4 row_mask:0xf bank_mask:0xf\n"
      "v_cvt_pk_bf16_f32 v250, v212, v213\n"
      "v_mul_f32_dpp v197, v222, v197 row_newbcast:4 row_mask:0xf bank_mask:0xf\n"
      "v_mfma_f32_16x16x32_bf16 v[200:203], v[160:163], a[240:243], v[200:203]\n"
      "v_exp_f32 v128, v128\n"
      "v_mfma_f32_16x16x32_bf16 v[200:203], v[176:179], a[244:247], v[200:203]\n"
      "s_nop 0\n"
      "v_cvt_pk_bf16_f32 v249, v246, v247\n"
      "v_cvt_pk_bf16_f32 v248, v244, v245\n"
      "v_mfma_f32_16x16x32_bf16 v[200:203], v[148:151], a[248:251], v[200:203]\n"
      "v_cvt_pk_bf16_f32 v215, v38, v39\n"
      "v_cvt_pk_bf16_f32 v246, v40, v41\n"
      "v_mfma_f32_16x16x32_bf16 v[200:203], v[124:127], a[252:255], v[200:203]\n"
      "v_cvt_pk_bf16_f32 v247, v42, v43\n"
      "v_pk_max_i16 v97, v97, 0\n"
      "s_nop 0\n"
      "v_smfmac_f32_16x16x64_bf16 v[92:95], v[48:51], v[246:253], v5\n"
      "buffer_load_dwordx4 v1, s[40:43], s95 offen offset:1024 lds\n"
      "v_mfma_f32_16x16x32_bf16 v[188:191], v[160:163], a[192:195], v[188:191]\n"
      "v_mul_f32_dpp v193, v222, v193 row_newbcast:4 row_mask:0xf bank_mask:0xf\n"
      "v_cvt_pk_bf16_f32 v213, v74, v75\n"
      "v_mfma_f32_16x16x32_bf16 v[188:191], v[176:179], a[196:199], v[188:191]\n"
      "v_exp_f32 v224, v224\n"
      "v_mfma_f32_16x16x32_bf16 v[188:191], v[148:151], a[200:203], v[188:191]\n"
      "v_exp_f32 v36, v36\n"
      "v_mfma_f32_16x16x32_bf16 v[188:191], v[124:127], a[204:207], v[188:191]\n"
      "v_cvt_pk_bf16_f32 v75, v130, v131\n"
      "v_add_f32 v92, v92, v93\n"
      "v_add_f32 v94, v94, v95\n"
      "v_mfma_f32_16x16x32_bf16 v[192:195], v[160:163], a[208:211], v[192:195]\n"
      "v_exp_f32 v92, v92\n"
      "v_mfma_f32_16x16x32_bf16 v[192:195], v[176:179], a[212:215], v[192:195]\n"
      "v_exp_f32 v94, v94\n"
      "v_mfma_f32_16x16x32_bf16 v[192:195], v[148:151], a[216:219], v[192:195]\n"
      "v_add_f32 v243, v92, v94\n"
      "v_cvt_pk_bf16_f32 v130, v92, v94\n"
      "v_mfma_f32_16x16x32_bf16 v[192:195], v[124:127], a[220:223], v[192:195]\n"
      "v_cvt_pk_bf16_f32 v212, v72, v73\n"
      "v_cvt_pk_bf16_f32 v74, v128, v129\n"
      "v_mfma_f32_16x16x32_bf16 v[196:199], v[160:163], a[224:227], v[196:199]\n"
      "v_cvt_pk_bf16_f32 v214, v36, v37\n"
      "v_add_f32 v242, v92, v94\n"
      "v_mfma_f32_16x16x32_bf16 v[196:199], v[176:179], a[228:231], v[196:199]\n"
      "s_nop 0\n"
      "v_permlane16_swap_b32 v242, v243\n"
      "v_mfma_f32_16x16x32_bf16 v[196:199], v[148:151], a[232:235], v[196:199]\n"
      "v_cvt_pk_bf16_f32 v131, v92, v94\n"
      "v_cvt_pk_bf16_f32 v188, v188, v192\n"
      "v_mfma_f32_16x16x32_bf16 v[196:199], v[124:127], a[236:239], v[196:199]\n"
      "v_permlane16_swap_b32 v130, v131\n"
      "v_mov_b64 v[128:129], v[242:243]\n"
      "v_smfmac_f32_16x16x64_bf16 v[242:245], v[12:15], v[212:219], v5\n"
      "v_exp_f32 v225, v225\n"
      "v_mfma_f32_16x16x4_4b_bf16 v[36:51], v[130:131], v[18:19], 0 cbsz:2 abid:0\n"
      "v_pk_max_i16 v28, v28, 0\n"
      "v_cvt_pk_bf16_f32 v72, v224, v225\n"
      "v_pk_max_i16 v35, v35, 0\n"
      "v_cvt_pk_bf16_f32 v73, v226, v227\n"
      "s_nop 1\n"
      "v_smfmac_f32_16x16x32_bf16 v[242:245], v[12:13], v[72:75], v5\n"
      "buffer_store_dwordx2 v[96:97], v2, s[44:47], s75 offen offset:256 nt sc1\n"
      "s_nop 1\n"
      "v_mfma_f32_16x16x32_bf16 v[48:51], v[212:215], v[232:235], v[48:51]\n"
      "v_maximum3_f32 v8, v8, v220, v221\n"
      "v_mfma_f32_16x16x32_bf16 v[48:51], v[216:219], v[236:239], v[48:51]\n"
      "s_nop 1\n"
      "v_add_f32 v242, v242, v243\n"
      "v_mfma_f32_16x16x32_bf16 v[48:51], v[72:75], v[228:231], v[48:51]\n"
      "v_rcp_f32 v243, v242\n"
      "v_mfma_f32_16x16x32_bf16 v[44:47], v[212:215], v[56:59], v[44:47]\n"
      "ds_bpermute_b32 v244, v4, v243\n"
      "v_mfma_f32_16x16x32_bf16 v[44:47], v[216:219], v[60:63], v[44:47]\n"
      "v_pk_max_i16 v29, v29, 0\n"
      "v_mfma_f32_16x16x32_bf16 v[44:47], v[72:75], v[116:119], v[44:47]\n"
      "v_cvt_pk_bf16_f32 v190, v190, v194\n"
      "v_pk_max_i16 v190, v190, 0\n"
      "v_mfma_f32_16x16x32_bf16 v[40:43], v[212:215], v[180:183], v[40:43]\n"
      "v_mfma_f32_16x16x32_bf16 v[40:43], v[216:219], v[184:187], v[40:43]\n"
      "v_add_u32 v10, 64, v10\n"
      "v_mfma_f32_16x16x32_bf16 v[40:43], v[72:75], v[120:123], v[40:43]\n"
      "buffer_load_dwordx4 v1, s[40:43], s95 offen offset:3072 lds\n"
      "v_mfma_f32_16x16x32_bf16 v[36:39], v[212:215], v[108:111], v[36:39]\n"
      "v_cvt_pk_bf16_f32 v192, v189, v193\n"
      "v_pk_max_i16 v34, v34, 0\n"
      "v_mfma_f32_16x16x32_bf16 v[36:39], v[216:219], v[112:115], v[36:39]\n"
      "v_pk_max_i16 v99, v99, 0\n"
      "v_pk_max_i16 v30, v30, 0\n"
      "v_mfma_f32_16x16x32_bf16 v[36:39], v[72:75], v[52:55], v[36:39]\n"
      "buffer_store_dwordx2 v[98:99], v2, s[44:47], s75 offen offset:768 nt sc1\n"
      "v_cvt_pk_bf16_f32 v194, v191, v195\n"
      "v_mfma_f32_16x16x4_4b_bf16 v[212:227], v[130:131], v[18:19], 0 cbsz:2 abid:2\n"
      "s_waitcnt lgkmcnt(0)\n"
      "v_mul_f32_dpp v43, v244, v43 row_newbcast:12 row_mask:0xf bank_mask:0xf\n"
      "v_mul_f32_dpp v41, v244, v41 row_newbcast:4 row_mask:0xf bank_mask:0xf\n"
      "v_mul_f32_dpp v40, v244, v40 row_newbcast:0 row_mask:0xf bank_mask:0xf\n"
      "v_mul_f32_dpp v42, v244, v42 row_newbcast:8 row_mask:0xf bank_mask:0xf\n"
      "v_mul_f32_dpp v39, v244, v39 row_newbcast:12 row_mask:0xf bank_mask:0xf\n"
      "v_mul_f32_dpp v51, v244, v51 row_newbcast:12 row_mask:0xf bank_mask:0xf\n"
      "v_mul_f32_dpp v37, v244, v37 row_newbcast:4 row_mask:0xf bank_mask:0xf\n"
      "v_cvt_pk_bf16_f32 v193, v197, v201\n"
      "v_mfma_f32_16x16x32_bf16 v[40:43], v[156:159], a[208:211], v[40:43]\n"
      "v_pk_max_i16 v194, v194, 0\n"
      "v_mul_f32_dpp v46, v244, v46 row_newbcast:8 row_mask:0xf bank_mask:0xf\n"
      "v_mfma_f32_16x16x32_bf16 v[40:43], v[88:91], a[212:215], v[40:43]\n"
      "v_mul_f32_dpp v44, v244, v44 row_newbcast:0 row_mask:0xf bank_mask:0xf\n"
      "v_mul_f32_dpp v48, v244, v48 row_newbcast:0 row_mask:0xf bank_mask:0xf\n"
      "v_mfma_f32_16x16x32_bf16 v[40:43], v[84:87], a[216:219], v[40:43]\n"
      "v_mul_f32_dpp v36, v244, v36 row_newbcast:0 row_mask:0xf bank_mask:0xf\n"
      "v_mul_f32_dpp v38, v244, v38 row_newbcast:8 row_mask:0xf bank_mask:0xf\n"
      "v_mfma_f32_16x16x32_bf16 v[40:43], v[152:155], a[220:223], v[40:43]\n"
      "v_mul_f32_dpp v47, v244, v47 row_newbcast:12 row_mask:0xf bank_mask:0xf\n"
      "v_cvt_pk_bf16_f32 v189, v196, v200\n"
      "v_mfma_f32_16x16x32_bf16 v[36:39], v[156:159], a[192:195], v[36:39]\n"
      "v_cvt_pk_bf16_f32 v195, v199, v203\n"
      "v_pk_max_i16 v195, v195, 0\n"
      "v_mfma_f32_16x16x32_bf16 v[36:39], v[88:91], a[196:199], v[36:39]\n"
      "s_add_u32 s44, s44, 102400\n s_addc_u32 s45, s45, 0\n"
      "v_mul_f32_dpp v49, v244, v49 row_newbcast:4 row_mask:0xf bank_mask:0xf\n"
      "v_mfma_f32_16x16x32_bf16 v[36:39], v[84:87], a[200:203], v[36:39]\n"
      "v_add_u32 v9, 64, v9\n"
      "v_mul_f32_dpp v45, v244, v45 row_newbcast:4 row_mask:0xf bank_mask:0xf\n"
      "v_mfma_f32_16x16x32_bf16 v[36:39], v[152:155], a[204:207], v[36:39]\n"
      "buffer_load_dwordx4 v1, s[40:43], s94 offen offset:2048 lds\n"
      "v_mfma_f32_16x16x32_bf16 v[44:47], v[156:159], a[224:227], v[44:47]\n"
      "v_mul_f32_dpp v50, v244, v50 row_newbcast:8 row_mask:0xf bank_mask:0xf\n"
      "v_pk_max_i16 v188, v188, 0\n"
      "v_mfma_f32_16x16x32_bf16 v[44:47], v[88:91], a[228:231], v[44:47]\n"
      "v_mfma_f32_16x16x32_bf16 v[44:47], v[84:87], a[232:235], v[44:47]\n"
      "v_pk_max_i16 v189, v189, 0\n"
      "v_mfma_f32_16x16x32_bf16 v[44:47], v[152:155], a[236:239], v[44:47]\n"
      "v_pk_max_i16 v192, v192, 0\n"
      "v_cvt_pk_bf16_f32 v191, v198, v202\n"
      "v_mfma_f32_16x16x32_bf16 v[48:51], v[156:159], a[240:243], v[48:51]\n"
      "v_maximum3_f32 v8, v8, v242, v243\n"
      "v_pk_max_i16 v191, v191, 0\n"
      "v_mfma_f32_16x16x32_bf16 v[48:51], v[88:91], a[244:247], v[48:51]\n"
      "buffer_load_dwordx4 v1, s[40:43], s94 offen offset:0 lds\n"
      "v_mfma_f32_16x16x32_bf16 v[48:51], v[84:87], a[248:251], v[48:51]\n"
      "s_add_u32 s40, s40, 102400\n s_addc_u32 s41, s41, 0\n"
      "v_pk_max_i16 v193, v193, 0\n"
      "v_mfma_f32_16x16x32_bf16 v[48:51], v[152:155], a[252:255], v[48:51]\n"
      "s_nop 0\n"
      "L_task_%=:\n"
      "v_mfma_f32_16x16x32_bf16 v[240:243], v[136:139], v[246:249], 0\n"
      "s_waitcnt vmcnt(29)\n"
      "ds_read_b128 v[92:95], v0 offset:20480\n"
      "ds_read_b128 v[96:99], v0 offset:21504\n"
      "v_mfma_f32_16x16x32_bf16 v[240:243], v[140:143], v[250:253], v[240:243]\n"
      "buffer_store_dwordx2 v[28:29], v2, s[44:47], s100 offen offset:0 nt sc1\n"
      "ds_read_b128 v[88:91], v0 offset:20992\n"
      "v_mfma_f32_16x16x32_bf16 v[156:159], v[76:79], v[246:249], 0\n"
      "s_waitcnt vmcnt(14)\n"
      "ds_read_b128 v[160:163], v0 offset:8192\n"
      "ds_read_b128 v[200:203], v0 offset:9728\n"
      "v_mfma_f32_16x16x32_bf16 v[156:159], v[80:83], v[250:253], v[156:159]\n"
      "ds_read_b128 v[84:87], v0 offset:9216\n"
      "ds_read_b128 v[148:151], v0 offset:8704\n"
      "v_mfma_f32_16x16x32_bf16 v[100:103], v[64:67], v[246:249], 0\n"
      "ds_read_b128 v[124:127], v0 offset:16384\n"
      "ds_read_b128 v[72:75], v0 offset:17408\n"
      "v_mfma_f32_16x16x32_bf16 v[100:103], v[68:71], v[250:253], v[100:103]\n"
      "s_nop 1\n"
      "v_exp_f32 v158, v158\n"
      "v_mfma_f32_16x16x32_bf16 v[64:67], v[164:167], v[246:249], 0\n"
      "v_cvt_pk_bf16_f32 v36, v36, v40\n"
      "v_mfma_f32_16x16x32_bf16 v[64:67], v[168:171], v[250:253], v[64:67]\n"
      "v_exp_f32 v157, v157\n"
      "v_mfma_f32_16x16x32_bf16 v[168:171], v[204:207], v[246:249], 0\n"
      "v_exp_f32 v103, v103\n"
      "v_mfma_f32_16x16x32_bf16 v[168:171], v[208:211], v[250:253], v[168:171]\n"
      "v_exp_f32 v102, v102\n"
      "v_mfma_f32_16x16x32_bf16 v[152:155], v[20:23], v[246:249], 0\n"
      "v_exp_f32 v242, v242\n"
      "v_mfma_f32_16x16x32_bf16 v[152:155], v[24:27], v[250:253], v[152:155]\n"
      "s_waitcnt lgkmcnt(5)\n"
      "v_mfma_f32_16x16x32_bf16 v[80:83], a[32:35], v[160:163], 0\n"
      "s_waitcnt lgkmcnt(2)\n"
      "v_mfma_f32_16x16x32_bf16 v[80:83], a[36:39], v[148:151], v[80:83]\n"
      "v_mfma_f32_16x16x32_bf16 v[80:83], a[40:43], v[84:87], v[80:83]\n"
      "v_cvt_pk_bf16_f32 v40, v37, v41\n"
      "v_mfma_f32_16x16x32_bf16 v[80:83], a[44:47], v[200:203], v[80:83]\n"
      "v_exp_f32 v159, v159\n"
      "v_mfma_f32_16x16x32_bf16 v[140:143], a[48:51], v[160:163], 0\n"
      "v_exp_f32 v153, v153\n"
      "v_mfma_f32_16x16x32_bf16 v[140:143], a[52:55], v[148:151], v[140:143]\n"
      "v_cvt_pk_bf16_f32 v211, v158, v159\n"
      "v_mfma_f32_16x16x32_bf16 v[140:143], a[56:59], v[84:87], v[140:143]\n"
      "v_exp_f32 v101, v101\n"
      "v_mfma_f32_16x16x32_bf16 v[140:143], a[60:63], v[200:203], v[140:143]\n"
      "ds_read_b128 v[176:179], v0 offset:22016\n"
      "v_mfma_f32_16x16x32_bf16 v[164:167], v[160:163], a[80:83], 0\n"
      "v_exp_f32 v243, v243\n"
      "v_mfma_f32_16x16x32_bf16 v[164:167], v[148:151], a[84:87], v[164:167]\n"
      "v_exp_f32 v66, v66\n"
      "v_mfma_f32_16x16x32_bf16 v[164:167], v[84:87], a[88:91], v[164:167]\n"
      "v_mfma_f32_16x16x32_bf16 v[164:167], v[200:203], a[92:95], v[164:167]\n"
      "buffer_store_dwordx2 v[190:191], v2, s[44:47], s101 offen offset:512 nt sc1\n"
      "v_mfma_f32_16x16x32_bf16 v[76:79], v[160:163], a[64:67], 0\n"
      "ds_read_b128 v[196:199], v0 offset:17920\n"
      "v_mfma_f32_16x16x32_bf16 v[76:79], v[148:151], a[68:71], v[76:79]\n"
      "v_mfma_f32_16x16x32_bf16 v[76:79], v[84:87], a[72:75], v[76:79]\n"
      "v_mfma_f32_16x16x32_bf16 v[76:79], v[200:203], a[76:79], v[76:79]\n"
      "s_add_u32 m0, s53, 20480\n"
      "v_exp_f32 v64, v64\n"
      "v_mfma_f32_16x16x32_bf16 v[136:139], v[160:163], a[112:115], 0\n"
      "v_exp_f32 v156, v156\n"
      "v_mfma_f32_16x16x32_bf16 v[136:139], v[148:151], a[116:119], v[136:139]\n"
      "v_cvt_pk_bf16_f32 v210, v156, v157\n"
      "v_mfma_f32_16x16x32_bf16 v[136:139], v[84:87], a[120:123], v[136:139]\n"
      "v_cvt_pk_bf16_f32 v38, v38, v42\n"
      "v_mfma_f32_16x16x32_bf16 v[136:139], v[200:203], a[124:127], v[136:139]\n"
      "v_exp_f32 v154, v154\n"
      "v_mfma_f32_16x16x32_bf16 v[68:71], a[0:3], v[160:163], 0\n"
      "v_exp_f32 v241, v241\n"
      "v_mfma_f32_16x16x32_bf16 v[68:71], a[4:7], v[148:151], v[68:71]\n"
      "v_exp_f32 v152, v152\n"
      "v_mfma_f32_16x16x32_bf16 v[68:71], a[8:11], v[84:87], v[68:71]\n"
      "v_exp_f32 v240, v240\n"
      "v_mfma_f32_16x16x32_bf16 v[68:71], a[12:15], v[200:203], v[68:71]\n"
      "v_cvt_pk_bf16_f32 v41, v45, v49\n"
      "v_pk_max_i16 v41, v41, 0\n"
      "v_mfma_f32_16x16x32_bf16 v[252:255], a[16:19], v[160:163], 0\n"
      "s_waitcnt lgkmcnt(1)\n"
      "buffer_load_dwordx4 v1, s[40:43], s74 offen offset:1024 lds\n"
      "v_mfma_f32_16x16x32_bf16 v[252:255], a[20:23], v[148:151], v[252:255]\n"
      "v_exp_f32 v155, v155\n"
      "v_mfma_f32_16x16x32_bf16 v[252:255], a[24:27], v[84:87], v[252:255]\n"
      "v_exp_f32 v65, v65\n"
      "v_mfma_f32_16x16x32_bf16 v[252:255], a[28:31], v[200:203], v[252:255]\n"
      "v_cvt_pk_bf16_f32 v209, v154, v155\n"
      "v_cvt_pk_bf16_f32 v208, v152, v153\n"
      "v_mfma_f32_16x16x32_bf16 v[152:155], v[160:163], a[96:99], 0\n"
      "v_cvt_pk_bf16_f32 v42, v39, v43\n"
      "v_cvt_pk_bf16_f32 v19, v102, v103\n"
      "v_mfma_f32_16x16x32_bf16 v[152:155], v[148:151], a[100:103], v[152:155]\n"
      "v_exp_f32 v100, v100\n"
      "v_mfma_f32_16x16x32_bf16 v[152:155], v[84:87], a[104:107], v[152:155]\n"
      "ds_read2_b32 v[160:161], v16 offset0:0 offset1:2\n"
      "v_mfma_f32_16x16x32_bf16 v[152:155], v[200:203], a[108:111], v[152:155]\n"
      "buffer_store_dwordx2 v[32:33], v2, s[44:47], s100 offen offset:256 nt sc1\n"
      "v_cvt_pk_bf16_f32 v25, v82, v83\n"
      "v_mfma_f32_16x16x32_bf16 v[148:151], a[176:179], v[92:95], 0\n"
      "v_exp_f32 v168, v168\n"
      "v_mfma_f32_16x16x32_bf16 v[148:151], a[180:183], v[88:91], v[148:151]\n"
      "v_cvt_pk_bf16_f32 v21, v242, v243\n"
      "v_mfma_f32_16x16x32_bf16 v[148:151], a[184:187], v[96:99], v[148:151]\n"
      "v_exp_f32 v170, v170\n"
      "v_mfma_f32_16x16x32_bf16 v[148:151], a[188:191], v[176:179], v[148:151]\n"
      "v_exp_f32 v67, v67\n"
      "v_mfma_f32_16x16x32_bf16 v[200:203], a[160:163], v[92:95], 0\n"
      "v_mfma_f32_16x16x32_bf16 v[200:203], a[164:167], v[88:91], v[200:203]\n"
      "v_exp_f32 v171, v171\n"
      "v_mfma_f32_16x16x32_bf16 v[200:203], a[168:171], v[96:99], v[200:203]\n"
      "v_mfma_f32_16x16x32_bf16 v[200:203], a[172:175], v[176:179], v[200:203]\n"
      "v_cvt_pk_bf16_f32 v205, v66, v67\n"
      "v_cvt_pk_bf16_f32 v204, v64, v65\n"
      "v_mfma_f32_16x16x32_bf16 v[64:67], a[32:35], v[92:95], 0\n"
      "v_mfma_f32_16x16x32_bf16 v[64:67], a[36:39], v[88:91], v[64:67]\n"
      "v_cvt_pk_bf16_f32 v18, v100, v101\n"
      "v_cvt_pk_bf16_f32 v207, v170, v171\n"
      "v_mfma_f32_16x16x32_bf16 v[64:67], a[40:43], v[96:99], v[64:67]\n"
      "v_mfma_f32_16x16x32_bf16 v[64:67], a[44:47], v[176:179], v[64:67]\n"
      "v_exp_f32 v169, v169\n"
      "v_mfma_f32_16x16x32_bf16 v[248:251], a[0:3], v[92:95], 0\n"
      "v_mfma_f32_16x16x32_bf16 v[248:251], a[4:7], v[88:91], v[248:251]\n"
      "v_cvt_pk_bf16_f32 v206, v168, v169\n"
      "v_mfma_f32_16x16x32_bf16 v[248:251], a[8:11], v[96:99], v[248:251]\n"
      "v_cvt_pk_bf16_f32 v244, v200, v201\n"
      "v_mfma_f32_16x16x32_bf16 v[248:251], a[12:15], v[176:179], v[248:251]\n"
      "ds_read_b128 v[100:103], v0 offset:16896\n"
      "v_cvt_pk_bf16_f32 v20, v240, v241\n"
      "v_smfmac_f32_16x16x64_bf16 v[128:131], v[12:15], v[204:211], v5\n"
      "v_mfma_f32_16x16x32_bf16 v[224:227], v[204:207], v[232:235], v[224:227]\n"
      "v_mfma_f32_16x16x32_bf16 v[224:227], v[208:211], v[236:239], v[224:227]\n"
      "v_cvt_pk_bf16_f32 v43, v47, v51\n"
      "v_cvt_pk_bf16_f32 v245, v202, v203\n"
      "v_mfma_f32_16x16x32_bf16 v[224:227], v[18:21], v[228:231], v[224:227]\n"
      "buffer_load_dwordx4 v1, s[40:43], s73 offen offset:2048 lds\n"
      "v_smfmac_f32_16x16x32_bf16 v[128:131], v[12:13], v[18:21], v5\n"
      "buffer_store_dwordx2 v[192:193], v2, s[44:47], s101 offen offset:256 nt sc1\n"
      "v_mfma_f32_16x16x32_bf16 v[232:235], a[16:19], v[92:95], 0\n"
      "v_mfma_f32_16x16x32_bf16 v[232:235], a[20:23], v[88:91], v[232:235]\n"
      "ds_read_b128 v[168:171], v9 offset:2048\n"
      "v_mfma_f32_16x16x32_bf16 v[232:235], a[24:27], v[96:99], v[232:235]\n"
      "ds_read2_b32 v[162:163], v16 offset0:128 offset1:130\n"
      "v_mfma_f32_16x16x32_bf16 v[232:235], a[28:31], v[176:179], v[232:235]\n"
      "v_cvt_pk_bf16_f32 v37, v44, v48\n"
      "v_cvt_pk_bf16_f32 v24, v80, v81\n"
      "v_mfma_f32_16x16x32_bf16 v[220:223], v[204:207], v[56:59], v[220:223]\n"
      "v_mfma_f32_16x16x32_bf16 v[220:223], v[208:211], v[60:63], v[220:223]\n"
      "s_waitcnt vmcnt(12)\n"
      "ds_read_b128 v[80:83], v0 offset:4096\n"
      "v_mfma_f32_16x16x32_bf16 v[220:223], v[18:21], v[116:119], v[220:223]\n"
      "buffer_load_dwordx4 v1, s[40:43], s74 offen offset:3072 lds\n"
      "v_mfma_f32_16x16x32_bf16 v[212:215], v[204:207], v[108:111], v[212:215]\n"
      "v_mfma_f32_16x16x32_bf16 v[212:215], v[208:211], v[112:115], v[212:215]\n"
      "ds_read_b128 v[116:119], v0 offset:5632\n"
      "v_add_f32 v128, v128, v129\n"
      "v_mfma_f32_16x16x32_bf16 v[212:215], v[18:21], v[52:55], v[212:215]\n"
      "v_cvt_pk_bf16_f32 v22, v252, v253\n"
      "v_cvt_pk_bf16_f32 v236, v136, v137\n"
      "v_mfma_f32_16x16x32_bf16 v[200:203], a[16:19], v[124:127], 0\n"
      "v_cvt_pk_bf16_f32 v136, v248, v249\n"
      "s_waitcnt lgkmcnt(4)\n"
      "v_mfma_f32_16x16x32_bf16 v[200:203], a[20:23], v[100:103], v[200:203]\n"
      "ds_read_b128 v[108:111], v0 offset:4608\n"
      "v_mfma_f32_16x16x32_bf16 v[200:203], a[24:27], v[72:75], v[200:203]\n"
      "v_rcp_f32 v129, v128\n"
      "v_mfma_f32_16x16x32_bf16 v[200:203], a[28:31], v[196:199], v[200:203]\n"
      "ds_bpermute_b32 v130, v4, v129 offset:128\n"
      "v_mfma_f32_16x16x32_bf16 v[216:219], v[204:207], v[180:183], v[216:219]\n"
      "v_mfma_f32_16x16x32_bf16 v[216:219], v[208:211], v[184:187], v[216:219]\n"
      "v_cvt_pk_bf16_f32 v184, v164, v165\n"
      "v_cvt_pk_bf16_f32 v27, v142, v143\n"
      "v_mfma_f32_16x16x32_bf16 v[216:219], v[18:21], v[120:123], v[216:219]\n"
      "v_mfma_f32_16x16x32_bf16 v[56:59], a[128:131], v[124:127], 0\n"
      "v_cvt_pk_bf16_f32 v237, v138, v139\n"
      "v_pk_max_i16 v38, v38, 0\n"
      "v_mfma_f32_16x16x32_bf16 v[56:59], a[132:135], v[100:103], v[56:59]\n"
      "ds_read_b128 v[52:55], v10 offset:0\n"
      "v_cvt_pk_bf16_f32 v185, v166, v167\n"
      "v_mfma_f32_16x16x32_bf16 v[56:59], a[136:139], v[72:75], v[56:59]\n"
      "v_cvt_pk_bf16_f32 v21, v70, v71\n"
      "v_mfma_f32_16x16x32_bf16 v[56:59], a[140:143], v[196:199], v[56:59]\n"
      "buffer_store_dwordx2 v[30:31], v2, s[44:47], s100 offen offset:512 nt sc1\n"
      "ds_read_b128 v[180:183], v0 offset:5120\n"
      "v_mfma_f32_16x16x32_bf16 v[208:211], a[48:51], v[92:95], 0\n"
      "v_mfma_f32_16x16x32_bf16 v[208:211], a[52:55], v[88:91], v[208:211]\n"
      "s_waitcnt lgkmcnt(2)\n"
      "v_mul_f32_dpp v219, v130, v219 row_newbcast:12 row_mask:0xf bank_mask:0xf\n"
      "v_mul_f32_dpp v216, v130, v216 row_newbcast:0 row_mask:0xf bank_mask:0xf\n"
      "v_mfma_f32_16x16x32_bf16 v[208:211], a[56:59], v[96:99], v[208:211]\n"
      "v_cvt_pk_bf16_f32 v20, v68, v69\n"
      "v_mul_f32_dpp v220, v130, v220 row_newbcast:0 row_mask:0xf bank_mask:0xf\n"
      "v_mfma_f32_16x16x32_bf16 v[208:211], a[60:63], v[176:179], v[208:211]\n"
      "v_cvt_pk_bf16_f32 v246, v148, v149\n"
      "v_mfma_f32_16x16x32_bf16 v[112:115], a[128:131], v[92:95], 0\n"
      "v_cvt_pk_bf16_f32 v60, v152, v153\n"
      "v_mfma_f32_16x16x32_bf16 v[112:115], a[132:135], v[88:91], v[112:115]\n"
      "v_mfma_f32_16x16x32_bf16 v[112:115], a[136:139], v[96:99], v[112:115]\n"
      "v_mfma_f32_16x16x32_bf16 v[112:115], a[140:143], v[176:179], v[112:115]\n"
      "v_pk_max_i16 v36, v36, 0\n"
      "v_cvt_pk_bf16_f32 v247, v150, v151\n"
      "v_mfma_f32_16x16x32_bf16 v[148:151], a[16:19], v[80:83], 0\n"
      "v_mfma_f32_16x16x32_bf16 v[148:151], a[20:23], v[108:111], v[148:151]\n"
      "s_waitcnt lgkmcnt(0)\n"
      "v_mfma_f32_16x16x32_bf16 v[148:151], a[24:27], v[180:183], v[148:151]\n"
      "ds_read_b128 v[164:167], v9 offset:0\n"
      "v_mul_f32_dpp v217, v130, v217 row_newbcast:4 row_mask:0xf bank_mask:0xf\n"
      "v_mfma_f32_16x16x32_bf16 v[148:151], a[28:31], v[116:119], v[148:151]\n"
      "v_mul_f32_dpp v214, v130, v214 row_newbcast:8 row_mask:0xf bank_mask:0xf\n"
      "v_mfma_f32_16x16x32_bf16 v[68:71], a[144:147], v[92:95], 0\n"
      "buffer_load_dwordx4 v1, s[40:43], s73 offen offset:0 lds\n"
      "v_mfma_f32_16x16x32_bf16 v[68:71], a[148:151], v[88:91], v[68:71]\n"
      "v_cvt_pk_bf16_f32 v142, v208, v209\n"
      "v_cvt_pk_bf16_f32 v26, v140, v141\n"
      "v_mfma_f32_16x16x32_bf16 v[68:71], a[152:155], v[96:99], v[68:71]\n"
      "v_mfma_f32_16x16x32_bf16 v[68:71], a[156:159], v[176:179], v[68:71]\n"
      "v_cvt_pk_bf16_f32 v39, v46, v50\n"
      "v_mfma_f32_16x16x32_bf16 v[48:51], a[32:35], v[80:83], 0\n"
      "v_mfma_f32_16x16x32_bf16 v[48:51], a[36:39], v[108:111], v[48:51]\n"
      "v_mul_f32_dpp v225, v130, v225 row_newbcast:4 row_mask:0xf bank_mask:0xf\n"
      "v_cvt_pk_bf16_f32 v139, v234, v235\n"
      "v_mfma_f32_16x16x32_bf16 v[48:51], a[40:43], v[180:183], v[48:51]\n"
      "s_add_u32 m0, s53, 16384\n"
      "v_cvt_pk_bf16_f32 v240, v112, v113\n"
      "v_mov_b64 v[152:153], 0\n"
      "v_mfma_f32_16x16x32_bf16 v[48:51], a[44:47], v[116:119], v[48:51]\n"
      "buffer_store_dwordx2 v[194:195], v2, s[44:47], s101 offen offset:768 nt sc1\n"
      "v_cvt_pk_bf16_f32 v23, v254, v255\n"
      "v_mfma_f32_16x16x32_bf16 v[120:123], a[32:35], v[124:127], 0\n"
      "v_cvt_pk_bf16_f32 v61, v154, v155\n"
      "v_cvt_pk_bf16_f32 v241, v114, v115\n"
      "v_mfma_f32_16x16x32_bf16 v[120:123], a[36:39], v[100:103], v[120:123]\n"
      "v_mfma_f32_16x16x32_bf16 v[120:123], a[40:43], v[72:75], v[120:123]\n"
      "v_mfma_f32_16x16x32_bf16 v[120:123], a[44:47], v[196:199], v[120:123]\n"
      "v_mov_b64 v[230:231], 0\n"
      "v_cvt_pk_bf16_f32 v208, v48, v49\n"
      "v_mfma_f32_16x16x32_bf16 v[28:31], v[80:83], a[112:115], 0\n"
      "v_mfma_f32_16x16x32_bf16 v[28:31], v[108:111], a[116:119], v[28:31]\n"
      "v_mfma_f32_16x16x32_bf16 v[28:31], v[180:183], a[120:123], v[28:31]\n"
      "v_cvt_pk_bf16_f32 v207, v150, v151\n"
      "v_mul_f32_dpp v212, v130, v212 row_newbcast:0 row_mask:0xf bank_mask:0xf\n"
      "v_mfma_f32_16x16x32_bf16 v[28:31], v[116:119], a[124:127], v[28:31]\n"
      "v_mul_f32_dpp v218, v130, v218 row_newbcast:8 row_mask:0xf bank_mask:0xf\n"
      "v_cvt_pk_bf16_f32 v143, v210, v211\n"
      "v_mfma_f32_16x16x32_bf16 v[44:47], a[0:3], v[80:83], 0\n"
      "v_mul_f32_dpp v221, v130, v221 row_newbcast:4 row_mask:0xf bank_mask:0xf\n"
      "v_mul_f32_dpp v224, v130, v224 row_newbcast:0 row_mask:0xf bank_mask:0xf\n"
      "v_mfma_f32_16x16x32_bf16 v[44:47], a[4:7], v[108:111], v[44:47]\n"
      "buffer_load_dwordx4 v1, s[40:43], s76 offen offset:3072 lds\n"
      "v_mfma_f32_16x16x32_bf16 v[44:47], a[8:11], v[180:183], v[44:47]\n"
      "L_h3_%=:\n"
      "s_cbranch_execz L_hr_%=\n"
      "v_mul_f32_dpp v215, v130, v215 row_newbcast:12 row_mask:0xf bank_mask:0xf\n"
      "v_cvt_pk_bf16_f32 v141, v66, v67\n"
      "v_mfma_f32_16x16x32_bf16 v[44:47], a[12:15], v[116:119], v[44:47]\n"
      "v_mfma_f32_16x16x32_bf16 v[154:157], a[48:51], v[80:83], 0\n"
      "v_pk_max_i16 v40, v40, 0\n"
      "v_cvt_pk_bf16_f32 v242, v68, v69\n"
      "v_mfma_f32_16x16x32_bf16 v[154:157], a[52:55], v[108:111], v[154:157]\n"
      "v_cvt_pk_bf16_f32 v138, v232, v233\n"
      "v_mfma_f32_16x16x32_bf16 v[154:157], a[56:59], v[180:183], v[154:157]\n"
      "v_cvt_pk_bf16_f32 v243, v70, v71\n"
      "v_mfma_f32_16x16x32_bf16 v[154:157], a[60:63], v[116:119], v[154:157]\n"
      "buffer_store_dwordx2 v[34:35], v2, s[44:47], s100 offen offset:768 nt sc1\n"
      "v_mul_f32_dpp v226, v130, v226 row_newbcast:8 row_mask:0xf bank_mask:0xf\n"
      "v_mfma_f32_16x16x32_bf16 v[112:115], a[0:3], v[124:127], 0\n"
      "v_mul_f32_dpp v213, v130, v213 row_newbcast:4 row_mask:0xf bank_mask:0xf\n"
      "v_mul_f32_dpp v223, v130, v223 row_newbcast:12 row_mask:0xf bank_mask:0xf\n"
      "v_mfma_f32_16x16x32_bf16 v[112:115], a[4:7], v[100:103], v[112:115]\n"
      "v_cvt_pk_bf16_f32 v66, v200, v201\n"
      "v_cvt_pk_bf16_f32 v235, v30, v31\n"
      "v_mfma_f32_16x16x32_bf16 v[112:115], a[8:11], v[72:75], v[112:115]\n"
      "v_mfma_f32_16x16x32_bf16 v[112:115], a[12:15], v[196:199], v[112:115]\n"
      "v_cvt_pk_bf16_f32 v67, v202, v203\n"
      "v_cvt_pk_bf16_f32 v68, v120, v121\n"
      "v_mfma_f32_16x16x32_bf16 v[252:255], v[80:83], a[96:99], 0\n"
      "v_cvt_pk_bf16_f32 v211, v156, v157\n"
      "v_mfma_f32_16x16x32_bf16 v[252:255], v[108:111], a[100:103], v[252:255]\n"
      "v_mul_f32_dpp v227, v130, v227 row_newbcast:12 row_mask:0xf bank_mask:0xf\n"
      "v_cvt_pk_bf16_f32 v140, v64, v65\n"
      "v_mfma_f32_16x16x32_bf16 v[252:255], v[180:183], a[104:107], v[252:255]\n"
      "v_cvt_pk_bf16_f32 v137, v250, v251\n"
      "v_cvt_pk_bf16_f32 v205, v46, v47\n"
      "v_mfma_f32_16x16x32_bf16 v[252:255], v[116:119], a[108:111], v[252:255]\n"
      "v_cvt_pk_bf16_f32 v64, v112, v113\n"
      "v_mfma_f32_16x16x32_bf16 v[248:251], a[48:51], v[124:127], 0\n"
      "ds_read_b128 v[192:195], v0 offset:12288\n"
      "v_cvt_pk_bf16_f32 v69, v122, v123\n"
      "v_mfma_f32_16x16x32_bf16 v[248:251], a[52:55], v[100:103], v[248:251]\n"
      "v_pk_max_i16 v42, v42, 0\n"
      "v_mfma_f32_16x16x32_bf16 v[248:251], a[56:59], v[72:75], v[248:251]\n"
      "v_mfma_f32_16x16x32_bf16 v[248:251], a[60:63], v[196:199], v[248:251]\n"
      "v_cvt_pk_bf16_f32 v234, v28, v29\n"
      "v_mul_f32_dpp v222, v130, v222 row_newbcast:8 row_mask:0xf bank_mask:0xf\n"
      "v_mfma_f32_16x16x32_bf16 v[32:35], v[136:139], v[240:243], 0\n"
      "v_mfma_f32_16x16x32_bf16 v[32:35], v[140:143], v[244:247], v[32:35]\n"
      "v_cvt_pk_bf16_f32 v210, v154, v155\n"
      "v_cvt_pk_bf16_f32 v206, v148, v149\n"
      "v_mfma_f32_16x16x32_bf16 v[216:219], v[144:147], a[208:211], v[216:219]\n"
      "v_cvt_pk_bf16_f32 v29, v58, v59\n"
      "v_mfma_f32_16x16x32_bf16 v[216:219], v[104:107], a[212:215], v[216:219]\n"
      "v_mfma_f32_16x16x32_bf16 v[216:219], v[172:175], a[216:219], v[216:219]\n"
      "v_cvt_pk_bf16_f32 v65, v114, v115\n"
      "v_mfma_f32_16x16x32_bf16 v[216:219], v[132:135], a[220:223], v[216:219]\n"
      "v_exp_f32 v33, v33\n"
      "v_mfma_f32_16x16x32_bf16 v[212:215], v[144:147], a[192:195], v[212:215]\n"
      "v_mov_b64 v[120:121], 0\n"
      "v_mfma_f32_16x16x32_bf16 v[212:215], v[104:107], a[196:199], v[212:215]\n"
      "v_cvt_pk_bf16_f32 v70, v248, v249\n"
      "v_cvt_pk_bf16_f32 v209, v50, v51\n"
      "v_mfma_f32_16x16x32_bf16 v[212:215], v[172:175], a[200:203], v[212:215]\n"
      "v_exp_f32 v34, v34\n"
      "v_mfma_f32_16x16x32_bf16 v[212:215], v[132:135], a[204:207], v[212:215]\n"
      "buffer_load_dwordx4 v1, s[40:43], s75 offen offset:0 lds\n"
      "v_mfma_f32_16x16x32_bf16 v[220:223], v[144:147], a[224:227], v[220:223]\n"
      "v_mfma_f32_16x16x32_bf16 v[220:223], v[104:107], a[228:231], v[220:223]\n"
      "ds_read_b128 v[48:51], v0 offset:13824\n"
      "v_cvt_pk_bf16_f32 v28, v56, v57\n"
      "v_mfma_f32_16x16x32_bf16 v[220:223], v[172:175], a[232:235], v[220:223]\n"
      "v_cvt_pk_bf16_f32 v204, v44, v45\n"
      "v_mov_b64 v[248:249], 0\n"
      "v_mfma_f32_16x16x32_bf16 v[220:223], v[132:135], a[236:239], v[220:223]\n"
      "v_mfma_f32_16x16x32_bf16 v[224:227], v[144:147], a[240:243], v[224:227]\n"
      "s_waitcnt vmcnt(13)\n"
      "ds_read_b128 v[44:47], v0 offset:0\n"
      "ds_read_b128 v[148:151], v0 offset:1024\n"
      "v_mfma_f32_16x16x32_bf16 v[224:227], v[104:107], a[244:247], v[224:227]\n"
      "ds_read_b128 v[200:203], v0 offset:12800\n"
      "v_cvt_pk_bf16_f32 v214, v214, v218\n"
      "v_mfma_f32_16x16x32_bf16 v[224:227], v[172:175], a[248:251], v[224:227]\n"
      "v_cvt_pk_bf16_f32 v113, v78, v79\n"
      "v_cvt_pk_bf16_f32 v212, v212, v216\n"
      "v_mfma_f32_16x16x32_bf16 v[224:227], v[132:135], a[252:255], v[224:227]\n"
      "v_cvt_pk_bf16_f32 v71, v250, v251\n"
      "s_waitcnt lgkmcnt(5)\n"
      "v_smfmac_f32_16x16x64_bf16 v[230:233], v[52:55], v[164:171], v5\n"
      "ds_read_b128 v[164:167], v0 offset:1536\n"
      "v_cvt_pk_bf16_f32 v218, v215, v219\n"
      "v_smfmac_f32_16x16x64_bf16 v[248:251], v[52:55], v[20:27], v5\n"
      "buffer_store_dwordx2 v[188:189], v2, s[44:47], s101 offen offset:0 nt sc1\n"
      "v_smfmac_f32_16x16x64_bf16 v[120:123], v[52:55], v[204:211], v5\n"
      "ds_read_b128 v[104:107], v0 offset:13312\n"
      "v_cvt_pk_bf16_f32 v59, v254, v255\n"
      "v_mfma_f32_16x16x32_bf16 v[132:135], v[80:83], a[64:67], 0\n"
      "v_mfma_f32_16x16x32_bf16 v[132:135], v[108:111], a[68:71], v[132:135]\n"
      "ds_read_b128 v[168:171], v0 offset:512\n"
      "v_add_f32 v230, v230, v231\n"
      "v_mfma_f32_16x16x32_bf16 v[132:135], v[180:183], a[72:75], v[132:135]\n"
      "v_mfma_f32_16x16x32_bf16 v[132:135], v[116:119], a[76:79], v[132:135]\n"
      "v_cvt_pk_bf16_f32 v216, v213, v217\n"
      "v_cvt_pk_bf16_f32 v215, v222, v226\n"
      "v_mfma_f32_16x16x32_bf16 v[154:157], v[80:83], a[80:83], 0\n"
      "v_pk_max_i16 v39, v39, 0\n"
      "v_mov_b64 v[144:145], 0\n"
      "v_mfma_f32_16x16x32_bf16 v[154:157], v[108:111], a[84:87], v[154:157]\n"
      "v_cvt_pk_bf16_f32 v217, v221, v225\n"
      "v_mfma_f32_16x16x32_bf16 v[154:157], v[180:183], a[88:91], v[154:157]\n"
      "v_cvt_pk_bf16_f32 v111, v134, v135\n"
      "v_cvt_pk_bf16_f32 v219, v223, v227\n"
      "v_mfma_f32_16x16x32_bf16 v[154:157], v[116:119], a[92:95], v[154:157]\n"
      "v_pk_max_i16 v37, v37, 0\n"
      "v_cvt_pk_bf16_f32 v112, v76, v77\n"
      "v_mfma_f32_16x16x32_bf16 v[80:83], v[64:67], v[240:243], 0\n"
      "v_maximum3_f32 v8, v8, v128, v129\n"
      "v_pk_max_i16 v43, v43, 0\n"
      "v_mfma_f32_16x16x32_bf16 v[80:83], v[68:71], v[244:247], v[80:83]\n"
      "v_pk_max_i16 v219, v219, 0\n"
      "s_waitcnt lgkmcnt(5)\n"
      "v_mfma_f32_16x16x32_bf16 v[172:175], v[44:47], a[80:83], 0\n"
      "s_waitcnt lgkmcnt(0)\n"
      "v_mfma_f32_16x16x32_bf16 v[172:175], v[168:171], a[84:87], v[172:175]\n"
      "v_cvt_pk_bf16_f32 v213, v220, v224\n"
      "v_mfma_f32_16x16x32_bf16 v[172:175], v[148:151], a[88:91], v[172:175]\n"
      "v_mfma_f32_16x16x32_bf16 v[172:175], v[164:167], a[92:95], v[172:175]\n"
      "buffer_store_dwordx2 v[42:43], v2, s[44:47], s34 offen offset:768 nt sc1\n"
      "v_add_f32 v248, v248, v249\n"
      "v_mfma_f32_16x16x32_bf16 v[76:79], v[44:47], a[112:115], 0\n"
      "v_exp_f32 v80, v80\n"
      "v_mfma_f32_16x16x32_bf16 v[76:79], v[168:171], a[116:119], v[76:79]\n"
      "v_cvt_pk_bf16_f32 v182, v154, v155\n"
      "v_pk_max_i16 v212, v212, 0\n"
      "v_mfma_f32_16x16x32_bf16 v[76:79], v[148:151], a[120:123], v[76:79]\n"
      "v_exp_f32 v82, v82\n"
      "v_mfma_f32_16x16x32_bf16 v[76:79], v[164:167], a[124:127], v[76:79]\n"
      "v_cvt_pk_bf16_f32 v180, v172, v173\n"
      "v_mov_b64 v[146:147], 0\n"
      "v_mfma_f32_16x16x32_bf16 v[224:227], v[44:47], a[96:99], 0\n"
      "v_mfma_f32_16x16x32_bf16 v[224:227], v[168:171], a[100:103], v[224:227]\n"
      "buffer_load_dwordx4 v1, s[40:43], s75 offen offset:2048 lds\n"
      "v_mfma_f32_16x16x32_bf16 v[224:227], v[148:151], a[104:107], v[224:227]\n"
      "v_cvt_pk_bf16_f32 v110, v132, v133\n"
      "v_mfma_f32_16x16x32_bf16 v[224:227], v[164:167], a[108:111], v[224:227]\n"
      "v_mov_b64 v[116:117], 0\n"
      "v_cvt_pk_bf16_f32 v232, v76, v77\n"
      "v_smfmac_f32_16x16x64_bf16 v[152:155], v[52:55], v[64:71], v5\n"
      "v_cvt_pk_bf16_f32 v233, v78, v79\n"
      "v_smfmac_f32_16x16x64_bf16 v[116:119], v[52:55], v[136:143], v5\n"
      "v_mov_b32 v231, 0\n"
      "v_mfma_f32_16x16x32_bf16 v[132:135], v[44:47], a[64:67], 0\n"
      "v_exp_f32 v248, v248\n"
      "v_mfma_f32_16x16x32_bf16 v[132:135], v[168:171], a[68:71], v[132:135]\n"
      "v_exp_f32 v81, v81\n"
      "v_mfma_f32_16x16x32_bf16 v[132:135], v[148:151], a[72:75], v[132:135]\n"
      "v_exp_f32_dpp v231, v230 quad_perm:[0,1,2,3] row_mask:0xf bank_mask:0x1\n"
      "v_mfma_f32_16x16x32_bf16 v[132:135], v[164:167], a[76:79], v[132:135]\n"
      "v_exp_f32 v32, v32\n"
      "v_mfma_f32_16x16x32_bf16 v[76:79], a[32:35], v[44:47], 0\n"
      "v_cvt_pk_bf16_f32 v58, v252, v253\n"
      "v_mfma_f32_16x16x32_bf16 v[76:79], a[36:39], v[168:171], v[76:79]\n"
      "v_cvt_pk_bf16_f32 v183, v156, v157\n"
      "v_mfma_f32_16x16x32_bf16 v[76:79], a[40:43], v[148:151], v[76:79]\n"
      "v_mfma_f32_16x16x32_bf16 v[76:79], a[44:47], v[164:167], v[76:79]\n"
      "buffer_store_dwordx2 v[40:41], v2, s[44:47], s34 offen offset:256 nt sc1\n"
      "v_mov_b32_dpp v230, v231 quad_perm:[1,1,3,3] row_mask:0xf bank_mask:0xf\n"
      "v_mfma_f32_16x16x32_bf16 v[252:255], a[16:19], v[44:47], 0\n"
      "v_cvt_pk_bf16_f32 v128, v80, v81\n"
      "v_add_f32 v152, v152, v153\n"
      "v_mfma_f32_16x16x32_bf16 v[252:255], a[20:23], v[168:171], v[252:255]\n"
      "v_add_f32 v116, v116, v117\n"
      "v_add_f32 v120, v120, v121\n"
      "v_mfma_f32_16x16x32_bf16 v[252:255], a[24:27], v[148:151], v[252:255]\n"
      "v_cvt_pk_bf16_f32 v109, v134, v135\n"
      "v_pk_max_i16 v217, v217, 0\n"
      "v_mfma_f32_16x16x32_bf16 v[252:255], a[28:31], v[164:167], v[252:255]\n"
      "v_exp_f32 v35, v35\n"
      "v_mfma_f32_16x16x32_bf16 v[220:223], v[204:207], v[240:243], 0\n"
      "v_cvt_pk_bf16_f32 v57, v226, v227\n"
      "v_cvt_pk_bf16_f32 v230, v231, v230\n"
      "v_mfma_f32_16x16x32_bf16 v[220:223], v[208:211], v[244:247], v[220:223]\n"
      "v_exp_f32 v83, v83\n"
      "v_mfma_f32_16x16x32_bf16 v[154:157], a[48:51], v[44:47], 0\n"
      "v_exp_f32 v116, v116\n"
      "v_mfma_f32_16x16x32_bf16 v[154:157], a[52:55], v[168:171], v[154:157]\n"
      "v_cvt_pk_bf16_f32 v181, v174, v175\n"
      "v_pk_max_i16 v218, v218, 0\n"
      "v_mfma_f32_16x16x32_bf16 v[154:157], a[56:59], v[148:151], v[154:157]\n"
      "v_cvt_pk_bf16_f32 v108, v132, v133\n"
      "v_cvt_pk_bf16_f32 v130, v32, v33\n"
      "v_mfma_f32_16x16x32_bf16 v[154:157], a[60:63], v[164:167], v[154:157]\n"
      "v_exp_f32 v222, v222\n"
      "v_mfma_f32_16x16x32_bf16 v[132:135], a[0:3], v[44:47], 0\n"
      "v_mov_b64 v[172:173], 0\n"
      "v_pk_max_i16 v214, v214, 0\n"
      "v_mfma_f32_16x16x32_bf16 v[132:135], a[4:7], v[168:171], v[132:135]\n"
      "v_cvt_pk_bf16_f32 v56, v224, v225\n"
      "v_mov_b64 v[224:225], 0\n"
      "v_mfma_f32_16x16x32_bf16 v[132:135], a[8:11], v[148:151], v[132:135]\n"
      "v_exp_f32 v223, v223\n"
      "v_mfma_f32_16x16x32_bf16 v[132:135], a[12:15], v[164:167], v[132:135]\n"
      "v_exp_f32 v152, v152\n"
      "v_mfma_f32_16x16x32_bf16 v[40:43], a[48:51], v[192:195], 0\n"
      "v_cvt_pk_bf16_f32 v169, v78, v79\n"
      "v_cndmask_b32_dpp v153, v116, v152, vcc quad_perm:[0,0,2,2] row_mask:0xf bank_mask:0xf\n"
      "v_mfma_f32_16x16x32_bf16 v[40:43], a[52:55], v[200:203], v[40:43]\n"
      "v_cvt_pk_bf16_f32 v171, v156, v157\n"
      "v_cvt_pk_bf16_f32 v166, v252, v253\n"
      "v_mfma_f32_16x16x32_bf16 v[40:43], a[56:59], v[104:107], v[40:43]\n"
      "v_cvt_pk_bf16_f32 v131, v34, v35\n"
      "v_cvt_pk_bf16_f32 v170, v154, v155\n"
      "v_mfma_f32_16x16x32_bf16 v[40:43], a[60:63], v[48:51], v[40:43]\n"
      "v_cvt_pk_bf16_f32 v129, v82, v83\n"
      "v_cvt_pk_bf16_f32 v165, v134, v135\n"
      "v_mfma_f32_16x16x32_bf16 v[148:151], a[0:3], v[192:195], 0\n"
      "v_mfma_f32_16x16x32_bf16 v[148:151], a[4:7], v[200:203], v[148:151]\n"
      "v_mov_b32_dpp v44, v160 row_ror:8 row_mask:0xf bank_mask:0xf\n"
      "v_mfma_f32_16x16x32_bf16 v[148:151], a[8:11], v[104:107], v[148:151]\n"
      "v_exp_f32 v221, v221\n"
      "v_mfma_f32_16x16x32_bf16 v[148:151], a[12:15], v[48:51], v[148:151]\n"
      "buffer_store_dwordx2 v[36:37], v2, s[44:47], s34 offen offset:0 nt sc1\n"
      "v_pk_max_i16 v213, v213, 0\n"
      "v_mfma_f32_16x16x32_bf16 v[32:35], a[16:19], v[192:195], 0\n"
      "v_cvt_pk_bf16_f32 v82, v40, v41\n"
      "v_cvt_pk_bf16_f32 v167, v254, v255\n"
      "v_mfma_f32_16x16x32_bf16 v[32:35], a[20:23], v[200:203], v[32:35]\n"
      "v_cvt_pk_bf16_f32 v168, v76, v77\n"
      "v_cvt_pk_bf16_f32 v164, v132, v133\n"
      "v_mfma_f32_16x16x32_bf16 v[32:35], a[24:27], v[104:107], v[32:35]\n"
      "buffer_load_dwordx4 v1, s[40:43], s76 offen offset:1024 lds\n"
      "v_mfma_f32_16x16x32_bf16 v[32:35], a[28:31], v[48:51], v[32:35]\n"
      "v_cvt_pk_bf16_f32 v76, v148, v149\n"
      "v_pk_max_i16 v216, v216, 0\n"
      "v_mfma_f32_16x16x32_bf16 v[132:135], a[32:35], v[192:195], 0\n"
      "v_exp_f32 v120, v120\n"
      "v_mfma_f32_16x16x32_bf16 v[132:135], a[36:39], v[200:203], v[132:135]\n"
      "v_mov_b32_dpp v45, v161 row_ror:8 row_mask:0xf bank_mask:0xf\n"
      "v_cvt_pk_bf16_f32 v251, v222, v223\n"
      "v_mfma_f32_16x16x32_bf16 v[132:135], a[40:43], v[104:107], v[132:135]\n"
      "v_exp_f32 v220, v220\n"
      "v_mfma_f32_16x16x32_bf16 v[132:135], a[44:47], v[48:51], v[132:135]\n"
      "v_cvt_pk_bf16_f32 v77, v150, v151\n"
      "s_add_u32 m0, s53, 12288\n"
      "v_smfmac_f32_16x16x64_bf16 v[224:227], v[52:55], v[164:171], v5\n"
      "v_cvt_pk_bf16_f32 v78, v32, v33\n"
      "v_mfma_f32_16x16x32_bf16 v[252:255], v[164:167], v[240:243], 0\n"
      "v_cvt_pk_bf16_f32 v83, v42, v43\n"
      "v_pk_max_i16 v215, v215, 0\n"
      "v_mfma_f32_16x16x32_bf16 v[252:255], v[168:171], v[244:247], v[252:255]\n"
      "v_cvt_pk_bf16_f32 v79, v34, v35\n"
      "v_cvt_pk_bf16_f32 v81, v134, v135\n"
      "v_mfma_f32_16x16x32_bf16 v[40:43], v[20:23], v[240:243], 0\n"
      "v_add_f32 v224, v224, v225\n"
      "v_cvt_pk_bf16_f32 v80, v132, v133\n"
      "v_mfma_f32_16x16x32_bf16 v[40:43], v[24:27], v[244:247], v[40:43]\n"
      "v_exp_f32 v224, v224\n"
      "v_smfmac_f32_16x16x64_bf16 v[172:175], v[52:55], v[76:83], v5\n"
      "buffer_load_dwordx4 v1, s[40:43], s77 offen offset:0 lds\n"
      "v_mfma_f32_16x16x32_bf16 v[52:55], a[176:179], v[124:127], 0\n"
      "v_exp_f32 v253, v253\n"
      "v_mfma_f32_16x16x32_bf16 v[52:55], a[180:183], v[100:103], v[52:55]\n"
      "v_cvt_pk_bf16_f32 v250, v220, v221\n"
      "v_mov_b32_dpp v47, v163 row_ror:8 row_mask:0xf bank_mask:0xf\n"
      "v_mfma_f32_16x16x32_bf16 v[52:55], a[184:187], v[72:75], v[52:55]\n"
      "v_exp_f32 v252, v252\n"
      "v_mfma_f32_16x16x32_bf16 v[52:55], a[188:191], v[196:199], v[52:55]\n"
      "v_exp_f32 v41, v41\n"
      "v_mfma_f32_16x16x32_bf16 v[220:223], a[160:163], v[124:127], 0\n"
      "v_exp_f32 v254, v254\n"
      "v_mfma_f32_16x16x32_bf16 v[220:223], a[164:167], v[100:103], v[220:223]\n"
      "v_add_f32 v172, v172, v173\n"
      "v_cndmask_b32_dpp v225, v248, v224, vcc quad_perm:[0,0,2,2] row_mask:0xf bank_mask:0xf\n"
      "v_mfma_f32_16x16x32_bf16 v[220:223], a[168:171], v[72:75], v[220:223]\n"
      "v_exp_f32 v43, v43\n"
      "v_mfma_f32_16x16x32_bf16 v[220:223], a[172:175], v[196:199], v[220:223]\n"
      "v_cvt_pk_bf16_f32 v35, v54, v55\n"
      "v_mfma_f32_16x16x32_bf16 v[132:135], a[144:147], v[124:127], 0\n"
      "v_exp_f32 v172, v172\n"
      "v_mfma_f32_16x16x32_bf16 v[132:135], a[148:151], v[100:103], v[132:135]\n"
      "v_cvt_pk_bf16_f32 v34, v52, v53\n"
      "v_cndmask_b32_dpp v121, v172, v120, vcc quad_perm:[0,0,2,2] row_mask:0xf bank_mask:0xf\n"
      "v_mfma_f32_16x16x32_bf16 v[132:135], a[152:155], v[72:75], v[132:135]\n"
      "s_not_b64 vcc, vcc\n"
      "v_cndmask_b32_dpp v249, v224, v248, vcc quad_perm:[1,1,3,3] row_mask:0xf bank_mask:0xf\n"
      "v_add_f32 v224, v224, v120\n"
      "v_mfma_f32_16x16x32_bf16 v[132:135], a[156:159], v[196:199], v[132:135]\n"
      "v_add_f32 v248, v248, v172\n"
      "v_cndmask_b32_dpp v117, v152, v116, vcc quad_perm:[1,1,3,3] row_mask:0xf bank_mask:0xf\n"
      "v_mfma_f32_16x16x32_bf16 v[52:55], v[76:79], v[240:243], 0\n"
      "v_cvt_pk_bf16_f32 v153, v153, v117\n"
      "v_cvt_pk_bf16_f32 v32, v220, v221\n"
      "v_mfma_f32_16x16x32_bf16 v[52:55], v[80:83], v[244:247], v[52:55]\n"
      "buffer_store_dwordx2 v[38:39], v2, s[44:47], s34 offen offset:512 nt sc1\n"
      "v_add_f32 v152, v152, v116\n"
      "v_mfma_f32_16x16x32_bf16 v[116:119], v[192:195], a[96:99], 0\n"
      "v_add_f32 v224, v224, v248\n"
      "v_cvt_pk_bf16_f32 v225, v225, v249\n"
      "v_mfma_f32_16x16x32_bf16 v[116:119], v[200:203], a[100:103], v[116:119]\n"
      "v_add_f32 v152, v152, v231\n"
      "v_cvt_pk_bf16_f32 v30, v132, v133\n"
      "v_mfma_f32_16x16x32_bf16 v[116:119], v[104:107], a[104:107], v[116:119]\n"
      "v_cvt_pk_bf16_f32 v33, v222, v223\n"
      "v_cvt_pk_bf16_f32 v31, v134, v135\n"
      "v_mfma_f32_16x16x32_bf16 v[116:119], v[48:51], a[108:111], v[116:119]\n"
      "ds_bpermute_b32 v174, v6, v225 offset:16\n"
      "v_mfma_f32_16x16x32_bf16 v[220:223], v[20:23], v[28:31], 0\n"
      "v_exp_f32 v54, v54\n"
      "v_mfma_f32_16x16x32_bf16 v[220:223], v[24:27], v[32:35], v[220:223]\n"
      "v_cvt_pk_bf16_f32 v248, v252, v253\n"
      "v_cndmask_b32_dpp v173, v120, v172, vcc quad_perm:[1,1,3,3] row_mask:0xf bank_mask:0xf\n"
      "v_mfma_f32_16x16x32_bf16 v[36:39], v[76:79], v[28:31], 0\n"
      "v_cvt_pk_bf16_f32 v121, v121, v173\n"
      "v_add_f32 v18, v224, v152\n"
      "v_mfma_f32_16x16x32_bf16 v[36:39], v[80:83], v[32:35], v[36:39]\n"
      "ds_bpermute_b32 v173, v6, v121\n"
      "v_mfma_f32_16x16x32_bf16 v[132:135], v[192:195], a[80:83], 0\n"
      "v_exp_f32 v40, v40\n"
      "v_mfma_f32_16x16x32_bf16 v[132:135], v[200:203], a[84:87], v[132:135]\n"
      "v_exp_f32 v52, v52\n"
      "v_mfma_f32_16x16x32_bf16 v[132:135], v[104:107], a[88:91], v[132:135]\n"
      "v_add_f32_dpp v18, v18, v18 row_ror:8 row_mask:0xf bank_mask:0xf\n"
      "v_mov_b32_dpp v46, v162 row_ror:8 row_mask:0xf bank_mask:0xf\n"
      "v_mfma_f32_16x16x32_bf16 v[132:135], v[48:51], a[92:95], v[132:135]\n"
      "v_exp_f32 v42, v42\n"
      "v_smfmac_f32_16x16x64_bf16 v[144:147], v[160:163], v[240:247], v5\n"
      "ds_bpermute_b32 v158, v7, v230 offset:248\n"
      "v_smfmac_f32_16x16x64_bf16 v[144:147], v[44:47], v[28:35], v5\n"
      "ds_bpermute_b32 v175, v6, v121 offset:16\n"
      "v_mfma_f32_16x16x32_bf16 v[120:123], v[204:207], v[28:31], 0\n"
      "v_exp_f32 v39, v39\n"
      "v_mfma_f32_16x16x32_bf16 v[120:123], v[208:211], v[32:35], v[120:123]\n"
      "v_exp_f32 v223, v223\n"
      "v_mfma_f32_16x16x32_bf16 v[148:151], v[164:167], v[28:31], 0\n"
      "v_exp_f32 v255, v255\n"
      "v_mfma_f32_16x16x32_bf16 v[148:151], v[168:171], v[32:35], v[148:151]\n"
      "v_cvt_pk_bf16_f32 v187, v134, v135\n"
      "v_cvt_pk_bf16_f32 v186, v132, v133\n"
      "v_mfma_f32_16x16x32_bf16 v[244:247], v[64:67], v[28:31], 0\n"
      "buffer_load_dwordx4 v1, s[40:43], s78 offen offset:1024 lds\n"
      "v_mfma_f32_16x16x32_bf16 v[244:247], v[68:71], v[32:35], v[244:247]\n"
      "buffer_store_dwordx2 v[218:219], v2, s[44:47], s35 offen offset:768 nt sc1\n"
      "v_cvt_pk_bf16_f32 v249, v254, v255\n"
      "v_mfma_f32_16x16x32_bf16 v[132:135], v[92:95], a[64:67], 0\n"
      "v_exp_f32 v151, v151\n"
      "v_mfma_f32_16x16x32_bf16 v[132:135], v[88:91], a[68:71], v[132:135]\n"
      "v_exp_f32 v149, v149\n"
      "v_mfma_f32_16x16x32_bf16 v[132:135], v[96:99], a[72:75], v[132:135]\n"
      "v_exp_f32 v122, v122\n"
      "v_mfma_f32_16x16x32_bf16 v[132:135], v[176:179], a[76:79], v[132:135]\n"
      "v_exp_f32 v53, v53\n"
      "v_mfma_f32_16x16x32_bf16 v[240:243], v[136:139], v[28:31], 0\n"
      "v_exp_f32 v245, v245\n"
      "v_mfma_f32_16x16x32_bf16 v[240:243], v[140:143], v[32:35], v[240:243]\n"
      "v_cvt_pk_bf16_f32 v253, v42, v43\n"
      "v_cvt_pk_bf16_f32 v62, v116, v117\n"
      "v_mfma_f32_16x16x32_bf16 v[32:35], v[192:195], a[112:115], 0\n"
      "v_exp_f32 v55, v55\n"
      "v_mfma_f32_16x16x32_bf16 v[32:35], v[200:203], a[116:119], v[32:35]\n"
      "ds_bpermute_b32 v159, v7, v230 offset:8\n"
      "v_mfma_f32_16x16x32_bf16 v[32:35], v[104:107], a[120:123], v[32:35]\n"
      "v_cvt_pk_bf16_f32 v254, v52, v53\n"
      "v_cvt_pk_bf16_f32 v63, v118, v119\n"
      "v_mfma_f32_16x16x32_bf16 v[32:35], v[48:51], a[124:127], v[32:35]\n"
      "v_exp_f32 v120, v120\n"
      "v_mfma_f32_16x16x32_bf16 v[28:31], v[192:195], a[64:67], 0\n"
      "v_exp_f32 v222, v222\n"
      "v_mfma_f32_16x16x32_bf16 v[28:31], v[200:203], a[68:71], v[28:31]\n"
      "v_exp_f32 v150, v150\n"
      "v_mfma_f32_16x16x32_bf16 v[28:31], v[104:107], a[72:75], v[28:31]\n"
      "v_exp_f32 v246, v246\n"
      "v_mfma_f32_16x16x32_bf16 v[28:31], v[48:51], a[76:79], v[28:31]\n"
      "ds_bpermute_b32 v172, v6, v225\n"
      "v_mfma_f32_16x16x32_bf16 v[116:119], v[124:127], a[64:67], 0\n"
      "v_exp_f32 v38, v38\n"
      "v_mfma_f32_16x16x32_bf16 v[116:119], v[100:103], a[68:71], v[116:119]\n"
      "v_exp_f32 v123, v123\n"
      "v_mfma_f32_16x16x32_bf16 v[116:119], v[72:75], a[72:75], v[116:119]\n"
      "v_exp_f32 v148, v148\n"
      "v_mfma_f32_16x16x32_bf16 v[116:119], v[196:199], a[76:79], v[116:119]\n"
      "v_exp_f32 v121, v121\n"
      "v_mfma_f32_16x16x32_bf16 v[224:227], v[124:127], a[112:115], 0\n"
      "v_cvt_pk_bf16_f32 v115, v30, v31\n"
      "v_cvt_pk_bf16_f32 v114, v28, v29\n"
      "v_mfma_f32_16x16x32_bf16 v[224:227], v[100:103], a[116:119], v[224:227]\n"
      "v_cvt_pk_bf16_f32 v29, v150, v151\n"
      "v_cvt_pk_bf16_f32 v255, v54, v55\n"
      "v_mfma_f32_16x16x32_bf16 v[224:227], v[72:75], a[120:123], v[224:227]\n"
      "v_exp_f32 v221, v221\n"
      "v_mfma_f32_16x16x32_bf16 v[224:227], v[196:199], a[124:127], v[224:227]\n"
      "buffer_store_dwordx2 v[216:217], v2, s[44:47], s35 offen offset:256 nt sc1\n"
      "v_add_f32 v146, v146, v147\n"
      "v_mfma_f32_16x16x32_bf16 v[154:157], v[124:127], a[80:83], 0\n"
      "v_cvt_pk_bf16_f32 v28, v148, v149\n"
      "v_cvt_pk_bf16_f32 v30, v120, v121\n"
      "v_mfma_f32_16x16x32_bf16 v[154:157], v[100:103], a[84:87], v[154:157]\n"
      "v_exp_f32 v241, v241\n"
      "v_mfma_f32_16x16x32_bf16 v[154:157], v[72:75], a[88:91], v[154:157]\n"
      "v_exp_f32 v242, v242\n"
      "v_mfma_f32_16x16x32_bf16 v[154:157], v[196:199], a[92:95], v[154:157]\n"
      "v_cvt_pk_bf16_f32 v229, v226, v227\n"
      "v_cvt_pk_bf16_f32 v252, v40, v41\n"
      "v_mfma_f32_16x16x32_bf16 v[40:43], v[92:95], a[112:115], 0\n"
      "v_exp_f32 v247, v247\n"
      "v_mfma_f32_16x16x32_bf16 v[40:43], v[88:91], a[116:119], v[40:43]\n"
      "v_exp_f32 v36, v36\n"
      "v_mfma_f32_16x16x32_bf16 v[40:43], v[96:99], a[120:123], v[40:43]\n"
      "v_exp_f32 v244, v244\n"
      "v_mfma_f32_16x16x32_bf16 v[40:43], v[176:179], a[124:127], v[40:43]\n"
      "v_exp_f32 v37, v37\n"
      "v_mfma_f32_16x16x32_bf16 v[148:151], v[124:127], a[96:99], 0\n"
      "v_cvt_pk_bf16_f32 v31, v122, v123\n"
      "v_cvt_pk_bf16_f32 v52, v116, v117\n"
      "v_mfma_f32_16x16x32_bf16 v[148:151], v[100:103], a[100:103], v[148:151]\n"
      "v_cvt_pk_bf16_f32 v239, v34, v35\n"
      "v_cvt_pk_bf16_f32 v228, v224, v225\n"
      "v_mfma_f32_16x16x32_bf16 v[148:151], v[72:75], a[104:107], v[148:151]\n"
      "v_exp_f32 v243, v243\n"
      "v_mfma_f32_16x16x32_bf16 v[148:151], v[196:199], a[108:111], v[148:151]\n"
      "v_cvt_pk_bf16_f32 v55, v134, v135\n"
      "v_cvt_pk_bf16_f32 v54, v132, v133\n"
      "v_mfma_f32_16x16x32_bf16 v[216:219], a[176:179], v[192:195], 0\n"
      "v_cvt_pk_bf16_f32 v53, v118, v119\n"
      "v_cvt_pk_bf16_f32 v230, v40, v41\n"
      "v_mfma_f32_16x16x32_bf16 v[216:219], a[180:183], v[200:203], v[216:219]\n"
      "v_cvt_pk_bf16_f32 v121, v156, v157\n"
      "v_cvt_pk_bf16_f32 v120, v154, v155\n"
      "v_mfma_f32_16x16x32_bf16 v[216:219], a[184:187], v[104:107], v[216:219]\n"
      "v_exp_f32 v220, v220\n"
      "v_mfma_f32_16x16x32_bf16 v[216:219], a[188:191], v[48:51], v[216:219]\n"
      "v_exp_f32 v240, v240\n"
      "v_mfma_f32_16x16x32_bf16 v[224:227], v[92:95], a[96:99], 0\n"
      "v_cvt_pk_bf16_f32 v134, v240, v241\n"
      "v_mfma_f32_16x16x32_bf16 v[224:227], v[88:91], a[100:103], v[224:227]\n"
      "v_cvt_pk_bf16_f32 v135, v242, v243\n"
      "v_cvt_pk_bf16_f32 v231, v42, v43\n"
      "v_mfma_f32_16x16x32_bf16 v[224:227], v[96:99], a[104:107], v[224:227]\n"
      "v_cvt_pk_bf16_f32 v132, v244, v245\n"
      "v_cvt_pk_bf16_f32 v42, v216, v217\n"
      "v_mfma_f32_16x16x32_bf16 v[224:227], v[176:179], a[108:111], v[224:227]\n"
      "buffer_store_dwordx2 v[212:213], v2, s[44:47], s35 offen offset:0 nt sc1\n"
      "v_cvt_pk_bf16_f32 v133, v246, v247\n"
      "v_mfma_f32_16x16x32_bf16 v[240:243], a[144:147], v[192:195], 0\n"
      "v_cvt_pk_bf16_f32 v238, v32, v33\n"
      "v_add_f32 v144, v144, v145\n"
      "v_mfma_f32_16x16x32_bf16 v[240:243], a[148:151], v[200:203], v[240:243]\n"
      "v_exp_f32 v144, v144\n"
      "v_mfma_f32_16x16x32_bf16 v[240:243], a[152:155], v[104:107], v[240:243]\n"
      "v_exp_f32 v146, v146\n"
      "v_mfma_f32_16x16x32_bf16 v[240:243], a[156:159], v[48:51], v[240:243]\n"
      "v_cvt_pk_bf16_f32 v35, v38, v39\n"
      "v_cvt_pk_bf16_f32 v34, v36, v37\n"
      "v_mfma_f32_16x16x32_bf16 v[244:247], v[92:95], a[80:83], 0\n"
      "v_cvt_pk_bf16_f32 v43, v218, v219\n"
      "v_cvt_pk_bf16_f32 v117, v150, v151\n"
      "v_mfma_f32_16x16x32_bf16 v[244:247], v[88:91], a[84:87], v[244:247]\n"
      "ds_read_b64 v[156:157], v11 offset:0\n"
      "v_cvt_pk_bf16_f32 v116, v148, v149\n"
      "v_mfma_f32_16x16x32_bf16 v[244:247], v[96:99], a[88:91], v[244:247]\n"
      "v_cvt_pk_bf16_f32 v149, v144, v146\n"
      "v_cvt_pk_bf16_f32 v119, v226, v227\n"
      "v_mfma_f32_16x16x32_bf16 v[244:247], v[176:179], a[92:95], v[244:247]\n"
      "v_cvt_pk_bf16_f32 v39, v242, v243\n"
      "v_cvt_pk_bf16_f32 v118, v224, v225\n"
      "v_mfma_f32_16x16x32_bf16 v[224:227], a[128:131], v[192:195], 0\n"
      "v_cvt_pk_bf16_f32 v148, v144, v146\n"
      "v_cvt_pk_bf16_f32 v38, v240, v241\n"
      "v_mfma_f32_16x16x32_bf16 v[224:227], a[132:135], v[200:203], v[224:227]\n"
      "ds_bpermute_b32 v145, v7, v153 offset:4\n"
      "v_mfma_f32_16x16x32_bf16 v[224:227], a[136:139], v[104:107], v[224:227]\n"
      "v_permlane16_swap_b32 v148, v149\n"
      "v_mfma_f32_16x16x32_bf16 v[224:227], a[140:143], v[48:51], v[224:227]\n"
      "v_cvt_pk_bf16_f32 v32, v220, v221\n"
      "v_cvt_pk_bf16_f32 v33, v222, v223\n"
      "v_mfma_f32_16x16x32_bf16 v[220:223], a[160:163], v[192:195], 0\n"
      "v_cvt_pk_bf16_f32 v122, v244, v245\n"
      "v_cvt_pk_bf16_f32 v123, v246, v247\n"
      "v_mfma_f32_16x16x32_bf16 v[220:223], a[164:167], v[200:203], v[220:223]\n"
      "v_add_f32 v245, v144, v146\n"
      "v_add_f32 v244, v144, v146\n"
      "v_mfma_f32_16x16x32_bf16 v[220:223], a[168:171], v[104:107], v[220:223]\n"
      "s_nop 0\n"
      "v_permlane16_swap_b32 v244, v245\n"
      "v_mfma_f32_16x16x32_bf16 v[220:223], a[172:175], v[48:51], v[220:223]\n"
      "v_cvt_pk_bf16_f32 v36, v224, v225\n"
      "v_mov_b64 v[188:189], v[244:245]\n"
      "v_smfmac_f32_16x16x64_bf16 v[244:247], v[12:15], v[248:255], v5\n"
      "buffer_store_dwordx2 v[214:215], v2, s[44:47], s35 offen offset:512 nt sc1\n"
      "v_cvt_pk_bf16_f32 v37, v226, v227\n"
      "v_smfmac_f32_16x16x64_bf16 v[188:191], v[12:15], v[28:35], v5\n"
      "s_nop 1\n"
      "v_cvt_pk_bf16_f32 v40, v220, v221\n"
      "v_smfmac_f32_16x16x32_bf16 v[244:247], v[12:13], v[128:131], v5\n"
      "ds_bpermute_b32 v144, v7, v153\n"
      "v_cvt_pk_bf16_f32 v41, v222, v223\n"
      "s_waitcnt lgkmcnt(2)\n"
      "v_mfma_f32_16x16x4_4b_bf16 v[212:227], v[148:149], v[156:157], 0 cbsz:2 abid:0\n"
      "s_nop 3\n"
      "v_add_f32 v244, v244, v245\n"
      "v_rcp_f32 v245, v244\n"
      "v_smfmac_f32_16x16x32_bf16 v[188:191], v[12:13], v[132:135], v5\n"
      "ds_bpermute_b32 v246, v4, v245\n"
      "v_add_u32 v16, 64, v16\n"
      "s_nop 0\n"
      "v_mfma_f32_16x16x32_bf16 v[216:219], v[248:251], v[180:183], v[216:219]\n"
      "v_add_f32_dpp v18, v18, v18 row_ror:4 row_mask:0xf bank_mask:0xf\n"
      "v_mfma_f32_16x16x32_bf16 v[216:219], v[252:255], v[184:187], v[216:219]\n"
      "buffer_load_dwordx4 v1, s[40:43], s78 offen offset:3072 lds\n"
      "v_mfma_f32_16x16x32_bf16 v[216:219], v[128:131], v[120:123], v[216:219]\n"
      "v_mfma_f32_16x16x32_bf16 v[212:215], v[248:251], v[108:111], v[212:215]\n"
      "v_mfma_f32_16x16x32_bf16 v[212:215], v[252:255], v[112:115], v[212:215]\n"
      "v_add_f32 v188, v188, v189\n"
      "v_add_f32_dpp v18, v18, v18 row_ror:2 row_mask:0xf bank_mask:0xf\n"
      "v_mfma_f32_16x16x32_bf16 v[212:215], v[128:131], v[52:55], v[212:215]\n"
      "v_rcp_f32 v189, v188\n"
      "v_mfma_f32_16x16x32_bf16 v[224:227], v[248:251], v[232:235], v[224:227]\n"
      "v_mfma_f32_16x16x32_bf16 v[224:227], v[252:255], v[236:239], v[224:227]\n"
      "ds_bpermute_b32 v190, v4, v189 offset:128\n"
      "v_mfma_f32_16x16x32_bf16 v[224:227], v[128:131], v[228:231], v[224:227]\n"
      "s_waitcnt lgkmcnt(1)\n"
      "v_mul_f32_dpp v217, v246, v217 row_newbcast:4 row_mask:0xf bank_mask:0xf\n"
      "v_add_f32_dpp v18, v18, v18 row_ror:1 row_mask:0xf bank_mask:0xf\n"
      "v_mfma_f32_16x16x32_bf16 v[220:223], v[248:251], v[56:59], v[220:223]\n"
      "v_mul_f32_dpp v219, v246, v219 row_newbcast:12 row_mask:0xf bank_mask:0xf\n"
      "v_mul_f32_dpp v212, v246, v212 row_newbcast:0 row_mask:0xf bank_mask:0xf\n"
      "v_mfma_f32_16x16x32_bf16 v[220:223], v[252:255], v[60:63], v[220:223]\n"
      "v_mul_f32_dpp v213, v246, v213 row_newbcast:4 row_mask:0xf bank_mask:0xf\n"
      "v_mul_f32_dpp v218, v246, v218 row_newbcast:8 row_mask:0xf bank_mask:0xf\n"
      "v_mfma_f32_16x16x32_bf16 v[220:223], v[128:131], v[116:119], v[220:223]\n"
      "v_mul_f32_dpp v216, v246, v216 row_newbcast:0 row_mask:0xf bank_mask:0xf\n"
      "v_mfma_f32_16x16x32_bf16 v[84:87], v[64:67], v[36:39], 0\n"
      "buffer_load_dwordx4 v1, s[40:43], s77 offen offset:2048 lds\n"
      "v_mfma_f32_16x16x32_bf16 v[84:87], v[68:71], v[40:43], v[84:87]\n"
      "v_mul_f32_dpp v226, v246, v226 row_newbcast:8 row_mask:0xf bank_mask:0xf\n"
      "v_mfma_f32_16x16x32_bf16 v[216:219], v[92:95], a[208:211], v[216:219]\n"
      "v_rcp_f32 v19, v18\n"
      "v_mfma_f32_16x16x32_bf16 v[216:219], v[88:91], a[212:215], v[216:219]\n"
      "v_mul_f32_dpp v214, v246, v214 row_newbcast:8 row_mask:0xf bank_mask:0xf\n"
      "v_mfma_f32_16x16x32_bf16 v[216:219], v[96:99], a[216:219], v[216:219]\n"
      "v_mul_f32_dpp v215, v246, v215 row_newbcast:12 row_mask:0xf bank_mask:0xf\n"
      "v_mul_f32_dpp v223, v246, v223 row_newbcast:12 row_mask:0xf bank_mask:0xf\n"
      "v_mfma_f32_16x16x32_bf16 v[216:219], v[176:179], a[220:223], v[216:219]\n"
      "v_maximum3_f32 v8, v8, v18, v19\n"
      "v_mul_f32_dpp v221, v246, v221 row_newbcast:4 row_mask:0xf bank_mask:0xf\n"
      "v_mfma_f32_16x16x32_bf16 v[212:215], v[92:95], a[192:195], v[212:215]\n"
      "v_mul_f32_dpp v220, v246, v220 row_newbcast:0 row_mask:0xf bank_mask:0xf\n"
      "v_mul_f32_dpp v222, v246, v222 row_newbcast:8 row_mask:0xf bank_mask:0xf\n"
      "v_mfma_f32_16x16x32_bf16 v[212:215], v[88:91], a[196:199], v[212:215]\n"
      "ds_read_b128 v[128:131], v0 offset:8192\n"
      "v_mfma_f32_16x16x32_bf16 v[212:215], v[96:99], a[200:203], v[212:215]\n"
      "v_add_u32 v9, 64, v9\n"
      "v_maximum3_f32 v8, v8, v244, v245\n"
      "v_mfma_f32_16x16x32_bf16 v[212:215], v[176:179], a[204:207], v[212:215]\n"
      "v_mfma_f32_16x16x32_bf16 v[220:223], v[92:95], a[224:227], v[220:223]\n"
      "v_exp_f32 v87, v87\n"
      "v_mfma_f32_16x16x32_bf16 v[220:223], v[88:91], a[228:231], v[220:223]\n"
      "v_mul_f32_dpp v225, v246, v225 row_newbcast:4 row_mask:0xf bank_mask:0xf\n"
      "v_mul_f32_dpp v224, v246, v224 row_newbcast:0 row_mask:0xf bank_mask:0xf\n"
      "v_mfma_f32_16x16x32_bf16 v[220:223], v[96:99], a[232:235], v[220:223]\n"
      "v_mul_f32_dpp v227, v246, v227 row_newbcast:12 row_mask:0xf bank_mask:0xf\n"
      "v_mfma_f32_16x16x32_bf16 v[220:223], v[176:179], a[236:239], v[220:223]\n"
      "ds_read_b128 v[152:155], v0 offset:8704\n"
      "v_mfma_f32_16x16x32_bf16 v[224:227], v[92:95], a[240:243], v[224:227]\n"
      "v_exp_f32 v84, v84\n"
      "v_mfma_f32_16x16x32_bf16 v[224:227], v[88:91], a[244:247], v[224:227]\n"
      "v_cvt_pk_bf16_f32 v212, v212, v216\n"
      "v_pk_max_i16 v212, v212, 0\n"
      "v_mfma_f32_16x16x32_bf16 v[224:227], v[96:99], a[248:251], v[224:227]\n"
      "v_exp_f32 v85, v85\n"
      "v_mfma_f32_16x16x32_bf16 v[224:227], v[176:179], a[252:255], v[224:227]\n"
      "L_h4_%=:\n"
      "s_cbranch_execz L_hr_%=\n"
      "v_cvt_pk_bf16_f32 v214, v214, v218\n"
      "v_pk_max_i16 v214, v214, 0\n"
      "v_mfma_f32_16x16x4_4b_bf16 v[240:255], v[148:149], v[156:157], 0 cbsz:2 abid:2\n"
      "ds_read_b128 v[148:151], v0 offset:9216\n"
      "v_cvt_pk_bf16_f32 v216, v213, v217\n"
      "v_cvt_pk_bf16_f32 v218, v215, v219\n"
      "s_nop 1\n"
      "v_cvt_pk_bf16_f32 v215, v222, v226\n"
      "v_pk_max_i16 v215, v215, 0\n"
      "v_mfma_f32_16x16x32_bf16 v[176:179], v[136:139], v[36:39], 0\n"
      "v_cvt_pk_bf16_f32 v213, v220, v224\n"
      "v_cvt_pk_bf16_f32 v217, v221, v225\n"
      "v_mfma_f32_16x16x32_bf16 v[176:179], v[140:143], v[40:43], v[176:179]\n"
      "buffer_store_dwordx2 v[214:215], v2, s[44:47], s73 offen offset:512 nt sc1\n"
      "v_cvt_pk_bf16_f32 v219, v223, v227\n"
      "v_mfma_f32_16x16x32_bf16 v[248:251], v[28:31], v[56:59], v[248:251]\n"
      "ds_read_b128 v[224:227], v0 offset:9728\n"
      "v_pk_max_i16 v217, v217, 0\n"
      "v_mfma_f32_16x16x32_bf16 v[248:251], v[32:35], v[60:63], v[248:251]\n"
      "v_exp_f32 v86, v86\n"
      "v_mfma_f32_16x16x32_bf16 v[248:251], v[132:135], v[116:119], v[248:251]\n"
      "v_exp_f32 v178, v178\n"
      "v_mfma_f32_16x16x32_bf16 v[240:243], v[28:31], v[108:111], v[240:243]\n"
      "v_exp_f32 v177, v177\n"
      "v_mfma_f32_16x16x32_bf16 v[240:243], v[32:35], v[112:115], v[240:243]\n"
      "v_cvt_pk_bf16_f32 v221, v86, v87\n"
      "v_cvt_pk_bf16_f32 v220, v84, v85\n"
      "v_mfma_f32_16x16x32_bf16 v[240:243], v[132:135], v[52:55], v[240:243]\n"
      "v_exp_f32 v179, v179\n"
      "v_mfma_f32_16x16x32_bf16 v[244:247], v[28:31], v[180:183], v[244:247]\n"
      "s_add_u32 m0, s53, 8192\n"
      "v_exp_f32 v176, v176\n"
      "v_mfma_f32_16x16x32_bf16 v[244:247], v[32:35], v[184:187], v[244:247]\n"
      "v_pk_max_i16 v213, v213, 0\n"
      "v_pk_max_i16 v219, v219, 0\n"
      "v_mfma_f32_16x16x32_bf16 v[244:247], v[132:135], v[120:123], v[244:247]\n"
      "s_waitcnt lgkmcnt(4)\n"
      "v_mul_f32_dpp v248, v190, v248 row_newbcast:0 row_mask:0xf bank_mask:0xf\n"
      "v_cvt_pk_bf16_f32 v222, v176, v177\n"
      "v_mfma_f32_16x16x32_bf16 v[252:255], v[28:31], v[232:235], v[252:255]\n"
      "v_mul_f32_dpp v250, v190, v250 row_newbcast:8 row_mask:0xf bank_mask:0xf\n"
      "v_mfma_f32_16x16x32_bf16 v[252:255], v[32:35], v[236:239], v[252:255]\n"
      "v_mul_f32_dpp v240, v190, v240 row_newbcast:0 row_mask:0xf bank_mask:0xf\n"
      "v_mul_f32_dpp v243, v190, v243 row_newbcast:12 row_mask:0xf bank_mask:0xf\n"
      "v_mfma_f32_16x16x32_bf16 v[252:255], v[132:135], v[228:231], v[252:255]\n"
      "s_waitcnt lgkmcnt(0)\n"
      "buffer_load_dwordx4 v1, s[40:43], s91 offen offset:3072 lds\n"
      "v_mfma_f32_16x16x4_4b_bf16 v[84:99], v[158:159], v[156:157], 0 cbsz:2 abid:0\n"
      "buffer_store_dwordx2 v[212:213], v2, s[44:47], s73 offen offset:0 nt sc1\n"
      "v_mul_f32_dpp v244, v190, v244 row_newbcast:0 row_mask:0xf bank_mask:0xf\n"
      "v_mul_f32_dpp v245, v190, v245 row_newbcast:4 row_mask:0xf bank_mask:0xf\n"
      "v_pk_max_i16 v218, v218, 0\n"
      "v_mfma_f32_16x16x32_bf16 v[28:31], v[164:167], v[36:39], 0\n"
      "v_mul_f32_dpp v253, v190, v253 row_newbcast:4 row_mask:0xf bank_mask:0xf\n"
      "v_mul_f32_dpp v252, v190, v252 row_newbcast:0 row_mask:0xf bank_mask:0xf\n"
      "v_mfma_f32_16x16x32_bf16 v[28:31], v[168:171], v[40:43], v[28:31]\n"
      "v_mul_f32_dpp v249, v190, v249 row_newbcast:4 row_mask:0xf bank_mask:0xf\n"
      "s_nop 0\n"
      "v_smfmac_f32_16x16x64_bf16 v[88:91], v[172:175], v[180:187], v5\n"
      "ds_read_b64 v[132:133], v11 offset:4096\n"
      "v_mul_f32_dpp v246, v190, v246 row_newbcast:8 row_mask:0xf bank_mask:0xf\n"
      "v_smfmac_f32_16x16x64_bf16 v[84:87], v[172:175], v[108:115], v5\n"
      "buffer_load_dwordx4 v1, s[40:43], s90 offen offset:0 lds\n"
      "v_smfmac_f32_16x16x64_bf16 v[92:95], v[172:175], v[56:63], v5\n"
      "v_smfmac_f32_16x16x64_bf16 v[96:99], v[172:175], v[232:239], v5\n"
      "v_mul_f32_dpp v247, v190, v247 row_newbcast:12 row_mask:0xf bank_mask:0xf\n"
      "v_mfma_f32_16x16x32_bf16 v[32:35], v[76:79], v[36:39], 0\n"
      "v_exp_f32 v30, v30\n"
      "v_mfma_f32_16x16x32_bf16 v[32:35], v[80:83], v[40:43], v[32:35]\n"
      "v_exp_f32 v29, v29\n"
      "v_smfmac_f32_16x16x32_bf16 v[92:95], v[144:145], v[116:119], v5\n"
      "v_smfmac_f32_16x16x32_bf16 v[88:91], v[144:145], v[120:123], v5\n"
      "v_maximum3_f32 v8, v8, v188, v189\n"
      "v_smfmac_f32_16x16x32_bf16 v[96:99], v[144:145], v[228:231], v5\n"
      "v_mul_f32_dpp v254, v190, v254 row_newbcast:8 row_mask:0xf bank_mask:0xf\n"
      "v_smfmac_f32_16x16x32_bf16 v[84:87], v[144:145], v[52:55], v5\n"
      "v_mul_f32_dpp v242, v190, v242 row_newbcast:8 row_mask:0xf bank_mask:0xf\n"
      "v_cvt_pk_bf16_f32 v223, v178, v179\n"
      "v_mul_f32_dpp v251, v190, v251 row_newbcast:12 row_mask:0xf bank_mask:0xf\n"
      "v_mfma_f32_16x16x32_bf16 v[212:215], v[20:23], v[36:39], 0\n"
      "v_mul_f32_dpp v241, v190, v241 row_newbcast:4 row_mask:0xf bank_mask:0xf\n"
      "v_mul_f32_dpp v255, v190, v255 row_newbcast:12 row_mask:0xf bank_mask:0xf\n"
      "v_mfma_f32_16x16x32_bf16 v[212:215], v[24:27], v[40:43], v[212:215]\n"
      "v_exp_f32 v31, v31\n"
      "v_mfma_f32_16x16x32_bf16 v[240:243], v[124:127], a[192:195], v[240:243]\n"
      "s_waitcnt lgkmcnt(0)\n"
      "v_and_b32 v191, 0xffff0000, v133\n"
      "v_add_f32 v96, v96, v97\n"
      "v_mfma_f32_16x16x32_bf16 v[240:243], v[100:103], a[196:199], v[240:243]\n"
      "v_fma_f32 v96, v96, v19, v191\n"
      "v_lshlrev_b32 v190, 16, v133\n"
      "v_mfma_f32_16x16x32_bf16 v[240:243], v[72:75], a[200:203], v[240:243]\n"
      "v_exp_f32 v214, v214\n"
      "v_mfma_f32_16x16x32_bf16 v[240:243], v[196:199], a[204:207], v[240:243]\n"
      "v_exp_f32 v213, v213\n"
      "v_mfma_f32_16x16x32_bf16 v[244:247], v[124:127], a[208:211], v[244:247]\n"
      "v_exp_f32 v28, v28\n"
      "v_mfma_f32_16x16x32_bf16 v[244:247], v[100:103], a[212:215], v[244:247]\n"
      "v_exp_f32 v32, v32\n"
      "v_mfma_f32_16x16x32_bf16 v[244:247], v[72:75], a[216:219], v[244:247]\n"
      "v_exp_f32 v212, v212\n"
      "v_mfma_f32_16x16x32_bf16 v[244:247], v[196:199], a[220:223], v[244:247]\n"
      "buffer_store_dwordx2 v[218:219], v2, s[44:47], s73 offen offset:768 nt sc1\n"
      "v_lshlrev_b32 v188, 16, v132\n"
      "v_mfma_f32_16x16x32_bf16 v[252:255], v[124:127], a[240:243], v[252:255]\n"
      "v_add_f32 v84, v84, v85\n"
      "v_fma_f32 v84, v84, v19, v188\n"
      "v_mfma_f32_16x16x32_bf16 v[252:255], v[100:103], a[244:247], v[252:255]\n"
      "v_and_b32 v189, 0xffff0000, v132\n"
      "v_add_f32 v92, v92, v93\n"
      "v_mfma_f32_16x16x32_bf16 v[252:255], v[72:75], a[248:251], v[252:255]\n"
      "v_fma_f32 v92, v92, v19, v190\n"
      "v_cvt_pk_bf16_f32 v85, v92, v96\n"
      "v_mfma_f32_16x16x32_bf16 v[252:255], v[196:199], a[252:255], v[252:255]\n"
      "v_exp_f32 v34, v34\n"
      "v_mfma_f32_16x16x32_bf16 v[172:175], v[204:207], v[36:39], 0\n"
      "buffer_load_dwordx4 v1, s[40:43], s91 offen offset:1024 lds\n"
      "v_mfma_f32_16x16x32_bf16 v[172:175], v[208:211], v[40:43], v[172:175]\n"
      "v_exp_f32 v215, v215\n"
      "v_mfma_f32_16x16x32_bf16 v[132:135], a[144:147], v[128:131], 0\n"
      "v_exp_f32 v33, v33\n"
      "v_mfma_f32_16x16x32_bf16 v[132:135], a[148:151], v[152:155], v[132:135]\n"
      "v_exp_f32 v35, v35\n"
      "v_mfma_f32_16x16x32_bf16 v[132:135], a[152:155], v[148:151], v[132:135]\n"
      "v_add_f32 v88, v88, v89\n"
      "v_fma_f32 v88, v88, v19, v189\n"
      "v_mfma_f32_16x16x32_bf16 v[132:135], a[156:159], v[224:227], v[132:135]\n"
      "v_exp_f32 v174, v174\n"
      "v_mfma_f32_16x16x32_bf16 v[144:147], a[128:131], v[128:131], 0\n"
      "v_pk_max_i16 v216, v216, 0\n"
      "v_mfma_f32_16x16x32_bf16 v[144:147], a[132:135], v[152:155], v[144:147]\n"
      "v_exp_f32 v172, v172\n"
      "v_mfma_f32_16x16x32_bf16 v[144:147], a[136:139], v[148:151], v[144:147]\n"
      "v_cvt_pk_bf16_f32 v99, v34, v35\n"
      "v_cvt_pk_bf16_f32 v98, v32, v33\n"
      "v_mfma_f32_16x16x32_bf16 v[144:147], a[140:143], v[224:227], v[144:147]\n"
      "v_cvt_pk_bf16_f32 v93, v30, v31\n"
      "v_cvt_pk_bf16_f32 v92, v28, v29\n"
      "v_mfma_f32_16x16x32_bf16 v[188:191], a[176:179], v[128:131], 0\n"
      "v_pk_max_i16 v85, v85, 0\n"
      "v_cvt_pk_bf16_f32 v84, v84, v88\n"
      "v_mfma_f32_16x16x32_bf16 v[188:191], a[180:183], v[152:155], v[188:191]\n"
      "v_cvt_pk_bf16_f32 v96, v212, v213\n"
      "v_pk_max_i16 v84, v84, 0\n"
      "v_mfma_f32_16x16x32_bf16 v[188:191], a[184:187], v[148:151], v[188:191]\n"
      "v_cvt_pk_bf16_f32 v31, v134, v135\n"
      "v_cvt_pk_bf16_f32 v28, v144, v145\n"
      "v_mfma_f32_16x16x32_bf16 v[188:191], a[188:191], v[224:227], v[188:191]\n"
      "v_cvt_pk_bf16_f32 v30, v132, v133\n"
      "v_cvt_pk_bf16_f32 v240, v240, v244\n"
      "v_mfma_f32_16x16x32_bf16 v[132:135], a[160:163], v[128:131], 0\n"
      "v_cvt_pk_bf16_f32 v244, v241, v245\n"
      "v_cvt_pk_bf16_f32 v29, v146, v147\n"
      "v_mfma_f32_16x16x32_bf16 v[132:135], a[164:167], v[152:155], v[132:135]\n"
      "v_exp_f32 v175, v175\n"
      "v_mfma_f32_16x16x32_bf16 v[132:135], a[168:171], v[148:151], v[132:135]\n"
      "v_exp_f32 v173, v173\n"
      "v_mfma_f32_16x16x32_bf16 v[132:135], a[172:175], v[224:227], v[132:135]\n"
      "buffer_store_dwordx2 v[216:217], v2, s[44:47], s73 offen offset:256 nt sc1\n"
      "v_mov_b64 v[176:177], 0\n"
      "v_mfma_f32_16x16x32_bf16 v[248:251], v[124:127], a[224:227], v[248:251]\n"
      "v_cvt_pk_bf16_f32 v95, v174, v175\n"
      "v_cvt_pk_bf16_f32 v34, v188, v189\n"
      "v_mfma_f32_16x16x32_bf16 v[248:251], v[100:103], a[228:231], v[248:251]\n"
      "v_cvt_pk_bf16_f32 v35, v190, v191\n"
      "v_cvt_pk_bf16_f32 v242, v242, v246\n"
      "v_mfma_f32_16x16x32_bf16 v[248:251], v[72:75], a[232:235], v[248:251]\n"
      "v_cvt_pk_bf16_f32 v33, v134, v135\n"
      "v_mov_b64 v[174:175], 0\n"
      "v_mfma_f32_16x16x32_bf16 v[248:251], v[196:199], a[236:239], v[248:251]\n"
      "v_cvt_pk_bf16_f32 v32, v132, v133\n"
      "v_cvt_pk_bf16_f32 v246, v243, v247\n"
      "v_smfmac_f32_16x16x64_bf16 v[174:177], v[160:163], v[36:43], v5\n"
      "buffer_load_dwordx4 v1, s[40:43], s90 offen offset:2048 lds\n"
      "v_smfmac_f32_16x16x64_bf16 v[174:177], v[44:47], v[28:35], v5\n"
      "ds_read_b128 v[72:75], v0 offset:4608\n"
      "v_pk_max_i16 v242, v242, 0\n"
      "v_mfma_f32_16x16x32_bf16 v[88:91], v[64:67], v[28:31], 0\n"
      "v_cvt_pk_bf16_f32 v245, v249, v253\n"
      "v_cvt_pk_bf16_f32 v94, v172, v173\n"
      "v_mfma_f32_16x16x32_bf16 v[88:91], v[68:71], v[32:35], v[88:91]\n"
      "v_cvt_pk_bf16_f32 v247, v251, v255\n"
      "v_pk_max_i16 v247, v247, 0\n"
      "v_mfma_f32_16x16x32_bf16 v[144:147], v[20:23], v[28:31], 0\n"
      "v_add_f32 v174, v174, v175\n"
      "v_add_f32 v176, v176, v177\n"
      "v_mfma_f32_16x16x32_bf16 v[144:147], v[24:27], v[32:35], v[144:147]\n"
      "v_exp_f32 v176, v176\n"
      "v_mfma_f32_16x16x32_bf16 v[196:199], v[76:79], v[28:31], 0\n"
      "v_exp_f32 v174, v174\n"
      "v_mfma_f32_16x16x32_bf16 v[196:199], v[80:83], v[32:35], v[196:199]\n"
      "v_add_f32 v213, v174, v176\n"
      "v_add_f32 v212, v174, v176\n"
      "v_mfma_f32_16x16x32_bf16 v[132:135], v[204:207], v[28:31], 0\n"
      "s_nop 0\n"
      "v_permlane16_swap_b32 v212, v213\n"
      "v_mfma_f32_16x16x32_bf16 v[132:135], v[208:211], v[32:35], v[132:135]\n"
      "v_cvt_pk_bf16_f32 v217, v174, v176\n"
      "v_cvt_pk_bf16_f32 v216, v174, v176\n"
      "v_mfma_f32_16x16x32_bf16 v[172:175], v[136:139], v[28:31], 0\n"
      "v_exp_f32 v197, v197\n"
      "v_mfma_f32_16x16x32_bf16 v[172:175], v[140:143], v[32:35], v[172:175]\n"
      "v_exp_f32 v144, v144\n"
      "v_mfma_f32_16x16x32_bf16 v[188:191], v[164:167], v[28:31], 0\n"
      "v_mov_b64 v[176:177], v[212:213]\n"
      "v_cvt_pk_bf16_f32 v97, v214, v215\n"
      "v_mfma_f32_16x16x32_bf16 v[188:191], v[168:171], v[32:35], v[188:191]\n"
      "v_permlane16_swap_b32 v216, v217\n"
      "v_smfmac_f32_16x16x64_bf16 v[212:215], v[12:15], v[92:99], v5\n"
      "buffer_store_dwordx2 v[84:85], v3, s[44:47], s79 offen offset:0 nt sc1\n"
      "v_pk_max_i16 v240, v240, 0\n"
      "v_mfma_f32_16x16x4_4b_bf16 v[28:43], v[216:217], v[156:157], 0 cbsz:2 abid:0\n"
      "ds_read_b128 v[124:127], v0 offset:5632\n"
      "v_cvt_pk_bf16_f32 v243, v250, v254\n"
      "v_exp_f32 v88, v88\n"
      "v_cvt_pk_bf16_f32 v241, v248, v252\n"
      "v_pk_max_i16 v241, v241, 0\n"
      "v_smfmac_f32_16x16x32_bf16 v[212:215], v[12:13], v[220:223], v5\n"
      "s_add_u32 m0, s53, 4096\n"
      "ds_read_b128 v[100:103], v0 offset:4096\n"
      "v_pk_max_i16 v243, v243, 0\n"
      "v_exp_f32 v145, v145\n"
      "v_mfma_f32_16x16x32_bf16 v[32:35], v[92:95], v[180:183], v[32:35]\n"
      "v_exp_f32 v132, v132\n"
      "v_mfma_f32_16x16x32_bf16 v[32:35], v[96:99], v[184:187], v[32:35]\n"
      "ds_read_b128 v[84:87], v0 offset:5120\n"
      "v_add_f32 v212, v212, v213\n"
      "v_mfma_f32_16x16x32_bf16 v[32:35], v[220:223], v[120:123], v[32:35]\n"
      "v_rcp_f32 v213, v212\n"
      "v_mfma_f32_16x16x32_bf16 v[28:31], v[92:95], v[108:111], v[28:31]\n"
      "ds_bpermute_b32 v214, v4, v213\n"
      "v_mfma_f32_16x16x32_bf16 v[28:31], v[96:99], v[112:115], v[28:31]\n"
      "v_exp_f32 v147, v147\n"
      "v_mfma_f32_16x16x32_bf16 v[28:31], v[220:223], v[52:55], v[28:31]\n"
      "v_exp_f32 v146, v146\n"
      "v_mfma_f32_16x16x32_bf16 v[36:39], v[92:95], v[56:59], v[36:39]\n"
      "v_exp_f32 v91, v91\n"
      "v_mfma_f32_16x16x32_bf16 v[36:39], v[96:99], v[60:63], v[36:39]\n"
      "v_exp_f32 v172, v172\n"
      "v_mfma_f32_16x16x32_bf16 v[36:39], v[220:223], v[116:119], v[36:39]\n"
      "v_exp_f32 v196, v196\n"
      "v_mfma_f32_16x16x32_bf16 v[40:43], v[92:95], v[232:235], v[40:43]\n"
      "v_exp_f32 v173, v173\n"
      "v_mfma_f32_16x16x32_bf16 v[40:43], v[96:99], v[236:239], v[40:43]\n"
      "v_exp_f32 v198, v198\n"
      "v_mfma_f32_16x16x32_bf16 v[40:43], v[220:223], v[228:231], v[40:43]\n"
      "s_waitcnt lgkmcnt(1)\n"
      "buffer_load_dwordx4 v1, s[40:43], s92 offen offset:0 lds\n"
      "v_mfma_f32_16x16x32_bf16 v[94:97], a[176:179], v[100:103], 0\n"
      "v_exp_f32 v134, v134\n"
      "v_mfma_f32_16x16x32_bf16 v[94:97], a[180:183], v[72:75], v[94:97]\n"
      "s_waitcnt lgkmcnt(0)\n"
      "v_mul_f32_dpp v28, v214, v28 row_newbcast:0 row_mask:0xf bank_mask:0xf\n"
      "v_mul_f32_dpp v29, v214, v29 row_newbcast:4 row_mask:0xf bank_mask:0xf\n"
      "v_mfma_f32_16x16x32_bf16 v[94:97], a[184:187], v[84:87], v[94:97]\n"
      "v_cvt_pk_bf16_f32 v253, v146, v147\n"
      "v_mul_f32_dpp v34, v214, v34 row_newbcast:8 row_mask:0xf bank_mask:0xf\n"
      "v_mfma_f32_16x16x32_bf16 v[94:97], a[188:191], v[124:127], v[94:97]\n"
      "v_exp_f32 v189, v189\n"
      "v_mfma_f32_16x16x32_bf16 v[218:221], a[128:131], v[100:103], 0\n"
      "v_exp_f32 v135, v135\n"
      "v_mfma_f32_16x16x32_bf16 v[218:221], a[132:135], v[72:75], v[218:221]\n"
      "v_mul_f32_dpp v32, v214, v32 row_newbcast:0 row_mask:0xf bank_mask:0xf\n"
      "v_mul_f32_dpp v33, v214, v33 row_newbcast:4 row_mask:0xf bank_mask:0xf\n"
      "v_mfma_f32_16x16x32_bf16 v[218:221], a[136:139], v[84:87], v[218:221]\n"
      "v_mul_f32_dpp v30, v214, v30 row_newbcast:8 row_mask:0xf bank_mask:0xf\n"
      "v_mul_f32_dpp v35, v214, v35 row_newbcast:12 row_mask:0xf bank_mask:0xf\n"
      "v_mfma_f32_16x16x32_bf16 v[218:221], a[140:143], v[124:127], v[218:221]\n"
      "v_exp_f32 v89, v89\n"
      "v_mfma_f32_16x16x32_bf16 v[32:35], v[192:195], a[208:211], v[32:35]\n"
      "v_mul_f32_dpp v38, v214, v38 row_newbcast:8 row_mask:0xf bank_mask:0xf\n"
      "v_mul_f32_dpp v42, v214, v42 row_newbcast:8 row_mask:0xf bank_mask:0xf\n"
      "v_mfma_f32_16x16x32_bf16 v[32:35], v[200:203], a[212:215], v[32:35]\n"
      "v_mul_f32_dpp v41, v214, v41 row_newbcast:4 row_mask:0xf bank_mask:0xf\n"
      "v_cvt_pk_bf16_f32 v252, v144, v145\n"
      "v_mfma_f32_16x16x32_bf16 v[32:35], v[104:107], a[216:219], v[32:35]\n"
      "v_mul_f32_dpp v36, v214, v36 row_newbcast:0 row_mask:0xf bank_mask:0xf\n"
      "v_mul_f32_dpp v31, v214, v31 row_newbcast:12 row_mask:0xf bank_mask:0xf\n"
      "v_mfma_f32_16x16x32_bf16 v[32:35], v[48:51], a[220:223], v[32:35]\n"
      "v_exp_f32 v188, v188\n"
      "v_mfma_f32_16x16x32_bf16 v[28:31], v[192:195], a[192:195], v[28:31]\n"
      "v_mul_f32_dpp v40, v214, v40 row_newbcast:0 row_mask:0xf bank_mask:0xf\n"
      "v_mfma_f32_16x16x32_bf16 v[28:31], v[200:203], a[196:199], v[28:31]\n"
      "v_exp_f32 v174, v174\n"
      "v_mfma_f32_16x16x32_bf16 v[28:31], v[104:107], a[200:203], v[28:31]\n"
      "v_mul_f32_dpp v37, v214, v37 row_newbcast:4 row_mask:0xf bank_mask:0xf\n"
      "v_mul_f32_dpp v39, v214, v39 row_newbcast:12 row_mask:0xf bank_mask:0xf\n"
      "v_mfma_f32_16x16x32_bf16 v[28:31], v[48:51], a[204:207], v[28:31]\n"
      "v_maximum3_f32 v8, v8, v212, v213\n"
      "v_cvt_pk_bf16_f32 v254, v196, v197\n"
      "v_mfma_f32_16x16x32_bf16 v[36:39], v[192:195], a[224:227], v[36:39]\n"
      "v_exp_f32 v191, v191\n"
      "v_mfma_f32_16x16x32_bf16 v[36:39], v[200:203], a[228:231], v[36:39]\n"
      "buffer_load_dwordx4 v1, s[40:43], s93 offen offset:3072 lds\n"
      "v_mfma_f32_16x16x32_bf16 v[36:39], v[104:107], a[232:235], v[36:39]\n"
      "v_mul_f32_dpp v43, v214, v43 row_newbcast:12 row_mask:0xf bank_mask:0xf\n"
      "v_cvt_pk_bf16_f32 v28, v28, v32\n"
      "v_mfma_f32_16x16x32_bf16 v[36:39], v[48:51], a[236:239], v[36:39]\n"
      "ds_read_b128 v[144:147], v0 offset:0\n"
      "v_cvt_pk_bf16_f32 v214, v172, v173\n"
      "v_mfma_f32_16x16x32_bf16 v[40:43], v[192:195], a[240:243], v[40:43]\n"
      "v_exp_f32 v199, v199\n"
      "v_mfma_f32_16x16x32_bf16 v[40:43], v[200:203], a[244:247], v[40:43]\n"
      "v_exp_f32 v133, v133\n"
      "v_mfma_f32_16x16x32_bf16 v[40:43], v[104:107], a[248:251], v[40:43]\n"
      "v_cvt_pk_bf16_f32 v248, v188, v189\n"
      "v_mfma_f32_16x16x32_bf16 v[40:43], v[48:51], a[252:255], v[40:43]\n"
      "v_exp_f32 v190, v190\n"
      "v_mfma_f32_16x16x32_bf16 v[104:107], a[160:163], v[100:103], 0\n"
      "v_exp_f32 v90, v90\n"
      "v_mfma_f32_16x16x32_bf16 v[104:107], a[164:167], v[72:75], v[104:107]\n"
      "v_cvt_pk_bf16_f32 v213, v90, v91\n"
      "v_mov_b64 v[50:51], 0\n"
      "v_mfma_f32_16x16x32_bf16 v[104:107], a[168:171], v[84:87], v[104:107]\n"
      "v_cvt_pk_bf16_f32 v249, v190, v191\n"
      "v_pk_max_i16 v246, v246, 0\n"
      "v_mfma_f32_16x16x32_bf16 v[104:107], a[172:175], v[124:127], v[104:107]\n"
      "v_cvt_pk_bf16_f32 v212, v88, v89\n"
      "v_cvt_pk_bf16_f32 v250, v132, v133\n"
      "v_mfma_f32_16x16x32_bf16 v[88:91], a[144:147], v[100:103], 0\n"
      "v_cvt_pk_bf16_f32 v99, v96, v97\n"
      "v_cvt_pk_bf16_f32 v255, v198, v199\n"
      "v_mfma_f32_16x16x32_bf16 v[88:91], a[148:151], v[72:75], v[88:91]\n"
      "v_cvt_pk_bf16_f32 v251, v134, v135\n"
      "v_cvt_pk_bf16_f32 v98, v94, v95\n"
      "v_mfma_f32_16x16x32_bf16 v[88:91], a[152:155], v[84:87], v[88:91]\n"
      "v_cvt_pk_bf16_f32 v32, v29, v33\n"
      "v_cvt_pk_bf16_f32 v33, v37, v41\n"
      "v_mfma_f32_16x16x32_bf16 v[88:91], a[156:159], v[124:127], v[88:91]\n"
      "v_exp_f32 v175, v175\n"
      "v_smfmac_f32_16x16x64_bf16 v[176:179], v[12:15], v[248:255], v5\n"
      "buffer_store_dwordx2 v[246:247], v2, s[44:47], s75 offen offset:768 nt sc1\n"
      "v_cvt_pk_bf16_f32 v215, v174, v175\n"
      "v_mfma_f32_16x16x4_4b_bf16 v[188:203], v[216:217], v[156:157], 0 cbsz:2 abid:2\n"
      "v_cvt_pk_bf16_f32 v30, v30, v34\n"
      "v_pk_max_i16 v245, v245, 0\n"
      "v_cvt_pk_bf16_f32 v93, v220, v221\n"
      "v_cvt_pk_bf16_f32 v29, v36, v40\n"
      "v_cvt_pk_bf16_f32 v96, v104, v105\n"
      "v_cvt_pk_bf16_f32 v97, v106, v107\n"
      "v_smfmac_f32_16x16x32_bf16 v[176:179], v[12:13], v[212:215], v5\n"
      "ds_read_b128 v[172:175], v0 offset:1024\n"
      "v_cvt_pk_bf16_f32 v34, v31, v35\n"
      "v_cvt_pk_bf16_f32 v95, v90, v91\n"
      "v_cvt_pk_bf16_f32 v94, v88, v89\n"
      "v_mfma_f32_16x16x32_bf16 v[200:203], v[248:251], v[232:235], v[200:203]\n"
      "v_cvt_pk_bf16_f32 v92, v218, v219\n"
      "v_cvt_pk_bf16_f32 v35, v39, v43\n"
      "v_mfma_f32_16x16x32_bf16 v[200:203], v[252:255], v[236:239], v[200:203]\n"
      "buffer_load_dwordx4 v1, s[40:43], s93 offen offset:1024 lds\n"
      "v_mfma_f32_16x16x32_bf16 v[200:203], v[212:215], v[228:231], v[200:203]\n"
      "ds_read_b128 v[104:107], v0 offset:512\n"
      "v_add_f32 v176, v176, v177\n"
      "v_mfma_f32_16x16x32_bf16 v[88:91], v[76:79], v[92:95], 0\n"
      "v_cvt_pk_bf16_f32 v31, v38, v42\n"
      "v_mov_b64 v[48:49], 0\n"
      "v_mfma_f32_16x16x32_bf16 v[88:91], v[80:83], v[96:99], v[88:91]\n"
      "ds_read_b128 v[132:135], v0 offset:1536\n"
      "v_pk_max_i16 v35, v35, 0\n"
      "v_mfma_f32_16x16x32_bf16 v[40:43], v[20:23], v[92:95], 0\n"
      "v_rcp_f32 v177, v176\n"
      "v_mfma_f32_16x16x32_bf16 v[40:43], v[24:27], v[96:99], v[40:43]\n"
      "ds_bpermute_b32 v178, v4, v177 offset:128\n"
      "v_smfmac_f32_16x16x64_bf16 v[48:51], v[160:163], v[92:99], v5\n"
      "buffer_store_dwordx2 v[240:241], v2, s[44:47], s75 offen offset:0 nt sc1\n"
      "v_pk_max_i16 v29, v29, 0\n"
      "v_mfma_f32_16x16x32_bf16 v[36:39], v[204:207], v[92:95], 0\n"
      "v_exp_f32 v90, v90\n"
      "v_mfma_f32_16x16x32_bf16 v[36:39], v[208:211], v[96:99], v[36:39]\n"
      "v_exp_f32 v88, v88\n"
      "v_mfma_f32_16x16x32_bf16 v[188:191], v[248:251], v[108:111], v[188:191]\n"
      "v_exp_f32 v42, v42\n"
      "v_mfma_f32_16x16x32_bf16 v[188:191], v[252:255], v[112:115], v[188:191]\n"
      "v_exp_f32 v41, v41\n"
      "v_mfma_f32_16x16x32_bf16 v[188:191], v[212:215], v[52:55], v[188:191]\n"
      "buffer_load_dwordx4 v1, s[40:43], s92 offen offset:2048 lds\n"
      "v_mfma_f32_16x16x32_bf16 v[196:199], v[248:251], v[56:59], v[196:199]\n"
      "v_exp_f32 v91, v91\n"
      "v_mfma_f32_16x16x32_bf16 v[196:199], v[252:255], v[60:63], v[196:199]\n"
      "v_exp_f32 v39, v39\n"
      "v_mfma_f32_16x16x32_bf16 v[196:199], v[212:215], v[116:119], v[196:199]\n"
      "v_exp_f32 v89, v89\n"
      "v_mfma_f32_16x16x32_bf16 v[192:195], v[248:251], v[180:183], v[192:195]\n"
      "v_exp_f32 v36, v36\n"
      "v_mfma_f32_16x16x32_bf16 v[192:195], v[252:255], v[184:187], v[192:195]\n"
      "s_waitcnt lgkmcnt(0)\n"
      "v_mul_f32_dpp v200, v178, v200 row_newbcast:0 row_mask:0xf bank_mask:0xf\n"
      "v_mul_f32_dpp v188, v178, v188 row_newbcast:0 row_mask:0xf bank_mask:0xf\n"
      "v_mfma_f32_16x16x32_bf16 v[192:195], v[212:215], v[120:123], v[192:195]\n"
      "v_mul_f32_dpp v201, v178, v201 row_newbcast:4 row_mask:0xf bank_mask:0xf\n"
      "v_cvt_pk_bf16_f32 v218, v88, v89\n"
      "v_mfma_f32_16x16x32_bf16 v[220:223], v[64:67], v[92:95], 0\n"
      "v_exp_f32 v40, v40\n"
      "v_mfma_f32_16x16x32_bf16 v[220:223], v[68:71], v[96:99], v[220:223]\n"
      "v_exp_f32 v43, v43\n"
      "v_mfma_f32_16x16x32_bf16 v[160:163], v[136:139], v[92:95], 0\n"
      "v_cvt_pk_bf16_f32 v217, v42, v43\n"
      "v_mul_f32_dpp v192, v178, v192 row_newbcast:0 row_mask:0xf bank_mask:0xf\n"
      "v_mfma_f32_16x16x32_bf16 v[160:163], v[140:143], v[96:99], v[160:163]\n"
      "v_cvt_pk_bf16_f32 v216, v40, v41\n"
      "v_mul_f32_dpp v203, v178, v203 row_newbcast:12 row_mask:0xf bank_mask:0xf\n"
      "v_mfma_f32_16x16x32_bf16 v[40:43], v[164:167], v[92:95], 0\n"
      "v_exp_f32 v37, v37\n"
      "v_mfma_f32_16x16x32_bf16 v[40:43], v[168:171], v[96:99], v[40:43]\n"
      "v_exp_f32 v221, v221\n"
      "v_mfma_f32_16x16x32_bf16 v[92:95], a[144:147], v[144:147], 0\n"
      "s_nop 0\n"
      "v_exp_f32 v161, v161\n"
      "v_mfma_f32_16x16x32_bf16 v[92:95], a[148:151], v[104:107], v[92:95]\n"
      "v_cvt_pk_bf16_f32 v219, v90, v91\n"
      "v_mul_f32_dpp v189, v178, v189 row_newbcast:4 row_mask:0xf bank_mask:0xf\n"
      "v_mfma_f32_16x16x32_bf16 v[92:95], a[152:155], v[172:175], v[92:95]\n"
      "v_exp_f32 v38, v38\n"
      "v_mfma_f32_16x16x32_bf16 v[92:95], a[156:159], v[132:135], v[92:95]\n"
      "buffer_store_dwordx2 v[242:243], v2, s[44:47], s75 offen offset:512 nt sc1\n"
      "v_mul_f32_dpp v191, v178, v191 row_newbcast:12 row_mask:0xf bank_mask:0xf\n"
      "v_mfma_f32_16x16x32_bf16 v[88:91], a[128:131], v[144:147], 0\n"
      "v_exp_f32 v220, v220\n"
      "v_mfma_f32_16x16x32_bf16 v[88:91], a[132:135], v[104:107], v[88:91]\n"
      "v_exp_f32 v43, v43\n"
      "v_mfma_f32_16x16x32_bf16 v[88:91], a[136:139], v[172:175], v[88:91]\n"
      "s_nop 0\n"
      "v_cvt_pk_bf16_f32 v249, v94, v95\n"
      "v_mul_f32_dpp v197, v178, v197 row_newbcast:4 row_mask:0xf bank_mask:0xf\n"
      "v_mfma_f32_16x16x32_bf16 v[88:91], a[140:143], v[132:135], v[88:91]\n"
      "v_exp_f32 v223, v223\n"
      "v_mfma_f32_16x16x32_bf16 v[212:215], a[176:179], v[144:147], 0\n"
      "v_mul_f32_dpp v198, v178, v198 row_newbcast:8 row_mask:0xf bank_mask:0xf\n"
      "v_mul_f32_dpp v190, v178, v190 row_newbcast:8 row_mask:0xf bank_mask:0xf\n"
      "v_mfma_f32_16x16x32_bf16 v[212:215], a[180:183], v[104:107], v[212:215]\n"
      "v_exp_f32 v163, v163\n"
      "v_mfma_f32_16x16x32_bf16 v[212:215], a[184:187], v[172:175], v[212:215]\n"
      "v_mul_f32_dpp v195, v178, v195 row_newbcast:12 row_mask:0xf bank_mask:0xf\n"
      "v_cvt_pk_bf16_f32 v247, v90, v91\n"
      "v_mfma_f32_16x16x32_bf16 v[212:215], a[188:191], v[132:135], v[212:215]\n"
      "s_not_b64 vcc, vcc\n"
      "v_mul_f32_dpp v194, v178, v194 row_newbcast:8 row_mask:0xf bank_mask:0xf\n"
      "v_cvt_pk_bf16_f32 v248, v92, v93\n"
      "v_mfma_f32_16x16x32_bf16 v[92:95], a[160:163], v[144:147], 0\n"
      "v_exp_f32 v160, v160\n"
      "v_mfma_f32_16x16x32_bf16 v[92:95], a[164:167], v[104:107], v[92:95]\n"
      "v_cvt_pk_bf16_f32 v246, v88, v89\n"
      "v_mul_f32_dpp v202, v178, v202 row_newbcast:8 row_mask:0xf bank_mask:0xf\n"
      "v_mfma_f32_16x16x32_bf16 v[92:95], a[168:171], v[172:175], v[92:95]\n"
      "v_cvt_pk_bf16_f32 v252, v212, v213\n"
      "v_cvt_pk_bf16_f32 v253, v214, v215\n"
      "v_mfma_f32_16x16x32_bf16 v[92:95], a[172:175], v[132:135], v[92:95]\n"
      "s_add_u32 m0, s53, 0\n"
      "v_exp_f32 v162, v162\n"
      "v_mfma_f32_16x16x32_bf16 v[188:191], v[128:131], a[192:195], v[188:191]\n"
      "buffer_load_dwordx4 v1, s[40:43], s94 offen offset:2048 lds\n"
      "v_mfma_f32_16x16x32_bf16 v[188:191], v[152:155], a[196:199], v[188:191]\n"
      "v_exp_f32 v42, v42\n"
      "v_mfma_f32_16x16x32_bf16 v[188:191], v[148:151], a[200:203], v[188:191]\n"
      "s_nop 0\n"
      "v_cvt_pk_bf16_f32 v250, v92, v93\n"
      "v_cvt_pk_bf16_f32 v251, v94, v95\n"
      "v_mfma_f32_16x16x32_bf16 v[188:191], v[224:227], a[204:207], v[188:191]\n"
      "v_cvt_pk_bf16_f32 v213, v42, v43\n"
      "v_mul_f32_dpp v199, v178, v199 row_newbcast:12 row_mask:0xf bank_mask:0xf\n"
      "v_smfmac_f32_16x16x64_bf16 v[48:51], v[44:47], v[246:253], v5\n"
      "v_cvt_pk_bf16_f32 v215, v38, v39\n"
      "v_mfma_f32_16x16x32_bf16 v[200:203], v[128:131], a[240:243], v[200:203]\n"
      "v_mul_f32_dpp v193, v178, v193 row_newbcast:4 row_mask:0xf bank_mask:0xf\n"
      "v_mul_f32_dpp v196, v178, v196 row_newbcast:0 row_mask:0xf bank_mask:0xf\n"
      "v_mfma_f32_16x16x32_bf16 v[200:203], v[152:155], a[244:247], v[200:203]\n"
      "v_exp_f32 v40, v40\n"
      "v_mfma_f32_16x16x32_bf16 v[200:203], v[148:151], a[248:251], v[200:203]\n"
      "v_exp_f32 v41, v41\n"
      "v_mfma_f32_16x16x32_bf16 v[200:203], v[224:227], a[252:255], v[200:203]\n"
      "v_add_f32 v50, v50, v51\n"
      "v_add_f32 v48, v48, v49\n"
      "v_mfma_f32_16x16x32_bf16 v[192:195], v[128:131], a[208:211], v[192:195]\n"
      "v_exp_f32 v48, v48\n"
      "v_mfma_f32_16x16x32_bf16 v[192:195], v[152:155], a[212:215], v[192:195]\n"
      "v_exp_f32 v50, v50\n"
      "v_mfma_f32_16x16x32_bf16 v[192:195], v[148:151], a[216:219], v[192:195]\n"
      "v_cvt_pk_bf16_f32 v242, v48, v50\n"
      "v_cvt_pk_bf16_f32 v243, v48, v50\n"
      "v_mfma_f32_16x16x32_bf16 v[192:195], v[224:227], a[220:223], v[192:195]\n"
      "v_add_f32 v88, v48, v50\n"
      "v_add_f32 v89, v48, v50\n"
      "v_mfma_f32_16x16x32_bf16 v[196:199], v[128:131], a[224:227], v[196:199]\n"
      "s_nop 0\n"
      "v_permlane16_swap_b32 v88, v89\n"
      "v_mfma_f32_16x16x32_bf16 v[196:199], v[152:155], a[228:231], v[196:199]\n"
      "v_permlane16_swap_b32 v242, v243\n"
      "v_mfma_f32_16x16x32_bf16 v[196:199], v[148:151], a[232:235], v[196:199]\n"
      "v_cvt_pk_bf16_f32 v212, v40, v41\n"
      "v_cvt_pk_bf16_f32 v214, v36, v37\n"
      "v_mfma_f32_16x16x32_bf16 v[196:199], v[224:227], a[236:239], v[196:199]\n"
      "v_cvt_pk_bf16_f32 v224, v220, v221\n"
      "v_mov_b64 v[128:129], v[88:89]\n"
      "v_smfmac_f32_16x16x64_bf16 v[88:91], v[12:15], v[212:219], v5\n"
      "v_cvt_pk_bf16_f32 v226, v160, v161\n"
      "v_mfma_f32_16x16x4_4b_bf16 v[36:51], v[242:243], v[156:157], 0 cbsz:2 abid:0\n"
      "v_exp_f32 v222, v222\n"
      "v_cvt_pk_bf16_f32 v227, v162, v163\n"
      "v_cvt_pk_bf16_f32 v225, v222, v223\n"
      "v_cvt_pk_bf16_f32 v188, v188, v192\n"
      "v_cvt_pk_bf16_f32 v190, v190, v194\n"
      "v_smfmac_f32_16x16x32_bf16 v[88:91], v[12:13], v[224:227], v5\n"
      "s_nop 3\n"
      "v_mfma_f32_16x16x32_bf16 v[40:43], v[212:215], v[180:183], v[40:43]\n"
      "buffer_load_dwordx4 v1, s[40:43], s95 offen offset:1024 lds\n"
      "v_mfma_f32_16x16x32_bf16 v[40:43], v[216:219], v[184:187], v[40:43]\n"
      "v_pk_max_i16 v244, v244, 0\n"
      "v_add_f32 v88, v88, v89\n"
      "v_mfma_f32_16x16x32_bf16 v[40:43], v[224:227], v[120:123], v[40:43]\n"
      "v_rcp_f32 v89, v88\n"
      "v_mfma_f32_16x16x32_bf16 v[48:51], v[212:215], v[232:235], v[48:51]\n"
      "ds_bpermute_b32 v90, v4, v89\n"
      "v_mfma_f32_16x16x32_bf16 v[48:51], v[216:219], v[236:239], v[48:51]\n"
      "v_cvt_pk_bf16_f32 v192, v189, v193\n"
      "v_cvt_pk_bf16_f32 v194, v191, v195\n"
      "v_mfma_f32_16x16x32_bf16 v[48:51], v[224:227], v[228:231], v[48:51]\n"
      "v_pk_max_i16 v192, v192, 0\n"
      "v_cvt_pk_bf16_f32 v189, v196, v200\n"
      "v_mfma_f32_16x16x32_bf16 v[36:39], v[212:215], v[108:111], v[36:39]\n"
      "v_pk_max_i16 v194, v194, 0\n"
      "v_cvt_pk_bf16_f32 v195, v199, v203\n"
      "v_mfma_f32_16x16x32_bf16 v[36:39], v[216:219], v[112:115], v[36:39]\n"
      "v_cvt_pk_bf16_f32 v193, v197, v201\n"
      "v_pk_max_i16 v31, v31, 0\n"
      "v_mfma_f32_16x16x32_bf16 v[36:39], v[224:227], v[52:55], v[36:39]\n"
      "v_mfma_f32_16x16x32_bf16 v[44:47], v[212:215], v[56:59], v[44:47]\n"
      "buffer_load_dwordx4 v1, s[40:43], s94 offen offset:0 lds\n"
      "v_mfma_f32_16x16x32_bf16 v[44:47], v[216:219], v[60:63], v[44:47]\n"
      "v_pk_max_i16 v189, v189, 0\n"
      "v_mfma_f32_16x16x32_bf16 v[44:47], v[224:227], v[116:119], v[44:47]\n"
      "buffer_store_dwordx2 v[244:245], v2, s[44:47], s75 offen offset:256 nt sc1\n"
      "v_cvt_pk_bf16_f32 v191, v198, v202\n"
      "v_mfma_f32_16x16x4_4b_bf16 v[212:227], v[242:243], v[156:157], 0 cbsz:2 abid:2\n"
      "s_waitcnt lgkmcnt(0)\n"
      "v_mul_f32_dpp v51, v90, v51 row_newbcast:12 row_mask:0xf bank_mask:0xf\n"
      "v_mul_f32_dpp v48, v90, v48 row_newbcast:0 row_mask:0xf bank_mask:0xf\n"
      "v_mul_f32_dpp v43, v90, v43 row_newbcast:12 row_mask:0xf bank_mask:0xf\n"
      "v_mul_f32_dpp v50, v90, v50 row_newbcast:8 row_mask:0xf bank_mask:0xf\n"
      "v_mul_f32_dpp v49, v90, v49 row_newbcast:4 row_mask:0xf bank_mask:0xf\n"
      "v_mul_f32_dpp v45, v90, v45 row_newbcast:4 row_mask:0xf bank_mask:0xf\n"
      "v_mul_f32_dpp v46, v90, v46 row_newbcast:8 row_mask:0xf bank_mask:0xf\n"
      "v_pk_max_i16 v191, v191, 0\n"
      "v_mfma_f32_16x16x32_bf16 v[48:51], v[100:103], a[240:243], v[48:51]\n"
      "v_pk_max_i16 v34, v34, 0\n"
      "v_mul_f32_dpp v41, v90, v41 row_newbcast:4 row_mask:0xf bank_mask:0xf\n"
      "v_mfma_f32_16x16x32_bf16 v[48:51], v[72:75], a[244:247], v[48:51]\n"
      "v_mul_f32_dpp v37, v90, v37 row_newbcast:4 row_mask:0xf bank_mask:0xf\n"
      "v_mul_f32_dpp v39, v90, v39 row_newbcast:12 row_mask:0xf bank_mask:0xf\n"
      "v_mfma_f32_16x16x32_bf16 v[48:51], v[84:87], a[248:251], v[48:51]\n"
      "v_mul_f32_dpp v40, v90, v40 row_newbcast:0 row_mask:0xf bank_mask:0xf\n"
      "v_mul_f32_dpp v42, v90, v42 row_newbcast:8 row_mask:0xf bank_mask:0xf\n"
      "v_mfma_f32_16x16x32_bf16 v[48:51], v[124:127], a[252:255], v[48:51]\n"
      "v_pk_max_i16 v33, v33, 0\n"
      "v_mfma_f32_16x16x32_bf16 v[40:43], v[100:103], a[208:211], v[40:43]\n"
      "v_maximum3_f32 v8, v8, v176, v177\n"
      "v_pk_max_i16 v30, v30, 0\n"
      "v_mfma_f32_16x16x32_bf16 v[40:43], v[72:75], a[212:215], v[40:43]\n"
      "v_mul_f32_dpp v44, v90, v44 row_newbcast:0 row_mask:0xf bank_mask:0xf\n"
      "v_pk_max_i16 v28, v28, 0\n"
      "v_mfma_f32_16x16x32_bf16 v[40:43], v[84:87], a[216:219], v[40:43]\n"
      "v_mul_f32_dpp v47, v90, v47 row_newbcast:12 row_mask:0xf bank_mask:0xf\n"
      "v_maximum3_f32 v8, v8, v88, v89\n"
      "v_mfma_f32_16x16x32_bf16 v[40:43], v[124:127], a[220:223], v[40:43]\n"
      "v_pk_max_i16 v190, v190, 0\n"
      "v_add_u32 v10, 64, v10\n"
      "v_mfma_f32_16x16x32_bf16 v[44:47], v[100:103], a[224:227], v[44:47]\n"
      "v_mul_f32_dpp v36, v90, v36 row_newbcast:0 row_mask:0xf bank_mask:0xf\n"
      "v_mul_f32_dpp v38, v90, v38 row_newbcast:8 row_mask:0xf bank_mask:0xf\n"
      "v_mfma_f32_16x16x32_bf16 v[44:47], v[72:75], a[228:231], v[44:47]\n"
      "v_add_u32 v11, 0x200, v11\n"
      "v_mfma_f32_16x16x32_bf16 v[44:47], v[84:87], a[232:235], v[44:47]\n"
      "s_add_u32 s44, s44, 102400\n s_addc_u32 s45, s45, 0\n"
      "v_pk_max_i16 v193, v193, 0\n"
      "v_mfma_f32_16x16x32_bf16 v[44:47], v[124:127], a[236:239], v[44:47]\n"
      "L_h5_%=:\n"
      "s_cbranch_execz L_hr_%=\n"
      "v_pk_max_i16 v195, v195, 0\n"
      "v_pk_max_i16 v32, v32, 0\n"
      "v_mfma_f32_16x16x32_bf16 v[36:39], v[100:103], a[192:195], v[36:39]\n"
      "v_pk_max_i16 v188, v188, 0\n"
      "v_mfma_f32_16x16x32_bf16 v[36:39], v[72:75], a[196:199], v[36:39]\n"
      "buffer_load_dwordx4 v1, s[40:43], s95 offen offset:3072 lds\n"
      "v_mfma_f32_16x16x32_bf16 v[36:39], v[84:87], a[200:203], v[36:39]\n"
      "s_add_u32 s40, s40, 102400\n s_addc_u32 s41, s41, 0\n"
      "s_add_u32 s38, s38, s38\n"
      "v_mfma_f32_16x16x32_bf16 v[36:39], v[124:127], a[204:207], v[36:39]\n"
      "s_cbranch_scc0 L_task_%=\n"
      "s_setpc_b64 s[28:29]\n"
      "L_e5_%=:\n"
      "s_getpc_b64 s[20:21]\n"
      "L_te_%=:\n"
      "s_lshl_b32 s22, %[wave], 12\n"
      "s_add_u32 s22, s22, L_ln_%=-L_te_%=\n"
      "v_mbcnt_lo_u32_b32 v17, -1, 0\n"
      "v_mbcnt_hi_u32_b32 v17, -1, v17\n"
      "v_lshlrev_b32 v17, 6, v17\n"
      "v_add_u32 v17, s22, v17\n"
      "v_min_u32 v17, L_ai_end-L_te_%=, v17\n"
      "global_load_dword v17, v17, s[20:21]\n"
      "s_add_u32 s28, s28, L_ln_%=-L_e5_%=\n"
      "s_addc_u32 s29, s29, 0\n"
      "s_branch L_task_%=\n"
      "L_h6_%=:\n"
      "s_cbranch_execz L_hr_%=\n"
      "L_ln_%=:\n"
      "v_mfma_f32_16x16x32_bf16 v[242:245], v[76:79], v[246:249], 0\n"
      "s_waitcnt vmcnt(13)\n"
      "ds_read_b128 v[200:203], v0 offset:8192\n"
      "s_nop 2\n"
      "v_cvt_pk_bf16_f32 v38, v38, v42\n"
      "v_mfma_f32_16x16x32_bf16 v[242:245], v[80:83], v[250:253], v[242:245]\n"
      "ds_read_b128 v[82:85], v0 offset:20480\n"
      "ds_read_b128 v[124:127], v0 offset:21504\n"
      "v_mfma_f32_16x16x32_bf16 v[156:159], v[204:207], v[246:249], 0\n"
      "ds_read_b128 v[86:89], v0 offset:9216\n"
      "v_mfma_f32_16x16x32_bf16 v[156:159], v[208:211], v[250:253], v[156:159]\n"
      "buffer_store_dwordx2 v[188:189], v2, s[44:47], s101 offen offset:0 nt sc1\n"
      "ds_read_b128 v[90:93], v0 offset:8704\n"
      "v_mfma_f32_16x16x32_bf16 v[148:151], v[136:139], v[246:249], 0\n"
      "ds_read_b128 v[176:179], v0 offset:9728\n"
      "v_mfma_f32_16x16x32_bf16 v[148:151], v[140:143], v[250:253], v[148:151]\n"
      "v_exp_f32 v242, v242\n"
      "v_mfma_f32_16x16x32_bf16 v[152:155], v[64:67], v[246:249], 0\n"
      "v_exp_f32 v243, v243\n"
      "v_mfma_f32_16x16x32_bf16 v[152:155], v[68:71], v[250:253], v[152:155]\n"
      "buffer_store_dwordx2 v[192:193], v2, s[44:47], s101 offen offset:256 nt sc1\n"
      "v_mfma_f32_16x16x32_bf16 v[96:99], v[164:167], v[246:249], 0\n"
      "v_exp_f32 v245, v245\n"
      "v_mfma_f32_16x16x32_bf16 v[96:99], v[168:171], v[250:253], v[96:99]\n"
      "v_exp_f32 v151, v151\n"
      "v_mfma_f32_16x16x32_bf16 v[168:171], v[20:23], v[246:249], 0\n"
      "ds_read_b128 v[68:71], v0 offset:16384\n"
      "v_cvt_pk_bf16_f32 v100, v242, v243\n"
      "v_mfma_f32_16x16x32_bf16 v[168:171], v[24:27], v[250:253], v[168:171]\n"
      "buffer_store_dwordx2 v[28:29], v2, s[44:47], s100 offen offset:0 nt sc1\n"
      "s_waitcnt lgkmcnt(6)\n"
      "v_mfma_f32_16x16x32_bf16 v[140:143], a[48:51], v[200:203], 0\n"
      "s_waitcnt lgkmcnt(2)\n"
      "v_mfma_f32_16x16x32_bf16 v[140:143], a[52:55], v[90:93], v[140:143]\n"
      "ds_read_b128 v[252:255], v0 offset:22016\n"
      "ds_read_b128 v[164:167], v0 offset:20992\n"
      "v_mfma_f32_16x16x32_bf16 v[140:143], a[56:59], v[86:89], v[140:143]\n"
      "s_waitcnt lgkmcnt(3)\n"
      "v_mfma_f32_16x16x32_bf16 v[140:143], a[60:63], v[176:179], v[140:143]\n"
      "v_cvt_pk_bf16_f32 v36, v36, v40\n"
      "v_mfma_f32_16x16x32_bf16 v[74:77], v[200:203], a[80:83], 0\n"
      "v_exp_f32 v156, v156\n"
      "v_mfma_f32_16x16x32_bf16 v[74:77], v[90:93], a[84:87], v[74:77]\n"
      "v_exp_f32 v168, v168\n"
      "v_mfma_f32_16x16x32_bf16 v[74:77], v[86:89], a[88:91], v[74:77]\n"
      "v_exp_f32 v97, v97\n"
      "v_mfma_f32_16x16x32_bf16 v[74:77], v[176:179], a[92:95], v[74:77]\n"
      "v_mfma_f32_16x16x32_bf16 v[208:211], a[16:19], v[200:203], 0\n"
      "v_mfma_f32_16x16x32_bf16 v[208:211], a[20:23], v[90:93], v[208:211]\n"
      "v_exp_f32 v155, v155\n"
      "v_mfma_f32_16x16x32_bf16 v[208:211], a[24:27], v[86:89], v[208:211]\n"
      "v_exp_f32 v150, v150\n"
      "v_mfma_f32_16x16x32_bf16 v[208:211], a[28:31], v[176:179], v[208:211]\n"
      "ds_read_b128 v[18:21], v0 offset:12288\n"
      "v_pk_max_i16 v36, v36, 0\n"
      "v_mfma_f32_16x16x32_bf16 v[204:207], a[128:131], v[82:85], 0\n"
      "v_cvt_pk_bf16_f32 v42, v39, v43\n"
      "s_waitcnt lgkmcnt(1)\n"
      "v_mfma_f32_16x16x32_bf16 v[204:207], a[132:135], v[164:167], v[204:207]\n"
      "v_mfma_f32_16x16x32_bf16 v[204:207], a[136:139], v[124:127], v[204:207]\n"
      "v_mfma_f32_16x16x32_bf16 v[204:207], a[140:143], v[252:255], v[204:207]\n"
      "v_exp_f32 v148, v148\n"
      "v_mfma_f32_16x16x32_bf16 v[248:251], a[32:35], v[200:203], 0\n"
      "v_exp_f32 v152, v152\n"
      "v_mfma_f32_16x16x32_bf16 v[248:251], a[36:39], v[90:93], v[248:251]\n"
      "v_mfma_f32_16x16x32_bf16 v[248:251], a[40:43], v[86:89], v[248:251]\n"
      "v_exp_f32 v149, v149\n"
      "v_mfma_f32_16x16x32_bf16 v[248:251], a[44:47], v[176:179], v[248:251]\n"
      "v_cvt_pk_bf16_f32 v43, v47, v51\n"
      "v_cvt_pk_bf16_f32 v40, v37, v41\n"
      "v_mfma_f32_16x16x32_bf16 v[26:29], v[200:203], a[64:67], 0\n"
      "v_cvt_pk_bf16_f32 v41, v45, v49\n"
      "v_cvt_pk_bf16_f32 v39, v46, v50\n"
      "v_mfma_f32_16x16x32_bf16 v[26:29], v[90:93], a[68:71], v[26:29]\n"
      "v_mfma_f32_16x16x32_bf16 v[26:29], v[86:89], a[72:75], v[26:29]\n"
      "ds_read_b128 v[22:25], v0 offset:17408\n"
      "v_mfma_f32_16x16x32_bf16 v[26:29], v[176:179], a[76:79], v[26:29]\n"
      "v_exp_f32 v158, v158\n"
      "v_mfma_f32_16x16x32_bf16 v[78:81], a[144:147], v[82:85], 0\n"
      "v_exp_f32 v96, v96\n"
      "v_mfma_f32_16x16x32_bf16 v[78:81], a[148:151], v[164:167], v[78:81]\n"
      "v_exp_f32 v159, v159\n"
      "v_mfma_f32_16x16x32_bf16 v[78:81], a[152:155], v[124:127], v[78:81]\n"
      "v_exp_f32 v170, v170\n"
      "v_mfma_f32_16x16x32_bf16 v[78:81], a[156:159], v[252:255], v[78:81]\n"
      "v_exp_f32 v244, v244\n"
      "v_mfma_f32_16x16x32_bf16 v[196:199], a[160:163], v[82:85], 0\n"
      "v_cvt_pk_bf16_f32 v101, v244, v245\n"
      "v_cvt_pk_bf16_f32 v37, v44, v48\n"
      "v_mfma_f32_16x16x32_bf16 v[196:199], a[164:167], v[164:167], v[196:199]\n"
      "v_exp_f32 v157, v157\n"
      "v_mfma_f32_16x16x32_bf16 v[196:199], a[168:171], v[124:127], v[196:199]\n"
      "v_exp_f32 v98, v98\n"
      "v_mfma_f32_16x16x32_bf16 v[196:199], a[172:175], v[252:255], v[196:199]\n"
      "v_cvt_pk_bf16_f32 v94, v96, v97\n"
      "v_mfma_f32_16x16x32_bf16 v[240:243], a[0:3], v[200:203], 0\n"
      "v_cvt_pk_bf16_f32 v97, v158, v159\n"
      "v_mfma_f32_16x16x32_bf16 v[240:243], a[4:7], v[90:93], v[240:243]\n"
      "v_exp_f32 v99, v99\n"
      "v_mfma_f32_16x16x32_bf16 v[240:243], a[8:11], v[86:89], v[240:243]\n"
      "v_exp_f32 v154, v154\n"
      "v_mfma_f32_16x16x32_bf16 v[240:243], a[12:15], v[176:179], v[240:243]\n"
      "v_cvt_pk_bf16_f32 v136, v26, v27\n"
      "v_mfma_f32_16x16x32_bf16 v[160:163], v[200:203], a[96:99], 0\n"
      "v_cvt_pk_bf16_f32 v95, v98, v99\n"
      "v_mfma_f32_16x16x32_bf16 v[160:163], v[90:93], a[100:103], v[160:163]\n"
      "v_mfma_f32_16x16x32_bf16 v[160:163], v[86:89], a[104:107], v[160:163]\n"
      "v_exp_f32 v169, v169\n"
      "v_mfma_f32_16x16x32_bf16 v[160:163], v[176:179], a[108:111], v[160:163]\n"
      "v_pk_max_i16 v42, v42, 0\n"
      "v_cvt_pk_bf16_f32 v96, v156, v157\n"
      "v_mfma_f32_16x16x32_bf16 v[46:49], v[200:203], a[112:115], 0\n"
      "v_exp_f32 v171, v171\n"
      "v_mfma_f32_16x16x32_bf16 v[46:49], v[90:93], a[116:119], v[46:49]\n"
      "v_exp_f32 v153, v153\n"
      "v_mfma_f32_16x16x32_bf16 v[46:49], v[86:89], a[120:123], v[46:49]\n"
      "ds_read_b128 v[90:93], v0 offset:17920\n"
      "v_mfma_f32_16x16x32_bf16 v[46:49], v[176:179], a[124:127], v[46:49]\n"
      "buffer_store_dwordx2 v[194:195], v2, s[44:47], s101 offen offset:768 nt sc1\n"
      "v_cvt_pk_bf16_f32 v137, v28, v29\n"
      "v_mfma_f32_16x16x32_bf16 v[200:203], a[0:3], v[82:85], 0\n"
      "v_mfma_f32_16x16x32_bf16 v[200:203], a[4:7], v[164:167], v[200:203]\n"
      "v_pk_max_i16 v39, v39, 0\n"
      "v_mfma_f32_16x16x32_bf16 v[200:203], a[8:11], v[124:127], v[200:203]\n"
      "v_cvt_pk_bf16_f32 v157, v80, v81\n"
      "v_mfma_f32_16x16x32_bf16 v[200:203], a[12:15], v[252:255], v[200:203]\n"
      "v_pk_max_i16 v37, v37, 0\n"
      "v_cvt_pk_bf16_f32 v44, v74, v75\n"
      "v_mfma_f32_16x16x32_bf16 v[26:29], a[48:51], v[82:85], 0\n"
      "v_cvt_pk_bf16_f32 v98, v168, v169\n"
      "v_cvt_pk_bf16_f32 v89, v150, v151\n"
      "v_mfma_f32_16x16x32_bf16 v[26:29], a[52:55], v[164:167], v[26:29]\n"
      "v_cvt_pk_bf16_f32 v65, v250, v251\n"
      "v_mfma_f32_16x16x32_bf16 v[26:29], a[56:59], v[124:127], v[26:29]\n"
      "v_cvt_pk_bf16_f32 v99, v170, v171\n"
      "v_mfma_f32_16x16x32_bf16 v[26:29], a[60:63], v[252:255], v[26:29]\n"
      "v_cvt_pk_bf16_f32 v87, v154, v155\n"
      "v_smfmac_f32_16x16x64_bf16 v[128:131], v[12:15], v[94:101], v5\n"
      "v_cvt_pk_bf16_f32 v86, v152, v153\n"
      "v_mfma_f32_16x16x32_bf16 v[220:223], v[94:97], v[56:59], v[220:223]\n"
      "v_cvt_pk_bf16_f32 v88, v148, v149\n"
      "v_mfma_f32_16x16x32_bf16 v[220:223], v[98:101], v[60:63], v[220:223]\n"
      "s_nop 0\n"
      "v_mfma_f32_16x16x32_bf16 v[220:223], v[86:89], v[116:119], v[220:223]\n"
      "s_waitcnt vmcnt(10)\n"
      "ds_read_b128 v[192:195], v0 offset:4096\n"
      "ds_read_b128 v[244:247], v0 offset:5120\n"
      "v_smfmac_f32_16x16x32_bf16 v[128:131], v[12:13], v[86:89], v5\n"
      "buffer_store_dwordx2 v[190:191], v2, s[44:47], s101 offen offset:512 nt sc1\n"
      "v_mfma_f32_16x16x32_bf16 v[72:75], a[16:19], v[82:85], 0\n"
      "ds_read_b128 v[148:151], v0 offset:5632\n"
      "v_mfma_f32_16x16x32_bf16 v[72:75], a[20:23], v[164:167], v[72:75]\n"
      "ds_read_b128 v[168:171], v0 offset:16896\n"
      "v_mfma_f32_16x16x32_bf16 v[72:75], a[24:27], v[124:127], v[72:75]\n"
      "v_mfma_f32_16x16x32_bf16 v[72:75], a[28:31], v[252:255], v[72:75]\n"
      "s_nop 0\n"
      "v_add_f32 v128, v128, v129\n"
      "v_mfma_f32_16x16x32_bf16 v[216:219], v[94:97], v[180:183], v[216:219]\n"
      "ds_read_b128 v[56:59], v10 offset:0\n"
      "v_cvt_pk_bf16_f32 v61, v242, v243\n"
      "v_mfma_f32_16x16x32_bf16 v[216:219], v[98:101], v[184:187], v[216:219]\n"
      "v_rcp_f32 v129, v128\n"
      "v_mfma_f32_16x16x32_bf16 v[216:219], v[86:89], v[120:123], v[216:219]\n"
      "ds_bpermute_b32 v130, v4, v129 offset:128\n"
      "v_mfma_f32_16x16x32_bf16 v[224:227], v[94:97], v[232:235], v[224:227]\n"
      "ds_read_b128 v[232:235], v9 offset:0\n"
      "v_cvt_pk_bf16_f32 v154, v204, v205\n"
      "v_mfma_f32_16x16x32_bf16 v[224:227], v[98:101], v[236:239], v[224:227]\n"
      "ds_read_b128 v[178:181], v0 offset:13312\n"
      "v_mfma_f32_16x16x32_bf16 v[224:227], v[86:89], v[228:231], v[224:227]\n"
      "buffer_store_dwordx2 v[36:37], v2, s[44:47], s34 offen offset:0 nt sc1\n"
      "v_pk_max_i16 v38, v38, 0\n"
      "v_mfma_f32_16x16x32_bf16 v[212:215], v[94:97], v[108:111], v[212:215]\n"
      "v_mfma_f32_16x16x32_bf16 v[212:215], v[98:101], v[112:115], v[212:215]\n"
      "v_cvt_pk_bf16_f32 v94, v200, v201\n"
      "v_mfma_f32_16x16x32_bf16 v[212:215], v[86:89], v[52:55], v[212:215]\n"
      "v_mov_b64 v[110:111], 0\n"
      "v_mfma_f32_16x16x32_bf16 v[228:231], a[48:51], v[68:71], 0\n"
      "s_waitcnt lgkmcnt(4)\n"
      "v_mfma_f32_16x16x32_bf16 v[228:231], a[52:55], v[168:171], v[228:231]\n"
      "v_maximum3_f32 v8, v8, v128, v129\n"
      "v_mfma_f32_16x16x32_bf16 v[228:231], a[56:59], v[22:25], v[228:231]\n"
      "s_waitcnt lgkmcnt(2)\n"
      "v_mul_f32_dpp v217, v130, v217 row_newbcast:4 row_mask:0xf bank_mask:0xf\n"
      "v_cvt_pk_bf16_f32 v100, v26, v27\n"
      "v_mfma_f32_16x16x32_bf16 v[228:231], a[60:63], v[90:93], v[228:231]\n"
      "buffer_store_dwordx2 v[38:39], v2, s[44:47], s34 offen offset:512 nt sc1\n"
      "v_mul_f32_dpp v224, v130, v224 row_newbcast:0 row_mask:0xf bank_mask:0xf\n"
      "v_mfma_f32_16x16x32_bf16 v[184:187], a[176:179], v[82:85], 0\n"
      "v_mfma_f32_16x16x32_bf16 v[184:187], a[180:183], v[164:167], v[184:187]\n"
      "v_cvt_pk_bf16_f32 v67, v142, v143\n"
      "v_mfma_f32_16x16x32_bf16 v[184:187], a[184:187], v[124:127], v[184:187]\n"
      "ds_read_b128 v[236:239], v9 offset:2048\n"
      "v_mfma_f32_16x16x32_bf16 v[184:187], a[188:191], v[252:255], v[184:187]\n"
      "v_mul_f32_dpp v215, v130, v215 row_newbcast:12 row_mask:0xf bank_mask:0xf\n"
      "v_cvt_pk_bf16_f32 v101, v28, v29\n"
      "v_mfma_f32_16x16x32_bf16 v[120:123], a[0:3], v[68:71], 0\n"
      "v_mul_f32_dpp v213, v130, v213 row_newbcast:4 row_mask:0xf bank_mask:0xf\n"
      "v_mfma_f32_16x16x32_bf16 v[120:123], a[4:7], v[168:171], v[120:123]\n"
      "v_mul_f32_dpp v214, v130, v214 row_newbcast:8 row_mask:0xf bank_mask:0xf\n"
      "v_mul_f32_dpp v227, v130, v227 row_newbcast:12 row_mask:0xf bank_mask:0xf\n"
      "v_mfma_f32_16x16x32_bf16 v[120:123], a[8:11], v[22:25], v[120:123]\n"
      "v_mfma_f32_16x16x32_bf16 v[120:123], a[12:15], v[90:93], v[120:123]\n"
      "v_mul_f32_dpp v221, v130, v221 row_newbcast:4 row_mask:0xf bank_mask:0xf\n"
      "v_mul_f32_dpp v216, v130, v216 row_newbcast:0 row_mask:0xf bank_mask:0xf\n"
      "v_mfma_f32_16x16x32_bf16 v[188:191], a[32:35], v[82:85], 0\n"
      "v_cvt_pk_bf16_f32 v53, v48, v49\n"
      "v_mul_f32_dpp v218, v130, v218 row_newbcast:8 row_mask:0xf bank_mask:0xf\n"
      "v_mfma_f32_16x16x32_bf16 v[188:191], a[36:39], v[164:167], v[188:191]\n"
      "v_cvt_pk_bf16_f32 v45, v76, v77\n"
      "v_cvt_pk_bf16_f32 v62, v208, v209\n"
      "v_mfma_f32_16x16x32_bf16 v[188:191], a[40:43], v[124:127], v[188:191]\n"
      "v_mul_f32_dpp v212, v130, v212 row_newbcast:0 row_mask:0xf bank_mask:0xf\n"
      "v_mfma_f32_16x16x32_bf16 v[188:191], a[44:47], v[252:255], v[188:191]\n"
      "v_mul_f32_dpp v226, v130, v226 row_newbcast:8 row_mask:0xf bank_mask:0xf\n"
      "s_waitcnt lgkmcnt(0)\n"
      "v_smfmac_f32_16x16x64_bf16 v[110:113], v[56:59], v[232:239], v5\n"
      "buffer_store_dwordx2 v[30:31], v2, s[44:47], s100 offen offset:512 nt sc1\n"
      "ds_read_b128 v[48:51], v0 offset:4608\n"
      "v_mfma_f32_16x16x32_bf16 v[212:215], v[144:147], a[192:195], v[212:215]\n"
      "v_cvt_pk_bf16_f32 v95, v202, v203\n"
      "v_pk_max_i16 v41, v41, 0\n"
      "v_mfma_f32_16x16x32_bf16 v[212:215], v[104:107], a[196:199], v[212:215]\n"
      "v_mul_f32_dpp v219, v130, v219 row_newbcast:12 row_mask:0xf bank_mask:0xf\n"
      "v_mul_f32_dpp v223, v130, v223 row_newbcast:12 row_mask:0xf bank_mask:0xf\n"
      "v_mfma_f32_16x16x32_bf16 v[212:215], v[172:175], a[200:203], v[212:215]\n"
      "v_cvt_pk_bf16_f32 v66, v140, v141\n"
      "v_mfma_f32_16x16x32_bf16 v[212:215], v[132:135], a[204:207], v[212:215]\n"
      "v_add_f32 v110, v110, v111\n"
      "v_mov_b32 v111, 0\n"
      "v_mfma_f32_16x16x32_bf16 v[216:219], v[144:147], a[208:211], v[216:219]\n"
      "v_cvt_pk_bf16_f32 v156, v78, v79\n"
      "v_pk_max_i16 v43, v43, 0\n"
      "v_mfma_f32_16x16x32_bf16 v[216:219], v[104:107], a[212:215], v[216:219]\n"
      "v_mul_f32_dpp v225, v130, v225 row_newbcast:4 row_mask:0xf bank_mask:0xf\n"
      "v_mfma_f32_16x16x32_bf16 v[216:219], v[172:175], a[216:219], v[216:219]\n"
      "v_mfma_f32_16x16x32_bf16 v[216:219], v[132:135], a[220:223], v[216:219]\n"
      "v_mfma_f32_16x16x32_bf16 v[112:115], a[16:19], v[68:71], 0\n"
      "v_cvt_pk_bf16_f32 v155, v206, v207\n"
      "v_cvt_pk_bf16_f32 v239, v230, v231\n"
      "v_mfma_f32_16x16x32_bf16 v[112:115], a[20:23], v[168:171], v[112:115]\n"
      "v_mul_f32_dpp v220, v130, v220 row_newbcast:0 row_mask:0xf bank_mask:0xf\n"
      "v_pk_max_i16 v40, v40, 0\n"
      "v_mfma_f32_16x16x32_bf16 v[112:115], a[24:27], v[22:25], v[112:115]\n"
      "v_cvt_pk_bf16_f32 v36, v160, v161\n"
      "v_mfma_f32_16x16x32_bf16 v[112:115], a[28:31], v[90:93], v[112:115]\n"
      "v_mfma_f32_16x16x32_bf16 v[204:207], v[192:195], a[112:115], 0\n"
      "s_waitcnt lgkmcnt(0)\n"
      "v_mfma_f32_16x16x32_bf16 v[204:207], v[48:51], a[116:119], v[204:207]\n"
      "v_cvt_pk_bf16_f32 v64, v248, v249\n"
      "v_mfma_f32_16x16x32_bf16 v[204:207], v[244:247], a[120:123], v[204:207]\n"
      "v_cvt_pk_bf16_f32 v161, v186, v187\n"
      "v_mfma_f32_16x16x32_bf16 v[204:207], v[148:151], a[124:127], v[204:207]\n"
      "v_cvt_pk_bf16_f32 v214, v214, v218\n"
      "v_mfma_f32_16x16x32_bf16 v[78:81], a[32:35], v[192:195], 0\n"
      "v_cvt_pk_bf16_f32 v212, v212, v216\n"
      "v_mfma_f32_16x16x32_bf16 v[78:81], a[36:39], v[48:51], v[78:81]\n"
      "v_mfma_f32_16x16x32_bf16 v[78:81], a[40:43], v[244:247], v[78:81]\n"
      "v_cvt_pk_bf16_f32 v216, v213, v217\n"
      "v_mfma_f32_16x16x32_bf16 v[78:81], a[44:47], v[148:151], v[78:81]\n"
      "v_cvt_pk_bf16_f32 v96, v72, v73\n"
      "v_cvt_pk_bf16_f32 v238, v228, v229\n"
      "v_mfma_f32_16x16x32_bf16 v[224:227], v[144:147], a[240:243], v[224:227]\n"
      "v_mul_f32_dpp v222, v130, v222 row_newbcast:8 row_mask:0xf bank_mask:0xf\n"
      "v_mfma_f32_16x16x32_bf16 v[224:227], v[104:107], a[244:247], v[224:227]\n"
      "v_mfma_f32_16x16x32_bf16 v[224:227], v[172:175], a[248:251], v[224:227]\n"
      "v_mfma_f32_16x16x32_bf16 v[224:227], v[132:135], a[252:255], v[224:227]\n"
      "v_cvt_pk_bf16_f32 v158, v196, v197\n"
      "v_cvt_pk_bf16_f32 v99, v190, v191\n"
      "v_mfma_f32_16x16x32_bf16 v[220:223], v[144:147], a[224:227], v[220:223]\n"
      "v_mfma_f32_16x16x32_bf16 v[220:223], v[104:107], a[228:231], v[220:223]\n"
      "v_cvt_pk_bf16_f32 v233, v122, v123\n"
      "v_mfma_f32_16x16x32_bf16 v[220:223], v[172:175], a[232:235], v[220:223]\n"
      "v_mfma_f32_16x16x32_bf16 v[220:223], v[132:135], a[236:239], v[220:223]\n"
      "v_exp_f32_dpp v111, v110 quad_perm:[0,1,2,3] row_mask:0xf bank_mask:0x1\n"
      "v_mfma_f32_16x16x32_bf16 v[104:107], a[48:51], v[192:195], 0\n"
      "v_mfma_f32_16x16x32_bf16 v[104:107], a[52:55], v[48:51], v[104:107]\n"
      "v_mfma_f32_16x16x32_bf16 v[104:107], a[56:59], v[244:247], v[104:107]\n"
      "v_cvt_pk_bf16_f32 v97, v74, v75\n"
      "v_cvt_pk_bf16_f32 v160, v184, v185\n"
      "v_mfma_f32_16x16x32_bf16 v[104:107], a[60:63], v[148:151], v[104:107]\n"
      "buffer_store_dwordx2 v[40:41], v2, s[44:47], s34 offen offset:256 nt sc1\n"
      "v_cvt_pk_bf16_f32 v235, v114, v115\n"
      "v_mfma_f32_16x16x32_bf16 v[132:135], a[32:35], v[68:71], 0\n"
      "v_mov_b32_dpp v110, v111 quad_perm:[1,1,3,3] row_mask:0xf bank_mask:0xf\n"
      "v_cvt_pk_bf16_f32 v213, v220, v224\n"
      "v_mfma_f32_16x16x32_bf16 v[132:135], a[36:39], v[168:171], v[132:135]\n"
      "v_cvt_pk_bf16_f32 v218, v215, v219\n"
      "v_cvt_pk_bf16_f32 v215, v222, v226\n"
      "v_mfma_f32_16x16x32_bf16 v[132:135], a[40:43], v[22:25], v[132:135]\n"
      "v_pk_max_i16 v212, v212, 0\n"
      "v_pk_max_i16 v214, v214, 0\n"
      "v_mfma_f32_16x16x32_bf16 v[132:135], a[44:47], v[90:93], v[132:135]\n"
      "ds_read_b128 v[174:177], v0 offset:12800\n"
      "ds_read_b128 v[26:29], v0 offset:13824\n"
      "v_mfma_f32_16x16x32_bf16 v[248:251], a[16:19], v[192:195], 0\n"
      "v_cvt_pk_bf16_f32 v234, v112, v113\n"
      "v_mov_b64 v[38:39], 0\n"
      "v_mfma_f32_16x16x32_bf16 v[248:251], a[20:23], v[48:51], v[248:251]\n"
      "v_cvt_pk_bf16_f32 v232, v120, v121\n"
      "v_cvt_pk_bf16_f32 v159, v198, v199\n"
      "v_mfma_f32_16x16x32_bf16 v[248:251], a[24:27], v[244:247], v[248:251]\n"
      "v_cvt_pk_bf16_f32 v219, v223, v227\n"
      "v_mov_b64 v[112:113], 0\n"
      "v_mfma_f32_16x16x32_bf16 v[248:251], a[28:31], v[148:151], v[248:251]\n"
      "v_cvt_pk_bf16_f32 v236, v132, v133\n"
      "v_pk_max_i16 v213, v213, 0\n"
      "v_mfma_f32_16x16x32_bf16 v[72:75], v[192:195], a[80:83], 0\n"
      "v_cvt_pk_bf16_f32 v237, v134, v135\n"
      "v_cvt_pk_bf16_f32 v110, v111, v110\n"
      "v_mfma_f32_16x16x32_bf16 v[72:75], v[48:51], a[84:87], v[72:75]\n"
      "v_cvt_pk_bf16_f32 v230, v104, v105\n"
      "v_mfma_f32_16x16x32_bf16 v[72:75], v[244:247], a[88:91], v[72:75]\n"
      "v_pk_max_i16 v215, v215, 0\n"
      "v_cvt_pk_bf16_f32 v60, v240, v241\n"
      "v_mfma_f32_16x16x32_bf16 v[72:75], v[148:151], a[92:95], v[72:75]\n"
      "v_pk_max_i16 v219, v219, 0\n"
      "v_cvt_pk_bf16_f32 v231, v106, v107\n"
      "v_mfma_f32_16x16x32_bf16 v[196:199], a[0:3], v[192:195], 0\n"
      "v_mfma_f32_16x16x32_bf16 v[196:199], a[4:7], v[48:51], v[196:199]\n"
      "v_cvt_pk_bf16_f32 v63, v210, v211\n"
      "v_mfma_f32_16x16x32_bf16 v[196:199], a[8:11], v[244:247], v[196:199]\n"
      "v_mov_b64 v[104:105], 0\n"
      "v_mfma_f32_16x16x32_bf16 v[196:199], a[12:15], v[148:151], v[196:199]\n"
      "v_cvt_pk_bf16_f32 v98, v188, v189\n"
      "v_cvt_pk_bf16_f32 v52, v46, v47\n"
      "v_smfmac_f32_16x16x64_bf16 v[112:115], v[56:59], v[232:239], v5\n"
      "s_waitcnt vmcnt(9)\n"
      "ds_read_b128 v[106:109], v0 offset:0\n"
      "ds_read_b128 v[144:147], v0 offset:1024\n"
      "v_mfma_f32_16x16x32_bf16 v[120:123], v[192:195], a[96:99], 0\n"
      "v_cvt_pk_bf16_f32 v217, v221, v225\n"
      "v_mov_b64 v[46:47], 0\n"
      "v_mfma_f32_16x16x32_bf16 v[120:123], v[48:51], a[100:103], v[120:123]\n"
      "v_mfma_f32_16x16x32_bf16 v[120:123], v[244:247], a[104:107], v[120:123]\n"
      "ds_read_b128 v[208:211], v0 offset:1536\n"
      "v_mfma_f32_16x16x32_bf16 v[120:123], v[148:151], a[108:111], v[120:123]\n"
      "buffer_store_dwordx2 v[214:215], v2, s[44:47], s35 offen offset:512 nt sc1\n"
      "v_mfma_f32_16x16x32_bf16 v[140:143], v[232:235], v[154:157], 0\n"
      "v_mfma_f32_16x16x32_bf16 v[140:143], v[236:239], v[158:161], v[140:143]\n"
      "buffer_store_dwordx2 v[34:35], v2, s[44:47], s100 offen offset:768 nt sc1\n"
      "ds_read_b128 v[188:191], v0 offset:512\n"
      "v_mfma_f32_16x16x32_bf16 v[184:187], v[94:97], v[154:157], 0\n"
      "v_mfma_f32_16x16x32_bf16 v[184:187], v[98:101], v[158:161], v[184:187]\n"
      "v_cvt_pk_bf16_f32 v225, v198, v199\n"
      "v_smfmac_f32_16x16x64_bf16 v[38:41], v[56:59], v[94:101], v5\n"
      "buffer_store_dwordx2 v[42:43], v2, s[44:47], s34 offen offset:768 nt sc1\n"
      "v_mfma_f32_16x16x32_bf16 v[132:135], a[16:19], v[18:21], 0\n"
      "s_waitcnt lgkmcnt(5)\n"
      "v_mfma_f32_16x16x32_bf16 v[132:135], a[20:23], v[174:177], v[132:135]\n"
      "v_exp_f32 v140, v140\n"
      "v_mfma_f32_16x16x32_bf16 v[132:135], a[24:27], v[178:181], v[132:135]\n"
      "s_waitcnt lgkmcnt(4)\n"
      "v_mfma_f32_16x16x32_bf16 v[132:135], a[28:31], v[26:29], v[132:135]\n"
      "buffer_store_dwordx2 v[32:33], v2, s[44:47], s100 offen offset:256 nt sc1\n"
      "v_cvt_pk_bf16_f32 v43, v74, v75\n"
      "v_mfma_f32_16x16x32_bf16 v[200:203], v[192:195], a[64:67], 0\n"
      "v_exp_f32 v186, v186\n"
      "v_mfma_f32_16x16x32_bf16 v[200:203], v[48:51], a[68:71], v[200:203]\n"
      "v_mfma_f32_16x16x32_bf16 v[200:203], v[244:247], a[72:75], v[200:203]\n"
      "v_cvt_pk_bf16_f32 v227, v250, v251\n"
      "v_cvt_pk_bf16_f32 v50, v204, v205\n"
      "v_mfma_f32_16x16x32_bf16 v[200:203], v[148:151], a[76:79], v[200:203]\n"
      "v_cvt_pk_bf16_f32 v204, v132, v133\n"
      "s_waitcnt lgkmcnt(3)\n"
      "v_mfma_f32_16x16x32_bf16 v[244:247], v[106:109], a[80:83], 0\n"
      "v_cvt_pk_bf16_f32 v226, v248, v249\n"
      "s_waitcnt lgkmcnt(0)\n"
      "v_mfma_f32_16x16x32_bf16 v[244:247], v[188:191], a[84:87], v[244:247]\n"
      "v_mfma_f32_16x16x32_bf16 v[244:247], v[144:147], a[88:91], v[244:247]\n"
      "v_cvt_pk_bf16_f32 v205, v134, v135\n"
      "v_mfma_f32_16x16x32_bf16 v[244:247], v[208:211], a[92:95], v[244:247]\n"
      "v_exp_f32 v185, v185\n"
      "v_mfma_f32_16x16x32_bf16 v[248:251], a[128:131], v[68:71], 0\n"
      "v_exp_f32 v143, v143\n"
      "v_mfma_f32_16x16x32_bf16 v[248:251], a[132:135], v[168:171], v[248:251]\n"
      "v_mfma_f32_16x16x32_bf16 v[248:251], a[136:139], v[22:25], v[248:251]\n"
      "v_exp_f32 v184, v184\n"
      "v_mfma_f32_16x16x32_bf16 v[248:251], a[140:143], v[90:93], v[248:251]\n"
      "v_cvt_pk_bf16_f32 v229, v80, v81\n"
      "v_add_f32 v38, v38, v39\n"
      "v_mfma_f32_16x16x32_bf16 v[116:119], a[48:51], v[106:109], 0\n"
      "v_cvt_pk_bf16_f32 v135, v202, v203\n"
      "v_cvt_pk_bf16_f32 v51, v206, v207\n"
      "v_mfma_f32_16x16x32_bf16 v[116:119], a[52:55], v[188:191], v[116:119]\n"
      "v_exp_f32 v141, v141\n"
      "v_mfma_f32_16x16x32_bf16 v[116:119], a[56:59], v[144:147], v[116:119]\n"
      "v_mfma_f32_16x16x32_bf16 v[116:119], a[60:63], v[208:211], v[116:119]\n"
      "v_add_f32 v112, v112, v113\n"
      "v_cvt_pk_bf16_f32 v224, v196, v197\n"
      "v_mfma_f32_16x16x32_bf16 v[192:195], a[0:3], v[18:21], 0\n"
      "v_mfma_f32_16x16x32_bf16 v[192:195], a[4:7], v[174:177], v[192:195]\n"
      "v_exp_f32 v112, v112\n"
      "v_mfma_f32_16x16x32_bf16 v[192:195], a[8:11], v[178:181], v[192:195]\n"
      "s_nop 1\n"
      "v_cvt_pk_bf16_f32 v197, v118, v119\n"
      "v_mfma_f32_16x16x32_bf16 v[192:195], a[12:15], v[26:29], v[192:195]\n"
      "v_pk_max_i16 v218, v218, 0\n"
      "v_cvt_pk_bf16_f32 v35, v122, v123\n"
      "v_mfma_f32_16x16x32_bf16 v[220:223], a[32:35], v[106:109], 0\n"
      "v_exp_f32 v187, v187\n"
      "v_mfma_f32_16x16x32_bf16 v[220:223], a[36:39], v[188:191], v[220:223]\n"
      "v_exp_f32 v142, v142\n"
      "v_mfma_f32_16x16x32_bf16 v[220:223], a[40:43], v[144:147], v[220:223]\n"
      "v_cvt_pk_bf16_f32 v34, v120, v121\n"
      "v_cvt_pk_bf16_f32 v196, v116, v117\n"
      "v_mfma_f32_16x16x32_bf16 v[220:223], a[44:47], v[208:211], v[220:223]\n"
      "v_cvt_pk_bf16_f32 v134, v200, v201\n"
      "v_mfma_f32_16x16x32_bf16 v[120:123], a[0:3], v[106:109], 0\n"
      "v_cvt_pk_bf16_f32 v37, v162, v163\n"
      "v_mfma_f32_16x16x32_bf16 v[120:123], a[4:7], v[188:191], v[120:123]\n"
      "ds_read_b64 v[30:31], v11 offset:0\n"
      "v_cvt_pk_bf16_f32 v119, v186, v187\n"
      "v_mfma_f32_16x16x32_bf16 v[120:123], a[8:11], v[144:147], v[120:123]\n"
      "v_cvt_pk_bf16_f32 v42, v72, v73\n"
      "v_mfma_f32_16x16x32_bf16 v[120:123], a[12:15], v[208:211], v[120:123]\n"
      "v_cvt_pk_bf16_f32 v118, v184, v185\n"
      "v_mfma_f32_16x16x32_bf16 v[148:151], v[106:109], a[64:67], 0\n"
      "v_pk_max_i16 v216, v216, 0\n"
      "v_cvt_pk_bf16_f32 v41, v246, v247\n"
      "v_mfma_f32_16x16x32_bf16 v[148:151], v[188:191], a[68:71], v[148:151]\n"
      "v_cvt_pk_bf16_f32 v228, v78, v79\n"
      "v_cvt_pk_bf16_f32 v203, v194, v195\n"
      "v_mfma_f32_16x16x32_bf16 v[148:151], v[144:147], a[72:75], v[148:151]\n"
      "v_mov_b64 v[102:103], 0\n"
      "v_cvt_pk_bf16_f32 v202, v192, v193\n"
      "v_mfma_f32_16x16x32_bf16 v[148:151], v[208:211], a[76:79], v[148:151]\n"
      "ds_read2_b32 v[198:199], v16 offset0:0 offset1:2\n"
      "v_smfmac_f32_16x16x64_bf16 v[46:49], v[56:59], v[60:67], v5\n"
      "buffer_store_dwordx2 v[212:213], v2, s[44:47], s35 offen offset:0 nt sc1\n"
      "ds_read2_b32 v[200:201], v16 offset0:128 offset1:130\n"
      "v_mfma_f32_16x16x32_bf16 v[72:75], a[16:19], v[106:109], 0\n"
      "v_exp_f32 v38, v38\n"
      "v_mfma_f32_16x16x32_bf16 v[72:75], a[20:23], v[188:191], v[72:75]\n"
      "v_cvt_pk_bf16_f32 v40, v244, v245\n"
      "v_mfma_f32_16x16x32_bf16 v[72:75], a[24:27], v[144:147], v[72:75]\n"
      "v_mfma_f32_16x16x32_bf16 v[72:75], a[28:31], v[208:211], v[72:75]\n"
      "v_pk_max_i16 v217, v217, 0\n"
      "v_cvt_pk_bf16_f32 v194, v220, v221\n"
      "v_mfma_f32_16x16x32_bf16 v[128:131], v[106:109], a[96:99], 0\n"
      "v_mov_b64 v[220:221], 0\n"
      "v_add_f32 v46, v46, v47\n"
      "v_mfma_f32_16x16x32_bf16 v[128:131], v[188:191], a[100:103], v[128:131]\n"
      "v_mfma_f32_16x16x32_bf16 v[128:131], v[144:147], a[104:107], v[128:131]\n"
      "v_cvt_pk_bf16_f32 v117, v142, v143\n"
      "v_cvt_pk_bf16_f32 v195, v222, v223\n"
      "v_mfma_f32_16x16x32_bf16 v[128:131], v[208:211], a[108:111], v[128:131]\n"
      "buffer_store_dwordx2 v[216:217], v2, s[44:47], s35 offen offset:256 nt sc1\n"
      "v_cvt_pk_bf16_f32 v132, v148, v149\n"
      "v_mfma_f32_16x16x32_bf16 v[184:187], a[32:35], v[18:21], 0\n"
      "v_mfma_f32_16x16x32_bf16 v[184:187], a[36:39], v[174:177], v[184:187]\n"
      "v_cvt_pk_bf16_f32 v244, v248, v249\n"
      "v_mfma_f32_16x16x32_bf16 v[184:187], a[40:43], v[178:181], v[184:187]\n"
      "v_cndmask_b32_dpp v113, v38, v112, vcc quad_perm:[0,0,2,2] row_mask:0xf bank_mask:0xf\n"
      "v_mfma_f32_16x16x32_bf16 v[184:187], a[44:47], v[26:29], v[184:187]\n"
      "v_cvt_pk_bf16_f32 v245, v250, v251\n"
      "v_cvt_pk_bf16_f32 v192, v72, v73\n"
      "v_smfmac_f32_16x16x64_bf16 v[220:223], v[56:59], v[224:231], v5\n"
      "ds_bpermute_b32 v80, v7, v110 offset:248\n"
      "v_mfma_f32_16x16x32_bf16 v[212:215], v[106:109], a[112:115], 0\n"
      "v_cvt_pk_bf16_f32 v32, v128, v129\n"
      "v_mfma_f32_16x16x32_bf16 v[212:215], v[188:191], a[116:119], v[212:215]\n"
      "v_mfma_f32_16x16x32_bf16 v[212:215], v[144:147], a[120:123], v[212:215]\n"
      "s_waitcnt lgkmcnt(2)\n"
      "v_mov_b32_dpp v241, v199 row_ror:8 row_mask:0xf bank_mask:0xf\n"
      "v_mov_b64 v[72:73], 0\n"
      "v_mfma_f32_16x16x32_bf16 v[212:215], v[208:211], a[124:127], v[212:215]\n"
      "s_waitcnt lgkmcnt(1)\n"
      "v_mov_b32_dpp v243, v201 row_ror:8 row_mask:0xf bank_mask:0xf\n"
      "v_mov_b64 v[216:217], 0\n"
      "v_mfma_f32_16x16x32_bf16 v[76:79], a[144:147], v[68:71], 0\n"
      "v_cvt_pk_bf16_f32 v191, v122, v123\n"
      "v_cvt_pk_bf16_f32 v190, v120, v121\n"
      "v_mfma_f32_16x16x32_bf16 v[76:79], a[148:151], v[168:171], v[76:79]\n"
      "v_mov_b32_dpp v240, v198 row_ror:8 row_mask:0xf bank_mask:0xf\n"
      "v_mfma_f32_16x16x32_bf16 v[76:79], a[152:155], v[22:25], v[76:79]\n"
      "v_cvt_pk_bf16_f32 v116, v140, v141\n"
      "v_cvt_pk_bf16_f32 v193, v74, v75\n"
      "v_mfma_f32_16x16x32_bf16 v[76:79], a[156:159], v[90:93], v[76:79]\n"
      "v_add_f32 v220, v220, v221\n"
      "v_smfmac_f32_16x16x64_bf16 v[72:75], v[56:59], v[190:197], v5\n"
      "v_cvt_pk_bf16_f32 v49, v214, v215\n"
      "v_mfma_f32_16x16x32_bf16 v[140:143], a[48:51], v[18:21], 0\n"
      "v_mov_b32_dpp v242, v200 row_ror:8 row_mask:0xf bank_mask:0xf\n"
      "v_cvt_pk_bf16_f32 v48, v212, v213\n"
      "v_mfma_f32_16x16x32_bf16 v[140:143], a[52:55], v[174:177], v[140:143]\n"
      "ds_bpermute_b32 v81, v7, v110 offset:8\n"
      "v_mfma_f32_16x16x32_bf16 v[140:143], a[56:59], v[178:181], v[140:143]\n"
      "s_nop 0\n"
      "v_add_f32 v72, v72, v73\n"
      "v_cvt_pk_bf16_f32 v207, v186, v187\n"
      "v_mfma_f32_16x16x32_bf16 v[140:143], a[60:63], v[26:29], v[140:143]\n"
      "buffer_store_dwordx2 v[218:219], v2, s[44:47], s35 offen offset:768 nt sc1\n"
      "v_cvt_pk_bf16_f32 v247, v78, v79\n"
      "v_mfma_f32_16x16x32_bf16 v[120:123], a[160:163], v[68:71], 0\n"
      "v_cvt_pk_bf16_f32 v246, v76, v77\n"
      "v_cvt_pk_bf16_f32 v206, v184, v185\n"
      "v_mfma_f32_16x16x32_bf16 v[120:123], a[164:167], v[168:171], v[120:123]\n"
      "v_exp_f32 v72, v72\n"
      "v_mfma_f32_16x16x32_bf16 v[120:123], a[168:171], v[22:25], v[120:123]\n"
      "v_cvt_pk_bf16_f32 v209, v142, v143\n"
      "v_cvt_pk_bf16_f32 v208, v140, v141\n"
      "v_mfma_f32_16x16x32_bf16 v[120:123], a[172:175], v[90:93], v[120:123]\n"
      "v_exp_f32 v46, v46\n"
      "v_smfmac_f32_16x16x64_bf16 v[216:219], v[56:59], v[202:209], v5\n"
      "v_cvt_pk_bf16_f32 v33, v130, v131\n"
      "v_mfma_f32_16x16x32_bf16 v[106:109], v[60:63], v[154:157], 0\n"
      "v_exp_f32 v220, v220\n"
      "v_mfma_f32_16x16x32_bf16 v[106:109], v[64:67], v[158:161], v[106:109]\n"
      "s_nop 1\n"
      "v_cvt_pk_bf16_f32 v248, v120, v121\n"
      "v_mfma_f32_16x16x32_bf16 v[144:147], v[224:227], v[154:157], 0\n"
      "v_cvt_pk_bf16_f32 v249, v122, v123\n"
      "v_add_f32 v216, v216, v217\n"
      "v_mfma_f32_16x16x32_bf16 v[144:147], v[228:231], v[158:161], v[144:147]\n"
      "v_exp_f32 v216, v216\n"
      "v_mfma_f32_16x16x32_bf16 v[120:123], a[176:179], v[68:71], 0\n"
      "s_nop 0\n"
      "v_cndmask_b32_dpp v221, v216, v220, vcc quad_perm:[0,0,2,2] row_mask:0xf bank_mask:0xf\n"
      "v_cndmask_b32_dpp v73, v46, v72, vcc quad_perm:[0,0,2,2] row_mask:0xf bank_mask:0xf\n"
      "v_mfma_f32_16x16x32_bf16 v[120:123], a[180:183], v[168:171], v[120:123]\n"
      "v_exp_f32 v107, v107\n"
      "v_mfma_f32_16x16x32_bf16 v[120:123], a[184:187], v[22:25], v[120:123]\n"
      "s_not_b64 vcc, vcc\n"
      "v_cndmask_b32_dpp v47, v72, v46, vcc quad_perm:[1,1,3,3] row_mask:0xf bank_mask:0xf\n"
      "v_cndmask_b32_dpp v39, v112, v38, vcc quad_perm:[1,1,3,3] row_mask:0xf bank_mask:0xf\n"
      "v_mfma_f32_16x16x32_bf16 v[120:123], a[188:191], v[90:93], v[120:123]\n"
      "v_exp_f32 v144, v144\n"
      "v_mfma_f32_16x16x32_bf16 v[76:79], v[18:21], a[80:83], 0\n"
      "v_add_f32 v72, v72, v220\n"
      "v_cndmask_b32_dpp v217, v220, v216, vcc quad_perm:[1,1,3,3] row_mask:0xf bank_mask:0xf\n"
      "v_mfma_f32_16x16x32_bf16 v[76:79], v[174:177], a[84:87], v[76:79]\n"
      "L_h7_%=:\n"
      "s_cbranch_execz L_hr_%=\n"
      "v_cvt_pk_bf16_f32 v221, v221, v217\n"
      "v_add_f32 v112, v112, v38\n"
      "v_mfma_f32_16x16x32_bf16 v[76:79], v[178:181], a[88:91], v[76:79]\n"
      "v_add_f32 v112, v112, v111\n"
      "v_cvt_pk_bf16_f32 v113, v113, v39\n"
      "v_mfma_f32_16x16x32_bf16 v[76:79], v[26:29], a[92:95], v[76:79]\n"
      "ds_bpermute_b32 v222, v7, v113\n"
      "v_mfma_f32_16x16x32_bf16 v[56:59], v[202:205], v[154:157], 0\n"
      "v_add_f32 v46, v46, v216\n"
      "v_cvt_pk_bf16_f32 v133, v150, v151\n"
      "v_mfma_f32_16x16x32_bf16 v[56:59], v[206:209], v[158:161], v[56:59]\n"
      "v_exp_f32 v109, v109\n"
      "v_mfma_f32_16x16x32_bf16 v[140:143], v[190:193], v[154:157], 0\n"
      "v_cvt_pk_bf16_f32 v73, v73, v47\n"
      "v_cvt_pk_bf16_f32 v251, v122, v123\n"
      "v_mfma_f32_16x16x32_bf16 v[140:143], v[194:197], v[158:161], v[140:143]\n"
      "v_add_f32 v72, v72, v46\n"
      "v_cvt_pk_bf16_f32 v250, v120, v121\n"
      "v_mfma_f32_16x16x32_bf16 v[86:89], v[232:235], v[244:247], 0\n"
      "ds_bpermute_b32 v128, v6, v73\n"
      "v_mfma_f32_16x16x32_bf16 v[86:89], v[236:239], v[248:251], v[86:89]\n"
      "v_exp_f32 v145, v145\n"
      "v_mfma_f32_16x16x32_bf16 v[120:123], v[190:193], v[244:247], 0\n"
      "v_exp_f32 v58, v58\n"
      "v_mfma_f32_16x16x32_bf16 v[120:123], v[194:197], v[248:251], v[120:123]\n"
      "v_exp_f32 v108, v108\n"
      "v_smfmac_f32_16x16x64_bf16 v[102:105], v[198:201], v[154:161], v5\n"
      "ds_bpermute_b32 v131, v6, v221 offset:16\n"
      "v_smfmac_f32_16x16x64_bf16 v[102:105], v[240:243], v[244:251], v5\n"
      "ds_bpermute_b32 v130, v6, v73 offset:16\n"
      "v_mfma_f32_16x16x32_bf16 v[186:189], v[202:205], v[244:247], 0\n"
      "v_cvt_pk_bf16_f32 v47, v78, v79\n"
      "v_cvt_pk_bf16_f32 v219, v108, v109\n"
      "v_mfma_f32_16x16x32_bf16 v[186:189], v[206:209], v[248:251], v[186:189]\n"
      "v_exp_f32 v56, v56\n"
      "v_mfma_f32_16x16x32_bf16 v[108:111], v[224:227], v[244:247], 0\n"
      "v_exp_f32 v86, v86\n"
      "v_mfma_f32_16x16x32_bf16 v[108:111], v[228:231], v[248:251], v[108:111]\n"
      "v_add_f32 v104, v104, v105\n"
      "v_add_f32 v172, v72, v112\n"
      "v_mfma_f32_16x16x32_bf16 v[160:163], v[18:21], a[96:99], 0\n"
      "ds_bpermute_b32 v129, v6, v221\n"
      "v_mfma_f32_16x16x32_bf16 v[160:163], v[174:177], a[100:103], v[160:163]\n"
      "v_exp_f32 v186, v186\n"
      "v_mfma_f32_16x16x32_bf16 v[160:163], v[178:181], a[104:107], v[160:163]\n"
      "v_exp_f32 v142, v142\n"
      "v_mfma_f32_16x16x32_bf16 v[160:163], v[26:29], a[108:111], v[160:163]\n"
      "v_exp_f32 v59, v59\n"
      "v_mfma_f32_16x16x32_bf16 v[148:151], v[60:63], v[244:247], 0\n"
      "ds_bpermute_b32 v223, v7, v113 offset:4\n"
      "v_mfma_f32_16x16x32_bf16 v[148:151], v[64:67], v[248:251], v[148:151]\n"
      "v_exp_f32 v188, v188\n"
      "v_mfma_f32_16x16x32_bf16 v[154:157], a[176:179], v[18:21], 0\n"
      "v_exp_f32 v104, v104\n"
      "v_mfma_f32_16x16x32_bf16 v[154:157], a[180:183], v[174:177], v[154:157]\n"
      "v_exp_f32 v189, v189\n"
      "v_mfma_f32_16x16x32_bf16 v[154:157], a[184:187], v[178:181], v[154:157]\n"
      "v_exp_f32 v108, v108\n"
      "v_mfma_f32_16x16x32_bf16 v[154:157], a[188:191], v[26:29], v[154:157]\n"
      "v_exp_f32 v150, v150\n"
      "v_mfma_f32_16x16x32_bf16 v[182:185], v[94:97], v[244:247], 0\n"
      "v_exp_f32 v148, v148\n"
      "v_mfma_f32_16x16x32_bf16 v[182:185], v[98:101], v[248:251], v[182:185]\n"
      "v_exp_f32 v111, v111\n"
      "v_mfma_f32_16x16x32_bf16 v[72:75], v[68:71], a[64:67], 0\n"
      "v_exp_f32 v106, v106\n"
      "v_mfma_f32_16x16x32_bf16 v[72:75], v[168:171], a[68:71], v[72:75]\n"
      "v_cvt_pk_bf16_f32 v218, v106, v107\n"
      "v_cvt_pk_bf16_f32 v39, v162, v163\n"
      "v_mfma_f32_16x16x32_bf16 v[72:75], v[22:25], a[72:75], v[72:75]\n"
      "v_exp_f32 v121, v121\n"
      "v_mfma_f32_16x16x32_bf16 v[72:75], v[90:93], a[76:79], v[72:75]\n"
      "v_cvt_pk_bf16_f32 v46, v76, v77\n"
      "v_cvt_pk_bf16_f32 v78, v154, v155\n"
      "v_mfma_f32_16x16x32_bf16 v[244:247], v[18:21], a[64:67], 0\n"
      "v_exp_f32 v109, v109\n"
      "v_mfma_f32_16x16x32_bf16 v[244:247], v[174:177], a[68:71], v[244:247]\n"
      "v_exp_f32 v110, v110\n"
      "v_mfma_f32_16x16x32_bf16 v[244:247], v[178:181], a[72:75], v[244:247]\n"
      "v_exp_f32 v151, v151\n"
      "v_mfma_f32_16x16x32_bf16 v[244:247], v[26:29], a[76:79], v[244:247]\n"
      "v_exp_f32 v182, v182\n"
      "v_mfma_f32_16x16x32_bf16 v[112:115], v[82:85], a[96:99], 0\n"
      "v_cvt_pk_bf16_f32 v221, v58, v59\n"
      "v_cvt_pk_bf16_f32 v38, v160, v161\n"
      "v_mfma_f32_16x16x32_bf16 v[112:115], v[164:167], a[100:103], v[112:115]\n"
      "v_exp_f32 v88, v88\n"
      "v_mfma_f32_16x16x32_bf16 v[112:115], v[124:127], a[104:107], v[112:115]\n"
      "v_exp_f32 v187, v187\n"
      "v_mfma_f32_16x16x32_bf16 v[112:115], v[252:255], a[108:111], v[112:115]\n"
      "v_exp_f32 v89, v89\n"
      "v_cvt_pk_bf16_f32 v139, v246, v247\n"
      "v_mfma_f32_16x16x32_bf16 v[248:251], v[18:21], a[112:115], 0\n"
      "v_exp_f32 v185, v185\n"
      "v_mfma_f32_16x16x32_bf16 v[248:251], v[174:177], a[116:119], v[248:251]\n"
      "v_cvt_pk_bf16_f32 v162, v186, v187\n"
      "v_cvt_pk_bf16_f32 v138, v244, v245\n"
      "v_mfma_f32_16x16x32_bf16 v[248:251], v[178:181], a[120:123], v[248:251]\n"
      "v_exp_f32 v123, v123\n"
      "v_mfma_f32_16x16x32_bf16 v[248:251], v[26:29], a[124:127], v[248:251]\n"
      "v_exp_f32 v149, v149\n"
      "v_mfma_f32_16x16x32_bf16 v[152:155], v[68:71], a[80:83], 0\n"
      "v_cvt_pk_bf16_f32 v79, v156, v157\n"
      "v_cvt_pk_bf16_f32 v160, v148, v149\n"
      "v_mfma_f32_16x16x32_bf16 v[152:155], v[168:171], a[84:87], v[152:155]\n"
      "v_exp_f32 v122, v122\n"
      "v_mfma_f32_16x16x32_bf16 v[152:155], v[22:25], a[88:91], v[152:155]\n"
      "v_exp_f32 v183, v183\n"
      "v_mfma_f32_16x16x32_bf16 v[152:155], v[90:93], a[92:95], v[152:155]\n"
      "v_exp_f32 v57, v57\n"
      "v_mfma_f32_16x16x32_bf16 v[244:247], v[68:71], a[96:99], 0\n"
      "v_cvt_pk_bf16_f32 v55, v250, v251\n"
      "v_cvt_pk_bf16_f32 v220, v56, v57\n"
      "v_mfma_f32_16x16x32_bf16 v[244:247], v[168:171], a[100:103], v[244:247]\n"
      "v_exp_f32 v141, v141\n"
      "v_mfma_f32_16x16x32_bf16 v[244:247], v[22:25], a[104:107], v[244:247]\n"
      "v_exp_f32 v146, v146\n"
      "v_mfma_f32_16x16x32_bf16 v[244:247], v[90:93], a[108:111], v[244:247]\n"
      "v_cvt_pk_bf16_f32 v161, v150, v151\n"
      "v_cvt_pk_bf16_f32 v163, v188, v189\n"
      "v_mfma_f32_16x16x32_bf16 v[186:189], a[160:163], v[18:21], 0\n"
      "v_exp_f32 v143, v143\n"
      "v_mfma_f32_16x16x32_bf16 v[186:189], a[164:167], v[174:177], v[186:189]\n"
      "v_exp_f32 v120, v120\n"
      "v_mfma_f32_16x16x32_bf16 v[186:189], a[168:171], v[178:181], v[186:189]\n"
      "v_cvt_pk_bf16_f32 v54, v248, v249\n"
      "v_cvt_pk_bf16_f32 v250, v112, v113\n"
      "v_mfma_f32_16x16x32_bf16 v[186:189], a[172:175], v[26:29], v[186:189]\n"
      "v_cvt_pk_bf16_f32 v248, v244, v245\n"
      "v_cvt_pk_bf16_f32 v251, v114, v115\n"
      "v_mfma_f32_16x16x32_bf16 v[112:115], v[82:85], a[80:83], 0\n"
      "v_cvt_pk_bf16_f32 v158, v108, v109\n"
      "v_cvt_pk_bf16_f32 v249, v246, v247\n"
      "v_mfma_f32_16x16x32_bf16 v[112:115], v[164:167], a[84:87], v[112:115]\n"
      "v_exp_f32 v87, v87\n"
      "v_mfma_f32_16x16x32_bf16 v[112:115], v[124:127], a[88:91], v[112:115]\n"
      "v_exp_f32 v184, v184\n"
      "v_mfma_f32_16x16x32_bf16 v[112:115], v[252:255], a[92:95], v[112:115]\n"
      "v_cvt_pk_bf16_f32 v59, v184, v185\n"
      "v_cvt_pk_bf16_f32 v77, v188, v189\n"
      "v_mfma_f32_16x16x32_bf16 v[106:109], v[82:85], a[64:67], 0\n"
      "v_exp_f32 v147, v147\n"
      "v_mfma_f32_16x16x32_bf16 v[106:109], v[164:167], a[68:71], v[106:109]\n"
      "v_cvt_pk_bf16_f32 v159, v110, v111\n"
      "v_cvt_pk_bf16_f32 v56, v86, v87\n"
      "v_mfma_f32_16x16x32_bf16 v[106:109], v[124:127], a[72:75], v[106:109]\n"
      "v_exp_f32 v140, v140\n"
      "v_mfma_f32_16x16x32_bf16 v[106:109], v[252:255], a[76:79], v[106:109]\n"
      "v_cvt_pk_bf16_f32 v58, v182, v183\n"
      "v_cvt_pk_bf16_f32 v57, v88, v89\n"
      "v_mfma_f32_16x16x32_bf16 v[148:151], a[144:147], v[18:21], 0\n"
      "v_cvt_pk_bf16_f32 v244, v152, v153\n"
      "v_cvt_pk_bf16_f32 v76, v186, v187\n"
      "v_mfma_f32_16x16x32_bf16 v[148:151], a[148:151], v[174:177], v[148:151]\n"
      "v_cvt_pk_bf16_f32 v156, v120, v121\n"
      "v_cvt_pk_bf16_f32 v217, v146, v147\n"
      "v_mfma_f32_16x16x32_bf16 v[148:151], a[152:155], v[178:181], v[148:151]\n"
      "v_add_f32 v102, v102, v103\n"
      "v_cvt_pk_bf16_f32 v245, v154, v155\n"
      "v_mfma_f32_16x16x32_bf16 v[148:151], a[156:159], v[26:29], v[148:151]\n"
      "v_exp_f32 v102, v102\n"
      "v_mfma_f32_16x16x32_bf16 v[152:155], v[68:71], a[112:115], 0\n"
      "v_add_f32 v182, v102, v104\n"
      "v_cvt_pk_bf16_f32 v216, v144, v145\n"
      "v_mfma_f32_16x16x32_bf16 v[152:155], v[168:171], a[116:119], v[152:155]\n"
      "v_cvt_pk_bf16_f32 v215, v142, v143\n"
      "v_cvt_pk_bf16_f32 v87, v102, v104\n"
      "v_mfma_f32_16x16x32_bf16 v[152:155], v[22:25], a[120:123], v[152:155]\n"
      "v_cvt_pk_bf16_f32 v214, v140, v141\n"
      "v_cvt_pk_bf16_f32 v86, v102, v104\n"
      "v_mfma_f32_16x16x32_bf16 v[152:155], v[90:93], a[124:127], v[152:155]\n"
      "v_cvt_pk_bf16_f32 v157, v122, v123\n"
      "v_cvt_pk_bf16_f32 v247, v114, v115\n"
      "v_mfma_f32_16x16x32_bf16 v[184:187], a[128:131], v[18:21], 0\n"
      "v_cvt_pk_bf16_f32 v211, v74, v75\n"
      "v_add_f32 v183, v102, v104\n"
      "v_mfma_f32_16x16x32_bf16 v[184:187], a[132:135], v[174:177], v[184:187]\n"
      "v_cvt_pk_bf16_f32 v210, v72, v73\n"
      "v_cvt_pk_bf16_f32 v75, v150, v151\n"
      "v_mfma_f32_16x16x32_bf16 v[184:187], a[136:139], v[178:181], v[184:187]\n"
      "v_cvt_pk_bf16_f32 v74, v148, v149\n"
      "v_cvt_pk_bf16_f32 v212, v106, v107\n"
      "v_mfma_f32_16x16x32_bf16 v[184:187], a[140:143], v[26:29], v[184:187]\n"
      "v_permlane16_swap_b32 v182, v183\n"
      "v_mfma_f32_16x16x32_bf16 v[144:147], v[82:85], a[112:115], 0\n"
      "v_permlane16_swap_b32 v86, v87\n"
      "v_mfma_f32_16x16x32_bf16 v[144:147], v[164:167], a[116:119], v[144:147]\n"
      "v_cvt_pk_bf16_f32 v213, v108, v109\n"
      "v_cvt_pk_bf16_f32 v106, v152, v153\n"
      "v_mfma_f32_16x16x32_bf16 v[144:147], v[124:127], a[120:123], v[144:147]\n"
      "s_nop 0\n"
      "v_cvt_pk_bf16_f32 v73, v186, v187\n"
      "v_mov_b64 v[102:103], v[182:183]\n"
      "v_mfma_f32_16x16x32_bf16 v[144:147], v[252:255], a[124:127], v[144:147]\n"
      "v_cvt_pk_bf16_f32 v107, v154, v155\n"
      "v_cvt_pk_bf16_f32 v72, v184, v185\n"
      "v_smfmac_f32_16x16x64_bf16 v[182:185], v[12:15], v[214:221], v5\n"
      "ds_read_b64 v[110:111], v11 offset:4096\n"
      "v_cvt_pk_bf16_f32 v246, v112, v113\n"
      "v_smfmac_f32_16x16x64_bf16 v[102:105], v[12:15], v[156:163], v5\n"
      "s_nop 1\n"
      "v_cvt_pk_bf16_f32 v109, v146, v147\n"
      "v_smfmac_f32_16x16x32_bf16 v[182:185], v[12:13], v[116:119], v5\n"
      "v_cvt_pk_bf16_f32 v108, v144, v145\n"
      "v_mfma_f32_16x16x4_4b_bf16 v[140:155], v[86:87], v[30:31], 0 cbsz:2 abid:0\n"
      "s_nop 5\n"
      "v_add_f32 v182, v182, v183\n"
      "v_smfmac_f32_16x16x32_bf16 v[102:105], v[12:13], v[56:59], v5\n"
      "s_nop 1\n"
      "v_mfma_f32_16x16x32_bf16 v[152:155], v[214:217], v[48:51], v[152:155]\n"
      "v_rcp_f32 v183, v182\n"
      "v_mfma_f32_16x16x32_bf16 v[152:155], v[218:221], v[52:55], v[152:155]\n"
      "ds_bpermute_b32 v184, v4, v183\n"
      "v_mfma_f32_16x16x32_bf16 v[152:155], v[116:119], v[106:109], v[152:155]\n"
      "v_mfma_f32_16x16x32_bf16 v[140:143], v[214:217], v[132:135], v[140:143]\n"
      "v_mfma_f32_16x16x32_bf16 v[140:143], v[218:221], v[136:139], v[140:143]\n"
      "v_add_f32_dpp v172, v172, v172 row_ror:8 row_mask:0xf bank_mask:0xf\n"
      "v_add_f32 v102, v102, v103\n"
      "v_mfma_f32_16x16x32_bf16 v[140:143], v[116:119], v[210:213], v[140:143]\n"
      "v_mfma_f32_16x16x32_bf16 v[144:147], v[214:217], v[40:43], v[144:147]\n"
      "v_mfma_f32_16x16x32_bf16 v[144:147], v[218:221], v[44:47], v[144:147]\n"
      "v_add_f32_dpp v172, v172, v172 row_ror:4 row_mask:0xf bank_mask:0xf\n"
      "v_mfma_f32_16x16x32_bf16 v[144:147], v[116:119], v[244:247], v[144:147]\n"
      "v_mfma_f32_16x16x32_bf16 v[148:151], v[214:217], v[32:35], v[148:151]\n"
      "v_mfma_f32_16x16x32_bf16 v[148:151], v[218:221], v[36:39], v[148:151]\n"
      "v_mfma_f32_16x16x32_bf16 v[148:151], v[116:119], v[248:251], v[148:151]\n"
      "s_waitcnt lgkmcnt(0)\n"
      "v_mul_f32_dpp v142, v184, v142 row_newbcast:8 row_mask:0xf bank_mask:0xf\n"
      "v_mul_f32_dpp v143, v184, v143 row_newbcast:12 row_mask:0xf bank_mask:0xf\n"
      "v_mfma_f32_16x16x32_bf16 v[214:217], v[190:193], v[72:75], 0\n"
      "v_mul_f32_dpp v141, v184, v141 row_newbcast:4 row_mask:0xf bank_mask:0xf\n"
      "v_mul_f32_dpp v140, v184, v140 row_newbcast:0 row_mask:0xf bank_mask:0xf\n"
      "v_mfma_f32_16x16x32_bf16 v[214:217], v[194:197], v[76:79], v[214:217]\n"
      "v_mul_f32_dpp v144, v184, v144 row_newbcast:0 row_mask:0xf bank_mask:0xf\n"
      "v_mul_f32_dpp v155, v184, v155 row_newbcast:12 row_mask:0xf bank_mask:0xf\n"
      "v_mfma_f32_16x16x32_bf16 v[186:189], v[94:97], v[72:75], 0\n"
      "v_add_f32_dpp v172, v172, v172 row_ror:2 row_mask:0xf bank_mask:0xf\n"
      "v_mul_f32_dpp v147, v184, v147 row_newbcast:12 row_mask:0xf bank_mask:0xf\n"
      "v_mfma_f32_16x16x32_bf16 v[186:189], v[98:101], v[76:79], v[186:189]\n"
      "v_mul_f32_dpp v145, v184, v145 row_newbcast:4 row_mask:0xf bank_mask:0xf\n"
      "v_mfma_f32_16x16x32_bf16 v[140:143], v[82:85], a[192:195], v[140:143]\n"
      "v_exp_f32 v215, v215\n"
      "v_mfma_f32_16x16x32_bf16 v[140:143], v[164:167], a[196:199], v[140:143]\n"
      "v_mul_f32_dpp v146, v184, v146 row_newbcast:8 row_mask:0xf bank_mask:0xf\n"
      "v_mul_f32_dpp v151, v184, v151 row_newbcast:12 row_mask:0xf bank_mask:0xf\n"
      "v_mfma_f32_16x16x32_bf16 v[140:143], v[124:127], a[200:203], v[140:143]\n"
      "v_mul_f32_dpp v150, v184, v150 row_newbcast:8 row_mask:0xf bank_mask:0xf\n"
      "v_mfma_f32_16x16x32_bf16 v[140:143], v[252:255], a[204:207], v[140:143]\n"
      "v_exp_f32 v188, v188\n"
      "v_mfma_f32_16x16x32_bf16 v[144:147], v[82:85], a[208:211], v[144:147]\n"
      "v_mul_f32_dpp v152, v184, v152 row_newbcast:0 row_mask:0xf bank_mask:0xf\n"
      "v_mfma_f32_16x16x32_bf16 v[144:147], v[164:167], a[212:215], v[144:147]\n"
      "v_mul_f32_dpp v153, v184, v153 row_newbcast:4 row_mask:0xf bank_mask:0xf\n"
      "v_mul_f32_dpp v149, v184, v149 row_newbcast:4 row_mask:0xf bank_mask:0xf\n"
      "v_mfma_f32_16x16x32_bf16 v[144:147], v[124:127], a[216:219], v[144:147]\n"
      "v_mul_f32_dpp v154, v184, v154 row_newbcast:8 row_mask:0xf bank_mask:0xf\n"
      "v_mfma_f32_16x16x32_bf16 v[144:147], v[252:255], a[220:223], v[144:147]\n"
      "v_exp_f32 v214, v214\n"
      "v_mfma_f32_16x16x32_bf16 v[152:155], v[82:85], a[240:243], v[152:155]\n"
      "v_mfma_f32_16x16x32_bf16 v[152:155], v[164:167], a[244:247], v[152:155]\n"
      "v_mfma_f32_16x16x32_bf16 v[152:155], v[124:127], a[248:251], v[152:155]\n"
      "v_mul_f32_dpp v148, v184, v148 row_newbcast:0 row_mask:0xf bank_mask:0xf\n"
      "v_add_f32_dpp v172, v172, v172 row_ror:1 row_mask:0xf bank_mask:0xf\n"
      "v_mfma_f32_16x16x32_bf16 v[152:155], v[252:255], a[252:255], v[152:155]\n"
      "v_rcp_f32 v103, v102\n"
      "v_mfma_f32_16x16x32_bf16 v[148:151], v[82:85], a[224:227], v[148:151]\n"
      "v_cvt_pk_bf16_f32 v140, v140, v144\n"
      "v_cvt_pk_bf16_f32 v142, v142, v146\n"
      "v_mfma_f32_16x16x32_bf16 v[148:151], v[164:167], a[228:231], v[148:151]\n"
      "ds_bpermute_b32 v104, v4, v103 offset:128\n"
      "v_mfma_f32_16x16x32_bf16 v[148:151], v[124:127], a[232:235], v[148:151]\n"
      "v_lshlrev_b32 v88, 16, v111\n"
      "v_cvt_pk_bf16_f32 v144, v141, v145\n"
      "v_mfma_f32_16x16x32_bf16 v[148:151], v[252:255], a[236:239], v[148:151]\n"
      "v_exp_f32 v189, v189\n"
      "v_mfma_f32_16x16x4_4b_bf16 v[112:127], v[86:87], v[30:31], 0 cbsz:2 abid:2\n"
      "v_exp_f32 v187, v187\n"
      "v_rcp_f32 v173, v172\n"
      "v_cvt_pk_bf16_f32 v146, v143, v147\n"
      "v_pk_max_i16 v146, v146, 0\n"
      "v_maximum3_f32 v8, v8, v172, v173\n"
      "v_maximum3_f32 v8, v8, v182, v183\n"
      "v_mfma_f32_16x16x32_bf16 v[182:185], v[60:63], v[72:75], 0\n"
      "v_exp_f32 v186, v186\n"
      "v_mfma_f32_16x16x32_bf16 v[182:185], v[64:67], v[76:79], v[182:185]\n"
      "v_cvt_pk_bf16_f32 v145, v149, v153\n"
      "v_mfma_f32_16x16x32_bf16 v[120:123], v[156:159], v[32:35], v[120:123]\n"
      "v_lshlrev_b32 v86, 16, v110\n"
      "v_cvt_pk_bf16_f32 v141, v148, v152\n"
      "v_mfma_f32_16x16x32_bf16 v[120:123], v[160:163], v[36:39], v[120:123]\n"
      "v_pk_max_i16 v145, v145, 0\n"
      "v_mfma_f32_16x16x32_bf16 v[120:123], v[56:59], v[248:251], v[120:123]\n"
      "v_exp_f32 v217, v217\n"
      "v_mfma_f32_16x16x32_bf16 v[112:115], v[156:159], v[132:135], v[112:115]\n"
      "v_maximum3_f32 v8, v8, v102, v103\n"
      "v_cvt_pk_bf16_f32 v143, v150, v154\n"
      "v_mfma_f32_16x16x32_bf16 v[112:115], v[160:163], v[136:139], v[112:115]\n"
      "v_pk_max_i16 v140, v140, 0\n"
      "v_cvt_pk_bf16_f32 v147, v151, v155\n"
      "v_mfma_f32_16x16x32_bf16 v[112:115], v[56:59], v[210:213], v[112:115]\n"
      "v_exp_f32 v216, v216\n"
      "v_mfma_f32_16x16x32_bf16 v[124:127], v[156:159], v[48:51], v[124:127]\n"
      "v_pk_max_i16 v144, v144, 0\n"
      "v_and_b32 v87, 0xffff0000, v110\n"
      "v_mfma_f32_16x16x32_bf16 v[124:127], v[160:163], v[52:55], v[124:127]\n"
      "v_exp_f32 v185, v185\n"
      "v_mfma_f32_16x16x32_bf16 v[124:127], v[56:59], v[106:109], v[124:127]\n"
      "s_waitcnt lgkmcnt(0)\n"
      "v_mul_f32_dpp v121, v104, v121 row_newbcast:4 row_mask:0xf bank_mask:0xf\n"
      "v_cvt_pk_bf16_f32 v219, v188, v189\n"
      "v_mfma_f32_16x16x32_bf16 v[116:119], v[156:159], v[40:43], v[116:119]\n"
      "v_cvt_pk_bf16_f32 v84, v214, v215\n"
      "v_mul_f32_dpp v120, v104, v120 row_newbcast:0 row_mask:0xf bank_mask:0xf\n"
      "v_mfma_f32_16x16x32_bf16 v[116:119], v[160:163], v[44:47], v[116:119]\n"
      "v_cvt_pk_bf16_f32 v85, v216, v217\n"
      "v_pk_max_i16 v147, v147, 0\n"
      "v_mfma_f32_16x16x32_bf16 v[116:119], v[56:59], v[244:247], v[116:119]\n"
      "v_mul_f32_dpp v126, v104, v126 row_newbcast:8 row_mask:0xf bank_mask:0xf\n"
      "v_mul_f32_dpp v124, v104, v124 row_newbcast:0 row_mask:0xf bank_mask:0xf\n"
      "v_mfma_f32_16x16x4_4b_bf16 v[148:163], v[80:81], v[30:31], 0 cbsz:2 abid:0\n"
      "v_and_b32 v89, 0xffff0000, v111\n"
      "v_mul_f32_dpp v122, v104, v122 row_newbcast:8 row_mask:0xf bank_mask:0xf\n"
      "v_mul_f32_dpp v113, v104, v113 row_newbcast:4 row_mask:0xf bank_mask:0xf\n"
      "v_pk_max_i16 v141, v141, 0\n"
      "v_pk_max_i16 v143, v143, 0\n"
      "v_mul_f32_dpp v112, v104, v112 row_newbcast:0 row_mask:0xf bank_mask:0xf\n"
      "v_exp_f32 v184, v184\n"
      "v_mfma_f32_16x16x32_bf16 v[56:59], v[232:235], v[72:75], 0\n"
      "v_mul_f32_dpp v117, v104, v117 row_newbcast:4 row_mask:0xf bank_mask:0xf\n"
      "v_pk_max_i16 v142, v142, 0\n"
      "v_mfma_f32_16x16x32_bf16 v[56:59], v[236:239], v[76:79], v[56:59]\n"
      "buffer_store_dwordx2 v[142:143], v2, s[44:47], s73 offen offset:512 nt sc1\n"
      "v_cvt_pk_bf16_f32 v218, v186, v187\n"
      "v_smfmac_f32_16x16x64_bf16 v[156:159], v[128:131], v[32:39], v5\n"
      "ds_read_b128 v[186:189], v0 offset:8192\n"
      "v_mul_f32_dpp v123, v104, v123 row_newbcast:12 row_mask:0xf bank_mask:0xf\n"
      "v_smfmac_f32_16x16x64_bf16 v[148:151], v[128:131], v[132:139], v5\n"
      "v_smfmac_f32_16x16x64_bf16 v[160:163], v[128:131], v[48:55], v5\n"
      "buffer_store_dwordx2 v[140:141], v2, s[44:47], s73 offen offset:0 nt sc1\n"
      "v_mul_f32_dpp v114, v104, v114 row_newbcast:8 row_mask:0xf bank_mask:0xf\n"
      "v_smfmac_f32_16x16x64_bf16 v[152:155], v[128:131], v[40:47], v5\n"
      "buffer_store_dwordx2 v[144:145], v2, s[44:47], s73 offen offset:256 nt sc1\n"
      "v_mul_f32_dpp v115, v104, v115 row_newbcast:12 row_mask:0xf bank_mask:0xf\n"
      "v_smfmac_f32_16x16x32_bf16 v[148:151], v[222:223], v[210:213], v5\n"
      "ds_read_b128 v[252:255], v0 offset:9216\n"
      "v_mul_f32_dpp v119, v104, v119 row_newbcast:12 row_mask:0xf bank_mask:0xf\n"
      "v_smfmac_f32_16x16x32_bf16 v[160:163], v[222:223], v[106:109], v5\n"
      "v_smfmac_f32_16x16x32_bf16 v[152:155], v[222:223], v[244:247], v5\n"
      "v_mov_b64 v[164:165], 0\n"
      "v_smfmac_f32_16x16x32_bf16 v[156:159], v[222:223], v[248:251], v5\n"
      "buffer_store_dwordx2 v[146:147], v2, s[44:47], s73 offen offset:768 nt sc1\n"
      "v_mfma_f32_16x16x32_bf16 v[80:83], v[202:205], v[72:75], 0\n"
      "v_mul_f32_dpp v116, v104, v116 row_newbcast:0 row_mask:0xf bank_mask:0xf\n"
      "v_add_f32 v148, v148, v149\n"
      "v_mfma_f32_16x16x32_bf16 v[80:83], v[206:209], v[76:79], v[80:83]\n"
      "s_nop 0\n"
      "v_add_f32 v152, v152, v153\n"
      "v_fma_f32 v152, v152, v173, v87\n"
      "v_mfma_f32_16x16x32_bf16 v[120:123], v[68:71], a[224:227], v[120:123]\n"
      "v_mul_f32_dpp v127, v104, v127 row_newbcast:12 row_mask:0xf bank_mask:0xf\n"
      "v_fma_f32 v148, v148, v173, v86\n"
      "v_mfma_f32_16x16x32_bf16 v[120:123], v[168:171], a[228:231], v[120:123]\n"
      "v_mul_f32_dpp v118, v104, v118 row_newbcast:8 row_mask:0xf bank_mask:0xf\n"
      "v_mul_f32_dpp v125, v104, v125 row_newbcast:4 row_mask:0xf bank_mask:0xf\n"
      "v_mfma_f32_16x16x32_bf16 v[120:123], v[22:25], a[232:235], v[120:123]\n"
      "ds_read_b128 v[140:143], v0 offset:9728\n"
      "v_add_f32 v156, v156, v157\n"
      "v_mfma_f32_16x16x32_bf16 v[120:123], v[90:93], a[236:239], v[120:123]\n"
      "v_cvt_pk_bf16_f32 v148, v148, v152\n"
      "v_add_f32 v160, v160, v161\n"
      "v_mfma_f32_16x16x32_bf16 v[102:105], v[224:227], v[72:75], 0\n"
      "ds_read_b128 v[220:223], v0 offset:8704\n"
      "v_pk_max_i16 v148, v148, 0\n"
      "v_mfma_f32_16x16x32_bf16 v[102:105], v[228:231], v[76:79], v[102:105]\n"
      "v_exp_f32 v56, v56\n"
      "v_mfma_f32_16x16x32_bf16 v[116:119], v[68:71], a[208:211], v[116:119]\n"
      "v_fma_f32 v156, v156, v173, v88\n"
      "v_fma_f32 v160, v160, v173, v89\n"
      "v_mfma_f32_16x16x32_bf16 v[116:119], v[168:171], a[212:215], v[116:119]\n"
      "v_cvt_pk_bf16_f32 v149, v156, v160\n"
      "v_pk_max_i16 v149, v149, 0\n"
      "v_mfma_f32_16x16x32_bf16 v[116:119], v[22:25], a[216:219], v[116:119]\n"
      "v_exp_f32 v102, v102\n"
      "v_mfma_f32_16x16x32_bf16 v[116:119], v[90:93], a[220:223], v[116:119]\n"
      "v_exp_f32 v81, v81\n"
      "v_mfma_f32_16x16x32_bf16 v[112:115], v[68:71], a[192:195], v[112:115]\n"
      "v_exp_f32 v103, v103\n"
      "v_mfma_f32_16x16x32_bf16 v[112:115], v[168:171], a[196:199], v[112:115]\n"
      "v_exp_f32 v104, v104\n"
      "v_mfma_f32_16x16x32_bf16 v[112:115], v[22:25], a[200:203], v[112:115]\n"
      "v_exp_f32 v58, v58\n"
      "v_mfma_f32_16x16x32_bf16 v[112:115], v[90:93], a[204:207], v[112:115]\n"
      "s_waitcnt lgkmcnt(3)\n"
      "v_mfma_f32_16x16x32_bf16 v[128:131], a[176:179], v[186:189], 0\n"
      "v_cvt_pk_bf16_f32 v86, v102, v103\n"
      "s_waitcnt lgkmcnt(0)\n"
      "v_mfma_f32_16x16x32_bf16 v[128:131], a[180:183], v[220:223], v[128:131]\n"
      "v_exp_f32 v105, v105\n"
      "v_mfma_f32_16x16x32_bf16 v[128:131], a[184:187], v[252:255], v[128:131]\n"
      "v_exp_f32 v183, v183\n"
      "v_mfma_f32_16x16x32_bf16 v[128:131], a[188:191], v[140:143], v[128:131]\n"
      "v_exp_f32 v59, v59\n"
      "v_mfma_f32_16x16x32_bf16 v[144:147], a[144:147], v[186:189], 0\n"
      "v_cvt_pk_bf16_f32 v87, v104, v105\n"
      "v_mfma_f32_16x16x32_bf16 v[144:147], a[148:151], v[220:223], v[144:147]\n"
      "v_exp_f32 v83, v83\n"
      "v_mfma_f32_16x16x32_bf16 v[144:147], a[152:155], v[252:255], v[144:147]\n"
      "v_cvt_pk_bf16_f32 v112, v112, v116\n"
      "v_cvt_pk_bf16_f32 v116, v113, v117\n"
      "v_mfma_f32_16x16x32_bf16 v[144:147], a[156:159], v[140:143], v[144:147]\n"
      "v_pk_max_i16 v116, v116, 0\n"
      "v_mov_b64 v[162:163], 0\n"
      "v_mfma_f32_16x16x32_bf16 v[102:105], a[128:131], v[186:189], 0\n"
      "v_exp_f32 v80, v80\n"
      "v_mfma_f32_16x16x32_bf16 v[102:105], a[132:135], v[220:223], v[102:105]\n"
      "v_exp_f32 v57, v57\n"
      "v_mfma_f32_16x16x32_bf16 v[102:105], a[136:139], v[252:255], v[102:105]\n"
      "v_pk_max_i16 v112, v112, 0\n"
      "v_cvt_pk_bf16_f32 v156, v128, v129\n"
      "v_mfma_f32_16x16x32_bf16 v[102:105], a[140:143], v[140:143], v[102:105]\n"
      "v_exp_f32 v182, v182\n"
      "v_smfmac_f32_16x16x64_bf16 v[162:165], v[198:201], v[72:79], v5\n"
      "v_cvt_pk_bf16_f32 v157, v130, v131\n"
      "v_mfma_f32_16x16x32_bf16 v[72:75], a[160:163], v[186:189], 0\n"
      "v_cvt_pk_bf16_f32 v114, v114, v118\n"
      "v_cvt_pk_bf16_f32 v152, v144, v145\n"
      "v_mfma_f32_16x16x32_bf16 v[72:75], a[164:167], v[220:223], v[72:75]\n"
      "s_nop 0\n"
      "v_cvt_pk_bf16_f32 v151, v104, v105\n"
      "v_mfma_f32_16x16x32_bf16 v[72:75], a[168:171], v[252:255], v[72:75]\n"
      "ds_read_b128 v[128:131], v0 offset:4608\n"
      "v_mfma_f32_16x16x32_bf16 v[72:75], a[172:175], v[140:143], v[72:75]\n"
      "v_exp_f32 v82, v82\n"
      "v_mfma_f32_16x16x32_bf16 v[124:127], v[68:71], a[240:243], v[124:127]\n"
      "v_cvt_pk_bf16_f32 v150, v102, v103\n"
      "v_cvt_pk_bf16_f32 v217, v58, v59\n"
      "v_mfma_f32_16x16x32_bf16 v[124:127], v[168:171], a[244:247], v[124:127]\n"
      "v_cvt_pk_bf16_f32 v89, v184, v185\n"
      "v_cvt_pk_bf16_f32 v153, v146, v147\n"
      "v_mfma_f32_16x16x32_bf16 v[124:127], v[22:25], a[248:251], v[124:127]\n"
      "v_cvt_pk_bf16_f32 v155, v74, v75\n"
      "v_cvt_pk_bf16_f32 v154, v72, v73\n"
      "v_mfma_f32_16x16x32_bf16 v[124:127], v[90:93], a[252:255], v[124:127]\n"
      "v_cvt_pk_bf16_f32 v88, v182, v183\n"
      "v_cvt_pk_bf16_f32 v118, v115, v119\n"
      "v_smfmac_f32_16x16x64_bf16 v[162:165], v[240:243], v[150:157], v5\n"
      "buffer_store_dwordx2 v[148:149], v3, s[44:47], s79 offen offset:0 nt sc1\n"
      "v_cvt_pk_bf16_f32 v91, v82, v83\n"
      "v_mfma_f32_16x16x32_bf16 v[170:173], v[232:235], v[150:153], 0\n"
      "s_nop 1\n"
      "v_cvt_pk_bf16_f32 v117, v121, v125\n"
      "v_mfma_f32_16x16x32_bf16 v[170:173], v[236:239], v[154:157], v[170:173]\n"
      "v_pk_max_i16 v117, v117, 0\n"
      "v_cvt_pk_bf16_f32 v119, v123, v127\n"
      "v_mfma_f32_16x16x32_bf16 v[144:147], v[224:227], v[150:153], 0\n"
      "v_add_f32 v164, v164, v165\n"
      "v_cvt_pk_bf16_f32 v113, v120, v124\n"
      "v_mfma_f32_16x16x32_bf16 v[144:147], v[228:231], v[154:157], v[144:147]\n"
      "v_cvt_pk_bf16_f32 v90, v80, v81\n"
      "v_add_f32 v162, v162, v163\n"
      "v_mfma_f32_16x16x32_bf16 v[166:169], v[60:63], v[150:153], 0\n"
      "v_exp_f32 v164, v164\n"
      "v_mfma_f32_16x16x32_bf16 v[166:169], v[64:67], v[154:157], v[166:169]\n"
      "v_exp_f32 v162, v162\n"
      "v_mfma_f32_16x16x32_bf16 v[158:161], v[94:97], v[150:153], 0\n"
      "v_cvt_pk_bf16_f32 v93, v162, v164\n"
      "v_cvt_pk_bf16_f32 v92, v162, v164\n"
      "v_mfma_f32_16x16x32_bf16 v[158:161], v[98:101], v[154:157], v[158:161]\n"
      "s_nop 0\n"
      "v_permlane16_swap_b32 v92, v93\n"
      "v_mfma_f32_16x16x32_bf16 v[102:105], v[202:205], v[150:153], 0\n"
      "v_exp_f32 v171, v171\n"
      "v_mfma_f32_16x16x32_bf16 v[102:105], v[206:209], v[154:157], v[102:105]\n"
      "v_add_f32 v120, v162, v164\n"
      "v_add_f32 v121, v162, v164\n"
      "v_mfma_f32_16x16x4_4b_bf16 v[68:83], v[92:93], v[30:31], 0 cbsz:2 abid:0\n"
      "s_nop 0\n"
      "v_permlane16_swap_b32 v120, v121\n"
      "v_pk_max_i16 v114, v114, 0\n"
      "v_pk_max_i16 v118, v118, 0\n"
      "v_exp_f32 v147, v147\n"
      "v_cvt_pk_bf16_f32 v115, v122, v126\n"
      "v_mov_b64 v[214:215], v[120:121]\n"
      "v_smfmac_f32_16x16x64_bf16 v[120:123], v[12:15], v[84:91], v5\n"
      "buffer_store_dwordx2 v[116:117], v2, s[44:47], s75 offen offset:256 nt sc1\n"
      "v_cvt_pk_bf16_f32 v216, v56, v57\n"
      "v_mfma_f32_16x16x32_bf16 v[182:185], v[190:193], v[150:153], 0\n"
      "v_exp_f32 v103, v103\n"
      "v_mfma_f32_16x16x32_bf16 v[182:185], v[194:197], v[154:157], v[182:185]\n"
      "v_exp_f32 v160, v160\n"
      "v_smfmac_f32_16x16x32_bf16 v[120:123], v[12:13], v[216:219], v5\n"
      "ds_read_b128 v[56:59], v0 offset:5120\n"
      "ds_read_b128 v[124:127], v0 offset:5632\n"
      "v_exp_f32 v104, v104\n"
      "v_mfma_f32_16x16x32_bf16 v[76:79], v[84:87], v[32:35], v[76:79]\n"
      "ds_read_b128 v[22:25], v0 offset:4096\n"
      "v_pk_max_i16 v119, v119, 0\n"
      "v_mfma_f32_16x16x32_bf16 v[76:79], v[88:91], v[36:39], v[76:79]\n"
      "s_nop 0\n"
      "v_add_f32 v120, v120, v121\n"
      "v_mfma_f32_16x16x32_bf16 v[76:79], v[216:219], v[248:251], v[76:79]\n"
      "v_rcp_f32 v121, v120\n"
      "v_mfma_f32_16x16x32_bf16 v[80:83], v[84:87], v[48:51], v[80:83]\n"
      "ds_bpermute_b32 v122, v4, v121\n"
      "v_mfma_f32_16x16x32_bf16 v[80:83], v[88:91], v[52:55], v[80:83]\n"
      "v_exp_f32 v172, v172\n"
      "v_mfma_f32_16x16x32_bf16 v[80:83], v[216:219], v[106:109], v[80:83]\n"
      "v_exp_f32 v146, v146\n"
      "v_mfma_f32_16x16x32_bf16 v[68:71], v[84:87], v[132:135], v[68:71]\n"
      "v_exp_f32 v184, v184\n"
      "v_mfma_f32_16x16x32_bf16 v[68:71], v[88:91], v[136:139], v[68:71]\n"
      "v_exp_f32 v173, v173\n"
      "v_mfma_f32_16x16x32_bf16 v[68:71], v[216:219], v[210:213], v[68:71]\n"
      "v_exp_f32 v166, v166\n"
      "v_mfma_f32_16x16x32_bf16 v[72:75], v[84:87], v[40:43], v[72:75]\n"
      "v_exp_f32 v169, v169\n"
      "v_mfma_f32_16x16x32_bf16 v[72:75], v[88:91], v[44:47], v[72:75]\n"
      "v_exp_f32 v167, v167\n"
      "v_mfma_f32_16x16x32_bf16 v[72:75], v[216:219], v[244:247], v[72:75]\n"
      "buffer_store_dwordx2 v[118:119], v2, s[44:47], s75 offen offset:768 nt sc1\n"
      "s_waitcnt lgkmcnt(1)\n"
      "v_mfma_f32_16x16x32_bf16 v[88:91], a[176:179], v[22:25], 0\n"
      "v_exp_f32 v182, v182\n"
      "v_mfma_f32_16x16x32_bf16 v[88:91], a[180:183], v[128:131], v[88:91]\n"
      "v_exp_f32 v168, v168\n"
      "v_mfma_f32_16x16x32_bf16 v[88:91], a[184:187], v[56:59], v[88:91]\n"
      "s_waitcnt lgkmcnt(0)\n"
      "v_mul_f32_dpp v77, v122, v77 row_newbcast:4 row_mask:0xf bank_mask:0xf\n"
      "v_mul_f32_dpp v74, v122, v74 row_newbcast:8 row_mask:0xf bank_mask:0xf\n"
      "v_mfma_f32_16x16x32_bf16 v[88:91], a[188:191], v[124:127], v[88:91]\n"
      "v_exp_f32 v144, v144\n"
      "v_mfma_f32_16x16x32_bf16 v[148:151], a[144:147], v[22:25], 0\n"
      "v_exp_f32 v158, v158\n"
      "v_mfma_f32_16x16x32_bf16 v[148:151], a[148:151], v[128:131], v[148:151]\n"
      "v_pk_max_i16 v115, v115, 0\n"
      "v_mul_f32_dpp v72, v122, v72 row_newbcast:0 row_mask:0xf bank_mask:0xf\n"
      "v_mfma_f32_16x16x32_bf16 v[148:151], a[152:155], v[56:59], v[148:151]\n"
      "v_mul_f32_dpp v79, v122, v79 row_newbcast:12 row_mask:0xf bank_mask:0xf\n"
      "v_mul_f32_dpp v80, v122, v80 row_newbcast:0 row_mask:0xf bank_mask:0xf\n"
      "v_mfma_f32_16x16x32_bf16 v[148:151], a[156:159], v[124:127], v[148:151]\n"
      "v_exp_f32 v185, v185\n"
      "v_mfma_f32_16x16x32_bf16 v[84:87], a[128:131], v[22:25], 0\n"
      "v_pk_max_i16 v113, v113, 0\n"
      "v_mul_f32_dpp v75, v122, v75 row_newbcast:12 row_mask:0xf bank_mask:0xf\n"
      "v_mfma_f32_16x16x32_bf16 v[84:87], a[132:135], v[128:131], v[84:87]\n"
      "v_mul_f32_dpp v73, v122, v73 row_newbcast:4 row_mask:0xf bank_mask:0xf\n"
      "v_mul_f32_dpp v69, v122, v69 row_newbcast:4 row_mask:0xf bank_mask:0xf\n"
      "v_mfma_f32_16x16x32_bf16 v[84:87], a[136:139], v[56:59], v[84:87]\n"
      "v_exp_f32 v145, v145\n"
      "v_mfma_f32_16x16x32_bf16 v[84:87], a[140:143], v[124:127], v[84:87]\n"
      "v_mul_f32_dpp v70, v122, v70 row_newbcast:8 row_mask:0xf bank_mask:0xf\n"
      "v_cvt_pk_bf16_f32 v117, v184, v185\n"
      "v_mfma_f32_16x16x32_bf16 v[72:75], v[18:21], a[208:211], v[72:75]\n"
      "v_exp_f32 v161, v161\n"
      "v_mfma_f32_16x16x32_bf16 v[72:75], v[174:177], a[212:215], v[72:75]\n"
      "v_mul_f32_dpp v82, v122, v82 row_newbcast:8 row_mask:0xf bank_mask:0xf\n"
      "v_mul_f32_dpp v71, v122, v71 row_newbcast:12 row_mask:0xf bank_mask:0xf\n"
      "v_mfma_f32_16x16x32_bf16 v[72:75], v[178:181], a[216:219], v[72:75]\n"
      "v_exp_f32 v183, v183\n"
      "v_mfma_f32_16x16x32_bf16 v[72:75], v[26:29], a[220:223], v[72:75]\n"
      "v_cvt_pk_bf16_f32 v116, v182, v183\n"
      "v_cvt_pk_bf16_f32 v119, v146, v147\n"
      "v_mfma_f32_16x16x32_bf16 v[182:185], a[160:163], v[22:25], 0\n"
      "v_mul_f32_dpp v83, v122, v83 row_newbcast:12 row_mask:0xf bank_mask:0xf\n"
      "v_mul_f32_dpp v68, v122, v68 row_newbcast:0 row_mask:0xf bank_mask:0xf\n"
      "v_mfma_f32_16x16x32_bf16 v[182:185], a[164:167], v[128:131], v[182:185]\n"
      "v_exp_f32 v159, v159\n"
      "v_mfma_f32_16x16x32_bf16 v[182:185], a[168:171], v[56:59], v[182:185]\n"
      "v_exp_f32 v102, v102\n"
      "v_mfma_f32_16x16x32_bf16 v[182:185], a[172:175], v[124:127], v[182:185]\n"
      "buffer_store_dwordx2 v[112:113], v2, s[44:47], s75 offen offset:0 nt sc1\n"
      "v_cvt_pk_bf16_f32 v118, v144, v145\n"
      "v_mfma_f32_16x16x32_bf16 v[68:71], v[18:21], a[192:195], v[68:71]\n"
      "v_mul_f32_dpp v81, v122, v81 row_newbcast:4 row_mask:0xf bank_mask:0xf\n"
      "v_cvt_pk_bf16_f32 v144, v84, v85\n"
      "v_mfma_f32_16x16x32_bf16 v[68:71], v[174:177], a[196:199], v[68:71]\n"
      "v_cvt_pk_bf16_f32 v147, v150, v151\n"
      "v_cvt_pk_bf16_f32 v146, v148, v149\n"
      "v_mfma_f32_16x16x32_bf16 v[68:71], v[178:181], a[200:203], v[68:71]\n"
      "L_h8_%=:\n"
      "s_cbranch_execz L_hr_%=\n"
      "v_cvt_pk_bf16_f32 v145, v86, v87\n"
      "v_maximum3_f32 v8, v8, v120, v121\n"
      "v_mfma_f32_16x16x32_bf16 v[68:71], v[26:29], a[204:207], v[68:71]\n"
      "v_cvt_pk_bf16_f32 v150, v88, v89\n"
      "v_mul_f32_dpp v78, v122, v78 row_newbcast:8 row_mask:0xf bank_mask:0xf\n"
      "v_mfma_f32_16x16x32_bf16 v[80:83], v[18:21], a[240:243], v[80:83]\n"
      "v_exp_f32 v170, v170\n"
      "v_mfma_f32_16x16x32_bf16 v[80:83], v[174:177], a[244:247], v[80:83]\n"
      "v_mul_f32_dpp v76, v122, v76 row_newbcast:0 row_mask:0xf bank_mask:0xf\n"
      "v_mov_b64 v[110:111], 0\n"
      "v_mfma_f32_16x16x32_bf16 v[80:83], v[178:181], a[248:251], v[80:83]\n"
      "v_cvt_pk_bf16_f32 v148, v182, v183\n"
      "v_cvt_pk_bf16_f32 v120, v166, v167\n"
      "v_mfma_f32_16x16x32_bf16 v[80:83], v[26:29], a[252:255], v[80:83]\n"
      "v_cvt_pk_bf16_f32 v70, v70, v74\n"
      "v_cvt_pk_bf16_f32 v122, v102, v103\n"
      "v_mfma_f32_16x16x32_bf16 v[76:79], v[18:21], a[224:227], v[76:79]\n"
      "v_cvt_pk_bf16_f32 v68, v68, v72\n"
      "v_pk_max_i16 v70, v70, 0\n"
      "v_mfma_f32_16x16x32_bf16 v[76:79], v[174:177], a[228:231], v[76:79]\n"
      "v_cvt_pk_bf16_f32 v72, v69, v73\n"
      "v_cvt_pk_bf16_f32 v121, v168, v169\n"
      "v_mfma_f32_16x16x32_bf16 v[76:79], v[178:181], a[232:235], v[76:79]\n"
      "v_cvt_pk_bf16_f32 v151, v90, v91\n"
      "v_cvt_pk_bf16_f32 v149, v184, v185\n"
      "v_mfma_f32_16x16x32_bf16 v[76:79], v[26:29], a[236:239], v[76:79]\n"
      "v_exp_f32 v105, v105\n"
      "v_mfma_f32_16x16x32_bf16 v[26:29], v[232:235], v[144:147], 0\n"
      "v_cvt_pk_bf16_f32 v123, v104, v105\n"
      "v_pk_max_i16 v68, v68, 0\n"
      "v_mfma_f32_16x16x32_bf16 v[26:29], v[236:239], v[148:151], v[26:29]\n"
      "ds_read_b128 v[18:21], v0 offset:0\n"
      "v_cvt_pk_bf16_f32 v84, v170, v171\n"
      "v_mfma_f32_16x16x32_bf16 v[174:177], v[224:227], v[144:147], 0\n"
      "ds_read_b128 v[88:91], v0 offset:1536\n"
      "v_cvt_pk_bf16_f32 v85, v172, v173\n"
      "v_mfma_f32_16x16x32_bf16 v[174:177], v[228:231], v[148:151], v[174:177]\n"
      "v_cvt_pk_bf16_f32 v74, v71, v75\n"
      "v_cvt_pk_bf16_f32 v75, v79, v83\n"
      "v_smfmac_f32_16x16x64_bf16 v[214:217], v[12:15], v[116:123], v5\n"
      "buffer_store_dwordx2 v[114:115], v2, s[44:47], s75 offen offset:512 nt sc1\n"
      "v_mov_b64 v[112:113], 0\n"
      "v_mfma_f32_16x16x32_bf16 v[178:181], v[202:205], v[144:147], 0\n"
      "v_cvt_pk_bf16_f32 v86, v158, v159\n"
      "v_cvt_pk_bf16_f32 v71, v78, v82\n"
      "v_mfma_f32_16x16x32_bf16 v[178:181], v[206:209], v[148:151], v[178:181]\n"
      "v_exp_f32 v26, v26\n"
      "v_mfma_f32_16x16x32_bf16 v[152:155], v[94:97], v[144:147], 0\n"
      "v_pk_max_i16 v75, v75, 0\n"
      "v_cvt_pk_bf16_f32 v73, v77, v81\n"
      "v_mfma_f32_16x16x32_bf16 v[152:155], v[98:101], v[148:151], v[152:155]\n"
      "v_cvt_pk_bf16_f32 v69, v76, v80\n"
      "v_cvt_pk_bf16_f32 v87, v160, v161\n"
      "v_mfma_f32_16x16x32_bf16 v[80:83], v[60:63], v[144:147], 0\n"
      "v_pk_max_i16 v71, v71, 0\n"
      "v_pk_max_i16 v69, v69, 0\n"
      "v_mfma_f32_16x16x32_bf16 v[80:83], v[64:67], v[148:151], v[80:83]\n"
      "v_pk_max_i16 v74, v74, 0\n"
      "v_pk_max_i16 v72, v72, 0\n"
      "v_smfmac_f32_16x16x32_bf16 v[214:217], v[12:13], v[84:87], v5\n"
      "s_add_u32 s44, s44, 102400\n s_addc_u32 s45, s45, 0\n"
      "ds_read_b128 v[76:79], v0 offset:1024\n"
      "v_mfma_f32_16x16x4_4b_bf16 v[156:171], v[92:93], v[30:31], 0 cbsz:2 abid:2\n"
      "v_exp_f32 v174, v174\n"
      "v_exp_f32 v180, v180\n"
      "v_exp_f32 v29, v29\n"
      "v_exp_f32 v155, v155\n"
      "v_smfmac_f32_16x16x64_bf16 v[110:113], v[198:201], v[144:151], v5\n"
      "buffer_store_dwordx2 v[68:69], v2, s[44:47], s100 offen offset:0 nt sc1\n"
      "v_add_f32 v214, v214, v215\n"
      "v_mfma_f32_16x16x32_bf16 v[198:201], v[190:193], v[144:147], 0\n"
      "v_rcp_f32 v215, v214\n"
      "v_mfma_f32_16x16x32_bf16 v[198:201], v[194:197], v[148:151], v[198:201]\n"
      "buffer_store_dwordx2 v[70:71], v2, s[44:47], s100 offen offset:512 nt sc1\n"
      "ds_read_b128 v[102:105], v0 offset:512\n"
      "v_mfma_f32_16x16x32_bf16 v[160:163], v[116:119], v[40:43], v[160:163]\n"
      "ds_bpermute_b32 v216, v4, v215 offset:128\n"
      "v_mfma_f32_16x16x32_bf16 v[160:163], v[120:123], v[44:47], v[160:163]\n"
      "v_exp_f32 v179, v179\n"
      "v_mfma_f32_16x16x32_bf16 v[160:163], v[84:87], v[244:247], v[160:163]\n"
      "v_exp_f32 v177, v177\n"
      "v_mfma_f32_16x16x32_bf16 v[168:171], v[116:119], v[48:51], v[168:171]\n"
      "v_exp_f32 v198, v198\n"
      "v_mfma_f32_16x16x32_bf16 v[168:171], v[120:123], v[52:55], v[168:171]\n"
      "v_exp_f32 v176, v176\n"
      "v_mfma_f32_16x16x32_bf16 v[168:171], v[84:87], v[106:109], v[168:171]\n"
      "v_exp_f32 v154, v154\n"
      "v_mfma_f32_16x16x32_bf16 v[156:159], v[116:119], v[132:135], v[156:159]\n"
      "v_exp_f32 v80, v80\n"
      "v_mfma_f32_16x16x32_bf16 v[156:159], v[120:123], v[136:139], v[156:159]\n"
      "v_exp_f32 v28, v28\n"
      "v_mfma_f32_16x16x32_bf16 v[156:159], v[84:87], v[210:213], v[156:159]\n"
      "v_exp_f32 v201, v201\n"
      "v_mfma_f32_16x16x32_bf16 v[164:167], v[116:119], v[32:35], v[164:167]\n"
      "v_exp_f32 v152, v152\n"
      "v_mfma_f32_16x16x32_bf16 v[164:167], v[120:123], v[36:39], v[164:167]\n"
      "v_exp_f32 v82, v82\n"
      "v_mfma_f32_16x16x32_bf16 v[164:167], v[84:87], v[248:251], v[164:167]\n"
      "s_waitcnt lgkmcnt(0)\n"
      "v_mul_f32_dpp v170, v216, v170 row_newbcast:8 row_mask:0xf bank_mask:0xf\n"
      "v_mul_f32_dpp v156, v216, v156 row_newbcast:0 row_mask:0xf bank_mask:0xf\n"
      "v_mfma_f32_16x16x32_bf16 v[120:123], a[144:147], v[18:21], 0\n"
      "v_mul_f32_dpp v157, v216, v157 row_newbcast:4 row_mask:0xf bank_mask:0xf\n"
      "v_mul_f32_dpp v158, v216, v158 row_newbcast:8 row_mask:0xf bank_mask:0xf\n"
      "v_mfma_f32_16x16x32_bf16 v[120:123], a[148:151], v[102:105], v[120:123]\n"
      "v_exp_f32 v153, v153\n"
      "v_mfma_f32_16x16x32_bf16 v[120:123], a[152:155], v[76:79], v[120:123]\n"
      "v_exp_f32 v175, v175\n"
      "v_mfma_f32_16x16x32_bf16 v[120:123], a[156:159], v[88:91], v[120:123]\n"
      "v_mul_f32_dpp v162, v216, v162 row_newbcast:8 row_mask:0xf bank_mask:0xf\n"
      "v_mul_f32_dpp v164, v216, v164 row_newbcast:0 row_mask:0xf bank_mask:0xf\n"
      "v_mfma_f32_16x16x32_bf16 v[84:87], a[128:131], v[18:21], 0\n"
      "v_mul_f32_dpp v166, v216, v166 row_newbcast:8 row_mask:0xf bank_mask:0xf\n"
      "v_mul_f32_dpp v159, v216, v159 row_newbcast:12 row_mask:0xf bank_mask:0xf\n"
      "v_mfma_f32_16x16x32_bf16 v[84:87], a[132:135], v[102:105], v[84:87]\n"
      "v_mul_f32_dpp v171, v216, v171 row_newbcast:12 row_mask:0xf bank_mask:0xf\n"
      "v_mul_f32_dpp v168, v216, v168 row_newbcast:0 row_mask:0xf bank_mask:0xf\n"
      "v_mfma_f32_16x16x32_bf16 v[84:87], a[136:139], v[76:79], v[84:87]\n"
      "v_mul_f32_dpp v167, v216, v167 row_newbcast:12 row_mask:0xf bank_mask:0xf\n"
      "v_cvt_pk_bf16_f32 v118, v120, v121\n"
      "v_mfma_f32_16x16x32_bf16 v[84:87], a[140:143], v[88:91], v[84:87]\n"
      "v_exp_f32 v178, v178\n"
      "v_mfma_f32_16x16x32_bf16 v[182:185], a[176:179], v[18:21], 0\n"
      "v_exp_f32 v181, v181\n"
      "v_mfma_f32_16x16x32_bf16 v[182:185], a[180:183], v[102:105], v[182:185]\n"
      "v_pk_max_i16 v73, v73, 0\n"
      "v_mul_f32_dpp v169, v216, v169 row_newbcast:4 row_mask:0xf bank_mask:0xf\n"
      "v_mfma_f32_16x16x32_bf16 v[182:185], a[184:187], v[76:79], v[182:185]\n"
      "v_exp_f32 v27, v27\n"
      "v_mfma_f32_16x16x32_bf16 v[182:185], a[188:191], v[88:91], v[182:185]\n"
      "v_cvt_pk_bf16_f32 v117, v86, v87\n"
      "v_cvt_pk_bf16_f32 v116, v84, v85\n"
      "v_mfma_f32_16x16x32_bf16 v[84:87], a[160:163], v[18:21], 0\n"
      "v_cvt_pk_bf16_f32 v119, v122, v123\n"
      "v_mul_f32_dpp v160, v216, v160 row_newbcast:0 row_mask:0xf bank_mask:0xf\n"
      "v_mfma_f32_16x16x32_bf16 v[84:87], a[164:167], v[102:105], v[84:87]\n"
      "v_exp_f32 v200, v200\n"
      "v_mfma_f32_16x16x32_bf16 v[84:87], a[168:171], v[76:79], v[84:87]\n"
      "v_exp_f32 v81, v81\n"
      "v_mfma_f32_16x16x32_bf16 v[84:87], a[172:175], v[88:91], v[84:87]\n"
      "v_cvt_pk_bf16_f32 v151, v180, v181\n"
      "v_mul_f32_dpp v161, v216, v161 row_newbcast:4 row_mask:0xf bank_mask:0xf\n"
      "v_mfma_f32_16x16x32_bf16 v[156:159], v[186:189], a[192:195], v[156:159]\n"
      "v_cvt_pk_bf16_f32 v150, v178, v179\n"
      "v_cvt_pk_bf16_f32 v71, v154, v155\n"
      "v_mfma_f32_16x16x32_bf16 v[156:159], v[220:223], a[196:199], v[156:159]\n"
      "v_cvt_pk_bf16_f32 v122, v182, v183\n"
      "v_cvt_pk_bf16_f32 v123, v184, v185\n"
      "v_mfma_f32_16x16x32_bf16 v[156:159], v[252:255], a[200:203], v[156:159]\n"
      "v_cvt_pk_bf16_f32 v120, v84, v85\n"
      "v_cvt_pk_bf16_f32 v121, v86, v87\n"
      "v_mfma_f32_16x16x32_bf16 v[156:159], v[140:143], a[204:207], v[156:159]\n"
      "v_cvt_pk_bf16_f32 v146, v174, v175\n"
      "v_cvt_pk_bf16_f32 v69, v28, v29\n"
      "v_smfmac_f32_16x16x64_bf16 v[110:113], v[240:243], v[116:123], v5\n"
      "buffer_store_dwordx2 v[74:75], v2, s[44:47], s100 offen offset:768 nt sc1\n"
      "v_cvt_pk_bf16_f32 v147, v176, v177\n"
      "v_mfma_f32_16x16x32_bf16 v[240:243], v[224:227], v[116:119], 0\n"
      "v_exp_f32 v199, v199\n"
      "v_mfma_f32_16x16x32_bf16 v[240:243], v[228:231], v[120:123], v[240:243]\n"
      "v_exp_f32 v83, v83\n"
      "v_mfma_f32_16x16x32_bf16 v[176:179], v[60:63], v[116:119], 0\n"
      "v_cvt_pk_bf16_f32 v68, v26, v27\n"
      "v_add_f32 v110, v110, v111\n"
      "v_mfma_f32_16x16x32_bf16 v[176:179], v[64:67], v[120:123], v[176:179]\n"
      "v_exp_f32 v110, v110\n"
      "v_mfma_f32_16x16x32_bf16 v[26:29], v[190:193], v[116:119], 0\n"
      "v_add_f32 v112, v112, v113\n"
      "v_cvt_pk_bf16_f32 v70, v152, v153\n"
      "v_mfma_f32_16x16x32_bf16 v[26:29], v[194:197], v[120:123], v[26:29]\n"
      "v_exp_f32 v112, v112\n"
      "v_mul_f32_dpp v163, v216, v163 row_newbcast:12 row_mask:0xf bank_mask:0xf\n"
      "v_cvt_pk_bf16_f32 v197, v110, v112\n"
      "v_mfma_f32_16x16x32_bf16 v[64:67], v[202:205], v[116:119], 0\n"
      "v_add_f32 v85, v110, v112\n"
      "v_cvt_pk_bf16_f32 v148, v80, v81\n"
      "v_mfma_f32_16x16x32_bf16 v[64:67], v[206:209], v[120:123], v[64:67]\n"
      "v_exp_f32 v178, v178\n"
      "v_mfma_f32_16x16x32_bf16 v[60:63], v[94:97], v[116:119], 0\n"
      "v_add_f32 v84, v110, v112\n"
      "v_cvt_pk_bf16_f32 v149, v82, v83\n"
      "v_mfma_f32_16x16x32_bf16 v[60:63], v[98:101], v[120:123], v[60:63]\n"
      "v_permlane16_swap_b32 v84, v85\n"
      "v_mfma_f32_16x16x32_bf16 v[160:163], v[186:189], a[208:211], v[160:163]\n"
      "s_nop 0\n"
      "v_exp_f32 v67, v67\n"
      "v_mfma_f32_16x16x32_bf16 v[160:163], v[220:223], a[212:215], v[160:163]\n"
      "v_cvt_pk_bf16_f32 v144, v198, v199\n"
      "v_cvt_pk_bf16_f32 v196, v110, v112\n"
      "v_mfma_f32_16x16x32_bf16 v[160:163], v[252:255], a[216:219], v[160:163]\n"
      "v_cvt_pk_bf16_f32 v145, v200, v201\n"
      "v_mov_b64 v[96:97], v[84:85]\n"
      "v_mfma_f32_16x16x32_bf16 v[160:163], v[140:143], a[220:223], v[160:163]\n"
      "v_permlane16_swap_b32 v196, v197\n"
      "v_smfmac_f32_16x16x64_bf16 v[84:87], v[12:15], v[144:151], v5\n"
      "buffer_store_dwordx2 v[72:73], v2, s[44:47], s100 offen offset:256 nt sc1\n"
      "v_mul_f32_dpp v165, v216, v165 row_newbcast:4 row_mask:0xf bank_mask:0xf\n"
      "v_mfma_f32_16x16x32_bf16 v[80:83], v[232:235], v[116:119], 0\n"
      "v_exp_f32 v27, v27\n"
      "v_mfma_f32_16x16x32_bf16 v[80:83], v[236:239], v[120:123], v[80:83]\n"
      "v_exp_f32 v29, v29\n"
      "v_mfma_f32_16x16x4_4b_bf16 v[224:239], v[196:197], v[30:31], 0 cbsz:2 abid:0\n"
      "v_exp_f32 v63, v63\n"
      "v_exp_f32 v28, v28\n"
      "v_cvt_pk_bf16_f32 v158, v158, v162\n"
      "v_cvt_pk_bf16_f32 v162, v159, v163\n"
      "v_smfmac_f32_16x16x32_bf16 v[84:87], v[12:13], v[68:71], v5\n"
      "v_pk_max_i16 v162, v162, 0\n"
      "v_exp_f32 v242, v242\n"
      "v_mfma_f32_16x16x32_bf16 v[168:171], v[186:189], a[240:243], v[168:171]\n"
      "v_exp_f32 v83, v83\n"
      "v_mfma_f32_16x16x32_bf16 v[168:171], v[220:223], a[244:247], v[168:171]\n"
      "v_exp_f32 v243, v243\n"
      "v_mfma_f32_16x16x32_bf16 v[168:171], v[252:255], a[248:251], v[168:171]\n"
      "v_exp_f32 v64, v64\n"
      "v_mfma_f32_16x16x32_bf16 v[168:171], v[140:143], a[252:255], v[168:171]\n"
      "v_add_f32 v84, v84, v85\n"
      "v_exp_f32 v241, v241\n"
      "v_mfma_f32_16x16x32_bf16 v[164:167], v[186:189], a[224:227], v[164:167]\n"
      "v_rcp_f32 v85, v84\n"
      "v_mfma_f32_16x16x32_bf16 v[164:167], v[220:223], a[228:231], v[164:167]\n"
      "v_cvt_pk_bf16_f32 v156, v156, v160\n"
      "v_cvt_pk_bf16_f32 v160, v157, v161\n"
      "v_mfma_f32_16x16x32_bf16 v[164:167], v[252:255], a[232:235], v[164:167]\n"
      "ds_bpermute_b32 v86, v4, v85\n"
      "v_mfma_f32_16x16x32_bf16 v[164:167], v[140:143], a[236:239], v[164:167]\n"
      "v_exp_f32 v81, v81\n"
      "v_mfma_f32_16x16x32_bf16 v[232:235], v[144:147], v[32:35], v[232:235]\n"
      "v_pk_max_i16 v158, v158, 0\n"
      "v_pk_max_i16 v160, v160, 0\n"
      "v_mfma_f32_16x16x32_bf16 v[232:235], v[148:151], v[36:39], v[232:235]\n"
      "v_exp_f32 v66, v66\n"
      "v_mfma_f32_16x16x32_bf16 v[232:235], v[68:71], v[248:251], v[232:235]\n"
      "s_nop 0\n"
      "v_cvt_pk_bf16_f32 v163, v167, v171\n"
      "v_cvt_pk_bf16_f32 v159, v166, v170\n"
      "v_mfma_f32_16x16x32_bf16 v[236:239], v[144:147], v[48:51], v[236:239]\n"
      "v_pk_max_i16 v159, v159, 0\n"
      "v_pk_max_i16 v163, v163, 0\n"
      "v_mfma_f32_16x16x32_bf16 v[236:239], v[148:151], v[52:55], v[236:239]\n"
      "v_exp_f32 v179, v179\n"
      "v_mfma_f32_16x16x32_bf16 v[236:239], v[68:71], v[106:109], v[236:239]\n"
      "buffer_store_dwordx2 v[158:159], v2, s[44:47], s101 offen offset:512 nt sc1\n"
      "v_pk_max_i16 v156, v156, 0\n"
      "v_exp_f32 v80, v80\n"
      "v_mfma_f32_16x16x32_bf16 v[228:231], v[144:147], v[40:43], v[228:231]\n"
      "v_exp_f32 v65, v65\n"
      "v_mfma_f32_16x16x32_bf16 v[228:231], v[148:151], v[44:47], v[228:231]\n"
      "s_waitcnt lgkmcnt(0)\n"
      "v_mul_f32_dpp v234, v86, v234 row_newbcast:8 row_mask:0xf bank_mask:0xf\n"
      "v_cvt_pk_bf16_f32 v161, v165, v169\n"
      "v_mfma_f32_16x16x32_bf16 v[228:231], v[68:71], v[244:247], v[228:231]\n"
      "buffer_store_dwordx2 v[162:163], v2, s[44:47], s101 offen offset:768 nt sc1\n"
      "v_pk_max_i16 v161, v161, 0\n"
      "v_mfma_f32_16x16x32_bf16 v[224:227], v[144:147], v[132:135], v[224:227]\n"
      "v_exp_f32 v240, v240\n"
      "v_mfma_f32_16x16x32_bf16 v[224:227], v[148:151], v[136:139], v[224:227]\n"
      "v_exp_f32 v62, v62\n"
      "v_mfma_f32_16x16x32_bf16 v[224:227], v[68:71], v[210:213], v[224:227]\n"
      "v_mul_f32_dpp v237, v86, v237 row_newbcast:4 row_mask:0xf bank_mask:0xf\n"
      "v_mul_f32_dpp v230, v86, v230 row_newbcast:8 row_mask:0xf bank_mask:0xf\n"
      "v_exp_f32 v60, v60\n"
      "v_mul_f32_dpp v235, v86, v235 row_newbcast:12 row_mask:0xf bank_mask:0xf\n"
      "v_mfma_f32_16x16x4_4b_bf16 v[180:195], v[196:197], v[30:31], 0 cbsz:2 abid:2\n"
      "v_mul_f32_dpp v233, v86, v233 row_newbcast:4 row_mask:0xf bank_mask:0xf\n"
      "v_mul_f32_dpp v229, v86, v229 row_newbcast:4 row_mask:0xf bank_mask:0xf\n"
      "v_mul_f32_dpp v228, v86, v228 row_newbcast:0 row_mask:0xf bank_mask:0xf\n"
      "v_mul_f32_dpp v231, v86, v231 row_newbcast:12 row_mask:0xf bank_mask:0xf\n"
      "v_exp_f32 v177, v177\n"
      "v_cvt_pk_bf16_f32 v252, v80, v81\n"
      "v_mul_f32_dpp v232, v86, v232 row_newbcast:0 row_mask:0xf bank_mask:0xf\n"
      "v_mfma_f32_16x16x32_bf16 v[228:231], v[22:25], a[208:211], v[228:231]\n"
      "v_mul_f32_dpp v225, v86, v225 row_newbcast:4 row_mask:0xf bank_mask:0xf\n"
      "v_mul_f32_dpp v227, v86, v227 row_newbcast:12 row_mask:0xf bank_mask:0xf\n"
      "v_mfma_f32_16x16x32_bf16 v[228:231], v[128:131], a[212:215], v[228:231]\n"
      "v_exp_f32 v176, v176\n"
      "v_mfma_f32_16x16x32_bf16 v[228:231], v[56:59], a[216:219], v[228:231]\n"
      "v_exp_f32 v26, v26\n"
      "v_mfma_f32_16x16x32_bf16 v[228:231], v[124:127], a[220:223], v[228:231]\n"
      "buffer_store_dwordx2 v[160:161], v2, s[44:47], s101 offen offset:256 nt sc1\n"
      "v_cvt_pk_bf16_f32 v75, v66, v67\n"
      "v_mfma_f32_16x16x32_bf16 v[232:235], v[22:25], a[224:227], v[232:235]\n"
      "v_cvt_pk_bf16_f32 v70, v240, v241\n"
      "v_mul_f32_dpp v224, v86, v224 row_newbcast:0 row_mask:0xf bank_mask:0xf\n"
      "v_mfma_f32_16x16x32_bf16 v[232:235], v[128:131], a[228:231], v[232:235]\n"
      "v_cvt_pk_bf16_f32 v68, v26, v27\n"
      "v_mul_f32_dpp v226, v86, v226 row_newbcast:8 row_mask:0xf bank_mask:0xf\n"
      "v_mfma_f32_16x16x32_bf16 v[232:235], v[56:59], a[232:235], v[232:235]\n"
      "v_cvt_pk_bf16_f32 v72, v176, v177\n"
      "v_cvt_pk_bf16_f32 v74, v64, v65\n"
      "v_mfma_f32_16x16x32_bf16 v[232:235], v[124:127], a[236:239], v[232:235]\n"
      "v_cvt_pk_bf16_f32 v71, v242, v243\n"
      "v_mul_f32_dpp v238, v86, v238 row_newbcast:8 row_mask:0xf bank_mask:0xf\n"
      "v_mfma_f32_16x16x32_bf16 v[224:227], v[22:25], a[192:195], v[224:227]\n"
      "v_cvt_pk_bf16_f32 v69, v28, v29\n"
      "v_mul_f32_dpp v236, v86, v236 row_newbcast:0 row_mask:0xf bank_mask:0xf\n"
      "v_mfma_f32_16x16x32_bf16 v[224:227], v[128:131], a[196:199], v[224:227]\n"
      "v_mul_f32_dpp v239, v86, v239 row_newbcast:12 row_mask:0xf bank_mask:0xf\n"
      "v_cvt_pk_bf16_f32 v157, v164, v168\n"
      "v_mfma_f32_16x16x32_bf16 v[224:227], v[56:59], a[200:203], v[224:227]\n"
      "v_pk_max_i16 v157, v157, 0\n"
      "v_cvt_pk_bf16_f32 v73, v178, v179\n"
      "v_mfma_f32_16x16x32_bf16 v[224:227], v[124:127], a[204:207], v[224:227]\n"
      "v_exp_f32 v61, v61\n"
      "v_smfmac_f32_16x16x64_bf16 v[96:99], v[12:15], v[68:75], v5\n"
      "buffer_store_dwordx2 v[156:157], v2, s[44:47], s101 offen offset:0 nt sc1\n"
      "v_exp_f32 v82, v82\n"
      "v_cvt_pk_bf16_f32 v255, v62, v63\n"
      "v_mfma_f32_16x16x32_bf16 v[236:239], v[22:25], a[240:243], v[236:239]\n"
      "v_cvt_pk_bf16_f32 v253, v82, v83\n"
      "v_cvt_pk_bf16_f32 v254, v60, v61\n"
      "v_mfma_f32_16x16x32_bf16 v[236:239], v[128:131], a[244:247], v[236:239]\n"
      "v_cvt_pk_bf16_f32 v226, v226, v230\n"
      "v_maximum3_f32 v8, v8, v214, v215\n"
      "v_smfmac_f32_16x16x32_bf16 v[96:99], v[12:13], v[252:255], v5\n"
      "v_cvt_pk_bf16_f32 v230, v227, v231\n"
      "v_pk_max_i16 v230, v230, 0\n"
      "v_pk_max_i16 v226, v226, 0\n"
      "v_mfma_f32_16x16x32_bf16 v[236:239], v[56:59], a[248:251], v[236:239]\n"
      "v_cvt_pk_bf16_f32 v224, v224, v228\n"
      "v_cvt_pk_bf16_f32 v228, v225, v229\n"
      "v_mfma_f32_16x16x32_bf16 v[236:239], v[124:127], a[252:255], v[236:239]\n"
      "s_nop 0\n"
      "v_add_f32 v96, v96, v97\n"
      "v_rcp_f32 v97, v96\n"
      "v_mfma_f32_16x16x32_bf16 v[180:183], v[68:71], v[132:135], v[180:183]\n"
      "s_not_b64 vcc, vcc\n"
      "ds_bpermute_b32 v98, v4, v97 offset:128\n"
      "v_mfma_f32_16x16x32_bf16 v[180:183], v[72:75], v[136:139], v[180:183]\n"
      "s_nop 0\n"
      "v_cvt_pk_bf16_f32 v227, v234, v238\n"
      "v_pk_max_i16 v227, v227, 0\n"
      "v_mfma_f32_16x16x32_bf16 v[180:183], v[252:255], v[210:213], v[180:183]\n"
      "buffer_store_dwordx2 v[226:227], v2, s[44:47], s34 offen offset:512 nt sc1\n"
      "v_mfma_f32_16x16x32_bf16 v[192:195], v[68:71], v[48:51], v[192:195]\n"
      "v_cvt_pk_bf16_f32 v229, v233, v237\n"
      "v_mfma_f32_16x16x32_bf16 v[192:195], v[72:75], v[52:55], v[192:195]\n"
      "v_mfma_f32_16x16x32_bf16 v[192:195], v[252:255], v[106:109], v[192:195]\n"
      "v_cvt_pk_bf16_f32 v231, v235, v239\n"
      "v_pk_max_i16 v231, v231, 0\n"
      "v_mfma_f32_16x16x32_bf16 v[184:187], v[68:71], v[40:43], v[184:187]\n"
      "v_mfma_f32_16x16x32_bf16 v[184:187], v[72:75], v[44:47], v[184:187]\n"
      "v_pk_max_i16 v228, v228, 0\n"
      "v_pk_max_i16 v229, v229, 0\n"
      "v_mfma_f32_16x16x32_bf16 v[184:187], v[252:255], v[244:247], v[184:187]\n"
      "buffer_store_dwordx2 v[230:231], v2, s[44:47], s34 offen offset:768 nt sc1\n"
      "buffer_store_dwordx2 v[228:229], v2, s[44:47], s34 offen offset:256 nt sc1\n"
      "v_mfma_f32_16x16x32_bf16 v[188:191], v[68:71], v[32:35], v[188:191]\n"
      "s_waitcnt lgkmcnt(0)\n"
      "v_mul_f32_dpp v181, v98, v181 row_newbcast:4 row_mask:0xf bank_mask:0xf\n"
      "v_mul_f32_dpp v195, v98, v195 row_newbcast:12 row_mask:0xf bank_mask:0xf\n"
      "v_mfma_f32_16x16x32_bf16 v[188:191], v[72:75], v[36:39], v[188:191]\n"
      "v_mul_f32_dpp v193, v98, v193 row_newbcast:4 row_mask:0xf bank_mask:0xf\n"
      "v_mul_f32_dpp v184, v98, v184 row_newbcast:0 row_mask:0xf bank_mask:0xf\n"
      "v_mfma_f32_16x16x32_bf16 v[188:191], v[252:255], v[248:251], v[188:191]\n"
      "v_mul_f32_dpp v192, v98, v192 row_newbcast:0 row_mask:0xf bank_mask:0xf\n"
      "v_mul_f32_dpp v194, v98, v194 row_newbcast:8 row_mask:0xf bank_mask:0xf\n"
      "v_mul_f32_dpp v183, v98, v183 row_newbcast:12 row_mask:0xf bank_mask:0xf\n"
      "v_pk_max_i16 v224, v224, 0\n"
      "v_mfma_f32_16x16x32_bf16 v[192:195], v[18:21], a[240:243], v[192:195]\n"
      "v_cvt_pk_bf16_f32 v225, v232, v236\n"
      "v_mfma_f32_16x16x32_bf16 v[192:195], v[102:105], a[244:247], v[192:195]\n"
      "v_mul_f32_dpp v185, v98, v185 row_newbcast:4 row_mask:0xf bank_mask:0xf\n"
      "v_maximum3_f32 v8, v8, v84, v85\n"
      "v_mfma_f32_16x16x32_bf16 v[192:195], v[76:79], a[248:251], v[192:195]\n"
      "v_mul_f32_dpp v182, v98, v182 row_newbcast:8 row_mask:0xf bank_mask:0xf\n"
      "v_mul_f32_dpp v186, v98, v186 row_newbcast:8 row_mask:0xf bank_mask:0xf\n"
      "v_mfma_f32_16x16x32_bf16 v[192:195], v[88:91], a[252:255], v[192:195]\n"
      "v_mul_f32_dpp v180, v98, v180 row_newbcast:0 row_mask:0xf bank_mask:0xf\n"
      "s_nop 1\n"
      "v_mfma_f32_16x16x32_bf16 v[180:183], v[18:21], a[192:195], v[180:183]\n"
      "v_mul_f32_dpp v190, v98, v190 row_newbcast:8 row_mask:0xf bank_mask:0xf\n"
      "v_mfma_f32_16x16x32_bf16 v[180:183], v[102:105], a[196:199], v[180:183]\n"
      "v_mul_f32_dpp v189, v98, v189 row_newbcast:4 row_mask:0xf bank_mask:0xf\n"
      "v_pk_max_i16 v225, v225, 0\n"
      "v_mfma_f32_16x16x32_bf16 v[180:183], v[76:79], a[200:203], v[180:183]\n"
      "v_mul_f32_dpp v187, v98, v187 row_newbcast:12 row_mask:0xf bank_mask:0xf\n"
      "v_mfma_f32_16x16x32_bf16 v[180:183], v[88:91], a[204:207], v[180:183]\n"
      "buffer_store_dwordx2 v[224:225], v2, s[44:47], s34 offen offset:0 nt sc1\n"
      "v_mul_f32_dpp v188, v98, v188 row_newbcast:0 row_mask:0xf bank_mask:0xf\n"
      "v_mfma_f32_16x16x32_bf16 v[184:187], v[18:21], a[208:211], v[184:187]\n"
      "v_mfma_f32_16x16x32_bf16 v[184:187], v[102:105], a[212:215], v[184:187]\n"
      "v_mfma_f32_16x16x32_bf16 v[184:187], v[76:79], a[216:219], v[184:187]\n"
      "v_mul_f32_dpp v191, v98, v191 row_newbcast:12 row_mask:0xf bank_mask:0xf\n"
      "v_mfma_f32_16x16x32_bf16 v[184:187], v[88:91], a[220:223], v[184:187]\n"
      "s_nop 0\n"
      "v_mfma_f32_16x16x32_bf16 v[188:191], v[18:21], a[224:227], v[188:191]\n"
      "v_mfma_f32_16x16x32_bf16 v[188:191], v[102:105], a[228:231], v[188:191]\n"
      "v_mfma_f32_16x16x32_bf16 v[188:191], v[76:79], a[232:235], v[188:191]\n"
      "v_mfma_f32_16x16x32_bf16 v[188:191], v[88:91], a[236:239], v[188:191]\n"
      "s_nop 2\n"
      "v_cvt_pk_bf16_f32 v182, v182, v186\n"
      "v_maximum3_f32 v8, v8, v96, v97\n"
      "v_pk_max_i16 v182, v182, 0\n"
      "v_cvt_pk_bf16_f32 v180, v180, v184\n"
      "v_cvt_pk_bf16_f32 v186, v183, v187\n"
      "v_cvt_pk_bf16_f32 v183, v190, v194\n"
      "v_pk_max_i16 v183, v183, 0\n"
      "v_cvt_pk_bf16_f32 v184, v181, v185\n"
      "buffer_store_dwordx2 v[182:183], v2, s[44:47], s35 offen offset:512 nt sc1\n"
      "v_cvt_pk_bf16_f32 v187, v191, v195\n"
      "v_cvt_pk_bf16_f32 v185, v189, v193\n"
      "v_pk_max_i16 v180, v180, 0\n"
      "v_pk_max_i16 v184, v184, 0\n"
      "v_cvt_pk_bf16_f32 v181, v188, v192\n"
      "v_pk_max_i16 v186, v186, 0\n"
      "v_pk_max_i16 v185, v185, 0\n"
      "buffer_store_dwordx2 v[184:185], v2, s[44:47], s35 offen offset:256 nt sc1\n"
      "v_pk_max_i16 v181, v181, 0\n"
      "buffer_store_dwordx2 v[180:181], v2, s[44:47], s35 offen offset:0 nt sc1\n"
      "v_pk_max_i16 v187, v187, 0\n"
      "buffer_store_dwordx2 v[186:187], v2, s[44:47], s35 offen offset:768 nt sc1\n"
      "L_h9_%=:\n"
      "s_cbranch_execz L_hr_%=\n"
      "L_end_%=:\n"
      "s_waitcnt vmcnt(0) lgkmcnt(0)\n"
      "v_cmp_nge_f32 vcc, 0x53800000, v8\n"
      "s_nop 4\n"
      "s_cbranch_vccnz L_ai_end\n"
      "s_endpgm\n"
      "L_ai_end:\n"
      "s_mov_b32 s40, s4\n"
      "s_and_b32 s41, s5, 0xffff\n"
      "s_mov_b32 s42, 104857600\n"
      "s_mov_b32 s44, s14\n"
      "s_and_b32 s45, s15, 0xffff\n"
      "s_mov_b32 s46, 104857600\n"
      "s_mov_b32 s48, s18\n"
      "s_add_u32 s49, s18, 102400\n"
      "s_mov_b32 s50, 8\n"
      "s_mov_b32 vcc_lo, 0x55555555\n"
      "s_mov_b32 vcc_hi, 0x55555555\n"
      "v_subrev_u32 v9, 448, v9\n"
      "v_subrev_u32 v10, 448, v10\n"
      "v_subrev_u32 v11, 3584, v11\n"
      "s_add_u32 s52, s48, 0\n"
      "s_add_u32 m0, s53, 0\n"
      "s_nop 0\n"
      "buffer_load_dwordx4 v1, s[40:43], s52 offen offset:0 lds\n"
      "buffer_load_dwordx4 v1, s[40:43], s52 offen offset:2048 lds\n"
      "s_add_u32 m0, s53, 896\n"
      "s_nop 0\n"
      "buffer_load_dwordx4 v1, s[40:43], s52 offen offset:128 lds\n"
      "buffer_load_dwordx4 v1, s[40:43], s52 offen offset:2176 lds\n"
      "s_add_u32 s52, s48, 4096\n"
      "s_add_u32 m0, s53, 4096\n"
      "s_nop 0\n"
      "buffer_load_dwordx4 v1, s[40:43], s52 offen offset:0 lds\n"
      "buffer_load_dwordx4 v1, s[40:43], s52 offen offset:2048 lds\n"
      "s_add_u32 m0, s53, 4992\n"
      "s_nop 0\n"
      "buffer_load_dwordx4 v1, s[40:43], s52 offen offset:128 lds\n"
      "buffer_load_dwordx4 v1, s[40:43], s52 offen offset:2176 lds\n"
      "s_add_u32 s52, s48, 8192\n"
      "s_add_u32 m0, s53, 8192\n"
      "s_nop 0\n"
      "buffer_load_dwordx4 v1, s[40:43], s52 offen offset:0 lds\n"
      "buffer_load_dwordx4 v1, s[40:43], s52 offen offset:2048 lds\n"
      "s_add_u32 m0, s53, 9088\n"
      "s_nop 0\n"
      "buffer_load_dwordx4 v1, s[40:43], s52 offen offset:128 lds\n"
      "buffer_load_dwordx4 v1, s[40:43], s52 offen offset:2176 lds\n"
      "s_add_u32 s52, s48, 12288\n"
      "s_add_u32 m0, s53, 12288\n"
      "s_nop 0\n"
      "buffer_load_dwordx4 v1, s[40:43], s52 offen offset:0 lds\n"
      "buffer_load_dwordx4 v1, s[40:43], s52 offen offset:2048 lds\n"
      "s_add_u32 m0, s53, 13184\n"
      "s_nop 0\n"
      "buffer_load_dwordx4 v1, s[40:43], s52 offen offset:128 lds\n"
      "buffer_load_dwordx4 v1, s[40:43], s52 offen offset:2176 lds\n"
      "s_add_u32 s52, s48, 16384\n"
      "s_add_u32 m0, s53, 16384\n"
      "s_nop 0\n"
      "buffer_load_dwordx4 v1, s[40:43], s52 offen offset:0 lds\n"
      "buffer_load_dwordx4 v1, s[40:43], s52 offen offset:2048 lds\n"
      "s_add_u32 m0, s53, 17280\n"
      "s_nop 0\n"
      "buffer_load_dwordx4 v1, s[40:43], s52 offen offset:128 lds\n"
      "buffer_load_dwordx4 v1, s[40:43], s52 offen offset:2176 lds\n"
      "s_add_u32 s52, s48, 20480\n"
      "s_add_u32 m0, s53, 20480\n"
      "s_nop 0\n"
      "buffer_load_dwordx4 v1, s[40:43], s52 offen offset:0 lds\n"
      "buffer_load_dwordx4 v1, s[40:43], s52 offen offset:2048 lds\n"
      "s_add_u32 m0, s53, 21376\n"
      "s_nop 0\n"
      "buffer_load_dwordx4 v1, s[40:43], s52 offen offset:128 lds\n"
      "buffer_load_dwordx4 v1, s[40:43], s52 offen offset:2176 lds\n"
      "s_waitcnt vmcnt(0)\n"
      "L_safe_%=:\n"
      "s_waitcnt vmcnt(45)\n"
      "ds_read_b128 v[148:151], v0 offset:12288\n"
      "ds_read_b128 v[136:139], v0 offset:12800\n"
      "ds_read_b128 v[140:143], v0 offset:13312\n"
      "ds_read_b128 v[144:147], v0 offset:13824\n"
      "s_waitcnt vmcnt(41)\n"
      "ds_read_b128 v[252:255], v0 offset:16384\n"
      "ds_read_b128 v[48:51], v0 offset:16896\n"
      "ds_read_b128 v[124:127], v0 offset:17408\n"
      "ds_read_b128 v[116:119], v0 offset:17920\n"
      "s_waitcnt lgkmcnt(7)\n"
      "v_mfma_f32_16x16x32_bf16 v[216:219], a[0:3], v[148:151], 0\n"
      "s_waitcnt lgkmcnt(6)\n"
      "v_mfma_f32_16x16x32_bf16 v[216:219], a[4:7], v[136:139], v[216:219]\n"
      "s_waitcnt lgkmcnt(5)\n"
      "v_mfma_f32_16x16x32_bf16 v[216:219], a[8:11], v[140:143], v[216:219]\n"
      "s_waitcnt lgkmcnt(4)\n"
      "v_mfma_f32_16x16x32_bf16 v[216:219], a[12:15], v[144:147], v[216:219]\n"
      "v_mfma_f32_16x16x32_bf16 v[96:99], a[16:19], v[148:151], 0\n"
      "v_mfma_f32_16x16x32_bf16 v[96:99], a[20:23], v[136:139], v[96:99]\n"
      "v_mfma_f32_16x16x32_bf16 v[96:99], a[24:27], v[140:143], v[96:99]\n"
      "v_mfma_f32_16x16x32_bf16 v[96:99], a[28:31], v[144:147], v[96:99]\n"
      "v_mfma_f32_16x16x32_bf16 v[24:27], a[32:35], v[148:151], 0\n"
      "v_mfma_f32_16x16x32_bf16 v[24:27], a[36:39], v[136:139], v[24:27]\n"
      "v_mfma_f32_16x16x32_bf16 v[24:27], a[40:43], v[140:143], v[24:27]\n"
      "v_mfma_f32_16x16x32_bf16 v[24:27], a[44:47], v[144:147], v[24:27]\n"
      "v_cvt_pk_bf16_f32 v92, v216, v217\n"
      "v_cvt_pk_bf16_f32 v93, v218, v219\n"
      "v_mfma_f32_16x16x32_bf16 v[216:219], a[48:51], v[148:151], 0\n"
      "v_mfma_f32_16x16x32_bf16 v[216:219], a[52:55], v[136:139], v[216:219]\n"
      "v_cvt_pk_bf16_f32 v94, v96, v97\n"
      "v_cvt_pk_bf16_f32 v95, v98, v99\n"
      "v_mfma_f32_16x16x32_bf16 v[216:219], a[56:59], v[140:143], v[216:219]\n"
      "v_mfma_f32_16x16x32_bf16 v[216:219], a[60:63], v[144:147], v[216:219]\n"
      "v_cvt_pk_bf16_f32 v96, v24, v25\n"
      "v_cvt_pk_bf16_f32 v97, v26, v27\n"
      "v_mfma_f32_16x16x32_bf16 v[248:251], v[148:151], a[64:67], 0\n"
      "v_mfma_f32_16x16x32_bf16 v[248:251], v[136:139], a[68:71], v[248:251]\n"
      "v_mfma_f32_16x16x32_bf16 v[248:251], v[140:143], a[72:75], v[248:251]\n"
      "v_mfma_f32_16x16x32_bf16 v[248:251], v[144:147], a[76:79], v[248:251]\n"
      "v_mfma_f32_16x16x32_bf16 v[24:27], v[148:151], a[80:83], 0\n"
      "v_mfma_f32_16x16x32_bf16 v[24:27], v[136:139], a[84:87], v[24:27]\n"
      "v_cvt_pk_bf16_f32 v98, v216, v217\n"
      "v_cvt_pk_bf16_f32 v99, v218, v219\n"
      "v_mfma_f32_16x16x32_bf16 v[24:27], v[140:143], a[88:91], v[24:27]\n"
      "v_mfma_f32_16x16x32_bf16 v[24:27], v[144:147], a[92:95], v[24:27]\n"
      "v_mfma_f32_16x16x32_bf16 v[216:219], v[148:151], a[96:99], 0\n"
      "v_mfma_f32_16x16x32_bf16 v[216:219], v[136:139], a[100:103], v[216:219]\n"
      "v_cvt_pk_bf16_f32 v58, v248, v249\n"
      "v_cvt_pk_bf16_f32 v59, v250, v251\n"
      "v_mfma_f32_16x16x32_bf16 v[216:219], v[140:143], a[104:107], v[216:219]\n"
      "v_mfma_f32_16x16x32_bf16 v[216:219], v[144:147], a[108:111], v[216:219]\n"
      "v_mfma_f32_16x16x32_bf16 v[248:251], v[148:151], a[112:115], 0\n"
      "v_mfma_f32_16x16x32_bf16 v[248:251], v[136:139], a[116:119], v[248:251]\n"
      "v_cvt_pk_bf16_f32 v134, v24, v25\n"
      "v_cvt_pk_bf16_f32 v135, v26, v27\n"
      "v_mfma_f32_16x16x32_bf16 v[248:251], v[140:143], a[120:123], v[248:251]\n"
      "v_mfma_f32_16x16x32_bf16 v[248:251], v[144:147], a[124:127], v[248:251]\n"
      "s_waitcnt vmcnt(37)\n"
      "ds_read_b128 v[24:27], v0 offset:20480\n"
      "ds_read_b128 v[152:155], v0 offset:20992\n"
      "ds_read_b128 v[20:23], v0 offset:21504\n"
      "ds_read_b128 v[156:159], v0 offset:22016\n"
      "v_cvt_pk_bf16_f32 v106, v216, v217\n"
      "v_cvt_pk_bf16_f32 v107, v218, v219\n"
      "s_waitcnt lgkmcnt(7)\n"
      "v_mfma_f32_16x16x32_bf16 v[220:223], a[0:3], v[252:255], 0\n"
      "s_waitcnt lgkmcnt(6)\n"
      "v_mfma_f32_16x16x32_bf16 v[220:223], a[4:7], v[48:51], v[220:223]\n"
      "v_cvt_pk_bf16_f32 v198, v248, v249\n"
      "v_cvt_pk_bf16_f32 v199, v250, v251\n"
      "s_waitcnt lgkmcnt(5)\n"
      "v_mfma_f32_16x16x32_bf16 v[220:223], a[8:11], v[124:127], v[220:223]\n"
      "s_waitcnt lgkmcnt(4)\n"
      "v_mfma_f32_16x16x32_bf16 v[220:223], a[12:15], v[116:119], v[220:223]\n"
      "v_mfma_f32_16x16x32_bf16 v[52:55], a[16:19], v[252:255], 0\n"
      "v_mfma_f32_16x16x32_bf16 v[52:55], a[20:23], v[48:51], v[52:55]\n"
      "v_mfma_f32_16x16x32_bf16 v[52:55], a[24:27], v[124:127], v[52:55]\n"
      "v_mfma_f32_16x16x32_bf16 v[52:55], a[28:31], v[116:119], v[52:55]\n"
      "v_mfma_f32_16x16x32_bf16 v[244:247], a[32:35], v[252:255], 0\n"
      "v_mfma_f32_16x16x32_bf16 v[244:247], a[36:39], v[48:51], v[244:247]\n"
      "v_mfma_f32_16x16x32_bf16 v[244:247], a[40:43], v[124:127], v[244:247]\n"
      "v_mfma_f32_16x16x32_bf16 v[244:247], a[44:47], v[116:119], v[244:247]\n"
      "v_cvt_pk_bf16_f32 v216, v220, v221\n"
      "v_cvt_pk_bf16_f32 v217, v222, v223\n"
      "v_mfma_f32_16x16x32_bf16 v[248:251], a[48:51], v[252:255], 0\n"
      "v_mfma_f32_16x16x32_bf16 v[248:251], a[52:55], v[48:51], v[248:251]\n"
      "v_cvt_pk_bf16_f32 v218, v52, v53\n"
      "v_cvt_pk_bf16_f32 v219, v54, v55\n"
      "v_mfma_f32_16x16x32_bf16 v[248:251], a[56:59], v[124:127], v[248:251]\n"
      "v_mfma_f32_16x16x32_bf16 v[248:251], a[60:63], v[116:119], v[248:251]\n"
      "v_cvt_pk_bf16_f32 v220, v244, v245\n"
      "v_cvt_pk_bf16_f32 v221, v246, v247\n"
      "v_mfma_f32_16x16x32_bf16 v[244:247], v[252:255], a[64:67], 0\n"
      "v_mfma_f32_16x16x32_bf16 v[244:247], v[48:51], a[68:71], v[244:247]\n"
      "v_mfma_f32_16x16x32_bf16 v[244:247], v[124:127], a[72:75], v[244:247]\n"
      "v_mfma_f32_16x16x32_bf16 v[244:247], v[116:119], a[76:79], v[244:247]\n"
      "v_mfma_f32_16x16x32_bf16 v[52:55], v[252:255], a[80:83], 0\n"
      "v_mfma_f32_16x16x32_bf16 v[52:55], v[48:51], a[84:87], v[52:55]\n"
      "v_cvt_pk_bf16_f32 v222, v248, v249\n"
      "v_cvt_pk_bf16_f32 v223, v250, v251\n"
      "v_mfma_f32_16x16x32_bf16 v[52:55], v[124:127], a[88:91], v[52:55]\n"
      "v_mfma_f32_16x16x32_bf16 v[52:55], v[116:119], a[92:95], v[52:55]\n"
      "v_mfma_f32_16x16x32_bf16 v[248:251], v[252:255], a[96:99], 0\n"
      "v_mfma_f32_16x16x32_bf16 v[248:251], v[48:51], a[100:103], v[248:251]\n"
      "v_cvt_pk_bf16_f32 v120, v244, v245\n"
      "v_cvt_pk_bf16_f32 v121, v246, v247\n"
      "v_mfma_f32_16x16x32_bf16 v[248:251], v[124:127], a[104:107], v[248:251]\n"
      "v_mfma_f32_16x16x32_bf16 v[248:251], v[116:119], a[108:111], v[248:251]\n"
      "v_mfma_f32_16x16x32_bf16 v[244:247], v[252:255], a[112:115], 0\n"
      "v_mfma_f32_16x16x32_bf16 v[244:247], v[48:51], a[116:119], v[244:247]\n"
      "v_cvt_pk_bf16_f32 v68, v52, v53\n"
      "v_cvt_pk_bf16_f32 v69, v54, v55\n"
      "v_mfma_f32_16x16x32_bf16 v[244:247], v[124:127], a[120:123], v[244:247]\n"
      "v_mfma_f32_16x16x32_bf16 v[244:247], v[116:119], a[124:127], v[244:247]\n"
      "s_waitcnt vmcnt(25)\n"
      "ds_read_b128 v[52:55], v0 offset:8192\n"
      "ds_read_b128 v[128:131], v0 offset:8704\n"
      "ds_read_b128 v[168:171], v0 offset:9216\n"
      "ds_read_b128 v[164:167], v0 offset:9728\n"
      "v_cvt_pk_bf16_f32 v36, v248, v249\n"
      "v_cvt_pk_bf16_f32 v37, v250, v251\n"
      "s_waitcnt lgkmcnt(7)\n"
      "v_mfma_f32_16x16x32_bf16 v[248:251], a[0:3], v[24:27], 0\n"
      "s_add_u32 m0, s53, 12288\n"
      "s_add_u32 s54, s49, 12288\n"
      "buffer_load_dwordx4 v1, s[40:43], s54 offen offset:0 lds\n"
      "v_cvt_pk_bf16_f32 v240, v244, v245\n"
      "v_cvt_pk_bf16_f32 v241, v246, v247\n"
      "s_waitcnt lgkmcnt(6)\n"
      "v_mfma_f32_16x16x32_bf16 v[248:251], a[4:7], v[152:155], v[248:251]\n"
      "s_waitcnt lgkmcnt(5)\n"
      "v_mfma_f32_16x16x32_bf16 v[248:251], a[8:11], v[20:23], v[248:251]\n"
      "buffer_load_dwordx4 v1, s[40:43], s54 offen offset:2048 lds\n"
      "s_add_u32 m0, s53, 13184\n"
      "s_waitcnt lgkmcnt(4)\n"
      "v_mfma_f32_16x16x32_bf16 v[248:251], a[12:15], v[156:159], v[248:251]\n"
      "v_mfma_f32_16x16x32_bf16 v[40:43], a[16:19], v[24:27], 0\n"
      "buffer_load_dwordx4 v1, s[40:43], s54 offen offset:128 lds\n"
      "v_mfma_f32_16x16x32_bf16 v[40:43], a[20:23], v[152:155], v[40:43]\n"
      "v_mfma_f32_16x16x32_bf16 v[40:43], a[24:27], v[20:23], v[40:43]\n"
      "buffer_load_dwordx4 v1, s[40:43], s54 offen offset:2176 lds\n"
      "v_mfma_f32_16x16x32_bf16 v[40:43], a[28:31], v[156:159], v[40:43]\n"
      "v_mfma_f32_16x16x32_bf16 v[100:103], a[32:35], v[24:27], 0\n"
      "v_mfma_f32_16x16x32_bf16 v[100:103], a[36:39], v[152:155], v[100:103]\n"
      "v_cvt_pk_bf16_f32 v244, v248, v249\n"
      "v_cvt_pk_bf16_f32 v245, v250, v251\n"
      "v_mfma_f32_16x16x32_bf16 v[100:103], a[40:43], v[20:23], v[100:103]\n"
      "v_mfma_f32_16x16x32_bf16 v[100:103], a[44:47], v[156:159], v[100:103]\n"
      "v_mfma_f32_16x16x32_bf16 v[44:47], a[48:51], v[24:27], 0\n"
      "v_mfma_f32_16x16x32_bf16 v[44:47], a[52:55], v[152:155], v[44:47]\n"
      "v_cvt_pk_bf16_f32 v246, v40, v41\n"
      "v_cvt_pk_bf16_f32 v247, v42, v43\n"
      "v_mfma_f32_16x16x32_bf16 v[44:47], a[56:59], v[20:23], v[44:47]\n"
      "v_mfma_f32_16x16x32_bf16 v[44:47], a[60:63], v[156:159], v[44:47]\n"
      "v_mfma_f32_16x16x32_bf16 v[40:43], v[24:27], a[64:67], 0\n"
      "v_mfma_f32_16x16x32_bf16 v[40:43], v[152:155], a[68:71], v[40:43]\n"
      "v_cvt_pk_bf16_f32 v248, v100, v101\n"
      "v_cvt_pk_bf16_f32 v249, v102, v103\n"
      "v_mfma_f32_16x16x32_bf16 v[40:43], v[20:23], a[72:75], v[40:43]\n"
      "v_mfma_f32_16x16x32_bf16 v[40:43], v[156:159], a[76:79], v[40:43]\n"
      "v_mfma_f32_16x16x32_bf16 v[100:103], v[24:27], a[80:83], 0\n"
      "v_mfma_f32_16x16x32_bf16 v[100:103], v[152:155], a[84:87], v[100:103]\n"
      "v_cvt_pk_bf16_f32 v250, v44, v45\n"
      "v_cvt_pk_bf16_f32 v251, v46, v47\n"
      "v_mfma_f32_16x16x32_bf16 v[100:103], v[20:23], a[88:91], v[100:103]\n"
      "v_mfma_f32_16x16x32_bf16 v[100:103], v[156:159], a[92:95], v[100:103]\n"
      "v_mfma_f32_16x16x32_bf16 v[44:47], v[24:27], a[96:99], 0\n"
      "v_mfma_f32_16x16x32_bf16 v[44:47], v[152:155], a[100:103], v[44:47]\n"
      "v_cvt_pk_bf16_f32 v122, v40, v41\n"
      "v_cvt_pk_bf16_f32 v123, v42, v43\n"
      "v_mfma_f32_16x16x32_bf16 v[44:47], v[20:23], a[104:107], v[44:47]\n"
      "v_mfma_f32_16x16x32_bf16 v[44:47], v[156:159], a[108:111], v[44:47]\n"
      "v_mfma_f32_16x16x32_bf16 v[40:43], v[24:27], a[112:115], 0\n"
      "v_mfma_f32_16x16x32_bf16 v[40:43], v[152:155], a[116:119], v[40:43]\n"
      "v_cvt_pk_bf16_f32 v70, v100, v101\n"
      "v_cvt_pk_bf16_f32 v71, v102, v103\n"
      "v_mfma_f32_16x16x32_bf16 v[40:43], v[20:23], a[120:123], v[40:43]\n"
      "v_mfma_f32_16x16x32_bf16 v[40:43], v[156:159], a[124:127], v[40:43]\n"
      "s_waitcnt vmcnt(21)\n"
      "ds_read_b128 v[100:103], v0 offset:4096\n"
      "ds_read_b128 v[108:111], v0 offset:4608\n"
      "ds_read_b128 v[176:179], v0 offset:5120\n"
      "ds_read_b128 v[172:175], v0 offset:5632\n"
      "v_cvt_pk_bf16_f32 v38, v44, v45\n"
      "v_cvt_pk_bf16_f32 v39, v46, v47\n"
      "s_waitcnt lgkmcnt(7)\n"
      "v_mfma_f32_16x16x32_bf16 v[44:47], a[0:3], v[52:55], 0\n"
      "s_add_u32 m0, s53, 16384\n"
      "s_add_u32 s54, s49, 16384\n"
      "buffer_load_dwordx4 v1, s[40:43], s54 offen offset:0 lds\n"
      "v_cvt_pk_bf16_f32 v242, v40, v41\n"
      "v_cvt_pk_bf16_f32 v243, v42, v43\n"
      "s_waitcnt lgkmcnt(6)\n"
      "v_mfma_f32_16x16x32_bf16 v[44:47], a[4:7], v[128:131], v[44:47]\n"
      "s_waitcnt lgkmcnt(5)\n"
      "v_mfma_f32_16x16x32_bf16 v[44:47], a[8:11], v[168:171], v[44:47]\n"
      "buffer_load_dwordx4 v1, s[40:43], s54 offen offset:2048 lds\n"
      "s_add_u32 m0, s53, 17280\n"
      "s_waitcnt lgkmcnt(4)\n"
      "v_mfma_f32_16x16x32_bf16 v[44:47], a[12:15], v[164:167], v[44:47]\n"
      "v_mfma_f32_16x16x32_bf16 v[112:115], a[16:19], v[52:55], 0\n"
      "buffer_load_dwordx4 v1, s[40:43], s54 offen offset:128 lds\n"
      "v_mfma_f32_16x16x32_bf16 v[112:115], a[20:23], v[128:131], v[112:115]\n"
      "v_mfma_f32_16x16x32_bf16 v[112:115], a[24:27], v[168:171], v[112:115]\n"
      "buffer_load_dwordx4 v1, s[40:43], s54 offen offset:2176 lds\n"
      "v_mfma_f32_16x16x32_bf16 v[112:115], a[28:31], v[164:167], v[112:115]\n"
      "v_mfma_f32_16x16x32_bf16 v[192:195], a[32:35], v[52:55], 0\n"
      "v_mfma_f32_16x16x32_bf16 v[192:195], a[36:39], v[128:131], v[192:195]\n"
      "v_cvt_pk_bf16_f32 v40, v44, v45\n"
      "v_cvt_pk_bf16_f32 v41, v46, v47\n"
      "v_mfma_f32_16x16x32_bf16 v[192:195], a[40:43], v[168:171], v[192:195]\n"
      "v_mfma_f32_16x16x32_bf16 v[192:195], a[44:47], v[164:167], v[192:195]\n"
      "v_mfma_f32_16x16x32_bf16 v[224:227], a[48:51], v[52:55], 0\n"
      "v_mfma_f32_16x16x32_bf16 v[224:227], a[52:55], v[128:131], v[224:227]\n"
      "v_cvt_pk_bf16_f32 v42, v112, v113\n"
      "v_cvt_pk_bf16_f32 v43, v114, v115\n"
      "v_mfma_f32_16x16x32_bf16 v[224:227], a[56:59], v[168:171], v[224:227]\n"
      "v_mfma_f32_16x16x32_bf16 v[224:227], a[60:63], v[164:167], v[224:227]\n"
      "v_mfma_f32_16x16x32_bf16 v[32:35], v[52:55], a[64:67], 0\n"
      "v_mfma_f32_16x16x32_bf16 v[32:35], v[128:131], a[68:71], v[32:35]\n"
      "v_cvt_pk_bf16_f32 v44, v192, v193\n"
      "v_cvt_pk_bf16_f32 v45, v194, v195\n"
      "v_mfma_f32_16x16x32_bf16 v[32:35], v[168:171], a[72:75], v[32:35]\n"
      "v_mfma_f32_16x16x32_bf16 v[32:35], v[164:167], a[76:79], v[32:35]\n"
      "v_mfma_f32_16x16x32_bf16 v[160:163], v[52:55], a[80:83], 0\n"
      "v_mfma_f32_16x16x32_bf16 v[160:163], v[128:131], a[84:87], v[160:163]\n"
      "v_cvt_pk_bf16_f32 v46, v224, v225\n"
      "v_cvt_pk_bf16_f32 v47, v226, v227\n"
      "v_mfma_f32_16x16x32_bf16 v[160:163], v[168:171], a[88:91], v[160:163]\n"
      "v_mfma_f32_16x16x32_bf16 v[160:163], v[164:167], a[92:95], v[160:163]\n"
      "v_mfma_f32_16x16x32_bf16 v[112:115], v[52:55], a[96:99], 0\n"
      "v_mfma_f32_16x16x32_bf16 v[112:115], v[128:131], a[100:103], v[112:115]\n"
      "v_cvt_pk_bf16_f32 v56, v32, v33\n"
      "v_cvt_pk_bf16_f32 v57, v34, v35\n"
      "v_mfma_f32_16x16x32_bf16 v[112:115], v[168:171], a[104:107], v[112:115]\n"
      "v_mfma_f32_16x16x32_bf16 v[112:115], v[164:167], a[108:111], v[112:115]\n"
      "v_mfma_f32_16x16x32_bf16 v[192:195], v[52:55], a[112:115], 0\n"
      "v_mfma_f32_16x16x32_bf16 v[192:195], v[128:131], a[116:119], v[192:195]\n"
      "v_cvt_pk_bf16_f32 v132, v160, v161\n"
      "v_cvt_pk_bf16_f32 v133, v162, v163\n"
      "v_mfma_f32_16x16x32_bf16 v[192:195], v[168:171], a[120:123], v[192:195]\n"
      "v_mfma_f32_16x16x32_bf16 v[192:195], v[164:167], a[124:127], v[192:195]\n"
      "s_waitcnt vmcnt(17)\n"
      "ds_read_b128 v[224:227], v0 offset:0\n"
      "ds_read_b128 v[32:35], v0 offset:512\n"
      "ds_read_b128 v[160:163], v0 offset:1024\n"
      "ds_read_b128 v[28:31], v0 offset:1536\n"
      "v_cvt_pk_bf16_f32 v104, v112, v113\n"
      "v_cvt_pk_bf16_f32 v105, v114, v115\n"
      "s_waitcnt lgkmcnt(7)\n"
      "v_mfma_f32_16x16x32_bf16 v[168:171], a[0:3], v[100:103], 0\n"
      "s_add_u32 m0, s53, 20480\n"
      "s_add_u32 s54, s49, 20480\n"
      "buffer_load_dwordx4 v1, s[40:43], s54 offen offset:0 lds\n"
      "v_cvt_pk_bf16_f32 v196, v192, v193\n"
      "v_cvt_pk_bf16_f32 v197, v194, v195\n"
      "s_waitcnt lgkmcnt(6)\n"
      "v_mfma_f32_16x16x32_bf16 v[168:171], a[4:7], v[108:111], v[168:171]\n"
      "s_waitcnt lgkmcnt(5)\n"
      "v_mfma_f32_16x16x32_bf16 v[168:171], a[8:11], v[176:179], v[168:171]\n"
      "buffer_load_dwordx4 v1, s[40:43], s54 offen offset:2048 lds\n"
      "s_add_u32 m0, s53, 21376\n"
      "s_waitcnt lgkmcnt(4)\n"
      "v_mfma_f32_16x16x32_bf16 v[168:171], a[12:15], v[172:175], v[168:171]\n"
      "v_mfma_f32_16x16x32_bf16 v[112:115], a[16:19], v[100:103], 0\n"
      "buffer_load_dwordx4 v1, s[40:43], s54 offen offset:128 lds\n"
      "v_mfma_f32_16x16x32_bf16 v[112:115], a[20:23], v[108:111], v[112:115]\n"
      "v_mfma_f32_16x16x32_bf16 v[112:115], a[24:27], v[176:179], v[112:115]\n"
      "buffer_load_dwordx4 v1, s[40:43], s54 offen offset:2176 lds\n"
      "v_mfma_f32_16x16x32_bf16 v[112:115], a[28:31], v[172:175], v[112:115]\n"
      "v_mfma_f32_16x16x32_bf16 v[192:195], a[32:35], v[100:103], 0\n"
      "v_mfma_f32_16x16x32_bf16 v[192:195], a[36:39], v[108:111], v[192:195]\n"
      "v_cvt_pk_bf16_f32 v164, v168, v169\n"
      "v_cvt_pk_bf16_f32 v165, v170, v171\n"
      "v_mfma_f32_16x16x32_bf16 v[192:195], a[40:43], v[176:179], v[192:195]\n"
      "v_mfma_f32_16x16x32_bf16 v[192:195], a[44:47], v[172:175], v[192:195]\n"
      "v_mfma_f32_16x16x32_bf16 v[52:55], a[48:51], v[100:103], 0\n"
      "v_mfma_f32_16x16x32_bf16 v[52:55], a[52:55], v[108:111], v[52:55]\n"
      "v_cvt_pk_bf16_f32 v166, v112, v113\n"
      "v_cvt_pk_bf16_f32 v167, v114, v115\n"
      "v_mfma_f32_16x16x32_bf16 v[52:55], a[56:59], v[176:179], v[52:55]\n"
      "v_mfma_f32_16x16x32_bf16 v[52:55], a[60:63], v[172:175], v[52:55]\n"
      "v_mfma_f32_16x16x32_bf16 v[112:115], v[100:103], a[64:67], 0\n"
      "v_mfma_f32_16x16x32_bf16 v[112:115], v[108:111], a[68:71], v[112:115]\n"
      "v_cvt_pk_bf16_f32 v168, v192, v193\n"
      "v_cvt_pk_bf16_f32 v169, v194, v195\n"
      "v_mfma_f32_16x16x32_bf16 v[112:115], v[176:179], a[72:75], v[112:115]\n"
      "v_mfma_f32_16x16x32_bf16 v[112:115], v[172:175], a[76:79], v[112:115]\n"
      "v_mfma_f32_16x16x32_bf16 v[192:195], v[100:103], a[80:83], 0\n"
      "v_mfma_f32_16x16x32_bf16 v[192:195], v[108:111], a[84:87], v[192:195]\n"
      "v_cvt_pk_bf16_f32 v170, v52, v53\n"
      "v_cvt_pk_bf16_f32 v171, v54, v55\n"
      "v_mfma_f32_16x16x32_bf16 v[192:195], v[176:179], a[88:91], v[192:195]\n"
      "v_mfma_f32_16x16x32_bf16 v[192:195], v[172:175], a[92:95], v[192:195]\n"
      "v_mfma_f32_16x16x32_bf16 v[88:91], v[100:103], a[96:99], 0\n"
      "v_mfma_f32_16x16x32_bf16 v[88:91], v[108:111], a[100:103], v[88:91]\n"
      "v_cvt_pk_bf16_f32 v54, v112, v113\n"
      "v_cvt_pk_bf16_f32 v55, v114, v115\n"
      "v_mfma_f32_16x16x32_bf16 v[88:91], v[176:179], a[104:107], v[88:91]\n"
      "v_mfma_f32_16x16x32_bf16 v[88:91], v[172:175], a[108:111], v[88:91]\n"
      "v_mfma_f32_16x16x32_bf16 v[112:115], v[100:103], a[112:115], 0\n"
      "v_mfma_f32_16x16x32_bf16 v[112:115], v[108:111], a[116:119], v[112:115]\n"
      "v_cvt_pk_bf16_f32 v130, v192, v193\n"
      "v_cvt_pk_bf16_f32 v131, v194, v195\n"
      "v_mfma_f32_16x16x32_bf16 v[112:115], v[176:179], a[120:123], v[112:115]\n"
      "v_mfma_f32_16x16x32_bf16 v[112:115], v[172:175], a[124:127], v[112:115]\n"
      "ds_read_b128 v[172:175], v9 offset:0\n"
      "ds_read_b128 v[176:179], v9 offset:2048\n"
      "v_cvt_pk_bf16_f32 v102, v88, v89\n"
      "v_cvt_pk_bf16_f32 v103, v90, v91\n"
      "s_waitcnt lgkmcnt(5)\n"
      "v_mfma_f32_16x16x32_bf16 v[88:91], a[0:3], v[224:227], 0\n"
      "s_waitcnt lgkmcnt(4)\n"
      "v_mfma_f32_16x16x32_bf16 v[88:91], a[4:7], v[32:35], v[88:91]\n"
      "s_waitcnt lgkmcnt(3)\n"
      "v_mfma_f32_16x16x32_bf16 v[88:91], a[8:11], v[160:163], v[88:91]\n"
      "s_waitcnt lgkmcnt(2)\n"
      "v_mfma_f32_16x16x32_bf16 v[88:91], a[12:15], v[28:31], v[88:91]\n"
      "v_cvt_pk_bf16_f32 v194, v112, v113\n"
      "v_cvt_pk_bf16_f32 v195, v114, v115\n"
      "v_mfma_f32_16x16x32_bf16 v[112:115], a[16:19], v[224:227], 0\n"
      "v_mfma_f32_16x16x32_bf16 v[112:115], a[20:23], v[32:35], v[112:115]\n"
      "v_mfma_f32_16x16x32_bf16 v[112:115], a[24:27], v[160:163], v[112:115]\n"
      "v_mfma_f32_16x16x32_bf16 v[112:115], a[28:31], v[28:31], v[112:115]\n"
      "v_mfma_f32_16x16x32_bf16 v[190:193], a[32:35], v[224:227], 0\n"
      "v_mfma_f32_16x16x32_bf16 v[190:193], a[36:39], v[32:35], v[190:193]\n"
      "v_cvt_pk_bf16_f32 v108, v88, v89\n"
      "v_cvt_pk_bf16_f32 v109, v90, v91\n"
      "v_mfma_f32_16x16x32_bf16 v[190:193], a[40:43], v[160:163], v[190:193]\n"
      "v_mfma_f32_16x16x32_bf16 v[190:193], a[44:47], v[28:31], v[190:193]\n"
      "v_mfma_f32_16x16x32_bf16 v[88:91], a[48:51], v[224:227], 0\n"
      "v_mfma_f32_16x16x32_bf16 v[88:91], a[52:55], v[32:35], v[88:91]\n"
      "v_cvt_pk_bf16_f32 v110, v112, v113\n"
      "v_cvt_pk_bf16_f32 v111, v114, v115\n"
      "v_mfma_f32_16x16x32_bf16 v[88:91], a[56:59], v[160:163], v[88:91]\n"
      "v_mfma_f32_16x16x32_bf16 v[88:91], a[60:63], v[28:31], v[88:91]\n"
      "v_add_u32 v9, 64, v9\n"
      "v_mfma_f32_16x16x32_bf16 v[228:231], v[224:227], a[64:67], 0\n"
      "v_cvt_pk_bf16_f32 v112, v190, v191\n"
      "v_cvt_pk_bf16_f32 v113, v192, v193\n"
      "v_mfma_f32_16x16x32_bf16 v[228:231], v[32:35], a[68:71], v[228:231]\n"
      "v_mfma_f32_16x16x32_bf16 v[228:231], v[160:163], a[72:75], v[228:231]\n"
      "v_mfma_f32_16x16x32_bf16 v[228:231], v[28:31], a[76:79], v[228:231]\n"
      "v_mfma_f32_16x16x32_bf16 v[190:193], v[224:227], a[80:83], 0\n"
      "v_cvt_pk_bf16_f32 v114, v88, v89\n"
      "v_cvt_pk_bf16_f32 v115, v90, v91\n"
      "v_mfma_f32_16x16x32_bf16 v[190:193], v[32:35], a[84:87], v[190:193]\n"
      "v_mfma_f32_16x16x32_bf16 v[190:193], v[160:163], a[88:91], v[190:193]\n"
      "v_mfma_f32_16x16x32_bf16 v[190:193], v[28:31], a[92:95], v[190:193]\n"
      "v_mfma_f32_16x16x32_bf16 v[88:91], v[224:227], a[96:99], 0\n"
      "v_mfma_f32_16x16x32_bf16 v[88:91], v[32:35], a[100:103], v[88:91]\n"
      "v_cvt_pk_bf16_f32 v52, v228, v229\n"
      "v_cvt_pk_bf16_f32 v53, v230, v231\n"
      "v_mfma_f32_16x16x32_bf16 v[88:91], v[160:163], a[104:107], v[88:91]\n"
      "v_mfma_f32_16x16x32_bf16 v[88:91], v[28:31], a[108:111], v[88:91]\n"
      "v_mfma_f32_16x16x32_bf16 v[228:231], v[224:227], a[112:115], 0\n"
      "v_mfma_f32_16x16x32_bf16 v[228:231], v[32:35], a[116:119], v[228:231]\n"
      "v_cvt_pk_bf16_f32 v128, v190, v191\n"
      "v_cvt_pk_bf16_f32 v129, v192, v193\n"
      "v_mfma_f32_16x16x32_bf16 v[228:231], v[160:163], a[120:123], v[228:231]\n"
      "v_mfma_f32_16x16x32_bf16 v[228:231], v[28:31], a[124:127], v[228:231]\n"
      "ds_read_b64 v[18:19], v11 offset:0\n"
      "v_mfma_f32_16x16x32_bf16 v[28:31], a[128:131], v[24:27], 0\n"
      "v_cvt_pk_bf16_f32 v100, v88, v89\n"
      "v_cvt_pk_bf16_f32 v101, v90, v91\n"
      "v_mfma_f32_16x16x32_bf16 v[28:31], a[132:135], v[152:155], v[28:31]\n"
      "v_mfma_f32_16x16x32_bf16 v[28:31], a[136:139], v[20:23], v[28:31]\n"
      "v_mfma_f32_16x16x32_bf16 v[28:31], a[140:143], v[156:159], v[28:31]\n"
      "v_mfma_f32_16x16x32_bf16 v[88:91], a[144:147], v[24:27], 0\n"
      "v_cvt_pk_bf16_f32 v192, v228, v229\n"
      "v_cvt_pk_bf16_f32 v193, v230, v231\n"
      "v_mfma_f32_16x16x32_bf16 v[88:91], a[148:151], v[152:155], v[88:91]\n"
      "v_mfma_f32_16x16x32_bf16 v[88:91], a[152:155], v[20:23], v[88:91]\n"
      "v_mfma_f32_16x16x32_bf16 v[88:91], a[156:159], v[156:159], v[88:91]\n"
      "v_mfma_f32_16x16x32_bf16 v[228:231], a[160:163], v[24:27], 0\n"
      "v_mfma_f32_16x16x32_bf16 v[228:231], a[164:167], v[152:155], v[228:231]\n"
      "v_cvt_pk_bf16_f32 v160, v28, v29\n"
      "v_cvt_pk_bf16_f32 v161, v30, v31\n"
      "v_mfma_f32_16x16x32_bf16 v[228:231], a[168:171], v[20:23], v[228:231]\n"
      "v_mfma_f32_16x16x32_bf16 v[228:231], a[172:175], v[156:159], v[228:231]\n"
      "v_mfma_f32_16x16x32_bf16 v[32:35], a[176:179], v[24:27], 0\n"
      "v_mfma_f32_16x16x32_bf16 v[32:35], a[180:183], v[152:155], v[32:35]\n"
      "v_cvt_pk_bf16_f32 v162, v88, v89\n"
      "v_cvt_pk_bf16_f32 v163, v90, v91\n"
      "v_mfma_f32_16x16x32_bf16 v[32:35], a[184:187], v[20:23], v[32:35]\n"
      "v_mfma_f32_16x16x32_bf16 v[32:35], a[188:191], v[156:159], v[32:35]\n"
      "s_waitcnt lgkmcnt(2)\n"
      "v_mfma_f32_16x16x32_bf16 v[28:31], v[172:175], v[160:163], 0\n"
      "v_cvt_pk_bf16_f32 v88, v228, v229\n"
      "v_cvt_pk_bf16_f32 v89, v230, v231\n"
      "s_nop 3\n"
      "v_cvt_pk_bf16_f32 v90, v32, v33\n"
      "v_cvt_pk_bf16_f32 v91, v34, v35\n"
      "s_waitcnt lgkmcnt(1)\n"
      "s_nop 0\n"
      "v_mfma_f32_16x16x32_bf16 v[28:31], v[176:179], v[88:91], v[28:31]\n"
      "v_mfma_f32_16x16x32_bf16 v[228:231], v[108:111], v[160:163], 0\n"
      "v_mfma_f32_16x16x32_bf16 v[228:231], v[112:115], v[88:91], v[228:231]\n"
      "v_mfma_f32_16x16x32_bf16 v[32:35], v[164:167], v[160:163], 0\n"
      "v_mfma_f32_16x16x32_bf16 v[32:35], v[168:171], v[88:91], v[32:35]\n"
      "v_mfma_f32_16x16x32_bf16 v[224:227], v[40:43], v[160:163], 0\n"
      "v_mfma_f32_16x16x32_bf16 v[224:227], v[44:47], v[88:91], v[224:227]\n"
      "v_mfma_f32_16x16x32_bf16 v[200:203], v[92:95], v[160:163], 0\n"
      "v_mfma_f32_16x16x32_bf16 v[200:203], v[96:99], v[88:91], v[200:203]\n"
      "v_max_f32 v190, v28, v29\n"
      "v_max3_f32 v190, v190, v30, v31\n"
      "v_max3_f32 v190, v190, v228, v229\n"
      "v_max3_f32 v190, v190, v230, v231\n"
      "v_max3_f32 v190, v190, v32, v33\n"
      "v_mfma_f32_16x16x32_bf16 v[180:183], v[216:219], v[160:163], 0\n"
      "v_max3_f32 v190, v190, v34, v35\n"
      "v_max3_f32 v190, v190, v224, v225\n"
      "v_max3_f32 v190, v190, v226, v227\n"
      "v_max3_f32 v190, v190, v200, v201\n"
      "v_max3_f32 v190, v190, v202, v203\n"
      "v_mfma_f32_16x16x32_bf16 v[180:183], v[220:223], v[88:91], v[180:183]\n"
      "v_mfma_f32_16x16x32_bf16 v[208:211], v[244:247], v[160:163], 0\n"
      "v_mfma_f32_16x16x32_bf16 v[208:211], v[248:251], v[88:91], v[208:211]\n"
      "v_mfma_f32_16x16x32_bf16 v[184:187], a[128:131], v[252:255], 0\n"
      "v_mfma_f32_16x16x32_bf16 v[184:187], a[132:135], v[48:51], v[184:187]\n"
      "v_mfma_f32_16x16x32_bf16 v[184:187], a[136:139], v[124:127], v[184:187]\n"
      "v_mfma_f32_16x16x32_bf16 v[184:187], a[140:143], v[116:119], v[184:187]\n"
      "v_mfma_f32_16x16x32_bf16 v[88:91], a[144:147], v[252:255], 0\n"
      "v_mfma_f32_16x16x32_bf16 v[88:91], a[148:151], v[48:51], v[88:91]\n"
      "v_max3_f32 v190, v190, v180, v181\n"
      "v_max3_f32 v190, v190, v182, v183\n"
      "v_max3_f32 v190, v190, v208, v209\n"
      "v_max3_f32 v190, v190, v210, v211\n"
      "v_mov_b32 v162, v190\n"
      "v_mfma_f32_16x16x32_bf16 v[88:91], a[152:155], v[124:127], v[88:91]\n"
      "v_cvt_pk_bf16_f32 v160, v184, v185\n"
      "v_permlane16_swap_b32 v190, v162\n"
      "v_max_f32 v190, v190, v162\n"
      "v_mov_b32 v162, v190\n"
      "v_cvt_pk_bf16_f32 v161, v186, v187\n"
      "v_mfma_f32_16x16x32_bf16 v[88:91], a[156:159], v[116:119], v[88:91]\n"
      "v_permlane32_swap_b32 v190, v162\n"
      "v_max_f32 v190, v190, v162\n"
      "v_sub_f32 v28, v28, v190\n"
      "v_sub_f32 v29, v29, v190\n"
      "v_sub_f32 v30, v30, v190\n"
      "v_sub_f32 v31, v31, v190\n"
      "v_exp_f32 v28, v28\n"
      "v_mfma_f32_16x16x32_bf16 v[184:187], a[160:163], v[252:255], 0\n"
      "v_exp_f32 v29, v29\n"
      "v_exp_f32 v30, v30\n"
      "v_exp_f32 v31, v31\n"
      "v_cvt_pk_bf16_f32 v162, v28, v29\n"
      "v_cvt_pk_bf16_f32 v163, v30, v31\n"
      "v_mfma_f32_16x16x32_bf16 v[184:187], a[164:167], v[48:51], v[184:187]\n"
      "v_sub_f32 v228, v228, v190\n"
      "v_sub_f32 v229, v229, v190\n"
      "v_sub_f32 v230, v230, v190\n"
      "v_sub_f32 v231, v231, v190\n"
      "v_sub_f32 v32, v32, v190\n"
      "v_sub_f32 v33, v33, v190\n"
      "v_sub_f32 v34, v34, v190\n"
      "v_sub_f32 v35, v35, v190\n"
      "v_mfma_f32_16x16x32_bf16 v[184:187], a[168:171], v[124:127], v[184:187]\n"
      "v_sub_f32 v224, v224, v190\n"
      "v_sub_f32 v225, v225, v190\n"
      "v_sub_f32 v226, v226, v190\n"
      "v_sub_f32 v227, v227, v190\n"
      "v_sub_f32 v200, v200, v190\n"
      "v_sub_f32 v201, v201, v190\n"
      "v_sub_f32 v202, v202, v190\n"
      "v_sub_f32 v203, v203, v190\n"
      "v_mfma_f32_16x16x32_bf16 v[184:187], a[172:175], v[116:119], v[184:187]\n"
      "v_add_f32 v188, v28, v29\n"
      "v_add_f32 v189, v30, v31\n"
      "v_exp_f32 v228, v228\n"
      "v_exp_f32 v229, v229\n"
      "v_exp_f32 v230, v230\n"
      "v_cvt_pk_bf16_f32 v28, v228, v229\n"
      "v_mfma_f32_16x16x32_bf16 v[84:87], a[176:179], v[252:255], 0\n"
      "v_exp_f32 v231, v231\n"
      "v_exp_f32 v32, v32\n"
      "v_cvt_pk_bf16_f32 v29, v230, v231\n"
      "v_exp_f32 v33, v33\n"
      "v_mfma_f32_16x16x32_bf16 v[84:87], a[180:183], v[48:51], v[84:87]\n"
      "v_exp_f32 v34, v34\n"
      "v_exp_f32 v35, v35\n"
      "v_cvt_pk_bf16_f32 v30, v32, v33\n"
      "v_cvt_pk_bf16_f32 v31, v34, v35\n"
      "v_exp_f32 v224, v224\n"
      "v_sub_f32 v180, v180, v190\n"
      "v_mfma_f32_16x16x32_bf16 v[84:87], a[184:187], v[124:127], v[84:87]\n"
      "v_exp_f32 v225, v225\n"
      "v_exp_f32 v226, v226\n"
      "v_exp_f32 v227, v227\n"
      "v_cvt_pk_bf16_f32 v32, v224, v225\n"
      "v_cvt_pk_bf16_f32 v33, v226, v227\n"
      "v_mfma_f32_16x16x32_bf16 v[84:87], a[188:191], v[116:119], v[84:87]\n"
      "v_exp_f32 v200, v200\n"
      "v_exp_f32 v201, v201\n"
      "v_exp_f32 v202, v202\n"
      "v_exp_f32 v203, v203\n"
      "s_waitcnt lgkmcnt(0)\n"
      "v_mfma_f32_16x16x4_4b_bf16 v[224:239], v[162:163], v[18:19], 0 cbsz:2 abid:0\n"
      "v_cvt_pk_bf16_f32 v34, v200, v201\n"
      "v_cvt_pk_bf16_f32 v35, v202, v203\n"
      "v_sub_f32 v181, v181, v190\n"
      "v_sub_f32 v182, v182, v190\n"
      "v_sub_f32 v183, v183, v190\n"
      "v_sub_f32 v208, v208, v190\n"
      "v_sub_f32 v209, v209, v190\n"
      "v_sub_f32 v210, v210, v190\n"
      "v_sub_f32 v211, v211, v190\n"
      "v_exp_f32 v180, v180\n"
      "v_exp_f32 v181, v181\n"
      "v_exp_f32 v182, v182\n"
      "v_exp_f32 v183, v183\n"
      "v_cvt_pk_bf16_f32 v200, v180, v181\n"
      "v_cvt_pk_bf16_f32 v201, v182, v183\n"
      "v_exp_f32 v208, v208\n"
      "v_exp_f32 v209, v209\n"
      "v_smfmac_f32_16x16x64_bf16 v[188:191], v[12:15], v[28:35], v5\n"
      "v_exp_f32 v210, v210\n"
      "v_exp_f32 v211, v211\n"
      "v_mfma_f32_16x16x32_bf16 v[224:227], v[28:31], v[52:55], v[224:227]\n"
      "v_cvt_pk_bf16_f32 v202, v208, v209\n"
      "v_cvt_pk_bf16_f32 v203, v210, v211\n"
      "v_cvt_pk_bf16_f32 v162, v88, v89\n"
      "v_cvt_pk_bf16_f32 v163, v90, v91\n"
      "v_mfma_f32_16x16x32_bf16 v[224:227], v[32:35], v[56:59], v[224:227]\n"
      "v_cvt_pk_bf16_f32 v88, v184, v185\n"
      "v_cvt_pk_bf16_f32 v89, v186, v187\n"
      "v_cvt_pk_bf16_f32 v90, v84, v85\n"
      "v_mfma_f32_16x16x32_bf16 v[224:227], v[200:203], v[120:123], v[224:227]\n"
      "v_cvt_pk_bf16_f32 v91, v86, v87\n"
      "v_smfmac_f32_16x16x32_bf16 v[188:191], v[12:13], v[200:203], v5\n"
      "v_mfma_f32_16x16x32_bf16 v[228:231], v[28:31], v[128:131], v[228:231]\n"
      "v_mfma_f32_16x16x32_bf16 v[228:231], v[32:35], v[132:135], v[228:231]\n"
      "v_mfma_f32_16x16x32_bf16 v[228:231], v[200:203], v[68:71], v[228:231]\n"
      "v_mfma_f32_16x16x32_bf16 v[232:235], v[28:31], v[100:103], v[232:235]\n"
      "v_mfma_f32_16x16x32_bf16 v[232:235], v[32:35], v[104:107], v[232:235]\n"
      "v_mfma_f32_16x16x32_bf16 v[232:235], v[200:203], v[36:39], v[232:235]\n"
      "v_mfma_f32_16x16x32_bf16 v[236:239], v[28:31], v[192:195], v[236:239]\n"
      "v_mfma_f32_16x16x32_bf16 v[236:239], v[32:35], v[196:199], v[236:239]\n"
      "v_add_f32 v188, v188, v189\n"
      "v_rcp_f32 v189, v188\n"
      "v_mfma_f32_16x16x32_bf16 v[236:239], v[200:203], v[240:243], v[236:239]\n"
      "ds_bpermute_b32 v190, v4, v189\n"
      "s_waitcnt lgkmcnt(0)\n"
      "v_mul_f32_dpp v224, v190, v224 row_newbcast:0 row_mask:0xf bank_mask:0xf\n"
      "v_mul_f32_dpp v225, v190, v225 row_newbcast:4 row_mask:0xf bank_mask:0xf\n"
      "v_mul_f32_dpp v226, v190, v226 row_newbcast:8 row_mask:0xf bank_mask:0xf\n"
      "v_mul_f32_dpp v227, v190, v227 row_newbcast:12 row_mask:0xf bank_mask:0xf\n"
      "v_mul_f32_dpp v228, v190, v228 row_newbcast:0 row_mask:0xf bank_mask:0xf\n"
      "v_mul_f32_dpp v229, v190, v229 row_newbcast:4 row_mask:0xf bank_mask:0xf\n"
      "v_mfma_f32_16x16x32_bf16 v[224:227], v[24:27], a[192:195], v[224:227]\n"
      "v_mul_f32_dpp v230, v190, v230 row_newbcast:8 row_mask:0xf bank_mask:0xf\n"
      "v_mul_f32_dpp v231, v190, v231 row_newbcast:12 row_mask:0xf bank_mask:0xf\n"
      "v_mfma_f32_16x16x32_bf16 v[224:227], v[152:155], a[196:199], v[224:227]\n"
      "v_mul_f32_dpp v232, v190, v232 row_newbcast:0 row_mask:0xf bank_mask:0xf\n"
      "v_mul_f32_dpp v233, v190, v233 row_newbcast:4 row_mask:0xf bank_mask:0xf\n"
      "v_mul_f32_dpp v234, v190, v234 row_newbcast:8 row_mask:0xf bank_mask:0xf\n"
      "v_mfma_f32_16x16x32_bf16 v[224:227], v[20:23], a[200:203], v[224:227]\n"
      "v_mul_f32_dpp v235, v190, v235 row_newbcast:12 row_mask:0xf bank_mask:0xf\n"
      "v_mul_f32_dpp v236, v190, v236 row_newbcast:0 row_mask:0xf bank_mask:0xf\n"
      "v_mul_f32_dpp v237, v190, v237 row_newbcast:4 row_mask:0xf bank_mask:0xf\n"
      "v_mfma_f32_16x16x32_bf16 v[224:227], v[156:159], a[204:207], v[224:227]\n"
      "v_mul_f32_dpp v238, v190, v238 row_newbcast:8 row_mask:0xf bank_mask:0xf\n"
      "v_mul_f32_dpp v239, v190, v239 row_newbcast:12 row_mask:0xf bank_mask:0xf\n"
      "v_mfma_f32_16x16x32_bf16 v[228:231], v[24:27], a[208:211], v[228:231]\n"
      "v_mfma_f32_16x16x32_bf16 v[228:231], v[152:155], a[212:215], v[228:231]\n"
      "v_mfma_f32_16x16x32_bf16 v[228:231], v[20:23], a[216:219], v[228:231]\n"
      "v_mfma_f32_16x16x32_bf16 v[228:231], v[156:159], a[220:223], v[228:231]\n"
      "v_mfma_f32_16x16x32_bf16 v[232:235], v[24:27], a[224:227], v[232:235]\n"
      "v_mfma_f32_16x16x32_bf16 v[232:235], v[152:155], a[228:231], v[232:235]\n"
      "v_mfma_f32_16x16x32_bf16 v[232:235], v[20:23], a[232:235], v[232:235]\n"
      "v_mfma_f32_16x16x32_bf16 v[232:235], v[156:159], a[236:239], v[232:235]\n"
      "v_mfma_f32_16x16x32_bf16 v[236:239], v[24:27], a[240:243], v[236:239]\n"
      "v_mfma_f32_16x16x32_bf16 v[236:239], v[152:155], a[244:247], v[236:239]\n"
      "v_mfma_f32_16x16x32_bf16 v[236:239], v[20:23], a[248:251], v[236:239]\n"
      "v_mfma_f32_16x16x32_bf16 v[236:239], v[156:159], a[252:255], v[236:239]\n"
      "v_cvt_pk_bf16_f32 v224, v224, v228\n"
      "v_cvt_pk_bf16_f32 v228, v225, v229\n"
      "v_pk_max_i16 v224, v224, 0\n"
      "v_pk_max_i16 v228, v228, 0\n"
      "v_mfma_f32_16x16x32_bf16 v[20:23], v[172:175], v[160:163], 0\n"
      "v_cvt_pk_bf16_f32 v226, v226, v230\n"
      "v_cvt_pk_bf16_f32 v230, v227, v231\n"
      "v_pk_max_i16 v226, v226, 0\n"
      "v_cvt_pk_bf16_f32 v225, v232, v236\n"
      "v_mfma_f32_16x16x32_bf16 v[20:23], v[176:179], v[88:91], v[20:23]\n"
      "v_cvt_pk_bf16_f32 v229, v233, v237\n"
      "v_pk_max_i16 v225, v225, 0\n"
      "v_pk_max_i16 v229, v229, 0\n"
      "v_cvt_pk_bf16_f32 v227, v234, v238\n"
      "v_mfma_f32_16x16x32_bf16 v[24:27], v[108:111], v[160:163], 0\n"
      "v_cvt_pk_bf16_f32 v231, v235, v239\n"
      "v_pk_max_i16 v227, v227, 0\n"
      "v_pk_max_i16 v230, v230, 0\n"
      "v_max_f32 v186, v20, v21\n"
      "v_mfma_f32_16x16x32_bf16 v[24:27], v[112:115], v[88:91], v[24:27]\n"
      "v_max3_f32 v186, v186, v22, v23\n"
      "v_pk_max_i16 v231, v231, 0\n"
      "v_mfma_f32_16x16x32_bf16 v[28:31], v[164:167], v[160:163], 0\n"
      "v_mfma_f32_16x16x32_bf16 v[28:31], v[168:171], v[88:91], v[28:31]\n"
      "v_mfma_f32_16x16x32_bf16 v[236:239], v[40:43], v[160:163], 0\n"
      "v_mfma_f32_16x16x32_bf16 v[236:239], v[44:47], v[88:91], v[236:239]\n"
      "v_mfma_f32_16x16x32_bf16 v[232:235], v[92:95], v[160:163], 0\n"
      "v_mfma_f32_16x16x32_bf16 v[232:235], v[96:99], v[88:91], v[232:235]\n"
      "v_max3_f32 v186, v186, v24, v25\n"
      "v_max3_f32 v186, v186, v26, v27\n"
      "v_mfma_f32_16x16x32_bf16 v[156:159], v[216:219], v[160:163], 0\n"
      "v_mfma_f32_16x16x32_bf16 v[156:159], v[220:223], v[88:91], v[156:159]\n"
      "v_max3_f32 v186, v186, v28, v29\n"
      "v_max3_f32 v186, v186, v30, v31\n"
      "v_max3_f32 v186, v186, v236, v237\n"
      "v_max3_f32 v186, v186, v238, v239\n"
      "v_max3_f32 v186, v186, v232, v233\n"
      "v_max3_f32 v186, v186, v234, v235\n"
      "v_mfma_f32_16x16x32_bf16 v[152:155], v[244:247], v[160:163], 0\n"
      "v_mfma_f32_16x16x32_bf16 v[152:155], v[248:251], v[88:91], v[152:155]\n"
      "v_max3_f32 v186, v186, v156, v157\n"
      "v_max3_f32 v186, v186, v158, v159\n"
      "v_mfma_f32_16x16x32_bf16 v[160:163], a[128:131], v[148:151], 0\n"
      "v_mfma_f32_16x16x32_bf16 v[160:163], a[132:135], v[136:139], v[160:163]\n"
      "s_add_u32 s56, s48, 20480\n"
      "buffer_store_dwordx2 v[224:225], v2, s[44:47], s56 offen offset:0 nt sc1\n"
      "buffer_store_dwordx2 v[228:229], v2, s[44:47], s56 offen offset:256 nt sc1\n"
      "v_mfma_f32_16x16x32_bf16 v[160:163], a[136:139], v[140:143], v[160:163]\n"
      "v_max3_f32 v186, v186, v152, v153\n"
      "v_max3_f32 v186, v186, v154, v155\n"
      "v_mov_b32 v88, v186\n"
      "buffer_store_dwordx2 v[226:227], v2, s[44:47], s56 offen offset:512 nt sc1\n"
      "v_mfma_f32_16x16x32_bf16 v[160:163], a[140:143], v[144:147], v[160:163]\n"
      "v_permlane16_swap_b32 v186, v88\n"
      "v_max_f32 v186, v186, v88\n"
      "v_mfma_f32_16x16x32_bf16 v[32:35], a[144:147], v[148:151], 0\n"
      "v_mov_b32 v88, v186\n"
      "buffer_store_dwordx2 v[230:231], v2, s[44:47], s56 offen offset:768 nt sc1\n"
      "v_mfma_f32_16x16x32_bf16 v[32:35], a[148:151], v[136:139], v[32:35]\n"
      "v_permlane32_swap_b32 v186, v88\n"
      "v_max_f32 v186, v186, v88\n"
      "v_sub_f32 v20, v20, v186\n"
      "v_sub_f32 v21, v21, v186\n"
      "v_sub_f32 v22, v22, v186\n"
      "v_sub_f32 v23, v23, v186\n"
      "v_sub_f32 v24, v24, v186\n"
      "v_mfma_f32_16x16x32_bf16 v[32:35], a[152:155], v[140:143], v[32:35]\n"
      "v_exp_f32 v20, v20\n"
      "v_exp_f32 v21, v21\n"
      "v_exp_f32 v22, v22\n"
      "v_cvt_pk_bf16_f32 v190, v20, v21\n"
      "v_mfma_f32_16x16x32_bf16 v[32:35], a[156:159], v[144:147], v[32:35]\n"
      "v_exp_f32 v23, v23\n"
      "v_sub_f32 v25, v25, v186\n"
      "v_cvt_pk_bf16_f32 v191, v22, v23\n"
      "v_sub_f32 v26, v26, v186\n"
      "v_sub_f32 v27, v27, v186\n"
      "v_sub_f32 v28, v28, v186\n"
      "v_mfma_f32_16x16x32_bf16 v[88:91], a[160:163], v[148:151], 0\n"
      "v_sub_f32 v29, v29, v186\n"
      "v_sub_f32 v30, v30, v186\n"
      "v_sub_f32 v31, v31, v186\n"
      "v_sub_f32 v236, v236, v186\n"
      "v_sub_f32 v237, v237, v186\n"
      "v_sub_f32 v238, v238, v186\n"
      "v_sub_f32 v239, v239, v186\n"
      "v_mfma_f32_16x16x32_bf16 v[88:91], a[164:167], v[136:139], v[88:91]\n"
      "v_sub_f32 v232, v232, v186\n"
      "v_sub_f32 v233, v233, v186\n"
      "v_sub_f32 v234, v234, v186\n"
      "v_sub_f32 v235, v235, v186\n"
      "v_add_f32 v184, v20, v21\n"
      "v_add_f32 v185, v22, v23\n"
      "v_mfma_f32_16x16x32_bf16 v[88:91], a[168:171], v[140:143], v[88:91]\n"
      "v_exp_f32 v24, v24\n"
      "v_exp_f32 v25, v25\n"
      "v_exp_f32 v26, v26\n"
      "v_exp_f32 v27, v27\n"
      "v_mfma_f32_16x16x32_bf16 v[88:91], a[172:175], v[144:147], v[88:91]\n"
      "v_cvt_pk_bf16_f32 v20, v24, v25\n"
      "v_cvt_pk_bf16_f32 v21, v26, v27\n"
      "v_exp_f32 v28, v28\n"
      "v_exp_f32 v29, v29\n"
      "v_mfma_f32_16x16x32_bf16 v[208:211], a[176:179], v[148:151], 0\n"
      "v_exp_f32 v30, v30\n"
      "v_exp_f32 v31, v31\n"
      "v_cvt_pk_bf16_f32 v22, v28, v29\n"
      "v_cvt_pk_bf16_f32 v23, v30, v31\n"
      "v_exp_f32 v236, v236\n"
      "v_mfma_f32_16x16x32_bf16 v[208:211], a[180:183], v[136:139], v[208:211]\n"
      "v_exp_f32 v237, v237\n"
      "v_exp_f32 v238, v238\n"
      "v_exp_f32 v239, v239\n"
      "v_cvt_pk_bf16_f32 v24, v236, v237\n"
      "v_mfma_f32_16x16x32_bf16 v[208:211], a[184:187], v[140:143], v[208:211]\n"
      "v_cvt_pk_bf16_f32 v25, v238, v239\n"
      "v_exp_f32 v232, v232\n"
      "v_exp_f32 v233, v233\n"
      "v_exp_f32 v234, v234\n"
      "v_mfma_f32_16x16x32_bf16 v[208:211], a[188:191], v[144:147], v[208:211]\n"
      "v_exp_f32 v235, v235\n"
      "v_cvt_pk_bf16_f32 v26, v232, v233\n"
      "v_cvt_pk_bf16_f32 v27, v234, v235\n"
      "v_sub_f32 v156, v156, v186\n"
      "v_sub_f32 v157, v157, v186\n"
      "v_sub_f32 v158, v158, v186\n"
      "v_mfma_f32_16x16x4_4b_bf16 v[224:239], v[190:191], v[18:19], 0 cbsz:2 abid:0\n"
      "v_sub_f32 v159, v159, v186\n"
      "v_sub_f32 v152, v152, v186\n"
      "v_sub_f32 v153, v153, v186\n"
      "v_sub_f32 v154, v154, v186\n"
      "v_sub_f32 v155, v155, v186\n"
      "v_exp_f32 v156, v156\n"
      "v_exp_f32 v157, v157\n"
      "v_exp_f32 v158, v158\n"
      "v_exp_f32 v159, v159\n"
      "v_cvt_pk_bf16_f32 v28, v156, v157\n"
      "v_cvt_pk_bf16_f32 v29, v158, v159\n"
      "v_exp_f32 v152, v152\n"
      "v_exp_f32 v153, v153\n"
      "v_cvt_pk_bf16_f32 v156, v160, v161\n"
      "v_smfmac_f32_16x16x64_bf16 v[184:187], v[12:15], v[20:27], v5\n"
      "v_exp_f32 v154, v154\n"
      "v_exp_f32 v155, v155\n"
      "v_mfma_f32_16x16x32_bf16 v[224:227], v[20:23], v[52:55], v[224:227]\n"
      "v_cvt_pk_bf16_f32 v30, v152, v153\n"
      "v_cvt_pk_bf16_f32 v31, v154, v155\n"
      "v_cvt_pk_bf16_f32 v157, v162, v163\n"
      "v_mfma_f32_16x16x32_bf16 v[224:227], v[24:27], v[56:59], v[224:227]\n"
      "v_cvt_pk_bf16_f32 v158, v32, v33\n"
      "v_cvt_pk_bf16_f32 v159, v34, v35\n"
      "v_cvt_pk_bf16_f32 v32, v88, v89\n"
      "v_mfma_f32_16x16x32_bf16 v[224:227], v[28:31], v[120:123], v[224:227]\n"
      "v_cvt_pk_bf16_f32 v33, v90, v91\n"
      "v_cvt_pk_bf16_f32 v34, v208, v209\n"
      "v_cvt_pk_bf16_f32 v35, v210, v211\n"
      "v_smfmac_f32_16x16x32_bf16 v[184:187], v[12:13], v[28:31], v5\n"
      "v_mfma_f32_16x16x32_bf16 v[228:231], v[20:23], v[128:131], v[228:231]\n"
      "v_mfma_f32_16x16x32_bf16 v[228:231], v[24:27], v[132:135], v[228:231]\n"
      "v_mfma_f32_16x16x32_bf16 v[228:231], v[28:31], v[68:71], v[228:231]\n"
      "v_mfma_f32_16x16x32_bf16 v[232:235], v[20:23], v[100:103], v[232:235]\n"
      "v_mfma_f32_16x16x32_bf16 v[232:235], v[24:27], v[104:107], v[232:235]\n"
      "v_mfma_f32_16x16x32_bf16 v[232:235], v[28:31], v[36:39], v[232:235]\n"
      "v_mfma_f32_16x16x32_bf16 v[236:239], v[20:23], v[192:195], v[236:239]\n"
      "v_mfma_f32_16x16x32_bf16 v[236:239], v[24:27], v[196:199], v[236:239]\n"
      "v_add_f32 v184, v184, v185\n"
      "v_rcp_f32 v185, v184\n"
      "v_mfma_f32_16x16x32_bf16 v[236:239], v[28:31], v[240:243], v[236:239]\n"
      "ds_bpermute_b32 v186, v4, v185\n"
      "s_waitcnt lgkmcnt(0)\n"
      "v_mul_f32_dpp v224, v186, v224 row_newbcast:0 row_mask:0xf bank_mask:0xf\n"
      "v_mul_f32_dpp v225, v186, v225 row_newbcast:4 row_mask:0xf bank_mask:0xf\n"
      "v_mul_f32_dpp v226, v186, v226 row_newbcast:8 row_mask:0xf bank_mask:0xf\n"
      "v_mul_f32_dpp v227, v186, v227 row_newbcast:12 row_mask:0xf bank_mask:0xf\n"
      "v_mul_f32_dpp v228, v186, v228 row_newbcast:0 row_mask:0xf bank_mask:0xf\n"
      "v_mul_f32_dpp v229, v186, v229 row_newbcast:4 row_mask:0xf bank_mask:0xf\n"
      "v_mfma_f32_16x16x32_bf16 v[224:227], v[252:255], a[192:195], v[224:227]\n"
      "v_mul_f32_dpp v230, v186, v230 row_newbcast:8 row_mask:0xf bank_mask:0xf\n"
      "v_mul_f32_dpp v231, v186, v231 row_newbcast:12 row_mask:0xf bank_mask:0xf\n"
      "v_mfma_f32_16x16x32_bf16 v[224:227], v[48:51], a[196:199], v[224:227]\n"
      "v_mul_f32_dpp v232, v186, v232 row_newbcast:0 row_mask:0xf bank_mask:0xf\n"
      "v_mul_f32_dpp v233, v186, v233 row_newbcast:4 row_mask:0xf bank_mask:0xf\n"
      "v_mul_f32_dpp v234, v186, v234 row_newbcast:8 row_mask:0xf bank_mask:0xf\n"
      "v_mfma_f32_16x16x32_bf16 v[224:227], v[124:127], a[200:203], v[224:227]\n"
      "v_mul_f32_dpp v235, v186, v235 row_newbcast:12 row_mask:0xf bank_mask:0xf\n"
      "v_mul_f32_dpp v236, v186, v236 row_newbcast:0 row_mask:0xf bank_mask:0xf\n"
      "v_mul_f32_dpp v237, v186, v237 row_newbcast:4 row_mask:0xf bank_mask:0xf\n"
      "v_mul_f32_dpp v238, v186, v238 row_newbcast:8 row_mask:0xf bank_mask:0xf\n"
      "v_mfma_f32_16x16x32_bf16 v[224:227], v[116:119], a[204:207], v[224:227]\n"
      "v_mul_f32_dpp v239, v186, v239 row_newbcast:12 row_mask:0xf bank_mask:0xf\n"
      "v_mfma_f32_16x16x32_bf16 v[228:231], v[252:255], a[208:211], v[228:231]\n"
      "v_mfma_f32_16x16x32_bf16 v[228:231], v[48:51], a[212:215], v[228:231]\n"
      "v_mfma_f32_16x16x32_bf16 v[228:231], v[124:127], a[216:219], v[228:231]\n"
      "v_mfma_f32_16x16x32_bf16 v[228:231], v[116:119], a[220:223], v[228:231]\n"
      "v_mfma_f32_16x16x32_bf16 v[232:235], v[252:255], a[224:227], v[232:235]\n"
      "v_mfma_f32_16x16x32_bf16 v[232:235], v[48:51], a[228:231], v[232:235]\n"
      "v_mfma_f32_16x16x32_bf16 v[232:235], v[124:127], a[232:235], v[232:235]\n"
      "v_mfma_f32_16x16x32_bf16 v[232:235], v[116:119], a[236:239], v[232:235]\n"
      "v_mfma_f32_16x16x32_bf16 v[236:239], v[252:255], a[240:243], v[236:239]\n"
      "v_mfma_f32_16x16x32_bf16 v[236:239], v[48:51], a[244:247], v[236:239]\n"
      "v_mfma_f32_16x16x32_bf16 v[236:239], v[124:127], a[248:251], v[236:239]\n"
      "v_mfma_f32_16x16x32_bf16 v[236:239], v[116:119], a[252:255], v[236:239]\n"
      "v_cvt_pk_bf16_f32 v224, v224, v228\n"
      "v_cvt_pk_bf16_f32 v228, v225, v229\n"
      "v_pk_max_i16 v224, v224, 0\n"
      "v_pk_max_i16 v228, v228, 0\n"
      "v_cvt_pk_bf16_f32 v226, v226, v230\n"
      "v_mfma_f32_16x16x32_bf16 v[20:23], v[172:175], v[156:159], 0\n"
      "v_cvt_pk_bf16_f32 v230, v227, v231\n"
      "v_pk_max_i16 v226, v226, 0\n"
      "v_cvt_pk_bf16_f32 v225, v232, v236\n"
      "v_cvt_pk_bf16_f32 v229, v233, v237\n"
      "v_mfma_f32_16x16x32_bf16 v[20:23], v[176:179], v[32:35], v[20:23]\n"
      "v_pk_max_i16 v225, v225, 0\n"
      "v_pk_max_i16 v229, v229, 0\n"
      "v_cvt_pk_bf16_f32 v227, v234, v238\n"
      "v_cvt_pk_bf16_f32 v231, v235, v239\n"
      "v_pk_max_i16 v227, v227, 0\n"
      "v_mfma_f32_16x16x32_bf16 v[88:91], v[108:111], v[156:159], 0\n"
      "v_pk_max_i16 v230, v230, 0\n"
      "v_pk_max_i16 v231, v231, 0\n"
      "v_max_f32 v190, v20, v21\n"
      "v_max3_f32 v190, v190, v22, v23\n"
      "v_mfma_f32_16x16x32_bf16 v[88:91], v[112:115], v[32:35], v[88:91]\n"
      "v_mfma_f32_16x16x32_bf16 v[24:27], v[164:167], v[156:159], 0\n"
      "v_mfma_f32_16x16x32_bf16 v[24:27], v[168:171], v[32:35], v[24:27]\n"
      "v_mfma_f32_16x16x32_bf16 v[236:239], v[40:43], v[156:159], 0\n"
      "v_mfma_f32_16x16x32_bf16 v[236:239], v[44:47], v[32:35], v[236:239]\n"
      "v_mfma_f32_16x16x32_bf16 v[232:235], v[92:95], v[156:159], 0\n"
      "v_mfma_f32_16x16x32_bf16 v[232:235], v[96:99], v[32:35], v[232:235]\n"
      "v_mfma_f32_16x16x32_bf16 v[160:163], v[216:219], v[156:159], 0\n"
      "v_mfma_f32_16x16x32_bf16 v[160:163], v[220:223], v[32:35], v[160:163]\n"
      "v_max3_f32 v190, v190, v88, v89\n"
      "v_max3_f32 v190, v190, v90, v91\n"
      "v_max3_f32 v190, v190, v24, v25\n"
      "v_max3_f32 v190, v190, v26, v27\n"
      "v_max3_f32 v190, v190, v236, v237\n"
      "v_max3_f32 v190, v190, v238, v239\n"
      "v_max3_f32 v190, v190, v232, v233\n"
      "v_mfma_f32_16x16x32_bf16 v[152:155], v[244:247], v[156:159], 0\n"
      "v_max3_f32 v190, v190, v234, v235\n"
      "v_max3_f32 v190, v190, v160, v161\n"
      "v_max3_f32 v190, v190, v162, v163\n"
      "v_mfma_f32_16x16x32_bf16 v[152:155], v[248:251], v[32:35], v[152:155]\n"
      "ds_read_b128 v[116:119], v0 offset:8192\n"
      "ds_read_b128 v[124:127], v0 offset:8704\n"
      "ds_read_b128 v[48:51], v0 offset:9216\n"
      "ds_read_b128 v[252:255], v0 offset:9728\n"
      "s_add_u32 s56, s48, 16384\n"
      "buffer_store_dwordx2 v[224:225], v2, s[44:47], s56 offen offset:0 nt sc1\n"
      "buffer_store_dwordx2 v[228:229], v2, s[44:47], s56 offen offset:256 nt sc1\n"
      "buffer_store_dwordx2 v[226:227], v2, s[44:47], s56 offen offset:512 nt sc1\n"
      "v_max3_f32 v190, v190, v152, v153\n"
      "v_max3_f32 v190, v190, v154, v155\n"
      "v_mov_b32 v32, v190\n"
      "buffer_store_dwordx2 v[230:231], v2, s[44:47], s56 offen offset:768 nt sc1\n"
      "s_nop 0\n"
      "v_permlane16_swap_b32 v190, v32\n"
      "v_max_f32 v190, v190, v32\n"
      "v_mov_b32 v32, v190\n"
      "s_nop 1\n"
      "v_permlane32_swap_b32 v190, v32\n"
      "v_max_f32 v190, v190, v32\n"
      "v_sub_f32 v20, v20, v190\n"
      "v_sub_f32 v21, v21, v190\n"
      "v_sub_f32 v22, v22, v190\n"
      "s_waitcnt lgkmcnt(3)\n"
      "v_mfma_f32_16x16x32_bf16 v[32:35], a[128:131], v[116:119], 0\n"
      "v_sub_f32 v23, v23, v190\n"
      "v_sub_f32 v88, v88, v190\n"
      "s_waitcnt lgkmcnt(2)\n"
      "v_mfma_f32_16x16x32_bf16 v[32:35], a[132:135], v[124:127], v[32:35]\n"
      "v_exp_f32 v20, v20\n"
      "s_waitcnt lgkmcnt(1)\n"
      "v_mfma_f32_16x16x32_bf16 v[32:35], a[136:139], v[48:51], v[32:35]\n"
      "v_exp_f32 v21, v21\n"
      "s_waitcnt lgkmcnt(0)\n"
      "v_mfma_f32_16x16x32_bf16 v[32:35], a[140:143], v[252:255], v[32:35]\n"
      "v_exp_f32 v22, v22\n"
      "v_mfma_f32_16x16x32_bf16 v[28:31], a[144:147], v[116:119], 0\n"
      "v_exp_f32 v23, v23\n"
      "v_mfma_f32_16x16x32_bf16 v[28:31], a[148:151], v[124:127], v[28:31]\n"
      "v_cvt_pk_bf16_f32 v156, v20, v21\n"
      "v_cvt_pk_bf16_f32 v157, v22, v23\n"
      "v_mfma_f32_16x16x32_bf16 v[28:31], a[152:155], v[48:51], v[28:31]\n"
      "v_sub_f32 v89, v89, v190\n"
      "v_sub_f32 v90, v90, v190\n"
      "v_sub_f32 v91, v91, v190\n"
      "v_sub_f32 v24, v24, v190\n"
      "v_mfma_f32_16x16x32_bf16 v[28:31], a[156:159], v[252:255], v[28:31]\n"
      "v_sub_f32 v25, v25, v190\n"
      "v_sub_f32 v26, v26, v190\n"
      "v_sub_f32 v27, v27, v190\n"
      "v_sub_f32 v236, v236, v190\n"
      "v_sub_f32 v237, v237, v190\n"
      "v_sub_f32 v238, v238, v190\n"
      "v_mfma_f32_16x16x32_bf16 v[184:187], a[160:163], v[116:119], 0\n"
      "v_sub_f32 v239, v239, v190\n"
      "v_sub_f32 v232, v232, v190\n"
      "v_sub_f32 v233, v233, v190\n"
      "v_sub_f32 v234, v234, v190\n"
      "v_sub_f32 v235, v235, v190\n"
      "v_add_f32 v86, v20, v21\n"
      "v_add_f32 v87, v22, v23\n"
      "v_mfma_f32_16x16x32_bf16 v[184:187], a[164:167], v[124:127], v[184:187]\n"
      "v_exp_f32 v88, v88\n"
      "v_exp_f32 v89, v89\n"
      "v_exp_f32 v90, v90\n"
      "v_mfma_f32_16x16x32_bf16 v[184:187], a[168:171], v[48:51], v[184:187]\n"
      "v_exp_f32 v91, v91\n"
      "v_cvt_pk_bf16_f32 v20, v88, v89\n"
      "v_cvt_pk_bf16_f32 v21, v90, v91\n"
      "v_exp_f32 v24, v24\n"
      "v_mfma_f32_16x16x32_bf16 v[184:187], a[172:175], v[252:255], v[184:187]\n"
      "v_exp_f32 v25, v25\n"
      "v_exp_f32 v26, v26\n"
      "v_exp_f32 v27, v27\n"
      "v_cvt_pk_bf16_f32 v22, v24, v25\n"
      "v_mfma_f32_16x16x32_bf16 v[208:211], a[176:179], v[116:119], 0\n"
      "v_cvt_pk_bf16_f32 v23, v26, v27\n"
      "v_exp_f32 v236, v236\n"
      "v_exp_f32 v237, v237\n"
      "v_exp_f32 v238, v238\n"
      "v_mfma_f32_16x16x32_bf16 v[208:211], a[180:183], v[124:127], v[208:211]\n"
      "v_exp_f32 v239, v239\n"
      "v_cvt_pk_bf16_f32 v24, v236, v237\n"
      "v_cvt_pk_bf16_f32 v25, v238, v239\n"
      "v_exp_f32 v232, v232\n"
      "v_mfma_f32_16x16x32_bf16 v[208:211], a[184:187], v[48:51], v[208:211]\n"
      "v_exp_f32 v233, v233\n"
      "v_exp_f32 v234, v234\n"
      "v_exp_f32 v235, v235\n"
      "v_cvt_pk_bf16_f32 v26, v232, v233\n"
      "v_mfma_f32_16x16x32_bf16 v[208:211], a[188:191], v[252:255], v[208:211]\n"
      "v_cvt_pk_bf16_f32 v27, v234, v235\n"
      "v_sub_f32 v160, v160, v190\n"
      "v_sub_f32 v161, v161, v190\n"
      "v_sub_f32 v162, v162, v190\n"
      "v_sub_f32 v163, v163, v190\n"
      "v_sub_f32 v152, v152, v190\n"
      "v_mfma_f32_16x16x4_4b_bf16 v[224:239], v[156:157], v[18:19], 0 cbsz:2 abid:0\n"
      "s_add_u32 m0, s53, 8192\n"
      "s_add_u32 s54, s49, 8192\n"
      "buffer_load_dwordx4 v1, s[40:43], s54 offen offset:0 lds\n"
      "v_sub_f32 v153, v153, v190\n"
      "v_sub_f32 v154, v154, v190\n"
      "v_sub_f32 v155, v155, v190\n"
      "v_exp_f32 v160, v160\n"
      "v_exp_f32 v161, v161\n"
      "v_exp_f32 v162, v162\n"
      "v_exp_f32 v163, v163\n"
      "v_cvt_pk_bf16_f32 v188, v160, v161\n"
      "v_cvt_pk_bf16_f32 v189, v162, v163\n"
      "v_exp_f32 v152, v152\n"
      "v_exp_f32 v153, v153\n"
      "v_exp_f32 v154, v154\n"
      "v_smfmac_f32_16x16x64_bf16 v[86:89], v[12:15], v[20:27], v5\n"
      "v_exp_f32 v155, v155\n"
      "v_cvt_pk_bf16_f32 v190, v152, v153\n"
      "v_mfma_f32_16x16x32_bf16 v[224:227], v[20:23], v[52:55], v[224:227]\n"
      "buffer_load_dwordx4 v1, s[40:43], s54 offen offset:2048 lds\n"
      "s_add_u32 m0, s53, 9088\n"
      "v_cvt_pk_bf16_f32 v191, v154, v155\n"
      "v_cvt_pk_bf16_f32 v156, v32, v33\n"
      "v_cvt_pk_bf16_f32 v157, v34, v35\n"
      "v_mfma_f32_16x16x32_bf16 v[224:227], v[24:27], v[56:59], v[224:227]\n"
      "v_cvt_pk_bf16_f32 v158, v28, v29\n"
      "v_cvt_pk_bf16_f32 v159, v30, v31\n"
      "v_cvt_pk_bf16_f32 v32, v184, v185\n"
      "v_mfma_f32_16x16x32_bf16 v[224:227], v[188:191], v[120:123], v[224:227]\n"
      "buffer_load_dwordx4 v1, s[40:43], s54 offen offset:128 lds\n"
      "v_cvt_pk_bf16_f32 v33, v186, v187\n"
      "v_cvt_pk_bf16_f32 v34, v208, v209\n"
      "v_cvt_pk_bf16_f32 v35, v210, v211\n"
      "v_smfmac_f32_16x16x32_bf16 v[86:89], v[12:13], v[188:191], v5\n"
      "v_mfma_f32_16x16x32_bf16 v[228:231], v[20:23], v[128:131], v[228:231]\n"
      "buffer_load_dwordx4 v1, s[40:43], s54 offen offset:2176 lds\n"
      "v_mfma_f32_16x16x32_bf16 v[228:231], v[24:27], v[132:135], v[228:231]\n"
      "v_mfma_f32_16x16x32_bf16 v[228:231], v[188:191], v[68:71], v[228:231]\n"
      "v_mfma_f32_16x16x32_bf16 v[232:235], v[20:23], v[100:103], v[232:235]\n"
      "v_mfma_f32_16x16x32_bf16 v[232:235], v[24:27], v[104:107], v[232:235]\n"
      "v_mfma_f32_16x16x32_bf16 v[232:235], v[188:191], v[36:39], v[232:235]\n"
      "v_mfma_f32_16x16x32_bf16 v[236:239], v[20:23], v[192:195], v[236:239]\n"
      "v_add_f32 v86, v86, v87\n"
      "v_rcp_f32 v87, v86\n"
      "v_mfma_f32_16x16x32_bf16 v[236:239], v[24:27], v[196:199], v[236:239]\n"
      "ds_bpermute_b32 v88, v4, v87\n"
      "v_mfma_f32_16x16x32_bf16 v[236:239], v[188:191], v[240:243], v[236:239]\n"
      "s_waitcnt lgkmcnt(0)\n"
      "v_mul_f32_dpp v224, v88, v224 row_newbcast:0 row_mask:0xf bank_mask:0xf\n"
      "v_mul_f32_dpp v225, v88, v225 row_newbcast:4 row_mask:0xf bank_mask:0xf\n"
      "v_mul_f32_dpp v226, v88, v226 row_newbcast:8 row_mask:0xf bank_mask:0xf\n"
      "v_mul_f32_dpp v227, v88, v227 row_newbcast:12 row_mask:0xf bank_mask:0xf\n"
      "v_mul_f32_dpp v228, v88, v228 row_newbcast:0 row_mask:0xf bank_mask:0xf\n"
      "v_mul_f32_dpp v229, v88, v229 row_newbcast:4 row_mask:0xf bank_mask:0xf\n"
      "v_mfma_f32_16x16x32_bf16 v[224:227], v[148:151], a[192:195], v[224:227]\n"
      "v_mul_f32_dpp v230, v88, v230 row_newbcast:8 row_mask:0xf bank_mask:0xf\n"
      "v_mul_f32_dpp v231, v88, v231 row_newbcast:12 row_mask:0xf bank_mask:0xf\n"
      "v_mfma_f32_16x16x32_bf16 v[224:227], v[136:139], a[196:199], v[224:227]\n"
      "v_mul_f32_dpp v232, v88, v232 row_newbcast:0 row_mask:0xf bank_mask:0xf\n"
      "v_mul_f32_dpp v233, v88, v233 row_newbcast:4 row_mask:0xf bank_mask:0xf\n"
      "v_mul_f32_dpp v234, v88, v234 row_newbcast:8 row_mask:0xf bank_mask:0xf\n"
      "v_mfma_f32_16x16x32_bf16 v[224:227], v[140:143], a[200:203], v[224:227]\n"
      "v_mul_f32_dpp v235, v88, v235 row_newbcast:12 row_mask:0xf bank_mask:0xf\n"
      "v_mul_f32_dpp v236, v88, v236 row_newbcast:0 row_mask:0xf bank_mask:0xf\n"
      "v_mul_f32_dpp v237, v88, v237 row_newbcast:4 row_mask:0xf bank_mask:0xf\n"
      "v_mul_f32_dpp v238, v88, v238 row_newbcast:8 row_mask:0xf bank_mask:0xf\n"
      "v_mfma_f32_16x16x32_bf16 v[224:227], v[144:147], a[204:207], v[224:227]\n"
      "v_mul_f32_dpp v239, v88, v239 row_newbcast:12 row_mask:0xf bank_mask:0xf\n"
      "v_mfma_f32_16x16x32_bf16 v[228:231], v[148:151], a[208:211], v[228:231]\n"
      "v_mfma_f32_16x16x32_bf16 v[228:231], v[136:139], a[212:215], v[228:231]\n"
      "v_mfma_f32_16x16x32_bf16 v[228:231], v[140:143], a[216:219], v[228:231]\n"
      "v_mfma_f32_16x16x32_bf16 v[228:231], v[144:147], a[220:223], v[228:231]\n"
      "v_mfma_f32_16x16x32_bf16 v[232:235], v[148:151], a[224:227], v[232:235]\n"
      "v_mfma_f32_16x16x32_bf16 v[232:235], v[136:139], a[228:231], v[232:235]\n"
      "v_mfma_f32_16x16x32_bf16 v[232:235], v[140:143], a[232:235], v[232:235]\n"
      "v_mfma_f32_16x16x32_bf16 v[232:235], v[144:147], a[236:239], v[232:235]\n"
      "v_mfma_f32_16x16x32_bf16 v[236:239], v[148:151], a[240:243], v[236:239]\n"
      "v_mfma_f32_16x16x32_bf16 v[236:239], v[136:139], a[244:247], v[236:239]\n"
      "v_mfma_f32_16x16x32_bf16 v[236:239], v[140:143], a[248:251], v[236:239]\n"
      "v_mfma_f32_16x16x32_bf16 v[236:239], v[144:147], a[252:255], v[236:239]\n"
      "v_cvt_pk_bf16_f32 v224, v224, v228\n"
      "v_cvt_pk_bf16_f32 v228, v225, v229\n"
      "v_pk_max_i16 v224, v224, 0\n"
      "v_pk_max_i16 v228, v228, 0\n"
      "v_cvt_pk_bf16_f32 v226, v226, v230\n"
      "v_mfma_f32_16x16x32_bf16 v[140:143], v[172:175], v[156:159], 0\n"
      "v_cvt_pk_bf16_f32 v230, v227, v231\n"
      "v_pk_max_i16 v226, v226, 0\n"
      "v_cvt_pk_bf16_f32 v225, v232, v236\n"
      "v_cvt_pk_bf16_f32 v229, v233, v237\n"
      "v_mfma_f32_16x16x32_bf16 v[140:143], v[176:179], v[32:35], v[140:143]\n"
      "v_pk_max_i16 v225, v225, 0\n"
      "v_pk_max_i16 v229, v229, 0\n"
      "v_cvt_pk_bf16_f32 v227, v234, v238\n"
      "v_cvt_pk_bf16_f32 v231, v235, v239\n"
      "v_pk_max_i16 v227, v227, 0\n"
      "v_mfma_f32_16x16x32_bf16 v[88:91], v[108:111], v[156:159], 0\n"
      "v_pk_max_i16 v230, v230, 0\n"
      "v_pk_max_i16 v231, v231, 0\n"
      "v_max_f32 v188, v140, v141\n"
      "v_max3_f32 v188, v188, v142, v143\n"
      "v_mfma_f32_16x16x32_bf16 v[88:91], v[112:115], v[32:35], v[88:91]\n"
      "v_mfma_f32_16x16x32_bf16 v[144:147], v[164:167], v[156:159], 0\n"
      "v_mfma_f32_16x16x32_bf16 v[144:147], v[168:171], v[32:35], v[144:147]\n"
      "v_mfma_f32_16x16x32_bf16 v[236:239], v[40:43], v[156:159], 0\n"
      "v_mfma_f32_16x16x32_bf16 v[236:239], v[44:47], v[32:35], v[236:239]\n"
      "v_mfma_f32_16x16x32_bf16 v[232:235], v[92:95], v[156:159], 0\n"
      "v_mfma_f32_16x16x32_bf16 v[232:235], v[96:99], v[32:35], v[232:235]\n"
      "v_mfma_f32_16x16x32_bf16 v[160:163], v[216:219], v[156:159], 0\n"
      "v_mfma_f32_16x16x32_bf16 v[160:163], v[220:223], v[32:35], v[160:163]\n"
      "v_max3_f32 v188, v188, v88, v89\n"
      "v_max3_f32 v188, v188, v90, v91\n"
      "v_max3_f32 v188, v188, v144, v145\n"
      "v_max3_f32 v188, v188, v146, v147\n"
      "v_max3_f32 v188, v188, v236, v237\n"
      "v_max3_f32 v188, v188, v238, v239\n"
      "v_max3_f32 v188, v188, v232, v233\n"
      "v_mfma_f32_16x16x32_bf16 v[152:155], v[244:247], v[156:159], 0\n"
      "v_max3_f32 v188, v188, v234, v235\n"
      "v_max3_f32 v188, v188, v160, v161\n"
      "v_max3_f32 v188, v188, v162, v163\n"
      "v_mfma_f32_16x16x32_bf16 v[152:155], v[248:251], v[32:35], v[152:155]\n"
      "ds_read_b128 v[136:139], v0 offset:4096\n"
      "ds_read_b128 v[148:151], v0 offset:4608\n"
      "ds_read_b128 v[24:27], v0 offset:5120\n"
      "ds_read_b128 v[20:23], v0 offset:5632\n"
      "s_add_u32 s56, s48, 12288\n"
      "buffer_store_dwordx2 v[224:225], v2, s[44:47], s56 offen offset:0 nt sc1\n"
      "buffer_store_dwordx2 v[228:229], v2, s[44:47], s56 offen offset:256 nt sc1\n"
      "buffer_store_dwordx2 v[226:227], v2, s[44:47], s56 offen offset:512 nt sc1\n"
      "v_max3_f32 v188, v188, v152, v153\n"
      "v_max3_f32 v188, v188, v154, v155\n"
      "v_mov_b32 v32, v188\n"
      "buffer_store_dwordx2 v[230:231], v2, s[44:47], s56 offen offset:768 nt sc1\n"
      "s_nop 0\n"
      "v_permlane16_swap_b32 v188, v32\n"
      "v_max_f32 v188, v188, v32\n"
      "v_mov_b32 v32, v188\n"
      "s_nop 1\n"
      "v_permlane32_swap_b32 v188, v32\n"
      "v_max_f32 v188, v188, v32\n"
      "v_sub_f32 v140, v140, v188\n"
      "v_sub_f32 v141, v141, v188\n"
      "v_sub_f32 v142, v142, v188\n"
      "s_waitcnt lgkmcnt(3)\n"
      "v_mfma_f32_16x16x32_bf16 v[32:35], a[128:131], v[136:139], 0\n"
      "v_sub_f32 v143, v143, v188\n"
      "v_sub_f32 v88, v88, v188\n"
      "s_waitcnt lgkmcnt(2)\n"
      "v_mfma_f32_16x16x32_bf16 v[32:35], a[132:135], v[148:151], v[32:35]\n"
      "v_exp_f32 v140, v140\n"
      "s_waitcnt lgkmcnt(1)\n"
      "v_mfma_f32_16x16x32_bf16 v[32:35], a[136:139], v[24:27], v[32:35]\n"
      "v_exp_f32 v141, v141\n"
      "s_waitcnt lgkmcnt(0)\n"
      "v_mfma_f32_16x16x32_bf16 v[32:35], a[140:143], v[20:23], v[32:35]\n"
      "v_exp_f32 v142, v142\n"
      "v_mfma_f32_16x16x32_bf16 v[28:31], a[144:147], v[136:139], 0\n"
      "v_exp_f32 v143, v143\n"
      "v_mfma_f32_16x16x32_bf16 v[28:31], a[148:151], v[148:151], v[28:31]\n"
      "v_cvt_pk_bf16_f32 v156, v140, v141\n"
      "v_cvt_pk_bf16_f32 v157, v142, v143\n"
      "v_mfma_f32_16x16x32_bf16 v[28:31], a[152:155], v[24:27], v[28:31]\n"
      "v_sub_f32 v89, v89, v188\n"
      "v_sub_f32 v90, v90, v188\n"
      "v_sub_f32 v91, v91, v188\n"
      "v_sub_f32 v144, v144, v188\n"
      "v_mfma_f32_16x16x32_bf16 v[28:31], a[156:159], v[20:23], v[28:31]\n"
      "v_sub_f32 v145, v145, v188\n"
      "v_sub_f32 v146, v146, v188\n"
      "v_sub_f32 v147, v147, v188\n"
      "v_sub_f32 v236, v236, v188\n"
      "v_sub_f32 v237, v237, v188\n"
      "v_sub_f32 v238, v238, v188\n"
      "v_mfma_f32_16x16x32_bf16 v[184:187], a[160:163], v[136:139], 0\n"
      "v_sub_f32 v239, v239, v188\n"
      "v_sub_f32 v232, v232, v188\n"
      "v_sub_f32 v233, v233, v188\n"
      "v_sub_f32 v234, v234, v188\n"
      "v_sub_f32 v235, v235, v188\n"
      "v_add_f32 v86, v140, v141\n"
      "v_add_f32 v87, v142, v143\n"
      "v_mfma_f32_16x16x32_bf16 v[184:187], a[164:167], v[148:151], v[184:187]\n"
      "v_exp_f32 v88, v88\n"
      "v_exp_f32 v89, v89\n"
      "v_exp_f32 v90, v90\n"
      "v_mfma_f32_16x16x32_bf16 v[184:187], a[168:171], v[24:27], v[184:187]\n"
      "v_exp_f32 v91, v91\n"
      "v_cvt_pk_bf16_f32 v140, v88, v89\n"
      "v_cvt_pk_bf16_f32 v141, v90, v91\n"
      "v_exp_f32 v144, v144\n"
      "v_mfma_f32_16x16x32_bf16 v[184:187], a[172:175], v[20:23], v[184:187]\n"
      "v_exp_f32 v145, v145\n"
      "v_exp_f32 v146, v146\n"
      "v_exp_f32 v147, v147\n"
      "v_cvt_pk_bf16_f32 v142, v144, v145\n"
      "v_mfma_f32_16x16x32_bf16 v[208:211], a[176:179], v[136:139], 0\n"
      "v_cvt_pk_bf16_f32 v143, v146, v147\n"
      "v_exp_f32 v236, v236\n"
      "v_exp_f32 v237, v237\n"
      "v_exp_f32 v238, v238\n"
      "v_mfma_f32_16x16x32_bf16 v[208:211], a[180:183], v[148:151], v[208:211]\n"
      "v_exp_f32 v239, v239\n"
      "v_cvt_pk_bf16_f32 v144, v236, v237\n"
      "v_cvt_pk_bf16_f32 v145, v238, v239\n"
      "v_exp_f32 v232, v232\n"
      "v_mfma_f32_16x16x32_bf16 v[208:211], a[184:187], v[24:27], v[208:211]\n"
      "v_exp_f32 v233, v233\n"
      "v_exp_f32 v234, v234\n"
      "v_exp_f32 v235, v235\n"
      "v_cvt_pk_bf16_f32 v146, v232, v233\n"
      "v_mfma_f32_16x16x32_bf16 v[208:211], a[188:191], v[20:23], v[208:211]\n"
      "v_cvt_pk_bf16_f32 v147, v234, v235\n"
      "v_sub_f32 v160, v160, v188\n"
      "v_sub_f32 v161, v161, v188\n"
      "v_sub_f32 v162, v162, v188\n"
      "v_sub_f32 v163, v163, v188\n"
      "v_sub_f32 v152, v152, v188\n"
      "v_mfma_f32_16x16x4_4b_bf16 v[224:239], v[156:157], v[18:19], 0 cbsz:2 abid:0\n"
      "s_add_u32 m0, s53, 4096\n"
      "s_add_u32 s54, s49, 4096\n"
      "buffer_load_dwordx4 v1, s[40:43], s54 offen offset:0 lds\n"
      "v_sub_f32 v153, v153, v188\n"
      "v_sub_f32 v154, v154, v188\n"
      "v_sub_f32 v155, v155, v188\n"
      "v_exp_f32 v160, v160\n"
      "v_exp_f32 v161, v161\n"
      "v_exp_f32 v162, v162\n"
      "v_exp_f32 v163, v163\n"
      "v_cvt_pk_bf16_f32 v188, v160, v161\n"
      "v_cvt_pk_bf16_f32 v189, v162, v163\n"
      "v_exp_f32 v152, v152\n"
      "v_exp_f32 v153, v153\n"
      "v_exp_f32 v154, v154\n"
      "v_smfmac_f32_16x16x64_bf16 v[86:89], v[12:15], v[140:147], v5\n"
      "v_exp_f32 v155, v155\n"
      "v_cvt_pk_bf16_f32 v190, v152, v153\n"
      "v_mfma_f32_16x16x32_bf16 v[224:227], v[140:143], v[52:55], v[224:227]\n"
      "buffer_load_dwordx4 v1, s[40:43], s54 offen offset:2048 lds\n"
      "s_add_u32 m0, s53, 4992\n"
      "v_cvt_pk_bf16_f32 v191, v154, v155\n"
      "v_cvt_pk_bf16_f32 v156, v32, v33\n"
      "v_cvt_pk_bf16_f32 v157, v34, v35\n"
      "v_mfma_f32_16x16x32_bf16 v[224:227], v[144:147], v[56:59], v[224:227]\n"
      "v_cvt_pk_bf16_f32 v158, v28, v29\n"
      "v_cvt_pk_bf16_f32 v159, v30, v31\n"
      "v_cvt_pk_bf16_f32 v32, v184, v185\n"
      "v_cvt_pk_bf16_f32 v33, v186, v187\n"
      "v_mfma_f32_16x16x32_bf16 v[224:227], v[188:191], v[120:123], v[224:227]\n"
      "buffer_load_dwordx4 v1, s[40:43], s54 offen offset:128 lds\n"
      "v_cvt_pk_bf16_f32 v34, v208, v209\n"
      "v_cvt_pk_bf16_f32 v35, v210, v211\n"
      "v_smfmac_f32_16x16x32_bf16 v[86:89], v[12:13], v[188:191], v5\n"
      "v_mfma_f32_16x16x32_bf16 v[228:231], v[140:143], v[128:131], v[228:231]\n"
      "buffer_load_dwordx4 v1, s[40:43], s54 offen offset:2176 lds\n"
      "v_mfma_f32_16x16x32_bf16 v[228:231], v[144:147], v[132:135], v[228:231]\n"
      "v_mfma_f32_16x16x32_bf16 v[228:231], v[188:191], v[68:71], v[228:231]\n"
      "v_mfma_f32_16x16x32_bf16 v[232:235], v[140:143], v[100:103], v[232:235]\n"
      "v_mfma_f32_16x16x32_bf16 v[232:235], v[144:147], v[104:107], v[232:235]\n"
      "v_mfma_f32_16x16x32_bf16 v[232:235], v[188:191], v[36:39], v[232:235]\n"
      "v_mfma_f32_16x16x32_bf16 v[236:239], v[140:143], v[192:195], v[236:239]\n"
      "v_add_f32 v86, v86, v87\n"
      "v_rcp_f32 v87, v86\n"
      "v_mfma_f32_16x16x32_bf16 v[236:239], v[144:147], v[196:199], v[236:239]\n"
      "ds_bpermute_b32 v88, v4, v87\n"
      "v_mfma_f32_16x16x32_bf16 v[236:239], v[188:191], v[240:243], v[236:239]\n"
      "s_waitcnt lgkmcnt(0)\n"
      "v_mul_f32_dpp v224, v88, v224 row_newbcast:0 row_mask:0xf bank_mask:0xf\n"
      "v_mul_f32_dpp v225, v88, v225 row_newbcast:4 row_mask:0xf bank_mask:0xf\n"
      "v_mul_f32_dpp v226, v88, v226 row_newbcast:8 row_mask:0xf bank_mask:0xf\n"
      "v_mul_f32_dpp v227, v88, v227 row_newbcast:12 row_mask:0xf bank_mask:0xf\n"
      "v_mul_f32_dpp v228, v88, v228 row_newbcast:0 row_mask:0xf bank_mask:0xf\n"
      "v_mul_f32_dpp v229, v88, v229 row_newbcast:4 row_mask:0xf bank_mask:0xf\n"
      "v_mfma_f32_16x16x32_bf16 v[224:227], v[116:119], a[192:195], v[224:227]\n"
      "v_mul_f32_dpp v230, v88, v230 row_newbcast:8 row_mask:0xf bank_mask:0xf\n"
      "v_mul_f32_dpp v231, v88, v231 row_newbcast:12 row_mask:0xf bank_mask:0xf\n"
      "v_mfma_f32_16x16x32_bf16 v[224:227], v[124:127], a[196:199], v[224:227]\n"
      "v_mul_f32_dpp v232, v88, v232 row_newbcast:0 row_mask:0xf bank_mask:0xf\n"
      "v_mul_f32_dpp v233, v88, v233 row_newbcast:4 row_mask:0xf bank_mask:0xf\n"
      "v_mul_f32_dpp v234, v88, v234 row_newbcast:8 row_mask:0xf bank_mask:0xf\n"
      "v_mul_f32_dpp v235, v88, v235 row_newbcast:12 row_mask:0xf bank_mask:0xf\n"
      "v_mfma_f32_16x16x32_bf16 v[224:227], v[48:51], a[200:203], v[224:227]\n"
      "v_mul_f32_dpp v236, v88, v236 row_newbcast:0 row_mask:0xf bank_mask:0xf\n"
      "v_mul_f32_dpp v237, v88, v237 row_newbcast:4 row_mask:0xf bank_mask:0xf\n"
      "v_mul_f32_dpp v238, v88, v238 row_newbcast:8 row_mask:0xf bank_mask:0xf\n"
      "v_mfma_f32_16x16x32_bf16 v[224:227], v[252:255], a[204:207], v[224:227]\n"
      "v_mul_f32_dpp v239, v88, v239 row_newbcast:12 row_mask:0xf bank_mask:0xf\n"
      "v_mfma_f32_16x16x32_bf16 v[228:231], v[116:119], a[208:211], v[228:231]\n"
      "v_mfma_f32_16x16x32_bf16 v[228:231], v[124:127], a[212:215], v[228:231]\n"
      "v_mfma_f32_16x16x32_bf16 v[228:231], v[48:51], a[216:219], v[228:231]\n"
      "v_mfma_f32_16x16x32_bf16 v[228:231], v[252:255], a[220:223], v[228:231]\n"
      "v_mfma_f32_16x16x32_bf16 v[232:235], v[116:119], a[224:227], v[232:235]\n"
      "v_mfma_f32_16x16x32_bf16 v[232:235], v[124:127], a[228:231], v[232:235]\n"
      "v_mfma_f32_16x16x32_bf16 v[232:235], v[48:51], a[232:235], v[232:235]\n"
      "v_mfma_f32_16x16x32_bf16 v[232:235], v[252:255], a[236:239], v[232:235]\n"
      "v_mfma_f32_16x16x32_bf16 v[236:239], v[116:119], a[240:243], v[236:239]\n"
      "v_mfma_f32_16x16x32_bf16 v[236:239], v[124:127], a[244:247], v[236:239]\n"
      "v_mfma_f32_16x16x32_bf16 v[236:239], v[48:51], a[248:251], v[236:239]\n"
      "v_mfma_f32_16x16x32_bf16 v[236:239], v[252:255], a[252:255], v[236:239]\n"
      "v_cvt_pk_bf16_f32 v224, v224, v228\n"
      "v_cvt_pk_bf16_f32 v228, v225, v229\n"
      "v_pk_max_i16 v224, v224, 0\n"
      "v_pk_max_i16 v228, v228, 0\n"
      "v_cvt_pk_bf16_f32 v226, v226, v230\n"
      "v_mfma_f32_16x16x32_bf16 v[140:143], v[172:175], v[156:159], 0\n"
      "v_cvt_pk_bf16_f32 v230, v227, v231\n"
      "v_pk_max_i16 v226, v226, 0\n"
      "v_cvt_pk_bf16_f32 v225, v232, v236\n"
      "v_cvt_pk_bf16_f32 v229, v233, v237\n"
      "v_mfma_f32_16x16x32_bf16 v[140:143], v[176:179], v[32:35], v[140:143]\n"
      "v_pk_max_i16 v225, v225, 0\n"
      "v_pk_max_i16 v229, v229, 0\n"
      "v_cvt_pk_bf16_f32 v227, v234, v238\n"
      "v_cvt_pk_bf16_f32 v231, v235, v239\n"
      "v_pk_max_i16 v227, v227, 0\n"
      "v_mfma_f32_16x16x32_bf16 v[88:91], v[108:111], v[156:159], 0\n"
      "v_pk_max_i16 v230, v230, 0\n"
      "v_pk_max_i16 v231, v231, 0\n"
      "v_max_f32 v188, v140, v141\n"
      "v_max3_f32 v188, v188, v142, v143\n"
      "v_mfma_f32_16x16x32_bf16 v[88:91], v[112:115], v[32:35], v[88:91]\n"
      "v_mfma_f32_16x16x32_bf16 v[144:147], v[164:167], v[156:159], 0\n"
      "v_mfma_f32_16x16x32_bf16 v[144:147], v[168:171], v[32:35], v[144:147]\n"
      "v_mfma_f32_16x16x32_bf16 v[236:239], v[40:43], v[156:159], 0\n"
      "v_mfma_f32_16x16x32_bf16 v[236:239], v[44:47], v[32:35], v[236:239]\n"
      "v_mfma_f32_16x16x32_bf16 v[232:235], v[92:95], v[156:159], 0\n"
      "v_mfma_f32_16x16x32_bf16 v[232:235], v[96:99], v[32:35], v[232:235]\n"
      "v_mfma_f32_16x16x32_bf16 v[160:163], v[216:219], v[156:159], 0\n"
      "v_mfma_f32_16x16x32_bf16 v[160:163], v[220:223], v[32:35], v[160:163]\n"
      "v_max3_f32 v188, v188, v88, v89\n"
      "v_max3_f32 v188, v188, v90, v91\n"
      "v_max3_f32 v188, v188, v144, v145\n"
      "v_max3_f32 v188, v188, v146, v147\n"
      "v_max3_f32 v188, v188, v236, v237\n"
      "v_max3_f32 v188, v188, v238, v239\n"
      "v_max3_f32 v188, v188, v232, v233\n"
      "v_mfma_f32_16x16x32_bf16 v[152:155], v[244:247], v[156:159], 0\n"
      "v_max3_f32 v188, v188, v234, v235\n"
      "v_max3_f32 v188, v188, v160, v161\n"
      "v_max3_f32 v188, v188, v162, v163\n"
      "v_mfma_f32_16x16x32_bf16 v[152:155], v[248:251], v[32:35], v[152:155]\n"
      "ds_read_b128 v[124:127], v0 offset:0\n"
      "ds_read_b128 v[48:51], v0 offset:512\n"
      "ds_read_b128 v[116:119], v0 offset:1024\n"
      "ds_read_b128 v[252:255], v0 offset:1536\n"
      "s_add_u32 s56, s48, 8192\n"
      "buffer_store_dwordx2 v[224:225], v2, s[44:47], s56 offen offset:0 nt sc1\n"
      "buffer_store_dwordx2 v[228:229], v2, s[44:47], s56 offen offset:256 nt sc1\n"
      "buffer_store_dwordx2 v[226:227], v2, s[44:47], s56 offen offset:512 nt sc1\n"
      "v_max3_f32 v188, v188, v152, v153\n"
      "v_max3_f32 v188, v188, v154, v155\n"
      "v_mov_b32 v32, v188\n"
      "buffer_store_dwordx2 v[230:231], v2, s[44:47], s56 offen offset:768 nt sc1\n"
      "s_nop 0\n"
      "v_permlane16_swap_b32 v188, v32\n"
      "v_max_f32 v188, v188, v32\n"
      "v_mov_b32 v32, v188\n"
      "s_nop 1\n"
      "v_permlane32_swap_b32 v188, v32\n"
      "v_max_f32 v188, v188, v32\n"
      "v_sub_f32 v140, v140, v188\n"
      "v_sub_f32 v141, v141, v188\n"
      "v_sub_f32 v142, v142, v188\n"
      "s_waitcnt lgkmcnt(3)\n"
      "v_mfma_f32_16x16x32_bf16 v[32:35], a[128:131], v[124:127], 0\n"
      "v_sub_f32 v143, v143, v188\n"
      "v_sub_f32 v88, v88, v188\n"
      "s_waitcnt lgkmcnt(2)\n"
      "v_mfma_f32_16x16x32_bf16 v[32:35], a[132:135], v[48:51], v[32:35]\n"
      "v_exp_f32 v140, v140\n"
      "s_waitcnt lgkmcnt(1)\n"
      "v_mfma_f32_16x16x32_bf16 v[32:35], a[136:139], v[116:119], v[32:35]\n"
      "v_exp_f32 v141, v141\n"
      "s_waitcnt lgkmcnt(0)\n"
      "v_mfma_f32_16x16x32_bf16 v[32:35], a[140:143], v[252:255], v[32:35]\n"
      "v_exp_f32 v142, v142\n"
      "v_mfma_f32_16x16x32_bf16 v[28:31], a[144:147], v[124:127], 0\n"
      "v_exp_f32 v143, v143\n"
      "v_mfma_f32_16x16x32_bf16 v[28:31], a[148:151], v[48:51], v[28:31]\n"
      "v_cvt_pk_bf16_f32 v190, v140, v141\n"
      "v_cvt_pk_bf16_f32 v191, v142, v143\n"
      "v_mfma_f32_16x16x32_bf16 v[28:31], a[152:155], v[116:119], v[28:31]\n"
      "v_sub_f32 v89, v89, v188\n"
      "v_sub_f32 v90, v90, v188\n"
      "v_sub_f32 v91, v91, v188\n"
      "v_sub_f32 v144, v144, v188\n"
      "v_mfma_f32_16x16x32_bf16 v[28:31], a[156:159], v[252:255], v[28:31]\n"
      "v_sub_f32 v145, v145, v188\n"
      "v_sub_f32 v146, v146, v188\n"
      "v_sub_f32 v147, v147, v188\n"
      "v_sub_f32 v236, v236, v188\n"
      "v_sub_f32 v237, v237, v188\n"
      "v_sub_f32 v238, v238, v188\n"
      "v_mfma_f32_16x16x32_bf16 v[184:187], a[160:163], v[124:127], 0\n"
      "v_sub_f32 v239, v239, v188\n"
      "v_sub_f32 v232, v232, v188\n"
      "v_sub_f32 v233, v233, v188\n"
      "v_sub_f32 v234, v234, v188\n"
      "v_sub_f32 v235, v235, v188\n"
      "v_add_f32 v86, v140, v141\n"
      "v_add_f32 v87, v142, v143\n"
      "v_mfma_f32_16x16x32_bf16 v[184:187], a[164:167], v[48:51], v[184:187]\n"
      "v_exp_f32 v88, v88\n"
      "v_exp_f32 v89, v89\n"
      "v_exp_f32 v90, v90\n"
      "v_mfma_f32_16x16x32_bf16 v[184:187], a[168:171], v[116:119], v[184:187]\n"
      "v_exp_f32 v91, v91\n"
      "v_cvt_pk_bf16_f32 v140, v88, v89\n"
      "v_cvt_pk_bf16_f32 v141, v90, v91\n"
      "v_exp_f32 v144, v144\n"
      "v_mfma_f32_16x16x32_bf16 v[184:187], a[172:175], v[252:255], v[184:187]\n"
      "v_exp_f32 v145, v145\n"
      "v_exp_f32 v146, v146\n"
      "v_exp_f32 v147, v147\n"
      "v_cvt_pk_bf16_f32 v142, v144, v145\n"
      "v_mfma_f32_16x16x32_bf16 v[208:211], a[176:179], v[124:127], 0\n"
      "v_cvt_pk_bf16_f32 v143, v146, v147\n"
      "v_exp_f32 v236, v236\n"
      "v_exp_f32 v237, v237\n"
      "v_exp_f32 v238, v238\n"
      "v_mfma_f32_16x16x32_bf16 v[208:211], a[180:183], v[48:51], v[208:211]\n"
      "v_exp_f32 v239, v239\n"
      "v_cvt_pk_bf16_f32 v144, v236, v237\n"
      "v_cvt_pk_bf16_f32 v145, v238, v239\n"
      "v_exp_f32 v232, v232\n"
      "v_mfma_f32_16x16x32_bf16 v[208:211], a[184:187], v[116:119], v[208:211]\n"
      "v_exp_f32 v233, v233\n"
      "v_exp_f32 v234, v234\n"
      "v_exp_f32 v235, v235\n"
      "v_cvt_pk_bf16_f32 v146, v232, v233\n"
      "v_mfma_f32_16x16x32_bf16 v[208:211], a[188:191], v[252:255], v[208:211]\n"
      "ds_read_b128 v[156:159], v10 offset:0\n"
      "v_cvt_pk_bf16_f32 v147, v234, v235\n"
      "v_sub_f32 v160, v160, v188\n"
      "v_sub_f32 v161, v161, v188\n"
      "v_sub_f32 v162, v162, v188\n"
      "v_sub_f32 v163, v163, v188\n"
      "v_sub_f32 v152, v152, v188\n"
      "v_mfma_f32_16x16x4_4b_bf16 v[224:239], v[190:191], v[18:19], 0 cbsz:2 abid:0\n"
      "s_add_u32 m0, s53, 0\n"
      "s_add_u32 s54, s49, 0\n"
      "buffer_load_dwordx4 v1, s[40:43], s54 offen offset:0 lds\n"
      "v_sub_f32 v153, v153, v188\n"
      "v_sub_f32 v154, v154, v188\n"
      "v_sub_f32 v155, v155, v188\n"
      "v_exp_f32 v160, v160\n"
      "v_exp_f32 v161, v161\n"
      "v_exp_f32 v162, v162\n"
      "v_exp_f32 v163, v163\n"
      "v_cvt_pk_bf16_f32 v188, v160, v161\n"
      "v_cvt_pk_bf16_f32 v189, v162, v163\n"
      "v_exp_f32 v152, v152\n"
      "v_exp_f32 v153, v153\n"
      "v_exp_f32 v154, v154\n"
      "v_smfmac_f32_16x16x64_bf16 v[86:89], v[12:15], v[140:147], v5\n"
      "v_exp_f32 v155, v155\n"
      "v_cvt_pk_bf16_f32 v190, v152, v153\n"
      "v_mfma_f32_16x16x32_bf16 v[224:227], v[140:143], v[52:55], v[224:227]\n"
      "buffer_load_dwordx4 v1, s[40:43], s54 offen offset:2048 lds\n"
      "s_add_u32 m0, s53, 896\n"
      "v_cvt_pk_bf16_f32 v191, v154, v155\n"
      "v_cvt_pk_bf16_f32 v152, v32, v33\n"
      "v_cvt_pk_bf16_f32 v153, v34, v35\n"
      "v_cvt_pk_bf16_f32 v154, v28, v29\n"
      "v_mfma_f32_16x16x32_bf16 v[224:227], v[144:147], v[56:59], v[224:227]\n"
      "v_cvt_pk_bf16_f32 v155, v30, v31\n"
      "v_cvt_pk_bf16_f32 v32, v184, v185\n"
      "v_cvt_pk_bf16_f32 v33, v186, v187\n"
      "v_cvt_pk_bf16_f32 v34, v208, v209\n"
      "v_mfma_f32_16x16x32_bf16 v[224:227], v[188:191], v[120:123], v[224:227]\n"
      "buffer_load_dwordx4 v1, s[40:43], s54 offen offset:128 lds\n"
      "v_cvt_pk_bf16_f32 v35, v210, v211\n"
      "v_smfmac_f32_16x16x32_bf16 v[86:89], v[12:13], v[188:191], v5\n"
      "v_mfma_f32_16x16x32_bf16 v[228:231], v[140:143], v[128:131], v[228:231]\n"
      "buffer_load_dwordx4 v1, s[40:43], s54 offen offset:2176 lds\n"
      "v_mfma_f32_16x16x32_bf16 v[228:231], v[144:147], v[132:135], v[228:231]\n"
      "v_mfma_f32_16x16x32_bf16 v[228:231], v[188:191], v[68:71], v[228:231]\n"
      "v_add_u32 v10, 64, v10\n"
      "v_mfma_f32_16x16x32_bf16 v[232:235], v[140:143], v[100:103], v[232:235]\n"
      "v_mfma_f32_16x16x32_bf16 v[232:235], v[144:147], v[104:107], v[232:235]\n"
      "v_mfma_f32_16x16x32_bf16 v[232:235], v[188:191], v[36:39], v[232:235]\n"
      "v_add_f32 v86, v86, v87\n"
      "v_rcp_f32 v87, v86\n"
      "v_mfma_f32_16x16x32_bf16 v[236:239], v[140:143], v[192:195], v[236:239]\n"
      "ds_bpermute_b32 v88, v4, v87\n"
      "v_mfma_f32_16x16x32_bf16 v[236:239], v[144:147], v[196:199], v[236:239]\n"
      "v_mfma_f32_16x16x32_bf16 v[236:239], v[188:191], v[240:243], v[236:239]\n"
      "s_waitcnt lgkmcnt(0)\n"
      "v_mul_f32_dpp v224, v88, v224 row_newbcast:0 row_mask:0xf bank_mask:0xf\n"
      "v_mul_f32_dpp v225, v88, v225 row_newbcast:4 row_mask:0xf bank_mask:0xf\n"
      "v_mul_f32_dpp v226, v88, v226 row_newbcast:8 row_mask:0xf bank_mask:0xf\n"
      "v_mul_f32_dpp v227, v88, v227 row_newbcast:12 row_mask:0xf bank_mask:0xf\n"
      "v_mul_f32_dpp v228, v88, v228 row_newbcast:0 row_mask:0xf bank_mask:0xf\n"
      "v_mul_f32_dpp v229, v88, v229 row_newbcast:4 row_mask:0xf bank_mask:0xf\n"
      "v_mfma_f32_16x16x32_bf16 v[224:227], v[136:139], a[192:195], v[224:227]\n"
      "v_mul_f32_dpp v230, v88, v230 row_newbcast:8 row_mask:0xf bank_mask:0xf\n"
      "v_mul_f32_dpp v231, v88, v231 row_newbcast:12 row_mask:0xf bank_mask:0xf\n"
      "v_mul_f32_dpp v232, v88, v232 row_newbcast:0 row_mask:0xf bank_mask:0xf\n"
      "v_mfma_f32_16x16x32_bf16 v[224:227], v[148:151], a[196:199], v[224:227]\n"
      "v_mul_f32_dpp v233, v88, v233 row_newbcast:4 row_mask:0xf bank_mask:0xf\n"
      "v_mul_f32_dpp v234, v88, v234 row_newbcast:8 row_mask:0xf bank_mask:0xf\n"
      "v_mul_f32_dpp v235, v88, v235 row_newbcast:12 row_mask:0xf bank_mask:0xf\n"
      "v_mul_f32_dpp v236, v88, v236 row_newbcast:0 row_mask:0xf bank_mask:0xf\n"
      "v_mfma_f32_16x16x32_bf16 v[224:227], v[24:27], a[200:203], v[224:227]\n"
      "v_mul_f32_dpp v237, v88, v237 row_newbcast:4 row_mask:0xf bank_mask:0xf\n"
      "v_mul_f32_dpp v238, v88, v238 row_newbcast:8 row_mask:0xf bank_mask:0xf\n"
      "v_mul_f32_dpp v239, v88, v239 row_newbcast:12 row_mask:0xf bank_mask:0xf\n"
      "v_mfma_f32_16x16x32_bf16 v[224:227], v[20:23], a[204:207], v[224:227]\n"
      "v_mfma_f32_16x16x32_bf16 v[228:231], v[136:139], a[208:211], v[228:231]\n"
      "v_mfma_f32_16x16x32_bf16 v[228:231], v[148:151], a[212:215], v[228:231]\n"
      "v_mfma_f32_16x16x32_bf16 v[228:231], v[24:27], a[216:219], v[228:231]\n"
      "v_mfma_f32_16x16x32_bf16 v[228:231], v[20:23], a[220:223], v[228:231]\n"
      "v_mfma_f32_16x16x32_bf16 v[232:235], v[136:139], a[224:227], v[232:235]\n"
      "v_mfma_f32_16x16x32_bf16 v[232:235], v[148:151], a[228:231], v[232:235]\n"
      "v_mfma_f32_16x16x32_bf16 v[232:235], v[24:27], a[232:235], v[232:235]\n"
      "v_mfma_f32_16x16x32_bf16 v[232:235], v[20:23], a[236:239], v[232:235]\n"
      "v_mfma_f32_16x16x32_bf16 v[236:239], v[136:139], a[240:243], v[236:239]\n"
      "v_mfma_f32_16x16x32_bf16 v[236:239], v[148:151], a[244:247], v[236:239]\n"
      "v_mfma_f32_16x16x32_bf16 v[236:239], v[24:27], a[248:251], v[236:239]\n"
      "v_mfma_f32_16x16x32_bf16 v[236:239], v[20:23], a[252:255], v[236:239]\n"
      "v_cvt_pk_bf16_f32 v224, v224, v228\n"
      "v_cvt_pk_bf16_f32 v228, v225, v229\n"
      "v_pk_max_i16 v224, v224, 0\n"
      "v_pk_max_i16 v228, v228, 0\n"
      "v_cvt_pk_bf16_f32 v226, v226, v230\n"
      "v_cvt_pk_bf16_f32 v230, v227, v231\n"
      "v_pk_max_i16 v226, v226, 0\n"
      "v_mfma_f32_16x16x32_bf16 v[148:151], v[172:175], v[152:155], 0\n"
      "v_cvt_pk_bf16_f32 v225, v232, v236\n"
      "v_cvt_pk_bf16_f32 v229, v233, v237\n"
      "v_pk_max_i16 v225, v225, 0\n"
      "v_pk_max_i16 v229, v229, 0\n"
      "v_cvt_pk_bf16_f32 v227, v234, v238\n"
      "v_cvt_pk_bf16_f32 v231, v235, v239\n"
      "v_pk_max_i16 v227, v227, 0\n"
      "v_mfma_f32_16x16x32_bf16 v[148:151], v[176:179], v[32:35], v[148:151]\n"
      "v_pk_max_i16 v230, v230, 0\n"
      "v_pk_max_i16 v231, v231, 0\n"
      "v_mfma_f32_16x16x32_bf16 v[136:139], v[108:111], v[152:155], 0\n"
      "v_mfma_f32_16x16x32_bf16 v[136:139], v[112:115], v[32:35], v[136:139]\n"
      "v_mfma_f32_16x16x32_bf16 v[236:239], v[164:167], v[152:155], 0\n"
      "v_mfma_f32_16x16x32_bf16 v[236:239], v[168:171], v[32:35], v[236:239]\n"
      "v_mfma_f32_16x16x32_bf16 v[160:163], v[40:43], v[152:155], 0\n"
      "v_mfma_f32_16x16x32_bf16 v[160:163], v[44:47], v[32:35], v[160:163]\n"
      "v_max_f32 v208, v148, v149\n"
      "v_max3_f32 v208, v208, v150, v151\n"
      "v_mfma_f32_16x16x32_bf16 v[28:31], v[92:95], v[152:155], 0\n"
      "v_mfma_f32_16x16x32_bf16 v[28:31], v[96:99], v[32:35], v[28:31]\n"
      "v_max3_f32 v208, v208, v136, v137\n"
      "v_max3_f32 v208, v208, v138, v139\n"
      "v_max3_f32 v208, v208, v236, v237\n"
      "v_max3_f32 v208, v208, v238, v239\n"
      "v_max3_f32 v208, v208, v160, v161\n"
      "v_max3_f32 v208, v208, v162, v163\n"
      "v_mfma_f32_16x16x32_bf16 v[24:27], v[216:219], v[152:155], 0\n"
      "v_mfma_f32_16x16x32_bf16 v[24:27], v[220:223], v[32:35], v[24:27]\n"
      "v_max3_f32 v208, v208, v28, v29\n"
      "v_max3_f32 v208, v208, v30, v31\n"
      "v_mfma_f32_16x16x32_bf16 v[20:23], v[244:247], v[152:155], 0\n"
      "v_mfma_f32_16x16x32_bf16 v[20:23], v[248:251], v[32:35], v[20:23]\n"
      "ds_read_b64 v[152:153], v11 offset:4096\n"
      "s_nop 2\n"
      "v_max3_f32 v208, v208, v24, v25\n"
      "v_max3_f32 v208, v208, v26, v27\n"
      "s_nop 1\n"
      "v_max3_f32 v208, v208, v20, v21\n"
      "v_max3_f32 v208, v208, v22, v23\n"
      "v_mov_b32 v32, v208\n"
      "s_nop 1\n"
      "v_permlane16_swap_b32 v208, v32\n"
      "v_max_f32 v208, v208, v32\n"
      "v_mov_b32 v32, v208\n"
      "s_nop 1\n"
      "v_permlane32_swap_b32 v208, v32\n"
      "v_max_f32 v208, v208, v32\n"
      "v_sub_f32 v148, v148, v208\n"
      "v_sub_f32 v149, v149, v208\n"
      "v_sub_f32 v150, v150, v208\n"
      "v_sub_f32 v151, v151, v208\n"
      "v_exp_f32 v148, v148\n"
      "v_exp_f32 v149, v149\n"
      "v_exp_f32 v150, v150\n"
      "v_exp_f32 v151, v151\n"
      "v_cvt_pk_bf16_f32 v234, v148, v149\n"
      "v_cvt_pk_bf16_f32 v235, v150, v151\n"
      "v_sub_f32 v136, v136, v208\n"
      "v_sub_f32 v137, v137, v208\n"
      "v_sub_f32 v138, v138, v208\n"
      "v_sub_f32 v139, v139, v208\n"
      "v_sub_f32 v236, v236, v208\n"
      "v_sub_f32 v237, v237, v208\n"
      "v_sub_f32 v238, v238, v208\n"
      "v_sub_f32 v239, v239, v208\n"
      "v_sub_f32 v160, v160, v208\n"
      "v_sub_f32 v161, v161, v208\n"
      "v_sub_f32 v162, v162, v208\n"
      "v_sub_f32 v163, v163, v208\n"
      "v_sub_f32 v28, v28, v208\n"
      "v_sub_f32 v29, v29, v208\n"
      "v_sub_f32 v30, v30, v208\n"
      "v_sub_f32 v31, v31, v208\n"
      "v_add_f32 v32, v148, v149\n"
      "v_add_f32 v33, v150, v151\n"
      "v_exp_f32 v136, v136\n"
      "v_exp_f32 v137, v137\n"
      "v_exp_f32 v138, v138\n"
      "v_exp_f32 v139, v139\n"
      "v_cvt_pk_bf16_f32 v232, v136, v137\n"
      "v_cvt_pk_bf16_f32 v233, v138, v139\n"
      "v_exp_f32 v236, v236\n"
      "v_mfma_f32_16x16x4_4b_bf16 v[136:151], v[234:235], v[18:19], 0 cbsz:2 abid:0\n"
      "v_exp_f32 v237, v237\n"
      "v_exp_f32 v238, v238\n"
      "v_exp_f32 v239, v239\n"
      "v_cvt_pk_bf16_f32 v234, v236, v237\n"
      "v_cvt_pk_bf16_f32 v235, v238, v239\n"
      "v_exp_f32 v160, v160\n"
      "v_exp_f32 v161, v161\n"
      "v_exp_f32 v162, v162\n"
      "v_exp_f32 v163, v163\n"
      "v_cvt_pk_bf16_f32 v236, v160, v161\n"
      "v_cvt_pk_bf16_f32 v237, v162, v163\n"
      "v_exp_f32 v28, v28\n"
      "v_exp_f32 v29, v29\n"
      "v_exp_f32 v30, v30\n"
      "v_exp_f32 v31, v31\n"
      "v_cvt_pk_bf16_f32 v238, v28, v29\n"
      "v_cvt_pk_bf16_f32 v239, v30, v31\n"
      "v_sub_f32 v24, v24, v208\n"
      "v_sub_f32 v25, v25, v208\n"
      "v_sub_f32 v26, v26, v208\n"
      "v_sub_f32 v27, v27, v208\n"
      "v_sub_f32 v20, v20, v208\n"
      "v_sub_f32 v21, v21, v208\n"
      "v_sub_f32 v22, v22, v208\n"
      "v_sub_f32 v23, v23, v208\n"
      "v_exp_f32 v24, v24\n"
      "v_exp_f32 v25, v25\n"
      "v_exp_f32 v26, v26\n"
      "v_exp_f32 v27, v27\n"
      "v_cvt_pk_bf16_f32 v208, v24, v25\n"
      "v_cvt_pk_bf16_f32 v209, v26, v27\n"
      "v_exp_f32 v20, v20\n"
      "v_exp_f32 v21, v21\n"
      "v_exp_f32 v22, v22\n"
      "v_exp_f32 v23, v23\n"
      "v_cvt_pk_bf16_f32 v210, v20, v21\n"
      "v_cvt_pk_bf16_f32 v211, v22, v23\n"
      "v_smfmac_f32_16x16x64_bf16 v[32:35], v[12:15], v[232:239], v5\n"
      "s_add_u32 s56, s48, 4096\n"
      "buffer_store_dwordx2 v[224:225], v2, s[44:47], s56 offen offset:0 nt sc1\n"
      "v_mfma_f32_16x16x32_bf16 v[136:139], v[232:235], v[52:55], v[136:139]\n"
      "buffer_store_dwordx2 v[228:229], v2, s[44:47], s56 offen offset:256 nt sc1\n"
      "v_mfma_f32_16x16x32_bf16 v[136:139], v[236:239], v[56:59], v[136:139]\n"
      "buffer_store_dwordx2 v[226:227], v2, s[44:47], s56 offen offset:512 nt sc1\n"
      "v_mfma_f32_16x16x32_bf16 v[136:139], v[208:211], v[120:123], v[136:139]\n"
      "buffer_store_dwordx2 v[230:231], v2, s[44:47], s56 offen offset:768 nt sc1\n"
      "v_smfmac_f32_16x16x32_bf16 v[32:35], v[12:13], v[208:211], v5\n"
      "s_waitcnt lgkmcnt(0)\n"
      "v_lshlrev_b32 v228, 16, v152\n"
      "v_and_b32 v229, 0xffff0000, v152\n"
      "v_mfma_f32_16x16x32_bf16 v[140:143], v[232:235], v[128:131], v[140:143]\n"
      "v_mov_b64 v[224:225], 0\n"
      "v_lshlrev_b32 v230, 16, v153\n"
      "v_and_b32 v231, 0xffff0000, v153\n"
      "v_mfma_f32_16x16x32_bf16 v[140:143], v[236:239], v[132:135], v[140:143]\n"
      "v_mov_b64 v[20:21], 0\n"
      "v_add_f32 v32, v32, v33\n"
      "v_rcp_f32 v33, v32\n"
      "v_mfma_f32_16x16x32_bf16 v[140:143], v[208:211], v[68:71], v[140:143]\n"
      "ds_bpermute_b32 v34, v4, v33\n"
      "v_mov_b64 v[28:29], 0\n"
      "v_mfma_f32_16x16x32_bf16 v[144:147], v[232:235], v[100:103], v[144:147]\n"
      "v_mov_b64 v[160:161], 0\n"
      "v_mfma_f32_16x16x32_bf16 v[144:147], v[236:239], v[104:107], v[144:147]\n"
      "v_mov_b64 v[24:25], 0\n"
      "v_mfma_f32_16x16x32_bf16 v[144:147], v[208:211], v[36:39], v[144:147]\n"
      "v_mov_b64 v[32:33], 0\n"
      "v_mfma_f32_16x16x32_bf16 v[148:151], v[232:235], v[192:195], v[148:151]\n"
      "v_mov_b64 v[232:233], 0\n"
      "v_mfma_f32_16x16x32_bf16 v[148:151], v[236:239], v[196:199], v[148:151]\n"
      "v_mfma_f32_16x16x32_bf16 v[148:151], v[208:211], v[240:243], v[148:151]\n"
      "s_waitcnt lgkmcnt(0)\n"
      "v_mul_f32_dpp v136, v34, v136 row_newbcast:0 row_mask:0xf bank_mask:0xf\n"
      "v_mul_f32_dpp v137, v34, v137 row_newbcast:4 row_mask:0xf bank_mask:0xf\n"
      "v_mul_f32_dpp v138, v34, v138 row_newbcast:8 row_mask:0xf bank_mask:0xf\n"
      "v_mul_f32_dpp v139, v34, v139 row_newbcast:12 row_mask:0xf bank_mask:0xf\n"
      "v_mul_f32_dpp v140, v34, v140 row_newbcast:0 row_mask:0xf bank_mask:0xf\n"
      "v_mul_f32_dpp v141, v34, v141 row_newbcast:4 row_mask:0xf bank_mask:0xf\n"
      "v_mfma_f32_16x16x32_bf16 v[136:139], v[124:127], a[192:195], v[136:139]\n"
      "v_mul_f32_dpp v142, v34, v142 row_newbcast:8 row_mask:0xf bank_mask:0xf\n"
      "v_mul_f32_dpp v143, v34, v143 row_newbcast:12 row_mask:0xf bank_mask:0xf\n"
      "v_mul_f32_dpp v144, v34, v144 row_newbcast:0 row_mask:0xf bank_mask:0xf\n"
      "v_mfma_f32_16x16x32_bf16 v[136:139], v[48:51], a[196:199], v[136:139]\n"
      "v_mul_f32_dpp v145, v34, v145 row_newbcast:4 row_mask:0xf bank_mask:0xf\n"
      "v_mul_f32_dpp v146, v34, v146 row_newbcast:8 row_mask:0xf bank_mask:0xf\n"
      "v_mul_f32_dpp v147, v34, v147 row_newbcast:12 row_mask:0xf bank_mask:0xf\n"
      "v_mul_f32_dpp v148, v34, v148 row_newbcast:0 row_mask:0xf bank_mask:0xf\n"
      "v_mfma_f32_16x16x32_bf16 v[136:139], v[116:119], a[200:203], v[136:139]\n"
      "v_mul_f32_dpp v149, v34, v149 row_newbcast:4 row_mask:0xf bank_mask:0xf\n"
      "v_mul_f32_dpp v150, v34, v150 row_newbcast:8 row_mask:0xf bank_mask:0xf\n"
      "v_mul_f32_dpp v151, v34, v151 row_newbcast:12 row_mask:0xf bank_mask:0xf\n"
      "v_add_u32 v11, 0x200, v11\n"
      "v_mfma_f32_16x16x32_bf16 v[136:139], v[252:255], a[204:207], v[136:139]\n"
      "v_mfma_f32_16x16x32_bf16 v[140:143], v[124:127], a[208:211], v[140:143]\n"
      "v_mfma_f32_16x16x32_bf16 v[140:143], v[48:51], a[212:215], v[140:143]\n"
      "v_mfma_f32_16x16x32_bf16 v[140:143], v[116:119], a[216:219], v[140:143]\n"
      "v_mfma_f32_16x16x32_bf16 v[140:143], v[252:255], a[220:223], v[140:143]\n"
      "s_add_u32 s56, s48, 0\n"
      "v_mfma_f32_16x16x32_bf16 v[144:147], v[124:127], a[224:227], v[144:147]\n"
      "v_mfma_f32_16x16x32_bf16 v[144:147], v[48:51], a[228:231], v[144:147]\n"
      "v_mfma_f32_16x16x32_bf16 v[144:147], v[116:119], a[232:235], v[144:147]\n"
      "v_mfma_f32_16x16x32_bf16 v[144:147], v[252:255], a[236:239], v[144:147]\n"
      "v_mfma_f32_16x16x32_bf16 v[148:151], v[124:127], a[240:243], v[148:151]\n"
      "v_mfma_f32_16x16x32_bf16 v[148:151], v[48:51], a[244:247], v[148:151]\n"
      "v_mfma_f32_16x16x32_bf16 v[148:151], v[116:119], a[248:251], v[148:151]\n"
      "v_cvt_pk_bf16_f32 v136, v136, v140\n"
      "v_cvt_pk_bf16_f32 v140, v137, v141\n"
      "v_pk_max_i16 v136, v136, 0\n"
      "v_pk_max_i16 v140, v140, 0\n"
      "v_cvt_pk_bf16_f32 v138, v138, v142\n"
      "v_cvt_pk_bf16_f32 v142, v139, v143\n"
      "v_pk_max_i16 v138, v138, 0\n"
      "v_mfma_f32_16x16x32_bf16 v[148:151], v[252:255], a[252:255], v[148:151]\n"
      "v_pk_max_i16 v142, v142, 0\n"
      "v_smfmac_f32_16x16x64_bf16 v[224:227], v[156:159], v[108:115], v5\n"
      "v_smfmac_f32_16x16x64_bf16 v[20:23], v[156:159], v[164:171], v5\n"
      "v_smfmac_f32_16x16x64_bf16 v[28:31], v[156:159], v[40:47], v5\n"
      "v_smfmac_f32_16x16x64_bf16 v[160:163], v[156:159], v[92:99], v5\n"
      "v_smfmac_f32_16x16x64_bf16 v[24:27], v[156:159], v[216:223], v5\n"
      "v_smfmac_f32_16x16x64_bf16 v[32:35], v[156:159], v[244:251], v5\n"
      "v_smfmac_f32_16x16x64_bf16 v[232:235], v[156:159], v[172:179], v5\n"
      "v_cvt_pk_bf16_f32 v137, v144, v148\n"
      "v_cvt_pk_bf16_f32 v141, v145, v149\n"
      "v_add_f32 v224, v224, v225\n"
      "v_add_f32 v20, v20, v21\n"
      "v_add_f32 v28, v28, v29\n"
      "v_add_f32 v160, v160, v161\n"
      "v_add_f32 v24, v24, v25\n"
      "v_add_f32 v32, v32, v33\n"
      "v_add_f32 v232, v232, v233\n"
      "v_max3_f32 v148, v224, v20, v28\n"
      "v_max3_f32 v148, v148, v160, v24\n"
      "v_max3_f32 v148, v148, v32, v232\n"
      "v_mov_b32 v233, 0\n"
      "v_pk_max_i16 v137, v137, 0\n"
      "v_max_f32_dpp v148, v148, v148 row_ror:8 row_mask:0xf bank_mask:0xf\n"
      "v_pk_max_i16 v141, v141, 0\n"
      "v_cvt_pk_bf16_f32 v139, v146, v150\n"
      "v_max_f32_dpp v148, v148, v148 row_ror:4 row_mask:0xf bank_mask:0xf\n"
      "v_cvt_pk_bf16_f32 v143, v147, v151\n"
      "v_pk_max_i16 v139, v139, 0\n"
      "v_max_f32_dpp v148, v148, v148 row_ror:2 row_mask:0xf bank_mask:0xf\n"
      "v_pk_max_i16 v143, v143, 0\n"
      "buffer_store_dwordx2 v[136:137], v2, s[44:47], s56 offen offset:0 nt sc1\n"
      "v_max_f32_dpp v148, v148, v148 row_ror:1 row_mask:0xf bank_mask:0xf\n"
      "v_sub_f32 v232, v232, v148\n"
      "v_sub_f32 v224, v224, v148\n"
      "v_sub_f32 v20, v20, v148\n"
      "v_exp_f32_dpp v233, v232 quad_perm:[0,1,2,3] row_mask:0xf bank_mask:0x1\n"
      "v_sub_f32 v28, v28, v148\n"
      "v_sub_f32 v160, v160, v148\n"
      "v_mov_b32_dpp v232, v233 quad_perm:[1,1,3,3] row_mask:0xf bank_mask:0xf\n"
      "v_cvt_pk_bf16_f32 v232, v233, v232\n"
      "ds_bpermute_b32 v136, v7, v232 offset:248\n"
      "ds_bpermute_b32 v137, v7, v232 offset:8\n"
      "v_sub_f32 v24, v24, v148\n"
      "v_sub_f32 v32, v32, v148\n"
      "v_exp_f32 v224, v224\n"
      "v_exp_f32 v20, v20\n"
      "v_exp_f32 v28, v28\n"
      "v_exp_f32 v160, v160\n"
      "v_exp_f32 v24, v24\n"
      "v_exp_f32 v32, v32\n"
      "v_cndmask_b32_dpp v225, v28, v224, vcc quad_perm:[0,0,2,2] row_mask:0xf bank_mask:0xf\n"
      "v_cndmask_b32_dpp v21, v160, v20, vcc quad_perm:[0,0,2,2] row_mask:0xf bank_mask:0xf\n"
      "v_cndmask_b32_dpp v25, v32, v24, vcc quad_perm:[0,0,2,2] row_mask:0xf bank_mask:0xf\n"
      "s_not_b64 vcc, vcc\n"
      "v_cndmask_b32_dpp v29, v224, v28, vcc quad_perm:[1,1,3,3] row_mask:0xf bank_mask:0xf\n"
      "v_cndmask_b32_dpp v161, v20, v160, vcc quad_perm:[1,1,3,3] row_mask:0xf bank_mask:0xf\n"
      "v_cvt_pk_bf16_f32 v225, v225, v29\n"
      "v_cvt_pk_bf16_f32 v21, v21, v161\n"
      "ds_bpermute_b32 v148, v6, v225\n"
      "ds_bpermute_b32 v149, v6, v21\n"
      "ds_bpermute_b32 v150, v6, v225 offset:16\n"
      "ds_bpermute_b32 v151, v6, v21 offset:16\n"
      "v_cndmask_b32_dpp v33, v24, v32, vcc quad_perm:[1,1,3,3] row_mask:0xf bank_mask:0xf\n"
      "v_cvt_pk_bf16_f32 v25, v25, v33\n"
      "ds_bpermute_b32 v146, v7, v25\n"
      "ds_bpermute_b32 v147, v7, v25 offset:4\n"
      "buffer_store_dwordx2 v[140:141], v2, s[44:47], s56 offen offset:256 nt sc1\n"
      "buffer_store_dwordx2 v[138:139], v2, s[44:47], s56 offen offset:512 nt sc1\n"
      "s_waitcnt lgkmcnt(6)\n"
      "v_mfma_f32_16x16x4_4b_bf16 v[164:179], v[136:137], v[18:19], 0 cbsz:2 abid:0\n"
      "buffer_store_dwordx2 v[142:143], v2, s[44:47], s56 offen offset:768 nt sc1\n"
      "s_not_b64 vcc, vcc\n"
      "v_add_f32 v224, v224, v20\n"
      "v_add_f32 v28, v28, v160\n"
      "v_add_f32 v24, v24, v32\n"
      "v_add_f32 v224, v224, v28\n"
      "v_add_f32 v24, v24, v233\n"
      "v_add_f32 v142, v224, v24\n"
      "s_add_u32 s56, s48, 24576\n"
      "s_nop 0\n"
      "v_add_f32_dpp v142, v142, v142 row_ror:8 row_mask:0xf bank_mask:0xf\n"
      "s_nop 1\n"
      "v_add_f32_dpp v142, v142, v142 row_ror:4 row_mask:0xf bank_mask:0xf\n"
      "s_nop 1\n"
      "v_add_f32_dpp v142, v142, v142 row_ror:2 row_mask:0xf bank_mask:0xf\n"
      "s_waitcnt lgkmcnt(2)\n"
      "v_smfmac_f32_16x16x64_bf16 v[164:167], v[148:151], v[52:59], v5\n"
      "v_smfmac_f32_16x16x64_bf16 v[168:171], v[148:151], v[128:135], v5\n"
      "v_add_f32_dpp v142, v142, v142 row_ror:1 row_mask:0xf bank_mask:0xf\n"
      "v_smfmac_f32_16x16x64_bf16 v[172:175], v[148:151], v[100:107], v5\n"
      "v_smfmac_f32_16x16x64_bf16 v[176:179], v[148:151], v[192:199], v5\n"
      "v_rcp_f32 v143, v142\n"
      "s_waitcnt lgkmcnt(0)\n"
      "v_smfmac_f32_16x16x32_bf16 v[164:167], v[146:147], v[120:123], v5\n"
      "v_smfmac_f32_16x16x32_bf16 v[168:171], v[146:147], v[68:71], v5\n"
      "s_nop 0\n"
      "v_smfmac_f32_16x16x32_bf16 v[172:175], v[146:147], v[36:39], v5\n"
      "v_smfmac_f32_16x16x32_bf16 v[176:179], v[146:147], v[240:243], v5\n"
      "s_nop 3\n"
      "v_add_f32 v164, v164, v165\n"
      "v_fma_f32 v164, v164, v143, v228\n"
      "v_add_f32 v168, v168, v169\n"
      "v_fma_f32 v168, v168, v143, v229\n"
      "v_cvt_pk_bf16_f32 v164, v164, v168\n"
      "v_pk_max_i16 v164, v164, 0\n"
      "v_add_f32 v172, v172, v173\n"
      "v_fma_f32 v172, v172, v143, v230\n"
      "v_add_f32 v176, v176, v177\n"
      "v_fma_f32 v176, v176, v143, v231\n"
      "v_cvt_pk_bf16_f32 v165, v172, v176\n"
      "v_pk_max_i16 v165, v165, 0\n"
      "buffer_store_dwordx2 v[164:165], v3, s[44:47], s56 offen offset:0 nt sc1\n"
      "s_mov_b32 s48, s49\n"
      "s_add_u32 s49, s49, 102400\n"
      "s_sub_u32 s50, s50, 1\n"
      "s_cmp_eq_u32 s50, 1\n"
      "s_cselect_b32 s49, 0x80000000, s49\n"
      "s_cmp_lg_u32 s50, 0\n"
      "s_cbranch_scc1 L_safe_%=\n"
      "s_waitcnt vmcnt(0) lgkmcnt(0)\n"
      "L_done_%=:\n"
      "s_endpgm\n"
      ".p2align 12\n"
      :
      : [ka] "s"(__builtin_amdgcn_kernarg_segment_ptr()), [g] "s"(blockIdx.x), [wave] "s"(wave), [ldsb] "s"(ldsb)
      : "v0", "v1", "v2", "v3", "v4", "v5", "v6", "v7", "v8", "v9", "v10", "v11", "v12", "v13", "v14", "v15", "v16", "v17", "v18", "v19", "v20", "v21", "v22", "v23", "v24", "v25", "v26", "v27", "v28", "v29", "v30", "v31", "v32", "v33", "v34", "v35", "v36", "v37", "v38", "v39", "v40", "v41", "v42", "v43", "v44", "v45", "v46", "v47", "v48", "v49", "v50", "v51", "v52", "v53", "v54", "v55", "v56", "v57", "v58", "v59", "v60", "v61", "v62", "v63", "v64", "v65", "v66", "v67", "v68", "v69", "v70", "v71", "v72", "v73", "v74", "v75", "v76", "v77", "v78", "v79", "v80", "v81", "v82", "v83", "v84", "v85", "v86", "v87", "v88", "v89", "v90", "v91", "v92", "v93", "v94", "v95", "v96", "v97", "v98", "v99", "v100", "v101", "v102", "v103", "v104", "v105", "v106", "v107", "v108", "v109", "v110", "v111", "v112", "v113", "v114", "v115", "v116", "v117", "v118", "v119", "v120", "v121", "v122", "v123", "v124", "v125", "v126", "v127", "v128", "v129", "v130", "v131", "v132", "v133", "v134", "v135", "v136", "v137", "v138", "v139", "v140", "v141", "v142", "v143", "v144", "v145", "v146", "v147", "v148", "v149", "v150", "v151", "v152", "v153", "v154", "v155", "v156", "v157", "v158", "v159", "v160", "v161", "v162", "v163", "v164", "v165", "v166", "v167", "v168", "v169", "v170", "v171", "v172", "v173", "v174", "v175", "v176", "v177", "v178", "v179", "v180", "v181", "v182", "v183", "v184", "v185", "v186", "v187", "v188", "v189", "v190", "v191", "v192", "v193", "v194", "v195", "v196", "v197", "v198", "v199", "v200", "v201", "v202", "v203", "v204", "v205", "v206", "v207", "v208", "v209", "v210", "v211", "v212", "v213", "v214", "v215", "v216", "v217", "v218", "v219", "v220", "v221", "v222", "v223", "v224", "v225", "v226", "v227", "v228", "v229", "v230", "v231", "v232", "v233", "v234", "v235", "v236", "v237", "v238", "v239", "v240", "v241", "v242", "v243", "v244", "v245", "v246", "v247", "v248", "v249", "v250", "v251", "v252", "v253", "v254", "v255", "a0", "a1", "a2", "a3", "a4", "a5", "a6", "a7", "a8", "a9", "a10", "a11", "a12", "a13", "a14", "a15", "a16", "a17", "a18", "a19", "a20", "a21", "a22", "a23", "a24", "a25", "a26", "a27", "a28", "a29", "a30", "a31", "a32", "a33", "a34", "a35", "a36", "a37", "a38", "a39", "a40", "a41", "a42", "a43", "a44", "a45", "a46", "a47", "a48", "a49", "a50", "a51", "a52", "a53", "a54", "a55", "a56", "a57", "a58", "a59", "a60", "a61", "a62", "a63", "a64", "a65", "a66", "a67", "a68", "a69", "a70", "a71", "a72", "a73", "a74", "a75", "a76", "a77", "a78", "a79", "a80", "a81", "a82", "a83", "a84", "a85", "a86", "a87", "a88", "a89", "a90", "a91", "a92", "a93", "a94", "a95", "a96", "a97", "a98", "a99", "a100", "a101", "a102", "a103", "a104", "a105", "a106", "a107", "a108", "a109", "a110", "a111", "a112", "a113", "a114", "a115", "a116", "a117", "a118", "a119", "a120", "a121", "a122", "a123", "a124", "a125", "a126", "a127", "a128", "a129", "a130", "a131", "a132", "a133", "a134", "a135", "a136", "a137", "a138", "a139", "a140", "a141", "a142", "a143", "a144", "a145", "a146", "a147", "a148", "a149", "a150", "a151", "a152", "a153", "a154", "a155", "a156", "a157", "a158", "a159", "a160", "a161", "a162", "a163", "a164", "a165", "a166", "a167", "a168", "a169", "a170", "a171", "a172", "a173", "a174", "a175", "a176", "a177", "a178", "a179", "a180", "a181", "a182", "a183", "a184", "a185", "a186", "a187", "a188", "a189", "a190", "a191", "a192", "a193", "a194", "a195", "a196", "a197", "a198", "a199", "a200", "a201", "a202", "a203", "a204", "a205", "a206", "a207", "a208", "a209", "a210", "a211", "a212", "a213", "a214", "a215", "a216", "a217", "a218", "a219", "a220", "a221", "a222", "a223", "a224", "a225", "a226", "a227", "a228", "a229", "a230", "a231", "a232", "a233", "a234", "a235", "a236", "a237", "a238", "a239", "a240", "a241", "a242", "a243", "a244", "a245", "a246", "a247", "a248", "a249", "a250", "a251", "a252", "a253", "a254", "a255", "s4", "s5", "s6", "s7", "s8", "s9", "s10", "s11", "s12", "s13", "s14", "s15", "s16", "s17", "s18", "s19", "s20", "s21", "s22", "s23", "s24", "s25", "s26", "s27", "s28", "s29", "s30", "s31", "s32", "s33", "s38", "s39", "s40", "s41", "s42", "s43", "s44", "s45", "s46", "s47", "s48", "s49", "s50", "s51", "s52", "s53", "s54", "s55", "s56", "s57", "s73", "s74", "s75", "s76", "s77", "s78", "s79", "s90", "s91", "s92", "s93", "s94", "s95", "s100", "s101", "s34", "s35", "vcc", "scc", "m0", "memory");
}

extern "C" void kernel_launch(void* const* d_in, const int* in_sizes, int n_in,
                              void* d_out, int out_size, void* d_ws, size_t ws_size,
                              hipStream_t stream) {
  const uint16_t* e = (const uint16_t*)d_in[0];
  const uint16_t* wq = (const uint16_t*)d_in[1];
  const uint16_t* wk = (const uint16_t*)d_in[2];
  const uint16_t* wv = (const uint16_t*)d_in[3];
  const uint16_t* wres = (const uint16_t*)d_in[4];
  uint16_t* out = (uint16_t*)d_out;
  autoint_kernel<<<256, 256, 0, stream>>>(e, wq, wk, wv, wres, out, (uint8_t*)d_ws);
}
```

```cpp
#include <hip/hip_runtime.h>
#include <stdint.h>

__global__ __launch_bounds__(256, 1)
void autoint_kernel(const uint16_t* __restrict__ e, const uint16_t* __restrict__ wq,
                    const uint16_t* __restrict__ wk, const uint16_t* __restrict__ wv,
                    const uint16_t* __restrict__ wres, uint16_t* out, uint8_t* ws) {
  __shared__ __attribute__((aligned(16))) uint8_t lds[163840];
  const unsigned wave = __builtin_amdgcn_readfirstlane(threadIdx.x >> 6);
  const unsigned ldsb = __builtin_amdgcn_readfirstlane((unsigned)(size_t)lds);
  asm volatile(
      "s_load_dwordx8 s[4:11], %[ka], 0x0\n"
      "s_load_dwordx4 s[12:15], %[ka], 0x20\n"
      "s_load_dwordx2 s[16:17], %[ka], 0x30\n"
      "s_lshl_b32 s22, %[wave], 3\n"
      "s_add_u32 s22, s22, 8\n"
      "s_load_dwordx2 s[20:21], %[ka], s22\n"
      "v_mbcnt_lo_u32_b32 v240, -1, 0\n"
      "v_mbcnt_hi_u32_b32 v240, -1, v240\n"
      "s_getpc_b64 s[50:51]\n"
      "L_ai_pc:\n"
      "s_bfe_u32 s54, %[g], 0x20003\n"
      "s_lshl2_add_u32 s54, s54, %[wave]\n"
      "s_lshl_b32 s54, s54, 13\n"
      "v_lshlrev_b32 v250, 7, v240\n"
      "v_add_u32 v250, s54, v250\n"
      "v_min_u32 v250, L_ai_end-L_ai_pc, v250\n"
      "global_load_dword v17, v250, s[50:51]\n"
      "s_bfe_u32 s19, %[g], 0x10003\n"
      "s_and_b32 s18, %[g], 7\n"
      "s_lshr_b32 s54, %[g], 4\n"
      "s_lshl3_add_u32 s18, s54, s18\n"
      "s_lshl_b32 s18, s18, 2\n"
      "s_add_u32 s18, s18, %[wave]\n"
      "s_mul_i32 s18, s18, 25600\n"
      "s_mov_b32 s46, 32768\n"
      "s_mov_b32 s47, 0x20000\n"
      "v_lshrrev_b32 v234, 3, v240\n"
      "v_and_b32 v235, 7, v240\n"
      "v_lshlrev_b32 v233, 4, v235\n"
      "v_lshl_add_u32 v246, v234, 9, v233\n"
      "v_lshl_add_u32 v236, v234, 10, v233\n"
      "s_lshl_b32 s52, %[wave], 12\n"
      "s_lshl_b32 s55, s19, 14\n"
      "s_add_u32 s52, s52, s55\n"
      "s_lshl_b32 s57, %[wave], 13\n"
      "s_lshl_b32 s55, s19, 7\n"
      "s_add_u32 s57, s57, s55\n"
      "v_lshlrev_b32 v230, 7, v235\n"
      "s_lshr_b32 s23, %[g], 3\n"
      "s_lshl_b32 s23, s23, 10\n"
      "s_sub_u32 s49, s18, 13108224\n"
      "s_ashr_i32 s50, s49, 31\n"
      "s_mov_b32 s42, 104884224\n"
      "s_mov_b32 s43, 0x20000\n"
      "v_and_b32 v241, 15, v240\n"
      "v_lshrrev_b32 v242, 4, v240\n"
      "v_lshrrev_b32 v243, 2, v241\n"
      "v_and_b32 v244, 3, v240\n"
      "s_mul_i32 s53, %[wave], 24576\n"
      "s_add_u32 s53, s53, %[ldsb]\n"
      "s_add_u32 s53, s53, 65536\n"
      "v_and_b32 v248, 2, v234\n"
      "v_xor_b32 v248, v248, v244\n"
      "v_lshlrev_b32 v248, 4, v248\n"
      "v_lshrrev_b32 v249, 5, v240\n"
      "v_lshl_add_u32 v249, v249, 6, v248\n"
      "v_bfe_u32 v248, v240, 2, 3\n"
      "v_lshl_add_u32 v1, v248, 8, v249\n"
      "v_and_b32 v248, 1, v242\n"
      "v_mul_u32_u24 v247, 13106176, v248\n"
      "v_add_u32 v247, v247, v1\n"
      "s_mov_b32 s73, 13128704\n"
      "s_mov_b32 s74, 13127808\n"
      "s_mov_b32 s75, 13124608\n"
      "s_mov_b32 s76, 13123712\n"
      "s_mov_b32 s77, 13120512\n"
      "s_mov_b32 s78, 13119616\n"
      "s_mov_b32 s79, 13132800\n"
      "s_mov_b32 s90, 13116416\n"
      "s_mov_b32 s91, 13115520\n"
      "s_mov_b32 s92, 13112320\n"
      "s_mov_b32 s93, 13111424\n"
      "s_mov_b32 s94, 13108224\n"
      "s_mov_b32 s95, 13107328\n"
      "s_mov_b32 s100, 13312\n"
      "s_mov_b32 s101, 9216\n"
      "s_mov_b32 s34, 5120\n"
      "s_mov_b32 s35, 1024\n"
      "s_mov_b32 s51, 0x3e38aa3b\n"
      "s_lshl_b32 s54, %[wave], 10\n"
      "s_add_u32 s54, s54, %[ldsb]\n"
      "v_lshlrev_b32 v255, 8, v242\n"
      "s_waitcnt lgkmcnt(0)\n"
      "s_mov_b32 s44, s20\n"
      "s_and_b32 s45, s21, 0xffff\n"
      "buffer_load_dword v17, v230, s[44:47], s23 offen\n"
      "s_mov_b32 s44, s8\n s_and_b32 s45, s9, 0xffff\n"
      "buffer_load_dwordx4 v[64:67], v246, s[44:47], s52 offen offset:0\n"
      "s_lshr_b32 s33, %[g], 3\n"
      "s_lshl2_add_u32 s33, s33, %[wave]\n"
      "s_bitcmp1_b32 s33, 6\n"
      "s_cselect_b64 s[24:25], s[14:15], s[4:5]\n"
      "s_and_b32 s33, s33, 63\n"
      "s_lshl_b32 s33, s33, 21\n"
      "s_min_u32 s33, s33, 104857596\n"
      "s_load_dword s32, s[24:25], s33\n"
      "s_add_u32 s40, s4, s49\n"
      "s_addc_u32 s41, s5, s50\n"
      "s_and_b32 s41, s41, 0xffff\n"
      "buffer_load_dwordx4 v[68:71], v246, s[44:47], s52 offen offset:128\n"
      "buffer_load_dwordx4 v[72:75], v246, s[44:47], s52 offen offset:256\n"
      "buffer_load_dwordx4 v[76:79], v246, s[44:47], s52 offen offset:384\n"
      "s_mov_b32 s44, s10\n s_and_b32 s45, s11, 0xffff\n"
      "buffer_load_dwordx4 v[80:83], v246, s[44:47], s52 offen offset:0\n"
      "buffer_load_dwordx4 v[84:87], v246, s[44:47], s52 offen offset:128\n"
      "buffer_load_dwordx4 v[88:91], v246, s[44:47], s52 offen offset:256\n"
      "buffer_load_dwordx4 v[92:95], v246, s[44:47], s52 offen offset:384\n"
      "s_add_u32 m0, s53, 0\n s_mov_b32 s30, 13132800\n"
      "s_nop 0\n"
      "buffer_load_dwordx4 v247, s[40:43], s30 offen offset:0 lds\n"
      "v_lshl_add_u32 v255, v235, 5, v255\n"
      "v_lshrrev_b32 v248, 2, v235\n"
      "v_lshl_add_u32 v255, v248, 4, v255\n"
      "v_and_b32 v248, 1, v234\n"
      "v_lshl_add_u32 v255, v248, 3, v255\n"
      "v_add_u32 v255, s54, v255\n"
      "v_xor_b32 v232, 16, v255\n"
      "v_mov_b32 v238, 0x05040100\n"
      "v_mov_b32 v239, 0x07060302\n"
      "v_xor_b32 v254, v248, v240\n"
      "s_mov_b32 s30, 39345152\n"
      "s_nop 0\n"
      "buffer_load_dwordx4 v247, s[40:43], s30 offen offset:2048 lds\n"
      "v_lshlrev_b32 v254, 4, v254\n"
      "v_add_u32 v254, %[ldsb], v254\n"
      "v_lshlrev_b32 v245, 3, v241\n"
      "v_lshrrev_b32 v248, 3, v241\n"
      "v_lshlrev_b32 v248, 11, v248\n"
      "v_lshl_add_u32 v248, v235, 6, v248\n"
      "v_lshrrev_b32 v249, 1, v241\n"
      "v_and_b32 v249, 2, v249\n"
      "v_xor_b32 v249, v249, v242\n"
      "v_lshl_add_u32 v248, v249, 4, v248\n"
      "s_mov_b32 s30, 13131904\n"
      "s_nop 0\n"
      "buffer_load_dwordx4 v247, s[40:43], s30 offen offset:1024 lds\n"
      "v_add_u32 v0, s53, v248\n"
      "s_lshl_b32 s54, s19, 7\n"
      "v_add_u32 v248, s54, v245\n"
      "v_lshl_add_u32 v2, v242, 10, v248\n"
      "v_lshl_add_u32 v3, v242, 8, v248\n"
      "v_and_b32 v4, 60, v240\n"
      "v_and_b32 v248, 1, v240\n"
      "v_mul_u32_u24 v5, 0xaaaa, v248\n"
      "v_add_u32 v5, 0x4444, v5\n"
      "v_lshlrev_b32 v7, 6, v243\n"
      "s_mov_b32 s30, 39344256\n"
      "s_nop 0\n"
      "buffer_load_dwordx4 v247, s[40:43], s30 offen offset:3072 lds\n"
      "v_lshl_add_u32 v7, v248, 3, v7\n"
      "v_and_b32 v249, 1, v242\n"
      "v_lshl_add_u32 v6, v249, 5, v7\n"
      "v_lshrrev_b32 v250, 1, v242\n"
      "v_lshl_add_u32 v6, v250, 2, v6\n"
      "v_lshl_add_u32 v7, v242, 4, v7\n"
      "v_mov_b32 v8, 1.0\n"
      "s_lshl_b32 s54, %[wave], 14\n"
      "s_add_u32 s54, s54, %[ldsb]\n"
      "v_lshlrev_b32 v16, 10, v242\n"
      "v_lshrrev_b32 v231, 1, v241\n"
      "v_lshl_add_u32 v16, v231, 4, v16\n"
      "v_lshl_add_u32 v16, v248, 2, v16\n"
      "v_add_u32 v16, s54, v16\n"
      "v_lshrrev_b32 v231, 3, v241\n"
      "v_lshl_add_u32 v16, v231, 20, v16\n"
      "v_lshlrev_b32 v9, 9, v242\n"
      "v_add_u32 v9, s54, v9\n"
      "v_lshl_add_u32 v251, v241, 4, v9\n"
      "v_lshl_add_u32 v9, v244, 4, v9\n"
      "s_add_u32 s54, s54, 4096\n"
      "v_lshlrev_b32 v10, 10, v242\n"
      "v_lshl_add_u32 v10, v243, 4, v10\n"
      "v_lshl_add_u32 v10, v248, 9, v10\n"
      "v_add_u32 v10, s54, v10\n"
      "v_lshlrev_b32 v252, 10, v250\n"
      "v_lshl_add_u32 v252, v241, 4, v252\n"
      "v_lshl_add_u32 v252, v249, 3, v252\n"
      "v_add_u32 v252, s54, v252\n"
      "s_add_u32 s54, s54, 4096\n"
      "v_lshlrev_b32 v11, 3, v240\n"
      "v_add_u32 v11, s54, v11\n"
      "v_lshl_add_u32 v253, v242, 9, v245\n"
      "v_add_u32 v253, s54, v253\n"
      "v_mov_b32 v12, 0x3f803f80\n"
      "v_mov_b32 v13, 0x3f803f80\n"
      "v_mov_b32 v14, 0x3f803f80\n"
      "v_mov_b32 v15, 0x3f803f80\n"
      "s_mov_b32 vcc_lo, 0x55555555\n"
      "s_mov_b32 vcc_hi, 0x55555555\n"
      "s_mov_b32 s26, 16384\n"
      "s_mov_b32 s27, 0x20000\n"
      "s_branch L_hs_%=\n"
      "L_hr_%=:\n"
      "s_setpc_b64 vcc\n"
      "L_e5_%=:\n"
      "s_getpc_b64 s[56:57]\n"
      "L_te_%=:\n"
      "s_lshl_b32 s55, %[wave], 12\n"
      "s_add_u32 s55, s55, L_ln_%=-L_te_%=\n"
      "v_mbcnt_lo_u32_b32 v17, -1, 0\n"
      "v_mbcnt_hi_u32_b32 v17, -1, v17\n"
      "v_lshlrev_b32 v17, 6, v17\n"
      "v_add_u32 v17, s55, v17\n"
      "v_min_u32 v17, L_ai_end-L_te_%=, v17\n"
      "global_load_dword v17, v17, s[56:57]\n"
      "s_branch L_task_%=\n"
      "L_hs_%=:\n"
      "s_mov_b32 s38, 0xc000000\n"
      "s_mov_b64 exec, 0\n"
      "s_cmp_eq_u32 %[wave], 0\n"
      "s_cbranch_scc0 L_hn1o0_%=\n"
      "s_call_b64 vcc, L_h0_%=\n"
      "L_hn1o0_%=:\n"
      "s_cmp_eq_u32 %[wave], 1\n"
      "s_cbranch_scc0 L_hn1o1_%=\n"
      "s_call_b64 vcc, L_h1_%=\n"
      "L_hn1o1_%=:\n"
      "s_cmp_eq_u32 %[wave], 2\n"
      "s_cbranch_scc0 L_hn1o2_%=\n"
      "s_call_b64 vcc, L_h2_%=\n"
      "L_hn1o2_%=:\n"
      "s_cmp_eq_u32 %[wave], 3\n"
      "s_cbranch_scc0 L_hn1o3_%=\n"
      "s_nop 0\n"
      "L_hn1o3_%=:\n"
      "s_mov_b64 exec, -1\n"
      "s_mov_b32 vcc_lo, 0x55555555\n"
      "s_mov_b32 vcc_hi, 0x55555555\n"
      "s_add_u32 m0, s53, 20480\n"
      "s_nop 0\n"
      "buffer_load_dwordx4 v1, s[40:43], s73 offen offset:0 lds\n"
      "s_mov_b32 s44, s12\n s_and_b32 s45, s13, 0xffff\n"
      "buffer_load_dwordx4 v1, s[40:43], s73 offen offset:2048 lds\n"
      "s_waitcnt vmcnt(12)\n"
      "v_perm_b32 v116, v68, v64, v238\n"
      "v_perm_b32 v222, v69, v65, v239\n"
      "v_perm_b32 v60, v69, v65, v238\n"
      "s_waitcnt vmcnt(10)\n"
      "v_perm_b32 v193, v79, v75, v238\n"
      "v_perm_b32 v225, v76, v72, v239\n"
      "buffer_load_dwordx4 v1, s[40:43], s74 offen offset:1024 lds\n"
      "buffer_load_dwordx4 v1, s[40:43], s74 offen offset:3072 lds\n"
      "s_mov_b64 exec, 0\n"
      "s_cmp_eq_u32 %[wave], 0\n"
      "s_cbranch_scc0 L_hn2o0_%=\n"
      "s_call_b64 vcc, L_h3_%=\n"
      "s_call_b64 vcc, L_h7_%=\n"
      "s_nop 0\n"
      "L_hn2o0_%=:\n"
      "s_cmp_eq_u32 %[wave], 1\n"
      "s_cbranch_scc0 L_hn2o1_%=\n"
      "s_call_b64 vcc, L_h4_%=\n"
      "s_nop 0\n"
      "s_nop 0\n"
      "L_hn2o1_%=:\n"
      "s_cmp_eq_u32 %[wave], 2\n"
      "s_cbranch_scc0 L_hn2o2_%=\n"
      "s_call_b64 vcc, L_h5_%=\n"
      "s_nop 0\n"
      "s_nop 0\n"
      "L_hn2o2_%=:\n"
      "s_cmp_eq_u32 %[wave], 3\n"
      "s_cbranch_scc0 L_hn2o3_%=\n"
      "s_call_b64 vcc, L_h6_%=\n"
      "s_nop 0\n"
      "s_nop 0\n"
      "L_hn2o3_%=:\n"
      "s_mov_b64 exec, -1\n"
      "s_mov_b32 vcc_lo, 0x55555555\n"
      "s_mov_b32 vcc_hi, 0x55555555\n"
      "v_perm_b32 v50, v70, v66, v239\n"
      "s_waitcnt vmcnt(10)\n"
      "v_perm_b32 v194, v84, v80, v238\n"
      "s_waitcnt vmcnt(8)\n"
      "v_perm_b32 v63, v93, v89, v239\n"
      "v_perm_b32 v195, v92, v88, v238\n"
      "v_perm_b32 v51, v78, v74, v239\n"
      "v_perm_b32 v221, v94, v90, v239\n"
      "v_perm_b32 v226, v85, v81, v238\n"
      "v_perm_b32 v98, v84, v80, v239\n"
      "v_perm_b32 v192, v71, v67, v238\n"
      "v_perm_b32 v61, v77, v73, v238\n"
      "buffer_load_dwordx4 v[228:231], v236, s[44:47], s57 offen offset:0\n"
      "v_perm_b32 v119, v94, v90, v238\n"
      "ds_write_b64 v232, v[50:51] offset:4096\n"
      "v_perm_b32 v118, v86, v82, v238\n"
      "v_perm_b32 v117, v76, v72, v238\n"
      "v_perm_b32 v101, v95, v91, v238\n"
      "ds_write_b64 v255, v[116:117] offset:0\n"
      "buffer_load_dwordx4 v[50:53], v236, s[44:47], s57 offen offset:256\n"
      "v_perm_b32 v227, v93, v89, v238\n"
      "v_perm_b32 v117, v95, v91, v239\n"
      "v_perm_b32 v62, v85, v81, v239\n"
      "v_perm_b32 v100, v87, v83, v238\n"
      "v_perm_b32 v220, v86, v82, v239\n"
      "v_perm_b32 v93, v79, v75, v239\n"
      "ds_write_b64 v232, v[100:101] offset:24576\n"
      "v_perm_b32 v116, v87, v83, v239\n"
      "ds_write_b64 v232, v[118:119] offset:16384\n"
      "v_perm_b32 v75, v78, v74, v238\n"
      "v_perm_b32 v223, v77, v73, v239\n"
      "ds_write_b64 v255, v[222:223] offset:12288\n"
      "ds_write_b64 v255, v[60:61] offset:8192\n"
      "v_perm_b32 v99, v92, v88, v239\n"
      "ds_write_b64 v255, v[98:99] offset:20480\n"
      "ds_write_b64 v232, v[220:221] offset:20480\n"
      "v_perm_b32 v74, v70, v66, v238\n"
      "ds_write_b64 v232, v[74:75] offset:0\n"
      "ds_write_b64 v255, v[226:227] offset:24576\n"
      "ds_write_b64 v255, v[194:195] offset:16384\n"
      "ds_write_b64 v255, v[62:63] offset:28672\n"
      "ds_write_b64 v232, v[116:117] offset:28672\n"
      "v_perm_b32 v224, v68, v64, v239\n"
      "ds_write_b64 v232, v[192:193] offset:8192\n"
      "ds_write_b64 v255, v[224:225] offset:4096\n"
      "v_perm_b32 v92, v71, v67, v239\n"
      "ds_write_b64 v232, v[92:93] offset:12288\n"
      "s_waitcnt lgkmcnt(0)\n"
      "s_barrier\n"
      "buffer_load_dwordx4 v[68:71], v236, s[44:47], s57 offen offset:512\n"
      "buffer_load_dwordx4 v[20:23], v236, s[44:47], s57 offen offset:768\n"
      "ds_read_b128 a[56:59], v254 offset:14336\n"
      "ds_read_b128 a[88:91], v254 offset:22528\n"
      "ds_read_b128 a[104:107], v254 offset:26624\n"
      "ds_read_b128 a[124:127], v254 offset:31744\n"
      "ds_read_b128 a[84:87], v254 offset:21504\n"
      "ds_read_b128 a[0:3], v254 offset:0\n"
      "ds_read_b128 a[48:51], v254 offset:12288\n"
      "ds_read_b128 a[108:111], v254 offset:27648\n"
      "ds_read_b128 a[76:79], v254 offset:19456\n"
      "s_mov_b32 s44, s6\n s_and_b32 s45, s7, 0xffff\n"
      "ds_read_b128 a[60:63], v254 offset:15360\n"
      "ds_read_b128 a[28:31], v254 offset:7168\n"
      "s_waitcnt vmcnt(8)\n"
      "ds_read_b128 v[72:75], v0 offset:0\n"
      "ds_read_b128 a[24:27], v254 offset:6144\n"
      "ds_read_b128 a[8:11], v254 offset:2048\n"
      "ds_read_b128 a[52:55], v254 offset:13312\n"
      "ds_read_b128 a[96:99], v254 offset:24576\n"
      "buffer_load_dwordx4 v[60:63], v246, s[44:47], s52 offen offset:0\n"
      "ds_read_b128 a[116:119], v254 offset:29696\n"
      "buffer_load_dwordx4 v[76:79], v246, s[44:47], s52 offen offset:128\n"
      "ds_read_b128 v[224:227], v0 offset:512\n"
      "ds_read_b128 a[12:15], v254 offset:3072\n"
      "ds_read_b128 a[4:7], v254 offset:1024\n"
      "ds_read_b128 a[72:75], v254 offset:18432\n"
      "ds_read_b128 a[68:71], v254 offset:17408\n"
      "ds_read_b128 v[220:223], v0 offset:1536\n"
      "ds_read_b128 a[40:43], v254 offset:10240\n"
      "s_mov_b32 s30, 65561600\n"
      "ds_read_b128 a[36:39], v254 offset:9216\n"
      "ds_read_b128 v[92:95], v0 offset:1024\n"
      "buffer_load_dwordx4 v[88:91], v246, s[44:47], s52 offen offset:256\n"
      "ds_read_b128 a[64:67], v254 offset:16384\n"
      "buffer_load_dwordx4 v[84:87], v246, s[44:47], s52 offen offset:384\n"
      "ds_read_b128 a[112:115], v254 offset:28672\n"
      "ds_read_b128 a[120:123], v254 offset:30720\n"
      "ds_read_b128 a[100:103], v254 offset:25600\n"
      "ds_read_b128 a[92:95], v254 offset:23552\n"
      "ds_read_b128 a[44:47], v254 offset:11264\n"
      "ds_read_b128 a[20:23], v254 offset:5120\n"
      "s_add_u32 m0, s53, 4096\n"
      "ds_read_b128 a[80:83], v254 offset:20480\n"
      "s_add_u32 s44, s14, s49\n s_addc_u32 s45, s15, s50\n s_and_b32 s45, s45, 0xffff\n s_mov_b32 s46, 13133824\n"
      "ds_read_b128 a[16:19], v254 offset:4096\n"
      "buffer_load_dwordx4 v247, s[40:43], s30 offen offset:0 lds\n"
      "s_waitcnt lgkmcnt(15)\n"
      "v_mfma_f32_16x16x32_bf16 v[116:119], v[72:75], a[96:99], 0\n"
      "s_waitcnt lgkmcnt(5)\n"
      "v_mfma_f32_16x16x32_bf16 v[116:119], v[224:227], a[100:103], v[116:119]\n"
      "v_mfma_f32_16x16x32_bf16 v[116:119], v[92:95], a[104:107], v[116:119]\n"
      "v_mfma_f32_16x16x32_bf16 v[116:119], v[220:223], a[108:111], v[116:119]\n"
      "ds_read_b128 a[32:35], v254 offset:8192\n"
      "v_mfma_f32_16x16x32_bf16 v[64:67], a[0:3], v[72:75], 0\n"
      "v_mfma_f32_16x16x32_bf16 v[64:67], a[4:7], v[224:227], v[64:67]\n"
      "v_mfma_f32_16x16x32_bf16 v[64:67], a[8:11], v[92:95], v[64:67]\n"
      "v_mfma_f32_16x16x32_bf16 v[64:67], a[12:15], v[220:223], v[64:67]\n"
      "s_mov_b32 s30, 91773952\n"
      "buffer_load_dwordx4 v247, s[40:43], s30 offen offset:2048 lds\n"
      "v_mfma_f32_16x16x32_bf16 v[98:101], a[48:51], v[72:75], 0\n"
      "v_mfma_f32_16x16x32_bf16 v[98:101], a[52:55], v[224:227], v[98:101]\n"
      "v_mfma_f32_16x16x32_bf16 v[98:101], a[56:59], v[92:95], v[98:101]\n"
      "v_mfma_f32_16x16x32_bf16 v[98:101], a[60:63], v[220:223], v[98:101]\n"
      "s_waitcnt lgkmcnt(2)\n"
      "v_mfma_f32_16x16x32_bf16 v[136:139], v[72:75], a[80:83], 0\n"
      "v_mfma_f32_16x16x32_bf16 v[136:139], v[224:227], a[84:87], v[136:139]\n"
      "v_mfma_f32_16x16x32_bf16 v[136:139], v[92:95], a[88:91], v[136:139]\n"
      "v_mfma_f32_16x16x32_bf16 v[136:139], v[220:223], a[92:95], v[136:139]\n"
      "v_mfma_f32_16x16x32_bf16 v[204:207], v[72:75], a[112:115], 0\n"
      "s_mov_b32 s30, 65560704\n"
      "v_mfma_f32_16x16x32_bf16 v[204:207], v[224:227], a[116:119], v[204:207]\n"
      "v_mfma_f32_16x16x32_bf16 v[204:207], v[92:95], a[120:123], v[204:207]\n"
      "v_mfma_f32_16x16x32_bf16 v[204:207], v[220:223], a[124:127], v[204:207]\n"
      "buffer_load_dwordx4 v247, s[40:43], s30 offen offset:1024 lds\n"
      "s_waitcnt lgkmcnt(1)\n"
      "v_mfma_f32_16x16x32_bf16 v[104:107], a[16:19], v[72:75], 0\n"
      "v_cvt_pk_bf16_f32 v234, v98, v99\n"
      "v_mfma_f32_16x16x32_bf16 v[104:107], a[20:23], v[224:227], v[104:107]\n"
      "v_mfma_f32_16x16x32_bf16 v[104:107], a[24:27], v[92:95], v[104:107]\n"
      "v_mfma_f32_16x16x32_bf16 v[104:107], a[28:31], v[220:223], v[104:107]\n"
      "s_nop 0\n"
      "v_cvt_pk_bf16_f32 v210, v204, v205\n"
      "s_nop 5\n"
      "v_cvt_pk_bf16_f32 v54, v104, v105\n"
      "s_waitcnt lgkmcnt(0)\n"
      "v_mfma_f32_16x16x32_bf16 v[96:99], a[32:35], v[72:75], 0\n"
      "v_mfma_f32_16x16x32_bf16 v[96:99], a[36:39], v[224:227], v[96:99]\n"
      "v_mfma_f32_16x16x32_bf16 v[96:99], a[40:43], v[92:95], v[96:99]\n"
      "v_mfma_f32_16x16x32_bf16 v[96:99], a[44:47], v[220:223], v[96:99]\n"
      "s_mov_b32 s30, 91773056\n"
      "buffer_load_dwordx4 v247, s[40:43], s30 offen offset:3072 lds\n"
      "s_waitcnt vmcnt(12)\n"
      "ds_read_b128 v[212:215], v0 offset:20480\n"
      "ds_read_b128 v[124:127], v0 offset:20992\n"
      "v_mfma_f32_16x16x32_bf16 v[28:31], v[72:75], a[64:67], 0\n"
      "ds_read_b128 v[188:191], v0 offset:21504\n"
      "ds_read_b128 v[244:247], v0 offset:22016\n"
      "v_mfma_f32_16x16x32_bf16 v[28:31], v[224:227], a[68:71], v[28:31]\n"
      "v_mfma_f32_16x16x32_bf16 v[28:31], v[92:95], a[72:75], v[28:31]\n"
      "v_mfma_f32_16x16x32_bf16 v[28:31], v[220:223], a[76:79], v[28:31]\n"
      "v_cvt_pk_bf16_f32 v233, v98, v99\n"
      "s_add_u32 m0, s53, 16384\n"
      "s_nop 0\n"
      "buffer_load_dwordx4 v1, s[40:43], s75 offen offset:0 lds\n"
      "buffer_load_dwordx4 v1, s[40:43], s75 offen offset:2048 lds\n"
      "s_waitcnt vmcnt(12)\n"
      "v_perm_b32 v44, v50, v228, v238\n"
      "v_perm_b32 v198, v53, v231, v239\n"
      "v_perm_b32 v104, v52, v230, v238\n"
      "v_perm_b32 v98, v50, v228, v239\n"
      "s_waitcnt lgkmcnt(3)\n"
      "v_mfma_f32_16x16x32_bf16 v[148:151], v[212:215], a[112:115], 0\n"
      "s_waitcnt lgkmcnt(2)\n"
      "v_mfma_f32_16x16x32_bf16 v[148:151], v[124:127], a[116:119], v[148:151]\n"
      "v_perm_b32 v204, v51, v229, v239\n"
      "s_waitcnt lgkmcnt(1)\n"
      "v_mfma_f32_16x16x32_bf16 v[148:151], v[188:191], a[120:123], v[148:151]\n"
      "s_waitcnt lgkmcnt(0)\n"
      "v_mfma_f32_16x16x32_bf16 v[148:151], v[244:247], a[124:127], v[148:151]\n"
      "v_perm_b32 v194, v51, v229, v238\n"
      "v_perm_b32 v50, v52, v230, v239\n"
      "v_mfma_f32_16x16x32_bf16 v[184:187], v[212:215], a[64:67], 0\n"
      "buffer_load_dwordx4 v1, s[40:43], s76 offen offset:1024 lds\n"
      "v_mfma_f32_16x16x32_bf16 v[184:187], v[124:127], a[68:71], v[184:187]\n"
      "v_mfma_f32_16x16x32_bf16 v[184:187], v[188:191], a[72:75], v[184:187]\n"
      "s_waitcnt vmcnt(11)\n"
      "v_perm_b32 v51, v22, v70, v239\n"
      "v_perm_b32 v99, v20, v68, v239\n"
      "v_mfma_f32_16x16x32_bf16 v[184:187], v[244:247], a[76:79], v[184:187]\n"
      "ds_write_b64 v232, v[50:51] offset:53248\n"
      "buffer_load_dwordx4 v1, s[40:43], s76 offen offset:3072 lds\n"
      "ds_write_b64 v255, v[98:99] offset:53248\n"
      "v_perm_b32 v195, v21, v69, v238\n"
      "s_waitcnt vmcnt(11)\n"
      "v_lshlrev_b32 v228, 16, v60\n"
      "v_lshlrev_b32 v166, 16, v61\n"
      "v_lshlrev_b32 v98, 16, v62\n"
      "v_lshlrev_b32 v236, 16, v63\n"
      "v_mul_f32 v166, s51, v166\n"
      "v_mul_f32 v98, s51, v98\n"
      "v_mfma_f32_16x16x32_bf16 v[48:51], a[16:19], v[212:215], 0\n"
      "v_perm_b32 v105, v22, v70, v238\n"
      "v_and_b32 v82, 0xffff0000, v63\n"
      "v_mfma_f32_16x16x32_bf16 v[48:51], a[20:23], v[124:127], v[48:51]\n"
      "v_perm_b32 v45, v20, v68, v238\n"
      "v_perm_b32 v199, v23, v71, v239\n"
      "v_mfma_f32_16x16x32_bf16 v[48:51], a[24:27], v[188:191], v[48:51]\n"
      "v_and_b32 v230, 0xffff0000, v60\n"
      "v_mul_f32 v230, s51, v230\n"
      "v_mfma_f32_16x16x32_bf16 v[48:51], a[28:31], v[244:247], v[48:51]\n"
      "ds_write_b64 v255, v[44:45] offset:49152\n"
      "ds_write_b64 v255, v[194:195] offset:57344\n"
      "ds_write_b64 v232, v[104:105] offset:49152\n"
      "v_mfma_f32_16x16x32_bf16 v[44:47], a[0:3], v[212:215], 0\n"
      "s_waitcnt vmcnt(9)\n"
      "v_lshlrev_b32 v20, 16, v89\n"
      "v_lshlrev_b32 v104, 16, v91\n"
      "v_mfma_f32_16x16x32_bf16 v[44:47], a[4:7], v[124:127], v[44:47]\n"
      "ds_write_b64 v232, v[198:199] offset:61440\n"
      "v_lshlrev_b32 v99, 16, v78\n"
      "v_lshlrev_b32 v167, 16, v77\n"
      "v_and_b32 v198, 0xffff0000, v90\n"
      "v_and_b32 v164, 0xffff0000, v89\n"
      "v_mul_f32 v198, s51, v198\n"
      "s_waitcnt vmcnt(8)\n"
      "v_lshlrev_b32 v35, 16, v86\n"
      "v_mul_f32 v35, s51, v35\n"
      "v_and_b32 v60, 0xffff0000, v62\n"
      "v_mul_f32 v99, s51, v99\n"
      "v_lshlrev_b32 v229, 16, v76\n"
      "v_mul_f32 v229, s51, v229\n"
      "v_lshlrev_b32 v105, 16, v87\n"
      "v_perm_b32 v68, v53, v231, v238\n"
      "v_and_b32 v199, 0xffff0000, v86\n"
      "v_perm_b32 v205, v21, v69, v239\n"
      "v_and_b32 v231, 0xffff0000, v76\n"
      "v_cvt_pk_bf16_f32 v53, v66, v67\n"
      "v_mul_f32 v167, s51, v167\n"
      "v_mul_f32 v199, s51, v199\n"
      "v_lshlrev_b32 v33, 16, v84\n"
      "v_cvt_pk_bf16_f32 v76, v166, v167\n"
      "v_mul_f32 v231, s51, v231\n"
      "v_and_b32 v62, 0xffff0000, v88\n"
      "v_lshlrev_b32 v34, 16, v90\n"
      "v_and_b32 v67, 0xffff0000, v77\n"
      "v_mul_f32 v34, s51, v34\n"
      "v_and_b32 v165, 0xffff0000, v85\n"
      "v_cvt_pk_bf16_f32 v239, v34, v35\n"
      "v_mul_f32 v228, s51, v228\n"
      "v_and_b32 v66, 0xffff0000, v61\n"
      "v_mul_f32 v20, s51, v20\n"
      "v_mul_f32 v67, s51, v67\n"
      "v_and_b32 v90, 0xffff0000, v91\n"
      "v_cvt_pk_bf16_f32 v89, v198, v199\n"
      "v_mul_f32 v90, s51, v90\n"
      "v_mul_f32 v105, s51, v105\n"
      "v_mul_f32 v104, s51, v104\n"
      "v_mul_f32 v82, s51, v82\n"
      "v_and_b32 v83, 0xffff0000, v79\n"
      "v_mul_f32 v62, s51, v62\n"
      "v_and_b32 v63, 0xffff0000, v84\n"
      "v_and_b32 v91, 0xffff0000, v87\n"
      "v_mul_f32 v91, s51, v91\n"
      "v_mul_f32 v66, s51, v66\n"
      "v_and_b32 v61, 0xffff0000, v78\n"
      "v_mul_f32 v236, s51, v236\n"
      "v_lshlrev_b32 v21, 16, v85\n"
      "v_lshlrev_b32 v237, 16, v79\n"
      "v_mul_f32 v237, s51, v237\n"
      "v_mul_f32 v61, s51, v61\n"
      "v_perm_b32 v69, v23, v71, v238\n"
      "v_mul_f32 v21, s51, v21\n"
      "v_mul_f32 v33, s51, v33\n"
      "v_cvt_pk_bf16_f32 v79, v90, v91\n"
      "v_cvt_pk_bf16_f32 v91, v104, v105\n"
      "v_lshlrev_b32 v32, 16, v88\n"
      "v_cvt_pk_bf16_f32 v238, v98, v99\n"
      "v_mul_f32 v83, s51, v83\n"
      "v_cvt_pk_bf16_f32 v78, v82, v83\n"
      "v_mul_f32 v32, s51, v32\n"
      "v_cvt_pk_bf16_f32 v195, v32, v33\n"
      "v_cvt_pk_bf16_f32 v77, v20, v21\n"
      "v_mul_f32 v63, s51, v63\n"
      "v_cvt_pk_bf16_f32 v90, v236, v237\n"
      "v_cvt_pk_bf16_f32 v194, v228, v229\n"
      "v_mul_f32 v165, s51, v165\n"
      "v_cvt_pk_bf16_f32 v237, v62, v63\n"
      "v_mul_f32 v164, s51, v164\n"
      "v_mul_f32 v60, s51, v60\n"
      "v_cvt_pk_bf16_f32 v88, v60, v61\n"
      "v_cvt_pk_bf16_f32 v61, v164, v165\n"
      "v_cvt_pk_bf16_f32 v60, v66, v67\n"
      "v_cvt_pk_bf16_f32 v236, v230, v231\n"
      "v_mfma_f32_16x16x32_bf16 v[228:231], v[212:215], a[96:99], 0\n"
      "s_add_u32 m0, s53, 12288\n"
      "ds_write_b64 v232, v[90:91] offset:40960\n"
      "ds_write_b64 v255, v[204:205] offset:61440\n"
      "ds_write_b64 v255, v[60:61] offset:45056\n"
      "ds_write_b64 v232, v[238:239] offset:32768\n"
      "ds_write_b64 v255, v[76:77] offset:40960\n"
      "ds_write_b64 v232, v[68:69] offset:57344\n"
      "ds_write_b64 v232, v[88:89] offset:36864\n"
      "ds_write_b64 v232, v[78:79] offset:45056\n"
      "ds_write_b64 v255, v[236:237] offset:36864\n"
      "ds_write_b64 v255, v[194:195] offset:32768\n"
      "buffer_load_dwordx4 v1, s[40:43], s77 offen offset:0 lds\n"
      "v_cvt_pk_bf16_f32 v235, v100, v101\n"
      "v_mfma_f32_16x16x32_bf16 v[236:239], a[48:51], v[212:215], 0\n"
      "v_mfma_f32_16x16x32_bf16 v[236:239], a[52:55], v[124:127], v[236:239]\n"
      "v_mfma_f32_16x16x32_bf16 v[236:239], a[56:59], v[188:191], v[236:239]\n"
      "buffer_load_dwordx4 v1, s[40:43], s77 offen offset:2048 lds\n"
      "v_mfma_f32_16x16x32_bf16 v[236:239], a[60:63], v[244:247], v[236:239]\n"
      "buffer_load_dwordx4 v1, s[40:43], s78 offen offset:1024 lds\n"
      "v_mfma_f32_16x16x32_bf16 v[76:79], v[212:215], a[80:83], 0\n"
      "v_mfma_f32_16x16x32_bf16 v[76:79], v[124:127], a[84:87], v[76:79]\n"
      "v_mfma_f32_16x16x32_bf16 v[76:79], v[188:191], a[88:91], v[76:79]\n"
      "v_mfma_f32_16x16x32_bf16 v[76:79], v[244:247], a[92:95], v[76:79]\n"
      "s_waitcnt lgkmcnt(0)\n"
      "s_barrier\n"
      "ds_read_b128 a[156:159], v254 offset:39936\n"
      "buffer_load_dwordx4 v1, s[40:43], s78 offen offset:3072 lds\n"
      "ds_read_b128 a[144:147], v254 offset:36864\n"
      "v_mfma_f32_16x16x32_bf16 v[164:167], a[32:35], v[212:215], 0\n"
      "ds_read_b128 a[148:151], v254 offset:37888\n"
      "v_mfma_f32_16x16x32_bf16 v[164:167], a[36:39], v[124:127], v[164:167]\n"
      "ds_read_b128 a[152:155], v254 offset:38912\n"
      "v_mfma_f32_16x16x32_bf16 v[164:167], a[40:43], v[188:191], v[164:167]\n"
      "v_mfma_f32_16x16x32_bf16 v[164:167], a[44:47], v[244:247], v[164:167]\n"
      "ds_read_b128 a[180:183], v254 offset:46080\n"
      "v_cvt_pk_bf16_f32 v99, v118, v119\n"
      "v_mfma_f32_16x16x32_bf16 v[228:231], v[124:127], a[100:103], v[228:231]\n"
      "ds_read_b128 a[164:167], v254 offset:41984\n"
      "v_mfma_f32_16x16x32_bf16 v[228:231], v[188:191], a[104:107], v[228:231]\n"
      "ds_read_b128 a[172:175], v254 offset:44032\n"
      "v_mfma_f32_16x16x32_bf16 v[228:231], v[244:247], a[108:111], v[228:231]\n"
      "ds_read_b128 a[176:179], v254 offset:45056\n"
      "v_cvt_pk_bf16_f32 v211, v206, v207\n"
      "v_mfma_f32_16x16x32_bf16 v[44:47], a[8:11], v[188:191], v[44:47]\n"
      "ds_read_b128 a[188:191], v254 offset:48128\n"
      "v_mfma_f32_16x16x32_bf16 v[44:47], a[12:15], v[244:247], v[44:47]\n"
      "ds_read_b128 a[160:163], v254 offset:40960\n"
      "s_waitcnt lgkmcnt(8)\n"
      "v_mfma_f32_16x16x32_bf16 v[88:91], a[144:147], v[72:75], 0\n"
      "ds_read_b128 a[184:187], v254 offset:47104\n"
      "s_waitcnt lgkmcnt(8)\n"
      "v_mfma_f32_16x16x32_bf16 v[88:91], a[148:151], v[224:227], v[88:91]\n"
      "ds_read_b128 a[192:195], v254 offset:49152\n"
      "s_waitcnt lgkmcnt(8)\n"
      "v_mfma_f32_16x16x32_bf16 v[88:91], a[152:155], v[92:95], v[88:91]\n"
      "ds_read_b128 a[204:207], v254 offset:52224\n"
      "s_add_u32 m0, s53, 8192\n"
      "v_mfma_f32_16x16x32_bf16 v[88:91], a[156:159], v[220:223], v[88:91]\n"
      "ds_read_b128 a[248:251], v254 offset:63488\n"
      "v_mfma_f32_16x16x32_bf16 v[68:71], a[144:147], v[212:215], 0\n"
      "ds_read_b128 a[168:171], v254 offset:43008\n"
      "v_cvt_pk_bf16_f32 v82, v28, v29\n"
      "v_mfma_f32_16x16x32_bf16 v[68:71], a[148:151], v[124:127], v[68:71]\n"
      "ds_read_b128 a[216:219], v254 offset:55296\n"
      "v_mfma_f32_16x16x32_bf16 v[68:71], a[152:155], v[188:191], v[68:71]\n"
      "ds_read_b128 a[240:243], v254 offset:61440\n"
      "v_mfma_f32_16x16x32_bf16 v[68:71], a[156:159], v[244:247], v[68:71]\n"
      "ds_read_b128 a[208:211], v254 offset:53248\n"
      "s_waitcnt lgkmcnt(10)\n"
      "v_mfma_f32_16x16x32_bf16 v[84:87], a[176:179], v[72:75], 0\n"
      "ds_read_b128 a[132:135], v254 offset:33792\n"
      "v_mfma_f32_16x16x32_bf16 v[84:87], a[180:183], v[224:227], v[84:87]\n"
      "s_waitcnt lgkmcnt(8)\n"
      "v_mfma_f32_16x16x32_bf16 v[84:87], a[184:187], v[92:95], v[84:87]\n"
      "L_h0_%=:\n"
      "s_cbranch_execz L_hr_%=\n"
      "v_mfma_f32_16x16x32_bf16 v[84:87], a[188:191], v[220:223], v[84:87]\n"
      "ds_read_b128 a[252:255], v254 offset:64512\n"
      "v_cvt_pk_bf16_f32 v52, v64, v65\n"
      "v_mfma_f32_16x16x32_bf16 v[64:67], a[176:179], v[212:215], 0\n"
      "buffer_load_dwordx4 v1, s[40:43], s90 offen offset:0 lds\n"
      "v_mfma_f32_16x16x32_bf16 v[64:67], a[180:183], v[124:127], v[64:67]\n"
      "v_cvt_pk_bf16_f32 v255, v90, v91\n"
      "v_mfma_f32_16x16x32_bf16 v[64:67], a[184:187], v[188:191], v[64:67]\n"
      "ds_read_b128 a[128:131], v254 offset:32768\n"
      "v_mfma_f32_16x16x32_bf16 v[64:67], a[188:191], v[244:247], v[64:67]\n"
      "ds_read_b128 a[244:247], v254 offset:62464\n"
      "v_cvt_pk_bf16_f32 v119, v230, v231\n"
      "v_mfma_f32_16x16x32_bf16 v[60:63], a[160:163], v[212:215], 0\n"
      "ds_read_b128 a[224:227], v254 offset:57344\n"
      "v_cvt_pk_bf16_f32 v230, v136, v137\n"
      "v_mfma_f32_16x16x32_bf16 v[60:63], a[164:167], v[124:127], v[60:63]\n"
      "ds_read_b128 a[140:143], v254 offset:35840\n"
      "s_waitcnt lgkmcnt(9)\n"
      "v_mfma_f32_16x16x32_bf16 v[60:63], a[168:171], v[188:191], v[60:63]\n"
      "v_mfma_f32_16x16x32_bf16 v[60:63], a[172:175], v[244:247], v[60:63]\n"
      "ds_read_b128 a[136:139], v254 offset:34816\n"
      "v_mfma_f32_16x16x32_bf16 v[204:207], a[160:163], v[72:75], 0\n"
      "ds_read_b128 a[228:231], v254 offset:58368\n"
      "v_mfma_f32_16x16x32_bf16 v[204:207], a[164:167], v[224:227], v[204:207]\n"
      "v_cvt_pk_bf16_f32 v123, v78, v79\n"
      "v_mfma_f32_16x16x32_bf16 v[204:207], a[168:171], v[92:95], v[204:207]\n"
      "buffer_load_dwordx4 v1, s[40:43], s90 offen offset:2048 lds\n"
      "v_mfma_f32_16x16x32_bf16 v[204:207], a[172:175], v[220:223], v[204:207]\n"
      "s_waitcnt lgkmcnt(9)\n"
      "v_mfma_f32_16x16x32_bf16 v[40:43], v[72:75], a[240:243], 0\n"
      "ds_read_b128 a[236:239], v254 offset:60416\n"
      "s_waitcnt lgkmcnt(5)\n"
      "v_mfma_f32_16x16x32_bf16 v[40:43], v[224:227], a[244:247], v[40:43]\n"
      "v_mfma_f32_16x16x32_bf16 v[40:43], v[92:95], a[248:251], v[40:43]\n"
      "ds_read_b128 a[200:203], v254 offset:51200\n"
      "v_cvt_pk_bf16_f32 v199, v88, v89\n"
      "v_mfma_f32_16x16x32_bf16 v[40:43], v[220:223], a[252:255], v[40:43]\n"
      "ds_read_b128 a[196:199], v254 offset:50176\n"
      "v_mfma_f32_16x16x32_bf16 v[88:91], a[128:131], v[72:75], 0\n"
      "v_mfma_f32_16x16x32_bf16 v[88:91], a[132:135], v[224:227], v[88:91]\n"
      "s_waitcnt lgkmcnt(4)\n"
      "v_mfma_f32_16x16x32_bf16 v[88:91], a[136:139], v[92:95], v[88:91]\n"
      "ds_read_b128 a[220:223], v254 offset:56320\n"
      "v_mfma_f32_16x16x32_bf16 v[88:91], a[140:143], v[220:223], v[88:91]\n"
      "buffer_load_dwordx4 v1, s[40:43], s91 offen offset:1024 lds\n"
      "v_mfma_f32_16x16x32_bf16 v[32:35], a[128:131], v[212:215], 0\n"
      "ds_read_b128 a[232:235], v254 offset:59392\n"
      "v_mfma_f32_16x16x32_bf16 v[32:35], a[132:135], v[124:127], v[32:35]\n"
      "v_mfma_f32_16x16x32_bf16 v[32:35], a[136:139], v[188:191], v[32:35]\n"
      "s_waitcnt vmcnt(11)\n"
      "ds_read_b128 v[20:23], v0 offset:4096\n"
      "v_mfma_f32_16x16x32_bf16 v[32:35], a[140:143], v[244:247], v[32:35]\n"
      "ds_read_b128 a[212:215], v254 offset:54272\n"
      "ds_read_b128 v[100:103], v0 offset:4608\n"
      "v_mfma_f32_16x16x32_bf16 v[24:27], v[72:75], a[192:195], 0\n"
      "v_cvt_pk_bf16_f32 v198, v88, v89\n"
      "s_waitcnt lgkmcnt(5)\n"
      "v_mfma_f32_16x16x32_bf16 v[24:27], v[224:227], a[196:199], v[24:27]\n"
      "ds_read_b128 v[216:219], v0 offset:5632\n"
      "ds_read_b128 v[112:115], v0 offset:5120\n"
      "v_mfma_f32_16x16x32_bf16 v[24:27], v[92:95], a[200:203], v[24:27]\n"
      "v_cvt_pk_bf16_f32 v231, v138, v139\n"
      "v_mfma_f32_16x16x32_bf16 v[24:27], v[220:223], a[204:207], v[24:27]\n"
      "v_cvt_pk_bf16_f32 v254, v90, v91\n"
      "v_mfma_f32_16x16x32_bf16 v[152:155], v[72:75], a[224:227], 0\n"
      "buffer_load_dwordx4 v1, s[40:43], s91 offen offset:3072 lds\n"
      "v_mfma_f32_16x16x32_bf16 v[152:155], v[224:227], a[228:231], v[152:155]\n"
      "s_waitcnt lgkmcnt(5)\n"
      "v_mfma_f32_16x16x32_bf16 v[152:155], v[92:95], a[232:235], v[152:155]\n"
      "v_mfma_f32_16x16x32_bf16 v[152:155], v[220:223], a[236:239], v[152:155]\n"
      "v_mfma_f32_16x16x32_bf16 v[160:163], v[72:75], a[208:211], 0\n"
      "s_waitcnt lgkmcnt(3)\n"
      "v_mfma_f32_16x16x32_bf16 v[160:163], v[224:227], a[212:215], v[160:163]\n"
      "v_cvt_pk_bf16_f32 v140, v164, v165\n"
      "v_mfma_f32_16x16x32_bf16 v[160:163], v[92:95], a[216:219], v[160:163]\n"
      "v_cvt_pk_bf16_f32 v83, v30, v31\n"
      "v_mfma_f32_16x16x32_bf16 v[160:163], v[220:223], a[220:223], v[160:163]\n"
      "v_mfma_f32_16x16x32_bf16 v[194:197], a[176:179], v[20:23], 0\n"
      "s_waitcnt lgkmcnt(2)\n"
      "v_mfma_f32_16x16x32_bf16 v[194:197], a[180:183], v[100:103], v[194:197]\n"
      "s_waitcnt lgkmcnt(0)\n"
      "v_mfma_f32_16x16x32_bf16 v[194:197], a[184:187], v[112:115], v[194:197]\n"
      "v_cvt_pk_bf16_f32 v19, v154, v42\n"
      "v_cvt_pk_bf16_f32 v57, v155, v43\n"
      "v_mfma_f32_16x16x32_bf16 v[194:197], a[188:191], v[216:219], v[194:197]\n"
      "s_barrier\n"
      "v_mfma_f32_16x16x32_bf16 v[78:81], v[20:23], a[192:195], 0\n"
      "v_mfma_f32_16x16x32_bf16 v[78:81], v[100:103], a[196:199], v[78:81]\n"
      "v_mfma_f32_16x16x32_bf16 v[78:81], v[112:115], a[200:203], v[78:81]\n"
      "v_mfma_f32_16x16x32_bf16 v[78:81], v[216:219], a[204:207], v[78:81]\n"
      "ds_write_b64 v253, v[210:211] offset:384\n"
      "v_mfma_f32_16x16x32_bf16 v[28:31], v[20:23], a[80:83], 0\n"
      "v_mfma_f32_16x16x32_bf16 v[28:31], v[100:103], a[84:87], v[28:31]\n"
      "v_cvt_pk_bf16_f32 v55, v106, v107\n"
      "v_mfma_f32_16x16x32_bf16 v[28:31], v[112:115], a[88:91], v[28:31]\n"
      "v_cvt_pk_bf16_f32 v232, v96, v97\n"
      "v_mfma_f32_16x16x32_bf16 v[28:31], v[216:219], a[92:95], v[28:31]\n"
      "ds_write_b64 v252, v[254:255] offset:512\n"
      "v_mfma_f32_16x16x32_bf16 v[104:107], a[160:163], v[20:23], 0\n"
      "v_mfma_f32_16x16x32_bf16 v[104:107], a[164:167], v[100:103], v[104:107]\n"
      "v_mfma_f32_16x16x32_bf16 v[104:107], a[168:171], v[112:115], v[104:107]\n"
      "v_mfma_f32_16x16x32_bf16 v[104:107], a[172:175], v[216:219], v[104:107]\n"
      "ds_write_b64 v253, v[230:231] offset:128\n"
      "v_mfma_f32_16x16x32_bf16 v[224:227], v[20:23], a[96:99], 0\n"
      "s_add_u32 m0, s53, 4096\n"
      "v_mfma_f32_16x16x32_bf16 v[224:227], v[100:103], a[100:103], v[224:227]\n"
      "v_mfma_f32_16x16x32_bf16 v[224:227], v[112:115], a[104:107], v[224:227]\n"
      "v_cvt_pk_bf16_f32 v92, v60, v61\n"
      "v_mfma_f32_16x16x32_bf16 v[224:227], v[216:219], a[108:111], v[224:227]\n"
      "ds_write_b128 v251, v[52:55] offset:0\n"
      "buffer_load_dwordx4 v1, s[40:43], s92 offen offset:0 lds\n"
      "v_cvt_pk_bf16_f32 v89, v34, v35\n"
      "v_mfma_f32_16x16x32_bf16 v[154:157], v[20:23], a[112:115], 0\n"
      "v_mfma_f32_16x16x32_bf16 v[154:157], v[100:103], a[116:119], v[154:157]\n"
      "v_mfma_f32_16x16x32_bf16 v[154:157], v[112:115], a[120:123], v[154:157]\n"
      "v_mfma_f32_16x16x32_bf16 v[154:157], v[216:219], a[124:127], v[154:157]\n"
      "s_nop 0\n"
      "v_cvt_pk_bf16_f32 v254, v224, v225\n"
      "v_cvt_pk_bf16_f32 v54, v184, v185\n"
      "v_mfma_f32_16x16x32_bf16 v[200:203], v[20:23], a[64:67], 0\n"
      "buffer_load_dwordx4 v1, s[40:43], s92 offen offset:2048 lds\n"
      "v_mfma_f32_16x16x32_bf16 v[200:203], v[100:103], a[68:71], v[200:203]\n"
      "v_cvt_pk_bf16_f32 v164, v24, v160\n"
      "v_mfma_f32_16x16x32_bf16 v[200:203], v[112:115], a[72:75], v[200:203]\n"
      "v_cvt_pk_bf16_f32 v56, v27, v163\n"
      "v_cvt_pk_bf16_f32 v230, v148, v149\n"
      "v_mfma_f32_16x16x32_bf16 v[200:203], v[216:219], a[76:79], v[200:203]\n"
      "v_cvt_pk_bf16_f32 v184, v204, v205\n"
      "v_cvt_pk_bf16_f32 v165, v152, v40\n"
      "v_mfma_f32_16x16x32_bf16 v[208:211], a[0:3], v[20:23], 0\n"
      "s_waitcnt vmcnt(10)\n"
      "ds_read_b128 v[172:175], v0 offset:16384\n"
      "ds_read_b128 v[128:131], v0 offset:17408\n"
      "v_mfma_f32_16x16x32_bf16 v[208:211], a[4:7], v[100:103], v[208:211]\n"
      "v_mfma_f32_16x16x32_bf16 v[208:211], a[8:11], v[112:115], v[208:211]\n"
      "buffer_load_dwordx4 v1, s[40:43], s93 offen offset:1024 lds\n"
      "v_mfma_f32_16x16x32_bf16 v[208:211], a[12:15], v[216:219], v[208:211]\n"
      "v_mfma_f32_16x16x32_bf16 v[180:183], v[20:23], a[240:243], 0\n"
      "v_cvt_pk_bf16_f32 v138, v48, v49\n"
      "v_mfma_f32_16x16x32_bf16 v[180:183], v[100:103], a[244:247], v[180:183]\n"
      "v_cvt_pk_bf16_f32 v231, v150, v151\n"
      "v_cvt_pk_bf16_f32 v27, v156, v157\n"
      "v_mfma_f32_16x16x32_bf16 v[180:183], v[112:115], a[248:251], v[180:183]\n"
      "v_mfma_f32_16x16x32_bf16 v[180:183], v[216:219], a[252:255], v[180:183]\n"
      "v_cvt_pk_bf16_f32 v18, v26, v162\n"
      "v_cvt_pk_bf16_f32 v136, v44, v45\n"
      "v_mfma_f32_16x16x32_bf16 v[176:179], a[16:19], v[20:23], 0\n"
      "ds_read_b128 v[36:39], v0 offset:17920\n"
      "ds_read_b128 v[72:75], v0 offset:16896\n"
      "v_mfma_f32_16x16x32_bf16 v[176:179], a[20:23], v[100:103], v[176:179]\n"
      "v_mfma_f32_16x16x32_bf16 v[176:179], a[24:27], v[112:115], v[176:179]\n"
      "v_cvt_pk_bf16_f32 v98, v116, v117\n"
      "v_mfma_f32_16x16x32_bf16 v[176:179], a[28:31], v[216:219], v[176:179]\n"
      "ds_write_b64 v253, v[164:165] offset:4096\n"
      "v_mfma_f32_16x16x32_bf16 v[58:61], a[128:131], v[20:23], 0\n"
      "v_mfma_f32_16x16x32_bf16 v[58:61], a[132:135], v[100:103], v[58:61]\n"
      "v_cvt_pk_bf16_f32 v26, v154, v155\n"
      "v_mfma_f32_16x16x32_bf16 v[58:61], a[136:139], v[112:115], v[58:61]\n"
      "v_mfma_f32_16x16x32_bf16 v[58:61], a[140:143], v[216:219], v[58:61]\n"
      "ds_write_b64 v253, v[82:83] offset:0\n"
      "v_cvt_pk_bf16_f32 v91, v70, v71\n"
      "v_mfma_f32_16x16x32_bf16 v[154:157], a[48:51], v[20:23], 0\n"
      "v_mfma_f32_16x16x32_bf16 v[154:157], a[52:55], v[100:103], v[154:157]\n"
      "v_mfma_f32_16x16x32_bf16 v[154:157], a[56:59], v[112:115], v[154:157]\n"
      "buffer_load_dwordx4 v1, s[40:43], s93 offen offset:3072 lds\n"
      "v_mfma_f32_16x16x32_bf16 v[154:157], a[60:63], v[216:219], v[154:157]\n"
      "ds_write_b64 v253, v[26:27] offset:2432\n"
      "v_mfma_f32_16x16x32_bf16 v[162:165], v[20:23], a[224:227], 0\n"
      "v_cvt_pk_bf16_f32 v185, v84, v85\n"
      "s_add_u32 m0, s53, 0\n"
      "v_mfma_f32_16x16x32_bf16 v[162:165], v[100:103], a[228:231], v[162:165]\n"
      "v_cvt_pk_bf16_f32 v88, v32, v33\n"
      "v_mfma_f32_16x16x32_bf16 v[162:165], v[112:115], a[232:235], v[162:165]\n"
      "v_mfma_f32_16x16x32_bf16 v[162:165], v[216:219], a[236:239], v[162:165]\n"
      "ds_write_b64 v252, v[198:199] offset:0\n"
      "v_cvt_pk_bf16_f32 v26, v154, v155\n"
      "v_mfma_f32_16x16x32_bf16 v[82:85], a[32:35], v[20:23], 0\n"
      "buffer_load_dwordx4 v1, s[40:43], s94 offen offset:0 lds\n"
      "v_mfma_f32_16x16x32_bf16 v[82:85], a[36:39], v[100:103], v[82:85]\n"
      "v_mfma_f32_16x16x32_bf16 v[82:85], a[40:43], v[112:115], v[82:85]\n"
      "buffer_load_dwordx4 v1, s[40:43], s94 offen offset:2048 lds\n"
      "v_mfma_f32_16x16x32_bf16 v[82:85], a[44:47], v[216:219], v[82:85]\n"
      "v_cvt_pk_bf16_f32 v221, v210, v211\n"
      "v_cvt_pk_bf16_f32 v160, v206, v207\n"
      "v_mfma_f32_16x16x32_bf16 v[240:243], v[20:23], a[208:211], 0\n"
      "v_cvt_pk_bf16_f32 v137, v46, v47\n"
      "v_cvt_pk_bf16_f32 v255, v226, v227\n"
      "v_mfma_f32_16x16x32_bf16 v[240:243], v[100:103], a[212:215], v[240:243]\n"
      "v_cvt_pk_bf16_f32 v71, v165, v183\n"
      "v_cvt_pk_bf16_f32 v27, v156, v157\n"
      "v_mfma_f32_16x16x32_bf16 v[240:243], v[112:115], a[216:219], v[240:243]\n"
      "v_mfma_f32_16x16x32_bf16 v[240:243], v[216:219], a[220:223], v[240:243]\n"
      "ds_write_b64 v253, v[98:99] offset:256\n"
      "s_waitcnt lgkmcnt(8)\n"
      "v_mfma_f32_16x16x32_bf16 v[154:157], a[16:19], v[172:175], 0\n"
      "s_waitcnt lgkmcnt(5)\n"
      "v_mfma_f32_16x16x32_bf16 v[154:157], a[20:23], v[72:75], v[154:157]\n"
      "v_cvt_pk_bf16_f32 v110, v200, v201\n"
      "v_mfma_f32_16x16x32_bf16 v[154:157], a[24:27], v[128:131], v[154:157]\n"
      "v_mfma_f32_16x16x32_bf16 v[154:157], a[28:31], v[36:39], v[154:157]\n"
      "v_mfma_f32_16x16x32_bf16 v[42:45], a[144:147], v[20:23], 0\n"
      "buffer_load_dwordx4 v1, s[40:43], s95 offen offset:1024 lds\n"
      "v_mfma_f32_16x16x32_bf16 v[42:45], a[148:151], v[100:103], v[42:45]\n"
      "v_cvt_pk_bf16_f32 v222, v176, v177\n"
      "v_mfma_f32_16x16x32_bf16 v[42:45], a[152:155], v[112:115], v[42:45]\n"
      "v_cvt_pk_bf16_f32 v142, v236, v237\n"
      "v_cvt_pk_bf16_f32 v111, v202, v203\n"
      "v_mfma_f32_16x16x32_bf16 v[42:45], a[156:159], v[216:219], v[42:45]\n"
      "v_cvt_pk_bf16_f32 v183, v194, v195\n"
      "v_cvt_pk_bf16_f32 v95, v66, v67\n"
      "v_mfma_f32_16x16x32_bf16 v[200:203], a[144:147], v[172:175], 0\n"
      "v_cvt_pk_bf16_f32 v205, v164, v182\n"
      "v_cvt_pk_bf16_f32 v113, v153, v41\n"
      "v_mfma_f32_16x16x32_bf16 v[200:203], a[148:151], v[72:75], v[200:203]\n"
      "v_mfma_f32_16x16x32_bf16 v[200:203], a[152:155], v[128:131], v[200:203]\n"
      "v_mfma_f32_16x16x32_bf16 v[200:203], a[156:159], v[36:39], v[200:203]\n"
      "v_cvt_pk_bf16_f32 v139, v50, v51\n"
      "v_cvt_pk_bf16_f32 v93, v62, v63\n"
      "v_mfma_f32_16x16x32_bf16 v[20:23], v[172:175], a[112:115], 0\n"
      "v_cvt_pk_bf16_f32 v143, v238, v239\n"
      "v_cvt_pk_bf16_f32 v164, v28, v29\n"
      "v_mfma_f32_16x16x32_bf16 v[20:23], v[72:75], a[116:119], v[20:23]\n"
      "v_mfma_f32_16x16x32_bf16 v[20:23], v[128:131], a[120:123], v[20:23]\n"
      "v_cvt_pk_bf16_f32 v112, v25, v161\n"
      "v_mfma_f32_16x16x32_bf16 v[20:23], v[36:39], a[124:127], v[20:23]\n"
      "ds_write_b64 v253, v[112:113] offset:4224\n"
      "v_cvt_pk_bf16_f32 v66, v154, v155\n"
      "v_mfma_f32_16x16x32_bf16 v[236:239], a[48:51], v[172:175], 0\n"
      "s_waitcnt vmcnt(11)\n"
      "ds_read_b128 v[48:51], v0 offset:12288\n"
      "ds_read_b128 v[224:227], v0 offset:13824\n"
      "v_mfma_f32_16x16x32_bf16 v[236:239], a[52:55], v[72:75], v[236:239]\n"
      "buffer_load_dwordx4 v1, s[40:43], s95 offen offset:3072 lds\n"
      "v_mfma_f32_16x16x32_bf16 v[236:239], a[56:59], v[128:131], v[236:239]\n"
      "v_cvt_pk_bf16_f32 v55, v186, v187\n"
      "v_cvt_pk_bf16_f32 v161, v86, v87\n"
      "v_mfma_f32_16x16x32_bf16 v[236:239], a[60:63], v[36:39], v[236:239]\n"
      "s_add_u32 m0, s53, 20480\n"
      "ds_write_b128 v251, v[232:235] offset:2048\n"
      "s_add_u32 s40, s40, 13107200\n s_addc_u32 s41, s41, 0\n"
      "ds_read_b128 v[216:219], v0 offset:12800\n"
      "v_mfma_f32_16x16x32_bf16 v[112:115], a[160:163], v[172:175], 0\n"
      "v_mfma_f32_16x16x32_bf16 v[112:115], a[164:167], v[72:75], v[112:115]\n"
      "v_mfma_f32_16x16x32_bf16 v[112:115], a[168:171], v[128:131], v[112:115]\n"
      "buffer_load_dwordx4 v1, s[40:43], s73 offen offset:0 lds\n"
      "v_mfma_f32_16x16x32_bf16 v[112:115], a[172:175], v[36:39], v[112:115]\n"
      "v_cvt_pk_bf16_f32 v70, v81, v243\n"
      "v_cvt_pk_bf16_f32 v148, v79, v241\n"
      "v_mfma_f32_16x16x32_bf16 v[232:235], v[172:175], a[96:99], 0\n"
      "v_mfma_f32_16x16x32_bf16 v[232:235], v[72:75], a[100:103], v[232:235]\n"
      "v_cvt_pk_bf16_f32 v149, v163, v181\n"
      "v_cvt_pk_bf16_f32 v90, v68, v69\n"
      "v_mfma_f32_16x16x32_bf16 v[232:235], v[128:131], a[104:107], v[232:235]\n"
      "v_cvt_pk_bf16_f32 v103, v202, v203\n"
      "v_mfma_f32_16x16x32_bf16 v[232:235], v[36:39], a[108:111], v[232:235]\n"
      "ds_write_b64 v253, v[70:71] offset:6528\n"
      "v_mfma_f32_16x16x32_bf16 v[96:99], a[32:35], v[172:175], 0\n"
      "v_cvt_pk_bf16_f32 v203, v44, v45\n"
      "v_cvt_pk_bf16_f32 v67, v156, v157\n"
      "v_mfma_f32_16x16x32_bf16 v[96:99], a[36:39], v[72:75], v[96:99]\n"
      "buffer_load_dwordx4 v1, s[40:43], s74 offen offset:1024 lds\n"
      "v_mfma_f32_16x16x32_bf16 v[96:99], a[40:43], v[128:131], v[96:99]\n"
      "v_mfma_f32_16x16x32_bf16 v[96:99], a[44:47], v[36:39], v[96:99]\n"
      "v_cvt_pk_bf16_f32 v71, v238, v239\n"
      "v_cvt_pk_bf16_f32 v25, v84, v85\n"
      "v_mfma_f32_16x16x32_bf16 v[156:159], v[172:175], a[64:67], 0\n"
      "ds_read_b128 v[132:135], v0 offset:13312\n"
      "v_cvt_pk_bf16_f32 v116, v232, v233\n"
      "v_mfma_f32_16x16x32_bf16 v[156:159], v[72:75], a[68:71], v[156:159]\n"
      "v_cvt_pk_bf16_f32 v28, v106, v107\n"
      "v_cvt_pk_bf16_f32 v94, v64, v65\n"
      "v_mfma_f32_16x16x32_bf16 v[156:159], v[128:131], a[72:75], v[156:159]\n"
      "v_mfma_f32_16x16x32_bf16 v[156:159], v[36:39], a[76:79], v[156:159]\n"
      "ds_write_b64 v253, v[18:19] offset:4352\n"
      "v_mfma_f32_16x16x32_bf16 v[62:65], a[128:131], v[172:175], 0\n"
      "v_cvt_pk_bf16_f32 v141, v166, v167\n"
      "v_cvt_pk_bf16_f32 v238, v78, v240\n"
      "v_mfma_f32_16x16x32_bf16 v[62:65], a[132:135], v[72:75], v[62:65]\n"
      "v_cvt_pk_bf16_f32 v78, v58, v59\n"
      "v_cvt_pk_bf16_f32 v165, v30, v31\n"
      "v_mfma_f32_16x16x32_bf16 v[62:65], a[136:139], v[128:131], v[62:65]\n"
      "v_mfma_f32_16x16x32_bf16 v[62:65], a[140:143], v[36:39], v[62:65]\n"
      "v_cvt_pk_bf16_f32 v53, v158, v159\n"
      "v_cvt_pk_bf16_f32 v118, v228, v229\n"
      "v_mfma_f32_16x16x32_bf16 v[152:155], v[136:139], v[88:91], 0\n"
      "v_mfma_f32_16x16x32_bf16 v[152:155], v[140:143], v[92:95], v[152:155]\n"
      "ds_write_b64 v253, v[110:111] offset:2048\n"
      "s_waitcnt lgkmcnt(7)\n"
      "v_mfma_f32_16x16x32_bf16 v[84:87], v[48:51], a[112:115], 0\n"
      "v_cvt_pk_bf16_f32 v182, v104, v105\n"
      "s_waitcnt lgkmcnt(4)\n"
      "v_mfma_f32_16x16x32_bf16 v[84:87], v[216:219], a[116:119], v[84:87]\n"
      "v_cvt_pk_bf16_f32 v101, v64, v65\n"
      "s_waitcnt lgkmcnt(2)\n"
      "v_mfma_f32_16x16x32_bf16 v[84:87], v[132:135], a[120:123], v[84:87]\n"
      "v_cvt_pk_bf16_f32 v100, v62, v63\n"
      "v_mfma_f32_16x16x32_bf16 v[84:87], v[224:227], a[124:127], v[84:87]\n"
      "ds_write_b64 v252, v[182:183] offset:2304\n"
      "v_cvt_pk_bf16_f32 v69, v98, v99\n"
      "v_mfma_f32_16x16x32_bf16 v[44:47], a[32:35], v[48:51], 0\n"
      "v_mfma_f32_16x16x32_bf16 v[44:47], a[36:39], v[216:219], v[44:47]\n"
      "v_cvt_pk_bf16_f32 v228, v20, v21\n"
      "v_mfma_f32_16x16x32_bf16 v[44:47], a[40:43], v[132:135], v[44:47]\n"
      "v_cvt_pk_bf16_f32 v52, v156, v157\n"
      "v_mfma_f32_16x16x32_bf16 v[44:47], a[44:47], v[224:227], v[44:47]\n"
      "ds_write_b64 v252, v[160:161] offset:2560\n"
      "v_mfma_f32_16x16x32_bf16 v[18:21], v[48:51], a[96:99], 0\n"
      "v_cvt_pk_bf16_f32 v117, v234, v235\n"
      "v_mfma_f32_16x16x32_bf16 v[18:21], v[216:219], a[100:103], v[18:21]\n"
      "v_cvt_pk_bf16_f32 v202, v60, v61\n"
      "v_cvt_pk_bf16_f32 v239, v162, v180\n"
      "v_mfma_f32_16x16x32_bf16 v[18:21], v[132:135], a[104:107], v[18:21]\n"
      "buffer_load_dwordx4 v1, s[40:43], s74 offen offset:3072 lds\n"
      "v_mfma_f32_16x16x32_bf16 v[18:21], v[224:227], a[108:111], v[18:21]\n"
      "ds_write_b64 v253, v[148:149] offset:6272\n"
      "v_cvt_pk_bf16_f32 v104, v112, v113\n"
      "v_mfma_f32_16x16x32_bf16 v[160:163], a[0:3], v[172:175], 0\n"
      "v_mfma_f32_16x16x32_bf16 v[160:163], a[4:7], v[72:75], v[160:163]\n"
      "v_mfma_f32_16x16x32_bf16 v[160:163], a[8:11], v[128:131], v[160:163]\n"
      "v_cvt_pk_bf16_f32 v102, v200, v201\n"
      "v_mfma_f32_16x16x32_bf16 v[160:163], a[12:15], v[36:39], v[160:163]\n"
      "ds_write_b64 v253, v[56:57] offset:4480\n"
      "v_mfma_f32_16x16x32_bf16 v[156:159], v[172:175], a[80:83], 0\n"
      "v_mfma_f32_16x16x32_bf16 v[156:159], v[72:75], a[84:87], v[156:159]\n"
      "v_cvt_pk_bf16_f32 v81, v46, v47\n"
      "v_mfma_f32_16x16x32_bf16 v[156:159], v[128:131], a[88:91], v[156:159]\n"
      "v_mfma_f32_16x16x32_bf16 v[156:159], v[36:39], a[92:95], v[156:159]\n"
      "ds_write_b64 v252, v[184:185] offset:2048\n"
      "ds_read_b128 v[232:235], v9 offset:0\n"
      "v_cvt_pk_bf16_f32 v70, v236, v237\n"
      "v_mfma_f32_16x16x32_bf16 v[60:63], a[176:179], v[172:175], 0\n"
      "v_exp_f32 v155, v155\n"
      "v_mfma_f32_16x16x32_bf16 v[60:63], a[180:183], v[72:75], v[60:63]\n"
      "v_mfma_f32_16x16x32_bf16 v[60:63], a[184:187], v[128:131], v[60:63]\n"
      "v_cvt_pk_bf16_f32 v68, v96, v97\n"
      "v_mfma_f32_16x16x32_bf16 v[60:63], a[188:191], v[36:39], v[60:63]\n"
      "ds_write_b64 v252, v[202:203] offset:768\n"
      "v_mfma_f32_16x16x32_bf16 v[56:59], v[48:51], a[64:67], 0\n"
      "s_waitcnt vmcnt(11)\n"
      "ds_read_b128 v[192:195], v0 offset:8192\n"
      "ds_read_b128 v[184:187], v0 offset:9216\n"
      "v_mfma_f32_16x16x32_bf16 v[56:59], v[216:219], a[68:71], v[56:59]\n"
      "v_cvt_pk_bf16_f32 v204, v80, v242\n"
      "v_cvt_pk_bf16_f32 v120, v156, v157\n"
      "v_mfma_f32_16x16x32_bf16 v[56:59], v[132:135], a[72:75], v[56:59]\n"
      "v_mfma_f32_16x16x32_bf16 v[56:59], v[224:227], a[76:79], v[56:59]\n"
      "ds_read_b128 v[240:243], v0 offset:9728\n"
      "ds_read_b128 v[200:203], v0 offset:8704\n"
      "v_mfma_f32_16x16x32_bf16 v[110:113], a[48:51], v[48:51], 0\n"
      "v_cvt_pk_bf16_f32 v65, v162, v163\n"
      "v_cvt_pk_bf16_f32 v29, v196, v197\n"
      "v_mfma_f32_16x16x32_bf16 v[110:113], a[52:55], v[216:219], v[110:113]\n"
      "v_cvt_pk_bf16_f32 v106, v60, v61\n"
      "v_cvt_pk_bf16_f32 v107, v62, v63\n"
      "v_mfma_f32_16x16x32_bf16 v[110:113], a[56:59], v[132:135], v[110:113]\n"
      "v_cvt_pk_bf16_f32 v64, v160, v161\n"
      "v_mfma_f32_16x16x32_bf16 v[110:113], a[60:63], v[224:227], v[110:113]\n"
      "ds_write_b64 v252, v[28:29] offset:2816\n"
      "v_cvt_pk_bf16_f32 v220, v208, v209\n"
      "v_mfma_f32_16x16x32_bf16 v[180:183], a[16:19], v[48:51], 0\n"
      "v_mfma_f32_16x16x32_bf16 v[180:183], a[20:23], v[216:219], v[180:183]\n"
      "v_mfma_f32_16x16x32_bf16 v[180:183], a[24:27], v[132:135], v[180:183]\n"
      "v_mfma_f32_16x16x32_bf16 v[180:183], a[28:31], v[224:227], v[180:183]\n"
      "ds_read_b128 v[32:35], v10 offset:0\n"
      "v_cvt_pk_bf16_f32 v24, v82, v83\n"
      "v_mfma_f32_16x16x32_bf16 v[160:163], v[64:67], v[88:91], 0\n"
      "v_exp_f32 v152, v152\n"
      "v_mfma_f32_16x16x32_bf16 v[160:163], v[68:71], v[92:95], v[160:163]\n"
      "v_exp_f32 v154, v154\n"
      "v_mfma_f32_16x16x32_bf16 v[96:99], a[0:3], v[48:51], 0\n"
      "v_cvt_pk_bf16_f32 v63, v20, v21\n"
      "v_mov_b64 v[144:145], 0\n"
      "v_mfma_f32_16x16x32_bf16 v[96:99], a[4:7], v[216:219], v[96:99]\n"
      "v_exp_f32 v153, v153\n"
      "v_mfma_f32_16x16x32_bf16 v[96:99], a[8:11], v[132:135], v[96:99]\n"
      "buffer_load_dwordx4 v1, s[40:43], s73 offen offset:2048 lds\n"
      "v_mfma_f32_16x16x32_bf16 v[96:99], a[12:15], v[224:227], v[96:99]\n"
      "ds_write_b64 v253, v[164:165] offset:2176\n"
      "s_waitcnt lgkmcnt(6)\n"
      "v_mfma_f32_16x16x32_bf16 v[166:169], a[48:51], v[192:195], 0\n"
      "s_waitcnt lgkmcnt(3)\n"
      "v_mfma_f32_16x16x32_bf16 v[166:169], a[52:55], v[200:203], v[166:169]\n"
      "v_cvt_pk_bf16_f32 v122, v76, v77\n"
      "v_mfma_f32_16x16x32_bf16 v[166:169], a[56:59], v[184:187], v[166:169]\n"
      "s_nop 0\n"
      "v_cvt_pk_bf16_f32 v77, v98, v99\n"
      "v_cvt_pk_bf16_f32 v223, v178, v179\n"
      "v_mfma_f32_16x16x32_bf16 v[166:169], a[60:63], v[240:243], v[166:169]\n"
      "s_add_u32 m0, s53, 16384\n"
      "ds_write_b64 v253, v[238:239] offset:6144\n"
      "v_cvt_pk_bf16_f32 v79, v42, v43\n"
      "v_mfma_f32_16x16x32_bf16 v[28:31], a[0:3], v[192:195], 0\n"
      "ds_read_b128 v[236:239], v9 offset:2048\n"
      "v_cvt_pk_bf16_f32 v121, v158, v159\n"
      "v_mfma_f32_16x16x32_bf16 v[28:31], a[4:7], v[200:203], v[28:31]\n"
      "v_mfma_f32_16x16x32_bf16 v[28:31], a[8:11], v[184:187], v[28:31]\n"
      "buffer_load_dwordx4 v1, s[40:43], s76 offen offset:1024 lds\n"
      "v_mfma_f32_16x16x32_bf16 v[28:31], a[12:15], v[240:243], v[28:31]\n"
      "ds_write_b64 v252, v[78:79] offset:256\n"
      "v_mfma_f32_16x16x32_bf16 v[208:211], a[32:35], v[192:195], 0\n"
      "v_cvt_pk_bf16_f32 v76, v96, v97\n"
      "v_mfma_f32_16x16x32_bf16 v[208:211], a[36:39], v[200:203], v[208:211]\n"
      "v_cvt_pk_bf16_f32 v82, v110, v111\n"
      "v_cvt_pk_bf16_f32 v80, v44, v45\n"
      "v_mfma_f32_16x16x32_bf16 v[208:211], a[40:43], v[184:187], v[208:211]\n"
      "v_mfma_f32_16x16x32_bf16 v[208:211], a[44:47], v[240:243], v[208:211]\n"
      "ds_write_b128 v251, v[24:27] offset:2304\n"
      "v_cvt_pk_bf16_f32 v20, v28, v29\n"
      "v_exp_f32 v160, v160\n"
      "v_mfma_f32_16x16x32_bf16 v[156:159], v[192:195], a[96:99], 0\n"
      "v_exp_f32 v163, v163\n"
      "v_mfma_f32_16x16x32_bf16 v[156:159], v[200:203], a[100:103], v[156:159]\n"
      "v_mfma_f32_16x16x32_bf16 v[156:159], v[184:187], a[104:107], v[156:159]\n"
      "v_exp_f32 v161, v161\n"
      "v_mfma_f32_16x16x32_bf16 v[156:159], v[240:243], a[108:111], v[156:159]\n"
      "v_mfma_f32_16x16x32_bf16 v[176:179], a[16:19], v[192:195], 0\n"
      "v_cvt_pk_bf16_f32 v25, v210, v211\n"
      "v_mfma_f32_16x16x32_bf16 v[176:179], a[20:23], v[200:203], v[176:179]\n"
      "v_cvt_pk_bf16_f32 v26, v166, v167\n"
      "v_cvt_pk_bf16_f32 v105, v114, v115\n"
      "v_mfma_f32_16x16x32_bf16 v[176:179], a[24:27], v[184:187], v[176:179]\n"
      "v_mfma_f32_16x16x32_bf16 v[176:179], a[28:31], v[240:243], v[176:179]\n"
      "v_cvt_pk_bf16_f32 v79, v182, v183\n"
      "v_cvt_pk_bf16_f32 v21, v30, v31\n"
      "v_mfma_f32_16x16x32_bf16 v[44:47], v[192:195], a[112:115], 0\n"
      "v_cvt_pk_bf16_f32 v62, v18, v19\n"
      "v_mfma_f32_16x16x32_bf16 v[44:47], v[200:203], a[116:119], v[44:47]\n"
      "s_waitcnt vmcnt(9)\n"
      "ds_read_b128 v[40:43], v0 offset:4096\n"
      "ds_read_b128 v[96:99], v0 offset:5632\n"
      "v_mfma_f32_16x16x32_bf16 v[44:47], v[184:187], a[120:123], v[44:47]\n"
      "v_mfma_f32_16x16x32_bf16 v[44:47], v[240:243], a[124:127], v[44:47]\n"
      "ds_write_b64 v253, v[254:255] offset:2304\n"
      "s_waitcnt lgkmcnt(5)\n"
      "v_smfmac_f32_16x16x64_bf16 v[144:147], v[32:35], v[232:239], v5\n"
      "ds_read_b128 v[108:111], v0 offset:4608\n"
      "v_cvt_pk_bf16_f32 v229, v22, v23\n"
      "v_mfma_f32_16x16x32_bf16 v[196:199], v[192:195], a[80:83], 0\n"
      "ds_read_b128 v[164:167], v0 offset:5120\n"
      "v_cvt_pk_bf16_f32 v24, v208, v209\n"
      "v_mfma_f32_16x16x32_bf16 v[196:199], v[200:203], a[84:87], v[196:199]\n"
      "buffer_load_dwordx4 v1, s[40:43], s76 offen offset:3072 lds\n"
      "v_mfma_f32_16x16x32_bf16 v[196:199], v[184:187], a[88:91], v[196:199]\n"
      "v_cvt_pk_bf16_f32 v22, v176, v177\n"
      "v_mfma_f32_16x16x32_bf16 v[196:199], v[240:243], a[92:95], v[196:199]\n"
      "v_cvt_pk_bf16_f32 v237, v46, v47\n"
      "v_mfma_f32_16x16x32_bf16 v[208:211], v[192:195], a[64:67], 0\n"
      "v_exp_f32 v162, v162\n"
      "v_mfma_f32_16x16x32_bf16 v[208:211], v[200:203], a[68:71], v[208:211]\n"
      "v_cvt_pk_bf16_f32 v176, v160, v161\n"
      "v_cvt_pk_bf16_f32 v23, v178, v179\n"
      "v_mfma_f32_16x16x32_bf16 v[208:211], v[184:187], a[72:75], v[208:211]\n"
      "v_mov_b64 v[192:193], 0\n"
      "v_cvt_pk_bf16_f32 v60, v156, v157\n"
      "v_mfma_f32_16x16x32_bf16 v[208:211], v[240:243], a[76:79], v[208:211]\n"
      "ds_write_b64 v253, v[204:205] offset:6400\n"
      "v_mov_b64 v[28:29], 0\n"
      "v_mfma_f32_16x16x32_bf16 v[232:235], v[48:51], a[80:83], 0\n"
      "v_mfma_f32_16x16x32_bf16 v[232:235], v[216:219], a[84:87], v[232:235]\n"
      "v_cvt_pk_bf16_f32 v61, v158, v159\n"
      "v_cvt_pk_bf16_f32 v177, v162, v163\n"
      "v_mfma_f32_16x16x32_bf16 v[232:235], v[132:135], a[88:91], v[232:235]\n"
      "v_mov_b64 v[202:203], 0\n"
      "v_mfma_f32_16x16x32_bf16 v[232:235], v[224:227], a[92:95], v[232:235]\n"
      "v_cvt_pk_bf16_f32 v115, v58, v59\n"
      "v_add_f32 v144, v144, v145\n"
      "v_smfmac_f32_16x16x64_bf16 v[202:205], v[32:35], v[136:143], v5\n"
      "s_waitcnt lgkmcnt(5)\n"
      "v_mfma_f32_16x16x32_bf16 v[160:163], a[32:35], v[40:43], 0\n"
      "v_cvt_pk_bf16_f32 v179, v154, v155\n"
      "s_waitcnt lgkmcnt(2)\n"
      "v_mfma_f32_16x16x32_bf16 v[160:163], a[36:39], v[108:111], v[160:163]\n"
      "s_waitcnt lgkmcnt(1)\n"
      "v_mfma_f32_16x16x32_bf16 v[160:163], a[40:43], v[164:167], v[160:163]\n"
      "v_cvt_pk_bf16_f32 v27, v168, v169\n"
      "v_mfma_f32_16x16x32_bf16 v[160:163], a[44:47], v[96:99], v[160:163]\n"
      "v_mov_b32 v145, 0\n"
      "v_cvt_pk_bf16_f32 v187, v234, v235\n"
      "v_mfma_f32_16x16x32_bf16 v[252:255], v[40:43], a[112:115], 0\n"
      "v_mfma_f32_16x16x32_bf16 v[252:255], v[108:111], a[116:119], v[252:255]\n"
      "v_add_u32 v10, 64, v10\n"
      "v_mfma_f32_16x16x32_bf16 v[252:255], v[164:167], a[120:123], v[252:255]\n"
      "v_cvt_pk_bf16_f32 v78, v180, v181\n"
      "v_cvt_pk_bf16_f32 v83, v112, v113\n"
      "v_mfma_f32_16x16x32_bf16 v[252:255], v[96:99], a[124:127], v[252:255]\n"
      "v_exp_f32_dpp v145, v144 quad_perm:[0,1,2,3] row_mask:0xf bank_mask:0x1\n"
      "v_mfma_f32_16x16x32_bf16 v[154:157], v[76:79], v[88:91], 0\n"
      "v_cvt_pk_bf16_f32 v239, v86, v87\n"
      "v_mfma_f32_16x16x32_bf16 v[154:157], v[80:83], v[92:95], v[154:157]\n"
      "v_cvt_pk_bf16_f32 v114, v56, v57\n"
      "v_smfmac_f32_16x16x64_bf16 v[192:195], v[32:35], v[64:71], v5\n"
      "buffer_load_dwordx4 v1, s[40:43], s75 offen offset:0 lds\n"
      "v_mfma_f32_16x16x32_bf16 v[168:171], a[48:51], v[40:43], 0\n"
      "v_cvt_pk_bf16_f32 v234, v252, v253\n"
      "v_cvt_pk_bf16_f32 v112, v208, v209\n"
      "v_mfma_f32_16x16x32_bf16 v[168:171], a[52:55], v[108:111], v[168:171]\n"
      "s_nop 0\n"
      "v_exp_f32 v157, v157\n"
      "v_mfma_f32_16x16x32_bf16 v[168:171], a[56:59], v[164:167], v[168:171]\n"
      "v_cvt_pk_bf16_f32 v185, v198, v199\n"
      "v_cvt_pk_bf16_f32 v184, v196, v197\n"
      "v_mfma_f32_16x16x32_bf16 v[168:171], a[60:63], v[96:99], v[168:171]\n"
      "v_mov_b64 v[146:147], 0\n"
      "v_mfma_f32_16x16x32_bf16 v[56:59], v[20:23], v[88:91], 0\n"
      "v_cvt_pk_bf16_f32 v236, v44, v45\n"
      "v_mfma_f32_16x16x32_bf16 v[56:59], v[24:27], v[92:95], v[56:59]\n"
      "ds_write_b128 v251, v[220:223] offset:256\n"
      "v_cvt_pk_bf16_f32 v208, v160, v161\n"
      "v_cvt_pk_bf16_f32 v209, v162, v163\n"
      "v_mfma_f32_16x16x32_bf16 v[160:163], v[40:43], a[80:83], 0\n"
      "v_cvt_pk_bf16_f32 v113, v210, v211\n"
      "v_cvt_pk_bf16_f32 v238, v84, v85\n"
      "v_mfma_f32_16x16x32_bf16 v[160:163], v[108:111], a[84:87], v[160:163]\n"
      "v_cvt_pk_bf16_f32 v210, v168, v169\n"
      "v_cvt_pk_bf16_f32 v235, v254, v255\n"
      "v_mfma_f32_16x16x32_bf16 v[160:163], v[164:167], a[88:91], v[160:163]\n"
      "v_mfma_f32_16x16x32_bf16 v[160:163], v[96:99], a[92:95], v[160:163]\n"
      "v_exp_f32 v56, v56\n"
      "v_mfma_f32_16x16x32_bf16 v[194:197], v[40:43], a[64:67], 0\n"
      "v_exp_f32 v58, v58\n"
      "v_mfma_f32_16x16x32_bf16 v[194:197], v[108:111], a[68:71], v[194:197]\n"
      "v_exp_f32 v59, v59\n"
      "v_mfma_f32_16x16x32_bf16 v[194:197], v[164:167], a[72:75], v[194:197]\n"
      "v_mfma_f32_16x16x32_bf16 v[194:197], v[96:99], a[76:79], v[194:197]\n"
      "v_exp_f32 v154, v154\n"
      "v_mfma_f32_16x16x32_bf16 v[84:87], a[0:3], v[40:43], 0\n"
      "v_cvt_pk_bf16_f32 v186, v232, v233\n"
      "v_add_f32 v192, v192, v193\n"
      "v_mfma_f32_16x16x32_bf16 v[84:87], a[4:7], v[108:111], v[84:87]\n"
      "v_add_f32 v202, v202, v203\n"
      "v_mfma_f32_16x16x32_bf16 v[84:87], a[8:11], v[164:167], v[84:87]\n"
      "buffer_load_dwordx4 v1, s[40:43], s75 offen offset:2048 lds\n"
      "v_mfma_f32_16x16x32_bf16 v[84:87], a[12:15], v[96:99], v[84:87]\n"
      "v_cvt_pk_bf16_f32 v178, v152, v153\n"
      "v_smfmac_f32_16x16x64_bf16 v[28:31], v[32:35], v[76:83], v5\n"
      "s_waitcnt vmcnt(8)\n"
      "ds_read_b128 v[44:47], v0 offset:0\n"
      "v_mfma_f32_16x16x32_bf16 v[180:183], v[40:43], a[96:99], 0\n"
      "L_h1_%=:\n"
      "s_cbranch_execz L_hr_%=\n"
      "ds_read_b128 v[248:251], v0 offset:512\n"
      "ds_read_b128 v[252:255], v0 offset:1024\n"
      "v_mfma_f32_16x16x32_bf16 v[180:183], v[108:111], a[100:103], v[180:183]\n"
      "v_exp_f32 v57, v57\n"
      "v_mfma_f32_16x16x32_bf16 v[180:183], v[164:167], a[104:107], v[180:183]\n"
      "ds_read_b128 v[240:243], v0 offset:1536\n"
      "v_cvt_pk_bf16_f32 v211, v170, v171\n"
      "v_mfma_f32_16x16x32_bf16 v[180:183], v[96:99], a[108:111], v[180:183]\n"
      "v_exp_f32 v156, v156\n"
      "v_smfmac_f32_16x16x64_bf16 v[146:149], v[32:35], v[20:27], v5\n"
      "v_add_f32 v28, v28, v29\n"
      "v_mfma_f32_16x16x32_bf16 v[168:171], a[16:19], v[40:43], 0\n"
      "v_mfma_f32_16x16x32_bf16 v[168:171], a[20:23], v[108:111], v[168:171]\n"
      "v_exp_f32 v202, v202\n"
      "v_mfma_f32_16x16x32_bf16 v[168:171], a[24:27], v[164:167], v[168:171]\n"
      "v_exp_f32 v28, v28\n"
      "v_mfma_f32_16x16x32_bf16 v[168:171], a[28:31], v[96:99], v[168:171]\n"
      "v_cvt_pk_bf16_f32 v99, v156, v157\n"
      "s_waitcnt lgkmcnt(3)\n"
      "v_mfma_f32_16x16x32_bf16 v[108:111], v[44:47], a[112:115], 0\n"
      "s_waitcnt lgkmcnt(2)\n"
      "v_mfma_f32_16x16x32_bf16 v[108:111], v[248:251], a[116:119], v[108:111]\n"
      "v_cvt_pk_bf16_f32 v97, v58, v59\n"
      "s_waitcnt lgkmcnt(1)\n"
      "v_mfma_f32_16x16x32_bf16 v[108:111], v[252:255], a[120:123], v[108:111]\n"
      "s_waitcnt lgkmcnt(0)\n"
      "v_mfma_f32_16x16x32_bf16 v[108:111], v[240:243], a[124:127], v[108:111]\n"
      "v_cvt_pk_bf16_f32 v204, v84, v85\n"
      "v_cvt_pk_bf16_f32 v205, v86, v87\n"
      "v_mfma_f32_16x16x32_bf16 v[40:43], a[32:35], v[44:47], 0\n"
      "v_mfma_f32_16x16x32_bf16 v[40:43], a[36:39], v[248:251], v[40:43]\n"
      "v_exp_f32 v192, v192\n"
      "v_mfma_f32_16x16x32_bf16 v[40:43], a[40:43], v[252:255], v[40:43]\n"
      "v_mfma_f32_16x16x32_bf16 v[40:43], a[44:47], v[240:243], v[40:43]\n"
      "v_cvt_pk_bf16_f32 v206, v168, v169\n"
      "v_cvt_pk_bf16_f32 v207, v170, v171\n"
      "v_mfma_f32_16x16x32_bf16 v[164:167], a[48:51], v[44:47], 0\n"
      "s_add_u32 m0, s53, 12288\n"
      "v_exp_f32 v155, v155\n"
      "v_mfma_f32_16x16x32_bf16 v[164:167], a[52:55], v[248:251], v[164:167]\n"
      "s_nop 1\n"
      "v_cvt_pk_bf16_f32 v169, v42, v43\n"
      "v_mfma_f32_16x16x32_bf16 v[164:167], a[56:59], v[252:255], v[164:167]\n"
      "v_cvt_pk_bf16_f32 v98, v154, v155\n"
      "v_cvt_pk_bf16_f32 v232, v108, v109\n"
      "v_mfma_f32_16x16x32_bf16 v[164:167], a[60:63], v[240:243], v[164:167]\n"
      "v_cvt_pk_bf16_f32 v96, v56, v57\n"
      "v_cvt_pk_bf16_f32 v233, v110, v111\n"
      "v_mfma_f32_16x16x32_bf16 v[156:159], a[16:19], v[44:47], 0\n"
      "buffer_load_dwordx4 v1, s[40:43], s77 offen offset:0 lds\n"
      "v_mfma_f32_16x16x32_bf16 v[156:159], a[20:23], v[248:251], v[156:159]\n"
      "v_mov_b32_dpp v144, v145 quad_perm:[1,1,3,3] row_mask:0xf bank_mask:0xf\n"
      "v_cvt_pk_bf16_f32 v111, v196, v197\n"
      "v_mfma_f32_16x16x32_bf16 v[156:159], a[24:27], v[252:255], v[156:159]\n"
      "v_cvt_pk_bf16_f32 v59, v182, v183\n"
      "v_mfma_f32_16x16x32_bf16 v[156:159], a[28:31], v[240:243], v[156:159]\n"
      "v_cvt_pk_bf16_f32 v168, v40, v41\n"
      "v_cvt_pk_bf16_f32 v110, v194, v195\n"
      "v_mfma_f32_16x16x32_bf16 v[196:199], a[0:3], v[44:47], 0\n"
      "v_cvt_pk_bf16_f32 v58, v180, v181\n"
      "v_mfma_f32_16x16x32_bf16 v[196:199], a[4:7], v[248:251], v[196:199]\n"
      "v_mfma_f32_16x16x32_bf16 v[196:199], a[8:11], v[252:255], v[196:199]\n"
      "v_cvt_pk_bf16_f32 v183, v162, v163\n"
      "v_cvt_pk_bf16_f32 v182, v160, v161\n"
      "v_mfma_f32_16x16x32_bf16 v[196:199], a[12:15], v[240:243], v[196:199]\n"
      "v_cvt_pk_bf16_f32 v171, v166, v167\n"
      "v_cvt_pk_bf16_f32 v166, v156, v157\n"
      "v_mfma_f32_16x16x32_bf16 v[160:163], v[44:47], a[64:67], 0\n"
      "v_cvt_pk_bf16_f32 v167, v158, v159\n"
      "v_cvt_pk_bf16_f32 v170, v164, v165\n"
      "v_mfma_f32_16x16x32_bf16 v[160:163], v[248:251], a[68:71], v[160:163]\n"
      "v_mov_b64 v[156:157], 0\n"
      "v_cvt_pk_bf16_f32 v144, v145, v144\n"
      "v_mfma_f32_16x16x32_bf16 v[160:163], v[252:255], a[72:75], v[160:163]\n"
      "v_cndmask_b32_dpp v193, v202, v192, vcc quad_perm:[0,0,2,2] row_mask:0xf bank_mask:0xf\n"
      "v_cvt_pk_bf16_f32 v164, v196, v197\n"
      "v_mfma_f32_16x16x32_bf16 v[160:163], v[240:243], a[76:79], v[160:163]\n"
      "v_mov_b64 v[84:85], 0\n"
      "v_cvt_pk_bf16_f32 v165, v198, v199\n"
      "v_smfmac_f32_16x16x64_bf16 v[156:159], v[32:35], v[204:211], v5\n"
      "ds_read2_b32 v[222:223], v16 offset0:128 offset1:130\n"
      "ds_read2_b32 v[220:221], v16 offset0:0 offset1:2\n"
      "v_smfmac_f32_16x16x64_bf16 v[84:87], v[32:35], v[164:171], v5\n"
      "s_nop 1\n"
      "v_cvt_pk_bf16_f32 v108, v160, v161\n"
      "v_mfma_f32_16x16x32_bf16 v[196:199], v[44:47], a[80:83], 0\n"
      "v_add_f32 v146, v146, v147\n"
      "v_cvt_pk_bf16_f32 v109, v162, v163\n"
      "v_mfma_f32_16x16x32_bf16 v[196:199], v[248:251], a[84:87], v[196:199]\n"
      "v_add_f32 v156, v156, v157\n"
      "v_add_f32 v84, v84, v85\n"
      "v_mfma_f32_16x16x32_bf16 v[196:199], v[252:255], a[88:91], v[196:199]\n"
      "v_exp_f32 v146, v146\n"
      "v_mfma_f32_16x16x32_bf16 v[196:199], v[240:243], a[92:95], v[196:199]\n"
      "buffer_load_dwordx4 v1, s[40:43], s77 offen offset:2048 lds\n"
      "v_mfma_f32_16x16x32_bf16 v[158:161], v[44:47], a[96:99], 0\n"
      "v_exp_f32 v84, v84\n"
      "v_mfma_f32_16x16x32_bf16 v[158:161], v[248:251], a[100:103], v[158:161]\n"
      "v_exp_f32 v156, v156\n"
      "v_mfma_f32_16x16x32_bf16 v[158:161], v[252:255], a[104:107], v[158:161]\n"
      "s_nop 0\n"
      "v_cndmask_b32_dpp v157, v28, v156, vcc quad_perm:[0,0,2,2] row_mask:0xf bank_mask:0xf\n"
      "v_cndmask_b32_dpp v85, v146, v84, vcc quad_perm:[0,0,2,2] row_mask:0xf bank_mask:0xf\n"
      "v_mfma_f32_16x16x32_bf16 v[158:161], v[240:243], a[108:111], v[158:161]\n"
      "s_not_b64 vcc, vcc\n"
      "v_cndmask_b32_dpp v203, v192, v202, vcc quad_perm:[1,1,3,3] row_mask:0xf bank_mask:0xf\n"
      "v_mov_b64 v[248:249], 0\n"
      "v_cndmask_b32_dpp v147, v84, v146, vcc quad_perm:[1,1,3,3] row_mask:0xf bank_mask:0xf\n"
      "v_cvt_pk_bf16_f32 v193, v193, v203\n"
      "v_mfma_f32_16x16x32_bf16 v[148:151], v[164:167], v[88:91], 0\n"
      "v_cndmask_b32_dpp v29, v156, v28, vcc quad_perm:[1,1,3,3] row_mask:0xf bank_mask:0xf\n"
      "v_cvt_pk_bf16_f32 v180, v196, v197\n"
      "v_mfma_f32_16x16x32_bf16 v[148:151], v[168:171], v[92:95], v[148:151]\n"
      "s_waitcnt lgkmcnt(0)\n"
      "v_mov_b32_dpp v44, v220 row_ror:8 row_mask:0xf bank_mask:0xf\n"
      "v_add_f32 v192, v192, v202\n"
      "v_mov_b64 v[250:251], 0\n"
      "v_mfma_f32_16x16x32_bf16 v[240:243], v[164:167], v[100:103], 0\n"
      "v_add_f32 v84, v84, v156\n"
      "v_cvt_pk_bf16_f32 v157, v157, v29\n"
      "v_mfma_f32_16x16x32_bf16 v[240:243], v[168:171], v[104:107], v[240:243]\n"
      "v_add_f32 v192, v192, v145\n"
      "v_mov_b32_dpp v45, v221 row_ror:8 row_mask:0xf bank_mask:0xf\n"
      "v_mov_b32_dpp v47, v223 row_ror:8 row_mask:0xf bank_mask:0xf\n"
      "v_mfma_f32_16x16x32_bf16 v[152:155], v[204:207], v[88:91], 0\n"
      "v_cvt_pk_bf16_f32 v56, v158, v159\n"
      "v_add_f32 v146, v146, v28\n"
      "v_mfma_f32_16x16x32_bf16 v[152:155], v[208:211], v[92:95], v[152:155]\n"
      "v_cvt_pk_bf16_f32 v85, v85, v147\n"
      "v_exp_f32 v242, v242\n"
      "v_add_f32 v84, v84, v146\n"
      "v_mov_b32_dpp v46, v222 row_ror:8 row_mask:0xf bank_mask:0xf\n"
      "v_smfmac_f32_16x16x64_bf16 v[248:251], v[220:223], v[88:95], v5\n"
      "ds_bpermute_b32 v43, v6, v157 offset:16\n"
      "v_smfmac_f32_16x16x64_bf16 v[248:251], v[44:47], v[100:107], v5\n"
      "ds_bpermute_b32 v255, v7, v193 offset:4\n"
      "v_exp_f32 v240, v240\n"
      "v_mfma_f32_16x16x32_bf16 v[32:35], v[76:79], v[100:103], 0\n"
      "v_exp_f32 v243, v243\n"
      "v_mfma_f32_16x16x32_bf16 v[32:35], v[80:83], v[104:107], v[32:35]\n"
      "v_exp_f32 v148, v148\n"
      "v_mfma_f32_16x16x32_bf16 v[200:203], v[204:207], v[100:103], 0\n"
      "ds_bpermute_b32 v41, v6, v157\n"
      "v_mfma_f32_16x16x32_bf16 v[200:203], v[208:211], v[104:107], v[200:203]\n"
      "ds_bpermute_b32 v254, v7, v193\n"
      "v_add_f32 v90, v84, v192\n"
      "v_mfma_f32_16x16x32_bf16 v[156:159], v[136:139], v[100:103], 0\n"
      "s_nop 0\n"
      "v_exp_f32 v35, v35\n"
      "v_mfma_f32_16x16x32_bf16 v[156:159], v[140:143], v[104:107], v[156:159]\n"
      "v_exp_f32 v34, v34\n"
      "v_mfma_f32_16x16x32_bf16 v[192:195], v[20:23], v[100:103], 0\n"
      "ds_bpermute_b32 v40, v6, v85\n"
      "v_mfma_f32_16x16x32_bf16 v[192:195], v[24:27], v[104:107], v[192:195]\n"
      "v_add_f32 v250, v250, v251\n"
      "v_exp_f32 v201, v201\n"
      "v_exp_f32 v149, v149\n"
      "v_cvt_pk_bf16_f32 v57, v160, v161\n"
      "v_mfma_f32_16x16x32_bf16 v[92:95], a[128:131], v[48:51], 0\n"
      "ds_bpermute_b32 v42, v6, v85 offset:16\n"
      "v_mfma_f32_16x16x32_bf16 v[92:95], a[132:135], v[216:219], v[92:95]\n"
      "v_exp_f32 v158, v158\n"
      "v_mfma_f32_16x16x32_bf16 v[92:95], a[136:139], v[132:135], v[92:95]\n"
      "v_exp_f32 v150, v150\n"
      "v_mfma_f32_16x16x32_bf16 v[92:95], a[140:143], v[224:227], v[92:95]\n"
      "v_exp_f32 v192, v192\n"
      "v_exp_f32 v155, v155\n"
      "v_exp_f32 v153, v153\n"
      "v_add_f32 v248, v248, v249\n"
      "v_exp_f32 v248, v248\n"
      "v_exp_f32 v250, v250\n"
      "v_mfma_f32_16x16x32_bf16 v[86:89], v[64:67], v[100:103], 0\n"
      "v_exp_f32 v152, v152\n"
      "v_mfma_f32_16x16x32_bf16 v[86:89], v[68:71], v[104:107], v[86:89]\n"
      "v_exp_f32 v195, v195\n"
      "v_exp_f32 v154, v154\n"
      "v_mfma_f32_16x16x32_bf16 v[100:103], a[144:147], v[48:51], 0\n"
      "v_exp_f32 v157, v157\n"
      "v_mfma_f32_16x16x32_bf16 v[100:103], a[148:151], v[216:219], v[100:103]\n"
      "v_exp_f32 v241, v241\n"
      "v_mfma_f32_16x16x32_bf16 v[100:103], a[152:155], v[132:135], v[100:103]\n"
      "v_exp_f32 v203, v203\n"
      "v_mfma_f32_16x16x32_bf16 v[100:103], a[156:159], v[224:227], v[100:103]\n"
      "v_cvt_pk_bf16_f32 v181, v198, v199\n"
      "v_exp_f32 v89, v89\n"
      "v_exp_f32 v200, v200\n"
      "v_exp_f32 v194, v194\n"
      "v_exp_f32 v193, v193\n"
      "v_cvt_pk_bf16_f32 v29, v94, v95\n"
      "v_cvt_pk_bf16_f32 v196, v240, v241\n"
      "v_exp_f32 v202, v202\n"
      "v_exp_f32 v33, v33\n"
      "v_cvt_pk_bf16_f32 v31, v102, v103\n"
      "v_cvt_pk_bf16_f32 v197, v242, v243\n"
      "v_exp_f32 v159, v159\n"
      "v_cvt_pk_bf16_f32 v199, v202, v203\n"
      "v_cvt_pk_bf16_f32 v30, v100, v101\n"
      "v_exp_f32 v86, v86\n"
      "v_add_f32 v100, v248, v250\n"
      "v_cvt_pk_bf16_f32 v95, v154, v155\n"
      "v_exp_f32 v151, v151\n"
      "v_cvt_pk_bf16_f32 v198, v200, v201\n"
      "v_cvt_pk_bf16_f32 v28, v92, v93\n"
      "v_cvt_pk_bf16_f32 v162, v248, v250\n"
      "v_cvt_pk_bf16_f32 v200, v192, v193\n"
      "v_cvt_pk_bf16_f32 v94, v152, v153\n"
      "v_cvt_pk_bf16_f32 v203, v34, v35\n"
      "v_cvt_pk_bf16_f32 v163, v248, v250\n"
      "v_mfma_f32_16x16x32_bf16 v[240:243], a[176:179], v[48:51], 0\n"
      "v_cvt_pk_bf16_f32 v201, v194, v195\n"
      "v_add_f32 v101, v248, v250\n"
      "v_mfma_f32_16x16x32_bf16 v[240:243], a[180:183], v[216:219], v[240:243]\n"
      "v_permlane16_swap_b32 v162, v163\n"
      "v_mfma_f32_16x16x32_bf16 v[240:243], a[184:187], v[132:135], v[240:243]\n"
      "v_exp_f32 v88, v88\n"
      "v_mfma_f32_16x16x32_bf16 v[240:243], a[188:191], v[224:227], v[240:243]\n"
      "ds_read_b64 v[18:19], v11 offset:0\n"
      "v_permlane16_swap_b32 v100, v101\n"
      "v_mov_b64 v[248:249], v[100:101]\n"
      "v_mfma_f32_16x16x32_bf16 v[152:155], a[160:163], v[48:51], 0\n"
      "v_exp_f32 v32, v32\n"
      "v_mfma_f32_16x16x32_bf16 v[152:155], a[164:167], v[216:219], v[152:155]\n"
      "v_cvt_pk_bf16_f32 v202, v32, v33\n"
      "v_cvt_pk_bf16_f32 v193, v88, v89\n"
      "v_mfma_f32_16x16x32_bf16 v[152:155], a[168:171], v[132:135], v[152:155]\n"
      "v_exp_f32 v156, v156\n"
      "v_mfma_f32_16x16x32_bf16 v[152:155], a[172:175], v[224:227], v[152:155]\n"
      "v_cvt_pk_bf16_f32 v92, v148, v149\n"
      "v_cvt_pk_bf16_f32 v93, v150, v151\n"
      "v_cvt_pk_bf16_f32 v194, v156, v157\n"
      "v_smfmac_f32_16x16x64_bf16 v[248:251], v[12:15], v[196:203], v5\n"
      "ds_bpermute_b32 v253, v7, v144 offset:8\n"
      "v_smfmac_f32_16x16x64_bf16 v[100:103], v[12:15], v[92:99], v5\n"
      "ds_bpermute_b32 v252, v7, v144 offset:248\n"
      "v_cvt_pk_bf16_f32 v195, v158, v159\n"
      "v_cvt_pk_bf16_f32 v35, v242, v243\n"
      "v_exp_f32 v87, v87\n"
      "v_cvt_pk_bf16_f32 v32, v152, v153\n"
      "v_cvt_pk_bf16_f32 v33, v154, v155\n"
      "v_smfmac_f32_16x16x32_bf16 v[100:103], v[12:13], v[176:179], v5\n"
      "v_cvt_pk_bf16_f32 v192, v86, v87\n"
      "v_cvt_pk_bf16_f32 v34, v240, v241\n"
      "s_waitcnt lgkmcnt(2)\n"
      "v_mfma_f32_16x16x4_4b_bf16 v[146:161], v[162:163], v[18:19], 0 cbsz:2 abid:0\n"
      "s_nop 3\n"
      "v_add_f32 v100, v100, v101\n"
      "v_rcp_f32 v101, v100\n"
      "v_smfmac_f32_16x16x32_bf16 v[248:251], v[12:13], v[192:195], v5\n"
      "ds_read_b64 v[88:89], v11 offset:4096\n"
      "ds_bpermute_b32 v102, v4, v101\n"
      "s_nop 0\n"
      "v_mfma_f32_16x16x32_bf16 v[150:153], v[92:95], v[180:183], v[150:153]\n"
      "v_mfma_f32_16x16x32_bf16 v[150:153], v[96:99], v[184:187], v[150:153]\n"
      "v_add_f32_dpp v90, v90, v90 row_ror:8 row_mask:0xf bank_mask:0xf\n"
      "v_mfma_f32_16x16x32_bf16 v[150:153], v[176:179], v[120:123], v[150:153]\n"
      "s_nop 0\n"
      "v_add_f32_dpp v90, v90, v90 row_ror:4 row_mask:0xf bank_mask:0xf\n"
      "v_mfma_f32_16x16x32_bf16 v[146:149], v[92:95], v[108:111], v[146:149]\n"
      "buffer_load_dwordx4 v1, s[40:43], s78 offen offset:1024 lds\n"
      "v_mfma_f32_16x16x32_bf16 v[146:149], v[96:99], v[112:115], v[146:149]\n"
      "v_add_f32_dpp v90, v90, v90 row_ror:2 row_mask:0xf bank_mask:0xf\n"
      "v_mfma_f32_16x16x32_bf16 v[146:149], v[176:179], v[52:55], v[146:149]\n"
      "s_nop 0\n"
      "v_add_f32_dpp v90, v90, v90 row_ror:1 row_mask:0xf bank_mask:0xf\n"
      "v_mfma_f32_16x16x32_bf16 v[158:161], v[92:95], v[232:235], v[158:161]\n"
      "v_rcp_f32 v91, v90\n"
      "v_mfma_f32_16x16x32_bf16 v[158:161], v[96:99], v[236:239], v[158:161]\n"
      "v_mfma_f32_16x16x32_bf16 v[158:161], v[176:179], v[228:231], v[158:161]\n"
      "s_waitcnt lgkmcnt(0)\n"
      "s_nop 0\n"
      "v_mul_f32_dpp v149, v102, v149 row_newbcast:12 row_mask:0xf bank_mask:0xf\n"
      "v_mfma_f32_16x16x32_bf16 v[154:157], v[92:95], v[56:59], v[154:157]\n"
      "v_mul_f32_dpp v153, v102, v153 row_newbcast:12 row_mask:0xf bank_mask:0xf\n"
      "v_mul_f32_dpp v150, v102, v150 row_newbcast:0 row_mask:0xf bank_mask:0xf\n"
      "v_mfma_f32_16x16x32_bf16 v[154:157], v[96:99], v[60:63], v[154:157]\n"
      "v_mul_f32_dpp v151, v102, v151 row_newbcast:4 row_mask:0xf bank_mask:0xf\n"
      "v_mul_f32_dpp v152, v102, v152 row_newbcast:8 row_mask:0xf bank_mask:0xf\n"
      "v_mfma_f32_16x16x32_bf16 v[154:157], v[176:179], v[116:119], v[154:157]\n"
      "v_add_u32 v11, 0x200, v11\n"
      "v_mul_f32_dpp v146, v102, v146 row_newbcast:0 row_mask:0xf bank_mask:0xf\n"
      "v_mfma_f32_16x16x32_bf16 v[150:153], v[212:215], a[208:211], v[150:153]\n"
      "v_mul_f32_dpp v148, v102, v148 row_newbcast:8 row_mask:0xf bank_mask:0xf\n"
      "v_mul_f32_dpp v147, v102, v147 row_newbcast:4 row_mask:0xf bank_mask:0xf\n"
      "v_mfma_f32_16x16x32_bf16 v[150:153], v[124:127], a[212:215], v[150:153]\n"
      "v_mfma_f32_16x16x32_bf16 v[150:153], v[188:191], a[216:219], v[150:153]\n"
      "v_maximum3_f32 v8, v8, v90, v91\n"
      "v_mul_f32_dpp v161, v102, v161 row_newbcast:12 row_mask:0xf bank_mask:0xf\n"
      "v_mfma_f32_16x16x32_bf16 v[150:153], v[244:247], a[220:223], v[150:153]\n"
      "v_mul_f32_dpp v155, v102, v155 row_newbcast:4 row_mask:0xf bank_mask:0xf\n"
      "v_mul_f32_dpp v156, v102, v156 row_newbcast:8 row_mask:0xf bank_mask:0xf\n"
      "v_mfma_f32_16x16x32_bf16 v[146:149], v[212:215], a[192:195], v[146:149]\n"
      "v_maximum3_f32 v8, v8, v100, v101\n"
      "v_mul_f32_dpp v157, v102, v157 row_newbcast:12 row_mask:0xf bank_mask:0xf\n"
      "v_mfma_f32_16x16x32_bf16 v[146:149], v[124:127], a[196:199], v[146:149]\n"
      "v_mul_f32_dpp v154, v102, v154 row_newbcast:0 row_mask:0xf bank_mask:0xf\n"
      "v_mfma_f32_16x16x32_bf16 v[146:149], v[188:191], a[200:203], v[146:149]\n"
      "v_mul_f32_dpp v159, v102, v159 row_newbcast:4 row_mask:0xf bank_mask:0xf\n"
      "v_mul_f32_dpp v160, v102, v160 row_newbcast:8 row_mask:0xf bank_mask:0xf\n"
      "v_mfma_f32_16x16x32_bf16 v[146:149], v[244:247], a[204:207], v[146:149]\n"
      "v_mul_f32_dpp v158, v102, v158 row_newbcast:0 row_mask:0xf bank_mask:0xf\n"
      "v_add_f32 v248, v248, v249\n"
      "v_mfma_f32_16x16x32_bf16 v[154:157], v[212:215], a[224:227], v[154:157]\n"
      "v_rcp_f32 v249, v248\n"
      "v_mfma_f32_16x16x32_bf16 v[154:157], v[124:127], a[228:231], v[154:157]\n"
      "v_mfma_f32_16x16x32_bf16 v[154:157], v[188:191], a[232:235], v[154:157]\n"
      "ds_bpermute_b32 v250, v4, v249 offset:128\n"
      "v_mfma_f32_16x16x32_bf16 v[154:157], v[244:247], a[236:239], v[154:157]\n"
      "v_maximum3_f32 v8, v8, v248, v249\n"
      "v_cvt_pk_bf16_f32 v146, v146, v150\n"
      "v_mfma_f32_16x16x4_4b_bf16 v[92:107], v[162:163], v[18:19], 0 cbsz:2 abid:2\n"
      "v_cvt_pk_bf16_f32 v150, v147, v151\n"
      "v_pk_max_i16 v150, v150, 0\n"
      "buffer_load_dwordx4 v1, s[40:43], s78 offen offset:3072 lds\n"
      "v_cvt_pk_bf16_f32 v148, v148, v152\n"
      "v_mfma_f32_16x16x32_bf16 v[158:161], v[212:215], a[240:243], v[158:161]\n"
      "v_mfma_f32_16x16x32_bf16 v[158:161], v[124:127], a[244:247], v[158:161]\n"
      "v_mfma_f32_16x16x32_bf16 v[158:161], v[188:191], a[248:251], v[158:161]\n"
      "v_mfma_f32_16x16x32_bf16 v[158:161], v[244:247], a[252:255], v[158:161]\n"
      "v_cvt_pk_bf16_f32 v152, v149, v153\n"
      "v_pk_max_i16 v146, v146, 0\n"
      "v_mfma_f32_16x16x32_bf16 v[92:95], v[196:199], v[108:111], v[92:95]\n"
      "v_mfma_f32_16x16x32_bf16 v[92:95], v[200:203], v[112:115], v[92:95]\n"
      "v_and_b32 v213, 0xffff0000, v88\n"
      "v_add_u32 v16, 64, v16\n"
      "v_mfma_f32_16x16x32_bf16 v[92:95], v[192:195], v[52:55], v[92:95]\n"
      "s_nop 0\n"
      "v_cvt_pk_bf16_f32 v153, v157, v161\n"
      "v_pk_max_i16 v152, v152, 0\n"
      "v_mfma_f32_16x16x32_bf16 v[100:103], v[196:199], v[56:59], v[100:103]\n"
      "v_cvt_pk_bf16_f32 v149, v156, v160\n"
      "v_pk_max_i16 v149, v149, 0\n"
      "v_mfma_f32_16x16x32_bf16 v[100:103], v[200:203], v[60:63], v[100:103]\n"
      "v_cvt_pk_bf16_f32 v147, v154, v158\n"
      "v_mfma_f32_16x16x32_bf16 v[100:103], v[192:195], v[116:119], v[100:103]\n"
      "ds_read_b128 v[240:243], v0 offset:9216\n"
      "ds_read_b128 v[160:163], v0 offset:8192\n"
      "v_mfma_f32_16x16x32_bf16 v[104:107], v[196:199], v[232:235], v[104:107]\n"
      "s_add_u32 m0, s53, 8192\n"
      "ds_read_b128 v[124:127], v0 offset:9728\n"
      "v_cvt_pk_bf16_f32 v151, v155, v159\n"
      "v_mfma_f32_16x16x32_bf16 v[104:107], v[200:203], v[236:239], v[104:107]\n"
      "s_waitcnt lgkmcnt(3)\n"
      "v_mul_f32_dpp v95, v250, v95 row_newbcast:12 row_mask:0xf bank_mask:0xf\n"
      "v_mul_f32_dpp v92, v250, v92 row_newbcast:0 row_mask:0xf bank_mask:0xf\n"
      "v_mfma_f32_16x16x32_bf16 v[104:107], v[192:195], v[228:231], v[104:107]\n"
      "ds_read_b128 v[84:87], v0 offset:8704\n"
      "v_pk_max_i16 v148, v148, 0\n"
      "v_mfma_f32_16x16x32_bf16 v[96:99], v[196:199], v[180:183], v[96:99]\n"
      "v_mfma_f32_16x16x32_bf16 v[96:99], v[200:203], v[184:187], v[96:99]\n"
      "v_pk_max_i16 v151, v151, 0\n"
      "v_pk_max_i16 v153, v153, 0\n"
      "v_mfma_f32_16x16x32_bf16 v[96:99], v[192:195], v[120:123], v[96:99]\n"
      "v_and_b32 v215, 0xffff0000, v89\n"
      "v_lshlrev_b32 v212, 16, v88\n"
      "v_mfma_f32_16x16x4_4b_bf16 v[188:203], v[252:253], v[18:19], 0 cbsz:2 abid:0\n"
      "buffer_store_dwordx2 v[152:153], v2, s[44:47], s73 offen offset:768 nt sc1\n"
      "v_mul_f32_dpp v103, v250, v103 row_newbcast:12 row_mask:0xf bank_mask:0xf\n"
      "v_mul_f32_dpp v93, v250, v93 row_newbcast:4 row_mask:0xf bank_mask:0xf\n"
      "v_mul_f32_dpp v101, v250, v101 row_newbcast:4 row_mask:0xf bank_mask:0xf\n"
      "v_mul_f32_dpp v100, v250, v100 row_newbcast:0 row_mask:0xf bank_mask:0xf\n"
      "v_mfma_f32_16x16x32_bf16 v[176:179], v[20:23], v[28:31], 0\n"
      "v_mul_f32_dpp v96, v250, v96 row_newbcast:0 row_mask:0xf bank_mask:0xf\n"
      "v_mul_f32_dpp v106, v250, v106 row_newbcast:8 row_mask:0xf bank_mask:0xf\n"
      "v_mfma_f32_16x16x32_bf16 v[176:179], v[24:27], v[32:35], v[176:179]\n"
      "v_mul_f32_dpp v98, v250, v98 row_newbcast:8 row_mask:0xf bank_mask:0xf\n"
      "v_mul_f32_dpp v99, v250, v99 row_newbcast:12 row_mask:0xf bank_mask:0xf\n"
      "v_smfmac_f32_16x16x64_bf16 v[196:199], v[40:43], v[56:63], v5\n"
      "v_smfmac_f32_16x16x64_bf16 v[192:195], v[40:43], v[180:187], v5\n"
      "s_waitcnt lgkmcnt(0)\n"
      "buffer_load_dwordx4 v1, s[40:43], s91 offen offset:3072 lds\n"
      "v_smfmac_f32_16x16x64_bf16 v[188:191], v[40:43], v[108:115], v5\n"
      "v_mul_f32_dpp v94, v250, v94 row_newbcast:8 row_mask:0xf bank_mask:0xf\n"
      "v_smfmac_f32_16x16x64_bf16 v[200:203], v[40:43], v[232:239], v5\n"
      "v_mul_f32_dpp v107, v250, v107 row_newbcast:12 row_mask:0xf bank_mask:0xf\n"
      "v_mfma_f32_16x16x32_bf16 v[244:247], v[204:207], v[28:31], 0\n"
      "v_mul_f32_dpp v102, v250, v102 row_newbcast:8 row_mask:0xf bank_mask:0xf\n"
      "v_mul_f32_dpp v97, v250, v97 row_newbcast:4 row_mask:0xf bank_mask:0xf\n"
      "v_mfma_f32_16x16x32_bf16 v[244:247], v[208:211], v[32:35], v[244:247]\n"
      "v_lshlrev_b32 v214, 16, v89\n"
      "v_mul_f32_dpp v104, v250, v104 row_newbcast:0 row_mask:0xf bank_mask:0xf\n"
      "v_smfmac_f32_16x16x32_bf16 v[200:203], v[254:255], v[228:231], v5\n"
      "v_mul_f32_dpp v105, v250, v105 row_newbcast:4 row_mask:0xf bank_mask:0xf\n"
      "v_smfmac_f32_16x16x32_bf16 v[196:199], v[254:255], v[116:119], v5\n"
      "v_pk_max_i16 v147, v147, 0\n"
      "v_smfmac_f32_16x16x32_bf16 v[188:191], v[254:255], v[52:55], v5\n"
      "buffer_load_dwordx4 v1, s[40:43], s91 offen offset:1024 lds\n"
      "v_smfmac_f32_16x16x32_bf16 v[192:195], v[254:255], v[120:123], v5\n"
      "buffer_store_dwordx2 v[146:147], v2, s[44:47], s73 offen offset:0 nt sc1\n"
      "v_mfma_f32_16x16x32_bf16 v[152:155], v[64:67], v[28:31], 0\n"
      "v_exp_f32 v179, v179\n"
      "v_mfma_f32_16x16x32_bf16 v[152:155], v[68:71], v[32:35], v[152:155]\n"
      "v_exp_f32 v246, v246\n"
      "v_mfma_f32_16x16x32_bf16 v[104:107], v[172:175], a[240:243], v[104:107]\n"
      "v_exp_f32 v244, v244\n"
      "v_mfma_f32_16x16x32_bf16 v[104:107], v[72:75], a[244:247], v[104:107]\n"
      "v_add_f32 v188, v188, v189\n"
      "v_add_f32 v196, v196, v197\n"
      "v_mfma_f32_16x16x32_bf16 v[104:107], v[128:131], a[248:251], v[104:107]\n"
      "v_add_f32 v192, v192, v193\n"
      "v_fma_f32 v192, v192, v91, v213\n"
      "v_mfma_f32_16x16x32_bf16 v[104:107], v[36:39], a[252:255], v[104:107]\n"
      "v_exp_f32 v177, v177\n"
      "v_mfma_f32_16x16x32_bf16 v[252:255], v[136:139], v[28:31], 0\n"
      "v_fma_f32 v188, v188, v91, v212\n"
      "v_fma_f32 v196, v196, v91, v214\n"
      "v_mfma_f32_16x16x32_bf16 v[252:255], v[140:143], v[32:35], v[252:255]\n"
      "v_exp_f32 v152, v152\n"
      "v_mfma_f32_16x16x32_bf16 v[96:99], v[172:175], a[208:211], v[96:99]\n"
      "v_add_f32 v200, v200, v201\n"
      "v_fma_f32 v200, v200, v91, v215\n"
      "v_mfma_f32_16x16x32_bf16 v[96:99], v[72:75], a[212:215], v[96:99]\n"
      "v_cvt_pk_bf16_f32 v189, v196, v200\n"
      "v_cvt_pk_bf16_f32 v188, v188, v192\n"
      "v_mfma_f32_16x16x32_bf16 v[96:99], v[128:131], a[216:219], v[96:99]\n"
      "v_exp_f32 v255, v255\n"
      "v_mfma_f32_16x16x32_bf16 v[96:99], v[36:39], a[220:223], v[96:99]\n"
      "v_exp_f32 v155, v155\n"
      "v_mfma_f32_16x16x32_bf16 v[88:91], a[144:147], v[160:163], 0\n"
      "v_exp_f32 v247, v247\n"
      "v_mfma_f32_16x16x32_bf16 v[88:91], a[148:151], v[84:87], v[88:91]\n"
      "v_exp_f32 v245, v245\n"
      "v_mfma_f32_16x16x32_bf16 v[88:91], a[152:155], v[240:243], v[88:91]\n"
      "v_exp_f32 v153, v153\n"
      "v_mfma_f32_16x16x32_bf16 v[88:91], a[156:159], v[124:127], v[88:91]\n"
      "v_exp_f32 v176, v176\n"
      "v_mfma_f32_16x16x32_bf16 v[156:159], v[164:167], v[28:31], 0\n"
      "v_exp_f32 v178, v178\n"
      "v_mfma_f32_16x16x32_bf16 v[156:159], v[168:171], v[32:35], v[156:159]\n"
      "v_cvt_pk_bf16_f32 v196, v176, v177\n"
      "v_mfma_f32_16x16x32_bf16 v[40:43], v[76:79], v[28:31], 0\n"
      "v_cvt_pk_bf16_f32 v197, v178, v179\n"
      "v_cvt_pk_bf16_f32 v194, v244, v245\n"
      "v_mfma_f32_16x16x32_bf16 v[40:43], v[80:83], v[32:35], v[40:43]\n"
      "buffer_store_dwordx2 v[148:149], v2, s[44:47], s73 offen offset:512 nt sc1\n"
      "v_mfma_f32_16x16x32_bf16 v[92:95], v[172:175], a[192:195], v[92:95]\n"
      "v_exp_f32 v252, v252\n"
      "v_mfma_f32_16x16x32_bf16 v[92:95], v[72:75], a[196:199], v[92:95]\n"
      "v_exp_f32 v253, v253\n"
      "v_mfma_f32_16x16x32_bf16 v[92:95], v[128:131], a[200:203], v[92:95]\n"
      "v_exp_f32 v159, v159\n"
      "v_mfma_f32_16x16x32_bf16 v[92:95], v[36:39], a[204:207], v[92:95]\n"
      "v_exp_f32 v43, v43\n"
      "v_mfma_f32_16x16x32_bf16 v[212:215], a[160:163], v[160:163], 0\n"
      "buffer_load_dwordx4 v1, s[40:43], s90 offen offset:2048 lds\n"
      "v_mfma_f32_16x16x32_bf16 v[212:215], a[164:167], v[84:87], v[212:215]\n"
      "v_exp_f32 v42, v42\n"
      "v_mfma_f32_16x16x32_bf16 v[212:215], a[168:171], v[240:243], v[212:215]\n"
      "v_exp_f32 v157, v157\n"
      "v_mfma_f32_16x16x32_bf16 v[212:215], a[172:175], v[124:127], v[212:215]\n"
      "v_exp_f32 v40, v40\n"
      "v_mfma_f32_16x16x32_bf16 v[100:103], v[172:175], a[224:227], v[100:103]\n"
      "v_exp_f32 v254, v254\n"
      "v_mfma_f32_16x16x32_bf16 v[100:103], v[72:75], a[228:231], v[100:103]\n"
      "v_exp_f32 v158, v158\n"
      "v_mfma_f32_16x16x32_bf16 v[100:103], v[128:131], a[232:235], v[100:103]\n"
      "v_exp_f32 v156, v156\n"
      "v_mfma_f32_16x16x32_bf16 v[100:103], v[36:39], a[236:239], v[100:103]\n"
      "v_exp_f32 v41, v41\n"
      "v_mfma_f32_16x16x32_bf16 v[172:175], a[128:131], v[160:163], 0\n"
      "v_cvt_pk_bf16_f32 v192, v156, v157\n"
      "v_mov_b64 v[128:129], 0\n"
      "v_mfma_f32_16x16x32_bf16 v[172:175], a[132:135], v[84:87], v[172:175]\n"
      "v_cvt_pk_bf16_f32 v176, v152, v153\n"
      "v_cvt_pk_bf16_f32 v178, v252, v253\n"
      "v_mfma_f32_16x16x32_bf16 v[172:175], a[136:139], v[240:243], v[172:175]\n"
      "v_mov_b64 v[130:131], 0\n"
      "v_cvt_pk_bf16_f32 v198, v40, v41\n"
      "v_mfma_f32_16x16x32_bf16 v[172:175], a[140:143], v[124:127], v[172:175]\n"
      "v_exp_f32 v154, v154\n"
      "v_mfma_f32_16x16x32_bf16 v[72:75], a[176:179], v[160:163], 0\n"
      "v_cvt_pk_bf16_f32 v195, v246, v247\n"
      "v_cvt_pk_bf16_f32 v252, v212, v213\n"
      "v_mfma_f32_16x16x32_bf16 v[72:75], a[180:183], v[84:87], v[72:75]\n"
      "v_cvt_pk_bf16_f32 v179, v254, v255\n"
      "v_cvt_pk_bf16_f32 v250, v88, v89\n"
      "v_mfma_f32_16x16x32_bf16 v[72:75], a[184:187], v[240:243], v[72:75]\n"
      "v_cvt_pk_bf16_f32 v249, v174, v175\n"
      "v_cvt_pk_bf16_f32 v251, v90, v91\n"
      "v_mfma_f32_16x16x32_bf16 v[72:75], a[188:191], v[124:127], v[72:75]\n"
      "v_cvt_pk_bf16_f32 v248, v172, v173\n"
      "v_pk_max_i16 v189, v189, 0\n"
      "v_cvt_pk_bf16_f32 v92, v92, v96\n"
      "v_cvt_pk_bf16_f32 v94, v94, v98\n"
      "v_cvt_pk_bf16_f32 v96, v93, v97\n"
      "v_cvt_pk_bf16_f32 v253, v214, v215\n"
      "v_mfma_f32_16x16x32_bf16 v[172:175], v[136:139], v[248:251], 0\n"
      "s_nop 0\n"
      "v_cvt_pk_bf16_f32 v255, v74, v75\n"
      "v_cvt_pk_bf16_f32 v254, v72, v73\n"
      "v_smfmac_f32_16x16x64_bf16 v[128:131], v[220:223], v[28:35], v5\n"
      "buffer_load_dwordx4 v1, s[40:43], s90 offen offset:0 lds\n"
      "v_smfmac_f32_16x16x64_bf16 v[128:131], v[44:47], v[248:255], v5\n"
      "buffer_store_dwordx2 v[150:151], v2, s[44:47], s73 offen offset:256 nt sc1\n"
      "v_pk_max_i16 v188, v188, 0\n"
      "v_cvt_pk_bf16_f32 v199, v42, v43\n"
      "v_mfma_f32_16x16x32_bf16 v[200:203], v[76:79], v[248:251], 0\n"
      "v_pk_max_i16 v92, v92, 0\n"
      "v_cvt_pk_bf16_f32 v177, v154, v155\n"
      "v_mfma_f32_16x16x32_bf16 v[200:203], v[80:83], v[252:255], v[200:203]\n"
      "s_nop 0\n"
      "v_add_f32 v130, v130, v131\n"
      "v_exp_f32 v130, v130\n"
      "v_mfma_f32_16x16x32_bf16 v[172:175], v[140:143], v[252:255], v[172:175]\n"
      "v_add_f32 v128, v128, v129\n"
      "v_exp_f32 v128, v128\n"
      "v_mfma_f32_16x16x32_bf16 v[150:153], v[204:207], v[248:251], 0\n"
      "v_cvt_pk_bf16_f32 v193, v158, v159\n"
      "v_add_f32 v213, v128, v130\n"
      "v_mfma_f32_16x16x32_bf16 v[150:153], v[208:211], v[252:255], v[150:153]\n"
      "v_add_f32 v212, v128, v130\n"
      "v_cvt_pk_bf16_f32 v155, v128, v130\n"
      "v_mfma_f32_16x16x32_bf16 v[244:247], v[64:67], v[248:251], 0\n"
      "v_permlane16_swap_b32 v212, v213\n"
      "v_mfma_f32_16x16x32_bf16 v[244:247], v[68:71], v[252:255], v[244:247]\n"
      "v_cvt_pk_bf16_f32 v154, v128, v130\n"
      "v_mov_b64 v[148:149], v[212:213]\n"
      "v_smfmac_f32_16x16x64_bf16 v[212:215], v[12:15], v[192:199], v5\n"
      "v_cvt_pk_bf16_f32 v98, v95, v99\n"
      "v_mfma_f32_16x16x32_bf16 v[128:131], v[164:167], v[248:251], 0\n"
      "s_add_u32 m0, s53, 4096\n"
      "v_exp_f32 v203, v203\n"
      "v_mfma_f32_16x16x32_bf16 v[128:131], v[168:171], v[252:255], v[128:131]\n"
      "v_permlane16_swap_b32 v154, v155\n"
      "v_smfmac_f32_16x16x32_bf16 v[212:215], v[12:13], v[176:179], v5\n"
      "v_pk_max_i16 v94, v94, 0\n"
      "v_mfma_f32_16x16x4_4b_bf16 v[28:43], v[154:155], v[18:19], 0 cbsz:2 abid:0\n"
      "v_exp_f32 v151, v151\n"
      "v_cvt_pk_bf16_f32 v97, v101, v105\n"
      "v_cvt_pk_bf16_f32 v95, v102, v106\n"
      "v_cvt_pk_bf16_f32 v93, v100, v104\n"
      "v_exp_f32 v130, v130\n"
      "v_cvt_pk_bf16_f32 v99, v103, v107\n"
      "v_mfma_f32_16x16x32_bf16 v[144:147], v[20:23], v[248:251], 0\n"
      "ds_read_b128 v[100:103], v0 offset:4096\n"
      "v_add_f32 v212, v212, v213\n"
      "v_mfma_f32_16x16x32_bf16 v[144:147], v[24:27], v[252:255], v[144:147]\n"
      "v_rcp_f32 v213, v212\n"
      "v_mfma_f32_16x16x32_bf16 v[28:31], v[192:195], v[108:111], v[28:31]\n"
      "ds_bpermute_b32 v214, v4, v213\n"
      "v_mfma_f32_16x16x32_bf16 v[28:31], v[196:199], v[112:115], v[28:31]\n"
      "v_exp_f32 v201, v201\n"
      "v_mfma_f32_16x16x32_bf16 v[28:31], v[176:179], v[52:55], v[28:31]\n"
      "buffer_store_dwordx2 v[188:189], v3, s[44:47], s79 offen offset:0 nt sc1\n"
      "ds_read_b128 v[72:75], v0 offset:5632\n"
      "v_mfma_f32_16x16x32_bf16 v[32:35], v[192:195], v[180:183], v[32:35]\n"
      "ds_read_b128 v[156:159], v0 offset:4608\n"
      "ds_read_b128 v[88:91], v0 offset:5120\n"
      "v_mfma_f32_16x16x32_bf16 v[32:35], v[196:199], v[184:187], v[32:35]\n"
      "v_exp_f32 v202, v202\n"
      "v_mfma_f32_16x16x32_bf16 v[32:35], v[176:179], v[120:123], v[32:35]\n"
      "v_exp_f32 v147, v147\n"
      "v_mfma_f32_16x16x32_bf16 v[40:43], v[192:195], v[232:235], v[40:43]\n"
      "v_exp_f32 v131, v131\n"
      "v_mfma_f32_16x16x32_bf16 v[40:43], v[196:199], v[236:239], v[40:43]\n"
      "v_exp_f32 v129, v129\n"
      "v_mfma_f32_16x16x32_bf16 v[40:43], v[176:179], v[228:231], v[40:43]\n"
      "v_exp_f32 v150, v150\n"
      "v_mfma_f32_16x16x32_bf16 v[36:39], v[192:195], v[56:59], v[36:39]\n"
      "v_exp_f32 v128, v128\n"
      "v_mfma_f32_16x16x32_bf16 v[36:39], v[196:199], v[60:63], v[36:39]\n"
      "v_cvt_pk_bf16_f32 v248, v128, v129\n"
      "v_cvt_pk_bf16_f32 v249, v130, v131\n"
      "v_mfma_f32_16x16x32_bf16 v[36:39], v[176:179], v[116:119], v[36:39]\n"
      "s_waitcnt lgkmcnt(3)\n"
      "v_mul_f32_dpp v34, v214, v34 row_newbcast:8 row_mask:0xf bank_mask:0xf\n"
      "v_mul_f32_dpp v28, v214, v28 row_newbcast:0 row_mask:0xf bank_mask:0xf\n"
      "v_mfma_f32_16x16x32_bf16 v[176:179], a[160:163], v[100:103], 0\n"
      "v_mul_f32_dpp v32, v214, v32 row_newbcast:0 row_mask:0xf bank_mask:0xf\n"
      "s_waitcnt lgkmcnt(1)\n"
      "v_mfma_f32_16x16x32_bf16 v[176:179], a[164:167], v[156:159], v[176:179]\n"
      "s_waitcnt lgkmcnt(0)\n"
      "buffer_load_dwordx4 v1, s[40:43], s92 offen offset:2048 lds\n"
      "v_mfma_f32_16x16x32_bf16 v[176:179], a[168:171], v[88:91], v[176:179]\n"
      "v_exp_f32 v246, v246\n"
      "v_mfma_f32_16x16x32_bf16 v[176:179], a[172:175], v[72:75], v[176:179]\n"
      "v_cvt_pk_bf16_f32 v250, v150, v151\n"
      "v_mul_f32_dpp v38, v214, v38 row_newbcast:8 row_mask:0xf bank_mask:0xf\n"
      "v_mfma_f32_16x16x32_bf16 v[128:131], a[176:179], v[100:103], 0\n"
      "v_mul_f32_dpp v42, v214, v42 row_newbcast:8 row_mask:0xf bank_mask:0xf\n"
      "v_mul_f32_dpp v29, v214, v29 row_newbcast:4 row_mask:0xf bank_mask:0xf\n"
      "v_mfma_f32_16x16x32_bf16 v[128:131], a[180:183], v[156:159], v[128:131]\n"
      "v_mul_f32_dpp v40, v214, v40 row_newbcast:0 row_mask:0xf bank_mask:0xf\n"
      "v_mul_f32_dpp v35, v214, v35 row_newbcast:12 row_mask:0xf bank_mask:0xf\n"
      "v_mfma_f32_16x16x32_bf16 v[128:131], a[184:187], v[88:91], v[128:131]\n"
      "L_h2_%=:\n"
      "s_cbranch_execz L_hr_%=\n"
      "v_exp_f32 v172, v172\n"
      "v_mfma_f32_16x16x32_bf16 v[128:131], a[188:191], v[72:75], v[128:131]\n"
      "v_pk_max_i16 v95, v95, 0\n"
      "v_mul_f32_dpp v36, v214, v36 row_newbcast:0 row_mask:0xf bank_mask:0xf\n"
      "v_mfma_f32_16x16x32_bf16 v[196:199], a[144:147], v[100:103], 0\n"
      "v_mul_f32_dpp v33, v214, v33 row_newbcast:4 row_mask:0xf bank_mask:0xf\n"
      "v_maximum3_f32 v8, v8, v212, v213\n"
      "v_mfma_f32_16x16x32_bf16 v[196:199], a[148:151], v[156:159], v[196:199]\n"
      "v_pk_max_i16 v98, v98, 0\n"
      "v_mul_f32_dpp v39, v214, v39 row_newbcast:12 row_mask:0xf bank_mask:0xf\n"
      "v_mfma_f32_16x16x32_bf16 v[196:199], a[152:155], v[88:91], v[196:199]\n"
      "v_mul_f32_dpp v43, v214, v43 row_newbcast:12 row_mask:0xf bank_mask:0xf\n"
      "v_mul_f32_dpp v37, v214, v37 row_newbcast:4 row_mask:0xf bank_mask:0xf\n"
      "v_mfma_f32_16x16x32_bf16 v[196:199], a[156:159], v[72:75], v[196:199]\n"
      "v_mul_f32_dpp v31, v214, v31 row_newbcast:12 row_mask:0xf bank_mask:0xf\n"
      "v_mul_f32_dpp v30, v214, v30 row_newbcast:8 row_mask:0xf bank_mask:0xf\n"
      "v_mfma_f32_16x16x32_bf16 v[32:35], v[48:51], a[208:211], v[32:35]\n"
      "v_exp_f32 v247, v247\n"
      "v_mfma_f32_16x16x32_bf16 v[32:35], v[216:219], a[212:215], v[32:35]\n"
      "v_exp_f32 v174, v174\n"
      "v_mfma_f32_16x16x32_bf16 v[32:35], v[132:135], a[216:219], v[32:35]\n"
      "v_exp_f32 v200, v200\n"
      "v_mfma_f32_16x16x32_bf16 v[32:35], v[224:227], a[220:223], v[32:35]\n"
      "v_exp_f32 v152, v152\n"
      "v_mfma_f32_16x16x32_bf16 v[28:31], v[48:51], a[192:195], v[28:31]\n"
      "buffer_load_dwordx4 v1, s[40:43], s93 offen offset:1024 lds\n"
      "v_mfma_f32_16x16x32_bf16 v[28:31], v[216:219], a[196:199], v[28:31]\n"
      "v_exp_f32 v146, v146\n"
      "v_mfma_f32_16x16x32_bf16 v[28:31], v[132:135], a[200:203], v[28:31]\n"
      "v_mul_f32_dpp v41, v214, v41 row_newbcast:4 row_mask:0xf bank_mask:0xf\n"
      "v_cvt_pk_bf16_f32 v215, v198, v199\n"
      "v_mfma_f32_16x16x32_bf16 v[28:31], v[224:227], a[204:207], v[28:31]\n"
      "buffer_store_dwordx2 v[94:95], v2, s[44:47], s75 offen offset:512 nt sc1\n"
      "v_cvt_pk_bf16_f32 v214, v196, v197\n"
      "v_mfma_f32_16x16x32_bf16 v[40:43], v[48:51], a[240:243], v[40:43]\n"
      "v_exp_f32 v244, v244\n"
      "v_mfma_f32_16x16x32_bf16 v[40:43], v[216:219], a[244:247], v[40:43]\n"
      "v_exp_f32 v173, v173\n"
      "v_mfma_f32_16x16x32_bf16 v[40:43], v[132:135], a[248:251], v[40:43]\n"
      "v_cvt_pk_bf16_f32 v254, v200, v201\n"
      "v_cvt_pk_bf16_f32 v30, v30, v34\n"
      "v_mfma_f32_16x16x32_bf16 v[40:43], v[224:227], a[252:255], v[40:43]\n"
      "v_exp_f32 v144, v144\n"
      "v_mfma_f32_16x16x32_bf16 v[36:39], v[48:51], a[224:227], v[36:39]\n"
      "v_exp_f32 v175, v175\n"
      "v_mfma_f32_16x16x32_bf16 v[36:39], v[216:219], a[228:231], v[36:39]\n"
      "v_exp_f32 v145, v145\n"
      "v_mfma_f32_16x16x32_bf16 v[36:39], v[132:135], a[232:235], v[36:39]\n"
      "v_exp_f32 v153, v153\n"
      "v_mfma_f32_16x16x32_bf16 v[36:39], v[224:227], a[236:239], v[36:39]\n"
      "v_cvt_pk_bf16_f32 v251, v152, v153\n"
      "v_pk_max_i16 v99, v99, 0\n"
      "v_mfma_f32_16x16x32_bf16 v[224:227], a[128:131], v[100:103], 0\n"
      "v_cvt_pk_bf16_f32 v51, v174, v175\n"
      "v_cvt_pk_bf16_f32 v255, v202, v203\n"
      "v_mfma_f32_16x16x32_bf16 v[224:227], a[132:135], v[156:159], v[224:227]\n"
      "ds_read_b128 v[104:107], v0 offset:512\n"
      "v_cvt_pk_bf16_f32 v253, v146, v147\n"
      "v_mfma_f32_16x16x32_bf16 v[224:227], a[136:139], v[88:91], v[224:227]\n"
      "v_pk_max_i16 v93, v93, 0\n"
      "v_cvt_pk_bf16_f32 v252, v144, v145\n"
      "v_mfma_f32_16x16x32_bf16 v[224:227], a[140:143], v[72:75], v[224:227]\n"
      "v_exp_f32 v245, v245\n"
      "v_smfmac_f32_16x16x64_bf16 v[148:151], v[12:15], v[248:255], v5\n"
      "buffer_store_dwordx2 v[92:93], v2, s[44:47], s75 offen offset:0 nt sc1\n"
      "v_cvt_pk_bf16_f32 v50, v172, v173\n"
      "v_mfma_f32_16x16x4_4b_bf16 v[188:203], v[154:155], v[18:19], 0 cbsz:2 abid:2\n"
      "v_cvt_pk_bf16_f32 v49, v246, v247\n"
      "v_cvt_pk_bf16_f32 v48, v244, v245\n"
      "v_cvt_pk_bf16_f32 v34, v31, v35\n"
      "v_cvt_pk_bf16_f32 v35, v39, v43\n"
      "v_cvt_pk_bf16_f32 v28, v28, v32\n"
      "v_cvt_pk_bf16_f32 v212, v224, v225\n"
      "v_smfmac_f32_16x16x32_bf16 v[148:151], v[12:13], v[48:51], v5\n"
      "ds_read_b128 v[132:135], v0 offset:1536\n"
      "v_cvt_pk_bf16_f32 v217, v178, v179\n"
      "v_cvt_pk_bf16_f32 v219, v130, v131\n"
      "v_cvt_pk_bf16_f32 v213, v226, v227\n"
      "v_mfma_f32_16x16x32_bf16 v[188:191], v[248:251], v[108:111], v[188:191]\n"
      "v_pk_max_i16 v34, v34, 0\n"
      "v_mov_b64 v[152:153], 0\n"
      "v_mfma_f32_16x16x32_bf16 v[188:191], v[252:255], v[112:115], v[188:191]\n"
      "v_cvt_pk_bf16_f32 v216, v176, v177\n"
      "v_add_f32 v148, v148, v149\n"
      "v_mfma_f32_16x16x32_bf16 v[188:191], v[48:51], v[52:55], v[188:191]\n"
      "v_mov_b64 v[154:155], 0\n"
      "v_cvt_pk_bf16_f32 v218, v128, v129\n"
      "v_mfma_f32_16x16x32_bf16 v[224:227], v[76:79], v[212:215], 0\n"
      "buffer_load_dwordx4 v1, s[40:43], s93 offen offset:3072 lds\n"
      "v_mfma_f32_16x16x32_bf16 v[224:227], v[80:83], v[216:219], v[224:227]\n"
      "v_rcp_f32 v149, v148\n"
      "v_mfma_f32_16x16x32_bf16 v[176:179], v[20:23], v[212:215], 0\n"
      "ds_read_b128 v[172:175], v0 offset:1024\n"
      "v_cvt_pk_bf16_f32 v31, v38, v42\n"
      "v_mfma_f32_16x16x32_bf16 v[176:179], v[24:27], v[216:219], v[176:179]\n"
      "v_cvt_pk_bf16_f32 v32, v29, v33\n"
      "v_cvt_pk_bf16_f32 v33, v37, v41\n"
      "v_smfmac_f32_16x16x64_bf16 v[152:155], v[220:223], v[212:219], v5\n"
      "ds_read_b128 v[144:147], v0 offset:0\n"
      "v_cvt_pk_bf16_f32 v29, v36, v40\n"
      "v_mfma_f32_16x16x32_bf16 v[192:195], v[248:251], v[180:183], v[192:195]\n"
      "ds_bpermute_b32 v150, v4, v149 offset:128\n"
      "v_mfma_f32_16x16x32_bf16 v[192:195], v[252:255], v[184:187], v[192:195]\n"
      "v_exp_f32 v179, v179\n"
      "v_mfma_f32_16x16x32_bf16 v[192:195], v[48:51], v[120:123], v[192:195]\n"
      "v_exp_f32 v225, v225\n"
      "v_mfma_f32_16x16x32_bf16 v[92:95], v[204:207], v[212:215], 0\n"
      "v_exp_f32 v224, v224\n"
      "v_mfma_f32_16x16x32_bf16 v[92:95], v[208:211], v[216:219], v[92:95]\n"
      "v_exp_f32 v226, v226\n"
      "v_mfma_f32_16x16x32_bf16 v[128:131], v[64:67], v[212:215], 0\n"
      "v_exp_f32 v176, v176\n"
      "v_mfma_f32_16x16x32_bf16 v[128:131], v[68:71], v[216:219], v[128:131]\n"
      "buffer_load_dwordx4 v1, s[40:43], s92 offen offset:0 lds\n"
      "v_mfma_f32_16x16x32_bf16 v[196:199], v[248:251], v[56:59], v[196:199]\n"
      "v_exp_f32 v177, v177\n"
      "v_mfma_f32_16x16x32_bf16 v[196:199], v[252:255], v[60:63], v[196:199]\n"
      "v_exp_f32 v178, v178\n"
      "v_mfma_f32_16x16x32_bf16 v[196:199], v[48:51], v[116:119], v[196:199]\n"
      "v_exp_f32 v227, v227\n"
      "v_mfma_f32_16x16x32_bf16 v[200:203], v[248:251], v[232:235], v[200:203]\n"
      "v_cvt_pk_bf16_f32 v221, v178, v179\n"
      "v_cvt_pk_bf16_f32 v220, v176, v177\n"
      "v_mfma_f32_16x16x32_bf16 v[200:203], v[252:255], v[236:239], v[200:203]\n"
      "v_exp_f32 v93, v93\n"
      "v_mfma_f32_16x16x32_bf16 v[200:203], v[48:51], v[228:231], v[200:203]\n"
      "s_waitcnt lgkmcnt(0)\n"
      "v_mul_f32_dpp v196, v150, v196 row_newbcast:0 row_mask:0xf bank_mask:0xf\n"
      "v_mul_f32_dpp v191, v150, v191 row_newbcast:12 row_mask:0xf bank_mask:0xf\n"
      "v_mfma_f32_16x16x32_bf16 v[36:39], v[164:167], v[212:215], 0\n"
      "v_mul_f32_dpp v192, v150, v192 row_newbcast:0 row_mask:0xf bank_mask:0xf\n"
      "v_mul_f32_dpp v188, v150, v188 row_newbcast:0 row_mask:0xf bank_mask:0xf\n"
      "v_mfma_f32_16x16x32_bf16 v[36:39], v[168:171], v[216:219], v[36:39]\n"
      "s_nop 0\n"
      "v_mul_f32_dpp v201, v150, v201 row_newbcast:4 row_mask:0xf bank_mask:0xf\n"
      "v_mul_f32_dpp v194, v150, v194 row_newbcast:8 row_mask:0xf bank_mask:0xf\n"
      "v_mfma_f32_16x16x32_bf16 v[176:179], v[136:139], v[212:215], 0\n"
      "v_mul_f32_dpp v200, v150, v200 row_newbcast:0 row_mask:0xf bank_mask:0xf\n"
      "v_mfma_f32_16x16x32_bf16 v[176:179], v[140:143], v[216:219], v[176:179]\n"
      "v_exp_f32 v92, v92\n"
      "v_mfma_f32_16x16x32_bf16 v[216:219], a[160:163], v[144:147], 0\n"
      "v_cvt_pk_bf16_f32 v222, v224, v225\n"
      "v_mul_f32_dpp v197, v150, v197 row_newbcast:4 row_mask:0xf bank_mask:0xf\n"
      "v_mfma_f32_16x16x32_bf16 v[216:219], a[164:167], v[104:107], v[216:219]\n"
      "v_exp_f32 v37, v37\n"
      "v_mfma_f32_16x16x32_bf16 v[216:219], a[168:171], v[172:175], v[216:219]\n"
      "v_exp_f32 v95, v95\n"
      "v_mfma_f32_16x16x32_bf16 v[216:219], a[172:175], v[132:135], v[216:219]\n"
      "v_exp_f32 v129, v129\n"
      "v_mfma_f32_16x16x32_bf16 v[252:255], a[144:147], v[144:147], 0\n"
      "v_exp_f32 v176, v176\n"
      "v_mfma_f32_16x16x32_bf16 v[252:255], a[148:151], v[104:107], v[252:255]\n"
      "v_exp_f32 v94, v94\n"
      "v_mfma_f32_16x16x32_bf16 v[252:255], a[152:155], v[172:175], v[252:255]\n"
      "v_exp_f32 v38, v38\n"
      "v_mfma_f32_16x16x32_bf16 v[252:255], a[156:159], v[132:135], v[252:255]\n"
      "v_cvt_pk_bf16_f32 v223, v226, v227\n"
      "v_cvt_pk_bf16_f32 v251, v218, v219\n"
      "v_mfma_f32_16x16x32_bf16 v[48:51], a[176:179], v[144:147], 0\n"
      "v_exp_f32 v39, v39\n"
      "v_mfma_f32_16x16x32_bf16 v[48:51], a[180:183], v[104:107], v[48:51]\n"
      "v_cvt_pk_bf16_f32 v250, v216, v217\n"
      "v_mul_f32_dpp v198, v150, v198 row_newbcast:8 row_mask:0xf bank_mask:0xf\n"
      "v_mfma_f32_16x16x32_bf16 v[48:51], a[184:187], v[172:175], v[48:51]\n"
      "v_cvt_pk_bf16_f32 v248, v252, v253\n"
      "v_mul_f32_dpp v190, v150, v190 row_newbcast:8 row_mask:0xf bank_mask:0xf\n"
      "v_mfma_f32_16x16x32_bf16 v[48:51], a[188:191], v[132:135], v[48:51]\n"
      "v_mul_f32_dpp v189, v150, v189 row_newbcast:4 row_mask:0xf bank_mask:0xf\n"
      "v_cvt_pk_bf16_f32 v249, v254, v255\n"
      "v_mfma_f32_16x16x32_bf16 v[224:227], a[128:131], v[144:147], 0\n"
      "v_exp_f32 v131, v131\n"
      "v_mfma_f32_16x16x32_bf16 v[224:227], a[132:135], v[104:107], v[224:227]\n"
      "s_add_u32 m0, s53, 0\n"
      "v_exp_f32 v36, v36\n"
      "v_mfma_f32_16x16x32_bf16 v[224:227], a[136:139], v[172:175], v[224:227]\n"
      "v_cvt_pk_bf16_f32 v252, v48, v49\n"
      "v_cvt_pk_bf16_f32 v253, v50, v51\n"
      "v_mfma_f32_16x16x32_bf16 v[224:227], a[140:143], v[132:135], v[224:227]\n"
      "v_exp_f32 v177, v177\n"
      "v_mfma_f32_16x16x32_bf16 v[188:191], v[160:163], a[192:195], v[188:191]\n"
      "v_mul_f32_dpp v199, v150, v199 row_newbcast:12 row_mask:0xf bank_mask:0xf\n"
      "v_mul_f32_dpp v203, v150, v203 row_newbcast:12 row_mask:0xf bank_mask:0xf\n"
      "v_mfma_f32_16x16x32_bf16 v[188:191], v[84:87], a[196:199], v[188:191]\n"
      "v_cvt_pk_bf16_f32 v217, v38, v39\n"
      "v_cvt_pk_bf16_f32 v218, v92, v93\n"
      "v_mfma_f32_16x16x32_bf16 v[188:191], v[240:243], a[200:203], v[188:191]\n"
      "v_cvt_pk_bf16_f32 v247, v226, v227\n"
      "v_cvt_pk_bf16_f32 v246, v224, v225\n"
      "v_mfma_f32_16x16x32_bf16 v[188:191], v[124:127], a[204:207], v[188:191]\n"
      "v_mul_f32_dpp v195, v150, v195 row_newbcast:12 row_mask:0xf bank_mask:0xf\n"
      "v_mul_f32_dpp v193, v150, v193 row_newbcast:4 row_mask:0xf bank_mask:0xf\n"
      "v_smfmac_f32_16x16x64_bf16 v[152:155], v[44:47], v[246:253], v5\n"
      "buffer_load_dwordx4 v1, s[40:43], s95 offen offset:3072 lds\n"
      "v_mfma_f32_16x16x32_bf16 v[196:199], v[160:163], a[224:227], v[196:199]\n"
      "v_mul_f32_dpp v202, v150, v202 row_newbcast:8 row_mask:0xf bank_mask:0xf\n"
      "v_cvt_pk_bf16_f32 v219, v94, v95\n"
      "v_mfma_f32_16x16x32_bf16 v[196:199], v[84:87], a[228:231], v[196:199]\n"
      "v_exp_f32 v128, v128\n"
      "v_mfma_f32_16x16x32_bf16 v[196:199], v[240:243], a[232:235], v[196:199]\n"
      "v_cvt_pk_bf16_f32 v224, v128, v129\n"
      "v_add_f32 v152, v152, v153\n"
      "v_mfma_f32_16x16x32_bf16 v[196:199], v[124:127], a[236:239], v[196:199]\n"
      "v_add_f32 v154, v154, v155\n"
      "v_cvt_pk_bf16_f32 v216, v36, v37\n"
      "v_mfma_f32_16x16x32_bf16 v[192:195], v[160:163], a[208:211], v[192:195]\n"
      "v_exp_f32 v152, v152\n"
      "v_mfma_f32_16x16x32_bf16 v[192:195], v[84:87], a[212:215], v[192:195]\n"
      "v_exp_f32 v179, v179\n"
      "v_mfma_f32_16x16x32_bf16 v[192:195], v[240:243], a[216:219], v[192:195]\n"
      "v_exp_f32 v178, v178\n"
      "v_mfma_f32_16x16x32_bf16 v[192:195], v[124:127], a[220:223], v[192:195]\n"
      "v_exp_f32 v154, v154\n"
      "v_mfma_f32_16x16x32_bf16 v[200:203], v[160:163], a[240:243], v[200:203]\n"
      "v_cvt_pk_bf16_f32 v94, v152, v154\n"
      "v_add_f32 v160, v152, v154\n"
      "v_mfma_f32_16x16x32_bf16 v[200:203], v[84:87], a[244:247], v[200:203]\n"
      "v_add_f32 v161, v152, v154\n"
      "v_cvt_pk_bf16_f32 v95, v152, v154\n"
      "v_mfma_f32_16x16x32_bf16 v[200:203], v[240:243], a[248:251], v[200:203]\n"
      "s_nop 0\n"
      "v_permlane16_swap_b32 v94, v95\n"
      "v_mfma_f32_16x16x32_bf16 v[200:203], v[124:127], a[252:255], v[200:203]\n"
      "v_permlane16_swap_b32 v160, v161\n"
      "v_mov_b64 v[128:129], v[160:161]\n"
      "v_pk_max_i16 v32, v32, 0\n"
      "v_smfmac_f32_16x16x64_bf16 v[160:163], v[12:15], v[216:223], v5\n"
      "buffer_store_dwordx2 v[98:99], v2, s[44:47], s75 offen offset:768 nt sc1\n"
      "v_cvt_pk_bf16_f32 v188, v188, v192\n"
      "v_mfma_f32_16x16x4_4b_bf16 v[36:51], v[94:95], v[18:19], 0 cbsz:2 abid:0\n"
      "v_cvt_pk_bf16_f32 v227, v178, v179\n"
      "v_exp_f32 v130, v130\n"
      "v_cvt_pk_bf16_f32 v226, v176, v177\n"
      "v_cvt_pk_bf16_f32 v225, v130, v131\n"
      "v_pk_max_i16 v29, v29, 0\n"
      "s_nop 0\n"
      "v_smfmac_f32_16x16x32_bf16 v[160:163], v[12:13], v[224:227], v5\n"
      "buffer_load_dwordx4 v1, s[40:43], s94 offen offset:2048 lds\n"
      "v_pk_max_i16 v28, v28, 0\n"
      "s_nop 0\n"
      "v_mfma_f32_16x16x32_bf16 v[40:43], v[216:219], v[180:183], v[40:43]\n"
      "v_pk_max_i16 v97, v97, 0\n"
      "v_cvt_pk_bf16_f32 v192, v189, v193\n"
      "v_mfma_f32_16x16x32_bf16 v[40:43], v[220:223], v[184:187], v[40:43]\n"
      "s_not_b64 vcc, vcc\n"
      "v_pk_max_i16 v96, v96, 0\n"
      "v_add_f32 v160, v160, v161\n"
      "v_mfma_f32_16x16x32_bf16 v[40:43], v[224:227], v[120:123], v[40:43]\n"
      "v_rcp_f32 v161, v160\n"
      "v_mfma_f32_16x16x32_bf16 v[36:39], v[216:219], v[108:111], v[36:39]\n"
      "ds_bpermute_b32 v162, v4, v161\n"
      "v_mfma_f32_16x16x32_bf16 v[36:39], v[220:223], v[112:115], v[36:39]\n"
      "v_maximum3_f32 v8, v8, v148, v149\n"
      "v_mfma_f32_16x16x32_bf16 v[36:39], v[224:227], v[52:55], v[36:39]\n"
      "v_pk_max_i16 v188, v188, 0\n"
      "v_mfma_f32_16x16x32_bf16 v[48:51], v[216:219], v[232:235], v[48:51]\n"
      "v_mfma_f32_16x16x32_bf16 v[48:51], v[220:223], v[236:239], v[48:51]\n"
      "v_cvt_pk_bf16_f32 v190, v190, v194\n"
      "v_cvt_pk_bf16_f32 v194, v191, v195\n"
      "v_mfma_f32_16x16x32_bf16 v[48:51], v[224:227], v[228:231], v[48:51]\n"
      "buffer_store_dwordx2 v[96:97], v2, s[44:47], s75 offen offset:256 nt sc1\n"
      "v_cvt_pk_bf16_f32 v195, v199, v203\n"
      "v_mfma_f32_16x16x32_bf16 v[44:47], v[216:219], v[56:59], v[44:47]\n"
      "buffer_load_dwordx4 v1, s[40:43], s95 offen offset:1024 lds\n"
      "v_mfma_f32_16x16x32_bf16 v[44:47], v[220:223], v[60:63], v[44:47]\n"
      "v_cvt_pk_bf16_f32 v189, v196, v200\n"
      "v_maximum3_f32 v8, v8, v160, v161\n"
      "v_mfma_f32_16x16x32_bf16 v[44:47], v[224:227], v[116:119], v[44:47]\n"
      "v_cvt_pk_bf16_f32 v193, v197, v201\n"
      "v_cvt_pk_bf16_f32 v191, v198, v202\n"
      "v_mfma_f32_16x16x4_4b_bf16 v[212:227], v[94:95], v[18:19], 0 cbsz:2 abid:2\n"
      "s_waitcnt lgkmcnt(0)\n"
      "v_mul_f32_dpp v38, v162, v38 row_newbcast:8 row_mask:0xf bank_mask:0xf\n"
      "v_mul_f32_dpp v39, v162, v39 row_newbcast:12 row_mask:0xf bank_mask:0xf\n"
      "v_mul_f32_dpp v37, v162, v37 row_newbcast:4 row_mask:0xf bank_mask:0xf\n"
      "v_mul_f32_dpp v36, v162, v36 row_newbcast:0 row_mask:0xf bank_mask:0xf\n"
      "v_pk_max_i16 v192, v192, 0\n"
      "v_mul_f32_dpp v46, v162, v46 row_newbcast:8 row_mask:0xf bank_mask:0xf\n"
      "v_mul_f32_dpp v47, v162, v47 row_newbcast:12 row_mask:0xf bank_mask:0xf\n"
      "v_mul_f32_dpp v41, v162, v41 row_newbcast:4 row_mask:0xf bank_mask:0xf\n"
      "v_mfma_f32_16x16x32_bf16 v[36:39], v[100:103], a[192:195], v[36:39]\n"
      "v_mul_f32_dpp v45, v162, v45 row_newbcast:4 row_mask:0xf bank_mask:0xf\n"
      "v_mul_f32_dpp v50, v162, v50 row_newbcast:8 row_mask:0xf bank_mask:0xf\n"
      "v_mfma_f32_16x16x32_bf16 v[36:39], v[156:159], a[196:199], v[36:39]\n"
      "v_mul_f32_dpp v49, v162, v49 row_newbcast:4 row_mask:0xf bank_mask:0xf\n"
      "v_mul_f32_dpp v44, v162, v44 row_newbcast:0 row_mask:0xf bank_mask:0xf\n"
      "v_mfma_f32_16x16x32_bf16 v[36:39], v[88:91], a[200:203], v[36:39]\n"
      "v_mul_f32_dpp v51, v162, v51 row_newbcast:12 row_mask:0xf bank_mask:0xf\n"
      "v_mul_f32_dpp v48, v162, v48 row_newbcast:0 row_mask:0xf bank_mask:0xf\n"
      "v_mfma_f32_16x16x32_bf16 v[36:39], v[72:75], a[204:207], v[36:39]\n"
      "v_pk_max_i16 v193, v193, 0\n"
      "v_pk_max_i16 v190, v190, 0\n"
      "v_mfma_f32_16x16x32_bf16 v[48:51], v[100:103], a[240:243], v[48:51]\n"
      "v_mfma_f32_16x16x32_bf16 v[48:51], v[156:159], a[244:247], v[48:51]\n"
      "v_mul_f32_dpp v43, v162, v43 row_newbcast:12 row_mask:0xf bank_mask:0xf\n"
      "v_pk_max_i16 v35, v35, 0\n"
      "v_mfma_f32_16x16x32_bf16 v[48:51], v[88:91], a[248:251], v[48:51]\n"
      "v_pk_max_i16 v30, v30, 0\n"
      "v_pk_max_i16 v195, v195, 0\n"
      "v_mfma_f32_16x16x32_bf16 v[48:51], v[72:75], a[252:255], v[48:51]\n"
      "v_pk_max_i16 v189, v189, 0\n"
      "v_add_u32 v9, 64, v9\n"
      "v_mfma_f32_16x16x32_bf16 v[44:47], v[100:103], a[224:227], v[44:47]\n"
      "v_mul_f32_dpp v42, v162, v42 row_newbcast:8 row_mask:0xf bank_mask:0xf\n"
      "v_mul_f32_dpp v40, v162, v40 row_newbcast:0 row_mask:0xf bank_mask:0xf\n"
      "v_mfma_f32_16x16x32_bf16 v[44:47], v[156:159], a[228:231], v[44:47]\n"
      "v_pk_max_i16 v31, v31, 0\n"
      "v_mfma_f32_16x16x32_bf16 v[44:47], v[88:91], a[232:235], v[44:47]\n"
      "s_add_u32 s44, s44, 13107200\n s_addc_u32 s45, s45, 0\n"
      "v_mfma_f32_16x16x32_bf16 v[44:47], v[72:75], a[236:239], v[44:47]\n"
      "v_pk_max_i16 v33, v33, 0\n"
      "v_pk_max_i16 v191, v191, 0\n"
      "v_mfma_f32_16x16x32_bf16 v[40:43], v[100:103], a[208:211], v[40:43]\n"
      "v_pk_max_i16 v194, v194, 0\n"
      "v_mfma_f32_16x16x32_bf16 v[40:43], v[156:159], a[212:215], v[40:43]\n"
      "buffer_load_dwordx4 v1, s[40:43], s94 offen offset:0 lds\n"
      "v_mfma_f32_16x16x32_bf16 v[40:43], v[88:91], a[216:219], v[40:43]\n"
      "s_add_u32 s40, s40, 13107200\n s_addc_u32 s41, s41, 0\n"
      "v_mfma_f32_16x16x32_bf16 v[40:43], v[72:75], a[220:223], v[40:43]\n"
      "s_nop 0\n"
      "L_task_%=:\n"
      "v_mfma_f32_16x16x32_bf16 v[240:243], v[136:139], v[246:249], 0\n"
      "s_waitcnt vmcnt(29)\n"
      "ds_read_b128 v[92:95], v0 offset:20480\n"
      "ds_read_b128 v[96:99], v0 offset:21504\n"
      "v_mfma_f32_16x16x32_bf16 v[240:243], v[140:143], v[250:253], v[240:243]\n"
      "buffer_store_dwordx2 v[28:29], v2, s[44:47], s100 offen offset:0 nt sc1\n"
      "ds_read_b128 v[88:91], v0 offset:20992\n"
      "v_mfma_f32_16x16x32_bf16 v[156:159], v[76:79], v[246:249], 0\n"
      "s_waitcnt vmcnt(14)\n"
      "ds_read_b128 v[160:163], v0 offset:8192\n"
      "ds_read_b128 v[200:203], v0 offset:9728\n"
      "v_mfma_f32_16x16x32_bf16 v[156:159], v[80:83], v[250:253], v[156:159]\n"
      "ds_read_b128 v[84:87], v0 offset:9216\n"
      "ds_read_b128 v[148:151], v0 offset:8704\n"
      "v_mfma_f32_16x16x32_bf16 v[100:103], v[64:67], v[246:249], 0\n"
      "ds_read_b128 v[124:127], v0 offset:16384\n"
      "ds_read_b128 v[72:75], v0 offset:17408\n"
      "v_mfma_f32_16x16x32_bf16 v[100:103], v[68:71], v[250:253], v[100:103]\n"
      "s_nop 1\n"
      "v_exp_f32 v158, v158\n"
      "v_mfma_f32_16x16x32_bf16 v[64:67], v[164:167], v[246:249], 0\n"
      "v_cvt_pk_bf16_f32 v36, v36, v40\n"
      "v_mfma_f32_16x16x32_bf16 v[64:67], v[168:171], v[250:253], v[64:67]\n"
      "v_exp_f32 v157, v157\n"
      "v_mfma_f32_16x16x32_bf16 v[168:171], v[204:207], v[246:249], 0\n"
      "v_exp_f32 v103, v103\n"
      "v_mfma_f32_16x16x32_bf16 v[168:171], v[208:211], v[250:253], v[168:171]\n"
      "v_exp_f32 v102, v102\n"
      "v_mfma_f32_16x16x32_bf16 v[152:155], v[20:23], v[246:249], 0\n"
      "v_exp_f32 v242, v242\n"
      "v_mfma_f32_16x16x32_bf16 v[152:155], v[24:27], v[250:253], v[152:155]\n"
      "s_waitcnt lgkmcnt(5)\n"
      "v_mfma_f32_16x16x32_bf16 v[80:83], a[32:35], v[160:163], 0\n"
      "s_waitcnt lgkmcnt(2)\n"
      "v_mfma_f32_16x16x32_bf16 v[80:83], a[36:39], v[148:151], v[80:83]\n"
      "v_mfma_f32_16x16x32_bf16 v[80:83], a[40:43], v[84:87], v[80:83]\n"
      "v_cvt_pk_bf16_f32 v40, v37, v41\n"
      "v_mfma_f32_16x16x32_bf16 v[80:83], a[44:47], v[200:203], v[80:83]\n"
      "v_exp_f32 v159, v159\n"
      "v_mfma_f32_16x16x32_bf16 v[140:143], a[48:51], v[160:163], 0\n"
      "v_exp_f32 v153, v153\n"
      "v_mfma_f32_16x16x32_bf16 v[140:143], a[52:55], v[148:151], v[140:143]\n"
      "v_cvt_pk_bf16_f32 v211, v158, v159\n"
      "v_mfma_f32_16x16x32_bf16 v[140:143], a[56:59], v[84:87], v[140:143]\n"
      "v_exp_f32 v101, v101\n"
      "v_mfma_f32_16x16x32_bf16 v[140:143], a[60:63], v[200:203], v[140:143]\n"
      "ds_read_b128 v[176:179], v0 offset:22016\n"
      "v_mfma_f32_16x16x32_bf16 v[164:167], v[160:163], a[80:83], 0\n"
      "v_exp_f32 v243, v243\n"
      "v_mfma_f32_16x16x32_bf16 v[164:167], v[148:151], a[84:87], v[164:167]\n"
      "v_exp_f32 v66, v66\n"
      "v_mfma_f32_16x16x32_bf16 v[164:167], v[84:87], a[88:91], v[164:167]\n"
      "v_mfma_f32_16x16x32_bf16 v[164:167], v[200:203], a[92:95], v[164:167]\n"
      "buffer_store_dwordx2 v[190:191], v2, s[44:47], s101 offen offset:512 nt sc1\n"
      "v_mfma_f32_16x16x32_bf16 v[76:79], v[160:163], a[64:67], 0\n"
      "ds_read_b128 v[196:199], v0 offset:17920\n"
      "v_mfma_f32_16x16x32_bf16 v[76:79], v[148:151], a[68:71], v[76:79]\n"
      "v_mfma_f32_16x16x32_bf16 v[76:79], v[84:87], a[72:75], v[76:79]\n"
      "v_mfma_f32_16x16x32_bf16 v[76:79], v[200:203], a[76:79], v[76:79]\n"
      "s_add_u32 m0, s53, 20480\n"
      "v_exp_f32 v64, v64\n"
      "v_mfma_f32_16x16x32_bf16 v[136:139], v[160:163], a[112:115], 0\n"
      "v_exp_f32 v156, v156\n"
      "v_mfma_f32_16x16x32_bf16 v[136:139], v[148:151], a[116:119], v[136:139]\n"
      "v_cvt_pk_bf16_f32 v210, v156, v157\n"
      "v_mfma_f32_16x16x32_bf16 v[136:139], v[84:87], a[120:123], v[136:139]\n"
      "v_cvt_pk_bf16_f32 v38, v38, v42\n"
      "v_mfma_f32_16x16x32_bf16 v[136:139], v[200:203], a[124:127], v[136:139]\n"
      "v_exp_f32 v154, v154\n"
      "v_mfma_f32_16x16x32_bf16 v[68:71], a[0:3], v[160:163], 0\n"
      "v_exp_f32 v241, v241\n"
      "v_mfma_f32_16x16x32_bf16 v[68:71], a[4:7], v[148:151], v[68:71]\n"
      "v_exp_f32 v152, v152\n"
      "v_mfma_f32_16x16x32_bf16 v[68:71], a[8:11], v[84:87], v[68:71]\n"
      "v_exp_f32 v240, v240\n"
      "v_mfma_f32_16x16x32_bf16 v[68:71], a[12:15], v[200:203], v[68:71]\n"
      "v_cvt_pk_bf16_f32 v41, v45, v49\n"
      "v_pk_max_i16 v41, v41, 0\n"
      "v_mfma_f32_16x16x32_bf16 v[252:255], a[16:19], v[160:163], 0\n"
      "s_waitcnt lgkmcnt(1)\n"
      "buffer_load_dwordx4 v1, s[40:43], s74 offen offset:1024 lds\n"
      "v_mfma_f32_16x16x32_bf16 v[252:255], a[20:23], v[148:151], v[252:255]\n"
      "v_exp_f32 v155, v155\n"
      "v_mfma_f32_16x16x32_bf16 v[252:255], a[24:27], v[84:87], v[252:255]\n"
      "v_exp_f32 v65, v65\n"
      "v_mfma_f32_16x16x32_bf16 v[252:255], a[28:31], v[200:203], v[252:255]\n"
      "v_cvt_pk_bf16_f32 v209, v154, v155\n"
      "v_cvt_pk_bf16_f32 v208, v152, v153\n"
      "v_mfma_f32_16x16x32_bf16 v[152:155], v[160:163], a[96:99], 0\n"
      "v_cvt_pk_bf16_f32 v42, v39, v43\n"
      "v_cvt_pk_bf16_f32 v19, v102, v103\n"
      "v_mfma_f32_16x16x32_bf16 v[152:155], v[148:151], a[100:103], v[152:155]\n"
      "v_exp_f32 v100, v100\n"
      "v_mfma_f32_16x16x32_bf16 v[152:155], v[84:87], a[104:107], v[152:155]\n"
      "ds_read2_b32 v[160:161], v16 offset0:0 offset1:2\n"
      "v_mfma_f32_16x16x32_bf16 v[152:155], v[200:203], a[108:111], v[152:155]\n"
      "buffer_store_dwordx2 v[32:33], v2, s[44:47], s100 offen offset:256 nt sc1\n"
      "v_cvt_pk_bf16_f32 v25, v82, v83\n"
      "v_mfma_f32_16x16x32_bf16 v[148:151], a[176:179], v[92:95], 0\n"
      "v_exp_f32 v168, v168\n"
      "v_mfma_f32_16x16x32_bf16 v[148:151], a[180:183], v[88:91], v[148:151]\n"
      "v_cvt_pk_bf16_f32 v21, v242, v243\n"
      "v_mfma_f32_16x16x32_bf16 v[148:151], a[184:187], v[96:99], v[148:151]\n"
      "v_exp_f32 v170, v170\n"
      "v_mfma_f32_16x16x32_bf16 v[148:151], a[188:191], v[176:179], v[148:151]\n"
      "v_exp_f32 v67, v67\n"
      "v_mfma_f32_16x16x32_bf16 v[200:203], a[160:163], v[92:95], 0\n"
      "v_mfma_f32_16x16x32_bf16 v[200:203], a[164:167], v[88:91], v[200:203]\n"
      "v_exp_f32 v171, v171\n"
      "v_mfma_f32_16x16x32_bf16 v[200:203], a[168:171], v[96:99], v[200:203]\n"
      "v_mfma_f32_16x16x32_bf16 v[200:203], a[172:175], v[176:179], v[200:203]\n"
      "v_cvt_pk_bf16_f32 v205, v66, v67\n"
      "v_cvt_pk_bf16_f32 v204, v64, v65\n"
      "v_mfma_f32_16x16x32_bf16 v[64:67], a[32:35], v[92:95], 0\n"
      "v_mfma_f32_16x16x32_bf16 v[64:67], a[36:39], v[88:91], v[64:67]\n"
      "v_cvt_pk_bf16_f32 v18, v100, v101\n"
      "v_cvt_pk_bf16_f32 v207, v170, v171\n"
      "v_mfma_f32_16x16x32_bf16 v[64:67], a[40:43], v[96:99], v[64:67]\n"
      "v_mfma_f32_16x16x32_bf16 v[64:67], a[44:47], v[176:179], v[64:67]\n"
      "v_exp_f32 v169, v169\n"
      "v_mfma_f32_16x16x32_bf16 v[248:251], a[0:3], v[92:95], 0\n"
      "v_mfma_f32_16x16x32_bf16 v[248:251], a[4:7], v[88:91], v[248:251]\n"
      "v_cvt_pk_bf16_f32 v206, v168, v169\n"
      "v_mfma_f32_16x16x32_bf16 v[248:251], a[8:11], v[96:99], v[248:251]\n"
      "v_cvt_pk_bf16_f32 v244, v200, v201\n"
      "v_mfma_f32_16x16x32_bf16 v[248:251], a[12:15], v[176:179], v[248:251]\n"
      "ds_read_b128 v[100:103], v0 offset:16896\n"
      "v_cvt_pk_bf16_f32 v20, v240, v241\n"
      "v_smfmac_f32_16x16x64_bf16 v[128:131], v[12:15], v[204:211], v5\n"
      "v_mfma_f32_16x16x32_bf16 v[224:227], v[204:207], v[232:235], v[224:227]\n"
      "v_mfma_f32_16x16x32_bf16 v[224:227], v[208:211], v[236:239], v[224:227]\n"
      "v_cvt_pk_bf16_f32 v43, v47, v51\n"
      "v_cvt_pk_bf16_f32 v245, v202, v203\n"
      "v_mfma_f32_16x16x32_bf16 v[224:227], v[18:21], v[228:231], v[224:227]\n"
      "buffer_load_dwordx4 v1, s[40:43], s73 offen offset:2048 lds\n"
      "v_smfmac_f32_16x16x32_bf16 v[128:131], v[12:13], v[18:21], v5\n"
      "buffer_store_dwordx2 v[192:193], v2, s[44:47], s101 offen offset:256 nt sc1\n"
      "v_mfma_f32_16x16x32_bf16 v[232:235], a[16:19], v[92:95], 0\n"
      "v_mfma_f32_16x16x32_bf16 v[232:235], a[20:23], v[88:91], v[232:235]\n"
      "ds_read_b128 v[168:171], v9 offset:2048\n"
      "v_mfma_f32_16x16x32_bf16 v[232:235], a[24:27], v[96:99], v[232:235]\n"
      "ds_read2_b32 v[162:163], v16 offset0:128 offset1:130\n"
      "v_mfma_f32_16x16x32_bf16 v[232:235], a[28:31], v[176:179], v[232:235]\n"
      "v_cvt_pk_bf16_f32 v37, v44, v48\n"
      "v_cvt_pk_bf16_f32 v24, v80, v81\n"
      "v_mfma_f32_16x16x32_bf16 v[220:223], v[204:207], v[56:59], v[220:223]\n"
      "v_mfma_f32_16x16x32_bf16 v[220:223], v[208:211], v[60:63], v[220:223]\n"
      "s_waitcnt vmcnt(12)\n"
      "ds_read_b128 v[80:83], v0 offset:4096\n"
      "v_mfma_f32_16x16x32_bf16 v[220:223], v[18:21], v[116:119], v[220:223]\n"
      "buffer_load_dwordx4 v1, s[40:43], s74 offen offset:3072 lds\n"
      "v_mfma_f32_16x16x32_bf16 v[212:215], v[204:207], v[108:111], v[212:215]\n"
      "v_mfma_f32_16x16x32_bf16 v[212:215], v[208:211], v[112:115], v[212:215]\n"
      "ds_read_b128 v[116:119], v0 offset:5632\n"
      "v_add_f32 v128, v128, v129\n"
      "v_mfma_f32_16x16x32_bf16 v[212:215], v[18:21], v[52:55], v[212:215]\n"
      "v_cvt_pk_bf16_f32 v22, v252, v253\n"
      "v_cvt_pk_bf16_f32 v236, v136, v137\n"
      "v_mfma_f32_16x16x32_bf16 v[200:203], a[16:19], v[124:127], 0\n"
      "v_cvt_pk_bf16_f32 v136, v248, v249\n"
      "s_waitcnt lgkmcnt(4)\n"
      "v_mfma_f32_16x16x32_bf16 v[200:203], a[20:23], v[100:103], v[200:203]\n"
      "ds_read_b128 v[108:111], v0 offset:4608\n"
      "v_mfma_f32_16x16x32_bf16 v[200:203], a[24:27], v[72:75], v[200:203]\n"
      "v_rcp_f32 v129, v128\n"
      "v_mfma_f32_16x16x32_bf16 v[200:203], a[28:31], v[196:199], v[200:203]\n"
      "ds_bpermute_b32 v130, v4, v129 offset:128\n"
      "v_mfma_f32_16x16x32_bf16 v[216:219], v[204:207], v[180:183], v[216:219]\n"
      "v_mfma_f32_16x16x32_bf16 v[216:219], v[208:211], v[184:187], v[216:219]\n"
      "v_cvt_pk_bf16_f32 v184, v164, v165\n"
      "v_cvt_pk_bf16_f32 v27, v142, v143\n"
      "v_mfma_f32_16x16x32_bf16 v[216:219], v[18:21], v[120:123], v[216:219]\n"
      "v_mfma_f32_16x16x32_bf16 v[56:59], a[128:131], v[124:127], 0\n"
      "v_cvt_pk_bf16_f32 v237, v138, v139\n"
      "v_pk_max_i16 v38, v38, 0\n"
      "v_mfma_f32_16x16x32_bf16 v[56:59], a[132:135], v[100:103], v[56:59]\n"
      "ds_read_b128 v[52:55], v10 offset:0\n"
      "v_cvt_pk_bf16_f32 v185, v166, v167\n"
      "v_mfma_f32_16x16x32_bf16 v[56:59], a[136:139], v[72:75], v[56:59]\n"
      "v_cvt_pk_bf16_f32 v21, v70, v71\n"
      "v_mfma_f32_16x16x32_bf16 v[56:59], a[140:143], v[196:199], v[56:59]\n"
      "buffer_store_dwordx2 v[30:31], v2, s[44:47], s100 offen offset:512 nt sc1\n"
      "ds_read_b128 v[180:183], v0 offset:5120\n"
      "v_mfma_f32_16x16x32_bf16 v[208:211], a[48:51], v[92:95], 0\n"
      "v_mfma_f32_16x16x32_bf16 v[208:211], a[52:55], v[88:91], v[208:211]\n"
      "s_waitcnt lgkmcnt(2)\n"
      "v_mul_f32_dpp v219, v130, v219 row_newbcast:12 row_mask:0xf bank_mask:0xf\n"
      "v_mul_f32_dpp v216, v130, v216 row_newbcast:0 row_mask:0xf bank_mask:0xf\n"
      "v_mfma_f32_16x16x32_bf16 v[208:211], a[56:59], v[96:99], v[208:211]\n"
      "v_cvt_pk_bf16_f32 v20, v68, v69\n"
      "v_mul_f32_dpp v220, v130, v220 row_newbcast:0 row_mask:0xf bank_mask:0xf\n"
      "v_mfma_f32_16x16x32_bf16 v[208:211], a[60:63], v[176:179], v[208:211]\n"
      "v_cvt_pk_bf16_f32 v246, v148, v149\n"
      "v_mfma_f32_16x16x32_bf16 v[112:115], a[128:131], v[92:95], 0\n"
      "v_cvt_pk_bf16_f32 v60, v152, v153\n"
      "v_mfma_f32_16x16x32_bf16 v[112:115], a[132:135], v[88:91], v[112:115]\n"
      "v_mfma_f32_16x16x32_bf16 v[112:115], a[136:139], v[96:99], v[112:115]\n"
      "v_mfma_f32_16x16x32_bf16 v[112:115], a[140:143], v[176:179], v[112:115]\n"
      "v_pk_max_i16 v36, v36, 0\n"
      "v_cvt_pk_bf16_f32 v247, v150, v151\n"
      "v_mfma_f32_16x16x32_bf16 v[148:151], a[16:19], v[80:83], 0\n"
      "v_mfma_f32_16x16x32_bf16 v[148:151], a[20:23], v[108:111], v[148:151]\n"
      "s_waitcnt lgkmcnt(0)\n"
      "v_mfma_f32_16x16x32_bf16 v[148:151], a[24:27], v[180:183], v[148:151]\n"
      "ds_read_b128 v[164:167], v9 offset:0\n"
      "v_mul_f32_dpp v217, v130, v217 row_newbcast:4 row_mask:0xf bank_mask:0xf\n"
      "v_mfma_f32_16x16x32_bf16 v[148:151], a[28:31], v[116:119], v[148:151]\n"
      "v_mul_f32_dpp v214, v130, v214 row_newbcast:8 row_mask:0xf bank_mask:0xf\n"
      "v_mfma_f32_16x16x32_bf16 v[68:71], a[144:147], v[92:95], 0\n"
      "buffer_load_dwordx4 v1, s[40:43], s73 offen offset:0 lds\n"
      "v_mfma_f32_16x16x32_bf16 v[68:71], a[148:151], v[88:91], v[68:71]\n"
      "v_cvt_pk_bf16_f32 v142, v208, v209\n"
      "v_cvt_pk_bf16_f32 v26, v140, v141\n"
      "v_mfma_f32_16x16x32_bf16 v[68:71], a[152:155], v[96:99], v[68:71]\n"
      "v_mfma_f32_16x16x32_bf16 v[68:71], a[156:159], v[176:179], v[68:71]\n"
      "v_cvt_pk_bf16_f32 v39, v46, v50\n"
      "v_mfma_f32_16x16x32_bf16 v[48:51], a[32:35], v[80:83], 0\n"
      "v_mfma_f32_16x16x32_bf16 v[48:51], a[36:39], v[108:111], v[48:51]\n"
      "v_mul_f32_dpp v225, v130, v225 row_newbcast:4 row_mask:0xf bank_mask:0xf\n"
      "v_cvt_pk_bf16_f32 v139, v234, v235\n"
      "v_mfma_f32_16x16x32_bf16 v[48:51], a[40:43], v[180:183], v[48:51]\n"
      "s_add_u32 m0, s53, 16384\n"
      "v_cvt_pk_bf16_f32 v240, v112, v113\n"
      "v_mov_b64 v[152:153], 0\n"
      "v_mfma_f32_16x16x32_bf16 v[48:51], a[44:47], v[116:119], v[48:51]\n"
      "buffer_store_dwordx2 v[194:195], v2, s[44:47], s101 offen offset:768 nt sc1\n"
      "v_cvt_pk_bf16_f32 v23, v254, v255\n"
      "v_mfma_f32_16x16x32_bf16 v[120:123], a[32:35], v[124:127], 0\n"
      "v_cvt_pk_bf16_f32 v61, v154, v155\n"
      "v_cvt_pk_bf16_f32 v241, v114, v115\n"
      "v_mfma_f32_16x16x32_bf16 v[120:123], a[36:39], v[100:103], v[120:123]\n"
      "v_mfma_f32_16x16x32_bf16 v[120:123], a[40:43], v[72:75], v[120:123]\n"
      "v_mfma_f32_16x16x32_bf16 v[120:123], a[44:47], v[196:199], v[120:123]\n"
      "v_mov_b64 v[230:231], 0\n"
      "v_cvt_pk_bf16_f32 v208, v48, v49\n"
      "v_mfma_f32_16x16x32_bf16 v[28:31], v[80:83], a[112:115], 0\n"
      "v_mfma_f32_16x16x32_bf16 v[28:31], v[108:111], a[116:119], v[28:31]\n"
      "v_mfma_f32_16x16x32_bf16 v[28:31], v[180:183], a[120:123], v[28:31]\n"
      "v_cvt_pk_bf16_f32 v207, v150, v151\n"
      "v_mul_f32_dpp v212, v130, v212 row_newbcast:0 row_mask:0xf bank_mask:0xf\n"
      "v_mfma_f32_16x16x32_bf16 v[28:31], v[116:119], a[124:127], v[28:31]\n"
      "v_mul_f32_dpp v218, v130, v218 row_newbcast:8 row_mask:0xf bank_mask:0xf\n"
      "v_cvt_pk_bf16_f32 v143, v210, v211\n"
      "v_mfma_f32_16x16x32_bf16 v[44:47], a[0:3], v[80:83], 0\n"
      "L_h3_%=:\n"
      "s_cbranch_execz L_hr_%=\n"
      "v_mul_f32_dpp v221, v130, v221 row_newbcast:4 row_mask:0xf bank_mask:0xf\n"
      "v_mul_f32_dpp v224, v130, v224 row_newbcast:0 row_mask:0xf bank_mask:0xf\n"
      "v_mfma_f32_16x16x32_bf16 v[44:47], a[4:7], v[108:111], v[44:47]\n"
      "buffer_load_dwordx4 v1, s[40:43], s76 offen offset:3072 lds\n"
      "v_mfma_f32_16x16x32_bf16 v[44:47], a[8:11], v[180:183], v[44:47]\n"
      "v_mul_f32_dpp v215, v130, v215 row_newbcast:12 row_mask:0xf bank_mask:0xf\n"
      "v_cvt_pk_bf16_f32 v141, v66, v67\n"
      "v_mfma_f32_16x16x32_bf16 v[44:47], a[12:15], v[116:119], v[44:47]\n"
      "v_mfma_f32_16x16x32_bf16 v[154:157], a[48:51], v[80:83], 0\n"
      "v_pk_max_i16 v40, v40, 0\n"
      "v_cvt_pk_bf16_f32 v242, v68, v69\n"
      "v_mfma_f32_16x16x32_bf16 v[154:157], a[52:55], v[108:111], v[154:157]\n"
      "v_cvt_pk_bf16_f32 v138, v232, v233\n"
      "v_mfma_f32_16x16x32_bf16 v[154:157], a[56:59], v[180:183], v[154:157]\n"
      "v_cvt_pk_bf16_f32 v243, v70, v71\n"
      "v_mfma_f32_16x16x32_bf16 v[154:157], a[60:63], v[116:119], v[154:157]\n"
      "buffer_store_dwordx2 v[34:35], v2, s[44:47], s100 offen offset:768 nt sc1\n"
      "v_mul_f32_dpp v226, v130, v226 row_newbcast:8 row_mask:0xf bank_mask:0xf\n"
      "v_mfma_f32_16x16x32_bf16 v[112:115], a[0:3], v[124:127], 0\n"
      "v_mul_f32_dpp v213, v130, v213 row_newbcast:4 row_mask:0xf bank_mask:0xf\n"
      "v_mul_f32_dpp v223, v130, v223 row_newbcast:12 row_mask:0xf bank_mask:0xf\n"
      "v_mfma_f32_16x16x32_bf16 v[112:115], a[4:7], v[100:103], v[112:115]\n"
      "v_cvt_pk_bf16_f32 v66, v200, v201\n"
      "v_cvt_pk_bf16_f32 v235, v30, v31\n"
      "v_mfma_f32_16x16x32_bf16 v[112:115], a[8:11], v[72:75], v[112:115]\n"
      "v_mfma_f32_16x16x32_bf16 v[112:115], a[12:15], v[196:199], v[112:115]\n"
      "v_cvt_pk_bf16_f32 v67, v202, v203\n"
      "v_cvt_pk_bf16_f32 v68, v120, v121\n"
      "v_mfma_f32_16x16x32_bf16 v[252:255], v[80:83], a[96:99], 0\n"
      "v_cvt_pk_bf16_f32 v211, v156, v157\n"
      "v_mfma_f32_16x16x32_bf16 v[252:255], v[108:111], a[100:103], v[252:255]\n"
      "v_mul_f32_dpp v227, v130, v227 row_newbcast:12 row_mask:0xf bank_mask:0xf\n"
      "v_cvt_pk_bf16_f32 v140, v64, v65\n"
      "v_mfma_f32_16x16x32_bf16 v[252:255], v[180:183], a[104:107], v[252:255]\n"
      "v_cvt_pk_bf16_f32 v137, v250, v251\n"
      "v_cvt_pk_bf16_f32 v205, v46, v47\n"
      "v_mfma_f32_16x16x32_bf16 v[252:255], v[116:119], a[108:111], v[252:255]\n"
      "v_cvt_pk_bf16_f32 v64, v112, v113\n"
      "v_mfma_f32_16x16x32_bf16 v[248:251], a[48:51], v[124:127], 0\n"
      "ds_read_b128 v[192:195], v0 offset:12288\n"
      "v_cvt_pk_bf16_f32 v69, v122, v123\n"
      "v_mfma_f32_16x16x32_bf16 v[248:251], a[52:55], v[100:103], v[248:251]\n"
      "v_pk_max_i16 v42, v42, 0\n"
      "v_mfma_f32_16x16x32_bf16 v[248:251], a[56:59], v[72:75], v[248:251]\n"
      "v_mfma_f32_16x16x32_bf16 v[248:251], a[60:63], v[196:199], v[248:251]\n"
      "v_cvt_pk_bf16_f32 v234, v28, v29\n"
      "v_mul_f32_dpp v222, v130, v222 row_newbcast:8 row_mask:0xf bank_mask:0xf\n"
      "v_mfma_f32_16x16x32_bf16 v[32:35], v[136:139], v[240:243], 0\n"
      "v_mfma_f32_16x16x32_bf16 v[32:35], v[140:143], v[244:247], v[32:35]\n"
      "v_cvt_pk_bf16_f32 v210, v154, v155\n"
      "v_cvt_pk_bf16_f32 v206, v148, v149\n"
      "v_mfma_f32_16x16x32_bf16 v[216:219], v[144:147], a[208:211], v[216:219]\n"
      "v_cvt_pk_bf16_f32 v29, v58, v59\n"
      "v_mfma_f32_16x16x32_bf16 v[216:219], v[104:107], a[212:215], v[216:219]\n"
      "v_mfma_f32_16x16x32_bf16 v[216:219], v[172:175], a[216:219], v[216:219]\n"
      "v_cvt_pk_bf16_f32 v65, v114, v115\n"
      "v_mfma_f32_16x16x32_bf16 v[216:219], v[132:135], a[220:223], v[216:219]\n"
      "v_exp_f32 v33, v33\n"
      "v_mfma_f32_16x16x32_bf16 v[212:215], v[144:147], a[192:195], v[212:215]\n"
      "v_mov_b64 v[120:121], 0\n"
      "v_mfma_f32_16x16x32_bf16 v[212:215], v[104:107], a[196:199], v[212:215]\n"
      "v_cvt_pk_bf16_f32 v70, v248, v249\n"
      "v_cvt_pk_bf16_f32 v209, v50, v51\n"
      "v_mfma_f32_16x16x32_bf16 v[212:215], v[172:175], a[200:203], v[212:215]\n"
      "v_exp_f32 v34, v34\n"
      "v_mfma_f32_16x16x32_bf16 v[212:215], v[132:135], a[204:207], v[212:215]\n"
      "buffer_load_dwordx4 v1, s[40:43], s75 offen offset:0 lds\n"
      "v_mfma_f32_16x16x32_bf16 v[220:223], v[144:147], a[224:227], v[220:223]\n"
      "v_mfma_f32_16x16x32_bf16 v[220:223], v[104:107], a[228:231], v[220:223]\n"
      "ds_read_b128 v[48:51], v0 offset:13824\n"
      "v_cvt_pk_bf16_f32 v28, v56, v57\n"
      "v_mfma_f32_16x16x32_bf16 v[220:223], v[172:175], a[232:235], v[220:223]\n"
      "v_cvt_pk_bf16_f32 v204, v44, v45\n"
      "v_mov_b64 v[248:249], 0\n"
      "v_mfma_f32_16x16x32_bf16 v[220:223], v[132:135], a[236:239], v[220:223]\n"
      "v_mfma_f32_16x16x32_bf16 v[224:227], v[144:147], a[240:243], v[224:227]\n"
      "s_waitcnt vmcnt(13)\n"
      "ds_read_b128 v[44:47], v0 offset:0\n"
      "ds_read_b128 v[148:151], v0 offset:1024\n"
      "v_mfma_f32_16x16x32_bf16 v[224:227], v[104:107], a[244:247], v[224:227]\n"
      "ds_read_b128 v[200:203], v0 offset:12800\n"
      "v_cvt_pk_bf16_f32 v214, v214, v218\n"
      "v_mfma_f32_16x16x32_bf16 v[224:227], v[172:175], a[248:251], v[224:227]\n"
      "v_cvt_pk_bf16_f32 v113, v78, v79\n"
      "v_cvt_pk_bf16_f32 v212, v212, v216\n"
      "v_mfma_f32_16x16x32_bf16 v[224:227], v[132:135], a[252:255], v[224:227]\n"
      "v_cvt_pk_bf16_f32 v71, v250, v251\n"
      "s_waitcnt lgkmcnt(5)\n"
      "v_smfmac_f32_16x16x64_bf16 v[230:233], v[52:55], v[164:171], v5\n"
      "ds_read_b128 v[164:167], v0 offset:1536\n"
      "v_cvt_pk_bf16_f32 v218, v215, v219\n"
      "v_smfmac_f32_16x16x64_bf16 v[248:251], v[52:55], v[20:27], v5\n"
      "buffer_store_dwordx2 v[188:189], v2, s[44:47], s101 offen offset:0 nt sc1\n"
      "v_smfmac_f32_16x16x64_bf16 v[120:123], v[52:55], v[204:211], v5\n"
      "ds_read_b128 v[104:107], v0 offset:13312\n"
      "v_cvt_pk_bf16_f32 v59, v254, v255\n"
      "v_mfma_f32_16x16x32_bf16 v[132:135], v[80:83], a[64:67], 0\n"
      "v_mfma_f32_16x16x32_bf16 v[132:135], v[108:111], a[68:71], v[132:135]\n"
      "ds_read_b128 v[168:171], v0 offset:512\n"
      "v_add_f32 v230, v230, v231\n"
      "v_mfma_f32_16x16x32_bf16 v[132:135], v[180:183], a[72:75], v[132:135]\n"
      "v_mfma_f32_16x16x32_bf16 v[132:135], v[116:119], a[76:79], v[132:135]\n"
      "v_cvt_pk_bf16_f32 v216, v213, v217\n"
      "v_cvt_pk_bf16_f32 v215, v222, v226\n"
      "v_mfma_f32_16x16x32_bf16 v[154:157], v[80:83], a[80:83], 0\n"
      "v_pk_max_i16 v39, v39, 0\n"
      "v_mov_b64 v[144:145], 0\n"
      "v_mfma_f32_16x16x32_bf16 v[154:157], v[108:111], a[84:87], v[154:157]\n"
      "v_cvt_pk_bf16_f32 v217, v221, v225\n"
      "v_mfma_f32_16x16x32_bf16 v[154:157], v[180:183], a[88:91], v[154:157]\n"
      "v_cvt_pk_bf16_f32 v111, v134, v135\n"
      "v_cvt_pk_bf16_f32 v219, v223, v227\n"
      "v_mfma_f32_16x16x32_bf16 v[154:157], v[116:119], a[92:95], v[154:157]\n"
      "v_pk_max_i16 v37, v37, 0\n"
      "v_cvt_pk_bf16_f32 v112, v76, v77\n"
      "v_mfma_f32_16x16x32_bf16 v[80:83], v[64:67], v[240:243], 0\n"
      "v_maximum3_f32 v8, v8, v128, v129\n"
      "v_pk_max_i16 v43, v43, 0\n"
      "v_mfma_f32_16x16x32_bf16 v[80:83], v[68:71], v[244:247], v[80:83]\n"
      "v_pk_max_i16 v219, v219, 0\n"
      "s_waitcnt lgkmcnt(5)\n"
      "v_mfma_f32_16x16x32_bf16 v[172:175], v[44:47], a[80:83], 0\n"
      "s_waitcnt lgkmcnt(0)\n"
      "v_mfma_f32_16x16x32_bf16 v[172:175], v[168:171], a[84:87], v[172:175]\n"
      "v_cvt_pk_bf16_f32 v213, v220, v224\n"
      "v_mfma_f32_16x16x32_bf16 v[172:175], v[148:151], a[88:91], v[172:175]\n"
      "v_mfma_f32_16x16x32_bf16 v[172:175], v[164:167], a[92:95], v[172:175]\n"
      "buffer_store_dwordx2 v[42:43], v2, s[44:47], s34 offen offset:768 nt sc1\n"
      "v_add_f32 v248, v248, v249\n"
      "v_mfma_f32_16x16x32_bf16 v[76:79], v[44:47], a[112:115], 0\n"
      "v_exp_f32 v80, v80\n"
      "v_mfma_f32_16x16x32_bf16 v[76:79], v[168:171], a[116:119], v[76:79]\n"
      "v_cvt_pk_bf16_f32 v182, v154, v155\n"
      "v_pk_max_i16 v212, v212, 0\n"
      "v_mfma_f32_16x16x32_bf16 v[76:79], v[148:151], a[120:123], v[76:79]\n"
      "v_exp_f32 v82, v82\n"
      "v_mfma_f32_16x16x32_bf16 v[76:79], v[164:167], a[124:127], v[76:79]\n"
      "v_cvt_pk_bf16_f32 v180, v172, v173\n"
      "v_mov_b64 v[146:147], 0\n"
      "v_mfma_f32_16x16x32_bf16 v[224:227], v[44:47], a[96:99], 0\n"
      "v_mfma_f32_16x16x32_bf16 v[224:227], v[168:171], a[100:103], v[224:227]\n"
      "buffer_load_dwordx4 v1, s[40:43], s75 offen offset:2048 lds\n"
      "v_mfma_f32_16x16x32_bf16 v[224:227], v[148:151], a[104:107], v[224:227]\n"
      "v_cvt_pk_bf16_f32 v110, v132, v133\n"
      "v_mfma_f32_16x16x32_bf16 v[224:227], v[164:167], a[108:111], v[224:227]\n"
      "v_mov_b64 v[116:117], 0\n"
      "v_cvt_pk_bf16_f32 v232, v76, v77\n"
      "v_smfmac_f32_16x16x64_bf16 v[152:155], v[52:55], v[64:71], v5\n"
      "v_cvt_pk_bf16_f32 v233, v78, v79\n"
      "v_smfmac_f32_16x16x64_bf16 v[116:119], v[52:55], v[136:143], v5\n"
      "v_mov_b32 v231, 0\n"
      "v_mfma_f32_16x16x32_bf16 v[132:135], v[44:47], a[64:67], 0\n"
      "v_exp_f32 v248, v248\n"
      "v_mfma_f32_16x16x32_bf16 v[132:135], v[168:171], a[68:71], v[132:135]\n"
      "v_exp_f32 v81, v81\n"
      "v_mfma_f32_16x16x32_bf16 v[132:135], v[148:151], a[72:75], v[132:135]\n"
      "v_exp_f32_dpp v231, v230 quad_perm:[0,1,2,3] row_mask:0xf bank_mask:0x1\n"
      "v_mfma_f32_16x16x32_bf16 v[132:135], v[164:167], a[76:79], v[132:135]\n"
      "v_exp_f32 v32, v32\n"
      "v_mfma_f32_16x16x32_bf16 v[76:79], a[32:35], v[44:47], 0\n"
      "v_cvt_pk_bf16_f32 v58, v252, v253\n"
      "v_mfma_f32_16x16x32_bf16 v[76:79], a[36:39], v[168:171], v[76:79]\n"
      "v_cvt_pk_bf16_f32 v183, v156, v157\n"
      "v_mfma_f32_16x16x32_bf16 v[76:79], a[40:43], v[148:151], v[76:79]\n"
      "v_mfma_f32_16x16x32_bf16 v[76:79], a[44:47], v[164:167], v[76:79]\n"
      "buffer_store_dwordx2 v[40:41], v2, s[44:47], s34 offen offset:256 nt sc1\n"
      "v_mov_b32_dpp v230, v231 quad_perm:[1,1,3,3] row_mask:0xf bank_mask:0xf\n"
      "v_mfma_f32_16x16x32_bf16 v[252:255], a[16:19], v[44:47], 0\n"
      "v_cvt_pk_bf16_f32 v128, v80, v81\n"
      "v_add_f32 v152, v152, v153\n"
      "v_mfma_f32_16x16x32_bf16 v[252:255], a[20:23], v[168:171], v[252:255]\n"
      "v_add_f32 v116, v116, v117\n"
      "v_add_f32 v120, v120, v121\n"
      "v_mfma_f32_16x16x32_bf16 v[252:255], a[24:27], v[148:151], v[252:255]\n"
      "v_cvt_pk_bf16_f32 v109, v134, v135\n"
      "v_pk_max_i16 v217, v217, 0\n"
      "v_mfma_f32_16x16x32_bf16 v[252:255], a[28:31], v[164:167], v[252:255]\n"
      "v_exp_f32 v35, v35\n"
      "v_mfma_f32_16x16x32_bf16 v[220:223], v[204:207], v[240:243], 0\n"
      "v_cvt_pk_bf16_f32 v57, v226, v227\n"
      "v_cvt_pk_bf16_f32 v230, v231, v230\n"
      "v_mfma_f32_16x16x32_bf16 v[220:223], v[208:211], v[244:247], v[220:223]\n"
      "v_exp_f32 v83, v83\n"
      "v_mfma_f32_16x16x32_bf16 v[154:157], a[48:51], v[44:47], 0\n"
      "v_exp_f32 v116, v116\n"
      "v_mfma_f32_16x16x32_bf16 v[154:157], a[52:55], v[168:171], v[154:157]\n"
      "v_cvt_pk_bf16_f32 v181, v174, v175\n"
      "v_pk_max_i16 v218, v218, 0\n"
      "v_mfma_f32_16x16x32_bf16 v[154:157], a[56:59], v[148:151], v[154:157]\n"
      "v_cvt_pk_bf16_f32 v108, v132, v133\n"
      "v_cvt_pk_bf16_f32 v130, v32, v33\n"
      "v_mfma_f32_16x16x32_bf16 v[154:157], a[60:63], v[164:167], v[154:157]\n"
      "v_exp_f32 v222, v222\n"
      "v_mfma_f32_16x16x32_bf16 v[132:135], a[0:3], v[44:47], 0\n"
      "v_mov_b64 v[172:173], 0\n"
      "v_pk_max_i16 v214, v214, 0\n"
      "v_mfma_f32_16x16x32_bf16 v[132:135], a[4:7], v[168:171], v[132:135]\n"
      "v_cvt_pk_bf16_f32 v56, v224, v225\n"
      "v_mov_b64 v[224:225], 0\n"
      "v_mfma_f32_16x16x32_bf16 v[132:135], a[8:11], v[148:151], v[132:135]\n"
      "v_exp_f32 v223, v223\n"
      "v_mfma_f32_16x16x32_bf16 v[132:135], a[12:15], v[164:167], v[132:135]\n"
      "v_exp_f32 v152, v152\n"
      "v_mfma_f32_16x16x32_bf16 v[40:43], a[48:51], v[192:195], 0\n"
      "v_cvt_pk_bf16_f32 v169, v78, v79\n"
      "v_cndmask_b32_dpp v153, v116, v152, vcc quad_perm:[0,0,2,2] row_mask:0xf bank_mask:0xf\n"
      "v_mfma_f32_16x16x32_bf16 v[40:43], a[52:55], v[200:203], v[40:43]\n"
      "v_cvt_pk_bf16_f32 v171, v156, v157\n"
      "v_cvt_pk_bf16_f32 v166, v252, v253\n"
      "v_mfma_f32_16x16x32_bf16 v[40:43], a[56:59], v[104:107], v[40:43]\n"
      "v_cvt_pk_bf16_f32 v131, v34, v35\n"
      "v_cvt_pk_bf16_f32 v170, v154, v155\n"
      "v_mfma_f32_16x16x32_bf16 v[40:43], a[60:63], v[48:51], v[40:43]\n"
      "v_cvt_pk_bf16_f32 v129, v82, v83\n"
      "v_cvt_pk_bf16_f32 v165, v134, v135\n"
      "v_mfma_f32_16x16x32_bf16 v[148:151], a[0:3], v[192:195], 0\n"
      "v_mfma_f32_16x16x32_bf16 v[148:151], a[4:7], v[200:203], v[148:151]\n"
      "v_mov_b32_dpp v44, v160 row_ror:8 row_mask:0xf bank_mask:0xf\n"
      "v_mfma_f32_16x16x32_bf16 v[148:151], a[8:11], v[104:107], v[148:151]\n"
      "v_exp_f32 v221, v221\n"
      "v_mfma_f32_16x16x32_bf16 v[148:151], a[12:15], v[48:51], v[148:151]\n"
      "buffer_store_dwordx2 v[36:37], v2, s[44:47], s34 offen offset:0 nt sc1\n"
      "v_pk_max_i16 v213, v213, 0\n"
      "v_mfma_f32_16x16x32_bf16 v[32:35], a[16:19], v[192:195], 0\n"
      "v_cvt_pk_bf16_f32 v82, v40, v41\n"
      "v_cvt_pk_bf16_f32 v167, v254, v255\n"
      "v_mfma_f32_16x16x32_bf16 v[32:35], a[20:23], v[200:203], v[32:35]\n"
      "v_cvt_pk_bf16_f32 v168, v76, v77\n"
      "v_cvt_pk_bf16_f32 v164, v132, v133\n"
      "v_mfma_f32_16x16x32_bf16 v[32:35], a[24:27], v[104:107], v[32:35]\n"
      "buffer_load_dwordx4 v1, s[40:43], s76 offen offset:1024 lds\n"
      "v_mfma_f32_16x16x32_bf16 v[32:35], a[28:31], v[48:51], v[32:35]\n"
      "v_cvt_pk_bf16_f32 v76, v148, v149\n"
      "v_pk_max_i16 v216, v216, 0\n"
      "v_mfma_f32_16x16x32_bf16 v[132:135], a[32:35], v[192:195], 0\n"
      "v_exp_f32 v120, v120\n"
      "v_mfma_f32_16x16x32_bf16 v[132:135], a[36:39], v[200:203], v[132:135]\n"
      "v_mov_b32_dpp v45, v161 row_ror:8 row_mask:0xf bank_mask:0xf\n"
      "v_cvt_pk_bf16_f32 v251, v222, v223\n"
      "v_mfma_f32_16x16x32_bf16 v[132:135], a[40:43], v[104:107], v[132:135]\n"
      "v_exp_f32 v220, v220\n"
      "v_mfma_f32_16x16x32_bf16 v[132:135], a[44:47], v[48:51], v[132:135]\n"
      "v_cvt_pk_bf16_f32 v77, v150, v151\n"
      "s_add_u32 m0, s53, 12288\n"
      "v_smfmac_f32_16x16x64_bf16 v[224:227], v[52:55], v[164:171], v5\n"
      "v_cvt_pk_bf16_f32 v78, v32, v33\n"
      "v_mfma_f32_16x16x32_bf16 v[252:255], v[164:167], v[240:243], 0\n"
      "v_cvt_pk_bf16_f32 v83, v42, v43\n"
      "v_pk_max_i16 v215, v215, 0\n"
      "v_mfma_f32_16x16x32_bf16 v[252:255], v[168:171], v[244:247], v[252:255]\n"
      "v_cvt_pk_bf16_f32 v79, v34, v35\n"
      "v_cvt_pk_bf16_f32 v81, v134, v135\n"
      "v_mfma_f32_16x16x32_bf16 v[40:43], v[20:23], v[240:243], 0\n"
      "v_add_f32 v224, v224, v225\n"
      "v_cvt_pk_bf16_f32 v80, v132, v133\n"
      "v_mfma_f32_16x16x32_bf16 v[40:43], v[24:27], v[244:247], v[40:43]\n"
      "v_exp_f32 v224, v224\n"
      "v_smfmac_f32_16x16x64_bf16 v[172:175], v[52:55], v[76:83], v5\n"
      "buffer_load_dwordx4 v1, s[40:43], s77 offen offset:0 lds\n"
      "v_mfma_f32_16x16x32_bf16 v[52:55], a[176:179], v[124:127], 0\n"
      "v_exp_f32 v253, v253\n"
      "v_mfma_f32_16x16x32_bf16 v[52:55], a[180:183], v[100:103], v[52:55]\n"
      "v_cvt_pk_bf16_f32 v250, v220, v221\n"
      "v_mov_b32_dpp v47, v163 row_ror:8 row_mask:0xf bank_mask:0xf\n"
      "v_mfma_f32_16x16x32_bf16 v[52:55], a[184:187], v[72:75], v[52:55]\n"
      "v_exp_f32 v252, v252\n"
      "v_mfma_f32_16x16x32_bf16 v[52:55], a[188:191], v[196:199], v[52:55]\n"
      "v_exp_f32 v41, v41\n"
      "v_mfma_f32_16x16x32_bf16 v[220:223], a[160:163], v[124:127], 0\n"
      "v_exp_f32 v254, v254\n"
      "v_mfma_f32_16x16x32_bf16 v[220:223], a[164:167], v[100:103], v[220:223]\n"
      "v_add_f32 v172, v172, v173\n"
      "v_cndmask_b32_dpp v225, v248, v224, vcc quad_perm:[0,0,2,2] row_mask:0xf bank_mask:0xf\n"
      "v_mfma_f32_16x16x32_bf16 v[220:223], a[168:171], v[72:75], v[220:223]\n"
      "v_exp_f32 v43, v43\n"
      "v_mfma_f32_16x16x32_bf16 v[220:223], a[172:175], v[196:199], v[220:223]\n"
      "v_cvt_pk_bf16_f32 v35, v54, v55\n"
      "v_mfma_f32_16x16x32_bf16 v[132:135], a[144:147], v[124:127], 0\n"
      "v_exp_f32 v172, v172\n"
      "v_mfma_f32_16x16x32_bf16 v[132:135], a[148:151], v[100:103], v[132:135]\n"
      "v_cvt_pk_bf16_f32 v34, v52, v53\n"
      "v_cndmask_b32_dpp v121, v172, v120, vcc quad_perm:[0,0,2,2] row_mask:0xf bank_mask:0xf\n"
      "v_mfma_f32_16x16x32_bf16 v[132:135], a[152:155], v[72:75], v[132:135]\n"
      "s_not_b64 vcc, vcc\n"
      "v_cndmask_b32_dpp v249, v224, v248, vcc quad_perm:[1,1,3,3] row_mask:0xf bank_mask:0xf\n"
      "v_add_f32 v224, v224, v120\n"
      "v_mfma_f32_16x16x32_bf16 v[132:135], a[156:159], v[196:199], v[132:135]\n"
      "v_add_f32 v248, v248, v172\n"
      "v_cndmask_b32_dpp v117, v152, v116, vcc quad_perm:[1,1,3,3] row_mask:0xf bank_mask:0xf\n"
      "v_mfma_f32_16x16x32_bf16 v[52:55], v[76:79], v[240:243], 0\n"
      "v_cvt_pk_bf16_f32 v153, v153, v117\n"
      "v_cvt_pk_bf16_f32 v32, v220, v221\n"
      "v_mfma_f32_16x16x32_bf16 v[52:55], v[80:83], v[244:247], v[52:55]\n"
      "buffer_store_dwordx2 v[38:39], v2, s[44:47], s34 offen offset:512 nt sc1\n"
      "v_add_f32 v152, v152, v116\n"
      "v_mfma_f32_16x16x32_bf16 v[116:119], v[192:195], a[96:99], 0\n"
      "v_add_f32 v224, v224, v248\n"
      "v_cvt_pk_bf16_f32 v225, v225, v249\n"
      "v_mfma_f32_16x16x32_bf16 v[116:119], v[200:203], a[100:103], v[116:119]\n"
      "v_add_f32 v152, v152, v231\n"
      "v_cvt_pk_bf16_f32 v30, v132, v133\n"
      "v_mfma_f32_16x16x32_bf16 v[116:119], v[104:107], a[104:107], v[116:119]\n"
      "v_cvt_pk_bf16_f32 v33, v222, v223\n"
      "v_cvt_pk_bf16_f32 v31, v134, v135\n"
      "v_mfma_f32_16x16x32_bf16 v[116:119], v[48:51], a[108:111], v[116:119]\n"
      "ds_bpermute_b32 v174, v6, v225 offset:16\n"
      "v_mfma_f32_16x16x32_bf16 v[220:223], v[20:23], v[28:31], 0\n"
      "v_exp_f32 v54, v54\n"
      "v_mfma_f32_16x16x32_bf16 v[220:223], v[24:27], v[32:35], v[220:223]\n"
      "v_cvt_pk_bf16_f32 v248, v252, v253\n"
      "v_cndmask_b32_dpp v173, v120, v172, vcc quad_perm:[1,1,3,3] row_mask:0xf bank_mask:0xf\n"
      "v_mfma_f32_16x16x32_bf16 v[36:39], v[76:79], v[28:31], 0\n"
      "v_cvt_pk_bf16_f32 v121, v121, v173\n"
      "v_add_f32 v18, v224, v152\n"
      "v_mfma_f32_16x16x32_bf16 v[36:39], v[80:83], v[32:35], v[36:39]\n"
      "ds_bpermute_b32 v173, v6, v121\n"
      "v_mfma_f32_16x16x32_bf16 v[132:135], v[192:195], a[80:83], 0\n"
      "v_exp_f32 v40, v40\n"
      "v_mfma_f32_16x16x32_bf16 v[132:135], v[200:203], a[84:87], v[132:135]\n"
      "v_exp_f32 v52, v52\n"
      "v_mfma_f32_16x16x32_bf16 v[132:135], v[104:107], a[88:91], v[132:135]\n"
      "v_add_f32_dpp v18, v18, v18 row_ror:8 row_mask:0xf bank_mask:0xf\n"
      "v_mov_b32_dpp v46, v162 row_ror:8 row_mask:0xf bank_mask:0xf\n"
      "v_mfma_f32_16x16x32_bf16 v[132:135], v[48:51], a[92:95], v[132:135]\n"
      "v_exp_f32 v42, v42\n"
      "v_smfmac_f32_16x16x64_bf16 v[144:147], v[160:163], v[240:247], v5\n"
      "ds_bpermute_b32 v158, v7, v230 offset:248\n"
      "v_smfmac_f32_16x16x64_bf16 v[144:147], v[44:47], v[28:35], v5\n"
      "ds_bpermute_b32 v175, v6, v121 offset:16\n"
      "v_mfma_f32_16x16x32_bf16 v[120:123], v[204:207], v[28:31], 0\n"
      "v_exp_f32 v39, v39\n"
      "v_mfma_f32_16x16x32_bf16 v[120:123], v[208:211], v[32:35], v[120:123]\n"
      "v_exp_f32 v223, v223\n"
      "v_mfma_f32_16x16x32_bf16 v[148:151], v[164:167], v[28:31], 0\n"
      "v_exp_f32 v255, v255\n"
      "v_mfma_f32_16x16x32_bf16 v[148:151], v[168:171], v[32:35], v[148:151]\n"
      "v_cvt_pk_bf16_f32 v187, v134, v135\n"
      "v_cvt_pk_bf16_f32 v186, v132, v133\n"
      "v_mfma_f32_16x16x32_bf16 v[244:247], v[64:67], v[28:31], 0\n"
      "buffer_load_dwordx4 v1, s[40:43], s78 offen offset:1024 lds\n"
      "v_mfma_f32_16x16x32_bf16 v[244:247], v[68:71], v[32:35], v[244:247]\n"
      "buffer_store_dwordx2 v[218:219], v2, s[44:47], s35 offen offset:768 nt sc1\n"
      "v_cvt_pk_bf16_f32 v249, v254, v255\n"
      "v_mfma_f32_16x16x32_bf16 v[132:135], v[92:95], a[64:67], 0\n"
      "v_exp_f32 v151, v151\n"
      "v_mfma_f32_16x16x32_bf16 v[132:135], v[88:91], a[68:71], v[132:135]\n"
      "v_exp_f32 v149, v149\n"
      "v_mfma_f32_16x16x32_bf16 v[132:135], v[96:99], a[72:75], v[132:135]\n"
      "v_exp_f32 v122, v122\n"
      "v_mfma_f32_16x16x32_bf16 v[132:135], v[176:179], a[76:79], v[132:135]\n"
      "v_exp_f32 v53, v53\n"
      "v_mfma_f32_16x16x32_bf16 v[240:243], v[136:139], v[28:31], 0\n"
      "v_exp_f32 v245, v245\n"
      "v_mfma_f32_16x16x32_bf16 v[240:243], v[140:143], v[32:35], v[240:243]\n"
      "v_cvt_pk_bf16_f32 v253, v42, v43\n"
      "v_cvt_pk_bf16_f32 v62, v116, v117\n"
      "v_mfma_f32_16x16x32_bf16 v[32:35], v[192:195], a[112:115], 0\n"
      "v_exp_f32 v55, v55\n"
      "v_mfma_f32_16x16x32_bf16 v[32:35], v[200:203], a[116:119], v[32:35]\n"
      "ds_bpermute_b32 v159, v7, v230 offset:8\n"
      "v_mfma_f32_16x16x32_bf16 v[32:35], v[104:107], a[120:123], v[32:35]\n"
      "v_cvt_pk_bf16_f32 v254, v52, v53\n"
      "v_cvt_pk_bf16_f32 v63, v118, v119\n"
      "v_mfma_f32_16x16x32_bf16 v[32:35], v[48:51], a[124:127], v[32:35]\n"
      "v_exp_f32 v120, v120\n"
      "v_mfma_f32_16x16x32_bf16 v[28:31], v[192:195], a[64:67], 0\n"
      "v_exp_f32 v222, v222\n"
      "v_mfma_f32_16x16x32_bf16 v[28:31], v[200:203], a[68:71], v[28:31]\n"
      "v_exp_f32 v150, v150\n"
      "v_mfma_f32_16x16x32_bf16 v[28:31], v[104:107], a[72:75], v[28:31]\n"
      "v_exp_f32 v246, v246\n"
      "v_mfma_f32_16x16x32_bf16 v[28:31], v[48:51], a[76:79], v[28:31]\n"
      "ds_bpermute_b32 v172, v6, v225\n"
      "v_mfma_f32_16x16x32_bf16 v[116:119], v[124:127], a[64:67], 0\n"
      "v_exp_f32 v38, v38\n"
      "v_mfma_f32_16x16x32_bf16 v[116:119], v[100:103], a[68:71], v[116:119]\n"
      "v_exp_f32 v123, v123\n"
      "v_mfma_f32_16x16x32_bf16 v[116:119], v[72:75], a[72:75], v[116:119]\n"
      "v_exp_f32 v148, v148\n"
      "v_mfma_f32_16x16x32_bf16 v[116:119], v[196:199], a[76:79], v[116:119]\n"
      "v_exp_f32 v121, v121\n"
      "v_mfma_f32_16x16x32_bf16 v[224:227], v[124:127], a[112:115], 0\n"
      "v_cvt_pk_bf16_f32 v115, v30, v31\n"
      "v_cvt_pk_bf16_f32 v114, v28, v29\n"
      "v_mfma_f32_16x16x32_bf16 v[224:227], v[100:103], a[116:119], v[224:227]\n"
      "v_cvt_pk_bf16_f32 v29, v150, v151\n"
      "v_cvt_pk_bf16_f32 v255, v54, v55\n"
      "v_mfma_f32_16x16x32_bf16 v[224:227], v[72:75], a[120:123], v[224:227]\n"
      "v_exp_f32 v221, v221\n"
      "v_mfma_f32_16x16x32_bf16 v[224:227], v[196:199], a[124:127], v[224:227]\n"
      "buffer_store_dwordx2 v[216:217], v2, s[44:47], s35 offen offset:256 nt sc1\n"
      "v_add_f32 v146, v146, v147\n"
      "v_mfma_f32_16x16x32_bf16 v[154:157], v[124:127], a[80:83], 0\n"
      "v_cvt_pk_bf16_f32 v28, v148, v149\n"
      "v_cvt_pk_bf16_f32 v30, v120, v121\n"
      "v_mfma_f32_16x16x32_bf16 v[154:157], v[100:103], a[84:87], v[154:157]\n"
      "v_exp_f32 v241, v241\n"
      "v_mfma_f32_16x16x32_bf16 v[154:157], v[72:75], a[88:91], v[154:157]\n"
      "v_exp_f32 v242, v242\n"
      "v_mfma_f32_16x16x32_bf16 v[154:157], v[196:199], a[92:95], v[154:157]\n"
      "v_cvt_pk_bf16_f32 v229, v226, v227\n"
      "v_cvt_pk_bf16_f32 v252, v40, v41\n"
      "v_mfma_f32_16x16x32_bf16 v[40:43], v[92:95], a[112:115], 0\n"
      "v_exp_f32 v247, v247\n"
      "v_mfma_f32_16x16x32_bf16 v[40:43], v[88:91], a[116:119], v[40:43]\n"
      "v_exp_f32 v36, v36\n"
      "v_mfma_f32_16x16x32_bf16 v[40:43], v[96:99], a[120:123], v[40:43]\n"
      "v_exp_f32 v244, v244\n"
      "v_mfma_f32_16x16x32_bf16 v[40:43], v[176:179], a[124:127], v[40:43]\n"
      "v_exp_f32 v37, v37\n"
      "v_mfma_f32_16x16x32_bf16 v[148:151], v[124:127], a[96:99], 0\n"
      "v_cvt_pk_bf16_f32 v31, v122, v123\n"
      "v_cvt_pk_bf16_f32 v52, v116, v117\n"
      "v_mfma_f32_16x16x32_bf16 v[148:151], v[100:103], a[100:103], v[148:151]\n"
      "v_cvt_pk_bf16_f32 v239, v34, v35\n"
      "v_cvt_pk_bf16_f32 v228, v224, v225\n"
      "v_mfma_f32_16x16x32_bf16 v[148:151], v[72:75], a[104:107], v[148:151]\n"
      "v_exp_f32 v243, v243\n"
      "v_mfma_f32_16x16x32_bf16 v[148:151], v[196:199], a[108:111], v[148:151]\n"
      "v_cvt_pk_bf16_f32 v55, v134, v135\n"
      "v_cvt_pk_bf16_f32 v54, v132, v133\n"
      "v_mfma_f32_16x16x32_bf16 v[216:219], a[176:179], v[192:195], 0\n"
      "v_cvt_pk_bf16_f32 v53, v118, v119\n"
      "v_cvt_pk_bf16_f32 v230, v40, v41\n"
      "v_mfma_f32_16x16x32_bf16 v[216:219], a[180:183], v[200:203], v[216:219]\n"
      "v_cvt_pk_bf16_f32 v121, v156, v157\n"
      "v_cvt_pk_bf16_f32 v120, v154, v155\n"
      "v_mfma_f32_16x16x32_bf16 v[216:219], a[184:187], v[104:107], v[216:219]\n"
      "v_exp_f32 v220, v220\n"
      "v_mfma_f32_16x16x32_bf16 v[216:219], a[188:191], v[48:51], v[216:219]\n"
      "v_exp_f32 v240, v240\n"
      "v_mfma_f32_16x16x32_bf16 v[224:227], v[92:95], a[96:99], 0\n"
      "v_cvt_pk_bf16_f32 v134, v240, v241\n"
      "v_mfma_f32_16x16x32_bf16 v[224:227], v[88:91], a[100:103], v[224:227]\n"
      "v_cvt_pk_bf16_f32 v135, v242, v243\n"
      "v_cvt_pk_bf16_f32 v231, v42, v43\n"
      "v_mfma_f32_16x16x32_bf16 v[224:227], v[96:99], a[104:107], v[224:227]\n"
      "v_cvt_pk_bf16_f32 v132, v244, v245\n"
      "v_cvt_pk_bf16_f32 v42, v216, v217\n"
      "v_mfma_f32_16x16x32_bf16 v[224:227], v[176:179], a[108:111], v[224:227]\n"
      "buffer_store_dwordx2 v[212:213], v2, s[44:47], s35 offen offset:0 nt sc1\n"
      "v_cvt_pk_bf16_f32 v133, v246, v247\n"
      "v_mfma_f32_16x16x32_bf16 v[240:243], a[144:147], v[192:195], 0\n"
      "v_cvt_pk_bf16_f32 v238, v32, v33\n"
      "v_add_f32 v144, v144, v145\n"
      "v_mfma_f32_16x16x32_bf16 v[240:243], a[148:151], v[200:203], v[240:243]\n"
      "v_exp_f32 v144, v144\n"
      "v_mfma_f32_16x16x32_bf16 v[240:243], a[152:155], v[104:107], v[240:243]\n"
      "v_exp_f32 v146, v146\n"
      "v_mfma_f32_16x16x32_bf16 v[240:243], a[156:159], v[48:51], v[240:243]\n"
      "v_cvt_pk_bf16_f32 v35, v38, v39\n"
      "v_cvt_pk_bf16_f32 v34, v36, v37\n"
      "v_mfma_f32_16x16x32_bf16 v[244:247], v[92:95], a[80:83], 0\n"
      "v_cvt_pk_bf16_f32 v43, v218, v219\n"
      "v_cvt_pk_bf16_f32 v117, v150, v151\n"
      "v_mfma_f32_16x16x32_bf16 v[244:247], v[88:91], a[84:87], v[244:247]\n"
      "ds_read_b64 v[156:157], v11 offset:0\n"
      "v_cvt_pk_bf16_f32 v116, v148, v149\n"
      "v_mfma_f32_16x16x32_bf16 v[244:247], v[96:99], a[88:91], v[244:247]\n"
      "v_cvt_pk_bf16_f32 v149, v144, v146\n"
      "v_cvt_pk_bf16_f32 v119, v226, v227\n"
      "v_mfma_f32_16x16x32_bf16 v[244:247], v[176:179], a[92:95], v[244:247]\n"
      "v_cvt_pk_bf16_f32 v39, v242, v243\n"
      "v_cvt_pk_bf16_f32 v118, v224, v225\n"
      "v_mfma_f32_16x16x32_bf16 v[224:227], a[128:131], v[192:195], 0\n"
      "v_cvt_pk_bf16_f32 v148, v144, v146\n"
      "v_cvt_pk_bf16_f32 v38, v240, v241\n"
      "v_mfma_f32_16x16x32_bf16 v[224:227], a[132:135], v[200:203], v[224:227]\n"
      "ds_bpermute_b32 v145, v7, v153 offset:4\n"
      "v_mfma_f32_16x16x32_bf16 v[224:227], a[136:139], v[104:107], v[224:227]\n"
      "v_permlane16_swap_b32 v148, v149\n"
      "v_mfma_f32_16x16x32_bf16 v[224:227], a[140:143], v[48:51], v[224:227]\n"
      "v_cvt_pk_bf16_f32 v32, v220, v221\n"
      "v_cvt_pk_bf16_f32 v33, v222, v223\n"
      "v_mfma_f32_16x16x32_bf16 v[220:223], a[160:163], v[192:195], 0\n"
      "v_cvt_pk_bf16_f32 v122, v244, v245\n"
      "v_cvt_pk_bf16_f32 v123, v246, v247\n"
      "v_mfma_f32_16x16x32_bf16 v[220:223], a[164:167], v[200:203], v[220:223]\n"
      "v_add_f32 v245, v144, v146\n"
      "v_add_f32 v244, v144, v146\n"
      "v_mfma_f32_16x16x32_bf16 v[220:223], a[168:171], v[104:107], v[220:223]\n"
      "s_nop 0\n"
      "v_permlane16_swap_b32 v244, v245\n"
      "v_mfma_f32_16x16x32_bf16 v[220:223], a[172:175], v[48:51], v[220:223]\n"
      "v_cvt_pk_bf16_f32 v36, v224, v225\n"
      "v_mov_b64 v[188:189], v[244:245]\n"
      "v_smfmac_f32_16x16x64_bf16 v[244:247], v[12:15], v[248:255], v5\n"
      "buffer_store_dwordx2 v[214:215], v2, s[44:47], s35 offen offset:512 nt sc1\n"
      "v_cvt_pk_bf16_f32 v37, v226, v227\n"
      "v_smfmac_f32_16x16x64_bf16 v[188:191], v[12:15], v[28:35], v5\n"
      "s_nop 1\n"
      "v_cvt_pk_bf16_f32 v40, v220, v221\n"
      "v_smfmac_f32_16x16x32_bf16 v[244:247], v[12:13], v[128:131], v5\n"
      "ds_bpermute_b32 v144, v7, v153\n"
      "v_cvt_pk_bf16_f32 v41, v222, v223\n"
      "s_waitcnt lgkmcnt(2)\n"
      "v_mfma_f32_16x16x4_4b_bf16 v[212:227], v[148:149], v[156:157], 0 cbsz:2 abid:0\n"
      "s_nop 3\n"
      "v_add_f32 v244, v244, v245\n"
      "v_rcp_f32 v245, v244\n"
      "v_smfmac_f32_16x16x32_bf16 v[188:191], v[12:13], v[132:135], v5\n"
      "ds_bpermute_b32 v246, v4, v245\n"
      "v_add_u32 v16, 64, v16\n"
      "s_nop 0\n"
      "v_mfma_f32_16x16x32_bf16 v[216:219], v[248:251], v[180:183], v[216:219]\n"
      "v_add_f32_dpp v18, v18, v18 row_ror:4 row_mask:0xf bank_mask:0xf\n"
      "v_mfma_f32_16x16x32_bf16 v[216:219], v[252:255], v[184:187], v[216:219]\n"
      "buffer_load_dwordx4 v1, s[40:43], s78 offen offset:3072 lds\n"
      "v_mfma_f32_16x16x32_bf16 v[216:219], v[128:131], v[120:123], v[216:219]\n"
      "v_mfma_f32_16x16x32_bf16 v[212:215], v[248:251], v[108:111], v[212:215]\n"
      "v_mfma_f32_16x16x32_bf16 v[212:215], v[252:255], v[112:115], v[212:215]\n"
      "v_add_f32 v188, v188, v189\n"
      "v_add_f32_dpp v18, v18, v18 row_ror:2 row_mask:0xf bank_mask:0xf\n"
      "v_mfma_f32_16x16x32_bf16 v[212:215], v[128:131], v[52:55], v[212:215]\n"
      "v_rcp_f32 v189, v188\n"
      "v_mfma_f32_16x16x32_bf16 v[224:227], v[248:251], v[232:235], v[224:227]\n"
      "v_mfma_f32_16x16x32_bf16 v[224:227], v[252:255], v[236:239], v[224:227]\n"
      "ds_bpermute_b32 v190, v4, v189 offset:128\n"
      "v_mfma_f32_16x16x32_bf16 v[224:227], v[128:131], v[228:231], v[224:227]\n"
      "s_waitcnt lgkmcnt(1)\n"
      "v_mul_f32_dpp v217, v246, v217 row_newbcast:4 row_mask:0xf bank_mask:0xf\n"
      "v_add_f32_dpp v18, v18, v18 row_ror:1 row_mask:0xf bank_mask:0xf\n"
      "v_mfma_f32_16x16x32_bf16 v[220:223], v[248:251], v[56:59], v[220:223]\n"
      "v_mul_f32_dpp v219, v246, v219 row_newbcast:12 row_mask:0xf bank_mask:0xf\n"
      "v_mul_f32_dpp v212, v246, v212 row_newbcast:0 row_mask:0xf bank_mask:0xf\n"
      "v_mfma_f32_16x16x32_bf16 v[220:223], v[252:255], v[60:63], v[220:223]\n"
      "v_mul_f32_dpp v213, v246, v213 row_newbcast:4 row_mask:0xf bank_mask:0xf\n"
      "v_mul_f32_dpp v218, v246, v218 row_newbcast:8 row_mask:0xf bank_mask:0xf\n"
      "v_mfma_f32_16x16x32_bf16 v[220:223], v[128:131], v[116:119], v[220:223]\n"
      "v_mul_f32_dpp v216, v246, v216 row_newbcast:0 row_mask:0xf bank_mask:0xf\n"
      "v_mfma_f32_16x16x32_bf16 v[84:87], v[64:67], v[36:39], 0\n"
      "buffer_load_dwordx4 v1, s[40:43], s77 offen offset:2048 lds\n"
      "v_mfma_f32_16x16x32_bf16 v[84:87], v[68:71], v[40:43], v[84:87]\n"
      "v_mul_f32_dpp v226, v246, v226 row_newbcast:8 row_mask:0xf bank_mask:0xf\n"
      "v_mfma_f32_16x16x32_bf16 v[216:219], v[92:95], a[208:211], v[216:219]\n"
      "v_rcp_f32 v19, v18\n"
      "v_mfma_f32_16x16x32_bf16 v[216:219], v[88:91], a[212:215], v[216:219]\n"
      "v_mul_f32_dpp v214, v246, v214 row_newbcast:8 row_mask:0xf bank_mask:0xf\n"
      "v_mfma_f32_16x16x32_bf16 v[216:219], v[96:99], a[216:219], v[216:219]\n"
      "v_mul_f32_dpp v215, v246, v215 row_newbcast:12 row_mask:0xf bank_mask:0xf\n"
      "v_mul_f32_dpp v223, v246, v223 row_newbcast:12 row_mask:0xf bank_mask:0xf\n"
      "v_mfma_f32_16x16x32_bf16 v[216:219], v[176:179], a[220:223], v[216:219]\n"
      "v_maximum3_f32 v8, v8, v18, v19\n"
      "v_mul_f32_dpp v221, v246, v221 row_newbcast:4 row_mask:0xf bank_mask:0xf\n"
      "v_mfma_f32_16x16x32_bf16 v[212:215], v[92:95], a[192:195], v[212:215]\n"
      "v_mul_f32_dpp v220, v246, v220 row_newbcast:0 row_mask:0xf bank_mask:0xf\n"
      "v_mul_f32_dpp v222, v246, v222 row_newbcast:8 row_mask:0xf bank_mask:0xf\n"
      "v_mfma_f32_16x16x32_bf16 v[212:215], v[88:91], a[196:199], v[212:215]\n"
      "ds_read_b128 v[128:131], v0 offset:8192\n"
      "v_mfma_f32_16x16x32_bf16 v[212:215], v[96:99], a[200:203], v[212:215]\n"
      "v_add_u32 v9, 64, v9\n"
      "v_maximum3_f32 v8, v8, v244, v245\n"
      "v_mfma_f32_16x16x32_bf16 v[212:215], v[176:179], a[204:207], v[212:215]\n"
      "v_mfma_f32_16x16x32_bf16 v[220:223], v[92:95], a[224:227], v[220:223]\n"
      "v_exp_f32 v87, v87\n"
      "v_mfma_f32_16x16x32_bf16 v[220:223], v[88:91], a[228:231], v[220:223]\n"
      "v_mul_f32_dpp v225, v246, v225 row_newbcast:4 row_mask:0xf bank_mask:0xf\n"
      "v_mul_f32_dpp v224, v246, v224 row_newbcast:0 row_mask:0xf bank_mask:0xf\n"
      "v_mfma_f32_16x16x32_bf16 v[220:223], v[96:99], a[232:235], v[220:223]\n"
      "v_mul_f32_dpp v227, v246, v227 row_newbcast:12 row_mask:0xf bank_mask:0xf\n"
      "v_mfma_f32_16x16x32_bf16 v[220:223], v[176:179], a[236:239], v[220:223]\n"
      "L_h4_%=:\n"
      "s_cbranch_execz L_hr_%=\n"
      "ds_read_b128 v[152:155], v0 offset:8704\n"
      "v_mfma_f32_16x16x32_bf16 v[224:227], v[92:95], a[240:243], v[224:227]\n"
      "v_exp_f32 v84, v84\n"
      "v_mfma_f32_16x16x32_bf16 v[224:227], v[88:91], a[244:247], v[224:227]\n"
      "v_cvt_pk_bf16_f32 v212, v212, v216\n"
      "v_pk_max_i16 v212, v212, 0\n"
      "v_mfma_f32_16x16x32_bf16 v[224:227], v[96:99], a[248:251], v[224:227]\n"
      "v_exp_f32 v85, v85\n"
      "v_mfma_f32_16x16x32_bf16 v[224:227], v[176:179], a[252:255], v[224:227]\n"
      "v_cvt_pk_bf16_f32 v214, v214, v218\n"
      "v_pk_max_i16 v214, v214, 0\n"
      "v_mfma_f32_16x16x4_4b_bf16 v[240:255], v[148:149], v[156:157], 0 cbsz:2 abid:2\n"
      "ds_read_b128 v[148:151], v0 offset:9216\n"
      "v_cvt_pk_bf16_f32 v216, v213, v217\n"
      "v_cvt_pk_bf16_f32 v218, v215, v219\n"
      "s_nop 1\n"
      "v_cvt_pk_bf16_f32 v215, v222, v226\n"
      "v_pk_max_i16 v215, v215, 0\n"
      "v_mfma_f32_16x16x32_bf16 v[176:179], v[136:139], v[36:39], 0\n"
      "v_cvt_pk_bf16_f32 v213, v220, v224\n"
      "v_cvt_pk_bf16_f32 v217, v221, v225\n"
      "v_mfma_f32_16x16x32_bf16 v[176:179], v[140:143], v[40:43], v[176:179]\n"
      "buffer_store_dwordx2 v[214:215], v2, s[44:47], s73 offen offset:512 nt sc1\n"
      "v_cvt_pk_bf16_f32 v219, v223, v227\n"
      "v_mfma_f32_16x16x32_bf16 v[248:251], v[28:31], v[56:59], v[248:251]\n"
      "ds_read_b128 v[224:227], v0 offset:9728\n"
      "v_pk_max_i16 v217, v217, 0\n"
      "v_mfma_f32_16x16x32_bf16 v[248:251], v[32:35], v[60:63], v[248:251]\n"
      "v_exp_f32 v86, v86\n"
      "v_mfma_f32_16x16x32_bf16 v[248:251], v[132:135], v[116:119], v[248:251]\n"
      "v_exp_f32 v178, v178\n"
      "v_mfma_f32_16x16x32_bf16 v[240:243], v[28:31], v[108:111], v[240:243]\n"
      "v_exp_f32 v177, v177\n"
      "v_mfma_f32_16x16x32_bf16 v[240:243], v[32:35], v[112:115], v[240:243]\n"
      "v_cvt_pk_bf16_f32 v221, v86, v87\n"
      "v_cvt_pk_bf16_f32 v220, v84, v85\n"
      "v_mfma_f32_16x16x32_bf16 v[240:243], v[132:135], v[52:55], v[240:243]\n"
      "v_exp_f32 v179, v179\n"
      "v_mfma_f32_16x16x32_bf16 v[244:247], v[28:31], v[180:183], v[244:247]\n"
      "s_add_u32 m0, s53, 8192\n"
      "v_exp_f32 v176, v176\n"
      "v_mfma_f32_16x16x32_bf16 v[244:247], v[32:35], v[184:187], v[244:247]\n"
      "v_pk_max_i16 v213, v213, 0\n"
      "v_pk_max_i16 v219, v219, 0\n"
      "v_mfma_f32_16x16x32_bf16 v[244:247], v[132:135], v[120:123], v[244:247]\n"
      "s_waitcnt lgkmcnt(4)\n"
      "v_mul_f32_dpp v248, v190, v248 row_newbcast:0 row_mask:0xf bank_mask:0xf\n"
      "v_cvt_pk_bf16_f32 v222, v176, v177\n"
      "v_mfma_f32_16x16x32_bf16 v[252:255], v[28:31], v[232:235], v[252:255]\n"
      "v_mul_f32_dpp v250, v190, v250 row_newbcast:8 row_mask:0xf bank_mask:0xf\n"
      "v_mfma_f32_16x16x32_bf16 v[252:255], v[32:35], v[236:239], v[252:255]\n"
      "v_mul_f32_dpp v240, v190, v240 row_newbcast:0 row_mask:0xf bank_mask:0xf\n"
      "v_mul_f32_dpp v243, v190, v243 row_newbcast:12 row_mask:0xf bank_mask:0xf\n"
      "v_mfma_f32_16x16x32_bf16 v[252:255], v[132:135], v[228:231], v[252:255]\n"
      "s_waitcnt lgkmcnt(0)\n"
      "buffer_load_dwordx4 v1, s[40:43], s91 offen offset:3072 lds\n"
      "v_mfma_f32_16x16x4_4b_bf16 v[84:99], v[158:159], v[156:157], 0 cbsz:2 abid:0\n"
      "buffer_store_dwordx2 v[212:213], v2, s[44:47], s73 offen offset:0 nt sc1\n"
      "v_mul_f32_dpp v244, v190, v244 row_newbcast:0 row_mask:0xf bank_mask:0xf\n"
      "v_mul_f32_dpp v245, v190, v245 row_newbcast:4 row_mask:0xf bank_mask:0xf\n"
      "v_pk_max_i16 v218, v218, 0\n"
      "v_mfma_f32_16x16x32_bf16 v[28:31], v[164:167], v[36:39], 0\n"
      "v_mul_f32_dpp v253, v190, v253 row_newbcast:4 row_mask:0xf bank_mask:0xf\n"
      "v_mul_f32_dpp v252, v190, v252 row_newbcast:0 row_mask:0xf bank_mask:0xf\n"
      "v_mfma_f32_16x16x32_bf16 v[28:31], v[168:171], v[40:43], v[28:31]\n"
      "v_mul_f32_dpp v249, v190, v249 row_newbcast:4 row_mask:0xf bank_mask:0xf\n"
      "s_nop 0\n"
      "v_smfmac_f32_16x16x64_bf16 v[88:91], v[172:175], v[180:187], v5\n"
      "ds_read_b64 v[132:133], v11 offset:4096\n"
      "v_mul_f32_dpp v246, v190, v246 row_newbcast:8 row_mask:0xf bank_mask:0xf\n"
      "v_smfmac_f32_16x16x64_bf16 v[84:87], v[172:175], v[108:115], v5\n"
      "buffer_load_dwordx4 v1, s[40:43], s90 offen offset:0 lds\n"
      "v_smfmac_f32_16x16x64_bf16 v[92:95], v[172:175], v[56:63], v5\n"
      "v_smfmac_f32_16x16x64_bf16 v[96:99], v[172:175], v[232:239], v5\n"
      "v_mul_f32_dpp v247, v190, v247 row_newbcast:12 row_mask:0xf bank_mask:0xf\n"
      "v_mfma_f32_16x16x32_bf16 v[32:35], v[76:79], v[36:39], 0\n"
      "v_exp_f32 v30, v30\n"
      "v_mfma_f32_16x16x32_bf16 v[32:35], v[80:83], v[40:43], v[32:35]\n"
      "v_exp_f32 v29, v29\n"
      "v_smfmac_f32_16x16x32_bf16 v[92:95], v[144:145], v[116:119], v5\n"
      "v_smfmac_f32_16x16x32_bf16 v[88:91], v[144:145], v[120:123], v5\n"
      "v_maximum3_f32 v8, v8, v188, v189\n"
      "v_smfmac_f32_16x16x32_bf16 v[96:99], v[144:145], v[228:231], v5\n"
      "v_mul_f32_dpp v254, v190, v254 row_newbcast:8 row_mask:0xf bank_mask:0xf\n"
      "v_smfmac_f32_16x16x32_bf16 v[84:87], v[144:145], v[52:55], v5\n"
      "v_mul_f32_dpp v242, v190, v242 row_newbcast:8 row_mask:0xf bank_mask:0xf\n"
      "v_cvt_pk_bf16_f32 v223, v178, v179\n"
      "v_mul_f32_dpp v251, v190, v251 row_newbcast:12 row_mask:0xf bank_mask:0xf\n"
      "v_mfma_f32_16x16x32_bf16 v[212:215], v[20:23], v[36:39], 0\n"
      "v_mul_f32_dpp v241, v190, v241 row_newbcast:4 row_mask:0xf bank_mask:0xf\n"
      "v_mul_f32_dpp v255, v190, v255 row_newbcast:12 row_mask:0xf bank_mask:0xf\n"
      "v_mfma_f32_16x16x32_bf16 v[212:215], v[24:27], v[40:43], v[212:215]\n"
      "v_exp_f32 v31, v31\n"
      "v_mfma_f32_16x16x32_bf16 v[240:243], v[124:127], a[192:195], v[240:243]\n"
      "s_waitcnt lgkmcnt(0)\n"
      "v_and_b32 v191, 0xffff0000, v133\n"
      "v_add_f32 v96, v96, v97\n"
      "v_mfma_f32_16x16x32_bf16 v[240:243], v[100:103], a[196:199], v[240:243]\n"
      "v_fma_f32 v96, v96, v19, v191\n"
      "v_lshlrev_b32 v190, 16, v133\n"
      "v_mfma_f32_16x16x32_bf16 v[240:243], v[72:75], a[200:203], v[240:243]\n"
      "v_exp_f32 v214, v214\n"
      "v_mfma_f32_16x16x32_bf16 v[240:243], v[196:199], a[204:207], v[240:243]\n"
      "v_exp_f32 v213, v213\n"
      "v_mfma_f32_16x16x32_bf16 v[244:247], v[124:127], a[208:211], v[244:247]\n"
      "v_exp_f32 v28, v28\n"
      "v_mfma_f32_16x16x32_bf16 v[244:247], v[100:103], a[212:215], v[244:247]\n"
      "v_exp_f32 v32, v32\n"
      "v_mfma_f32_16x16x32_bf16 v[244:247], v[72:75], a[216:219], v[244:247]\n"
      "v_exp_f32 v212, v212\n"
      "v_mfma_f32_16x16x32_bf16 v[244:247], v[196:199], a[220:223], v[244:247]\n"
      "buffer_store_dwordx2 v[218:219], v2, s[44:47], s73 offen offset:768 nt sc1\n"
      "v_lshlrev_b32 v188, 16, v132\n"
      "v_mfma_f32_16x16x32_bf16 v[252:255], v[124:127], a[240:243], v[252:255]\n"
      "v_add_f32 v84, v84, v85\n"
      "v_fma_f32 v84, v84, v19, v188\n"
      "v_mfma_f32_16x16x32_bf16 v[252:255], v[100:103], a[244:247], v[252:255]\n"
      "v_and_b32 v189, 0xffff0000, v132\n"
      "v_add_f32 v92, v92, v93\n"
      "v_mfma_f32_16x16x32_bf16 v[252:255], v[72:75], a[248:251], v[252:255]\n"
      "v_fma_f32 v92, v92, v19, v190\n"
      "v_cvt_pk_bf16_f32 v85, v92, v96\n"
      "v_mfma_f32_16x16x32_bf16 v[252:255], v[196:199], a[252:255], v[252:255]\n"
      "v_exp_f32 v34, v34\n"
      "v_mfma_f32_16x16x32_bf16 v[172:175], v[204:207], v[36:39], 0\n"
      "buffer_load_dwordx4 v1, s[40:43], s91 offen offset:1024 lds\n"
      "v_mfma_f32_16x16x32_bf16 v[172:175], v[208:211], v[40:43], v[172:175]\n"
      "v_exp_f32 v215, v215\n"
      "v_mfma_f32_16x16x32_bf16 v[132:135], a[144:147], v[128:131], 0\n"
      "v_exp_f32 v33, v33\n"
      "v_mfma_f32_16x16x32_bf16 v[132:135], a[148:151], v[152:155], v[132:135]\n"
      "v_exp_f32 v35, v35\n"
      "v_mfma_f32_16x16x32_bf16 v[132:135], a[152:155], v[148:151], v[132:135]\n"
      "v_add_f32 v88, v88, v89\n"
      "v_fma_f32 v88, v88, v19, v189\n"
      "v_mfma_f32_16x16x32_bf16 v[132:135], a[156:159], v[224:227], v[132:135]\n"
      "v_exp_f32 v174, v174\n"
      "v_mfma_f32_16x16x32_bf16 v[144:147], a[128:131], v[128:131], 0\n"
      "v_pk_max_i16 v216, v216, 0\n"
      "v_mfma_f32_16x16x32_bf16 v[144:147], a[132:135], v[152:155], v[144:147]\n"
      "v_exp_f32 v172, v172\n"
      "v_mfma_f32_16x16x32_bf16 v[144:147], a[136:139], v[148:151], v[144:147]\n"
      "v_cvt_pk_bf16_f32 v99, v34, v35\n"
      "v_cvt_pk_bf16_f32 v98, v32, v33\n"
      "v_mfma_f32_16x16x32_bf16 v[144:147], a[140:143], v[224:227], v[144:147]\n"
      "v_cvt_pk_bf16_f32 v93, v30, v31\n"
      "v_cvt_pk_bf16_f32 v92, v28, v29\n"
      "v_mfma_f32_16x16x32_bf16 v[188:191], a[176:179], v[128:131], 0\n"
      "v_pk_max_i16 v85, v85, 0\n"
      "v_cvt_pk_bf16_f32 v84, v84, v88\n"
      "v_mfma_f32_16x16x32_bf16 v[188:191], a[180:183], v[152:155], v[188:191]\n"
      "v_cvt_pk_bf16_f32 v96, v212, v213\n"
      "v_pk_max_i16 v84, v84, 0\n"
      "v_mfma_f32_16x16x32_bf16 v[188:191], a[184:187], v[148:151], v[188:191]\n"
      "v_cvt_pk_bf16_f32 v31, v134, v135\n"
      "v_cvt_pk_bf16_f32 v28, v144, v145\n"
      "v_mfma_f32_16x16x32_bf16 v[188:191], a[188:191], v[224:227], v[188:191]\n"
      "v_cvt_pk_bf16_f32 v30, v132, v133\n"
      "v_cvt_pk_bf16_f32 v240, v240, v244\n"
      "v_mfma_f32_16x16x32_bf16 v[132:135], a[160:163], v[128:131], 0\n"
      "v_cvt_pk_bf16_f32 v244, v241, v245\n"
      "v_cvt_pk_bf16_f32 v29, v146, v147\n"
      "v_mfma_f32_16x16x32_bf16 v[132:135], a[164:167], v[152:155], v[132:135]\n"
      "v_exp_f32 v175, v175\n"
      "v_mfma_f32_16x16x32_bf16 v[132:135], a[168:171], v[148:151], v[132:135]\n"
      "v_exp_f32 v173, v173\n"
      "v_mfma_f32_16x16x32_bf16 v[132:135], a[172:175], v[224:227], v[132:135]\n"
      "buffer_store_dwordx2 v[216:217], v2, s[44:47], s73 offen offset:256 nt sc1\n"
      "v_mov_b64 v[176:177], 0\n"
      "v_mfma_f32_16x16x32_bf16 v[248:251], v[124:127], a[224:227], v[248:251]\n"
      "v_cvt_pk_bf16_f32 v95, v174, v175\n"
      "v_cvt_pk_bf16_f32 v34, v188, v189\n"
      "v_mfma_f32_16x16x32_bf16 v[248:251], v[100:103], a[228:231], v[248:251]\n"
      "v_cvt_pk_bf16_f32 v35, v190, v191\n"
      "v_cvt_pk_bf16_f32 v242, v242, v246\n"
      "v_mfma_f32_16x16x32_bf16 v[248:251], v[72:75], a[232:235], v[248:251]\n"
      "v_cvt_pk_bf16_f32 v33, v134, v135\n"
      "v_mov_b64 v[174:175], 0\n"
      "v_mfma_f32_16x16x32_bf16 v[248:251], v[196:199], a[236:239], v[248:251]\n"
      "v_cvt_pk_bf16_f32 v32, v132, v133\n"
      "v_cvt_pk_bf16_f32 v246, v243, v247\n"
      "v_smfmac_f32_16x16x64_bf16 v[174:177], v[160:163], v[36:43], v5\n"
      "buffer_load_dwordx4 v1, s[40:43], s90 offen offset:2048 lds\n"
      "v_smfmac_f32_16x16x64_bf16 v[174:177], v[44:47], v[28:35], v5\n"
      "ds_read_b128 v[72:75], v0 offset:4608\n"
      "v_pk_max_i16 v242, v242, 0\n"
      "v_mfma_f32_16x16x32_bf16 v[88:91], v[64:67], v[28:31], 0\n"
      "v_cvt_pk_bf16_f32 v245, v249, v253\n"
      "v_cvt_pk_bf16_f32 v94, v172, v173\n"
      "v_mfma_f32_16x16x32_bf16 v[88:91], v[68:71], v[32:35], v[88:91]\n"
      "v_cvt_pk_bf16_f32 v247, v251, v255\n"
      "v_pk_max_i16 v247, v247, 0\n"
      "v_mfma_f32_16x16x32_bf16 v[144:147], v[20:23], v[28:31], 0\n"
      "v_add_f32 v174, v174, v175\n"
      "v_add_f32 v176, v176, v177\n"
      "v_mfma_f32_16x16x32_bf16 v[144:147], v[24:27], v[32:35], v[144:147]\n"
      "v_exp_f32 v176, v176\n"
      "v_mfma_f32_16x16x32_bf16 v[196:199], v[76:79], v[28:31], 0\n"
      "v_exp_f32 v174, v174\n"
      "v_mfma_f32_16x16x32_bf16 v[196:199], v[80:83], v[32:35], v[196:199]\n"
      "v_add_f32 v213, v174, v176\n"
      "v_add_f32 v212, v174, v176\n"
      "v_mfma_f32_16x16x32_bf16 v[132:135], v[204:207], v[28:31], 0\n"
      "s_nop 0\n"
      "v_permlane16_swap_b32 v212, v213\n"
      "v_mfma_f32_16x16x32_bf16 v[132:135], v[208:211], v[32:35], v[132:135]\n"
      "v_cvt_pk_bf16_f32 v217, v174, v176\n"
      "v_cvt_pk_bf16_f32 v216, v174, v176\n"
      "v_mfma_f32_16x16x32_bf16 v[172:175], v[136:139], v[28:31], 0\n"
      "v_exp_f32 v197, v197\n"
      "v_mfma_f32_16x16x32_bf16 v[172:175], v[140:143], v[32:35], v[172:175]\n"
      "v_exp_f32 v144, v144\n"
      "v_mfma_f32_16x16x32_bf16 v[188:191], v[164:167], v[28:31], 0\n"
      "v_mov_b64 v[176:177], v[212:213]\n"
      "v_cvt_pk_bf16_f32 v97, v214, v215\n"
      "v_mfma_f32_16x16x32_bf16 v[188:191], v[168:171], v[32:35], v[188:191]\n"
      "v_permlane16_swap_b32 v216, v217\n"
      "v_smfmac_f32_16x16x64_bf16 v[212:215], v[12:15], v[92:99], v5\n"
      "buffer_store_dwordx2 v[84:85], v3, s[44:47], s79 offen offset:0 nt sc1\n"
      "v_pk_max_i16 v240, v240, 0\n"
      "v_mfma_f32_16x16x4_4b_bf16 v[28:43], v[216:217], v[156:157], 0 cbsz:2 abid:0\n"
      "ds_read_b128 v[124:127], v0 offset:5632\n"
      "v_cvt_pk_bf16_f32 v243, v250, v254\n"
      "v_exp_f32 v88, v88\n"
      "v_cvt_pk_bf16_f32 v241, v248, v252\n"
      "v_pk_max_i16 v241, v241, 0\n"
      "v_smfmac_f32_16x16x32_bf16 v[212:215], v[12:13], v[220:223], v5\n"
      "s_add_u32 m0, s53, 4096\n"
      "ds_read_b128 v[100:103], v0 offset:4096\n"
      "v_pk_max_i16 v243, v243, 0\n"
      "v_exp_f32 v145, v145\n"
      "v_mfma_f32_16x16x32_bf16 v[32:35], v[92:95], v[180:183], v[32:35]\n"
      "v_exp_f32 v132, v132\n"
      "v_mfma_f32_16x16x32_bf16 v[32:35], v[96:99], v[184:187], v[32:35]\n"
      "ds_read_b128 v[84:87], v0 offset:5120\n"
      "v_add_f32 v212, v212, v213\n"
      "v_mfma_f32_16x16x32_bf16 v[32:35], v[220:223], v[120:123], v[32:35]\n"
      "v_rcp_f32 v213, v212\n"
      "v_mfma_f32_16x16x32_bf16 v[28:31], v[92:95], v[108:111], v[28:31]\n"
      "ds_bpermute_b32 v214, v4, v213\n"
      "v_mfma_f32_16x16x32_bf16 v[28:31], v[96:99], v[112:115], v[28:31]\n"
      "v_exp_f32 v147, v147\n"
      "v_mfma_f32_16x16x32_bf16 v[28:31], v[220:223], v[52:55], v[28:31]\n"
      "v_exp_f32 v146, v146\n"
      "v_mfma_f32_16x16x32_bf16 v[36:39], v[92:95], v[56:59], v[36:39]\n"
      "v_exp_f32 v91, v91\n"
      "v_mfma_f32_16x16x32_bf16 v[36:39], v[96:99], v[60:63], v[36:39]\n"
      "v_exp_f32 v172, v172\n"
      "v_mfma_f32_16x16x32_bf16 v[36:39], v[220:223], v[116:119], v[36:39]\n"
      "v_exp_f32 v196, v196\n"
      "v_mfma_f32_16x16x32_bf16 v[40:43], v[92:95], v[232:235], v[40:43]\n"
      "v_exp_f32 v173, v173\n"
      "v_mfma_f32_16x16x32_bf16 v[40:43], v[96:99], v[236:239], v[40:43]\n"
      "v_exp_f32 v198, v198\n"
      "v_mfma_f32_16x16x32_bf16 v[40:43], v[220:223], v[228:231], v[40:43]\n"
      "s_waitcnt lgkmcnt(1)\n"
      "buffer_load_dwordx4 v1, s[40:43], s92 offen offset:0 lds\n"
      "v_mfma_f32_16x16x32_bf16 v[94:97], a[176:179], v[100:103], 0\n"
      "v_exp_f32 v134, v134\n"
      "v_mfma_f32_16x16x32_bf16 v[94:97], a[180:183], v[72:75], v[94:97]\n"
      "s_waitcnt lgkmcnt(0)\n"
      "v_mul_f32_dpp v28, v214, v28 row_newbcast:0 row_mask:0xf bank_mask:0xf\n"
      "v_mul_f32_dpp v29, v214, v29 row_newbcast:4 row_mask:0xf bank_mask:0xf\n"
      "v_mfma_f32_16x16x32_bf16 v[94:97], a[184:187], v[84:87], v[94:97]\n"
      "v_cvt_pk_bf16_f32 v253, v146, v147\n"
      "v_mul_f32_dpp v34, v214, v34 row_newbcast:8 row_mask:0xf bank_mask:0xf\n"
      "v_mfma_f32_16x16x32_bf16 v[94:97], a[188:191], v[124:127], v[94:97]\n"
      "v_exp_f32 v189, v189\n"
      "v_mfma_f32_16x16x32_bf16 v[218:221], a[128:131], v[100:103], 0\n"
      "v_exp_f32 v135, v135\n"
      "v_mfma_f32_16x16x32_bf16 v[218:221], a[132:135], v[72:75], v[218:221]\n"
      "v_mul_f32_dpp v32, v214, v32 row_newbcast:0 row_mask:0xf bank_mask:0xf\n"
      "v_mul_f32_dpp v33, v214, v33 row_newbcast:4 row_mask:0xf bank_mask:0xf\n"
      "v_mfma_f32_16x16x32_bf16 v[218:221], a[136:139], v[84:87], v[218:221]\n"
      "v_mul_f32_dpp v30, v214, v30 row_newbcast:8 row_mask:0xf bank_mask:0xf\n"
      "v_mul_f32_dpp v35, v214, v35 row_newbcast:12 row_mask:0xf bank_mask:0xf\n"
      "v_mfma_f32_16x16x32_bf16 v[218:221], a[140:143], v[124:127], v[218:221]\n"
      "v_exp_f32 v89, v89\n"
      "v_mfma_f32_16x16x32_bf16 v[32:35], v[192:195], a[208:211], v[32:35]\n"
      "v_mul_f32_dpp v38, v214, v38 row_newbcast:8 row_mask:0xf bank_mask:0xf\n"
      "v_mul_f32_dpp v42, v214, v42 row_newbcast:8 row_mask:0xf bank_mask:0xf\n"
      "v_mfma_f32_16x16x32_bf16 v[32:35], v[200:203], a[212:215], v[32:35]\n"
      "v_mul_f32_dpp v41, v214, v41 row_newbcast:4 row_mask:0xf bank_mask:0xf\n"
      "v_cvt_pk_bf16_f32 v252, v144, v145\n"
      "v_mfma_f32_16x16x32_bf16 v[32:35], v[104:107], a[216:219], v[32:35]\n"
      "v_mul_f32_dpp v36, v214, v36 row_newbcast:0 row_mask:0xf bank_mask:0xf\n"
      "v_mul_f32_dpp v31, v214, v31 row_newbcast:12 row_mask:0xf bank_mask:0xf\n"
      "v_mfma_f32_16x16x32_bf16 v[32:35], v[48:51], a[220:223], v[32:35]\n"
      "v_exp_f32 v188, v188\n"
      "v_mfma_f32_16x16x32_bf16 v[28:31], v[192:195], a[192:195], v[28:31]\n"
      "v_mul_f32_dpp v40, v214, v40 row_newbcast:0 row_mask:0xf bank_mask:0xf\n"
      "v_mfma_f32_16x16x32_bf16 v[28:31], v[200:203], a[196:199], v[28:31]\n"
      "v_exp_f32 v174, v174\n"
      "v_mfma_f32_16x16x32_bf16 v[28:31], v[104:107], a[200:203], v[28:31]\n"
      "v_mul_f32_dpp v37, v214, v37 row_newbcast:4 row_mask:0xf bank_mask:0xf\n"
      "v_mul_f32_dpp v39, v214, v39 row_newbcast:12 row_mask:0xf bank_mask:0xf\n"
      "v_mfma_f32_16x16x32_bf16 v[28:31], v[48:51], a[204:207], v[28:31]\n"
      "v_maximum3_f32 v8, v8, v212, v213\n"
      "v_cvt_pk_bf16_f32 v254, v196, v197\n"
      "v_mfma_f32_16x16x32_bf16 v[36:39], v[192:195], a[224:227], v[36:39]\n"
      "v_exp_f32 v191, v191\n"
      "v_mfma_f32_16x16x32_bf16 v[36:39], v[200:203], a[228:231], v[36:39]\n"
      "buffer_load_dwordx4 v1, s[40:43], s93 offen offset:3072 lds\n"
      "v_mfma_f32_16x16x32_bf16 v[36:39], v[104:107], a[232:235], v[36:39]\n"
      "v_mul_f32_dpp v43, v214, v43 row_newbcast:12 row_mask:0xf bank_mask:0xf\n"
      "v_cvt_pk_bf16_f32 v28, v28, v32\n"
      "v_mfma_f32_16x16x32_bf16 v[36:39], v[48:51], a[236:239], v[36:39]\n"
      "ds_read_b128 v[144:147], v0 offset:0\n"
      "v_cvt_pk_bf16_f32 v214, v172, v173\n"
      "v_mfma_f32_16x16x32_bf16 v[40:43], v[192:195], a[240:243], v[40:43]\n"
      "v_exp_f32 v199, v199\n"
      "v_mfma_f32_16x16x32_bf16 v[40:43], v[200:203], a[244:247], v[40:43]\n"
      "v_exp_f32 v133, v133\n"
      "v_mfma_f32_16x16x32_bf16 v[40:43], v[104:107], a[248:251], v[40:43]\n"
      "v_cvt_pk_bf16_f32 v248, v188, v189\n"
      "v_mfma_f32_16x16x32_bf16 v[40:43], v[48:51], a[252:255], v[40:43]\n"
      "v_exp_f32 v190, v190\n"
      "v_mfma_f32_16x16x32_bf16 v[104:107], a[160:163], v[100:103], 0\n"
      "v_exp_f32 v90, v90\n"
      "v_mfma_f32_16x16x32_bf16 v[104:107], a[164:167], v[72:75], v[104:107]\n"
      "v_cvt_pk_bf16_f32 v213, v90, v91\n"
      "v_mov_b64 v[50:51], 0\n"
      "v_mfma_f32_16x16x32_bf16 v[104:107], a[168:171], v[84:87], v[104:107]\n"
      "v_cvt_pk_bf16_f32 v249, v190, v191\n"
      "v_pk_max_i16 v246, v246, 0\n"
      "v_mfma_f32_16x16x32_bf16 v[104:107], a[172:175], v[124:127], v[104:107]\n"
      "v_cvt_pk_bf16_f32 v212, v88, v89\n"
      "v_cvt_pk_bf16_f32 v250, v132, v133\n"
      "v_mfma_f32_16x16x32_bf16 v[88:91], a[144:147], v[100:103], 0\n"
      "v_cvt_pk_bf16_f32 v99, v96, v97\n"
      "v_cvt_pk_bf16_f32 v255, v198, v199\n"
      "v_mfma_f32_16x16x32_bf16 v[88:91], a[148:151], v[72:75], v[88:91]\n"
      "v_cvt_pk_bf16_f32 v251, v134, v135\n"
      "v_cvt_pk_bf16_f32 v98, v94, v95\n"
      "v_mfma_f32_16x16x32_bf16 v[88:91], a[152:155], v[84:87], v[88:91]\n"
      "v_cvt_pk_bf16_f32 v32, v29, v33\n"
      "v_cvt_pk_bf16_f32 v33, v37, v41\n"
      "v_mfma_f32_16x16x32_bf16 v[88:91], a[156:159], v[124:127], v[88:91]\n"
      "v_exp_f32 v175, v175\n"
      "v_smfmac_f32_16x16x64_bf16 v[176:179], v[12:15], v[248:255], v5\n"
      "buffer_store_dwordx2 v[246:247], v2, s[44:47], s75 offen offset:768 nt sc1\n"
      "v_cvt_pk_bf16_f32 v215, v174, v175\n"
      "v_mfma_f32_16x16x4_4b_bf16 v[188:203], v[216:217], v[156:157], 0 cbsz:2 abid:2\n"
      "v_cvt_pk_bf16_f32 v30, v30, v34\n"
      "v_pk_max_i16 v245, v245, 0\n"
      "v_cvt_pk_bf16_f32 v93, v220, v221\n"
      "v_cvt_pk_bf16_f32 v29, v36, v40\n"
      "v_cvt_pk_bf16_f32 v96, v104, v105\n"
      "v_cvt_pk_bf16_f32 v97, v106, v107\n"
      "v_smfmac_f32_16x16x32_bf16 v[176:179], v[12:13], v[212:215], v5\n"
      "ds_read_b128 v[172:175], v0 offset:1024\n"
      "v_cvt_pk_bf16_f32 v34, v31, v35\n"
      "v_cvt_pk_bf16_f32 v95, v90, v91\n"
      "v_cvt_pk_bf16_f32 v94, v88, v89\n"
      "v_mfma_f32_16x16x32_bf16 v[200:203], v[248:251], v[232:235], v[200:203]\n"
      "v_cvt_pk_bf16_f32 v92, v218, v219\n"
      "v_cvt_pk_bf16_f32 v35, v39, v43\n"
      "v_mfma_f32_16x16x32_bf16 v[200:203], v[252:255], v[236:239], v[200:203]\n"
      "buffer_load_dwordx4 v1, s[40:43], s93 offen offset:1024 lds\n"
      "v_mfma_f32_16x16x32_bf16 v[200:203], v[212:215], v[228:231], v[200:203]\n"
      "ds_read_b128 v[104:107], v0 offset:512\n"
      "v_add_f32 v176, v176, v177\n"
      "v_mfma_f32_16x16x32_bf16 v[88:91], v[76:79], v[92:95], 0\n"
      "v_cvt_pk_bf16_f32 v31, v38, v42\n"
      "v_mov_b64 v[48:49], 0\n"
      "v_mfma_f32_16x16x32_bf16 v[88:91], v[80:83], v[96:99], v[88:91]\n"
      "ds_read_b128 v[132:135], v0 offset:1536\n"
      "v_pk_max_i16 v35, v35, 0\n"
      "v_mfma_f32_16x16x32_bf16 v[40:43], v[20:23], v[92:95], 0\n"
      "v_rcp_f32 v177, v176\n"
      "v_mfma_f32_16x16x32_bf16 v[40:43], v[24:27], v[96:99], v[40:43]\n"
      "ds_bpermute_b32 v178, v4, v177 offset:128\n"
      "v_smfmac_f32_16x16x64_bf16 v[48:51], v[160:163], v[92:99], v5\n"
      "buffer_store_dwordx2 v[240:241], v2, s[44:47], s75 offen offset:0 nt sc1\n"
      "v_pk_max_i16 v29, v29, 0\n"
      "v_mfma_f32_16x16x32_bf16 v[36:39], v[204:207], v[92:95], 0\n"
      "v_exp_f32 v90, v90\n"
      "v_mfma_f32_16x16x32_bf16 v[36:39], v[208:211], v[96:99], v[36:39]\n"
      "v_exp_f32 v88, v88\n"
      "v_mfma_f32_16x16x32_bf16 v[188:191], v[248:251], v[108:111], v[188:191]\n"
      "v_exp_f32 v42, v42\n"
      "v_mfma_f32_16x16x32_bf16 v[188:191], v[252:255], v[112:115], v[188:191]\n"
      "v_exp_f32 v41, v41\n"
      "v_mfma_f32_16x16x32_bf16 v[188:191], v[212:215], v[52:55], v[188:191]\n"
      "buffer_load_dwordx4 v1, s[40:43], s92 offen offset:2048 lds\n"
      "v_mfma_f32_16x16x32_bf16 v[196:199], v[248:251], v[56:59], v[196:199]\n"
      "v_exp_f32 v91, v91\n"
      "v_mfma_f32_16x16x32_bf16 v[196:199], v[252:255], v[60:63], v[196:199]\n"
      "v_exp_f32 v39, v39\n"
      "v_mfma_f32_16x16x32_bf16 v[196:199], v[212:215], v[116:119], v[196:199]\n"
      "v_exp_f32 v89, v89\n"
      "v_mfma_f32_16x16x32_bf16 v[192:195], v[248:251], v[180:183], v[192:195]\n"
      "v_exp_f32 v36, v36\n"
      "v_mfma_f32_16x16x32_bf16 v[192:195], v[252:255], v[184:187], v[192:195]\n"
      "s_waitcnt lgkmcnt(0)\n"
      "v_mul_f32_dpp v200, v178, v200 row_newbcast:0 row_mask:0xf bank_mask:0xf\n"
      "v_mul_f32_dpp v188, v178, v188 row_newbcast:0 row_mask:0xf bank_mask:0xf\n"
      "v_mfma_f32_16x16x32_bf16 v[192:195], v[212:215], v[120:123], v[192:195]\n"
      "v_mul_f32_dpp v201, v178, v201 row_newbcast:4 row_mask:0xf bank_mask:0xf\n"
      "v_cvt_pk_bf16_f32 v218, v88, v89\n"
      "v_mfma_f32_16x16x32_bf16 v[220:223], v[64:67], v[92:95], 0\n"
      "v_exp_f32 v40, v40\n"
      "v_mfma_f32_16x16x32_bf16 v[220:223], v[68:71], v[96:99], v[220:223]\n"
      "v_exp_f32 v43, v43\n"
      "v_mfma_f32_16x16x32_bf16 v[160:163], v[136:139], v[92:95], 0\n"
      "v_cvt_pk_bf16_f32 v217, v42, v43\n"
      "v_mul_f32_dpp v192, v178, v192 row_newbcast:0 row_mask:0xf bank_mask:0xf\n"
      "v_mfma_f32_16x16x32_bf16 v[160:163], v[140:143], v[96:99], v[160:163]\n"
      "v_cvt_pk_bf16_f32 v216, v40, v41\n"
      "v_mul_f32_dpp v203, v178, v203 row_newbcast:12 row_mask:0xf bank_mask:0xf\n"
      "v_mfma_f32_16x16x32_bf16 v[40:43], v[164:167], v[92:95], 0\n"
      "v_exp_f32 v37, v37\n"
      "v_mfma_f32_16x16x32_bf16 v[40:43], v[168:171], v[96:99], v[40:43]\n"
      "v_exp_f32 v221, v221\n"
      "v_mfma_f32_16x16x32_bf16 v[92:95], a[144:147], v[144:147], 0\n"
      "s_nop 0\n"
      "v_exp_f32 v161, v161\n"
      "v_mfma_f32_16x16x32_bf16 v[92:95], a[148:151], v[104:107], v[92:95]\n"
      "v_cvt_pk_bf16_f32 v219, v90, v91\n"
      "v_mul_f32_dpp v189, v178, v189 row_newbcast:4 row_mask:0xf bank_mask:0xf\n"
      "v_mfma_f32_16x16x32_bf16 v[92:95], a[152:155], v[172:175], v[92:95]\n"
      "v_exp_f32 v38, v38\n"
      "v_mfma_f32_16x16x32_bf16 v[92:95], a[156:159], v[132:135], v[92:95]\n"
      "buffer_store_dwordx2 v[242:243], v2, s[44:47], s75 offen offset:512 nt sc1\n"
      "v_mul_f32_dpp v191, v178, v191 row_newbcast:12 row_mask:0xf bank_mask:0xf\n"
      "v_mfma_f32_16x16x32_bf16 v[88:91], a[128:131], v[144:147], 0\n"
      "v_exp_f32 v220, v220\n"
      "v_mfma_f32_16x16x32_bf16 v[88:91], a[132:135], v[104:107], v[88:91]\n"
      "v_exp_f32 v43, v43\n"
      "v_mfma_f32_16x16x32_bf16 v[88:91], a[136:139], v[172:175], v[88:91]\n"
      "s_nop 0\n"
      "v_cvt_pk_bf16_f32 v249, v94, v95\n"
      "v_mul_f32_dpp v197, v178, v197 row_newbcast:4 row_mask:0xf bank_mask:0xf\n"
      "v_mfma_f32_16x16x32_bf16 v[88:91], a[140:143], v[132:135], v[88:91]\n"
      "v_exp_f32 v223, v223\n"
      "v_mfma_f32_16x16x32_bf16 v[212:215], a[176:179], v[144:147], 0\n"
      "v_mul_f32_dpp v198, v178, v198 row_newbcast:8 row_mask:0xf bank_mask:0xf\n"
      "v_mul_f32_dpp v190, v178, v190 row_newbcast:8 row_mask:0xf bank_mask:0xf\n"
      "v_mfma_f32_16x16x32_bf16 v[212:215], a[180:183], v[104:107], v[212:215]\n"
      "v_exp_f32 v163, v163\n"
      "v_mfma_f32_16x16x32_bf16 v[212:215], a[184:187], v[172:175], v[212:215]\n"
      "v_mul_f32_dpp v195, v178, v195 row_newbcast:12 row_mask:0xf bank_mask:0xf\n"
      "v_cvt_pk_bf16_f32 v247, v90, v91\n"
      "v_mfma_f32_16x16x32_bf16 v[212:215], a[188:191], v[132:135], v[212:215]\n"
      "s_not_b64 vcc, vcc\n"
      "v_mul_f32_dpp v194, v178, v194 row_newbcast:8 row_mask:0xf bank_mask:0xf\n"
      "v_cvt_pk_bf16_f32 v248, v92, v93\n"
      "v_mfma_f32_16x16x32_bf16 v[92:95], a[160:163], v[144:147], 0\n"
      "v_exp_f32 v160, v160\n"
      "v_mfma_f32_16x16x32_bf16 v[92:95], a[164:167], v[104:107], v[92:95]\n"
      "v_cvt_pk_bf16_f32 v246, v88, v89\n"
      "v_mul_f32_dpp v202, v178, v202 row_newbcast:8 row_mask:0xf bank_mask:0xf\n"
      "v_mfma_f32_16x16x32_bf16 v[92:95], a[168:171], v[172:175], v[92:95]\n"
      "v_cvt_pk_bf16_f32 v252, v212, v213\n"
      "v_cvt_pk_bf16_f32 v253, v214, v215\n"
      "v_mfma_f32_16x16x32_bf16 v[92:95], a[172:175], v[132:135], v[92:95]\n"
      "s_add_u32 m0, s53, 0\n"
      "v_exp_f32 v162, v162\n"
      "v_mfma_f32_16x16x32_bf16 v[188:191], v[128:131], a[192:195], v[188:191]\n"
      "buffer_load_dwordx4 v1, s[40:43], s94 offen offset:2048 lds\n"
      "v_mfma_f32_16x16x32_bf16 v[188:191], v[152:155], a[196:199], v[188:191]\n"
      "v_exp_f32 v42, v42\n"
      "v_mfma_f32_16x16x32_bf16 v[188:191], v[148:151], a[200:203], v[188:191]\n"
      "s_nop 0\n"
      "v_cvt_pk_bf16_f32 v250, v92, v93\n"
      "v_cvt_pk_bf16_f32 v251, v94, v95\n"
      "v_mfma_f32_16x16x32_bf16 v[188:191], v[224:227], a[204:207], v[188:191]\n"
      "v_cvt_pk_bf16_f32 v213, v42, v43\n"
      "v_mul_f32_dpp v199, v178, v199 row_newbcast:12 row_mask:0xf bank_mask:0xf\n"
      "v_smfmac_f32_16x16x64_bf16 v[48:51], v[44:47], v[246:253], v5\n"
      "v_cvt_pk_bf16_f32 v215, v38, v39\n"
      "v_mfma_f32_16x16x32_bf16 v[200:203], v[128:131], a[240:243], v[200:203]\n"
      "v_mul_f32_dpp v193, v178, v193 row_newbcast:4 row_mask:0xf bank_mask:0xf\n"
      "v_mul_f32_dpp v196, v178, v196 row_newbcast:0 row_mask:0xf bank_mask:0xf\n"
      "v_mfma_f32_16x16x32_bf16 v[200:203], v[152:155], a[244:247], v[200:203]\n"
      "v_exp_f32 v40, v40\n"
      "v_mfma_f32_16x16x32_bf16 v[200:203], v[148:151], a[248:251], v[200:203]\n"
      "v_exp_f32 v41, v41\n"
      "v_mfma_f32_16x16x32_bf16 v[200:203], v[224:227], a[252:255], v[200:203]\n"
      "v_add_f32 v50, v50, v51\n"
      "v_add_f32 v48, v48, v49\n"
      "v_mfma_f32_16x16x32_bf16 v[192:195], v[128:131], a[208:211], v[192:195]\n"
      "v_exp_f32 v48, v48\n"
      "v_mfma_f32_16x16x32_bf16 v[192:195], v[152:155], a[212:215], v[192:195]\n"
      "v_exp_f32 v50, v50\n"
      "v_mfma_f32_16x16x32_bf16 v[192:195], v[148:151], a[216:219], v[192:195]\n"
      "v_cvt_pk_bf16_f32 v242, v48, v50\n"
      "v_cvt_pk_bf16_f32 v243, v48, v50\n"
      "v_mfma_f32_16x16x32_bf16 v[192:195], v[224:227], a[220:223], v[192:195]\n"
      "v_add_f32 v88, v48, v50\n"
      "v_add_f32 v89, v48, v50\n"
      "v_mfma_f32_16x16x32_bf16 v[196:199], v[128:131], a[224:227], v[196:199]\n"
      "s_nop 0\n"
      "v_permlane16_swap_b32 v88, v89\n"
      "v_mfma_f32_16x16x32_bf16 v[196:199], v[152:155], a[228:231], v[196:199]\n"
      "v_permlane16_swap_b32 v242, v243\n"
      "v_mfma_f32_16x16x32_bf16 v[196:199], v[148:151], a[232:235], v[196:199]\n"
      "v_cvt_pk_bf16_f32 v212, v40, v41\n"
      "v_cvt_pk_bf16_f32 v214, v36, v37\n"
      "v_mfma_f32_16x16x32_bf16 v[196:199], v[224:227], a[236:239], v[196:199]\n"
      "v_cvt_pk_bf16_f32 v224, v220, v221\n"
      "v_mov_b64 v[128:129], v[88:89]\n"
      "v_smfmac_f32_16x16x64_bf16 v[88:91], v[12:15], v[212:219], v5\n"
      "v_cvt_pk_bf16_f32 v226, v160, v161\n"
      "v_mfma_f32_16x16x4_4b_bf16 v[36:51], v[242:243], v[156:157], 0 cbsz:2 abid:0\n"
      "v_exp_f32 v222, v222\n"
      "v_cvt_pk_bf16_f32 v227, v162, v163\n"
      "v_cvt_pk_bf16_f32 v225, v222, v223\n"
      "v_cvt_pk_bf16_f32 v188, v188, v192\n"
      "v_cvt_pk_bf16_f32 v190, v190, v194\n"
      "v_smfmac_f32_16x16x32_bf16 v[88:91], v[12:13], v[224:227], v5\n"
      "s_nop 3\n"
      "v_mfma_f32_16x16x32_bf16 v[40:43], v[212:215], v[180:183], v[40:43]\n"
      "buffer_load_dwordx4 v1, s[40:43], s95 offen offset:1024 lds\n"
      "v_mfma_f32_16x16x32_bf16 v[40:43], v[216:219], v[184:187], v[40:43]\n"
      "v_pk_max_i16 v244, v244, 0\n"
      "v_add_f32 v88, v88, v89\n"
      "v_mfma_f32_16x16x32_bf16 v[40:43], v[224:227], v[120:123], v[40:43]\n"
      "v_rcp_f32 v89, v88\n"
      "v_mfma_f32_16x16x32_bf16 v[48:51], v[212:215], v[232:235], v[48:51]\n"
      "ds_bpermute_b32 v90, v4, v89\n"
      "v_mfma_f32_16x16x32_bf16 v[48:51], v[216:219], v[236:239], v[48:51]\n"
      "v_cvt_pk_bf16_f32 v192, v189, v193\n"
      "v_cvt_pk_bf16_f32 v194, v191, v195\n"
      "v_mfma_f32_16x16x32_bf16 v[48:51], v[224:227], v[228:231], v[48:51]\n"
      "v_pk_max_i16 v192, v192, 0\n"
      "v_cvt_pk_bf16_f32 v189, v196, v200\n"
      "v_mfma_f32_16x16x32_bf16 v[36:39], v[212:215], v[108:111], v[36:39]\n"
      "v_pk_max_i16 v194, v194, 0\n"
      "v_cvt_pk_bf16_f32 v195, v199, v203\n"
      "v_mfma_f32_16x16x32_bf16 v[36:39], v[216:219], v[112:115], v[36:39]\n"
      "v_cvt_pk_bf16_f32 v193, v197, v201\n"
      "v_pk_max_i16 v31, v31, 0\n"
      "v_mfma_f32_16x16x32_bf16 v[36:39], v[224:227], v[52:55], v[36:39]\n"
      "v_mfma_f32_16x16x32_bf16 v[44:47], v[212:215], v[56:59], v[44:47]\n"
      "buffer_load_dwordx4 v1, s[40:43], s94 offen offset:0 lds\n"
      "v_mfma_f32_16x16x32_bf16 v[44:47], v[216:219], v[60:63], v[44:47]\n"
      "v_pk_max_i16 v189, v189, 0\n"
      "v_mfma_f32_16x16x32_bf16 v[44:47], v[224:227], v[116:119], v[44:47]\n"
      "buffer_store_dwordx2 v[244:245], v2, s[44:47], s75 offen offset:256 nt sc1\n"
      "v_cvt_pk_bf16_f32 v191, v198, v202\n"
      "v_mfma_f32_16x16x4_4b_bf16 v[212:227], v[242:243], v[156:157], 0 cbsz:2 abid:2\n"
      "s_waitcnt lgkmcnt(0)\n"
      "v_mul_f32_dpp v51, v90, v51 row_newbcast:12 row_mask:0xf bank_mask:0xf\n"
      "v_mul_f32_dpp v48, v90, v48 row_newbcast:0 row_mask:0xf bank_mask:0xf\n"
      "v_mul_f32_dpp v43, v90, v43 row_newbcast:12 row_mask:0xf bank_mask:0xf\n"
      "v_mul_f32_dpp v50, v90, v50 row_newbcast:8 row_mask:0xf bank_mask:0xf\n"
      "v_mul_f32_dpp v49, v90, v49 row_newbcast:4 row_mask:0xf bank_mask:0xf\n"
      "v_mul_f32_dpp v45, v90, v45 row_newbcast:4 row_mask:0xf bank_mask:0xf\n"
      "v_mul_f32_dpp v46, v90, v46 row_newbcast:8 row_mask:0xf bank_mask:0xf\n"
      "v_pk_max_i16 v191, v191, 0\n"
      "v_mfma_f32_16x16x32_bf16 v[48:51], v[100:103], a[240:243], v[48:51]\n"
      "v_pk_max_i16 v34, v34, 0\n"
      "v_mul_f32_dpp v41, v90, v41 row_newbcast:4 row_mask:0xf bank_mask:0xf\n"
      "v_mfma_f32_16x16x32_bf16 v[48:51], v[72:75], a[244:247], v[48:51]\n"
      "v_mul_f32_dpp v37, v90, v37 row_newbcast:4 row_mask:0xf bank_mask:0xf\n"
      "v_mul_f32_dpp v39, v90, v39 row_newbcast:12 row_mask:0xf bank_mask:0xf\n"
      "v_mfma_f32_16x16x32_bf16 v[48:51], v[84:87], a[248:251], v[48:51]\n"
      "v_mul_f32_dpp v40, v90, v40 row_newbcast:0 row_mask:0xf bank_mask:0xf\n"
      "v_mul_f32_dpp v42, v90, v42 row_newbcast:8 row_mask:0xf bank_mask:0xf\n"
      "v_mfma_f32_16x16x32_bf16 v[48:51], v[124:127], a[252:255], v[48:51]\n"
      "v_pk_max_i16 v33, v33, 0\n"
      "v_mfma_f32_16x16x32_bf16 v[40:43], v[100:103], a[208:211], v[40:43]\n"
      "v_maximum3_f32 v8, v8, v176, v177\n"
      "v_pk_max_i16 v30, v30, 0\n"
      "v_mfma_f32_16x16x32_bf16 v[40:43], v[72:75], a[212:215], v[40:43]\n"
      "v_mul_f32_dpp v44, v90, v44 row_newbcast:0 row_mask:0xf bank_mask:0xf\n"
      "v_pk_max_i16 v28, v28, 0\n"
      "v_mfma_f32_16x16x32_bf16 v[40:43], v[84:87], a[216:219], v[40:43]\n"
      "v_mul_f32_dpp v47, v90, v47 row_newbcast:12 row_mask:0xf bank_mask:0xf\n"
      "v_maximum3_f32 v8, v8, v88, v89\n"
      "v_mfma_f32_16x16x32_bf16 v[40:43], v[124:127], a[220:223], v[40:43]\n"
      "v_pk_max_i16 v190, v190, 0\n"
      "v_add_u32 v10, 64, v10\n"
      "v_mfma_f32_16x16x32_bf16 v[44:47], v[100:103], a[224:227], v[44:47]\n"
      "v_mul_f32_dpp v36, v90, v36 row_newbcast:0 row_mask:0xf bank_mask:0xf\n"
      "v_mul_f32_dpp v38, v90, v38 row_newbcast:8 row_mask:0xf bank_mask:0xf\n"
      "v_mfma_f32_16x16x32_bf16 v[44:47], v[72:75], a[228:231], v[44:47]\n"
      "L_h5_%=:\n"
      "s_cbranch_execz L_hr_%=\n"
      "v_add_u32 v11, 0x200, v11\n"
      "v_mfma_f32_16x16x32_bf16 v[44:47], v[84:87], a[232:235], v[44:47]\n"
      "s_add_u32 s44, s44, 13107200\n s_addc_u32 s45, s45, 0\n"
      "v_pk_max_i16 v193, v193, 0\n"
      "v_mfma_f32_16x16x32_bf16 v[44:47], v[124:127], a[236:239], v[44:47]\n"
      "v_pk_max_i16 v195, v195, 0\n"
      "v_pk_max_i16 v32, v32, 0\n"
      "v_mfma_f32_16x16x32_bf16 v[36:39], v[100:103], a[192:195], v[36:39]\n"
      "v_pk_max_i16 v188, v188, 0\n"
      "v_mfma_f32_16x16x32_bf16 v[36:39], v[72:75], a[196:199], v[36:39]\n"
      "buffer_load_dwordx4 v1, s[40:43], s95 offen offset:3072 lds\n"
      "v_mfma_f32_16x16x32_bf16 v[36:39], v[84:87], a[200:203], v[36:39]\n"
      "s_add_u32 s40, s40, 13107200\n s_addc_u32 s41, s41, 0\n"
      "s_add_u32 s38, s38, s38\n"
      "v_mfma_f32_16x16x32_bf16 v[36:39], v[124:127], a[204:207], v[36:39]\n"
      "s_cbranch_scc0 L_task_%=\n"
      "s_cmp_lg_u32 s38, 0\n"
      "s_cbranch_scc1 L_e5_%=\n"
      "L_ln_%=:\n"
      "v_mfma_f32_16x16x32_bf16 v[88:91], v[204:207], v[246:249], 0\n"
      "s_waitcnt vmcnt(40)\n"
      "ds_read_b128 v[92:95], v0 offset:20480\n"
      "ds_read_b128 v[84:87], v0 offset:21504\n"
      "v_mfma_f32_16x16x32_bf16 v[88:91], v[208:211], v[250:253], v[88:91]\n"
      "s_waitcnt vmcnt(30)\n"
      "ds_read_b128 v[72:75], v0 offset:16384\n"
      "ds_read_b128 v[204:207], v0 offset:17920\n"
      "v_mfma_f32_16x16x32_bf16 v[124:127], v[164:167], v[246:249], 0\n"
      "ds_read_b128 v[148:151], v0 offset:16896\n"
      "ds_read_b128 v[164:167], v0 offset:17408\n"
      "v_mfma_f32_16x16x32_bf16 v[124:127], v[168:171], v[250:253], v[124:127]\n"
      "buffer_store_dwordx2 v[28:29], v2, s[44:47], s100 offen offset:0 nt sc1\n"
      "v_mfma_f32_16x16x32_bf16 v[208:211], v[76:79], v[246:249], 0\n"
      "v_mfma_f32_16x16x32_bf16 v[208:211], v[80:83], v[250:253], v[208:211]\n"
      "ds_read_b128 v[76:79], v0 offset:20992\n"
      "ds_read_b128 v[80:83], v0 offset:22016\n"
      "v_mfma_f32_16x16x32_bf16 v[156:159], v[64:67], v[246:249], 0\n"
      "v_cvt_pk_bf16_f32 v38, v38, v42\n"
      "v_cvt_pk_bf16_f32 v36, v36, v40\n"
      "v_mfma_f32_16x16x32_bf16 v[156:159], v[68:71], v[250:253], v[156:159]\n"
      "v_exp_f32 v89, v89\n"
      "v_mfma_f32_16x16x32_bf16 v[240:243], v[20:23], v[246:249], 0\n"
      "v_exp_f32 v124, v124\n"
      "v_mfma_f32_16x16x32_bf16 v[240:243], v[24:27], v[250:253], v[240:243]\n"
      "v_exp_f32 v91, v91\n"
      "v_mfma_f32_16x16x32_bf16 v[176:179], v[136:139], v[246:249], 0\n"
      "v_mfma_f32_16x16x32_bf16 v[176:179], v[140:143], v[250:253], v[176:179]\n"
      "buffer_store_dwordx2 v[190:191], v2, s[44:47], s101 offen offset:512 nt sc1\n"
      "s_waitcnt lgkmcnt(5)\n"
      "v_mfma_f32_16x16x32_bf16 v[140:143], a[32:35], v[72:75], 0\n"
      "s_waitcnt lgkmcnt(3)\n"
      "v_mfma_f32_16x16x32_bf16 v[140:143], a[36:39], v[148:151], v[140:143]\n"
      "s_waitcnt lgkmcnt(2)\n"
      "v_mfma_f32_16x16x32_bf16 v[140:143], a[40:43], v[164:167], v[140:143]\n"
      "v_cvt_pk_bf16_f32 v42, v39, v43\n"
      "v_mfma_f32_16x16x32_bf16 v[140:143], a[44:47], v[204:207], v[140:143]\n"
      "v_exp_f32 v240, v240\n"
      "v_mfma_f32_16x16x32_bf16 v[168:171], a[144:147], v[72:75], 0\n"
      "v_mfma_f32_16x16x32_bf16 v[168:171], a[148:151], v[148:151], v[168:171]\n"
      "v_exp_f32 v211, v211\n"
      "v_mfma_f32_16x16x32_bf16 v[168:171], a[152:155], v[164:167], v[168:171]\n"
      "v_exp_f32 v179, v179\n"
      "v_mfma_f32_16x16x32_bf16 v[168:171], a[156:159], v[204:207], v[168:171]\n"
      "v_exp_f32 v156, v156\n"
      "v_mfma_f32_16x16x32_bf16 v[160:163], a[32:35], v[92:95], 0\n"
      "v_pk_max_i16 v36, v36, 0\n"
      "s_waitcnt lgkmcnt(1)\n"
      "v_mfma_f32_16x16x32_bf16 v[160:163], a[36:39], v[76:79], v[160:163]\n"
      "v_mfma_f32_16x16x32_bf16 v[160:163], a[40:43], v[84:87], v[160:163]\n"
      "s_waitcnt lgkmcnt(0)\n"
      "v_mfma_f32_16x16x32_bf16 v[160:163], a[44:47], v[80:83], v[160:163]\n"
      "v_exp_f32 v208, v208\n"
      "v_mfma_f32_16x16x32_bf16 v[20:23], a[176:179], v[92:95], 0\n"
      "v_exp_f32 v127, v127\n"
      "v_mfma_f32_16x16x32_bf16 v[20:23], a[180:183], v[76:79], v[20:23]\n"
      "v_exp_f32 v157, v157\n"
      "v_mfma_f32_16x16x32_bf16 v[20:23], a[184:187], v[84:87], v[20:23]\n"
      "v_exp_f32 v243, v243\n"
      "v_mfma_f32_16x16x32_bf16 v[20:23], a[188:191], v[80:83], v[20:23]\n"
      "v_cvt_pk_bf16_f32 v100, v156, v157\n"
      "v_pk_max_i16 v42, v42, 0\n"
      "v_mfma_f32_16x16x32_bf16 v[196:199], a[48:51], v[72:75], 0\n"
      "v_exp_f32 v159, v159\n"
      "v_mfma_f32_16x16x32_bf16 v[196:199], a[52:55], v[148:151], v[196:199]\n"
      "v_exp_f32 v210, v210\n"
      "v_mfma_f32_16x16x32_bf16 v[196:199], a[56:59], v[164:167], v[196:199]\n"
      "v_exp_f32 v158, v158\n"
      "v_mfma_f32_16x16x32_bf16 v[196:199], a[60:63], v[204:207], v[196:199]\n"
      "v_exp_f32 v126, v126\n"
      "v_mfma_f32_16x16x32_bf16 v[26:29], a[0:3], v[92:95], 0\n"
      "v_mfma_f32_16x16x32_bf16 v[26:29], a[4:7], v[76:79], v[26:29]\n"
      "v_exp_f32 v241, v241\n"
      "v_mfma_f32_16x16x32_bf16 v[26:29], a[8:11], v[84:87], v[26:29]\n"
      "v_cvt_pk_bf16_f32 v101, v158, v159\n"
      "v_cvt_pk_bf16_f32 v40, v37, v41\n"
      "v_mfma_f32_16x16x32_bf16 v[26:29], a[12:15], v[80:83], v[26:29]\n"
      "buffer_store_dwordx2 v[192:193], v2, s[44:47], s101 offen offset:256 nt sc1\n"
      "v_cvt_pk_bf16_f32 v39, v46, v50\n"
      "v_mfma_f32_16x16x32_bf16 v[244:247], a[16:19], v[92:95], 0\n"
      "v_cvt_pk_bf16_f32 v252, v240, v241\n"
      "v_pk_max_i16 v39, v39, 0\n"
      "v_mfma_f32_16x16x32_bf16 v[244:247], a[20:23], v[76:79], v[244:247]\n"
      "v_exp_f32 v209, v209\n"
      "v_mfma_f32_16x16x32_bf16 v[244:247], a[24:27], v[84:87], v[244:247]\n"
      "v_exp_f32 v178, v178\n"
      "v_mfma_f32_16x16x32_bf16 v[244:247], a[28:31], v[80:83], v[244:247]\n"
      "v_exp_f32 v177, v177\n"
      "v_mfma_f32_16x16x32_bf16 v[152:155], a[128:131], v[92:95], 0\n"
      "v_mfma_f32_16x16x32_bf16 v[152:155], a[132:135], v[76:79], v[152:155]\n"
      "v_exp_f32 v176, v176\n"
      "v_mfma_f32_16x16x32_bf16 v[152:155], a[136:139], v[84:87], v[152:155]\n"
      "v_exp_f32 v88, v88\n"
      "v_mfma_f32_16x16x32_bf16 v[152:155], a[140:143], v[80:83], v[152:155]\n"
      "v_exp_f32 v90, v90\n"
      "v_mfma_f32_16x16x32_bf16 v[156:159], a[48:51], v[92:95], 0\n"
      "v_cvt_pk_bf16_f32 v251, v90, v91\n"
      "v_cvt_pk_bf16_f32 v254, v208, v209\n"
      "v_mfma_f32_16x16x32_bf16 v[156:159], a[52:55], v[76:79], v[156:159]\n"
      "v_mfma_f32_16x16x32_bf16 v[156:159], a[56:59], v[84:87], v[156:159]\n"
      "v_cvt_pk_bf16_f32 v68, v160, v161\n"
      "v_mfma_f32_16x16x32_bf16 v[156:159], a[60:63], v[80:83], v[156:159]\n"
      "v_cvt_pk_bf16_f32 v103, v178, v179\n"
      "v_cvt_pk_bf16_f32 v255, v210, v211\n"
      "v_mfma_f32_16x16x32_bf16 v[136:139], a[176:179], v[72:75], 0\n"
      "v_exp_f32 v242, v242\n"
      "v_mfma_f32_16x16x32_bf16 v[136:139], a[180:183], v[148:151], v[136:139]\n"
      "v_cvt_pk_bf16_f32 v241, v142, v143\n"
      "v_cvt_pk_bf16_f32 v250, v88, v89\n"
      "v_mfma_f32_16x16x32_bf16 v[136:139], a[184:187], v[164:167], v[136:139]\n"
      "v_cvt_pk_bf16_f32 v240, v140, v141\n"
      "v_cvt_pk_bf16_f32 v102, v176, v177\n"
      "v_mfma_f32_16x16x32_bf16 v[136:139], a[188:191], v[204:207], v[136:139]\n"
      "v_exp_f32 v125, v125\n"
      "v_mfma_f32_16x16x32_bf16 v[88:91], a[128:131], v[72:75], 0\n"
      "v_mfma_f32_16x16x32_bf16 v[88:91], a[132:135], v[148:151], v[88:91]\n"
      "v_cvt_pk_bf16_f32 v249, v126, v127\n"
      "v_mfma_f32_16x16x32_bf16 v[88:91], a[136:139], v[164:167], v[88:91]\n"
      "v_mfma_f32_16x16x32_bf16 v[88:91], a[140:143], v[204:207], v[88:91]\n"
      "buffer_store_dwordx2 v[30:31], v2, s[44:47], s100 offen offset:512 nt sc1\n"
      "v_cvt_pk_bf16_f32 v253, v242, v243\n"
      "v_mfma_f32_16x16x32_bf16 v[200:203], a[160:163], v[92:95], 0\n"
      "s_waitcnt vmcnt(17)\n"
      "ds_read_b128 v[190:193], v0 offset:8192\n"
      "v_mfma_f32_16x16x32_bf16 v[200:203], a[164:167], v[76:79], v[200:203]\n"
      "v_cvt_pk_bf16_f32 v69, v162, v163\n"
      "v_mfma_f32_16x16x32_bf16 v[200:203], a[168:171], v[84:87], v[200:203]\n"
      "v_cvt_pk_bf16_f32 v248, v124, v125\n"
      "v_mfma_f32_16x16x32_bf16 v[200:203], a[172:175], v[80:83], v[200:203]\n"
      "ds_read_b128 v[124:127], v9 offset:2048\n"
      "v_pk_max_i16 v40, v40, 0\n"
      "v_smfmac_f32_16x16x64_bf16 v[128:131], v[12:15], v[248:255], v5\n"
      "ds_read_b128 v[140:143], v0 offset:9216\n"
      "v_cvt_pk_bf16_f32 v243, v198, v199\n"
      "v_mfma_f32_16x16x32_bf16 v[220:223], v[248:251], v[56:59], v[220:223]\n"
      "ds_read_b128 v[208:211], v0 offset:9728\n"
      "v_mfma_f32_16x16x32_bf16 v[220:223], v[252:255], v[60:63], v[220:223]\n"
      "v_mfma_f32_16x16x32_bf16 v[220:223], v[100:103], v[116:119], v[220:223]\n"
      "v_cvt_pk_bf16_f32 v31, v22, v23\n"
      "v_smfmac_f32_16x16x32_bf16 v[128:131], v[12:13], v[100:103], v5\n"
      "buffer_store_dwordx2 v[194:195], v2, s[44:47], s101 offen offset:768 nt sc1\n"
      "v_mfma_f32_16x16x32_bf16 v[216:219], v[248:251], v[180:183], v[216:219]\n"
      "v_cvt_pk_bf16_f32 v65, v28, v29\n"
      "v_cvt_pk_bf16_f32 v43, v47, v51\n"
      "v_mfma_f32_16x16x32_bf16 v[216:219], v[252:255], v[184:187], v[216:219]\n"
      "v_cvt_pk_bf16_f32 v41, v45, v49\n"
      "v_mfma_f32_16x16x32_bf16 v[216:219], v[100:103], v[120:123], v[216:219]\n"
      "ds_read_b128 v[176:179], v0 offset:8704\n"
      "v_add_f32 v128, v128, v129\n"
      "v_mfma_f32_16x16x32_bf16 v[224:227], v[248:251], v[232:235], v[224:227]\n"
      "v_mfma_f32_16x16x32_bf16 v[224:227], v[252:255], v[236:239], v[224:227]\n"
      "v_rcp_f32 v129, v128\n"
      "v_mfma_f32_16x16x32_bf16 v[224:227], v[100:103], v[228:231], v[224:227]\n"
      "ds_bpermute_b32 v130, v4, v129 offset:128\n"
      "v_mfma_f32_16x16x32_bf16 v[232:235], a[144:147], v[92:95], 0\n"
      "v_mfma_f32_16x16x32_bf16 v[232:235], a[148:151], v[76:79], v[232:235]\n"
      "v_mfma_f32_16x16x32_bf16 v[232:235], a[152:155], v[84:87], v[232:235]\n"
      "ds_read_b128 v[116:119], v10 offset:0\n"
      "v_mfma_f32_16x16x32_bf16 v[232:235], a[156:159], v[80:83], v[232:235]\n"
      "ds_read_b128 v[120:123], v9 offset:0\n"
      "v_mfma_f32_16x16x32_bf16 v[212:215], v[248:251], v[108:111], v[212:215]\n"
      "v_cvt_pk_bf16_f32 v37, v44, v48\n"
      "v_mov_b64 v[44:45], 0\n"
      "v_mfma_f32_16x16x32_bf16 v[212:215], v[252:255], v[112:115], v[212:215]\n"
      "v_mfma_f32_16x16x32_bf16 v[212:215], v[100:103], v[52:55], v[212:215]\n"
      "v_cvt_pk_bf16_f32 v24, v152, v153\n"
      "v_mfma_f32_16x16x32_bf16 v[100:103], a[16:19], v[72:75], 0\n"
      "v_mfma_f32_16x16x32_bf16 v[100:103], a[20:23], v[148:151], v[100:103]\n"
      "v_cvt_pk_bf16_f32 v64, v26, v27\n"
      "v_cvt_pk_bf16_f32 v28, v200, v201\n"
      "v_mfma_f32_16x16x32_bf16 v[100:103], a[24:27], v[164:167], v[100:103]\n"
      "s_waitcnt lgkmcnt(2)\n"
      "v_mul_f32_dpp v216, v130, v216 row_newbcast:0 row_mask:0xf bank_mask:0xf\n"
      "v_mul_f32_dpp v217, v130, v217 row_newbcast:4 row_mask:0xf bank_mask:0xf\n"
      "v_mfma_f32_16x16x32_bf16 v[100:103], a[28:31], v[204:207], v[100:103]\n"
      "v_cvt_pk_bf16_f32 v67, v246, v247\n"
      "v_mul_f32_dpp v214, v130, v214 row_newbcast:8 row_mask:0xf bank_mask:0xf\n"
      "v_mfma_f32_16x16x32_bf16 v[252:255], a[160:163], v[72:75], 0\n"
      "v_mul_f32_dpp v212, v130, v212 row_newbcast:0 row_mask:0xf bank_mask:0xf\n"
      "v_cvt_pk_bf16_f32 v30, v20, v21\n"
      "v_mfma_f32_16x16x32_bf16 v[252:255], a[164:167], v[148:151], v[252:255]\n"
      "v_mul_f32_dpp v225, v130, v225 row_newbcast:4 row_mask:0xf bank_mask:0xf\n"
      "v_mfma_f32_16x16x32_bf16 v[252:255], a[168:171], v[164:167], v[252:255]\n"
      "v_pk_max_i16 v37, v37, 0\n"
      "v_mul_f32_dpp v223, v130, v223 row_newbcast:12 row_mask:0xf bank_mask:0xf\n"
      "v_mfma_f32_16x16x32_bf16 v[252:255], a[172:175], v[204:207], v[252:255]\n"
      "buffer_store_dwordx2 v[36:37], v2, s[44:47], s34 offen offset:0 nt sc1\n"
      "v_cvt_pk_bf16_f32 v238, v100, v101\n"
      "v_mfma_f32_16x16x32_bf16 v[48:51], a[0:3], v[72:75], 0\n"
      "v_cvt_pk_bf16_f32 v29, v202, v203\n"
      "v_mfma_f32_16x16x32_bf16 v[48:51], a[4:7], v[148:151], v[48:51]\n"
      "v_mul_f32_dpp v219, v130, v219 row_newbcast:12 row_mask:0xf bank_mask:0xf\n"
      "v_mfma_f32_16x16x32_bf16 v[48:51], a[8:11], v[164:167], v[48:51]\n"
      "v_mul_f32_dpp v213, v130, v213 row_newbcast:4 row_mask:0xf bank_mask:0xf\n"
      "v_mul_f32_dpp v227, v130, v227 row_newbcast:12 row_mask:0xf bank_mask:0xf\n"
      "v_mfma_f32_16x16x32_bf16 v[48:51], a[12:15], v[204:207], v[48:51]\n"
      "v_cvt_pk_bf16_f32 v58, v136, v137\n"
      "v_cvt_pk_bf16_f32 v27, v234, v235\n"
      "v_mfma_f32_16x16x32_bf16 v[228:231], v[190:193], a[96:99], 0\n"
      "v_mul_f32_dpp v224, v130, v224 row_newbcast:0 row_mask:0xf bank_mask:0xf\n"
      "v_mfma_f32_16x16x32_bf16 v[228:231], v[176:179], a[100:103], v[228:231]\n"
      "v_mov_b64 v[246:247], 0\n"
      "v_pk_max_i16 v38, v38, 0\n"
      "v_mfma_f32_16x16x32_bf16 v[228:231], v[140:143], a[104:107], v[228:231]\n"
      "v_pk_max_i16 v41, v41, 0\n"
      "v_mfma_f32_16x16x32_bf16 v[228:231], v[208:211], a[108:111], v[228:231]\n"
      "v_mul_f32_dpp v220, v130, v220 row_newbcast:0 row_mask:0xf bank_mask:0xf\n"
      "v_mfma_f32_16x16x32_bf16 v[184:187], v[190:193], a[80:83], 0\n"
      "v_mul_f32_dpp v222, v130, v222 row_newbcast:8 row_mask:0xf bank_mask:0xf\n"
      "v_mfma_f32_16x16x32_bf16 v[184:187], v[176:179], a[84:87], v[184:187]\n"
      "v_mfma_f32_16x16x32_bf16 v[184:187], v[140:143], a[88:91], v[184:187]\n"
      "v_mov_b64 v[152:153], 0\n"
      "v_mfma_f32_16x16x32_bf16 v[184:187], v[208:211], a[92:95], v[184:187]\n"
      "v_cvt_pk_bf16_f32 v71, v158, v159\n"
      "v_mfma_f32_16x16x32_bf16 v[20:23], v[190:193], a[112:115], 0\n"
      "v_cvt_pk_bf16_f32 v57, v254, v255\n"
      "v_mul_f32_dpp v218, v130, v218 row_newbcast:8 row_mask:0xf bank_mask:0xf\n"
      "v_mfma_f32_16x16x32_bf16 v[20:23], v[176:179], a[116:119], v[20:23]\n"
      "v_mul_f32_dpp v215, v130, v215 row_newbcast:12 row_mask:0xf bank_mask:0xf\n"
      "v_cvt_pk_bf16_f32 v136, v228, v229\n"
      "v_mfma_f32_16x16x32_bf16 v[20:23], v[140:143], a[120:123], v[20:23]\n"
      "v_cvt_pk_bf16_f32 v236, v48, v49\n"
      "v_mfma_f32_16x16x32_bf16 v[20:23], v[208:211], a[124:127], v[20:23]\n"
      "v_cvt_pk_bf16_f32 v55, v170, v171\n"
      "v_mul_f32_dpp v226, v130, v226 row_newbcast:8 row_mask:0xf bank_mask:0xf\n"
      "v_mfma_f32_16x16x32_bf16 v[212:215], v[144:147], a[192:195], v[212:215]\n"
      "v_cvt_pk_bf16_f32 v66, v244, v245\n"
      "v_cvt_pk_bf16_f32 v237, v50, v51\n"
      "v_mfma_f32_16x16x32_bf16 v[212:215], v[104:107], a[196:199], v[212:215]\n"
      "v_mfma_f32_16x16x32_bf16 v[212:215], v[172:175], a[200:203], v[212:215]\n"
      "v_cvt_pk_bf16_f32 v239, v102, v103\n"
      "v_mfma_f32_16x16x32_bf16 v[212:215], v[132:135], a[204:207], v[212:215]\n"
      "v_cvt_pk_bf16_f32 v53, v90, v91\n"
      "v_cvt_pk_bf16_f32 v242, v196, v197\n"
      "v_mfma_f32_16x16x32_bf16 v[248:251], a[32:35], v[190:193], 0\n"
      "v_cvt_pk_bf16_f32 v26, v232, v233\n"
      "v_mfma_f32_16x16x32_bf16 v[248:251], a[36:39], v[176:179], v[248:251]\n"
      "v_mfma_f32_16x16x32_bf16 v[248:251], a[40:43], v[140:143], v[248:251]\n"
      "v_mfma_f32_16x16x32_bf16 v[248:251], a[44:47], v[208:211], v[248:251]\n"
      "buffer_store_dwordx2 v[40:41], v2, s[44:47], s34 offen offset:256 nt sc1\n"
      "v_maximum3_f32 v8, v8, v128, v129\n"
      "v_mfma_f32_16x16x32_bf16 v[224:227], v[144:147], a[240:243], v[224:227]\n"
      "v_mfma_f32_16x16x32_bf16 v[224:227], v[104:107], a[244:247], v[224:227]\n"
      "v_mfma_f32_16x16x32_bf16 v[224:227], v[172:175], a[248:251], v[224:227]\n"
      "v_mfma_f32_16x16x32_bf16 v[224:227], v[132:135], a[252:255], v[224:227]\n"
      "ds_read_b128 v[232:235], v0 offset:12288\n"
      "v_cvt_pk_bf16_f32 v70, v156, v157\n"
      "v_mfma_f32_16x16x32_bf16 v[216:219], v[144:147], a[208:211], v[216:219]\n"
      "s_waitcnt vmcnt(13)\n"
      "ds_read_b128 v[156:159], v0 offset:4096\n"
      "ds_read_b128 v[48:51], v0 offset:5632\n"
      "v_mfma_f32_16x16x32_bf16 v[216:219], v[104:107], a[212:215], v[216:219]\n"
      "v_cvt_pk_bf16_f32 v108, v248, v249\n"
      "v_mul_f32_dpp v221, v130, v221 row_newbcast:4 row_mask:0xf bank_mask:0xf\n"
      "v_mfma_f32_16x16x32_bf16 v[216:219], v[172:175], a[216:219], v[216:219]\n"
      "v_mfma_f32_16x16x32_bf16 v[216:219], v[132:135], a[220:223], v[216:219]\n"
      "v_cvt_pk_bf16_f32 v25, v154, v155\n"
      "v_mfma_f32_16x16x32_bf16 v[100:103], a[0:3], v[190:193], 0\n"
      "ds_read2_b32 v[112:113], v16 offset0:0 offset1:2\n"
      "v_mfma_f32_16x16x32_bf16 v[100:103], a[4:7], v[176:179], v[100:103]\n"
      "v_mfma_f32_16x16x32_bf16 v[100:103], a[8:11], v[140:143], v[100:103]\n"
      "ds_read_b128 v[194:197], v0 offset:5120\n"
      "v_mfma_f32_16x16x32_bf16 v[100:103], a[12:15], v[208:211], v[100:103]\n"
      "s_waitcnt lgkmcnt(6)\n"
      "v_smfmac_f32_16x16x64_bf16 v[152:155], v[116:119], v[236:243], v5\n"
      "buffer_store_dwordx2 v[38:39], v2, s[44:47], s34 offen offset:512 nt sc1\n"
      "v_mfma_f32_16x16x32_bf16 v[128:131], v[236:239], v[24:27], 0\n"
      "v_mfma_f32_16x16x32_bf16 v[128:131], v[240:243], v[28:31], v[128:131]\n"
      "ds_read_b128 v[180:183], v0 offset:13824\n"
      "v_cvt_pk_bf16_f32 v52, v88, v89\n"
      "v_mfma_f32_16x16x32_bf16 v[88:91], v[64:67], v[24:27], 0\n"
      "v_cvt_pk_bf16_f32 v214, v214, v218\n"
      "v_mfma_f32_16x16x32_bf16 v[88:91], v[68:71], v[28:31], v[88:91]\n"
      "s_waitcnt lgkmcnt(6)\n"
      "v_smfmac_f32_16x16x64_bf16 v[246:249], v[116:119], v[120:127], v5\n"
      "ds_read_b128 v[120:123], v0 offset:4608\n"
      "v_mfma_f32_16x16x32_bf16 v[200:203], a[48:51], v[190:193], 0\n"
      "v_cvt_pk_bf16_f32 v229, v22, v23\n"
      "v_cvt_pk_bf16_f32 v109, v250, v251\n"
      "v_mfma_f32_16x16x32_bf16 v[200:203], a[52:55], v[176:179], v[200:203]\n"
      "v_cvt_pk_bf16_f32 v59, v138, v139\n"
      "v_cvt_pk_bf16_f32 v212, v212, v216\n"
      "v_mfma_f32_16x16x32_bf16 v[200:203], a[56:59], v[140:143], v[200:203]\n"
      "v_cvt_pk_bf16_f32 v216, v213, v217\n"
      "v_add_f32 v152, v152, v153\n"
      "v_mfma_f32_16x16x32_bf16 v[200:203], a[60:63], v[208:211], v[200:203]\n"
      "v_cvt_pk_bf16_f32 v218, v215, v219\n"
      "v_cvt_pk_bf16_f32 v41, v186, v187\n"
      "v_mfma_f32_16x16x32_bf16 v[220:223], v[144:147], a[224:227], v[220:223]\n"
      "v_mfma_f32_16x16x32_bf16 v[220:223], v[104:107], a[228:231], v[220:223]\n"
      "v_cvt_pk_bf16_f32 v40, v184, v185\n"
      "v_mfma_f32_16x16x32_bf16 v[220:223], v[172:175], a[232:235], v[220:223]\n"
      "v_exp_f32 v129, v129\n"
      "v_mfma_f32_16x16x32_bf16 v[220:223], v[132:135], a[236:239], v[220:223]\n"
      "buffer_store_dwordx2 v[188:189], v2, s[44:47], s101 offen offset:0 nt sc1\n"
      "s_waitcnt lgkmcnt(5)\n"
      "v_mfma_f32_16x16x32_bf16 v[36:39], a[0:3], v[156:159], 0\n"
      "v_cvt_pk_bf16_f32 v111, v202, v203\n"
      "s_waitcnt lgkmcnt(0)\n"
      "v_mfma_f32_16x16x32_bf16 v[36:39], a[4:7], v[120:123], v[36:39]\n"
      "v_mfma_f32_16x16x32_bf16 v[36:39], a[8:11], v[194:197], v[36:39]\n"
      "v_exp_f32 v89, v89\n"
      "v_mfma_f32_16x16x32_bf16 v[36:39], a[12:15], v[48:51], v[36:39]\n"
      "v_cvt_pk_bf16_f32 v105, v102, v103\n"
      "v_mfma_f32_16x16x32_bf16 v[124:127], v[156:159], a[80:83], 0\n"
      "v_pk_max_i16 v216, v216, 0\n"
      "v_cvt_pk_bf16_f32 v215, v222, v226\n"
      "v_mfma_f32_16x16x32_bf16 v[124:127], v[120:123], a[84:87], v[124:127]\n"
      "v_mfma_f32_16x16x32_bf16 v[124:127], v[194:197], a[88:91], v[124:127]\n"
      "v_cvt_pk_bf16_f32 v219, v223, v227\n"
      "v_mfma_f32_16x16x32_bf16 v[124:127], v[48:51], a[92:95], v[124:127]\n"
      "v_pk_max_i16 v219, v219, 0\n"
      "v_cvt_pk_bf16_f32 v54, v168, v169\n"
      "v_mfma_f32_16x16x32_bf16 v[172:175], v[190:193], a[64:67], 0\n"
      "v_cvt_pk_bf16_f32 v110, v200, v201\n"
      "v_mfma_f32_16x16x32_bf16 v[172:175], v[176:179], a[68:71], v[172:175]\n"
      "v_mfma_f32_16x16x32_bf16 v[172:175], v[140:143], a[72:75], v[172:175]\n"
      "v_mfma_f32_16x16x32_bf16 v[172:175], v[208:211], a[76:79], v[172:175]\n"
      "v_exp_f32 v130, v130\n"
      "v_mfma_f32_16x16x32_bf16 v[200:203], a[48:51], v[156:159], 0\n"
      "v_mfma_f32_16x16x32_bf16 v[200:203], a[52:55], v[120:123], v[200:203]\n"
      "v_cvt_pk_bf16_f32 v213, v220, v224\n"
      "v_cvt_pk_bf16_f32 v185, v38, v39\n"
      "v_mfma_f32_16x16x32_bf16 v[200:203], a[56:59], v[194:197], v[200:203]\n"
      "s_nop 1\n"
      "v_cvt_pk_bf16_f32 v144, v172, v173\n"
      "v_mfma_f32_16x16x32_bf16 v[200:203], a[60:63], v[48:51], v[200:203]\n"
      "v_exp_f32 v88, v88\n"
      "v_mfma_f32_16x16x32_bf16 v[168:171], a[16:19], v[190:193], 0\n"
      "ds_read_b128 v[248:251], v0 offset:13312\n"
      "v_pk_max_i16 v213, v213, 0\n"
      "v_mfma_f32_16x16x32_bf16 v[168:171], a[20:23], v[176:179], v[168:171]\n"
      "v_exp_f32 v91, v91\n"
      "v_mfma_f32_16x16x32_bf16 v[168:171], a[24:27], v[140:143], v[168:171]\n"
      "v_cvt_pk_bf16_f32 v56, v252, v253\n"
      "v_cvt_pk_bf16_f32 v39, v126, v127\n"
      "v_mfma_f32_16x16x32_bf16 v[168:171], a[28:31], v[208:211], v[168:171]\n"
      "ds_read_b128 v[252:255], v0 offset:12800\n"
      "v_mfma_f32_16x16x32_bf16 v[176:179], a[16:19], v[156:159], 0\n"
      "v_cvt_pk_bf16_f32 v191, v202, v203\n"
      "v_mfma_f32_16x16x32_bf16 v[176:179], a[20:23], v[120:123], v[176:179]\n"
      "v_mfma_f32_16x16x32_bf16 v[176:179], a[24:27], v[194:197], v[176:179]\n"
      "v_cvt_pk_bf16_f32 v38, v124, v125\n"
      "v_cvt_pk_bf16_f32 v217, v221, v225\n"
      "v_mfma_f32_16x16x32_bf16 v[176:179], a[28:31], v[48:51], v[176:179]\n"
      "buffer_store_dwordx2 v[34:35], v2, s[44:47], s100 offen offset:768 nt sc1\n"
      "v_mfma_f32_16x16x32_bf16 v[208:211], a[32:35], v[156:159], 0\n"
      "v_cvt_pk_bf16_f32 v190, v200, v201\n"
      "v_cvt_pk_bf16_f32 v106, v168, v169\n"
      "v_mfma_f32_16x16x32_bf16 v[208:211], a[36:39], v[120:123], v[208:211]\n"
      "v_mfma_f32_16x16x32_bf16 v[208:211], a[40:43], v[194:197], v[208:211]\n"
      "v_mfma_f32_16x16x32_bf16 v[208:211], a[44:47], v[48:51], v[208:211]\n"
      "s_nop 0\n"
      "v_cvt_pk_bf16_f32 v187, v178, v179\n"
      "v_mov_b64 v[138:139], 0\n"
      "v_mfma_f32_16x16x32_bf16 v[140:143], a[0:3], v[232:235], 0\n"
      "v_cvt_pk_bf16_f32 v126, v88, v89\n"
      "s_waitcnt lgkmcnt(0)\n"
      "v_mfma_f32_16x16x32_bf16 v[140:143], a[4:7], v[252:255], v[140:143]\n"
      "v_cvt_pk_bf16_f32 v186, v176, v177\n"
      "v_mfma_f32_16x16x32_bf16 v[140:143], a[8:11], v[248:251], v[140:143]\n"
      "v_exp_f32 v90, v90\n"
      "v_mfma_f32_16x16x32_bf16 v[140:143], a[12:15], v[180:183], v[140:143]\n"
      "v_pk_max_i16 v43, v43, 0\n"
      "v_pk_max_i16 v212, v212, 0\n"
      "v_mfma_f32_16x16x32_bf16 v[132:135], v[232:235], a[80:83], 0\n"
      "v_mfma_f32_16x16x32_bf16 v[132:135], v[252:255], a[84:87], v[132:135]\n"
      "v_exp_f32 v128, v128\n"
      "v_mfma_f32_16x16x32_bf16 v[132:135], v[248:251], a[88:91], v[132:135]\n"
      "v_mfma_f32_16x16x32_bf16 v[132:135], v[180:183], a[92:95], v[132:135]\n"
      "v_cvt_pk_bf16_f32 v124, v128, v129\n"
      "v_smfmac_f32_16x16x64_bf16 v[44:47], v[116:119], v[64:71], v5\n"
      "buffer_store_dwordx2 v[42:43], v2, s[44:47], s34 offen offset:768 nt sc1\n"
      "v_mfma_f32_16x16x32_bf16 v[200:203], a[16:19], v[232:235], 0\n"
      "v_exp_f32 v152, v152\n"
      "v_mfma_f32_16x16x32_bf16 v[200:203], a[20:23], v[252:255], v[200:203]\n"
      "v_cvt_pk_bf16_f32 v104, v100, v101\n"
      "v_add_f32 v246, v246, v247\n"
      "v_mfma_f32_16x16x32_bf16 v[200:203], a[24:27], v[248:251], v[200:203]\n"
      "v_cvt_pk_bf16_f32 v43, v134, v135\n"
      "v_add_f32 v44, v44, v45\n"
      "v_mfma_f32_16x16x32_bf16 v[200:203], a[28:31], v[180:183], v[200:203]\n"
      "v_pk_max_i16 v217, v217, 0\n"
      "v_cvt_pk_bf16_f32 v107, v170, v171\n"
      "v_mfma_f32_16x16x32_bf16 v[224:227], a[48:51], v[232:235], 0\n"
      "v_exp_f32 v131, v131\n"
      "v_mfma_f32_16x16x32_bf16 v[224:227], a[52:55], v[252:255], v[224:227]\n"
      "v_mov_b32 v247, 0\n"
      "v_mfma_f32_16x16x32_bf16 v[224:227], a[56:59], v[248:251], v[224:227]\n"
      "v_cvt_pk_bf16_f32 v189, v210, v211\n"
      "v_cvt_pk_bf16_f32 v188, v208, v209\n"
      "v_mfma_f32_16x16x32_bf16 v[224:227], a[60:63], v[180:183], v[224:227]\n"
      "v_cvt_pk_bf16_f32 v184, v36, v37\n"
      "v_cvt_pk_bf16_f32 v170, v200, v201\n"
      "v_mfma_f32_16x16x32_bf16 v[208:211], v[156:159], a[96:99], 0\n"
      "s_waitcnt vmcnt(11)\n"
      "ds_read_b128 v[34:37], v0 offset:0\n"
      "v_cvt_pk_bf16_f32 v127, v90, v91\n"
      "v_mfma_f32_16x16x32_bf16 v[208:211], v[120:123], a[100:103], v[208:211]\n"
      "ds_read_b128 v[176:179], v0 offset:1536\n"
      "v_cvt_pk_bf16_f32 v125, v130, v131\n"
      "v_mfma_f32_16x16x32_bf16 v[208:211], v[194:197], a[104:107], v[208:211]\n"
      "ds_read_b128 v[220:223], v0 offset:1024\n"
      "v_cvt_pk_bf16_f32 v228, v20, v21\n"
      "v_mfma_f32_16x16x32_bf16 v[208:211], v[48:51], a[108:111], v[208:211]\n"
      "ds_read2_b32 v[114:115], v16 offset0:128 offset1:130\n"
      "v_cvt_pk_bf16_f32 v171, v202, v203\n"
      "v_mfma_f32_16x16x32_bf16 v[88:91], v[156:159], a[64:67], 0\n"
      "v_cvt_pk_bf16_f32 v42, v132, v133\n"
      "v_mfma_f32_16x16x32_bf16 v[88:91], v[120:123], a[68:71], v[88:91]\n"
      "v_exp_f32_dpp v247, v246 quad_perm:[0,1,2,3] row_mask:0xf bank_mask:0x1\n"
      "v_mfma_f32_16x16x32_bf16 v[88:91], v[194:197], a[72:75], v[88:91]\n"
      "v_cvt_pk_bf16_f32 v137, v230, v231\n"
      "v_mov_b64 v[20:21], 0\n"
      "v_mfma_f32_16x16x32_bf16 v[88:91], v[48:51], a[76:79], v[88:91]\n"
      "buffer_store_dwordx2 v[212:213], v2, s[44:47], s35 offen offset:0 nt sc1\n"
      "ds_read_b128 v[128:131], v0 offset:512\n"
      "v_mfma_f32_16x16x32_bf16 v[100:103], v[156:159], a[112:115], 0\n"
      "v_mfma_f32_16x16x32_bf16 v[100:103], v[120:123], a[116:119], v[100:103]\n"
      "v_cvt_pk_bf16_f32 v168, v140, v141\n"
      "v_cvt_pk_bf16_f32 v145, v174, v175\n"
      "v_mfma_f32_16x16x32_bf16 v[100:103], v[194:197], a[120:123], v[100:103]\n"
      "v_cvt_pk_bf16_f32 v174, v224, v225\n"
      "v_mfma_f32_16x16x32_bf16 v[100:103], v[48:51], a[124:127], v[100:103]\n"
      "v_pk_max_i16 v218, v218, 0\n"
      "v_smfmac_f32_16x16x64_bf16 v[138:141], v[116:119], v[184:191], v5\n"
      "v_mfma_f32_16x16x32_bf16 v[120:123], v[104:107], v[24:27], 0\n"
      "v_pk_max_i16 v215, v215, 0\n"
      "v_cvt_pk_bf16_f32 v135, v210, v211\n"
      "v_mfma_f32_16x16x32_bf16 v[120:123], v[108:111], v[28:31], v[120:123]\n"
      "s_waitcnt lgkmcnt(1)\n"
      "v_mov_b32_dpp v194, v114 row_ror:8 row_mask:0xf bank_mask:0xf\n"
      "v_mov_b32_dpp v246, v247 quad_perm:[1,1,3,3] row_mask:0xf bank_mask:0xf\n"
      "v_smfmac_f32_16x16x64_bf16 v[20:23], v[116:119], v[104:111], v5\n"
      "v_add_f32 v138, v138, v139\n"
      "v_mfma_f32_16x16x32_bf16 v[48:51], a[32:35], v[34:37], 0\n"
      "s_waitcnt lgkmcnt(0)\n"
      "v_mfma_f32_16x16x32_bf16 v[48:51], a[36:39], v[128:131], v[48:51]\n"
      "v_mfma_f32_16x16x32_bf16 v[48:51], a[40:43], v[220:223], v[48:51]\n"
      "v_cvt_pk_bf16_f32 v246, v247, v246\n"
      "v_cvt_pk_bf16_f32 v175, v226, v227\n"
      "v_mfma_f32_16x16x32_bf16 v[48:51], a[44:47], v[176:179], v[48:51]\n"
      "v_exp_f32 v44, v44\n"
      "v_mfma_f32_16x16x32_bf16 v[200:203], a[48:51], v[34:37], 0\n"
      "v_exp_f32 v121, v121\n"
      "v_mfma_f32_16x16x32_bf16 v[200:203], a[52:55], v[128:131], v[200:203]\n"
      "v_cvt_pk_bf16_f32 v169, v142, v143\n"
      "v_cndmask_b32_dpp v153, v44, v152, vcc quad_perm:[0,0,2,2] row_mask:0xf bank_mask:0xf\n"
      "v_mfma_f32_16x16x32_bf16 v[200:203], a[56:59], v[220:223], v[200:203]\n"
      "v_exp_f32 v120, v120\n"
      "v_mfma_f32_16x16x32_bf16 v[200:203], a[60:63], v[176:179], v[200:203]\n"
      "v_cvt_pk_bf16_f32 v227, v102, v103\n"
      "v_pk_max_i16 v214, v214, 0\n"
      "v_mfma_f32_16x16x32_bf16 v[156:159], a[16:19], v[34:37], 0\n"
      "v_cvt_pk_bf16_f32 v134, v208, v209\n"
      "v_cvt_pk_bf16_f32 v226, v100, v101\n"
      "v_mfma_f32_16x16x32_bf16 v[156:159], a[20:23], v[128:131], v[156:159]\n"
      "v_mfma_f32_16x16x32_bf16 v[156:159], a[24:27], v[220:223], v[156:159]\n"
      "v_cvt_pk_bf16_f32 v143, v90, v91\n"
      "v_cvt_pk_bf16_f32 v142, v88, v89\n"
      "v_mfma_f32_16x16x32_bf16 v[156:159], a[28:31], v[176:179], v[156:159]\n"
      "buffer_store_dwordx2 v[214:215], v2, s[44:47], s35 offen offset:512 nt sc1\n"
      "v_mov_b32_dpp v192, v112 row_ror:8 row_mask:0xf bank_mask:0xf\n"
      "v_mfma_f32_16x16x32_bf16 v[88:91], a[0:3], v[34:37], 0\n"
      "v_add_f32 v20, v20, v21\n"
      "v_mfma_f32_16x16x32_bf16 v[88:91], a[4:7], v[128:131], v[88:91]\n"
      "v_cvt_pk_bf16_f32 v214, v200, v201\n"
      "v_cvt_pk_bf16_f32 v213, v50, v51\n"
      "v_mfma_f32_16x16x32_bf16 v[88:91], a[8:11], v[220:223], v[88:91]\n"
      "v_mfma_f32_16x16x32_bf16 v[88:91], a[12:15], v[176:179], v[88:91]\n"
      "v_cvt_pk_bf16_f32 v215, v202, v203\n"
      "v_cvt_pk_bf16_f32 v211, v158, v159\n"
      "v_mfma_f32_16x16x32_bf16 v[100:103], v[34:37], a[112:115], 0\n"
      "v_cvt_pk_bf16_f32 v212, v48, v49\n"
      "v_mfma_f32_16x16x32_bf16 v[100:103], v[128:131], a[116:119], v[100:103]\n"
      "v_mov_b32_dpp v195, v115 row_ror:8 row_mask:0xf bank_mask:0xf\n"
      "v_mfma_f32_16x16x32_bf16 v[100:103], v[220:223], a[120:123], v[100:103]\n"
      "ds_bpermute_b32 v158, v7, v246 offset:248\n"
      "v_mfma_f32_16x16x32_bf16 v[100:103], v[176:179], a[124:127], v[100:103]\n"
      "v_cvt_pk_bf16_f32 v208, v88, v89\n"
      "v_cvt_pk_bf16_f32 v209, v90, v91\n"
      "v_mfma_f32_16x16x32_bf16 v[200:203], a[32:35], v[232:235], 0\n"
      "v_exp_f32 v122, v122\n"
      "v_mfma_f32_16x16x32_bf16 v[200:203], a[36:39], v[252:255], v[200:203]\n"
      "v_cvt_pk_bf16_f32 v210, v156, v157\n"
      "v_mfma_f32_16x16x32_bf16 v[200:203], a[40:43], v[248:251], v[200:203]\n"
      "s_nop 0\n"
      "v_cvt_pk_bf16_f32 v225, v102, v103\n"
      "v_mfma_f32_16x16x32_bf16 v[200:203], a[44:47], v[180:183], v[200:203]\n"
      "v_cvt_pk_bf16_f32 v224, v100, v101\n"
      "v_mov_b64 v[88:89], 0\n"
      "v_mfma_f32_16x16x32_bf16 v[100:103], v[34:37], a[96:99], 0\n"
      "v_exp_f32 v123, v123\n"
      "v_mfma_f32_16x16x32_bf16 v[100:103], v[128:131], a[100:103], v[100:103]\n"
      "ds_bpermute_b32 v159, v7, v246 offset:8\n"
      "v_mfma_f32_16x16x32_bf16 v[100:103], v[220:223], a[104:107], v[100:103]\n"
      "v_mfma_f32_16x16x32_bf16 v[100:103], v[176:179], a[108:111], v[100:103]\n"
      "buffer_store_dwordx2 v[32:33], v2, s[44:47], s100 offen offset:256 nt sc1\n"
      "v_cvt_pk_bf16_f32 v173, v202, v203\n"
      "v_smfmac_f32_16x16x64_bf16 v[88:91], v[116:119], v[208:215], v5\n"
      "v_cvt_pk_bf16_f32 v172, v200, v201\n"
      "v_mfma_f32_16x16x32_bf16 v[48:51], v[232:235], a[112:115], 0\n"
      "v_exp_f32 v138, v138\n"
      "v_mfma_f32_16x16x32_bf16 v[48:51], v[252:255], a[116:119], v[48:51]\n"
      "v_mov_b32_dpp v193, v113 row_ror:8 row_mask:0xf bank_mask:0xf\n"
      "v_mov_b64 v[200:201], 0\n"
      "v_mfma_f32_16x16x32_bf16 v[48:51], v[248:251], a[120:123], v[48:51]\n"
      "v_cvt_pk_bf16_f32 v133, v102, v103\n"
      "v_cvt_pk_bf16_f32 v132, v100, v101\n"
      "v_mfma_f32_16x16x32_bf16 v[48:51], v[180:183], a[124:127], v[48:51]\n"
      "v_exp_f32 v20, v20\n"
      "v_smfmac_f32_16x16x64_bf16 v[200:203], v[116:119], v[168:175], v5\n"
      "v_add_f32 v88, v88, v89\n"
      "v_mfma_f32_16x16x32_bf16 v[100:103], v[34:37], a[80:83], 0\n"
      "v_mov_b64 v[116:117], 0\n"
      "v_mov_b64 v[118:119], 0\n"
      "v_mfma_f32_16x16x32_bf16 v[100:103], v[128:131], a[84:87], v[100:103]\n"
      "s_nop 0\n"
      "v_cvt_pk_bf16_f32 v230, v48, v49\n"
      "v_cvt_pk_bf16_f32 v231, v50, v51\n"
      "v_mfma_f32_16x16x32_bf16 v[100:103], v[220:223], a[88:91], v[100:103]\n"
      "v_exp_f32 v88, v88\n"
      "v_mfma_f32_16x16x32_bf16 v[100:103], v[176:179], a[92:95], v[100:103]\n"
      "v_add_f32 v200, v200, v201\n"
      "v_cndmask_b32_dpp v89, v20, v88, vcc quad_perm:[0,0,2,2] row_mask:0xf bank_mask:0xf\n"
      "v_mfma_f32_16x16x32_bf16 v[196:199], v[34:37], a[64:67], 0\n"
      "v_exp_f32 v200, v200\n"
      "v_mfma_f32_16x16x32_bf16 v[196:199], v[128:131], a[68:71], v[196:199]\n"
      "v_cvt_pk_bf16_f32 v49, v122, v123\n"
      "v_cndmask_b32_dpp v139, v200, v138, vcc quad_perm:[0,0,2,2] row_mask:0xf bank_mask:0xf\n"
      "v_mfma_f32_16x16x32_bf16 v[196:199], v[220:223], a[72:75], v[196:199]\n"
      "L_h6_%=:\n"
      "s_cbranch_execz L_hr_%=\n"
      "v_cvt_pk_bf16_f32 v37, v102, v103\n"
      "v_cvt_pk_bf16_f32 v48, v120, v121\n"
      "v_mfma_f32_16x16x32_bf16 v[196:199], v[176:179], a[76:79], v[196:199]\n"
      "buffer_store_dwordx2 v[216:217], v2, s[44:47], s35 offen offset:256 nt sc1\n"
      "v_cvt_pk_bf16_f32 v36, v100, v101\n"
      "v_mfma_f32_16x16x32_bf16 v[120:123], v[184:187], v[24:27], 0\n"
      "s_not_b64 vcc, vcc\n"
      "v_cndmask_b32_dpp v45, v152, v44, vcc quad_perm:[1,1,3,3] row_mask:0xf bank_mask:0xf\n"
      "v_cndmask_b32_dpp v201, v138, v200, vcc quad_perm:[1,1,3,3] row_mask:0xf bank_mask:0xf\n"
      "v_mfma_f32_16x16x32_bf16 v[120:123], v[188:191], v[28:31], v[120:123]\n"
      "v_cvt_pk_bf16_f32 v153, v153, v45\n"
      "v_cvt_pk_bf16_f32 v140, v196, v197\n"
      "v_mfma_f32_16x16x32_bf16 v[32:35], v[208:211], v[52:55], 0\n"
      "v_cndmask_b32_dpp v21, v88, v20, vcc quad_perm:[1,1,3,3] row_mask:0xf bank_mask:0xf\n"
      "v_add_f32 v88, v88, v138\n"
      "v_mfma_f32_16x16x32_bf16 v[32:35], v[212:215], v[56:59], v[32:35]\n"
      "v_add_f32 v152, v152, v44\n"
      "v_cvt_pk_bf16_f32 v139, v139, v201\n"
      "v_mfma_f32_16x16x32_bf16 v[100:103], v[168:171], v[24:27], 0\n"
      "v_cvt_pk_bf16_f32 v89, v89, v21\n"
      "v_add_f32 v20, v20, v200\n"
      "v_mfma_f32_16x16x32_bf16 v[100:103], v[172:175], v[28:31], v[100:103]\n"
      "v_add_f32 v152, v152, v247\n"
      "v_cvt_pk_bf16_f32 v141, v198, v199\n"
      "v_smfmac_f32_16x16x64_bf16 v[116:119], v[112:115], v[24:31], v5\n"
      "ds_bpermute_b32 v161, v6, v139\n"
      "v_smfmac_f32_16x16x64_bf16 v[116:119], v[192:195], v[52:59], v5\n"
      "ds_bpermute_b32 v19, v7, v153 offset:4\n"
      "v_mfma_f32_16x16x32_bf16 v[200:203], v[208:211], v[24:27], 0\n"
      "v_exp_f32 v122, v122\n"
      "v_mfma_f32_16x16x32_bf16 v[200:203], v[212:215], v[28:31], v[200:203]\n"
      "v_exp_f32 v102, v102\n"
      "v_mfma_f32_16x16x32_bf16 v[176:179], v[232:235], a[64:67], 0\n"
      "v_exp_f32 v101, v101\n"
      "v_mfma_f32_16x16x32_bf16 v[176:179], v[252:255], a[68:71], v[176:179]\n"
      "ds_bpermute_b32 v162, v6, v89 offset:16\n"
      "v_mfma_f32_16x16x32_bf16 v[176:179], v[248:251], a[72:75], v[176:179]\n"
      "v_exp_f32 v120, v120\n"
      "v_mfma_f32_16x16x32_bf16 v[176:179], v[180:183], a[76:79], v[176:179]\n"
      "v_exp_f32 v35, v35\n"
      "v_mfma_f32_16x16x32_bf16 v[244:247], v[168:171], v[52:55], 0\n"
      "v_exp_f32 v200, v200\n"
      "v_mfma_f32_16x16x32_bf16 v[244:247], v[172:175], v[56:59], v[244:247]\n"
      "v_exp_f32 v203, v203\n"
      "v_mfma_f32_16x16x32_bf16 v[24:27], v[236:239], v[52:55], 0\n"
      "v_exp_f32 v100, v100\n"
      "v_mfma_f32_16x16x32_bf16 v[24:27], v[240:243], v[56:59], v[24:27]\n"
      "v_exp_f32 v202, v202\n"
      "v_add_f32 v88, v88, v20\n"
      "v_mfma_f32_16x16x32_bf16 v[28:31], v[104:107], v[52:55], 0\n"
      "v_exp_f32 v121, v121\n"
      "v_mfma_f32_16x16x32_bf16 v[28:31], v[108:111], v[56:59], v[28:31]\n"
      "v_exp_f32 v244, v244\n"
      "v_mfma_f32_16x16x32_bf16 v[196:199], a[128:131], v[232:235], 0\n"
      "s_nop 0\n"
      "v_exp_f32 v24, v24\n"
      "v_mfma_f32_16x16x32_bf16 v[196:199], a[132:135], v[252:255], v[196:199]\n"
      "v_exp_f32 v27, v27\n"
      "v_mfma_f32_16x16x32_bf16 v[196:199], a[136:139], v[248:251], v[196:199]\n"
      "s_nop 0\n"
      "v_exp_f32 v30, v30\n"
      "v_mfma_f32_16x16x32_bf16 v[196:199], a[140:143], v[180:183], v[196:199]\n"
      "v_exp_f32 v201, v201\n"
      "v_mfma_f32_16x16x32_bf16 v[128:131], v[72:75], a[96:99], 0\n"
      "v_cvt_pk_bf16_f32 v147, v178, v179\n"
      "v_cvt_pk_bf16_f32 v50, v100, v101\n"
      "v_mfma_f32_16x16x32_bf16 v[128:131], v[148:151], a[100:103], v[128:131]\n"
      "v_cvt_pk_bf16_f32 v44, v200, v201\n"
      "v_cvt_pk_bf16_f32 v46, v120, v121\n"
      "v_mfma_f32_16x16x32_bf16 v[128:131], v[164:167], a[104:107], v[128:131]\n"
      "ds_bpermute_b32 v163, v6, v139 offset:16\n"
      "v_mfma_f32_16x16x32_bf16 v[128:131], v[204:207], a[108:111], v[128:131]\n"
      "buffer_store_dwordx2 v[218:219], v2, s[44:47], s35 offen offset:768 nt sc1\n"
      "v_exp_f32 v25, v25\n"
      "v_mfma_f32_16x16x32_bf16 v[20:23], v[184:187], v[52:55], 0\n"
      "v_exp_f32 v28, v28\n"
      "v_mfma_f32_16x16x32_bf16 v[20:23], v[188:191], v[56:59], v[20:23]\n"
      "v_exp_f32 v123, v123\n"
      "v_mfma_f32_16x16x32_bf16 v[220:223], v[64:67], v[52:55], 0\n"
      "v_add_f32 v118, v118, v119\n"
      "v_add_f32 v116, v116, v117\n"
      "v_mfma_f32_16x16x32_bf16 v[220:223], v[68:71], v[56:59], v[220:223]\n"
      "v_cvt_pk_bf16_f32 v146, v176, v177\n"
      "v_cvt_pk_bf16_f32 v45, v202, v203\n"
      "v_cvt_pk_bf16_f32 v47, v122, v123\n"
      "v_mfma_f32_16x16x32_bf16 v[52:55], v[92:95], a[96:99], 0\n"
      "v_exp_f32 v21, v21\n"
      "v_mfma_f32_16x16x32_bf16 v[52:55], v[76:79], a[100:103], v[52:55]\n"
      "ds_bpermute_b32 v160, v6, v89\n"
      "v_mfma_f32_16x16x32_bf16 v[52:55], v[84:87], a[104:107], v[52:55]\n"
      "v_exp_f32 v116, v116\n"
      "v_mfma_f32_16x16x32_bf16 v[52:55], v[80:83], a[108:111], v[52:55]\n"
      "v_exp_f32 v31, v31\n"
      "v_mfma_f32_16x16x32_bf16 v[154:157], a[176:179], v[232:235], 0\n"
      "v_exp_f32 v247, v247\n"
      "v_mfma_f32_16x16x32_bf16 v[154:157], a[180:183], v[252:255], v[154:157]\n"
      "v_exp_f32 v246, v246\n"
      "v_mfma_f32_16x16x32_bf16 v[154:157], a[184:187], v[248:251], v[154:157]\n"
      "v_exp_f32 v245, v245\n"
      "v_mfma_f32_16x16x32_bf16 v[154:157], a[188:191], v[180:183], v[154:157]\n"
      "v_exp_f32 v222, v222\n"
      "v_mfma_f32_16x16x32_bf16 v[176:179], v[92:95], a[80:83], 0\n"
      "v_exp_f32 v220, v220\n"
      "v_mfma_f32_16x16x32_bf16 v[176:179], v[76:79], a[84:87], v[176:179]\n"
      "v_cvt_pk_bf16_f32 v122, v244, v245\n"
      "v_cvt_pk_bf16_f32 v216, v196, v197\n"
      "v_mfma_f32_16x16x32_bf16 v[176:179], v[84:87], a[88:91], v[176:179]\n"
      "v_exp_f32 v103, v103\n"
      "v_mfma_f32_16x16x32_bf16 v[176:179], v[80:83], a[92:95], v[176:179]\n"
      "v_cvt_pk_bf16_f32 v51, v102, v103\n"
      "v_add_f32 v200, v88, v152\n"
      "v_mfma_f32_16x16x32_bf16 v[100:103], v[232:235], a[96:99], 0\n"
      "v_exp_f32 v118, v118\n"
      "v_mfma_f32_16x16x32_bf16 v[100:103], v[252:255], a[100:103], v[100:103]\n"
      "v_exp_f32 v26, v26\n"
      "v_mfma_f32_16x16x32_bf16 v[100:103], v[248:251], a[104:107], v[100:103]\n"
      "v_cvt_pk_bf16_f32 v123, v246, v247\n"
      "v_cvt_pk_bf16_f32 v246, v176, v177\n"
      "v_mfma_f32_16x16x32_bf16 v[100:103], v[180:183], a[108:111], v[100:103]\n"
      "v_cvt_pk_bf16_f32 v247, v178, v179\n"
      "v_cvt_pk_bf16_f32 v217, v198, v199\n"
      "v_mfma_f32_16x16x32_bf16 v[176:179], v[72:75], a[64:67], 0\n"
      "ds_bpermute_b32 v18, v7, v153\n"
      "v_mfma_f32_16x16x32_bf16 v[176:179], v[148:151], a[68:71], v[176:179]\n"
      "v_exp_f32 v34, v34\n"
      "v_mfma_f32_16x16x32_bf16 v[176:179], v[164:167], a[72:75], v[176:179]\n"
      "v_exp_f32 v22, v22\n"
      "v_mfma_f32_16x16x32_bf16 v[176:179], v[204:207], a[76:79], v[176:179]\n"
      "v_cvt_pk_bf16_f32 v138, v100, v101\n"
      "v_cvt_pk_bf16_f32 v60, v128, v129\n"
      "v_mfma_f32_16x16x32_bf16 v[196:199], v[72:75], a[80:83], 0\n"
      "v_cvt_pk_bf16_f32 v63, v54, v55\n"
      "v_cvt_pk_bf16_f32 v62, v52, v53\n"
      "v_mfma_f32_16x16x32_bf16 v[196:199], v[148:151], a[84:87], v[196:199]\n"
      "v_add_f32 v101, v116, v118\n"
      "v_cvt_pk_bf16_f32 v139, v102, v103\n"
      "v_mfma_f32_16x16x32_bf16 v[196:199], v[164:167], a[88:91], v[196:199]\n"
      "v_exp_f32 v32, v32\n"
      "v_mfma_f32_16x16x32_bf16 v[196:199], v[204:207], a[92:95], v[196:199]\n"
      "v_cvt_pk_bf16_f32 v61, v130, v131\n"
      "v_exp_f32 v223, v223\n"
      "v_mfma_f32_16x16x32_bf16 v[52:55], v[92:95], a[64:67], 0\n"
      "v_cvt_pk_bf16_f32 v89, v178, v179\n"
      "v_add_f32 v100, v116, v118\n"
      "v_mfma_f32_16x16x32_bf16 v[52:55], v[76:79], a[68:71], v[52:55]\n"
      "v_cvt_pk_bf16_f32 v203, v116, v118\n"
      "v_cvt_pk_bf16_f32 v88, v176, v177\n"
      "v_mfma_f32_16x16x32_bf16 v[52:55], v[84:87], a[72:75], v[52:55]\n"
      "v_cvt_pk_bf16_f32 v202, v116, v118\n"
      "v_cvt_pk_bf16_f32 v128, v24, v25\n"
      "v_mfma_f32_16x16x32_bf16 v[52:55], v[80:83], a[76:79], v[52:55]\n"
      "v_exp_f32 v23, v23\n"
      "v_mfma_f32_16x16x32_bf16 v[116:119], a[160:163], v[232:235], 0\n"
      "v_exp_f32 v221, v221\n"
      "v_mfma_f32_16x16x32_bf16 v[116:119], a[164:167], v[252:255], v[116:119]\n"
      "v_cvt_pk_bf16_f32 v131, v222, v223\n"
      "v_cvt_pk_bf16_f32 v130, v220, v221\n"
      "v_mfma_f32_16x16x32_bf16 v[116:119], a[168:171], v[248:251], v[116:119]\n"
      "v_cvt_pk_bf16_f32 v244, v196, v197\n"
      "v_cvt_pk_bf16_f32 v223, v156, v157\n"
      "v_mfma_f32_16x16x32_bf16 v[116:119], a[172:175], v[180:183], v[116:119]\n"
      "v_cvt_pk_bf16_f32 v91, v54, v55\n"
      "v_cvt_pk_bf16_f32 v222, v154, v155\n"
      "v_mfma_f32_16x16x32_bf16 v[176:179], a[144:147], v[232:235], 0\n"
      "v_cvt_pk_bf16_f32 v121, v30, v31\n"
      "v_cvt_pk_bf16_f32 v90, v52, v53\n"
      "v_mfma_f32_16x16x32_bf16 v[176:179], a[148:151], v[252:255], v[176:179]\n"
      "v_exp_f32 v33, v33\n"
      "v_mfma_f32_16x16x32_bf16 v[176:179], a[152:155], v[248:251], v[176:179]\n"
      "v_cvt_pk_bf16_f32 v245, v198, v199\n"
      "v_cvt_pk_bf16_f32 v220, v116, v117\n"
      "v_mfma_f32_16x16x32_bf16 v[176:179], a[156:159], v[180:183], v[176:179]\n"
      "ds_read_b64 v[156:157], v11 offset:0\n"
      "v_cvt_pk_bf16_f32 v221, v118, v119\n"
      "v_mfma_f32_16x16x32_bf16 v[52:55], v[72:75], a[112:115], 0\n"
      "v_exp_f32 v20, v20\n"
      "v_mfma_f32_16x16x32_bf16 v[52:55], v[148:151], a[116:119], v[52:55]\n"
      "v_permlane16_swap_b32 v100, v101\n"
      "v_mfma_f32_16x16x32_bf16 v[52:55], v[164:167], a[120:123], v[52:55]\n"
      "v_cvt_pk_bf16_f32 v118, v20, v21\n"
      "v_cvt_pk_bf16_f32 v119, v22, v23\n"
      "v_mfma_f32_16x16x32_bf16 v[52:55], v[204:207], a[124:127], v[52:55]\n"
      "v_permlane16_swap_b32 v202, v203\n"
      "v_mfma_f32_16x16x32_bf16 v[20:23], v[92:95], a[112:115], 0\n"
      "v_cvt_pk_bf16_f32 v116, v32, v33\n"
      "v_cvt_pk_bf16_f32 v117, v34, v35\n"
      "v_mfma_f32_16x16x32_bf16 v[20:23], v[76:79], a[116:119], v[20:23]\n"
      "v_exp_f32 v29, v29\n"
      "v_mfma_f32_16x16x32_bf16 v[20:23], v[84:87], a[120:123], v[20:23]\n"
      "v_cvt_pk_bf16_f32 v219, v178, v179\n"
      "v_cvt_pk_bf16_f32 v218, v176, v177\n"
      "v_mfma_f32_16x16x32_bf16 v[20:23], v[80:83], a[124:127], v[20:23]\n"
      "v_cvt_pk_bf16_f32 v196, v52, v53\n"
      "v_cvt_pk_bf16_f32 v120, v28, v29\n"
      "v_mov_b64 v[152:153], v[100:101]\n"
      "v_smfmac_f32_16x16x64_bf16 v[100:103], v[12:15], v[44:51], v5\n"
      "v_cvt_pk_bf16_f32 v129, v26, v27\n"
      "v_smfmac_f32_16x16x64_bf16 v[152:155], v[12:15], v[116:123], v5\n"
      "s_nop 1\n"
      "v_cvt_pk_bf16_f32 v199, v22, v23\n"
      "v_cvt_pk_bf16_f32 v198, v20, v21\n"
      "v_smfmac_f32_16x16x32_bf16 v[100:103], v[12:13], v[124:127], v5\n"
      "v_cvt_pk_bf16_f32 v197, v54, v55\n"
      "s_waitcnt lgkmcnt(0)\n"
      "v_mfma_f32_16x16x4_4b_bf16 v[20:35], v[202:203], v[156:157], 0 cbsz:2 abid:0\n"
      "s_nop 4\n"
      "v_add_f32 v100, v100, v101\n"
      "v_add_f32_dpp v200, v200, v200 row_ror:8 row_mask:0xf bank_mask:0xf\n"
      "v_smfmac_f32_16x16x32_bf16 v[152:155], v[12:13], v[128:131], v5\n"
      "s_nop 1\n"
      "v_mfma_f32_16x16x32_bf16 v[20:23], v[44:47], v[140:143], v[20:23]\n"
      "v_rcp_f32 v101, v100\n"
      "v_mfma_f32_16x16x32_bf16 v[20:23], v[48:51], v[144:147], v[20:23]\n"
      "ds_bpermute_b32 v102, v4, v101\n"
      "v_mfma_f32_16x16x32_bf16 v[20:23], v[124:127], v[88:91], v[20:23]\n"
      "v_mfma_f32_16x16x32_bf16 v[28:31], v[44:47], v[132:135], v[28:31]\n"
      "v_mfma_f32_16x16x32_bf16 v[28:31], v[48:51], v[136:139], v[28:31]\n"
      "v_mfma_f32_16x16x32_bf16 v[28:31], v[124:127], v[60:63], v[28:31]\n"
      "v_mfma_f32_16x16x32_bf16 v[24:27], v[44:47], v[36:39], v[24:27]\n"
      "v_add_f32_dpp v200, v200, v200 row_ror:4 row_mask:0xf bank_mask:0xf\n"
      "v_add_f32 v152, v152, v153\n"
      "v_mfma_f32_16x16x32_bf16 v[24:27], v[48:51], v[40:43], v[24:27]\n"
      "v_mfma_f32_16x16x32_bf16 v[24:27], v[124:127], v[244:247], v[24:27]\n"
      "v_rcp_f32 v153, v152\n"
      "v_mfma_f32_16x16x32_bf16 v[32:35], v[44:47], v[224:227], v[32:35]\n"
      "v_mfma_f32_16x16x32_bf16 v[32:35], v[48:51], v[228:231], v[32:35]\n"
      "s_waitcnt lgkmcnt(0)\n"
      "v_mul_f32_dpp v23, v102, v23 row_newbcast:12 row_mask:0xf bank_mask:0xf\n"
      "v_mfma_f32_16x16x32_bf16 v[32:35], v[124:127], v[196:199], v[32:35]\n"
      "s_nop 1\n"
      "v_mul_f32_dpp v24, v102, v24 row_newbcast:0 row_mask:0xf bank_mask:0xf\n"
      "v_mfma_f32_16x16x32_bf16 v[176:179], v[64:67], v[216:219], 0\n"
      "v_mul_f32_dpp v27, v102, v27 row_newbcast:12 row_mask:0xf bank_mask:0xf\n"
      "v_mfma_f32_16x16x32_bf16 v[176:179], v[68:71], v[220:223], v[176:179]\n"
      "v_mul_f32_dpp v21, v102, v21 row_newbcast:4 row_mask:0xf bank_mask:0xf\n"
      "v_mfma_f32_16x16x32_bf16 v[124:127], v[236:239], v[216:219], 0\n"
      "v_mfma_f32_16x16x32_bf16 v[124:127], v[240:243], v[220:223], v[124:127]\n"
      "v_mul_f32_dpp v30, v102, v30 row_newbcast:8 row_mask:0xf bank_mask:0xf\n"
      "v_mul_f32_dpp v25, v102, v25 row_newbcast:4 row_mask:0xf bank_mask:0xf\n"
      "v_mfma_f32_16x16x32_bf16 v[96:99], v[104:107], v[216:219], 0\n"
      "v_mul_f32_dpp v28, v102, v28 row_newbcast:0 row_mask:0xf bank_mask:0xf\n"
      "v_mul_f32_dpp v26, v102, v26 row_newbcast:8 row_mask:0xf bank_mask:0xf\n"
      "v_mfma_f32_16x16x32_bf16 v[96:99], v[108:111], v[220:223], v[96:99]\n"
      "v_mul_f32_dpp v32, v102, v32 row_newbcast:0 row_mask:0xf bank_mask:0xf\n"
      "v_mul_f32_dpp v22, v102, v22 row_newbcast:8 row_mask:0xf bank_mask:0xf\n"
      "v_mfma_f32_16x16x32_bf16 v[24:27], v[92:95], a[208:211], v[24:27]\n"
      "v_exp_f32 v126, v126\n"
      "v_mfma_f32_16x16x32_bf16 v[24:27], v[76:79], a[212:215], v[24:27]\n"
      "v_mul_f32_dpp v35, v102, v35 row_newbcast:12 row_mask:0xf bank_mask:0xf\n"
      "v_mul_f32_dpp v31, v102, v31 row_newbcast:12 row_mask:0xf bank_mask:0xf\n"
      "v_mfma_f32_16x16x32_bf16 v[24:27], v[84:87], a[216:219], v[24:27]\n"
      "v_mul_f32_dpp v29, v102, v29 row_newbcast:4 row_mask:0xf bank_mask:0xf\n"
      "v_mfma_f32_16x16x32_bf16 v[24:27], v[80:83], a[220:223], v[24:27]\n"
      "v_add_f32_dpp v200, v200, v200 row_ror:2 row_mask:0xf bank_mask:0xf\n"
      "v_mul_f32_dpp v33, v102, v33 row_newbcast:4 row_mask:0xf bank_mask:0xf\n"
      "v_mfma_f32_16x16x32_bf16 v[28:31], v[92:95], a[224:227], v[28:31]\n"
      "v_exp_f32 v179, v179\n"
      "v_mfma_f32_16x16x32_bf16 v[28:31], v[76:79], a[228:231], v[28:31]\n"
      "v_mul_f32_dpp v20, v102, v20 row_newbcast:0 row_mask:0xf bank_mask:0xf\n"
      "v_mul_f32_dpp v34, v102, v34 row_newbcast:8 row_mask:0xf bank_mask:0xf\n"
      "v_mfma_f32_16x16x32_bf16 v[28:31], v[84:87], a[232:235], v[28:31]\n"
      "v_exp_f32 v125, v125\n"
      "v_mfma_f32_16x16x32_bf16 v[28:31], v[80:83], a[236:239], v[28:31]\n"
      "v_add_f32_dpp v200, v200, v200 row_ror:1 row_mask:0xf bank_mask:0xf\n"
      "v_mfma_f32_16x16x32_bf16 v[20:23], v[92:95], a[192:195], v[20:23]\n"
      "v_rcp_f32 v201, v200\n"
      "v_mfma_f32_16x16x32_bf16 v[20:23], v[76:79], a[196:199], v[20:23]\n"
      "v_exp_f32 v124, v124\n"
      "v_mfma_f32_16x16x32_bf16 v[20:23], v[84:87], a[200:203], v[20:23]\n"
      "v_exp_f32 v127, v127\n"
      "v_mfma_f32_16x16x32_bf16 v[20:23], v[80:83], a[204:207], v[20:23]\n"
      "v_exp_f32 v97, v97\n"
      "v_mfma_f32_16x16x32_bf16 v[32:35], v[92:95], a[240:243], v[32:35]\n"
      "v_exp_f32 v178, v178\n"
      "v_mfma_f32_16x16x32_bf16 v[32:35], v[76:79], a[244:247], v[32:35]\n"
      "v_maximum3_f32 v8, v8, v200, v201\n"
      "v_mfma_f32_16x16x32_bf16 v[32:35], v[84:87], a[248:251], v[32:35]\n"
      "v_exp_f32 v99, v99\n"
      "v_mfma_f32_16x16x32_bf16 v[32:35], v[80:83], a[252:255], v[32:35]\n"
      "v_exp_f32 v176, v176\n"
      "v_mfma_f32_16x16x4_4b_bf16 v[44:59], v[202:203], v[156:157], 0 cbsz:2 abid:2\n"
      "ds_bpermute_b32 v154, v4, v153 offset:128\n"
      "v_cvt_pk_bf16_f32 v95, v178, v179\n"
      "v_maximum3_f32 v8, v8, v100, v101\n"
      "v_exp_f32 v177, v177\n"
      "v_cvt_pk_bf16_f32 v22, v22, v26\n"
      "v_mfma_f32_16x16x32_bf16 v[100:103], v[184:187], v[216:219], 0\n"
      "v_exp_f32 v98, v98\n"
      "v_mfma_f32_16x16x32_bf16 v[100:103], v[188:191], v[220:223], v[100:103]\n"
      "v_cvt_pk_bf16_f32 v20, v20, v24\n"
      "v_cvt_pk_bf16_f32 v24, v21, v25\n"
      "v_mfma_f32_16x16x32_bf16 v[48:51], v[116:119], v[36:39], v[48:51]\n"
      "v_cvt_pk_bf16_f32 v21, v28, v32\n"
      "v_cvt_pk_bf16_f32 v93, v126, v127\n"
      "v_mfma_f32_16x16x32_bf16 v[48:51], v[120:123], v[40:43], v[48:51]\n"
      "v_cvt_pk_bf16_f32 v26, v23, v27\n"
      "v_cvt_pk_bf16_f32 v27, v31, v35\n"
      "v_mfma_f32_16x16x32_bf16 v[48:51], v[128:131], v[244:247], v[48:51]\n"
      "v_pk_max_i16 v27, v27, 0\n"
      "v_cvt_pk_bf16_f32 v25, v29, v33\n"
      "v_mfma_f32_16x16x32_bf16 v[56:59], v[116:119], v[224:227], v[56:59]\n"
      "v_maximum3_f32 v8, v8, v152, v153\n"
      "v_mfma_f32_16x16x32_bf16 v[56:59], v[120:123], v[228:231], v[56:59]\n"
      "v_exp_f32 v102, v102\n"
      "v_mfma_f32_16x16x32_bf16 v[56:59], v[128:131], v[196:199], v[56:59]\n"
      "v_exp_f32 v100, v100\n"
      "v_mfma_f32_16x16x32_bf16 v[44:47], v[116:119], v[140:143], v[44:47]\n"
      "v_cvt_pk_bf16_f32 v94, v176, v177\n"
      "v_mfma_f32_16x16x32_bf16 v[44:47], v[120:123], v[144:147], v[44:47]\n"
      "s_waitcnt lgkmcnt(0)\n"
      "v_mul_f32_dpp v48, v154, v48 row_newbcast:0 row_mask:0xf bank_mask:0xf\n"
      "v_cvt_pk_bf16_f32 v92, v124, v125\n"
      "v_mfma_f32_16x16x32_bf16 v[44:47], v[128:131], v[88:91], v[44:47]\n"
      "ds_read_b128 v[80:83], v0 offset:8704\n"
      "v_mul_f32_dpp v59, v154, v59 row_newbcast:12 row_mask:0xf bank_mask:0xf\n"
      "v_mfma_f32_16x16x32_bf16 v[52:55], v[116:119], v[132:135], v[52:55]\n"
      "v_mfma_f32_16x16x32_bf16 v[52:55], v[120:123], v[136:139], v[52:55]\n"
      "v_cvt_pk_bf16_f32 v23, v30, v34\n"
      "v_pk_max_i16 v23, v23, 0\n"
      "v_mfma_f32_16x16x32_bf16 v[52:55], v[128:131], v[60:63], v[52:55]\n"
      "ds_read_b64 v[202:203], v11 offset:4096\n"
      "v_mul_f32_dpp v46, v154, v46 row_newbcast:8 row_mask:0xf bank_mask:0xf\n"
      "v_mfma_f32_16x16x32_bf16 v[28:31], v[168:171], v[216:219], 0\n"
      "v_mul_f32_dpp v49, v154, v49 row_newbcast:4 row_mask:0xf bank_mask:0xf\n"
      "v_mfma_f32_16x16x32_bf16 v[28:31], v[172:175], v[220:223], v[28:31]\n"
      "v_mul_f32_dpp v50, v154, v50 row_newbcast:8 row_mask:0xf bank_mask:0xf\n"
      "v_mul_f32_dpp v57, v154, v57 row_newbcast:4 row_mask:0xf bank_mask:0xf\n"
      "v_mfma_f32_16x16x4_4b_bf16 v[116:131], v[158:159], v[156:157], 0 cbsz:2 abid:0\n"
      "v_mul_f32_dpp v53, v154, v53 row_newbcast:4 row_mask:0xf bank_mask:0xf\n"
      "v_pk_max_i16 v22, v22, 0\n"
      "v_exp_f32 v96, v96\n"
      "v_mul_f32_dpp v45, v154, v45 row_newbcast:4 row_mask:0xf bank_mask:0xf\n"
      "v_mul_f32_dpp v55, v154, v55 row_newbcast:12 row_mask:0xf bank_mask:0xf\n"
      "v_mul_f32_dpp v44, v154, v44 row_newbcast:0 row_mask:0xf bank_mask:0xf\n"
      "v_pk_max_i16 v21, v21, 0\n"
      "v_mfma_f32_16x16x32_bf16 v[32:35], v[208:211], v[216:219], 0\n"
      "v_mul_f32_dpp v51, v154, v51 row_newbcast:12 row_mask:0xf bank_mask:0xf\n"
      "v_mul_f32_dpp v47, v154, v47 row_newbcast:12 row_mask:0xf bank_mask:0xf\n"
      "v_mfma_f32_16x16x32_bf16 v[32:35], v[212:215], v[220:223], v[32:35]\n"
      "v_mul_f32_dpp v54, v154, v54 row_newbcast:8 row_mask:0xf bank_mask:0xf\n"
      "v_mul_f32_dpp v58, v154, v58 row_newbcast:8 row_mask:0xf bank_mask:0xf\n"
      "v_smfmac_f32_16x16x64_bf16 v[116:119], v[160:163], v[140:147], v5\n"
      "v_mul_f32_dpp v52, v154, v52 row_newbcast:0 row_mask:0xf bank_mask:0xf\n"
      "v_mfma_f32_16x16x32_bf16 v[44:47], v[72:75], a[192:195], v[44:47]\n"
      "v_pk_max_i16 v24, v24, 0\n"
      "v_pk_max_i16 v25, v25, 0\n"
      "v_mfma_f32_16x16x32_bf16 v[44:47], v[148:151], a[196:199], v[44:47]\n"
      "v_exp_f32 v103, v103\n"
      "v_mfma_f32_16x16x32_bf16 v[44:47], v[164:167], a[200:203], v[44:47]\n"
      "v_mul_f32_dpp v56, v154, v56 row_newbcast:0 row_mask:0xf bank_mask:0xf\n"
      "v_pk_max_i16 v26, v26, 0\n"
      "v_mfma_f32_16x16x32_bf16 v[44:47], v[204:207], a[204:207], v[44:47]\n"
      "v_exp_f32 v29, v29\n"
      "v_mfma_f32_16x16x32_bf16 v[48:51], v[72:75], a[208:211], v[48:51]\n"
      "v_mov_b64 v[178:179], 0\n"
      "v_mfma_f32_16x16x32_bf16 v[48:51], v[148:151], a[212:215], v[48:51]\n"
      "v_exp_f32 v34, v34\n"
      "v_mfma_f32_16x16x32_bf16 v[48:51], v[164:167], a[216:219], v[48:51]\n"
      "v_mov_b64 v[176:177], 0\n"
      "v_mfma_f32_16x16x32_bf16 v[48:51], v[204:207], a[220:223], v[48:51]\n"
      "v_exp_f32 v28, v28\n"
      "v_smfmac_f32_16x16x64_bf16 v[176:179], v[112:115], v[216:223], v5\n"
      "buffer_store_dwordx2 v[26:27], v2, s[44:47], s73 offen offset:768 nt sc1\n"
      "ds_read_b128 v[84:87], v0 offset:9216\n"
      "v_smfmac_f32_16x16x64_bf16 v[120:123], v[160:163], v[36:43], v5\n"
      "buffer_store_dwordx2 v[24:25], v2, s[44:47], s73 offen offset:256 nt sc1\n"
      "ds_read_b128 v[76:79], v0 offset:9728\n"
      "v_smfmac_f32_16x16x64_bf16 v[128:131], v[160:163], v[224:231], v5\n"
      "ds_read_b128 v[152:155], v0 offset:8192\n"
      "v_smfmac_f32_16x16x64_bf16 v[124:127], v[160:163], v[132:139], v5\n"
      "v_cvt_pk_bf16_f32 v44, v44, v48\n"
      "v_mfma_f32_16x16x32_bf16 v[52:55], v[72:75], a[224:227], v[52:55]\n"
      "v_cvt_pk_bf16_f32 v48, v45, v49\n"
      "v_cvt_pk_bf16_f32 v219, v102, v103\n"
      "v_mfma_f32_16x16x32_bf16 v[52:55], v[148:151], a[228:231], v[52:55]\n"
      "v_exp_f32 v33, v33\n"
      "v_mfma_f32_16x16x32_bf16 v[52:55], v[164:167], a[232:235], v[52:55]\n"
      "v_cvt_pk_bf16_f32 v46, v46, v50\n"
      "v_cvt_pk_bf16_f32 v50, v47, v51\n"
      "v_mfma_f32_16x16x32_bf16 v[52:55], v[204:207], a[236:239], v[52:55]\n"
      "v_pk_max_i16 v20, v20, 0\n"
      "v_mfma_f32_16x16x32_bf16 v[56:59], v[72:75], a[240:243], v[56:59]\n"
      "v_exp_f32 v101, v101\n"
      "v_mfma_f32_16x16x32_bf16 v[56:59], v[148:151], a[244:247], v[56:59]\n"
      "v_exp_f32 v35, v35\n"
      "v_mfma_f32_16x16x32_bf16 v[56:59], v[164:167], a[248:251], v[56:59]\n"
      "v_exp_f32 v32, v32\n"
      "v_mfma_f32_16x16x32_bf16 v[56:59], v[204:207], a[252:255], v[56:59]\n"
      "v_pk_max_i16 v46, v46, 0\n"
      "v_pk_max_i16 v48, v48, 0\n"
      "v_smfmac_f32_16x16x32_bf16 v[128:131], v[18:19], v[196:199], v5\n"
      "buffer_store_dwordx2 v[22:23], v2, s[44:47], s73 offen offset:512 nt sc1\n"
      "s_waitcnt lgkmcnt(0)\n"
      "v_mfma_f32_16x16x32_bf16 v[164:167], a[144:147], v[152:155], 0\n"
      "v_mfma_f32_16x16x32_bf16 v[164:167], a[148:151], v[80:83], v[164:167]\n"
      "v_cvt_pk_bf16_f32 v218, v100, v101\n"
      "v_cvt_pk_bf16_f32 v47, v54, v58\n"
      "v_mfma_f32_16x16x32_bf16 v[164:167], a[152:155], v[84:87], v[164:167]\n"
      "v_cvt_pk_bf16_f32 v49, v53, v57\n"
      "v_pk_max_i16 v49, v49, 0\n"
      "v_mfma_f32_16x16x32_bf16 v[164:167], a[156:159], v[76:79], v[164:167]\n"
      "v_pk_max_i16 v44, v44, 0\n"
      "v_cvt_pk_bf16_f32 v51, v55, v59\n"
      "v_mfma_f32_16x16x32_bf16 v[204:207], a[128:131], v[152:155], 0\n"
      "v_exp_f32 v31, v31\n"
      "v_mfma_f32_16x16x32_bf16 v[204:207], a[132:135], v[80:83], v[204:207]\n"
      "v_pk_max_i16 v51, v51, 0\n"
      "v_and_b32 v75, 0xffff0000, v203\n"
      "v_mfma_f32_16x16x32_bf16 v[204:207], a[136:139], v[84:87], v[204:207]\n"
      "v_cvt_pk_bf16_f32 v222, v28, v29\n"
      "v_pk_max_i16 v50, v50, 0\n"
      "v_mfma_f32_16x16x32_bf16 v[204:207], a[140:143], v[76:79], v[204:207]\n"
      "buffer_store_dwordx2 v[50:51], v2, s[44:47], s75 offen offset:768 nt sc1\n"
      "v_cvt_pk_bf16_f32 v221, v98, v99\n"
      "v_mfma_f32_16x16x32_bf16 v[148:151], a[160:163], v[152:155], 0\n"
      "v_lshlrev_b32 v74, 16, v203\n"
      "v_and_b32 v73, 0xffff0000, v202\n"
      "v_mfma_f32_16x16x32_bf16 v[148:151], a[164:167], v[80:83], v[148:151]\n"
      "v_exp_f32 v30, v30\n"
      "v_mfma_f32_16x16x32_bf16 v[148:151], a[168:171], v[84:87], v[148:151]\n"
      "v_cvt_pk_bf16_f32 v220, v96, v97\n"
      "v_cvt_pk_bf16_f32 v45, v52, v56\n"
      "v_mfma_f32_16x16x32_bf16 v[148:151], a[172:175], v[76:79], v[148:151]\n"
      "v_lshlrev_b32 v72, 16, v202\n"
      "v_cvt_pk_bf16_f32 v217, v34, v35\n"
      "v_mfma_f32_16x16x32_bf16 v[96:99], a[176:179], v[152:155], 0\n"
      "v_add_f32 v128, v128, v129\n"
      "v_cvt_pk_bf16_f32 v223, v30, v31\n"
      "v_mfma_f32_16x16x32_bf16 v[96:99], a[180:183], v[80:83], v[96:99]\n"
      "v_cvt_pk_bf16_f32 v24, v204, v205\n"
      "v_cvt_pk_bf16_f32 v26, v164, v165\n"
      "v_mfma_f32_16x16x32_bf16 v[96:99], a[184:187], v[84:87], v[96:99]\n"
      "v_cvt_pk_bf16_f32 v25, v206, v207\n"
      "v_cvt_pk_bf16_f32 v27, v166, v167\n"
      "v_mfma_f32_16x16x32_bf16 v[96:99], a[188:191], v[76:79], v[96:99]\n"
      "v_cvt_pk_bf16_f32 v28, v148, v149\n"
      "v_fma_f32 v128, v128, v201, v75\n"
      "v_smfmac_f32_16x16x32_bf16 v[116:119], v[18:19], v[88:91], v5\n"
      "ds_read_b128 v[164:167], v0 offset:4608\n"
      "v_cvt_pk_bf16_f32 v216, v32, v33\n"
      "v_smfmac_f32_16x16x32_bf16 v[124:127], v[18:19], v[60:63], v5\n"
      "ds_read_b128 v[204:207], v0 offset:5120\n"
      "v_pk_max_i16 v47, v47, 0\n"
      "v_smfmac_f32_16x16x32_bf16 v[120:123], v[18:19], v[244:247], v5\n"
      "v_cvt_pk_bf16_f32 v29, v150, v151\n"
      "v_cvt_pk_bf16_f32 v31, v98, v99\n"
      "v_cvt_pk_bf16_f32 v30, v96, v97\n"
      "v_mfma_f32_16x16x32_bf16 v[52:55], v[64:67], v[24:27], 0\n"
      "s_nop 0\n"
      "v_add_f32 v124, v124, v125\n"
      "v_add_f32 v116, v116, v117\n"
      "v_mfma_f32_16x16x32_bf16 v[52:55], v[68:71], v[28:31], v[52:55]\n"
      "v_add_f32 v120, v120, v121\n"
      "v_fma_f32 v124, v124, v201, v74\n"
      "v_smfmac_f32_16x16x64_bf16 v[176:179], v[192:195], v[24:31], v5\n"
      "buffer_store_dwordx2 v[20:21], v2, s[44:47], s73 offen offset:0 nt sc1\n"
      "v_fma_f32 v120, v120, v201, v73\n"
      "v_mfma_f32_16x16x32_bf16 v[96:99], v[236:239], v[24:27], 0\n"
      "ds_read_b128 v[148:151], v0 offset:5632\n"
      "ds_read_b128 v[158:161], v0 offset:4096\n"
      "v_mfma_f32_16x16x32_bf16 v[96:99], v[240:243], v[28:31], v[96:99]\n"
      "buffer_store_dwordx2 v[48:49], v2, s[44:47], s75 offen offset:256 nt sc1\n"
      "v_fma_f32 v116, v116, v201, v72\n"
      "v_mfma_f32_16x16x32_bf16 v[56:59], v[208:211], v[24:27], 0\n"
      "v_add_f32 v178, v178, v179\n"
      "v_cvt_pk_bf16_f32 v117, v124, v128\n"
      "v_mfma_f32_16x16x32_bf16 v[56:59], v[212:215], v[28:31], v[56:59]\n"
      "v_exp_f32 v178, v178\n"
      "v_mfma_f32_16x16x32_bf16 v[124:127], v[168:171], v[24:27], 0\n"
      "v_cvt_pk_bf16_f32 v116, v116, v120\n"
      "v_add_f32 v176, v176, v177\n"
      "v_mfma_f32_16x16x32_bf16 v[124:127], v[172:175], v[28:31], v[124:127]\n"
      "v_exp_f32 v176, v176\n"
      "v_mfma_f32_16x16x32_bf16 v[48:51], v[184:187], v[24:27], 0\n"
      "v_cvt_pk_bf16_f32 v19, v176, v178\n"
      "v_cvt_pk_bf16_f32 v18, v176, v178\n"
      "v_mfma_f32_16x16x32_bf16 v[48:51], v[188:191], v[28:31], v[48:51]\n"
      "v_add_f32 v101, v176, v178\n"
      "v_pk_max_i16 v116, v116, 0\n"
      "v_mfma_f32_16x16x32_bf16 v[118:121], v[104:107], v[24:27], 0\n"
      "v_permlane16_swap_b32 v18, v19\n"
      "v_mfma_f32_16x16x32_bf16 v[118:121], v[108:111], v[28:31], v[118:121]\n"
      "v_exp_f32 v59, v59\n"
      "v_mfma_f32_16x16x4_4b_bf16 v[20:35], v[18:19], v[156:157], 0 cbsz:2 abid:0\n"
      "v_add_f32 v100, v176, v178\n"
      "v_pk_max_i16 v117, v117, 0\n"
      "v_exp_f32 v54, v54\n"
      "v_permlane16_swap_b32 v100, v101\n"
      "v_mov_b64 v[200:201], v[100:101]\n"
      "s_nop 0\n"
      "v_smfmac_f32_16x16x64_bf16 v[100:103], v[12:15], v[216:223], v5\n"
      "s_nop 2\n"
      "v_mfma_f32_16x16x32_bf16 v[28:31], v[216:219], v[132:135], v[28:31]\n"
      "v_exp_f32 v121, v121\n"
      "v_mfma_f32_16x16x32_bf16 v[28:31], v[220:223], v[136:139], v[28:31]\n"
      "v_exp_f32 v49, v49\n"
      "v_mfma_f32_16x16x32_bf16 v[28:31], v[92:95], v[60:63], v[28:31]\n"
      "v_exp_f32 v97, v97\n"
      "v_smfmac_f32_16x16x32_bf16 v[100:103], v[12:13], v[92:95], v5\n"
      "v_pk_max_i16 v45, v45, 0\n"
      "v_exp_f32 v118, v118\n"
      "v_mfma_f32_16x16x32_bf16 v[20:23], v[216:219], v[140:143], v[20:23]\n"
      "v_exp_f32 v120, v120\n"
      "v_mfma_f32_16x16x32_bf16 v[20:23], v[220:223], v[144:147], v[20:23]\n"
      "v_exp_f32 v50, v50\n"
      "v_mfma_f32_16x16x32_bf16 v[20:23], v[92:95], v[88:91], v[20:23]\n"
      "buffer_store_dwordx2 v[116:117], v3, s[44:47], s79 offen offset:0 nt sc1\n"
      "v_add_f32 v100, v100, v101\n"
      "v_mfma_f32_16x16x32_bf16 v[32:35], v[216:219], v[224:227], v[32:35]\n"
      "v_rcp_f32 v101, v100\n"
      "v_mfma_f32_16x16x32_bf16 v[32:35], v[220:223], v[228:231], v[32:35]\n"
      "ds_bpermute_b32 v102, v4, v101\n"
      "v_mfma_f32_16x16x32_bf16 v[32:35], v[92:95], v[196:199], v[32:35]\n"
      "s_waitcnt lgkmcnt(1)\n"
      "v_mfma_f32_16x16x32_bf16 v[128:131], a[160:163], v[158:161], 0\n"
      "v_exp_f32 v127, v127\n"
      "v_mfma_f32_16x16x32_bf16 v[128:131], a[164:167], v[164:167], v[128:131]\n"
      "v_exp_f32 v119, v119\n"
      "v_mfma_f32_16x16x32_bf16 v[128:131], a[168:171], v[204:207], v[128:131]\n"
      "v_exp_f32 v57, v57\n"
      "v_mfma_f32_16x16x32_bf16 v[128:131], a[172:175], v[148:151], v[128:131]\n"
      "v_exp_f32 v58, v58\n"
      "v_mfma_f32_16x16x32_bf16 v[24:27], v[216:219], v[36:39], v[24:27]\n"
      "v_exp_f32 v55, v55\n"
      "v_mfma_f32_16x16x32_bf16 v[24:27], v[220:223], v[40:43], v[24:27]\n"
      "v_exp_f32 v99, v99\n"
      "v_mfma_f32_16x16x32_bf16 v[24:27], v[92:95], v[244:247], v[24:27]\n"
      "v_exp_f32 v124, v124\n"
      "v_mfma_f32_16x16x32_bf16 v[220:223], a[176:179], v[158:161], 0\n"
      "v_exp_f32 v56, v56\n"
      "v_mfma_f32_16x16x32_bf16 v[220:223], a[180:183], v[164:167], v[220:223]\n"
      "s_waitcnt lgkmcnt(0)\n"
      "v_mul_f32_dpp v33, v102, v33 row_newbcast:4 row_mask:0xf bank_mask:0xf\n"
      "v_mul_f32_dpp v35, v102, v35 row_newbcast:12 row_mask:0xf bank_mask:0xf\n"
      "v_mfma_f32_16x16x32_bf16 v[220:223], a[184:187], v[204:207], v[220:223]\n"
      "v_mul_f32_dpp v34, v102, v34 row_newbcast:8 row_mask:0xf bank_mask:0xf\n"
      "v_mul_f32_dpp v32, v102, v32 row_newbcast:0 row_mask:0xf bank_mask:0xf\n"
      "v_mfma_f32_16x16x32_bf16 v[220:223], a[188:191], v[148:151], v[220:223]\n"
      "v_mul_f32_dpp v24, v102, v24 row_newbcast:0 row_mask:0xf bank_mask:0xf\n"
      "v_mul_f32_dpp v26, v102, v26 row_newbcast:8 row_mask:0xf bank_mask:0xf\n"
      "v_mfma_f32_16x16x32_bf16 v[92:95], a[128:131], v[158:161], 0\n"
      "v_mul_f32_dpp v30, v102, v30 row_newbcast:8 row_mask:0xf bank_mask:0xf\n"
      "v_mul_f32_dpp v29, v102, v29 row_newbcast:4 row_mask:0xf bank_mask:0xf\n"
      "v_mfma_f32_16x16x32_bf16 v[92:95], a[132:135], v[164:167], v[92:95]\n"
      "v_mul_f32_dpp v28, v102, v28 row_newbcast:0 row_mask:0xf bank_mask:0xf\n"
      "v_mul_f32_dpp v31, v102, v31 row_newbcast:12 row_mask:0xf bank_mask:0xf\n"
      "v_mfma_f32_16x16x32_bf16 v[92:95], a[136:139], v[204:207], v[92:95]\n"
      "v_exp_f32 v51, v51\n"
      "v_mfma_f32_16x16x32_bf16 v[92:95], a[140:143], v[148:151], v[92:95]\n"
      "buffer_store_dwordx2 v[46:47], v2, s[44:47], s75 offen offset:512 nt sc1\n"
      "v_cvt_pk_bf16_f32 v219, v50, v51\n"
      "v_mfma_f32_16x16x32_bf16 v[28:31], v[232:235], a[224:227], v[28:31]\n"
      "v_mul_f32_dpp v25, v102, v25 row_newbcast:4 row_mask:0xf bank_mask:0xf\n"
      "v_mul_f32_dpp v27, v102, v27 row_newbcast:12 row_mask:0xf bank_mask:0xf\n"
      "v_mfma_f32_16x16x32_bf16 v[28:31], v[252:255], a[228:231], v[28:31]\n"
      "v_exp_f32 v48, v48\n"
      "v_mfma_f32_16x16x32_bf16 v[28:31], v[248:251], a[232:235], v[28:31]\n"
      "v_mul_f32_dpp v20, v102, v20 row_newbcast:0 row_mask:0xf bank_mask:0xf\n"
      "v_cvt_pk_bf16_f32 v217, v58, v59\n"
      "v_mfma_f32_16x16x32_bf16 v[28:31], v[180:183], a[236:239], v[28:31]\n"
      "buffer_store_dwordx2 v[44:45], v2, s[44:47], s75 offen offset:0 nt sc1\n"
      "v_cvt_pk_bf16_f32 v218, v48, v49\n"
      "v_mfma_f32_16x16x32_bf16 v[32:35], v[232:235], a[240:243], v[32:35]\n"
      "v_cvt_pk_bf16_f32 v216, v56, v57\n"
      "v_mul_f32_dpp v23, v102, v23 row_newbcast:12 row_mask:0xf bank_mask:0xf\n"
      "v_mfma_f32_16x16x32_bf16 v[32:35], v[252:255], a[244:247], v[32:35]\n"
      "v_cvt_pk_bf16_f32 v122, v220, v221\n"
      "v_mfma_f32_16x16x32_bf16 v[32:35], v[248:251], a[248:251], v[32:35]\n"
      "s_add_u32 s44, s44, 13107200\n s_addc_u32 s45, s45, 0\n"
      "v_mov_b64 v[178:179], 0\n"
      "v_mfma_f32_16x16x32_bf16 v[32:35], v[180:183], a[252:255], v[32:35]\n"
      "v_mul_f32_dpp v21, v102, v21 row_newbcast:4 row_mask:0xf bank_mask:0xf\n"
      "v_maximum3_f32 v8, v8, v100, v101\n"
      "v_mfma_f32_16x16x32_bf16 v[48:51], a[144:147], v[158:161], 0\n"
      "v_mul_f32_dpp v22, v102, v22 row_newbcast:8 row_mask:0xf bank_mask:0xf\n"
      "v_cvt_pk_bf16_f32 v123, v222, v223\n"
      "v_mfma_f32_16x16x32_bf16 v[48:51], a[148:151], v[164:167], v[48:51]\n"
      "v_exp_f32 v96, v96\n"
      "v_mfma_f32_16x16x32_bf16 v[48:51], a[152:155], v[204:207], v[48:51]\n"
      "L_h7_%=:\n"
      "s_cbranch_execz L_hr_%=\n"
      "v_exp_f32 v53, v53\n"
      "v_mfma_f32_16x16x32_bf16 v[48:51], a[156:159], v[148:151], v[48:51]\n"
      "v_exp_f32 v125, v125\n"
      "v_mfma_f32_16x16x32_bf16 v[24:27], v[232:235], a[208:211], v[24:27]\n"
      "v_exp_f32 v98, v98\n"
      "v_mfma_f32_16x16x32_bf16 v[24:27], v[252:255], a[212:215], v[24:27]\n"
      "v_exp_f32 v126, v126\n"
      "v_mfma_f32_16x16x32_bf16 v[24:27], v[248:251], a[216:219], v[24:27]\n"
      "v_exp_f32 v52, v52\n"
      "v_mfma_f32_16x16x32_bf16 v[24:27], v[180:183], a[220:223], v[24:27]\n"
      "v_cvt_pk_bf16_f32 v221, v120, v121\n"
      "v_cvt_pk_bf16_f32 v117, v94, v95\n"
      "v_mfma_f32_16x16x32_bf16 v[20:23], v[232:235], a[192:195], v[20:23]\n"
      "v_cvt_pk_bf16_f32 v75, v54, v55\n"
      "v_cvt_pk_bf16_f32 v223, v126, v127\n"
      "v_mfma_f32_16x16x32_bf16 v[20:23], v[252:255], a[196:199], v[20:23]\n"
      "v_cvt_pk_bf16_f32 v222, v124, v125\n"
      "v_cvt_pk_bf16_f32 v73, v98, v99\n"
      "v_mfma_f32_16x16x32_bf16 v[20:23], v[248:251], a[200:203], v[20:23]\n"
      "v_cvt_pk_bf16_f32 v220, v118, v119\n"
      "v_cvt_pk_bf16_f32 v120, v128, v129\n"
      "v_mfma_f32_16x16x32_bf16 v[20:23], v[180:183], a[204:207], v[20:23]\n"
      "v_cvt_pk_bf16_f32 v74, v52, v53\n"
      "v_cvt_pk_bf16_f32 v119, v50, v51\n"
      "v_smfmac_f32_16x16x64_bf16 v[200:203], v[12:15], v[216:223], v5\n"
      "ds_read_b128 v[180:183], v0 offset:1024\n"
      "v_cvt_pk_bf16_f32 v118, v48, v49\n"
      "v_mfma_f32_16x16x4_4b_bf16 v[44:59], v[18:19], v[156:157], 0 cbsz:2 abid:2\n"
      "ds_read_b128 v[232:235], v0 offset:0\n"
      "v_cvt_pk_bf16_f32 v116, v92, v93\n"
      "v_cvt_pk_bf16_f32 v72, v96, v97\n"
      "v_cvt_pk_bf16_f32 v20, v20, v24\n"
      "v_cvt_pk_bf16_f32 v22, v22, v26\n"
      "v_cvt_pk_bf16_f32 v26, v23, v27\n"
      "v_smfmac_f32_16x16x32_bf16 v[200:203], v[12:13], v[72:75], v5\n"
      "ds_read_b128 v[124:127], v0 offset:1536\n"
      "v_cvt_pk_bf16_f32 v121, v130, v131\n"
      "v_pk_max_i16 v22, v22, 0\n"
      "v_cvt_pk_bf16_f32 v24, v21, v25\n"
      "v_mfma_f32_16x16x32_bf16 v[96:99], v[104:107], v[116:119], 0\n"
      "v_cvt_pk_bf16_f32 v21, v28, v32\n"
      "v_pk_max_i16 v20, v20, 0\n"
      "v_mfma_f32_16x16x32_bf16 v[96:99], v[108:111], v[120:123], v[96:99]\n"
      "v_pk_max_i16 v26, v26, 0\n"
      "v_cvt_pk_bf16_f32 v27, v31, v35\n"
      "v_mfma_f32_16x16x32_bf16 v[100:103], v[236:239], v[116:119], 0\n"
      "v_cvt_pk_bf16_f32 v25, v29, v33\n"
      "v_mov_b64 v[176:177], 0\n"
      "v_mfma_f32_16x16x32_bf16 v[100:103], v[240:243], v[120:123], v[100:103]\n"
      "v_pk_max_i16 v27, v27, 0\n"
      "v_cvt_pk_bf16_f32 v23, v30, v34\n"
      "v_mfma_f32_16x16x32_bf16 v[248:251], v[184:187], v[116:119], 0\n"
      "ds_read_b128 v[128:131], v0 offset:512\n"
      "v_add_f32 v200, v200, v201\n"
      "v_mfma_f32_16x16x32_bf16 v[248:251], v[188:191], v[120:123], v[248:251]\n"
      "v_exp_f32 v97, v97\n"
      "v_mfma_f32_16x16x32_bf16 v[52:55], v[216:219], v[132:135], v[52:55]\n"
      "v_exp_f32 v100, v100\n"
      "v_mfma_f32_16x16x32_bf16 v[52:55], v[220:223], v[136:139], v[52:55]\n"
      "v_rcp_f32 v201, v200\n"
      "v_mfma_f32_16x16x32_bf16 v[52:55], v[72:75], v[60:63], v[52:55]\n"
      "v_exp_f32 v98, v98\n"
      "v_mfma_f32_16x16x32_bf16 v[48:51], v[216:219], v[36:39], v[48:51]\n"
      "ds_bpermute_b32 v202, v4, v201 offset:128\n"
      "v_mfma_f32_16x16x32_bf16 v[48:51], v[220:223], v[40:43], v[48:51]\n"
      "v_exp_f32 v101, v101\n"
      "v_mfma_f32_16x16x32_bf16 v[48:51], v[72:75], v[244:247], v[48:51]\n"
      "v_exp_f32 v102, v102\n"
      "v_mfma_f32_16x16x32_bf16 v[28:31], v[168:171], v[116:119], 0\n"
      "v_pk_max_i16 v23, v23, 0\n"
      "v_pk_max_i16 v25, v25, 0\n"
      "v_mfma_f32_16x16x32_bf16 v[28:31], v[172:175], v[120:123], v[28:31]\n"
      "v_exp_f32 v248, v248\n"
      "v_mfma_f32_16x16x32_bf16 v[56:59], v[216:219], v[224:227], v[56:59]\n"
      "v_exp_f32 v251, v251\n"
      "v_mfma_f32_16x16x32_bf16 v[56:59], v[220:223], v[228:231], v[56:59]\n"
      "v_exp_f32 v99, v99\n"
      "v_mfma_f32_16x16x32_bf16 v[56:59], v[72:75], v[196:199], v[56:59]\n"
      "v_pk_max_i16 v24, v24, 0\n"
      "v_pk_max_i16 v21, v21, 0\n"
      "v_mfma_f32_16x16x32_bf16 v[44:47], v[216:219], v[140:143], v[44:47]\n"
      "v_exp_f32 v249, v249\n"
      "v_mfma_f32_16x16x32_bf16 v[44:47], v[220:223], v[144:147], v[44:47]\n"
      "v_exp_f32 v31, v31\n"
      "v_mfma_f32_16x16x32_bf16 v[44:47], v[72:75], v[88:91], v[44:47]\n"
      "buffer_store_dwordx2 v[20:21], v2, s[44:47], s100 offen offset:0 nt sc1\n"
      "v_cvt_pk_bf16_f32 v218, v248, v249\n"
      "v_mfma_f32_16x16x32_bf16 v[32:35], v[208:211], v[116:119], 0\n"
      "s_waitcnt lgkmcnt(0)\n"
      "v_mul_f32_dpp v51, v202, v51 row_newbcast:12 row_mask:0xf bank_mask:0xf\n"
      "v_mul_f32_dpp v53, v202, v53 row_newbcast:4 row_mask:0xf bank_mask:0xf\n"
      "v_mfma_f32_16x16x32_bf16 v[32:35], v[212:215], v[120:123], v[32:35]\n"
      "v_exp_f32 v250, v250\n"
      "v_mfma_f32_16x16x32_bf16 v[18:21], a[176:179], v[232:235], 0\n"
      "v_mul_f32_dpp v48, v202, v48 row_newbcast:0 row_mask:0xf bank_mask:0xf\n"
      "v_cvt_pk_bf16_f32 v219, v250, v251\n"
      "v_mfma_f32_16x16x32_bf16 v[18:21], a[180:183], v[128:131], v[18:21]\n"
      "v_mul_f32_dpp v49, v202, v49 row_newbcast:4 row_mask:0xf bank_mask:0xf\n"
      "v_mul_f32_dpp v50, v202, v50 row_newbcast:8 row_mask:0xf bank_mask:0xf\n"
      "v_mfma_f32_16x16x32_bf16 v[18:21], a[184:187], v[180:183], v[18:21]\n"
      "v_exp_f32 v28, v28\n"
      "v_mfma_f32_16x16x32_bf16 v[18:21], a[188:191], v[124:127], v[18:21]\n"
      "v_exp_f32 v33, v33\n"
      "v_smfmac_f32_16x16x64_bf16 v[176:179], v[112:115], v[116:123], v5\n"
      "buffer_store_dwordx2 v[22:23], v2, s[44:47], s100 offen offset:512 nt sc1\n"
      "v_mul_f32_dpp v52, v202, v52 row_newbcast:0 row_mask:0xf bank_mask:0xf\n"
      "v_mfma_f32_16x16x32_bf16 v[112:115], a[144:147], v[232:235], 0\n"
      "v_exp_f32 v32, v32\n"
      "v_mfma_f32_16x16x32_bf16 v[112:115], a[148:151], v[128:131], v[112:115]\n"
      "v_exp_f32 v29, v29\n"
      "v_mfma_f32_16x16x32_bf16 v[112:115], a[152:155], v[180:183], v[112:115]\n"
      "v_cvt_pk_bf16_f32 v255, v20, v21\n"
      "v_mul_f32_dpp v57, v202, v57 row_newbcast:4 row_mask:0xf bank_mask:0xf\n"
      "v_mfma_f32_16x16x32_bf16 v[112:115], a[156:159], v[124:127], v[112:115]\n"
      "v_exp_f32 v96, v96\n"
      "v_mfma_f32_16x16x32_bf16 v[220:223], a[128:131], v[232:235], 0\n"
      "v_exp_f32 v103, v103\n"
      "v_mfma_f32_16x16x32_bf16 v[220:223], a[132:135], v[128:131], v[220:223]\n"
      "v_mul_f32_dpp v47, v202, v47 row_newbcast:12 row_mask:0xf bank_mask:0xf\n"
      "v_cvt_pk_bf16_f32 v254, v18, v19\n"
      "v_mfma_f32_16x16x32_bf16 v[220:223], a[136:139], v[180:183], v[220:223]\n"
      "v_exp_f32 v35, v35\n"
      "v_mfma_f32_16x16x32_bf16 v[220:223], a[140:143], v[124:127], v[220:223]\n"
      "v_mul_f32_dpp v54, v202, v54 row_newbcast:8 row_mask:0xf bank_mask:0xf\n"
      "v_mul_f32_dpp v46, v202, v46 row_newbcast:8 row_mask:0xf bank_mask:0xf\n"
      "v_mfma_f32_16x16x32_bf16 v[20:23], v[64:67], v[116:119], 0\n"
      "v_mul_f32_dpp v59, v202, v59 row_newbcast:12 row_mask:0xf bank_mask:0xf\n"
      "v_mul_f32_dpp v44, v202, v44 row_newbcast:0 row_mask:0xf bank_mask:0xf\n"
      "v_mfma_f32_16x16x32_bf16 v[20:23], v[68:71], v[120:123], v[20:23]\n"
      "v_cvt_pk_bf16_f32 v251, v114, v115\n"
      "v_cvt_pk_bf16_f32 v250, v112, v113\n"
      "v_mfma_f32_16x16x32_bf16 v[120:123], a[160:163], v[232:235], 0\n"
      "v_mul_f32_dpp v45, v202, v45 row_newbcast:4 row_mask:0xf bank_mask:0xf\n"
      "v_cvt_pk_bf16_f32 v117, v102, v103\n"
      "v_mfma_f32_16x16x32_bf16 v[120:123], a[164:167], v[128:131], v[120:123]\n"
      "v_cvt_pk_bf16_f32 v249, v222, v223\n"
      "v_cvt_pk_bf16_f32 v116, v100, v101\n"
      "v_mfma_f32_16x16x32_bf16 v[120:123], a[168:171], v[180:183], v[120:123]\n"
      "v_exp_f32 v30, v30\n"
      "v_mfma_f32_16x16x32_bf16 v[120:123], a[172:175], v[124:127], v[120:123]\n"
      "v_exp_f32 v22, v22\n"
      "v_mfma_f32_16x16x32_bf16 v[48:51], v[152:155], a[208:211], v[48:51]\n"
      "v_mul_f32_dpp v55, v202, v55 row_newbcast:12 row_mask:0xf bank_mask:0xf\n"
      "v_cvt_pk_bf16_f32 v248, v220, v221\n"
      "v_mfma_f32_16x16x32_bf16 v[48:51], v[80:83], a[212:215], v[48:51]\n"
      "v_exp_f32 v34, v34\n"
      "v_mfma_f32_16x16x32_bf16 v[48:51], v[84:87], a[216:219], v[48:51]\n"
      "s_nop 0\n"
      "v_cvt_pk_bf16_f32 v252, v120, v121\n"
      "v_cvt_pk_bf16_f32 v253, v122, v123\n"
      "v_mfma_f32_16x16x32_bf16 v[48:51], v[76:79], a[220:223], v[48:51]\n"
      "v_cvt_pk_bf16_f32 v220, v96, v97\n"
      "v_mul_f32_dpp v56, v202, v56 row_newbcast:0 row_mask:0xf bank_mask:0xf\n"
      "v_mfma_f32_16x16x32_bf16 v[120:123], v[184:187], v[248:251], 0\n"
      "v_mul_f32_dpp v58, v202, v58 row_newbcast:8 row_mask:0xf bank_mask:0xf\n"
      "v_cvt_pk_bf16_f32 v221, v98, v99\n"
      "v_mfma_f32_16x16x32_bf16 v[120:123], v[188:191], v[252:255], v[120:123]\n"
      "v_cvt_pk_bf16_f32 v223, v30, v31\n"
      "v_cvt_pk_bf16_f32 v222, v28, v29\n"
      "v_smfmac_f32_16x16x64_bf16 v[176:179], v[192:195], v[248:255], v5\n"
      "buffer_store_dwordx2 v[24:25], v2, s[44:47], s100 offen offset:256 nt sc1\n"
      "v_cvt_pk_bf16_f32 v216, v32, v33\n"
      "v_mfma_f32_16x16x32_bf16 v[192:195], v[236:239], v[248:251], 0\n"
      "v_exp_f32 v20, v20\n"
      "v_mfma_f32_16x16x32_bf16 v[192:195], v[240:243], v[252:255], v[192:195]\n"
      "v_exp_f32 v23, v23\n"
      "v_mfma_f32_16x16x32_bf16 v[52:55], v[152:155], a[224:227], v[52:55]\n"
      "s_nop 0\n"
      "v_add_f32 v176, v176, v177\n"
      "v_add_f32 v178, v178, v179\n"
      "v_mfma_f32_16x16x32_bf16 v[52:55], v[80:83], a[228:231], v[52:55]\n"
      "v_exp_f32 v178, v178\n"
      "v_mfma_f32_16x16x32_bf16 v[52:55], v[84:87], a[232:235], v[52:55]\n"
      "v_exp_f32 v123, v123\n"
      "v_mfma_f32_16x16x32_bf16 v[52:55], v[76:79], a[236:239], v[52:55]\n"
      "v_exp_f32 v176, v176\n"
      "v_mfma_f32_16x16x32_bf16 v[236:239], v[64:67], v[248:251], 0\n"
      "v_cvt_pk_bf16_f32 v189, v176, v178\n"
      "v_add_f32 v240, v176, v178\n"
      "v_mfma_f32_16x16x32_bf16 v[236:239], v[68:71], v[252:255], v[236:239]\n"
      "v_cvt_pk_bf16_f32 v119, v22, v23\n"
      "v_cvt_pk_bf16_f32 v188, v176, v178\n"
      "v_mfma_f32_16x16x32_bf16 v[44:47], v[152:155], a[192:195], v[44:47]\n"
      "v_exp_f32 v121, v121\n"
      "v_mfma_f32_16x16x32_bf16 v[44:47], v[80:83], a[196:199], v[44:47]\n"
      "v_permlane16_swap_b32 v188, v189\n"
      "v_mfma_f32_16x16x32_bf16 v[44:47], v[84:87], a[200:203], v[44:47]\n"
      "v_cvt_pk_bf16_f32 v217, v34, v35\n"
      "v_add_f32 v241, v176, v178\n"
      "v_mfma_f32_16x16x32_bf16 v[44:47], v[76:79], a[204:207], v[44:47]\n"
      "v_exp_f32 v236, v236\n"
      "v_mfma_f32_16x16x32_bf16 v[92:95], v[168:171], v[248:251], 0\n"
      "v_permlane16_swap_b32 v240, v241\n"
      "v_mfma_f32_16x16x32_bf16 v[92:95], v[172:175], v[252:255], v[92:95]\n"
      "v_exp_f32 v21, v21\n"
      "v_mfma_f32_16x16x32_bf16 v[56:59], v[152:155], a[240:243], v[56:59]\n"
      "v_exp_f32 v120, v120\n"
      "v_mfma_f32_16x16x32_bf16 v[56:59], v[80:83], a[244:247], v[56:59]\n"
      "v_exp_f32 v194, v194\n"
      "v_mfma_f32_16x16x32_bf16 v[56:59], v[84:87], a[248:251], v[56:59]\n"
      "v_exp_f32 v237, v237\n"
      "v_mfma_f32_16x16x32_bf16 v[56:59], v[76:79], a[252:255], v[56:59]\n"
      "v_cvt_pk_bf16_f32 v170, v120, v121\n"
      "v_mov_b64 v[76:77], v[240:241]\n"
      "v_smfmac_f32_16x16x64_bf16 v[240:243], v[12:15], v[216:223], v5\n"
      "buffer_store_dwordx2 v[26:27], v2, s[44:47], s100 offen offset:768 nt sc1\n"
      "v_cvt_pk_bf16_f32 v118, v20, v21\n"
      "v_mfma_f32_16x16x32_bf16 v[84:87], v[208:211], v[248:251], 0\n"
      "v_exp_f32 v239, v239\n"
      "v_mfma_f32_16x16x32_bf16 v[84:87], v[212:215], v[252:255], v[84:87]\n"
      "v_cvt_pk_bf16_f32 v44, v44, v48\n"
      "v_cvt_pk_bf16_f32 v48, v45, v49\n"
      "v_cvt_pk_bf16_f32 v49, v53, v57\n"
      "v_cvt_pk_bf16_f32 v46, v46, v50\n"
      "v_smfmac_f32_16x16x32_bf16 v[240:243], v[12:13], v[116:119], v5\n"
      "v_pk_max_i16 v49, v49, 0\n"
      "v_mfma_f32_16x16x4_4b_bf16 v[20:35], v[188:189], v[156:157], 0 cbsz:2 abid:0\n"
      "v_cvt_pk_bf16_f32 v50, v47, v51\n"
      "v_cvt_pk_bf16_f32 v47, v54, v58\n"
      "v_pk_max_i16 v47, v47, 0\n"
      "v_cvt_pk_bf16_f32 v51, v55, v59\n"
      "v_cvt_pk_bf16_f32 v45, v52, v56\n"
      "v_exp_f32 v122, v122\n"
      "v_pk_max_i16 v44, v44, 0\n"
      "v_mfma_f32_16x16x32_bf16 v[212:215], v[104:107], v[248:251], 0\n"
      "v_pk_max_i16 v45, v45, 0\n"
      "v_pk_max_i16 v46, v46, 0\n"
      "v_mfma_f32_16x16x32_bf16 v[212:215], v[108:111], v[252:255], v[212:215]\n"
      "buffer_store_dwordx2 v[44:45], v2, s[44:47], s101 offen offset:0 nt sc1\n"
      "v_add_f32 v240, v240, v241\n"
      "v_mfma_f32_16x16x32_bf16 v[32:35], v[216:219], v[224:227], v[32:35]\n"
      "v_rcp_f32 v241, v240\n"
      "v_mfma_f32_16x16x32_bf16 v[32:35], v[220:223], v[228:231], v[32:35]\n"
      "ds_bpermute_b32 v242, v4, v241\n"
      "v_mfma_f32_16x16x32_bf16 v[32:35], v[116:119], v[196:199], v[32:35]\n"
      "v_exp_f32 v84, v84\n"
      "v_mfma_f32_16x16x32_bf16 v[20:23], v[216:219], v[140:143], v[20:23]\n"
      "v_exp_f32 v214, v214\n"
      "v_mfma_f32_16x16x32_bf16 v[20:23], v[220:223], v[144:147], v[20:23]\n"
      "v_exp_f32 v87, v87\n"
      "v_mfma_f32_16x16x32_bf16 v[20:23], v[116:119], v[88:91], v[20:23]\n"
      "v_exp_f32 v213, v213\n"
      "v_mfma_f32_16x16x32_bf16 v[24:27], v[216:219], v[36:39], v[24:27]\n"
      "v_exp_f32 v85, v85\n"
      "v_mfma_f32_16x16x32_bf16 v[24:27], v[220:223], v[40:43], v[24:27]\n"
      "v_exp_f32 v95, v95\n"
      "v_mfma_f32_16x16x32_bf16 v[24:27], v[116:119], v[244:247], v[24:27]\n"
      "buffer_store_dwordx2 v[46:47], v2, s[44:47], s101 offen offset:512 nt sc1\n"
      "v_pk_max_i16 v48, v48, 0\n"
      "v_mfma_f32_16x16x32_bf16 v[28:31], v[216:219], v[132:135], v[28:31]\n"
      "v_exp_f32 v86, v86\n"
      "v_mfma_f32_16x16x32_bf16 v[28:31], v[220:223], v[136:139], v[28:31]\n"
      "v_exp_f32 v215, v215\n"
      "v_mfma_f32_16x16x32_bf16 v[28:31], v[116:119], v[60:63], v[28:31]\n"
      "s_waitcnt lgkmcnt(0)\n"
      "v_mul_f32_dpp v21, v242, v21 row_newbcast:4 row_mask:0xf bank_mask:0xf\n"
      "v_mul_f32_dpp v24, v242, v24 row_newbcast:0 row_mask:0xf bank_mask:0xf\n"
      "v_mfma_f32_16x16x4_4b_bf16 v[104:119], v[188:189], v[156:157], 0 cbsz:2 abid:2\n"
      "v_mul_f32_dpp v26, v242, v26 row_newbcast:8 row_mask:0xf bank_mask:0xf\n"
      "v_cvt_pk_bf16_f32 v173, v214, v215\n"
      "v_mul_f32_dpp v27, v242, v27 row_newbcast:12 row_mask:0xf bank_mask:0xf\n"
      "v_mul_f32_dpp v32, v242, v32 row_newbcast:0 row_mask:0xf bank_mask:0xf\n"
      "v_mul_f32_dpp v23, v242, v23 row_newbcast:12 row_mask:0xf bank_mask:0xf\n"
      "v_mul_f32_dpp v31, v242, v31 row_newbcast:12 row_mask:0xf bank_mask:0xf\n"
      "v_mul_f32_dpp v25, v242, v25 row_newbcast:4 row_mask:0xf bank_mask:0xf\n"
      "v_mul_f32_dpp v35, v242, v35 row_newbcast:12 row_mask:0xf bank_mask:0xf\n"
      "v_cvt_pk_bf16_f32 v169, v86, v87\n"
      "v_mfma_f32_16x16x32_bf16 v[24:27], v[158:161], a[208:211], v[24:27]\n"
      "v_mul_f32_dpp v34, v242, v34 row_newbcast:8 row_mask:0xf bank_mask:0xf\n"
      "v_mul_f32_dpp v20, v242, v20 row_newbcast:0 row_mask:0xf bank_mask:0xf\n"
      "v_mfma_f32_16x16x32_bf16 v[24:27], v[164:167], a[212:215], v[24:27]\n"
      "v_exp_f32 v94, v94\n"
      "v_mfma_f32_16x16x32_bf16 v[24:27], v[204:207], a[216:219], v[24:27]\n"
      "v_exp_f32 v92, v92\n"
      "v_mfma_f32_16x16x32_bf16 v[24:27], v[148:151], a[220:223], v[24:27]\n"
      "buffer_store_dwordx2 v[48:49], v2, s[44:47], s101 offen offset:256 nt sc1\n"
      "v_mul_f32_dpp v22, v242, v22 row_newbcast:8 row_mask:0xf bank_mask:0xf\n"
      "v_mul_f32_dpp v29, v242, v29 row_newbcast:4 row_mask:0xf bank_mask:0xf\n"
      "v_cvt_pk_bf16_f32 v175, v94, v95\n"
      "v_mfma_f32_16x16x32_bf16 v[20:23], v[158:161], a[192:195], v[20:23]\n"
      "v_exp_f32 v212, v212\n"
      "v_mfma_f32_16x16x32_bf16 v[20:23], v[164:167], a[196:199], v[20:23]\n"
      "v_cvt_pk_bf16_f32 v172, v212, v213\n"
      "v_mul_f32_dpp v33, v242, v33 row_newbcast:4 row_mask:0xf bank_mask:0xf\n"
      "v_mfma_f32_16x16x32_bf16 v[20:23], v[204:207], a[200:203], v[20:23]\n"
      "v_exp_f32 v93, v93\n"
      "v_mfma_f32_16x16x32_bf16 v[20:23], v[148:151], a[204:207], v[20:23]\n"
      "v_cvt_pk_bf16_f32 v171, v122, v123\n"
      "v_cvt_pk_bf16_f32 v174, v92, v93\n"
      "v_pk_max_i16 v51, v51, 0\n"
      "v_mfma_f32_16x16x32_bf16 v[32:35], v[158:161], a[240:243], v[32:35]\n"
      "v_exp_f32 v192, v192\n"
      "v_mfma_f32_16x16x32_bf16 v[32:35], v[164:167], a[244:247], v[32:35]\n"
      "v_cvt_pk_bf16_f32 v168, v84, v85\n"
      "v_mul_f32_dpp v28, v242, v28 row_newbcast:0 row_mask:0xf bank_mask:0xf\n"
      "v_mfma_f32_16x16x32_bf16 v[32:35], v[204:207], a[248:251], v[32:35]\n"
      "v_exp_f32 v238, v238\n"
      "v_mfma_f32_16x16x32_bf16 v[32:35], v[148:151], a[252:255], v[32:35]\n"
      "v_mul_f32_dpp v30, v242, v30 row_newbcast:8 row_mask:0xf bank_mask:0xf\n"
      "v_pk_max_i16 v50, v50, 0\n"
      "v_smfmac_f32_16x16x64_bf16 v[76:79], v[12:15], v[168:175], v5\n"
      "buffer_store_dwordx2 v[50:51], v2, s[44:47], s101 offen offset:768 nt sc1\n"
      "v_cvt_pk_bf16_f32 v94, v236, v237\n"
      "v_mfma_f32_16x16x32_bf16 v[28:31], v[158:161], a[224:227], v[28:31]\n"
      "v_exp_f32 v193, v193\n"
      "v_mfma_f32_16x16x32_bf16 v[28:31], v[164:167], a[228:231], v[28:31]\n"
      "v_cvt_pk_bf16_f32 v92, v192, v193\n"
      "v_cvt_pk_bf16_f32 v95, v238, v239\n"
      "v_mfma_f32_16x16x32_bf16 v[28:31], v[204:207], a[232:235], v[28:31]\n"
      "v_exp_f32 v195, v195\n"
      "v_mfma_f32_16x16x32_bf16 v[28:31], v[148:151], a[236:239], v[28:31]\n"
      "v_cvt_pk_bf16_f32 v93, v194, v195\n"
      "v_cvt_pk_bf16_f32 v22, v22, v26\n"
      "v_cvt_pk_bf16_f32 v26, v23, v27\n"
      "v_pk_max_i16 v26, v26, 0\n"
      "v_smfmac_f32_16x16x32_bf16 v[76:79], v[12:13], v[92:95], v5\n"
      "s_nop 2\n"
      "v_cvt_pk_bf16_f32 v27, v31, v35\n"
      "v_pk_max_i16 v27, v27, 0\n"
      "v_mfma_f32_16x16x32_bf16 v[104:107], v[168:171], v[140:143], v[104:107]\n"
      "s_nop 1\n"
      "v_add_f32 v76, v76, v77\n"
      "v_mfma_f32_16x16x32_bf16 v[104:107], v[172:175], v[144:147], v[104:107]\n"
      "v_rcp_f32 v77, v76\n"
      "v_mfma_f32_16x16x32_bf16 v[104:107], v[92:95], v[88:91], v[104:107]\n"
      "buffer_store_dwordx2 v[26:27], v2, s[44:47], s34 offen offset:768 nt sc1\n"
      "ds_bpermute_b32 v78, v4, v77 offset:128\n"
      "v_mfma_f32_16x16x32_bf16 v[108:111], v[168:171], v[36:39], v[108:111]\n"
      "v_cvt_pk_bf16_f32 v20, v20, v24\n"
      "v_cvt_pk_bf16_f32 v24, v21, v25\n"
      "v_mfma_f32_16x16x32_bf16 v[108:111], v[172:175], v[40:43], v[108:111]\n"
      "v_mfma_f32_16x16x32_bf16 v[108:111], v[92:95], v[244:247], v[108:111]\n"
      "v_cvt_pk_bf16_f32 v25, v29, v33\n"
      "v_pk_max_i16 v20, v20, 0\n"
      "v_mfma_f32_16x16x32_bf16 v[116:119], v[168:171], v[224:227], v[116:119]\n"
      "v_pk_max_i16 v25, v25, 0\n"
      "v_pk_max_i16 v24, v24, 0\n"
      "v_mfma_f32_16x16x32_bf16 v[116:119], v[172:175], v[228:231], v[116:119]\n"
      "v_mfma_f32_16x16x32_bf16 v[116:119], v[92:95], v[196:199], v[116:119]\n"
      "buffer_store_dwordx2 v[24:25], v2, s[44:47], s34 offen offset:256 nt sc1\n"
      "v_pk_max_i16 v22, v22, 0\n"
      "v_mfma_f32_16x16x32_bf16 v[112:115], v[168:171], v[132:135], v[112:115]\n"
      "v_cvt_pk_bf16_f32 v23, v30, v34\n"
      "v_cvt_pk_bf16_f32 v21, v28, v32\n"
      "v_mfma_f32_16x16x32_bf16 v[112:115], v[172:175], v[136:139], v[112:115]\n"
      "v_mfma_f32_16x16x32_bf16 v[112:115], v[92:95], v[60:63], v[112:115]\n"
      "s_waitcnt lgkmcnt(0)\n"
      "v_mul_f32_dpp v110, v78, v110 row_newbcast:8 row_mask:0xf bank_mask:0xf\n"
      "v_mul_f32_dpp v109, v78, v109 row_newbcast:4 row_mask:0xf bank_mask:0xf\n"
      "v_mul_f32_dpp v111, v78, v111 row_newbcast:12 row_mask:0xf bank_mask:0xf\n"
      "v_mul_f32_dpp v108, v78, v108 row_newbcast:0 row_mask:0xf bank_mask:0xf\n"
      "v_mul_f32_dpp v106, v78, v106 row_newbcast:8 row_mask:0xf bank_mask:0xf\n"
      "v_pk_max_i16 v23, v23, 0\n"
      "v_mfma_f32_16x16x32_bf16 v[108:111], v[232:235], a[208:211], v[108:111]\n"
      "v_mul_f32_dpp v104, v78, v104 row_newbcast:0 row_mask:0xf bank_mask:0xf\n"
      "v_mfma_f32_16x16x32_bf16 v[108:111], v[128:131], a[212:215], v[108:111]\n"
      "v_mul_f32_dpp v105, v78, v105 row_newbcast:4 row_mask:0xf bank_mask:0xf\n"
      "v_pk_max_i16 v21, v21, 0\n"
      "v_mfma_f32_16x16x32_bf16 v[108:111], v[180:183], a[216:219], v[108:111]\n"
      "v_mul_f32_dpp v118, v78, v118 row_newbcast:8 row_mask:0xf bank_mask:0xf\n"
      "v_mul_f32_dpp v107, v78, v107 row_newbcast:12 row_mask:0xf bank_mask:0xf\n"
      "v_mfma_f32_16x16x32_bf16 v[108:111], v[124:127], a[220:223], v[108:111]\n"
      "buffer_store_dwordx2 v[22:23], v2, s[44:47], s34 offen offset:512 nt sc1\n"
      "v_mul_f32_dpp v116, v78, v116 row_newbcast:0 row_mask:0xf bank_mask:0xf\n"
      "v_mfma_f32_16x16x32_bf16 v[104:107], v[232:235], a[192:195], v[104:107]\n"
      "v_mul_f32_dpp v114, v78, v114 row_newbcast:8 row_mask:0xf bank_mask:0xf\n"
      "v_mul_f32_dpp v113, v78, v113 row_newbcast:4 row_mask:0xf bank_mask:0xf\n"
      "v_mfma_f32_16x16x32_bf16 v[104:107], v[128:131], a[196:199], v[104:107]\n"
      "v_mul_f32_dpp v119, v78, v119 row_newbcast:12 row_mask:0xf bank_mask:0xf\n"
      "v_mul_f32_dpp v117, v78, v117 row_newbcast:4 row_mask:0xf bank_mask:0xf\n"
      "v_mfma_f32_16x16x32_bf16 v[104:107], v[180:183], a[200:203], v[104:107]\n"
      "v_mfma_f32_16x16x32_bf16 v[104:107], v[124:127], a[204:207], v[104:107]\n"
      "buffer_store_dwordx2 v[20:21], v2, s[44:47], s34 offen offset:0 nt sc1\n"
      "v_mul_f32_dpp v115, v78, v115 row_newbcast:12 row_mask:0xf bank_mask:0xf\n"
      "v_mfma_f32_16x16x32_bf16 v[116:119], v[232:235], a[240:243], v[116:119]\n"
      "v_mul_f32_dpp v112, v78, v112 row_newbcast:0 row_mask:0xf bank_mask:0xf\n"
      "v_mfma_f32_16x16x32_bf16 v[116:119], v[128:131], a[244:247], v[116:119]\n"
      "v_mfma_f32_16x16x32_bf16 v[116:119], v[180:183], a[248:251], v[116:119]\n"
      "v_mfma_f32_16x16x32_bf16 v[116:119], v[124:127], a[252:255], v[116:119]\n"
      "s_nop 0\n"
      "v_cvt_pk_bf16_f32 v106, v106, v110\n"
      "v_mfma_f32_16x16x32_bf16 v[112:115], v[232:235], a[224:227], v[112:115]\n"
      "v_pk_max_i16 v106, v106, 0\n"
      "v_mfma_f32_16x16x32_bf16 v[112:115], v[128:131], a[228:231], v[112:115]\n"
      "v_cvt_pk_bf16_f32 v110, v107, v111\n"
      "v_mfma_f32_16x16x32_bf16 v[112:115], v[180:183], a[232:235], v[112:115]\n"
      "v_mfma_f32_16x16x32_bf16 v[112:115], v[124:127], a[236:239], v[112:115]\n"
      "s_nop 7\n"
      "v_cvt_pk_bf16_f32 v107, v114, v118\n"
      "v_pk_max_i16 v107, v107, 0\n"
      "buffer_store_dwordx2 v[106:107], v2, s[44:47], s35 offen offset:512 nt sc1\n"
      "v_cvt_pk_bf16_f32 v104, v104, v108\n"
      "v_cvt_pk_bf16_f32 v108, v105, v109\n"
      "v_cvt_pk_bf16_f32 v109, v113, v117\n"
      "v_pk_max_i16 v108, v108, 0\n"
      "v_cvt_pk_bf16_f32 v111, v115, v119\n"
      "v_pk_max_i16 v111, v111, 0\n"
      "v_pk_max_i16 v109, v109, 0\n"
      "v_maximum3_f32 v8, v8, v200, v201\n"
      "buffer_store_dwordx2 v[108:109], v2, s[44:47], s35 offen offset:256 nt sc1\n"
      "v_maximum3_f32 v8, v8, v240, v241\n"
      "v_pk_max_i16 v110, v110, 0\n"
      "s_not_b64 vcc, vcc\n"
      "v_cvt_pk_bf16_f32 v105, v112, v116\n"
      "v_pk_max_i16 v105, v105, 0\n"
      "buffer_store_dwordx2 v[110:111], v2, s[44:47], s35 offen offset:768 nt sc1\n"
      "v_pk_max_i16 v104, v104, 0\n"
      "v_maximum3_f32 v8, v8, v76, v77\n"
      "buffer_store_dwordx2 v[104:105], v2, s[44:47], s35 offen offset:0 nt sc1\n"
      "L_end_%=:\n"
      "s_waitcnt vmcnt(0) lgkmcnt(0)\n"
      "v_cmp_nge_f32 vcc, 0x53800000, v8\n"
      "s_nop 4\n"
      "s_cbranch_vccnz L_ai_end\n"
      "s_endpgm\n"
      "L_ai_end:\n"
      "s_mov_b32 s40, s4\n"
      "s_and_b32 s41, s5, 0xffff\n"
      "s_mov_b32 s42, 104857600\n"
      "s_mov_b32 s44, s14\n"
      "s_and_b32 s45, s15, 0xffff\n"
      "s_mov_b32 s46, 104857600\n"
      "s_mov_b32 s48, s18\n"
      "s_add_u32 s49, s18, 13107200\n"
      "s_mov_b32 s50, 8\n"
      "s_mov_b32 vcc_lo, 0x55555555\n"
      "s_mov_b32 vcc_hi, 0x55555555\n"
      "v_subrev_u32 v9, 448, v9\n"
      "v_subrev_u32 v10, 448, v10\n"
      "v_subrev_u32 v11, 3584, v11\n"
      "s_add_u32 s52, s48, 0\n"
      "s_add_u32 m0, s53, 0\n"
      "s_nop 0\n"
      "buffer_load_dwordx4 v1, s[40:43], s52 offen offset:0 lds\n"
      "buffer_load_dwordx4 v1, s[40:43], s52 offen offset:2048 lds\n"
      "s_add_u32 m0, s53, 896\n"
      "s_nop 0\n"
      "buffer_load_dwordx4 v1, s[40:43], s52 offen offset:128 lds\n"
      "buffer_load_dwordx4 v1, s[40:43], s52 offen offset:2176 lds\n"
      "s_add_u32 s52, s48, 4096\n"
      "s_add_u32 m0, s53, 4096\n"
      "s_nop 0\n"
      "buffer_load_dwordx4 v1, s[40:43], s52 offen offset:0 lds\n"
      "buffer_load_dwordx4 v1, s[40:43], s52 offen offset:2048 lds\n"
      "s_add_u32 m0, s53, 4992\n"
      "s_nop 0\n"
      "buffer_load_dwordx4 v1, s[40:43], s52 offen offset:128 lds\n"
      "buffer_load_dwordx4 v1, s[40:43], s52 offen offset:2176 lds\n"
      "s_add_u32 s52, s48, 8192\n"
      "s_add_u32 m0, s53, 8192\n"
      "s_nop 0\n"
      "buffer_load_dwordx4 v1, s[40:43], s52 offen offset:0 lds\n"
      "buffer_load_dwordx4 v1, s[40:43], s52 offen offset:2048 lds\n"
      "s_add_u32 m0, s53, 9088\n"
      "s_nop 0\n"
      "buffer_load_dwordx4 v1, s[40:43], s52 offen offset:128 lds\n"
      "buffer_load_dwordx4 v1, s[40:43], s52 offen offset:2176 lds\n"
      "s_add_u32 s52, s48, 12288\n"
      "s_add_u32 m0, s53, 12288\n"
      "s_nop 0\n"
      "buffer_load_dwordx4 v1, s[40:43], s52 offen offset:0 lds\n"
      "buffer_load_dwordx4 v1, s[40:43], s52 offen offset:2048 lds\n"
      "s_add_u32 m0, s53, 13184\n"
      "s_nop 0\n"
      "buffer_load_dwordx4 v1, s[40:43], s52 offen offset:128 lds\n"
      "buffer_load_dwordx4 v1, s[40:43], s52 offen offset:2176 lds\n"
      "s_add_u32 s52, s48, 16384\n"
      "s_add_u32 m0, s53, 16384\n"
      "s_nop 0\n"
      "buffer_load_dwordx4 v1, s[40:43], s52 offen offset:0 lds\n"
      "buffer_load_dwordx4 v1, s[40:43], s52 offen offset:2048 lds\n"
      "s_add_u32 m0, s53, 17280\n"
      "s_nop 0\n"
      "buffer_load_dwordx4 v1, s[40:43], s52 offen offset:128 lds\n"
      "buffer_load_dwordx4 v1, s[40:43], s52 offen offset:2176 lds\n"
      "s_add_u32 s52, s48, 20480\n"
      "s_add_u32 m0, s53, 20480\n"
      "s_nop 0\n"
      "buffer_load_dwordx4 v1, s[40:43], s52 offen offset:0 lds\n"
      "buffer_load_dwordx4 v1, s[40:43], s52 offen offset:2048 lds\n"
      "s_add_u32 m0, s53, 21376\n"
      "s_nop 0\n"
      "buffer_load_dwordx4 v1, s[40:43], s52 offen offset:128 lds\n"
      "buffer_load_dwordx4 v1, s[40:43], s52 offen offset:2176 lds\n"
      "s_waitcnt vmcnt(0)\n"
      "L_safe_%=:\n"
      "s_waitcnt vmcnt(45)\n"
      "ds_read_b128 v[148:151], v0 offset:12288\n"
      "ds_read_b128 v[136:139], v0 offset:12800\n"
      "ds_read_b128 v[140:143], v0 offset:13312\n"
      "ds_read_b128 v[144:147], v0 offset:13824\n"
      "s_waitcnt vmcnt(41)\n"
      "ds_read_b128 v[252:255], v0 offset:16384\n"
      "ds_read_b128 v[48:51], v0 offset:16896\n"
      "ds_read_b128 v[124:127], v0 offset:17408\n"
      "ds_read_b128 v[116:119], v0 offset:17920\n"
      "s_waitcnt lgkmcnt(7)\n"
      "v_mfma_f32_16x16x32_bf16 v[216:219], a[0:3], v[148:151], 0\n"
      "s_waitcnt lgkmcnt(6)\n"
      "v_mfma_f32_16x16x32_bf16 v[216:219], a[4:7], v[136:139], v[216:219]\n"
      "s_waitcnt lgkmcnt(5)\n"
      "v_mfma_f32_16x16x32_bf16 v[216:219], a[8:11], v[140:143], v[216:219]\n"
      "s_waitcnt lgkmcnt(4)\n"
      "v_mfma_f32_16x16x32_bf16 v[216:219], a[12:15], v[144:147], v[216:219]\n"
      "v_mfma_f32_16x16x32_bf16 v[96:99], a[16:19], v[148:151], 0\n"
      "v_mfma_f32_16x16x32_bf16 v[96:99], a[20:23], v[136:139], v[96:99]\n"
      "v_mfma_f32_16x16x32_bf16 v[96:99], a[24:27], v[140:143], v[96:99]\n"
      "v_mfma_f32_16x16x32_bf16 v[96:99], a[28:31], v[144:147], v[96:99]\n"
      "v_mfma_f32_16x16x32_bf16 v[24:27], a[32:35], v[148:151], 0\n"
      "v_mfma_f32_16x16x32_bf16 v[24:27], a[36:39], v[136:139], v[24:27]\n"
      "v_mfma_f32_16x16x32_bf16 v[24:27], a[40:43], v[140:143], v[24:27]\n"
      "v_mfma_f32_16x16x32_bf16 v[24:27], a[44:47], v[144:147], v[24:27]\n"
      "v_cvt_pk_bf16_f32 v92, v216, v217\n"
      "v_cvt_pk_bf16_f32 v93, v218, v219\n"
      "v_mfma_f32_16x16x32_bf16 v[216:219], a[48:51], v[148:151], 0\n"
      "v_mfma_f32_16x16x32_bf16 v[216:219], a[52:55], v[136:139], v[216:219]\n"
      "v_cvt_pk_bf16_f32 v94, v96, v97\n"
      "v_cvt_pk_bf16_f32 v95, v98, v99\n"
      "v_mfma_f32_16x16x32_bf16 v[216:219], a[56:59], v[140:143], v[216:219]\n"
      "v_mfma_f32_16x16x32_bf16 v[216:219], a[60:63], v[144:147], v[216:219]\n"
      "v_cvt_pk_bf16_f32 v96, v24, v25\n"
      "v_cvt_pk_bf16_f32 v97, v26, v27\n"
      "v_mfma_f32_16x16x32_bf16 v[248:251], v[148:151], a[64:67], 0\n"
      "v_mfma_f32_16x16x32_bf16 v[248:251], v[136:139], a[68:71], v[248:251]\n"
      "v_mfma_f32_16x16x32_bf16 v[248:251], v[140:143], a[72:75], v[248:251]\n"
      "v_mfma_f32_16x16x32_bf16 v[248:251], v[144:147], a[76:79], v[248:251]\n"
      "v_mfma_f32_16x16x32_bf16 v[24:27], v[148:151], a[80:83], 0\n"
      "v_mfma_f32_16x16x32_bf16 v[24:27], v[136:139], a[84:87], v[24:27]\n"
      "v_cvt_pk_bf16_f32 v98, v216, v217\n"
      "v_cvt_pk_bf16_f32 v99, v218, v219\n"
      "v_mfma_f32_16x16x32_bf16 v[24:27], v[140:143], a[88:91], v[24:27]\n"
      "v_mfma_f32_16x16x32_bf16 v[24:27], v[144:147], a[92:95], v[24:27]\n"
      "v_mfma_f32_16x16x32_bf16 v[216:219], v[148:151], a[96:99], 0\n"
      "v_mfma_f32_16x16x32_bf16 v[216:219], v[136:139], a[100:103], v[216:219]\n"
      "v_cvt_pk_bf16_f32 v58, v248, v249\n"
      "v_cvt_pk_bf16_f32 v59, v250, v251\n"
      "v_mfma_f32_16x16x32_bf16 v[216:219], v[140:143], a[104:107], v[216:219]\n"
      "v_mfma_f32_16x16x32_bf16 v[216:219], v[144:147], a[108:111], v[216:219]\n"
      "v_mfma_f32_16x16x32_bf16 v[248:251], v[148:151], a[112:115], 0\n"
      "v_mfma_f32_16x16x32_bf16 v[248:251], v[136:139], a[116:119], v[248:251]\n"
      "v_cvt_pk_bf16_f32 v134, v24, v25\n"
      "v_cvt_pk_bf16_f32 v135, v26, v27\n"
      "v_mfma_f32_16x16x32_bf16 v[248:251], v[140:143], a[120:123], v[248:251]\n"
      "v_mfma_f32_16x16x32_bf16 v[248:251], v[144:147], a[124:127], v[248:251]\n"
      "s_waitcnt vmcnt(37)\n"
      "ds_read_b128 v[24:27], v0 offset:20480\n"
      "ds_read_b128 v[152:155], v0 offset:20992\n"
      "ds_read_b128 v[20:23], v0 offset:21504\n"
      "ds_read_b128 v[156:159], v0 offset:22016\n"
      "v_cvt_pk_bf16_f32 v106, v216, v217\n"
      "v_cvt_pk_bf16_f32 v107, v218, v219\n"
      "s_waitcnt lgkmcnt(7)\n"
      "v_mfma_f32_16x16x32_bf16 v[220:223], a[0:3], v[252:255], 0\n"
      "s_waitcnt lgkmcnt(6)\n"
      "v_mfma_f32_16x16x32_bf16 v[220:223], a[4:7], v[48:51], v[220:223]\n"
      "v_cvt_pk_bf16_f32 v198, v248, v249\n"
      "v_cvt_pk_bf16_f32 v199, v250, v251\n"
      "s_waitcnt lgkmcnt(5)\n"
      "v_mfma_f32_16x16x32_bf16 v[220:223], a[8:11], v[124:127], v[220:223]\n"
      "s_waitcnt lgkmcnt(4)\n"
      "v_mfma_f32_16x16x32_bf16 v[220:223], a[12:15], v[116:119], v[220:223]\n"
      "v_mfma_f32_16x16x32_bf16 v[52:55], a[16:19], v[252:255], 0\n"
      "v_mfma_f32_16x16x32_bf16 v[52:55], a[20:23], v[48:51], v[52:55]\n"
      "v_mfma_f32_16x16x32_bf16 v[52:55], a[24:27], v[124:127], v[52:55]\n"
      "v_mfma_f32_16x16x32_bf16 v[52:55], a[28:31], v[116:119], v[52:55]\n"
      "v_mfma_f32_16x16x32_bf16 v[244:247], a[32:35], v[252:255], 0\n"
      "v_mfma_f32_16x16x32_bf16 v[244:247], a[36:39], v[48:51], v[244:247]\n"
      "v_mfma_f32_16x16x32_bf16 v[244:247], a[40:43], v[124:127], v[244:247]\n"
      "v_mfma_f32_16x16x32_bf16 v[244:247], a[44:47], v[116:119], v[244:247]\n"
      "v_cvt_pk_bf16_f32 v216, v220, v221\n"
      "v_cvt_pk_bf16_f32 v217, v222, v223\n"
      "v_mfma_f32_16x16x32_bf16 v[248:251], a[48:51], v[252:255], 0\n"
      "v_mfma_f32_16x16x32_bf16 v[248:251], a[52:55], v[48:51], v[248:251]\n"
      "v_cvt_pk_bf16_f32 v218, v52, v53\n"
      "v_cvt_pk_bf16_f32 v219, v54, v55\n"
      "v_mfma_f32_16x16x32_bf16 v[248:251], a[56:59], v[124:127], v[248:251]\n"
      "v_mfma_f32_16x16x32_bf16 v[248:251], a[60:63], v[116:119], v[248:251]\n"
      "v_cvt_pk_bf16_f32 v220, v244, v245\n"
      "v_cvt_pk_bf16_f32 v221, v246, v247\n"
      "v_mfma_f32_16x16x32_bf16 v[244:247], v[252:255], a[64:67], 0\n"
      "v_mfma_f32_16x16x32_bf16 v[244:247], v[48:51], a[68:71], v[244:247]\n"
      "v_mfma_f32_16x16x32_bf16 v[244:247], v[124:127], a[72:75], v[244:247]\n"
      "v_mfma_f32_16x16x32_bf16 v[244:247], v[116:119], a[76:79], v[244:247]\n"
      "v_mfma_f32_16x16x32_bf16 v[52:55], v[252:255], a[80:83], 0\n"
      "v_mfma_f32_16x16x32_bf16 v[52:55], v[48:51], a[84:87], v[52:55]\n"
      "v_cvt_pk_bf16_f32 v222, v248, v249\n"
      "v_cvt_pk_bf16_f32 v223, v250, v251\n"
      "v_mfma_f32_16x16x32_bf16 v[52:55], v[124:127], a[88:91], v[52:55]\n"
      "v_mfma_f32_16x16x32_bf16 v[52:55], v[116:119], a[92:95], v[52:55]\n"
      "v_mfma_f32_16x16x32_bf16 v[248:251], v[252:255], a[96:99], 0\n"
      "v_mfma_f32_16x16x32_bf16 v[248:251], v[48:51], a[100:103], v[248:251]\n"
      "v_cvt_pk_bf16_f32 v120, v244, v245\n"
      "v_cvt_pk_bf16_f32 v121, v246, v247\n"
      "v_mfma_f32_16x16x32_bf16 v[248:251], v[124:127], a[104:107], v[248:251]\n"
      "v_mfma_f32_16x16x32_bf16 v[248:251], v[116:119], a[108:111], v[248:251]\n"
      "v_mfma_f32_16x16x32_bf16 v[244:247], v[252:255], a[112:115], 0\n"
      "v_mfma_f32_16x16x32_bf16 v[244:247], v[48:51], a[116:119], v[244:247]\n"
      "v_cvt_pk_bf16_f32 v68, v52, v53\n"
      "v_cvt_pk_bf16_f32 v69, v54, v55\n"
      "v_mfma_f32_16x16x32_bf16 v[244:247], v[124:127], a[120:123], v[244:247]\n"
      "v_mfma_f32_16x16x32_bf16 v[244:247], v[116:119], a[124:127], v[244:247]\n"
      "s_waitcnt vmcnt(25)\n"
      "ds_read_b128 v[52:55], v0 offset:8192\n"
      "ds_read_b128 v[128:131], v0 offset:8704\n"
      "ds_read_b128 v[168:171], v0 offset:9216\n"
      "ds_read_b128 v[164:167], v0 offset:9728\n"
      "v_cvt_pk_bf16_f32 v36, v248, v249\n"
      "v_cvt_pk_bf16_f32 v37, v250, v251\n"
      "s_waitcnt lgkmcnt(7)\n"
      "v_mfma_f32_16x16x32_bf16 v[248:251], a[0:3], v[24:27], 0\n"
      "s_add_u32 m0, s53, 12288\n"
      "s_add_u32 s54, s49, 12288\n"
      "buffer_load_dwordx4 v1, s[40:43], s54 offen offset:0 lds\n"
      "v_cvt_pk_bf16_f32 v240, v244, v245\n"
      "v_cvt_pk_bf16_f32 v241, v246, v247\n"
      "s_waitcnt lgkmcnt(6)\n"
      "v_mfma_f32_16x16x32_bf16 v[248:251], a[4:7], v[152:155], v[248:251]\n"
      "s_waitcnt lgkmcnt(5)\n"
      "v_mfma_f32_16x16x32_bf16 v[248:251], a[8:11], v[20:23], v[248:251]\n"
      "buffer_load_dwordx4 v1, s[40:43], s54 offen offset:2048 lds\n"
      "s_add_u32 m0, s53, 13184\n"
      "s_waitcnt lgkmcnt(4)\n"
      "v_mfma_f32_16x16x32_bf16 v[248:251], a[12:15], v[156:159], v[248:251]\n"
      "v_mfma_f32_16x16x32_bf16 v[40:43], a[16:19], v[24:27], 0\n"
      "buffer_load_dwordx4 v1, s[40:43], s54 offen offset:128 lds\n"
      "v_mfma_f32_16x16x32_bf16 v[40:43], a[20:23], v[152:155], v[40:43]\n"
      "v_mfma_f32_16x16x32_bf16 v[40:43], a[24:27], v[20:23], v[40:43]\n"
      "buffer_load_dwordx4 v1, s[40:43], s54 offen offset:2176 lds\n"
      "v_mfma_f32_16x16x32_bf16 v[40:43], a[28:31], v[156:159], v[40:43]\n"
      "v_mfma_f32_16x16x32_bf16 v[100:103], a[32:35], v[24:27], 0\n"
      "v_mfma_f32_16x16x32_bf16 v[100:103], a[36:39], v[152:155], v[100:103]\n"
      "v_cvt_pk_bf16_f32 v244, v248, v249\n"
      "v_cvt_pk_bf16_f32 v245, v250, v251\n"
      "v_mfma_f32_16x16x32_bf16 v[100:103], a[40:43], v[20:23], v[100:103]\n"
      "v_mfma_f32_16x16x32_bf16 v[100:103], a[44:47], v[156:159], v[100:103]\n"
      "v_mfma_f32_16x16x32_bf16 v[44:47], a[48:51], v[24:27], 0\n"
      "v_mfma_f32_16x16x32_bf16 v[44:47], a[52:55], v[152:155], v[44:47]\n"
      "v_cvt_pk_bf16_f32 v246, v40, v41\n"
      "v_cvt_pk_bf16_f32 v247, v42, v43\n"
      "v_mfma_f32_16x16x32_bf16 v[44:47], a[56:59], v[20:23], v[44:47]\n"
      "v_mfma_f32_16x16x32_bf16 v[44:47], a[60:63], v[156:159], v[44:47]\n"
      "v_mfma_f32_16x16x32_bf16 v[40:43], v[24:27], a[64:67], 0\n"
      "v_mfma_f32_16x16x32_bf16 v[40:43], v[152:155], a[68:71], v[40:43]\n"
      "v_cvt_pk_bf16_f32 v248, v100, v101\n"
      "v_cvt_pk_bf16_f32 v249, v102, v103\n"
      "v_mfma_f32_16x16x32_bf16 v[40:43], v[20:23], a[72:75], v[40:43]\n"
      "v_mfma_f32_16x16x32_bf16 v[40:43], v[156:159], a[76:79], v[40:43]\n"
      "v_mfma_f32_16x16x32_bf16 v[100:103], v[24:27], a[80:83], 0\n"
      "v_mfma_f32_16x16x32_bf16 v[100:103], v[152:155], a[84:87], v[100:103]\n"
      "v_cvt_pk_bf16_f32 v250, v44, v45\n"
      "v_cvt_pk_bf16_f32 v251, v46, v47\n"
      "v_mfma_f32_16x16x32_bf16 v[100:103], v[20:23], a[88:91], v[100:103]\n"
      "v_mfma_f32_16x16x32_bf16 v[100:103], v[156:159], a[92:95], v[100:103]\n"
      "v_mfma_f32_16x16x32_bf16 v[44:47], v[24:27], a[96:99], 0\n"
      "v_mfma_f32_16x16x32_bf16 v[44:47], v[152:155], a[100:103], v[44:47]\n"
      "v_cvt_pk_bf16_f32 v122, v40, v41\n"
      "v_cvt_pk_bf16_f32 v123, v42, v43\n"
      "v_mfma_f32_16x16x32_bf16 v[44:47], v[20:23], a[104:107], v[44:47]\n"
      "v_mfma_f32_16x16x32_bf16 v[44:47], v[156:159], a[108:111], v[44:47]\n"
      "v_mfma_f32_16x16x32_bf16 v[40:43], v[24:27], a[112:115], 0\n"
      "v_mfma_f32_16x16x32_bf16 v[40:43], v[152:155], a[116:119], v[40:43]\n"
      "v_cvt_pk_bf16_f32 v70, v100, v101\n"
      "v_cvt_pk_bf16_f32 v71, v102, v103\n"
      "v_mfma_f32_16x16x32_bf16 v[40:43], v[20:23], a[120:123], v[40:43]\n"
      "v_mfma_f32_16x16x32_bf16 v[40:43], v[156:159], a[124:127], v[40:43]\n"
      "s_waitcnt vmcnt(21)\n"
      "ds_read_b128 v[100:103], v0 offset:4096\n"
      "ds_read_b128 v[108:111], v0 offset:4608\n"
      "ds_read_b128 v[176:179], v0 offset:5120\n"
      "ds_read_b128 v[172:175], v0 offset:5632\n"
      "v_cvt_pk_bf16_f32 v38, v44, v45\n"
      "v_cvt_pk_bf16_f32 v39, v46, v47\n"
      "s_waitcnt lgkmcnt(7)\n"
      "v_mfma_f32_16x16x32_bf16 v[44:47], a[0:3], v[52:55], 0\n"
      "s_add_u32 m0, s53, 16384\n"
      "s_add_u32 s54, s49, 16384\n"
      "buffer_load_dwordx4 v1, s[40:43], s54 offen offset:0 lds\n"
      "v_cvt_pk_bf16_f32 v242, v40, v41\n"
      "v_cvt_pk_bf16_f32 v243, v42, v43\n"
      "s_waitcnt lgkmcnt(6)\n"
      "v_mfma_f32_16x16x32_bf16 v[44:47], a[4:7], v[128:131], v[44:47]\n"
      "s_waitcnt lgkmcnt(5)\n"
      "v_mfma_f32_16x16x32_bf16 v[44:47], a[8:11], v[168:171], v[44:47]\n"
      "buffer_load_dwordx4 v1, s[40:43], s54 offen offset:2048 lds\n"
      "s_add_u32 m0, s53, 17280\n"
      "s_waitcnt lgkmcnt(4)\n"
      "v_mfma_f32_16x16x32_bf16 v[44:47], a[12:15], v[164:167], v[44:47]\n"
      "v_mfma_f32_16x16x32_bf16 v[112:115], a[16:19], v[52:55], 0\n"
      "buffer_load_dwordx4 v1, s[40:43], s54 offen offset:128 lds\n"
      "v_mfma_f32_16x16x32_bf16 v[112:115], a[20:23], v[128:131], v[112:115]\n"
      "v_mfma_f32_16x16x32_bf16 v[112:115], a[24:27], v[168:171], v[112:115]\n"
      "buffer_load_dwordx4 v1, s[40:43], s54 offen offset:2176 lds\n"
      "v_mfma_f32_16x16x32_bf16 v[112:115], a[28:31], v[164:167], v[112:115]\n"
      "v_mfma_f32_16x16x32_bf16 v[192:195], a[32:35], v[52:55], 0\n"
      "v_mfma_f32_16x16x32_bf16 v[192:195], a[36:39], v[128:131], v[192:195]\n"
      "v_cvt_pk_bf16_f32 v40, v44, v45\n"
      "v_cvt_pk_bf16_f32 v41, v46, v47\n"
      "v_mfma_f32_16x16x32_bf16 v[192:195], a[40:43], v[168:171], v[192:195]\n"
      "v_mfma_f32_16x16x32_bf16 v[192:195], a[44:47], v[164:167], v[192:195]\n"
      "v_mfma_f32_16x16x32_bf16 v[224:227], a[48:51], v[52:55], 0\n"
      "v_mfma_f32_16x16x32_bf16 v[224:227], a[52:55], v[128:131], v[224:227]\n"
      "v_cvt_pk_bf16_f32 v42, v112, v113\n"
      "v_cvt_pk_bf16_f32 v43, v114, v115\n"
      "v_mfma_f32_16x16x32_bf16 v[224:227], a[56:59], v[168:171], v[224:227]\n"
      "v_mfma_f32_16x16x32_bf16 v[224:227], a[60:63], v[164:167], v[224:227]\n"
      "v_mfma_f32_16x16x32_bf16 v[32:35], v[52:55], a[64:67], 0\n"
      "v_mfma_f32_16x16x32_bf16 v[32:35], v[128:131], a[68:71], v[32:35]\n"
      "v_cvt_pk_bf16_f32 v44, v192, v193\n"
      "v_cvt_pk_bf16_f32 v45, v194, v195\n"
      "v_mfma_f32_16x16x32_bf16 v[32:35], v[168:171], a[72:75], v[32:35]\n"
      "v_mfma_f32_16x16x32_bf16 v[32:35], v[164:167], a[76:79], v[32:35]\n"
      "v_mfma_f32_16x16x32_bf16 v[160:163], v[52:55], a[80:83], 0\n"
      "v_mfma_f32_16x16x32_bf16 v[160:163], v[128:131], a[84:87], v[160:163]\n"
      "v_cvt_pk_bf16_f32 v46, v224, v225\n"
      "v_cvt_pk_bf16_f32 v47, v226, v227\n"
      "v_mfma_f32_16x16x32_bf16 v[160:163], v[168:171], a[88:91], v[160:163]\n"
      "v_mfma_f32_16x16x32_bf16 v[160:163], v[164:167], a[92:95], v[160:163]\n"
      "v_mfma_f32_16x16x32_bf16 v[112:115], v[52:55], a[96:99], 0\n"
      "v_mfma_f32_16x16x32_bf16 v[112:115], v[128:131], a[100:103], v[112:115]\n"
      "v_cvt_pk_bf16_f32 v56, v32, v33\n"
      "v_cvt_pk_bf16_f32 v57, v34, v35\n"
      "v_mfma_f32_16x16x32_bf16 v[112:115], v[168:171], a[104:107], v[112:115]\n"
      "v_mfma_f32_16x16x32_bf16 v[112:115], v[164:167], a[108:111], v[112:115]\n"
      "v_mfma_f32_16x16x32_bf16 v[192:195], v[52:55], a[112:115], 0\n"
      "v_mfma_f32_16x16x32_bf16 v[192:195], v[128:131], a[116:119], v[192:195]\n"
      "v_cvt_pk_bf16_f32 v132, v160, v161\n"
      "v_cvt_pk_bf16_f32 v133, v162, v163\n"
      "v_mfma_f32_16x16x32_bf16 v[192:195], v[168:171], a[120:123], v[192:195]\n"
      "v_mfma_f32_16x16x32_bf16 v[192:195], v[164:167], a[124:127], v[192:195]\n"
      "s_waitcnt vmcnt(17)\n"
      "ds_read_b128 v[224:227], v0 offset:0\n"
      "ds_read_b128 v[32:35], v0 offset:512\n"
      "ds_read_b128 v[160:163], v0 offset:1024\n"
      "ds_read_b128 v[28:31], v0 offset:1536\n"
      "v_cvt_pk_bf16_f32 v104, v112, v113\n"
      "v_cvt_pk_bf16_f32 v105, v114, v115\n"
      "s_waitcnt lgkmcnt(7)\n"
      "v_mfma_f32_16x16x32_bf16 v[168:171], a[0:3], v[100:103], 0\n"
      "s_add_u32 m0, s53, 20480\n"
      "s_add_u32 s54, s49, 20480\n"
      "buffer_load_dwordx4 v1, s[40:43], s54 offen offset:0 lds\n"
      "v_cvt_pk_bf16_f32 v196, v192, v193\n"
      "v_cvt_pk_bf16_f32 v197, v194, v195\n"
      "s_waitcnt lgkmcnt(6)\n"
      "v_mfma_f32_16x16x32_bf16 v[168:171], a[4:7], v[108:111], v[168:171]\n"
      "s_waitcnt lgkmcnt(5)\n"
      "v_mfma_f32_16x16x32_bf16 v[168:171], a[8:11], v[176:179], v[168:171]\n"
      "buffer_load_dwordx4 v1, s[40:43], s54 offen offset:2048 lds\n"
      "s_add_u32 m0, s53, 21376\n"
      "s_waitcnt lgkmcnt(4)\n"
      "v_mfma_f32_16x16x32_bf16 v[168:171], a[12:15], v[172:175], v[168:171]\n"
      "v_mfma_f32_16x16x32_bf16 v[112:115], a[16:19], v[100:103], 0\n"
      "buffer_load_dwordx4 v1, s[40:43], s54 offen offset:128 lds\n"
      "v_mfma_f32_16x16x32_bf16 v[112:115], a[20:23], v[108:111], v[112:115]\n"
      "v_mfma_f32_16x16x32_bf16 v[112:115], a[24:27], v[176:179], v[112:115]\n"
      "buffer_load_dwordx4 v1, s[40:43], s54 offen offset:2176 lds\n"
      "v_mfma_f32_16x16x32_bf16 v[112:115], a[28:31], v[172:175], v[112:115]\n"
      "v_mfma_f32_16x16x32_bf16 v[192:195], a[32:35], v[100:103], 0\n"
      "v_mfma_f32_16x16x32_bf16 v[192:195], a[36:39], v[108:111], v[192:195]\n"
      "v_cvt_pk_bf16_f32 v164, v168, v169\n"
      "v_cvt_pk_bf16_f32 v165, v170, v171\n"
      "v_mfma_f32_16x16x32_bf16 v[192:195], a[40:43], v[176:179], v[192:195]\n"
      "v_mfma_f32_16x16x32_bf16 v[192:195], a[44:47], v[172:175], v[192:195]\n"
      "v_mfma_f32_16x16x32_bf16 v[52:55], a[48:51], v[100:103], 0\n"
      "v_mfma_f32_16x16x32_bf16 v[52:55], a[52:55], v[108:111], v[52:55]\n"
      "v_cvt_pk_bf16_f32 v166, v112, v113\n"
      "v_cvt_pk_bf16_f32 v167, v114, v115\n"
      "v_mfma_f32_16x16x32_bf16 v[52:55], a[56:59], v[176:179], v[52:55]\n"
      "v_mfma_f32_16x16x32_bf16 v[52:55], a[60:63], v[172:175], v[52:55]\n"
      "v_mfma_f32_16x16x32_bf16 v[112:115], v[100:103], a[64:67], 0\n"
      "v_mfma_f32_16x16x32_bf16 v[112:115], v[108:111], a[68:71], v[112:115]\n"
      "v_cvt_pk_bf16_f32 v168, v192, v193\n"
      "v_cvt_pk_bf16_f32 v169, v194, v195\n"
      "v_mfma_f32_16x16x32_bf16 v[112:115], v[176:179], a[72:75], v[112:115]\n"
      "v_mfma_f32_16x16x32_bf16 v[112:115], v[172:175], a[76:79], v[112:115]\n"
      "v_mfma_f32_16x16x32_bf16 v[192:195], v[100:103], a[80:83], 0\n"
      "v_mfma_f32_16x16x32_bf16 v[192:195], v[108:111], a[84:87], v[192:195]\n"
      "v_cvt_pk_bf16_f32 v170, v52, v53\n"
      "v_cvt_pk_bf16_f32 v171, v54, v55\n"
      "v_mfma_f32_16x16x32_bf16 v[192:195], v[176:179], a[88:91], v[192:195]\n"
      "v_mfma_f32_16x16x32_bf16 v[192:195], v[172:175], a[92:95], v[192:195]\n"
      "v_mfma_f32_16x16x32_bf16 v[88:91], v[100:103], a[96:99], 0\n"
      "v_mfma_f32_16x16x32_bf16 v[88:91], v[108:111], a[100:103], v[88:91]\n"
      "v_cvt_pk_bf16_f32 v54, v112, v113\n"
      "v_cvt_pk_bf16_f32 v55, v114, v115\n"
      "v_mfma_f32_16x16x32_bf16 v[88:91], v[176:179], a[104:107], v[88:91]\n"
      "v_mfma_f32_16x16x32_bf16 v[88:91], v[172:175], a[108:111], v[88:91]\n"
      "v_mfma_f32_16x16x32_bf16 v[112:115], v[100:103], a[112:115], 0\n"
      "v_mfma_f32_16x16x32_bf16 v[112:115], v[108:111], a[116:119], v[112:115]\n"
      "v_cvt_pk_bf16_f32 v130, v192, v193\n"
      "v_cvt_pk_bf16_f32 v131, v194, v195\n"
      "v_mfma_f32_16x16x32_bf16 v[112:115], v[176:179], a[120:123], v[112:115]\n"
      "v_mfma_f32_16x16x32_bf16 v[112:115], v[172:175], a[124:127], v[112:115]\n"
      "ds_read_b128 v[172:175], v9 offset:0\n"
      "ds_read_b128 v[176:179], v9 offset:2048\n"
      "v_cvt_pk_bf16_f32 v102, v88, v89\n"
      "v_cvt_pk_bf16_f32 v103, v90, v91\n"
      "s_waitcnt lgkmcnt(5)\n"
      "v_mfma_f32_16x16x32_bf16 v[88:91], a[0:3], v[224:227], 0\n"
      "s_waitcnt lgkmcnt(4)\n"
      "v_mfma_f32_16x16x32_bf16 v[88:91], a[4:7], v[32:35], v[88:91]\n"
      "s_waitcnt lgkmcnt(3)\n"
      "v_mfma_f32_16x16x32_bf16 v[88:91], a[8:11], v[160:163], v[88:91]\n"
      "s_waitcnt lgkmcnt(2)\n"
      "v_mfma_f32_16x16x32_bf16 v[88:91], a[12:15], v[28:31], v[88:91]\n"
      "v_cvt_pk_bf16_f32 v194, v112, v113\n"
      "v_cvt_pk_bf16_f32 v195, v114, v115\n"
      "v_mfma_f32_16x16x32_bf16 v[112:115], a[16:19], v[224:227], 0\n"
      "v_mfma_f32_16x16x32_bf16 v[112:115], a[20:23], v[32:35], v[112:115]\n"
      "v_mfma_f32_16x16x32_bf16 v[112:115], a[24:27], v[160:163], v[112:115]\n"
      "v_mfma_f32_16x16x32_bf16 v[112:115], a[28:31], v[28:31], v[112:115]\n"
      "v_mfma_f32_16x16x32_bf16 v[190:193], a[32:35], v[224:227], 0\n"
      "v_mfma_f32_16x16x32_bf16 v[190:193], a[36:39], v[32:35], v[190:193]\n"
      "v_cvt_pk_bf16_f32 v108, v88, v89\n"
      "v_cvt_pk_bf16_f32 v109, v90, v91\n"
      "v_mfma_f32_16x16x32_bf16 v[190:193], a[40:43], v[160:163], v[190:193]\n"
      "v_mfma_f32_16x16x32_bf16 v[190:193], a[44:47], v[28:31], v[190:193]\n"
      "v_mfma_f32_16x16x32_bf16 v[88:91], a[48:51], v[224:227], 0\n"
      "v_mfma_f32_16x16x32_bf16 v[88:91], a[52:55], v[32:35], v[88:91]\n"
      "v_cvt_pk_bf16_f32 v110, v112, v113\n"
      "v_cvt_pk_bf16_f32 v111, v114, v115\n"
      "v_mfma_f32_16x16x32_bf16 v[88:91], a[56:59], v[160:163], v[88:91]\n"
      "v_mfma_f32_16x16x32_bf16 v[88:91], a[60:63], v[28:31], v[88:91]\n"
      "v_add_u32 v9, 64, v9\n"
      "v_mfma_f32_16x16x32_bf16 v[228:231], v[224:227], a[64:67], 0\n"
      "v_cvt_pk_bf16_f32 v112, v190, v191\n"
      "v_cvt_pk_bf16_f32 v113, v192, v193\n"
      "v_mfma_f32_16x16x32_bf16 v[228:231], v[32:35], a[68:71], v[228:231]\n"
      "v_mfma_f32_16x16x32_bf16 v[228:231], v[160:163], a[72:75], v[228:231]\n"
      "v_mfma_f32_16x16x32_bf16 v[228:231], v[28:31], a[76:79], v[228:231]\n"
      "v_mfma_f32_16x16x32_bf16 v[190:193], v[224:227], a[80:83], 0\n"
      "v_cvt_pk_bf16_f32 v114, v88, v89\n"
      "v_cvt_pk_bf16_f32 v115, v90, v91\n"
      "v_mfma_f32_16x16x32_bf16 v[190:193], v[32:35], a[84:87], v[190:193]\n"
      "v_mfma_f32_16x16x32_bf16 v[190:193], v[160:163], a[88:91], v[190:193]\n"
      "v_mfma_f32_16x16x32_bf16 v[190:193], v[28:31], a[92:95], v[190:193]\n"
      "v_mfma_f32_16x16x32_bf16 v[88:91], v[224:227], a[96:99], 0\n"
      "v_mfma_f32_16x16x32_bf16 v[88:91], v[32:35], a[100:103], v[88:91]\n"
      "v_cvt_pk_bf16_f32 v52, v228, v229\n"
      "v_cvt_pk_bf16_f32 v53, v230, v231\n"
      "v_mfma_f32_16x16x32_bf16 v[88:91], v[160:163], a[104:107], v[88:91]\n"
      "v_mfma_f32_16x16x32_bf16 v[88:91], v[28:31], a[108:111], v[88:91]\n"
      "v_mfma_f32_16x16x32_bf16 v[228:231], v[224:227], a[112:115], 0\n"
      "v_mfma_f32_16x16x32_bf16 v[228:231], v[32:35], a[116:119], v[228:231]\n"
      "v_cvt_pk_bf16_f32 v128, v190, v191\n"
      "v_cvt_pk_bf16_f32 v129, v192, v193\n"
      "v_mfma_f32_16x16x32_bf16 v[228:231], v[160:163], a[120:123], v[228:231]\n"
      "v_mfma_f32_16x16x32_bf16 v[228:231], v[28:31], a[124:127], v[228:231]\n"
      "ds_read_b64 v[18:19], v11 offset:0\n"
      "v_mfma_f32_16x16x32_bf16 v[28:31], a[128:131], v[24:27], 0\n"
      "v_cvt_pk_bf16_f32 v100, v88, v89\n"
      "v_cvt_pk_bf16_f32 v101, v90, v91\n"
      "v_mfma_f32_16x16x32_bf16 v[28:31], a[132:135], v[152:155], v[28:31]\n"
      "v_mfma_f32_16x16x32_bf16 v[28:31], a[136:139], v[20:23], v[28:31]\n"
      "v_mfma_f32_16x16x32_bf16 v[28:31], a[140:143], v[156:159], v[28:31]\n"
      "v_mfma_f32_16x16x32_bf16 v[88:91], a[144:147], v[24:27], 0\n"
      "v_cvt_pk_bf16_f32 v192, v228, v229\n"
      "v_cvt_pk_bf16_f32 v193, v230, v231\n"
      "v_mfma_f32_16x16x32_bf16 v[88:91], a[148:151], v[152:155], v[88:91]\n"
      "v_mfma_f32_16x16x32_bf16 v[88:91], a[152:155], v[20:23], v[88:91]\n"
      "v_mfma_f32_16x16x32_bf16 v[88:91], a[156:159], v[156:159], v[88:91]\n"
      "v_mfma_f32_16x16x32_bf16 v[228:231], a[160:163], v[24:27], 0\n"
      "v_mfma_f32_16x16x32_bf16 v[228:231], a[164:167], v[152:155], v[228:231]\n"
      "v_cvt_pk_bf16_f32 v160, v28, v29\n"
      "v_cvt_pk_bf16_f32 v161, v30, v31\n"
      "v_mfma_f32_16x16x32_bf16 v[228:231], a[168:171], v[20:23], v[228:231]\n"
      "v_mfma_f32_16x16x32_bf16 v[228:231], a[172:175], v[156:159], v[228:231]\n"
      "v_mfma_f32_16x16x32_bf16 v[32:35], a[176:179], v[24:27], 0\n"
      "v_mfma_f32_16x16x32_bf16 v[32:35], a[180:183], v[152:155], v[32:35]\n"
      "v_cvt_pk_bf16_f32 v162, v88, v89\n"
      "v_cvt_pk_bf16_f32 v163, v90, v91\n"
      "v_mfma_f32_16x16x32_bf16 v[32:35], a[184:187], v[20:23], v[32:35]\n"
      "v_mfma_f32_16x16x32_bf16 v[32:35], a[188:191], v[156:159], v[32:35]\n"
      "s_waitcnt lgkmcnt(2)\n"
      "v_mfma_f32_16x16x32_bf16 v[28:31], v[172:175], v[160:163], 0\n"
      "v_cvt_pk_bf16_f32 v88, v228, v229\n"
      "v_cvt_pk_bf16_f32 v89, v230, v231\n"
      "s_nop 3\n"
      "v_cvt_pk_bf16_f32 v90, v32, v33\n"
      "v_cvt_pk_bf16_f32 v91, v34, v35\n"
      "s_waitcnt lgkmcnt(1)\n"
      "s_nop 0\n"
      "v_mfma_f32_16x16x32_bf16 v[28:31], v[176:179], v[88:91], v[28:31]\n"
      "v_mfma_f32_16x16x32_bf16 v[228:231], v[108:111], v[160:163], 0\n"
      "v_mfma_f32_16x16x32_bf16 v[228:231], v[112:115], v[88:91], v[228:231]\n"
      "v_mfma_f32_16x16x32_bf16 v[32:35], v[164:167], v[160:163], 0\n"
      "v_mfma_f32_16x16x32_bf16 v[32:35], v[168:171], v[88:91], v[32:35]\n"
      "v_mfma_f32_16x16x32_bf16 v[224:227], v[40:43], v[160:163], 0\n"
      "v_mfma_f32_16x16x32_bf16 v[224:227], v[44:47], v[88:91], v[224:227]\n"
      "v_mfma_f32_16x16x32_bf16 v[200:203], v[92:95], v[160:163], 0\n"
      "v_mfma_f32_16x16x32_bf16 v[200:203], v[96:99], v[88:91], v[200:203]\n"
      "v_max_f32 v190, v28, v29\n"
      "v_max3_f32 v190, v190, v30, v31\n"
      "v_max3_f32 v190, v190, v228, v229\n"
      "v_max3_f32 v190, v190, v230, v231\n"
      "v_max3_f32 v190, v190, v32, v33\n"
      "v_mfma_f32_16x16x32_bf16 v[180:183], v[216:219], v[160:163], 0\n"
      "v_max3_f32 v190, v190, v34, v35\n"
      "v_max3_f32 v190, v190, v224, v225\n"
      "v_max3_f32 v190, v190, v226, v227\n"
      "v_max3_f32 v190, v190, v200, v201\n"
      "v_max3_f32 v190, v190, v202, v203\n"
      "v_mfma_f32_16x16x32_bf16 v[180:183], v[220:223], v[88:91], v[180:183]\n"
      "v_mfma_f32_16x16x32_bf16 v[208:211], v[244:247], v[160:163], 0\n"
      "v_mfma_f32_16x16x32_bf16 v[208:211], v[248:251], v[88:91], v[208:211]\n"
      "v_mfma_f32_16x16x32_bf16 v[184:187], a[128:131], v[252:255], 0\n"
      "v_mfma_f32_16x16x32_bf16 v[184:187], a[132:135], v[48:51], v[184:187]\n"
      "v_mfma_f32_16x16x32_bf16 v[184:187], a[136:139], v[124:127], v[184:187]\n"
      "v_mfma_f32_16x16x32_bf16 v[184:187], a[140:143], v[116:119], v[184:187]\n"
      "v_mfma_f32_16x16x32_bf16 v[88:91], a[144:147], v[252:255], 0\n"
      "v_mfma_f32_16x16x32_bf16 v[88:91], a[148:151], v[48:51], v[88:91]\n"
      "v_max3_f32 v190, v190, v180, v181\n"
      "v_max3_f32 v190, v190, v182, v183\n"
      "v_max3_f32 v190, v190, v208, v209\n"
      "v_max3_f32 v190, v190, v210, v211\n"
      "v_mov_b32 v162, v190\n"
      "v_mfma_f32_16x16x32_bf16 v[88:91], a[152:155], v[124:127], v[88:91]\n"
      "v_cvt_pk_bf16_f32 v160, v184, v185\n"
      "v_permlane16_swap_b32 v190, v162\n"
      "v_max_f32 v190, v190, v162\n"
      "v_mov_b32 v162, v190\n"
      "v_cvt_pk_bf16_f32 v161, v186, v187\n"
      "v_mfma_f32_16x16x32_bf16 v[88:91], a[156:159], v[116:119], v[88:91]\n"
      "v_permlane32_swap_b32 v190, v162\n"
      "v_max_f32 v190, v190, v162\n"
      "v_sub_f32 v28, v28, v190\n"
      "v_sub_f32 v29, v29, v190\n"
      "v_sub_f32 v30, v30, v190\n"
      "v_sub_f32 v31, v31, v190\n"
      "v_exp_f32 v28, v28\n"
      "v_mfma_f32_16x16x32_bf16 v[184:187], a[160:163], v[252:255], 0\n"
      "v_exp_f32 v29, v29\n"
      "v_exp_f32 v30, v30\n"
      "v_exp_f32 v31, v31\n"
      "v_cvt_pk_bf16_f32 v162, v28, v29\n"
      "v_cvt_pk_bf16_f32 v163, v30, v31\n"
      "v_mfma_f32_16x16x32_bf16 v[184:187], a[164:167], v[48:51], v[184:187]\n"
      "v_sub_f32 v228, v228, v190\n"
      "v_sub_f32 v229, v229, v190\n"
      "v_sub_f32 v230, v230, v190\n"
      "v_sub_f32 v231, v231, v190\n"
      "v_sub_f32 v32, v32, v190\n"
      "v_sub_f32 v33, v33, v190\n"
      "v_sub_f32 v34, v34, v190\n"
      "v_sub_f32 v35, v35, v190\n"
      "v_mfma_f32_16x16x32_bf16 v[184:187], a[168:171], v[124:127], v[184:187]\n"
      "v_sub_f32 v224, v224, v190\n"
      "v_sub_f32 v225, v225, v190\n"
      "v_sub_f32 v226, v226, v190\n"
      "v_sub_f32 v227, v227, v190\n"
      "v_sub_f32 v200, v200, v190\n"
      "v_sub_f32 v201, v201, v190\n"
      "v_sub_f32 v202, v202, v190\n"
      "v_sub_f32 v203, v203, v190\n"
      "v_mfma_f32_16x16x32_bf16 v[184:187], a[172:175], v[116:119], v[184:187]\n"
      "v_add_f32 v188, v28, v29\n"
      "v_add_f32 v189, v30, v31\n"
      "v_exp_f32 v228, v228\n"
      "v_exp_f32 v229, v229\n"
      "v_exp_f32 v230, v230\n"
      "v_cvt_pk_bf16_f32 v28, v228, v229\n"
      "v_mfma_f32_16x16x32_bf16 v[84:87], a[176:179], v[252:255], 0\n"
      "v_exp_f32 v231, v231\n"
      "v_exp_f32 v32, v32\n"
      "v_cvt_pk_bf16_f32 v29, v230, v231\n"
      "v_exp_f32 v33, v33\n"
      "v_mfma_f32_16x16x32_bf16 v[84:87], a[180:183], v[48:51], v[84:87]\n"
      "v_exp_f32 v34, v34\n"
      "v_exp_f32 v35, v35\n"
      "v_cvt_pk_bf16_f32 v30, v32, v33\n"
      "v_cvt_pk_bf16_f32 v31, v34, v35\n"
      "v_exp_f32 v224, v224\n"
      "v_sub_f32 v180, v180, v190\n"
      "v_mfma_f32_16x16x32_bf16 v[84:87], a[184:187], v[124:127], v[84:87]\n"
      "v_exp_f32 v225, v225\n"
      "v_exp_f32 v226, v226\n"
      "v_exp_f32 v227, v227\n"
      "v_cvt_pk_bf16_f32 v32, v224, v225\n"
      "v_cvt_pk_bf16_f32 v33, v226, v227\n"
      "v_mfma_f32_16x16x32_bf16 v[84:87], a[188:191], v[116:119], v[84:87]\n"
      "v_exp_f32 v200, v200\n"
      "v_exp_f32 v201, v201\n"
      "v_exp_f32 v202, v202\n"
      "v_exp_f32 v203, v203\n"
      "s_waitcnt lgkmcnt(0)\n"
      "v_mfma_f32_16x16x4_4b_bf16 v[224:239], v[162:163], v[18:19], 0 cbsz:2 abid:0\n"
      "v_cvt_pk_bf16_f32 v34, v200, v201\n"
      "v_cvt_pk_bf16_f32 v35, v202, v203\n"
      "v_sub_f32 v181, v181, v190\n"
      "v_sub_f32 v182, v182, v190\n"
      "v_sub_f32 v183, v183, v190\n"
      "v_sub_f32 v208, v208, v190\n"
      "v_sub_f32 v209, v209, v190\n"
      "v_sub_f32 v210, v210, v190\n"
      "v_sub_f32 v211, v211, v190\n"
      "v_exp_f32 v180, v180\n"
      "v_exp_f32 v181, v181\n"
      "v_exp_f32 v182, v182\n"
      "v_exp_f32 v183, v183\n"
      "v_cvt_pk_bf16_f32 v200, v180, v181\n"
      "v_cvt_pk_bf16_f32 v201, v182, v183\n"
      "v_exp_f32 v208, v208\n"
      "v_exp_f32 v209, v209\n"
      "v_smfmac_f32_16x16x64_bf16 v[188:191], v[12:15], v[28:35], v5\n"
      "v_exp_f32 v210, v210\n"
      "v_exp_f32 v211, v211\n"
      "v_mfma_f32_16x16x32_bf16 v[224:227], v[28:31], v[52:55], v[224:227]\n"
      "v_cvt_pk_bf16_f32 v202, v208, v209\n"
      "v_cvt_pk_bf16_f32 v203, v210, v211\n"
      "v_cvt_pk_bf16_f32 v162, v88, v89\n"
      "v_cvt_pk_bf16_f32 v163, v90, v91\n"
      "v_mfma_f32_16x16x32_bf16 v[224:227], v[32:35], v[56:59], v[224:227]\n"
      "v_cvt_pk_bf16_f32 v88, v184, v185\n"
      "v_cvt_pk_bf16_f32 v89, v186, v187\n"
      "v_cvt_pk_bf16_f32 v90, v84, v85\n"
      "v_mfma_f32_16x16x32_bf16 v[224:227], v[200:203], v[120:123], v[224:227]\n"
      "v_cvt_pk_bf16_f32 v91, v86, v87\n"
      "v_smfmac_f32_16x16x32_bf16 v[188:191], v[12:13], v[200:203], v5\n"
      "v_mfma_f32_16x16x32_bf16 v[228:231], v[28:31], v[128:131], v[228:231]\n"
      "v_mfma_f32_16x16x32_bf16 v[228:231], v[32:35], v[132:135], v[228:231]\n"
      "v_mfma_f32_16x16x32_bf16 v[228:231], v[200:203], v[68:71], v[228:231]\n"
      "v_mfma_f32_16x16x32_bf16 v[232:235], v[28:31], v[100:103], v[232:235]\n"
      "v_mfma_f32_16x16x32_bf16 v[232:235], v[32:35], v[104:107], v[232:235]\n"
      "v_mfma_f32_16x16x32_bf16 v[232:235], v[200:203], v[36:39], v[232:235]\n"
      "v_mfma_f32_16x16x32_bf16 v[236:239], v[28:31], v[192:195], v[236:239]\n"
      "v_mfma_f32_16x16x32_bf16 v[236:239], v[32:35], v[196:199], v[236:239]\n"
      "v_add_f32 v188, v188, v189\n"
      "v_rcp_f32 v189, v188\n"
      "v_mfma_f32_16x16x32_bf16 v[236:239], v[200:203], v[240:243], v[236:239]\n"
      "ds_bpermute_b32 v190, v4, v189\n"
      "s_waitcnt lgkmcnt(0)\n"
      "v_mul_f32_dpp v224, v190, v224 row_newbcast:0 row_mask:0xf bank_mask:0xf\n"
      "v_mul_f32_dpp v225, v190, v225 row_newbcast:4 row_mask:0xf bank_mask:0xf\n"
      "v_mul_f32_dpp v226, v190, v226 row_newbcast:8 row_mask:0xf bank_mask:0xf\n"
      "v_mul_f32_dpp v227, v190, v227 row_newbcast:12 row_mask:0xf bank_mask:0xf\n"
      "v_mul_f32_dpp v228, v190, v228 row_newbcast:0 row_mask:0xf bank_mask:0xf\n"
      "v_mul_f32_dpp v229, v190, v229 row_newbcast:4 row_mask:0xf bank_mask:0xf\n"
      "v_mfma_f32_16x16x32_bf16 v[224:227], v[24:27], a[192:195], v[224:227]\n"
      "v_mul_f32_dpp v230, v190, v230 row_newbcast:8 row_mask:0xf bank_mask:0xf\n"
      "v_mul_f32_dpp v231, v190, v231 row_newbcast:12 row_mask:0xf bank_mask:0xf\n"
      "v_mfma_f32_16x16x32_bf16 v[224:227], v[152:155], a[196:199], v[224:227]\n"
      "v_mul_f32_dpp v232, v190, v232 row_newbcast:0 row_mask:0xf bank_mask:0xf\n"
      "v_mul_f32_dpp v233, v190, v233 row_newbcast:4 row_mask:0xf bank_mask:0xf\n"
      "v_mul_f32_dpp v234, v190, v234 row_newbcast:8 row_mask:0xf bank_mask:0xf\n"
      "v_mfma_f32_16x16x32_bf16 v[224:227], v[20:23], a[200:203], v[224:227]\n"
      "v_mul_f32_dpp v235, v190, v235 row_newbcast:12 row_mask:0xf bank_mask:0xf\n"
      "v_mul_f32_dpp v236, v190, v236 row_newbcast:0 row_mask:0xf bank_mask:0xf\n"
      "v_mul_f32_dpp v237, v190, v237 row_newbcast:4 row_mask:0xf bank_mask:0xf\n"
      "v_mfma_f32_16x16x32_bf16 v[224:227], v[156:159], a[204:207], v[224:227]\n"
      "v_mul_f32_dpp v238, v190, v238 row_newbcast:8 row_mask:0xf bank_mask:0xf\n"
      "v_mul_f32_dpp v239, v190, v239 row_newbcast:12 row_mask:0xf bank_mask:0xf\n"
      "v_mfma_f32_16x16x32_bf16 v[228:231], v[24:27], a[208:211], v[228:231]\n"
      "v_mfma_f32_16x16x32_bf16 v[228:231], v[152:155], a[212:215], v[228:231]\n"
      "v_mfma_f32_16x16x32_bf16 v[228:231], v[20:23], a[216:219], v[228:231]\n"
      "v_mfma_f32_16x16x32_bf16 v[228:231], v[156:159], a[220:223], v[228:231]\n"
      "v_mfma_f32_16x16x32_bf16 v[232:235], v[24:27], a[224:227], v[232:235]\n"
      "v_mfma_f32_16x16x32_bf16 v[232:235], v[152:155], a[228:231], v[232:235]\n"
      "v_mfma_f32_16x16x32_bf16 v[232:235], v[20:23], a[232:235], v[232:235]\n"
      "v_mfma_f32_16x16x32_bf16 v[232:235], v[156:159], a[236:239], v[232:235]\n"
      "v_mfma_f32_16x16x32_bf16 v[236:239], v[24:27], a[240:243], v[236:239]\n"
      "v_mfma_f32_16x16x32_bf16 v[236:239], v[152:155], a[244:247], v[236:239]\n"
      "v_mfma_f32_16x16x32_bf16 v[236:239], v[20:23], a[248:251], v[236:239]\n"
      "v_mfma_f32_16x16x32_bf16 v[236:239], v[156:159], a[252:255], v[236:239]\n"
      "v_cvt_pk_bf16_f32 v224, v224, v228\n"
      "v_cvt_pk_bf16_f32 v228, v225, v229\n"
      "v_pk_max_i16 v224, v224, 0\n"
      "v_pk_max_i16 v228, v228, 0\n"
      "v_mfma_f32_16x16x32_bf16 v[20:23], v[172:175], v[160:163], 0\n"
      "v_cvt_pk_bf16_f32 v226, v226, v230\n"
      "v_cvt_pk_bf16_f32 v230, v227, v231\n"
      "v_pk_max_i16 v226, v226, 0\n"
      "v_cvt_pk_bf16_f32 v225, v232, v236\n"
      "v_mfma_f32_16x16x32_bf16 v[20:23], v[176:179], v[88:91], v[20:23]\n"
      "v_cvt_pk_bf16_f32 v229, v233, v237\n"
      "v_pk_max_i16 v225, v225, 0\n"
      "v_pk_max_i16 v229, v229, 0\n"
      "v_cvt_pk_bf16_f32 v227, v234, v238\n"
      "v_mfma_f32_16x16x32_bf16 v[24:27], v[108:111], v[160:163], 0\n"
      "v_cvt_pk_bf16_f32 v231, v235, v239\n"
      "v_pk_max_i16 v227, v227, 0\n"
      "v_pk_max_i16 v230, v230, 0\n"
      "v_max_f32 v186, v20, v21\n"
      "v_mfma_f32_16x16x32_bf16 v[24:27], v[112:115], v[88:91], v[24:27]\n"
      "v_max3_f32 v186, v186, v22, v23\n"
      "v_pk_max_i16 v231, v231, 0\n"
      "v_mfma_f32_16x16x32_bf16 v[28:31], v[164:167], v[160:163], 0\n"
      "v_mfma_f32_16x16x32_bf16 v[28:31], v[168:171], v[88:91], v[28:31]\n"
      "v_mfma_f32_16x16x32_bf16 v[236:239], v[40:43], v[160:163], 0\n"
      "v_mfma_f32_16x16x32_bf16 v[236:239], v[44:47], v[88:91], v[236:239]\n"
      "v_mfma_f32_16x16x32_bf16 v[232:235], v[92:95], v[160:163], 0\n"
      "v_mfma_f32_16x16x32_bf16 v[232:235], v[96:99], v[88:91], v[232:235]\n"
      "v_max3_f32 v186, v186, v24, v25\n"
      "v_max3_f32 v186, v186, v26, v27\n"
      "v_mfma_f32_16x16x32_bf16 v[156:159], v[216:219], v[160:163], 0\n"
      "v_mfma_f32_16x16x32_bf16 v[156:159], v[220:223], v[88:91], v[156:159]\n"
      "v_max3_f32 v186, v186, v28, v29\n"
      "v_max3_f32 v186, v186, v30, v31\n"
      "v_max3_f32 v186, v186, v236, v237\n"
      "v_max3_f32 v186, v186, v238, v239\n"
      "v_max3_f32 v186, v186, v232, v233\n"
      "v_max3_f32 v186, v186, v234, v235\n"
      "v_mfma_f32_16x16x32_bf16 v[152:155], v[244:247], v[160:163], 0\n"
      "v_mfma_f32_16x16x32_bf16 v[152:155], v[248:251], v[88:91], v[152:155]\n"
      "v_max3_f32 v186, v186, v156, v157\n"
      "v_max3_f32 v186, v186, v158, v159\n"
      "v_mfma_f32_16x16x32_bf16 v[160:163], a[128:131], v[148:151], 0\n"
      "v_mfma_f32_16x16x32_bf16 v[160:163], a[132:135], v[136:139], v[160:163]\n"
      "s_add_u32 s56, s48, 20480\n"
      "buffer_store_dwordx2 v[224:225], v2, s[44:47], s56 offen offset:0 nt sc1\n"
      "buffer_store_dwordx2 v[228:229], v2, s[44:47], s56 offen offset:256 nt sc1\n"
      "v_mfma_f32_16x16x32_bf16 v[160:163], a[136:139], v[140:143], v[160:163]\n"
      "v_max3_f32 v186, v186, v152, v153\n"
      "v_max3_f32 v186, v186, v154, v155\n"
      "v_mov_b32 v88, v186\n"
      "buffer_store_dwordx2 v[226:227], v2, s[44:47], s56 offen offset:512 nt sc1\n"
      "v_mfma_f32_16x16x32_bf16 v[160:163], a[140:143], v[144:147], v[160:163]\n"
      "v_permlane16_swap_b32 v186, v88\n"
      "v_max_f32 v186, v186, v88\n"
      "v_mfma_f32_16x16x32_bf16 v[32:35], a[144:147], v[148:151], 0\n"
      "v_mov_b32 v88, v186\n"
      "buffer_store_dwordx2 v[230:231], v2, s[44:47], s56 offen offset:768 nt sc1\n"
      "v_mfma_f32_16x16x32_bf16 v[32:35], a[148:151], v[136:139], v[32:35]\n"
      "v_permlane32_swap_b32 v186, v88\n"
      "v_max_f32 v186, v186, v88\n"
      "v_sub_f32 v20, v20, v186\n"
      "v_sub_f32 v21, v21, v186\n"
      "v_sub_f32 v22, v22, v186\n"
      "v_sub_f32 v23, v23, v186\n"
      "v_sub_f32 v24, v24, v186\n"
      "v_mfma_f32_16x16x32_bf16 v[32:35], a[152:155], v[140:143], v[32:35]\n"
      "v_exp_f32 v20, v20\n"
      "v_exp_f32 v21, v21\n"
      "v_exp_f32 v22, v22\n"
      "v_cvt_pk_bf16_f32 v190, v20, v21\n"
      "v_mfma_f32_16x16x32_bf16 v[32:35], a[156:159], v[144:147], v[32:35]\n"
      "v_exp_f32 v23, v23\n"
      "v_sub_f32 v25, v25, v186\n"
      "v_cvt_pk_bf16_f32 v191, v22, v23\n"
      "v_sub_f32 v26, v26, v186\n"
      "v_sub_f32 v27, v27, v186\n"
      "v_sub_f32 v28, v28, v186\n"
      "v_mfma_f32_16x16x32_bf16 v[88:91], a[160:163], v[148:151], 0\n"
      "v_sub_f32 v29, v29, v186\n"
      "v_sub_f32 v30, v30, v186\n"
      "v_sub_f32 v31, v31, v186\n"
      "v_sub_f32 v236, v236, v186\n"
      "v_sub_f32 v237, v237, v186\n"
      "v_sub_f32 v238, v238, v186\n"
      "v_sub_f32 v239, v239, v186\n"
      "v_mfma_f32_16x16x32_bf16 v[88:91], a[164:167], v[136:139], v[88:91]\n"
      "v_sub_f32 v232, v232, v186\n"
      "v_sub_f32 v233, v233, v186\n"
      "v_sub_f32 v234, v234, v186\n"
      "v_sub_f32 v235, v235, v186\n"
      "v_add_f32 v184, v20, v21\n"
      "v_add_f32 v185, v22, v23\n"
      "v_mfma_f32_16x16x32_bf16 v[88:91], a[168:171], v[140:143], v[88:91]\n"
      "v_exp_f32 v24, v24\n"
      "v_exp_f32 v25, v25\n"
      "v_exp_f32 v26, v26\n"
      "v_exp_f32 v27, v27\n"
      "v_mfma_f32_16x16x32_bf16 v[88:91], a[172:175], v[144:147], v[88:91]\n"
      "v_cvt_pk_bf16_f32 v20, v24, v25\n"
      "v_cvt_pk_bf16_f32 v21, v26, v27\n"
      "v_exp_f32 v28, v28\n"
      "v_exp_f32 v29, v29\n"
      "v_mfma_f32_16x16x32_bf16 v[208:211], a[176:179], v[148:151], 0\n"
      "v_exp_f32 v30, v30\n"
      "v_exp_f32 v31, v31\n"
      "v_cvt_pk_bf16_f32 v22, v28, v29\n"
      "v_cvt_pk_bf16_f32 v23, v30, v31\n"
      "v_exp_f32 v236, v236\n"
      "v_mfma_f32_16x16x32_bf16 v[208:211], a[180:183], v[136:139], v[208:211]\n"
      "v_exp_f32 v237, v237\n"
      "v_exp_f32 v238, v238\n"
      "v_exp_f32 v239, v239\n"
      "v_cvt_pk_bf16_f32 v24, v236, v237\n"
      "v_mfma_f32_16x16x32_bf16 v[208:211], a[184:187], v[140:143], v[208:211]\n"
      "v_cvt_pk_bf16_f32 v25, v238, v239\n"
      "v_exp_f32 v232, v232\n"
      "v_exp_f32 v233, v233\n"
      "v_exp_f32 v234, v234\n"
      "v_mfma_f32_16x16x32_bf16 v[208:211], a[188:191], v[144:147], v[208:211]\n"
      "v_exp_f32 v235, v235\n"
      "v_cvt_pk_bf16_f32 v26, v232, v233\n"
      "v_cvt_pk_bf16_f32 v27, v234, v235\n"
      "v_sub_f32 v156, v156, v186\n"
      "v_sub_f32 v157, v157, v186\n"
      "v_sub_f32 v158, v158, v186\n"
      "v_mfma_f32_16x16x4_4b_bf16 v[224:239], v[190:191], v[18:19], 0 cbsz:2 abid:0\n"
      "v_sub_f32 v159, v159, v186\n"
      "v_sub_f32 v152, v152, v186\n"
      "v_sub_f32 v153, v153, v186\n"
      "v_sub_f32 v154, v154, v186\n"
      "v_sub_f32 v155, v155, v186\n"
      "v_exp_f32 v156, v156\n"
      "v_exp_f32 v157, v157\n"
      "v_exp_f32 v158, v158\n"
      "v_exp_f32 v159, v159\n"
      "v_cvt_pk_bf16_f32 v28, v156, v157\n"
      "v_cvt_pk_bf16_f32 v29, v158, v159\n"
      "v_exp_f32 v152, v152\n"
      "v_exp_f32 v153, v153\n"
      "v_cvt_pk_bf16_f32 v156, v160, v161\n"
      "v_smfmac_f32_16x16x64_bf16 v[184:187], v[12:15], v[20:27], v5\n"
      "v_exp_f32 v154, v154\n"
      "v_exp_f32 v155, v155\n"
      "v_mfma_f32_16x16x32_bf16 v[224:227], v[20:23], v[52:55], v[224:227]\n"
      "v_cvt_pk_bf16_f32 v30, v152, v153\n"
      "v_cvt_pk_bf16_f32 v31, v154, v155\n"
      "v_cvt_pk_bf16_f32 v157, v162, v163\n"
      "v_mfma_f32_16x16x32_bf16 v[224:227], v[24:27], v[56:59], v[224:227]\n"
      "v_cvt_pk_bf16_f32 v158, v32, v33\n"
      "v_cvt_pk_bf16_f32 v159, v34, v35\n"
      "v_cvt_pk_bf16_f32 v32, v88, v89\n"
      "v_mfma_f32_16x16x32_bf16 v[224:227], v[28:31], v[120:123], v[224:227]\n"
      "v_cvt_pk_bf16_f32 v33, v90, v91\n"
      "v_cvt_pk_bf16_f32 v34, v208, v209\n"
      "v_cvt_pk_bf16_f32 v35, v210, v211\n"
      "v_smfmac_f32_16x16x32_bf16 v[184:187], v[12:13], v[28:31], v5\n"
      "v_mfma_f32_16x16x32_bf16 v[228:231], v[20:23], v[128:131], v[228:231]\n"
      "v_mfma_f32_16x16x32_bf16 v[228:231], v[24:27], v[132:135], v[228:231]\n"
      "v_mfma_f32_16x16x32_bf16 v[228:231], v[28:31], v[68:71], v[228:231]\n"
      "v_mfma_f32_16x16x32_bf16 v[232:235], v[20:23], v[100:103], v[232:235]\n"
      "v_mfma_f32_16x16x32_bf16 v[232:235], v[24:27], v[104:107], v[232:235]\n"
      "v_mfma_f32_16x16x32_bf16 v[232:235], v[28:31], v[36:39], v[232:235]\n"
      "v_mfma_f32_16x16x32_bf16 v[236:239], v[20:23], v[192:195], v[236:239]\n"
      "v_mfma_f32_16x16x32_bf16 v[236:239], v[24:27], v[196:199], v[236:239]\n"
      "v_add_f32 v184, v184, v185\n"
      "v_rcp_f32 v185, v184\n"
      "v_mfma_f32_16x16x32_bf16 v[236:239], v[28:31], v[240:243], v[236:239]\n"
      "ds_bpermute_b32 v186, v4, v185\n"
      "s_waitcnt lgkmcnt(0)\n"
      "v_mul_f32_dpp v224, v186, v224 row_newbcast:0 row_mask:0xf bank_mask:0xf\n"
      "v_mul_f32_dpp v225, v186, v225 row_newbcast:4 row_mask:0xf bank_mask:0xf\n"
      "v_mul_f32_dpp v226, v186, v226 row_newbcast:8 row_mask:0xf bank_mask:0xf\n"
      "v_mul_f32_dpp v227, v186, v227 row_newbcast:12 row_mask:0xf bank_mask:0xf\n"
      "v_mul_f32_dpp v228, v186, v228 row_newbcast:0 row_mask:0xf bank_mask:0xf\n"
      "v_mul_f32_dpp v229, v186, v229 row_newbcast:4 row_mask:0xf bank_mask:0xf\n"
      "v_mfma_f32_16x16x32_bf16 v[224:227], v[252:255], a[192:195], v[224:227]\n"
      "v_mul_f32_dpp v230, v186, v230 row_newbcast:8 row_mask:0xf bank_mask:0xf\n"
      "v_mul_f32_dpp v231, v186, v231 row_newbcast:12 row_mask:0xf bank_mask:0xf\n"
      "v_mfma_f32_16x16x32_bf16 v[224:227], v[48:51], a[196:199], v[224:227]\n"
      "v_mul_f32_dpp v232, v186, v232 row_newbcast:0 row_mask:0xf bank_mask:0xf\n"
      "v_mul_f32_dpp v233, v186, v233 row_newbcast:4 row_mask:0xf bank_mask:0xf\n"
      "v_mul_f32_dpp v234, v186, v234 row_newbcast:8 row_mask:0xf bank_mask:0xf\n"
      "v_mfma_f32_16x16x32_bf16 v[224:227], v[124:127], a[200:203], v[224:227]\n"
      "v_mul_f32_dpp v235, v186, v235 row_newbcast:12 row_mask:0xf bank_mask:0xf\n"
      "v_mul_f32_dpp v236, v186, v236 row_newbcast:0 row_mask:0xf bank_mask:0xf\n"
      "v_mul_f32_dpp v237, v186, v237 row_newbcast:4 row_mask:0xf bank_mask:0xf\n"
      "v_mul_f32_dpp v238, v186, v238 row_newbcast:8 row_mask:0xf bank_mask:0xf\n"
      "v_mfma_f32_16x16x32_bf16 v[224:227], v[116:119], a[204:207], v[224:227]\n"
      "v_mul_f32_dpp v239, v186, v239 row_newbcast:12 row_mask:0xf bank_mask:0xf\n"
      "v_mfma_f32_16x16x32_bf16 v[228:231], v[252:255], a[208:211], v[228:231]\n"
      "v_mfma_f32_16x16x32_bf16 v[228:231], v[48:51], a[212:215], v[228:231]\n"
      "v_mfma_f32_16x16x32_bf16 v[228:231], v[124:127], a[216:219], v[228:231]\n"
      "v_mfma_f32_16x16x32_bf16 v[228:231], v[116:119], a[220:223], v[228:231]\n"
      "v_mfma_f32_16x16x32_bf16 v[232:235], v[252:255], a[224:227], v[232:235]\n"
      "v_mfma_f32_16x16x32_bf16 v[232:235], v[48:51], a[228:231], v[232:235]\n"
      "v_mfma_f32_16x16x32_bf16 v[232:235], v[124:127], a[232:235], v[232:235]\n"
      "v_mfma_f32_16x16x32_bf16 v[232:235], v[116:119], a[236:239], v[232:235]\n"
      "v_mfma_f32_16x16x32_bf16 v[236:239], v[252:255], a[240:243], v[236:239]\n"
      "v_mfma_f32_16x16x32_bf16 v[236:239], v[48:51], a[244:247], v[236:239]\n"
      "v_mfma_f32_16x16x32_bf16 v[236:239], v[124:127], a[248:251], v[236:239]\n"
      "v_mfma_f32_16x16x32_bf16 v[236:239], v[116:119], a[252:255], v[236:239]\n"
      "v_cvt_pk_bf16_f32 v224, v224, v228\n"
      "v_cvt_pk_bf16_f32 v228, v225, v229\n"
      "v_pk_max_i16 v224, v224, 0\n"
      "v_pk_max_i16 v228, v228, 0\n"
      "v_cvt_pk_bf16_f32 v226, v226, v230\n"
      "v_mfma_f32_16x16x32_bf16 v[20:23], v[172:175], v[156:159], 0\n"
      "v_cvt_pk_bf16_f32 v230, v227, v231\n"
      "v_pk_max_i16 v226, v226, 0\n"
      "v_cvt_pk_bf16_f32 v225, v232, v236\n"
      "v_cvt_pk_bf16_f32 v229, v233, v237\n"
      "v_mfma_f32_16x16x32_bf16 v[20:23], v[176:179], v[32:35], v[20:23]\n"
      "v_pk_max_i16 v225, v225, 0\n"
      "v_pk_max_i16 v229, v229, 0\n"
      "v_cvt_pk_bf16_f32 v227, v234, v238\n"
      "v_cvt_pk_bf16_f32 v231, v235, v239\n"
      "v_pk_max_i16 v227, v227, 0\n"
      "v_mfma_f32_16x16x32_bf16 v[88:91], v[108:111], v[156:159], 0\n"
      "v_pk_max_i16 v230, v230, 0\n"
      "v_pk_max_i16 v231, v231, 0\n"
      "v_max_f32 v190, v20, v21\n"
      "v_max3_f32 v190, v190, v22, v23\n"
      "v_mfma_f32_16x16x32_bf16 v[88:91], v[112:115], v[32:35], v[88:91]\n"
      "v_mfma_f32_16x16x32_bf16 v[24:27], v[164:167], v[156:159], 0\n"
      "v_mfma_f32_16x16x32_bf16 v[24:27], v[168:171], v[32:35], v[24:27]\n"
      "v_mfma_f32_16x16x32_bf16 v[236:239], v[40:43], v[156:159], 0\n"
      "v_mfma_f32_16x16x32_bf16 v[236:239], v[44:47], v[32:35], v[236:239]\n"
      "v_mfma_f32_16x16x32_bf16 v[232:235], v[92:95], v[156:159], 0\n"
      "v_mfma_f32_16x16x32_bf16 v[232:235], v[96:99], v[32:35], v[232:235]\n"
      "v_mfma_f32_16x16x32_bf16 v[160:163], v[216:219], v[156:159], 0\n"
      "v_mfma_f32_16x16x32_bf16 v[160:163], v[220:223], v[32:35], v[160:163]\n"
      "v_max3_f32 v190, v190, v88, v89\n"
      "v_max3_f32 v190, v190, v90, v91\n"
      "v_max3_f32 v190, v190, v24, v25\n"
      "v_max3_f32 v190, v190, v26, v27\n"
      "v_max3_f32 v190, v190, v236, v237\n"
      "v_max3_f32 v190, v190, v238, v239\n"
      "v_max3_f32 v190, v190, v232, v233\n"
      "v_mfma_f32_16x16x32_bf16 v[152:155], v[244:247], v[156:159], 0\n"
      "v_max3_f32 v190, v190, v234, v235\n"
      "v_max3_f32 v190, v190, v160, v161\n"
      "v_max3_f32 v190, v190, v162, v163\n"
      "v_mfma_f32_16x16x32_bf16 v[152:155], v[248:251], v[32:35], v[152:155]\n"
      "ds_read_b128 v[116:119], v0 offset:8192\n"
      "ds_read_b128 v[124:127], v0 offset:8704\n"
      "ds_read_b128 v[48:51], v0 offset:9216\n"
      "ds_read_b128 v[252:255], v0 offset:9728\n"
      "s_add_u32 s56, s48, 16384\n"
      "buffer_store_dwordx2 v[224:225], v2, s[44:47], s56 offen offset:0 nt sc1\n"
      "buffer_store_dwordx2 v[228:229], v2, s[44:47], s56 offen offset:256 nt sc1\n"
      "buffer_store_dwordx2 v[226:227], v2, s[44:47], s56 offen offset:512 nt sc1\n"
      "v_max3_f32 v190, v190, v152, v153\n"
      "v_max3_f32 v190, v190, v154, v155\n"
      "v_mov_b32 v32, v190\n"
      "buffer_store_dwordx2 v[230:231], v2, s[44:47], s56 offen offset:768 nt sc1\n"
      "s_nop 0\n"
      "v_permlane16_swap_b32 v190, v32\n"
      "v_max_f32 v190, v190, v32\n"
      "v_mov_b32 v32, v190\n"
      "s_nop 1\n"
      "v_permlane32_swap_b32 v190, v32\n"
      "v_max_f32 v190, v190, v32\n"
      "v_sub_f32 v20, v20, v190\n"
      "v_sub_f32 v21, v21, v190\n"
      "v_sub_f32 v22, v22, v190\n"
      "s_waitcnt lgkmcnt(3)\n"
      "v_mfma_f32_16x16x32_bf16 v[32:35], a[128:131], v[116:119], 0\n"
      "v_sub_f32 v23, v23, v190\n"
      "v_sub_f32 v88, v88, v190\n"
      "s_waitcnt lgkmcnt(2)\n"
      "v_mfma_f32_16x16x32_bf16 v[32:35], a[132:135], v[124:127], v[32:35]\n"
      "v_exp_f32 v20, v20\n"
      "s_waitcnt lgkmcnt(1)\n"
      "v_mfma_f32_16x16x32_bf16 v[32:35], a[136:139], v[48:51], v[32:35]\n"
      "v_exp_f32 v21, v21\n"
      "s_waitcnt lgkmcnt(0)\n"
      "v_mfma_f32_16x16x32_bf16 v[32:35], a[140:143], v[252:255], v[32:35]\n"
      "v_exp_f32 v22, v22\n"
      "v_mfma_f32_16x16x32_bf16 v[28:31], a[144:147], v[116:119], 0\n"
      "v_exp_f32 v23, v23\n"
      "v_mfma_f32_16x16x32_bf16 v[28:31], a[148:151], v[124:127], v[28:31]\n"
      "v_cvt_pk_bf16_f32 v156, v20, v21\n"
      "v_cvt_pk_bf16_f32 v157, v22, v23\n"
      "v_mfma_f32_16x16x32_bf16 v[28:31], a[152:155], v[48:51], v[28:31]\n"
      "v_sub_f32 v89, v89, v190\n"
      "v_sub_f32 v90, v90, v190\n"
      "v_sub_f32 v91, v91, v190\n"
      "v_sub_f32 v24, v24, v190\n"
      "v_mfma_f32_16x16x32_bf16 v[28:31], a[156:159], v[252:255], v[28:31]\n"
      "v_sub_f32 v25, v25, v190\n"
      "v_sub_f32 v26, v26, v190\n"
      "v_sub_f32 v27, v27, v190\n"
      "v_sub_f32 v236, v236, v190\n"
      "v_sub_f32 v237, v237, v190\n"
      "v_sub_f32 v238, v238, v190\n"
      "v_mfma_f32_16x16x32_bf16 v[184:187], a[160:163], v[116:119], 0\n"
      "v_sub_f32 v239, v239, v190\n"
      "v_sub_f32 v232, v232, v190\n"
      "v_sub_f32 v233, v233, v190\n"
      "v_sub_f32 v234, v234, v190\n"
      "v_sub_f32 v235, v235, v190\n"
      "v_add_f32 v86, v20, v21\n"
      "v_add_f32 v87, v22, v23\n"
      "v_mfma_f32_16x16x32_bf16 v[184:187], a[164:167], v[124:127], v[184:187]\n"
      "v_exp_f32 v88, v88\n"
      "v_exp_f32 v89, v89\n"
      "v_exp_f32 v90, v90\n"
      "v_mfma_f32_16x16x32_bf16 v[184:187], a[168:171], v[48:51], v[184:187]\n"
      "v_exp_f32 v91, v91\n"
      "v_cvt_pk_bf16_f32 v20, v88, v89\n"
      "v_cvt_pk_bf16_f32 v21, v90, v91\n"
      "v_exp_f32 v24, v24\n"
      "v_mfma_f32_16x16x32_bf16 v[184:187], a[172:175], v[252:255], v[184:187]\n"
      "v_exp_f32 v25, v25\n"
      "v_exp_f32 v26, v26\n"
      "v_exp_f32 v27, v27\n"
      "v_cvt_pk_bf16_f32 v22, v24, v25\n"
      "v_mfma_f32_16x16x32_bf16 v[208:211], a[176:179], v[116:119], 0\n"
      "v_cvt_pk_bf16_f32 v23, v26, v27\n"
      "v_exp_f32 v236, v236\n"
      "v_exp_f32 v237, v237\n"
      "v_exp_f32 v238, v238\n"
      "v_mfma_f32_16x16x32_bf16 v[208:211], a[180:183], v[124:127], v[208:211]\n"
      "v_exp_f32 v239, v239\n"
      "v_cvt_pk_bf16_f32 v24, v236, v237\n"
      "v_cvt_pk_bf16_f32 v25, v238, v239\n"
      "v_exp_f32 v232, v232\n"
      "v_mfma_f32_16x16x32_bf16 v[208:211], a[184:187], v[48:51], v[208:211]\n"
      "v_exp_f32 v233, v233\n"
      "v_exp_f32 v234, v234\n"
      "v_exp_f32 v235, v235\n"
      "v_cvt_pk_bf16_f32 v26, v232, v233\n"
      "v_mfma_f32_16x16x32_bf16 v[208:211], a[188:191], v[252:255], v[208:211]\n"
      "v_cvt_pk_bf16_f32 v27, v234, v235\n"
      "v_sub_f32 v160, v160, v190\n"
      "v_sub_f32 v161, v161, v190\n"
      "v_sub_f32 v162, v162, v190\n"
      "v_sub_f32 v163, v163, v190\n"
      "v_sub_f32 v152, v152, v190\n"
      "v_mfma_f32_16x16x4_4b_bf16 v[224:239], v[156:157], v[18:19], 0 cbsz:2 abid:0\n"
      "s_add_u32 m0, s53, 8192\n"
      "s_add_u32 s54, s49, 8192\n"
      "buffer_load_dwordx4 v1, s[40:43], s54 offen offset:0 lds\n"
      "v_sub_f32 v153, v153, v190\n"
      "v_sub_f32 v154, v154, v190\n"
      "v_sub_f32 v155, v155, v190\n"
      "v_exp_f32 v160, v160\n"
      "v_exp_f32 v161, v161\n"
      "v_exp_f32 v162, v162\n"
      "v_exp_f32 v163, v163\n"
      "v_cvt_pk_bf16_f32 v188, v160, v161\n"
      "v_cvt_pk_bf16_f32 v189, v162, v163\n"
      "v_exp_f32 v152, v152\n"
      "v_exp_f32 v153, v153\n"
      "v_exp_f32 v154, v154\n"
      "v_smfmac_f32_16x16x64_bf16 v[86:89], v[12:15], v[20:27], v5\n"
      "v_exp_f32 v155, v155\n"
      "v_cvt_pk_bf16_f32 v190, v152, v153\n"
      "v_mfma_f32_16x16x32_bf16 v[224:227], v[20:23], v[52:55], v[224:227]\n"
      "buffer_load_dwordx4 v1, s[40:43], s54 offen offset:2048 lds\n"
      "s_add_u32 m0, s53, 9088\n"
      "v_cvt_pk_bf16_f32 v191, v154, v155\n"
      "v_cvt_pk_bf16_f32 v156, v32, v33\n"
      "v_cvt_pk_bf16_f32 v157, v34, v35\n"
      "v_mfma_f32_16x16x32_bf16 v[224:227], v[24:27], v[56:59], v[224:227]\n"
      "v_cvt_pk_bf16_f32 v158, v28, v29\n"
      "v_cvt_pk_bf16_f32 v159, v30, v31\n"
      "v_cvt_pk_bf16_f32 v32, v184, v185\n"
      "v_mfma_f32_16x16x32_bf16 v[224:227], v[188:191], v[120:123], v[224:227]\n"
      "buffer_load_dwordx4 v1, s[40:43], s54 offen offset:128 lds\n"
      "v_cvt_pk_bf16_f32 v33, v186, v187\n"
      "v_cvt_pk_bf16_f32 v34, v208, v209\n"
      "v_cvt_pk_bf16_f32 v35, v210, v211\n"
      "v_smfmac_f32_16x16x32_bf16 v[86:89], v[12:13], v[188:191], v5\n"
      "v_mfma_f32_16x16x32_bf16 v[228:231], v[20:23], v[128:131], v[228:231]\n"
      "buffer_load_dwordx4 v1, s[40:43], s54 offen offset:2176 lds\n"
      "v_mfma_f32_16x16x32_bf16 v[228:231], v[24:27], v[132:135], v[228:231]\n"
      "v_mfma_f32_16x16x32_bf16 v[228:231], v[188:191], v[68:71], v[228:231]\n"
      "v_mfma_f32_16x16x32_bf16 v[232:235], v[20:23], v[100:103], v[232:235]\n"
      "v_mfma_f32_16x16x32_bf16 v[232:235], v[24:27], v[104:107], v[232:235]\n"
      "v_mfma_f32_16x16x32_bf16 v[232:235], v[188:191], v[36:39], v[232:235]\n"
      "v_mfma_f32_16x16x32_bf16 v[236:239], v[20:23], v[192:195], v[236:239]\n"
      "v_add_f32 v86, v86, v87\n"
      "v_rcp_f32 v87, v86\n"
      "v_mfma_f32_16x16x32_bf16 v[236:239], v[24:27], v[196:199], v[236:239]\n"
      "ds_bpermute_b32 v88, v4, v87\n"
      "v_mfma_f32_16x16x32_bf16 v[236:239], v[188:191], v[240:243], v[236:239]\n"
      "s_waitcnt lgkmcnt(0)\n"
      "v_mul_f32_dpp v224, v88, v224 row_newbcast:0 row_mask:0xf bank_mask:0xf\n"
      "v_mul_f32_dpp v225, v88, v225 row_newbcast:4 row_mask:0xf bank_mask:0xf\n"
      "v_mul_f32_dpp v226, v88, v226 row_newbcast:8 row_mask:0xf bank_mask:0xf\n"
      "v_mul_f32_dpp v227, v88, v227 row_newbcast:12 row_mask:0xf bank_mask:0xf\n"
      "v_mul_f32_dpp v228, v88, v228 row_newbcast:0 row_mask:0xf bank_mask:0xf\n"
      "v_mul_f32_dpp v229, v88, v229 row_newbcast:4 row_mask:0xf bank_mask:0xf\n"
      "v_mfma_f32_16x16x32_bf16 v[224:227], v[148:151], a[192:195], v[224:227]\n"
      "v_mul_f32_dpp v230, v88, v230 row_newbcast:8 row_mask:0xf bank_mask:0xf\n"
      "v_mul_f32_dpp v231, v88, v231 row_newbcast:12 row_mask:0xf bank_mask:0xf\n"
      "v_mfma_f32_16x16x32_bf16 v[224:227], v[136:139], a[196:199], v[224:227]\n"
      "v_mul_f32_dpp v232, v88, v232 row_newbcast:0 row_mask:0xf bank_mask:0xf\n"
      "v_mul_f32_dpp v233, v88, v233 row_newbcast:4 row_mask:0xf bank_mask:0xf\n"
      "v_mul_f32_dpp v234, v88, v234 row_newbcast:8 row_mask:0xf bank_mask:0xf\n"
      "v_mfma_f32_16x16x32_bf16 v[224:227], v[140:143], a[200:203], v[224:227]\n"
      "v_mul_f32_dpp v235, v88, v235 row_newbcast:12 row_mask:0xf bank_mask:0xf\n"
      "v_mul_f32_dpp v236, v88, v236 row_newbcast:0 row_mask:0xf bank_mask:0xf\n"
      "v_mul_f32_dpp v237, v88, v237 row_newbcast:4 row_mask:0xf bank_mask:0xf\n"
      "v_mul_f32_dpp v238, v88, v238 row_newbcast:8 row_mask:0xf bank_mask:0xf\n"
      "v_mfma_f32_16x16x32_bf16 v[224:227], v[144:147], a[204:207], v[224:227]\n"
      "v_mul_f32_dpp v239, v88, v239 row_newbcast:12 row_mask:0xf bank_mask:0xf\n"
      "v_mfma_f32_16x16x32_bf16 v[228:231], v[148:151], a[208:211], v[228:231]\n"
      "v_mfma_f32_16x16x32_bf16 v[228:231], v[136:139], a[212:215], v[228:231]\n"
      "v_mfma_f32_16x16x32_bf16 v[228:231], v[140:143], a[216:219], v[228:231]\n"
      "v_mfma_f32_16x16x32_bf16 v[228:231], v[144:147], a[220:223], v[228:231]\n"
      "v_mfma_f32_16x16x32_bf16 v[232:235], v[148:151], a[224:227], v[232:235]\n"
      "v_mfma_f32_16x16x32_bf16 v[232:235], v[136:139], a[228:231], v[232:235]\n"
      "v_mfma_f32_16x16x32_bf16 v[232:235], v[140:143], a[232:235], v[232:235]\n"
      "v_mfma_f32_16x16x32_bf16 v[232:235], v[144:147], a[236:239], v[232:235]\n"
      "v_mfma_f32_16x16x32_bf16 v[236:239], v[148:151], a[240:243], v[236:239]\n"
      "v_mfma_f32_16x16x32_bf16 v[236:239], v[136:139], a[244:247], v[236:239]\n"
      "v_mfma_f32_16x16x32_bf16 v[236:239], v[140:143], a[248:251], v[236:239]\n"
      "v_mfma_f32_16x16x32_bf16 v[236:239], v[144:147], a[252:255], v[236:239]\n"
      "v_cvt_pk_bf16_f32 v224, v224, v228\n"
      "v_cvt_pk_bf16_f32 v228, v225, v229\n"
      "v_pk_max_i16 v224, v224, 0\n"
      "v_pk_max_i16 v228, v228, 0\n"
      "v_cvt_pk_bf16_f32 v226, v226, v230\n"
      "v_mfma_f32_16x16x32_bf16 v[140:143], v[172:175], v[156:159], 0\n"
      "v_cvt_pk_bf16_f32 v230, v227, v231\n"
      "v_pk_max_i16 v226, v226, 0\n"
      "v_cvt_pk_bf16_f32 v225, v232, v236\n"
      "v_cvt_pk_bf16_f32 v229, v233, v237\n"
      "v_mfma_f32_16x16x32_bf16 v[140:143], v[176:179], v[32:35], v[140:143]\n"
      "v_pk_max_i16 v225, v225, 0\n"
      "v_pk_max_i16 v229, v229, 0\n"
      "v_cvt_pk_bf16_f32 v227, v234, v238\n"
      "v_cvt_pk_bf16_f32 v231, v235, v239\n"
      "v_pk_max_i16 v227, v227, 0\n"
      "v_mfma_f32_16x16x32_bf16 v[88:91], v[108:111], v[156:159], 0\n"
      "v_pk_max_i16 v230, v230, 0\n"
      "v_pk_max_i16 v231, v231, 0\n"
      "v_max_f32 v188, v140, v141\n"
      "v_max3_f32 v188, v188, v142, v143\n"
      "v_mfma_f32_16x16x32_bf16 v[88:91], v[112:115], v[32:35], v[88:91]\n"
      "v_mfma_f32_16x16x32_bf16 v[144:147], v[164:167], v[156:159], 0\n"
      "v_mfma_f32_16x16x32_bf16 v[144:147], v[168:171], v[32:35], v[144:147]\n"
      "v_mfma_f32_16x16x32_bf16 v[236:239], v[40:43], v[156:159], 0\n"
      "v_mfma_f32_16x16x32_bf16 v[236:239], v[44:47], v[32:35], v[236:239]\n"
      "v_mfma_f32_16x16x32_bf16 v[232:235], v[92:95], v[156:159], 0\n"
      "v_mfma_f32_16x16x32_bf16 v[232:235], v[96:99], v[32:35], v[232:235]\n"
      "v_mfma_f32_16x16x32_bf16 v[160:163], v[216:219], v[156:159], 0\n"
      "v_mfma_f32_16x16x32_bf16 v[160:163], v[220:223], v[32:35], v[160:163]\n"
      "v_max3_f32 v188, v188, v88, v89\n"
      "v_max3_f32 v188, v188, v90, v91\n"
      "v_max3_f32 v188, v188, v144, v145\n"
      "v_max3_f32 v188, v188, v146, v147\n"
      "v_max3_f32 v188, v188, v236, v237\n"
      "v_max3_f32 v188, v188, v238, v239\n"
      "v_max3_f32 v188, v188, v232, v233\n"
      "v_mfma_f32_16x16x32_bf16 v[152:155], v[244:247], v[156:159], 0\n"
      "v_max3_f32 v188, v188, v234, v235\n"
      "v_max3_f32 v188, v188, v160, v161\n"
      "v_max3_f32 v188, v188, v162, v163\n"
      "v_mfma_f32_16x16x32_bf16 v[152:155], v[248:251], v[32:35], v[152:155]\n"
      "ds_read_b128 v[136:139], v0 offset:4096\n"
      "ds_read_b128 v[148:151], v0 offset:4608\n"
      "ds_read_b128 v[24:27], v0 offset:5120\n"
      "ds_read_b128 v[20:23], v0 offset:5632\n"
      "s_add_u32 s56, s48, 12288\n"
      "buffer_store_dwordx2 v[224:225], v2, s[44:47], s56 offen offset:0 nt sc1\n"
      "buffer_store_dwordx2 v[228:229], v2, s[44:47], s56 offen offset:256 nt sc1\n"
      "buffer_store_dwordx2 v[226:227], v2, s[44:47], s56 offen offset:512 nt sc1\n"
      "v_max3_f32 v188, v188, v152, v153\n"
      "v_max3_f32 v188, v188, v154, v155\n"
      "v_mov_b32 v32, v188\n"
      "buffer_store_dwordx2 v[230:231], v2, s[44:47], s56 offen offset:768 nt sc1\n"
      "s_nop 0\n"
      "v_permlane16_swap_b32 v188, v32\n"
      "v_max_f32 v188, v188, v32\n"
      "v_mov_b32 v32, v188\n"
      "s_nop 1\n"
      "v_permlane32_swap_b32 v188, v32\n"
      "v_max_f32 v188, v188, v32\n"
      "v_sub_f32 v140, v140, v188\n"
      "v_sub_f32 v141, v141, v188\n"
      "v_sub_f32 v142, v142, v188\n"
      "s_waitcnt lgkmcnt(3)\n"
      "v_mfma_f32_16x16x32_bf16 v[32:35], a[128:131], v[136:139], 0\n"
      "v_sub_f32 v143, v143, v188\n"
      "v_sub_f32 v88, v88, v188\n"
      "s_waitcnt lgkmcnt(2)\n"
      "v_mfma_f32_16x16x32_bf16 v[32:35], a[132:135], v[148:151], v[32:35]\n"
      "v_exp_f32 v140, v140\n"
      "s_waitcnt lgkmcnt(1)\n"
      "v_mfma_f32_16x16x32_bf16 v[32:35], a[136:139], v[24:27], v[32:35]\n"
      "v_exp_f32 v141, v141\n"
      "s_waitcnt lgkmcnt(0)\n"
      "v_mfma_f32_16x16x32_bf16 v[32:35], a[140:143], v[20:23], v[32:35]\n"
      "v_exp_f32 v142, v142\n"
      "v_mfma_f32_16x16x32_bf16 v[28:31], a[144:147], v[136:139], 0\n"
      "v_exp_f32 v143, v143\n"
      "v_mfma_f32_16x16x32_bf16 v[28:31], a[148:151], v[148:151], v[28:31]\n"
      "v_cvt_pk_bf16_f32 v156, v140, v141\n"
      "v_cvt_pk_bf16_f32 v157, v142, v143\n"
      "v_mfma_f32_16x16x32_bf16 v[28:31], a[152:155], v[24:27], v[28:31]\n"
      "v_sub_f32 v89, v89, v188\n"
      "v_sub_f32 v90, v90, v188\n"
      "v_sub_f32 v91, v91, v188\n"
      "v_sub_f32 v144, v144, v188\n"
      "v_mfma_f32_16x16x32_bf16 v[28:31], a[156:159], v[20:23], v[28:31]\n"
      "v_sub_f32 v145, v145, v188\n"
      "v_sub_f32 v146, v146, v188\n"
      "v_sub_f32 v147, v147, v188\n"
      "v_sub_f32 v236, v236, v188\n"
      "v_sub_f32 v237, v237, v188\n"
      "v_sub_f32 v238, v238, v188\n"
      "v_mfma_f32_16x16x32_bf16 v[184:187], a[160:163], v[136:139], 0\n"
      "v_sub_f32 v239, v239, v188\n"
      "v_sub_f32 v232, v232, v188\n"
      "v_sub_f32 v233, v233, v188\n"
      "v_sub_f32 v234, v234, v188\n"
      "v_sub_f32 v235, v235, v188\n"
      "v_add_f32 v86, v140, v141\n"
      "v_add_f32 v87, v142, v143\n"
      "v_mfma_f32_16x16x32_bf16 v[184:187], a[164:167], v[148:151], v[184:187]\n"
      "v_exp_f32 v88, v88\n"
      "v_exp_f32 v89, v89\n"
      "v_exp_f32 v90, v90\n"
      "v_mfma_f32_16x16x32_bf16 v[184:187], a[168:171], v[24:27], v[184:187]\n"
      "v_exp_f32 v91, v91\n"
      "v_cvt_pk_bf16_f32 v140, v88, v89\n"
      "v_cvt_pk_bf16_f32 v141, v90, v91\n"
      "v_exp_f32 v144, v144\n"
      "v_mfma_f32_16x16x32_bf16 v[184:187], a[172:175], v[20:23], v[184:187]\n"
      "v_exp_f32 v145, v145\n"
      "v_exp_f32 v146, v146\n"
      "v_exp_f32 v147, v147\n"
      "v_cvt_pk_bf16_f32 v142, v144, v145\n"
      "v_mfma_f32_16x16x32_bf16 v[208:211], a[176:179], v[136:139], 0\n"
      "v_cvt_pk_bf16_f32 v143, v146, v147\n"
      "v_exp_f32 v236, v236\n"
      "v_exp_f32 v237, v237\n"
      "v_exp_f32 v238, v238\n"
      "v_mfma_f32_16x16x32_bf16 v[208:211], a[180:183], v[148:151], v[208:211]\n"
      "v_exp_f32 v239, v239\n"
      "v_cvt_pk_bf16_f32 v144, v236, v237\n"
      "v_cvt_pk_bf16_f32 v145, v238, v239\n"
      "v_exp_f32 v232, v232\n"
      "v_mfma_f32_16x16x32_bf16 v[208:211], a[184:187], v[24:27], v[208:211]\n"
      "v_exp_f32 v233, v233\n"
      "v_exp_f32 v234, v234\n"
      "v_exp_f32 v235, v235\n"
      "v_cvt_pk_bf16_f32 v146, v232, v233\n"
      "v_mfma_f32_16x16x32_bf16 v[208:211], a[188:191], v[20:23], v[208:211]\n"
      "v_cvt_pk_bf16_f32 v147, v234, v235\n"
      "v_sub_f32 v160, v160, v188\n"
      "v_sub_f32 v161, v161, v188\n"
      "v_sub_f32 v162, v162, v188\n"
      "v_sub_f32 v163, v163, v188\n"
      "v_sub_f32 v152, v152, v188\n"
      "v_mfma_f32_16x16x4_4b_bf16 v[224:239], v[156:157], v[18:19], 0 cbsz:2 abid:0\n"
      "s_add_u32 m0, s53, 4096\n"
      "s_add_u32 s54, s49, 4096\n"
      "buffer_load_dwordx4 v1, s[40:43], s54 offen offset:0 lds\n"
      "v_sub_f32 v153, v153, v188\n"
      "v_sub_f32 v154, v154, v188\n"
      "v_sub_f32 v155, v155, v188\n"
      "v_exp_f32 v160, v160\n"
      "v_exp_f32 v161, v161\n"
      "v_exp_f32 v162, v162\n"
      "v_exp_f32 v163, v163\n"
      "v_cvt_pk_bf16_f32 v188, v160, v161\n"
      "v_cvt_pk_bf16_f32 v189, v162, v163\n"
      "v_exp_f32 v152, v152\n"
      "v_exp_f32 v153, v153\n"
      "v_exp_f32 v154, v154\n"
      "v_smfmac_f32_16x16x64_bf16 v[86:89], v[12:15], v[140:147], v5\n"
      "v_exp_f32 v155, v155\n"
      "v_cvt_pk_bf16_f32 v190, v152, v153\n"
      "v_mfma_f32_16x16x32_bf16 v[224:227], v[140:143], v[52:55], v[224:227]\n"
      "buffer_load_dwordx4 v1, s[40:43], s54 offen offset:2048 lds\n"
      "s_add_u32 m0, s53, 4992\n"
      "v_cvt_pk_bf16_f32 v191, v154, v155\n"
      "v_cvt_pk_bf16_f32 v156, v32, v33\n"
      "v_cvt_pk_bf16_f32 v157, v34, v35\n"
      "v_mfma_f32_16x16x32_bf16 v[224:227], v[144:147], v[56:59], v[224:227]\n"
      "v_cvt_pk_bf16_f32 v158, v28, v29\n"
      "v_cvt_pk_bf16_f32 v159, v30, v31\n"
      "v_cvt_pk_bf16_f32 v32, v184, v185\n"
      "v_cvt_pk_bf16_f32 v33, v186, v187\n"
      "v_mfma_f32_16x16x32_bf16 v[224:227], v[188:191], v[120:123], v[224:227]\n"
      "buffer_load_dwordx4 v1, s[40:43], s54 offen offset:128 lds\n"
      "v_cvt_pk_bf16_f32 v34, v208, v209\n"
      "v_cvt_pk_bf16_f32 v35, v210, v211\n"
      "v_smfmac_f32_16x16x32_bf16 v[86:89], v[12:13], v[188:191], v5\n"
      "v_mfma_f32_16x16x32_bf16 v[228:231], v[140:143], v[128:131], v[228:231]\n"
      "buffer_load_dwordx4 v1, s[40:43], s54 offen offset:2176 lds\n"
      "v_mfma_f32_16x16x32_bf16 v[228:231], v[144:147], v[132:135], v[228:231]\n"
      "v_mfma_f32_16x16x32_bf16 v[228:231], v[188:191], v[68:71], v[228:231]\n"
      "v_mfma_f32_16x16x32_bf16 v[232:235], v[140:143], v[100:103], v[232:235]\n"
      "v_mfma_f32_16x16x32_bf16 v[232:235], v[144:147], v[104:107], v[232:235]\n"
      "v_mfma_f32_16x16x32_bf16 v[232:235], v[188:191], v[36:39], v[232:235]\n"
      "v_mfma_f32_16x16x32_bf16 v[236:239], v[140:143], v[192:195], v[236:239]\n"
      "v_add_f32 v86, v86, v87\n"
      "v_rcp_f32 v87, v86\n"
      "v_mfma_f32_16x16x32_bf16 v[236:239], v[144:147], v[196:199], v[236:239]\n"
      "ds_bpermute_b32 v88, v4, v87\n"
      "v_mfma_f32_16x16x32_bf16 v[236:239], v[188:191], v[240:243], v[236:239]\n"
      "s_waitcnt lgkmcnt(0)\n"
      "v_mul_f32_dpp v224, v88, v224 row_newbcast:0 row_mask:0xf bank_mask:0xf\n"
      "v_mul_f32_dpp v225, v88, v225 row_newbcast:4 row_mask:0xf bank_mask:0xf\n"
      "v_mul_f32_dpp v226, v88, v226 row_newbcast:8 row_mask:0xf bank_mask:0xf\n"
      "v_mul_f32_dpp v227, v88, v227 row_newbcast:12 row_mask:0xf bank_mask:0xf\n"
      "v_mul_f32_dpp v228, v88, v228 row_newbcast:0 row_mask:0xf bank_mask:0xf\n"
      "v_mul_f32_dpp v229, v88, v229 row_newbcast:4 row_mask:0xf bank_mask:0xf\n"
      "v_mfma_f32_16x16x32_bf16 v[224:227], v[116:119], a[192:195], v[224:227]\n"
      "v_mul_f32_dpp v230, v88, v230 row_newbcast:8 row_mask:0xf bank_mask:0xf\n"
      "v_mul_f32_dpp v231, v88, v231 row_newbcast:12 row_mask:0xf bank_mask:0xf\n"
      "v_mfma_f32_16x16x32_bf16 v[224:227], v[124:127], a[196:199], v[224:227]\n"
      "v_mul_f32_dpp v232, v88, v232 row_newbcast:0 row_mask:0xf bank_mask:0xf\n"
      "v_mul_f32_dpp v233, v88, v233 row_newbcast:4 row_mask:0xf bank_mask:0xf\n"
      "v_mul_f32_dpp v234, v88, v234 row_newbcast:8 row_mask:0xf bank_mask:0xf\n"
      "v_mul_f32_dpp v235, v88, v235 row_newbcast:12 row_mask:0xf bank_mask:0xf\n"
      "v_mfma_f32_16x16x32_bf16 v[224:227], v[48:51], a[200:203], v[224:227]\n"
      "v_mul_f32_dpp v236, v88, v236 row_newbcast:0 row_mask:0xf bank_mask:0xf\n"
      "v_mul_f32_dpp v237, v88, v237 row_newbcast:4 row_mask:0xf bank_mask:0xf\n"
      "v_mul_f32_dpp v238, v88, v238 row_newbcast:8 row_mask:0xf bank_mask:0xf\n"
      "v_mfma_f32_16x16x32_bf16 v[224:227], v[252:255], a[204:207], v[224:227]\n"
      "v_mul_f32_dpp v239, v88, v239 row_newbcast:12 row_mask:0xf bank_mask:0xf\n"
      "v_mfma_f32_16x16x32_bf16 v[228:231], v[116:119], a[208:211], v[228:231]\n"
      "v_mfma_f32_16x16x32_bf16 v[228:231], v[124:127], a[212:215], v[228:231]\n"
      "v_mfma_f32_16x16x32_bf16 v[228:231], v[48:51], a[216:219], v[228:231]\n"
      "v_mfma_f32_16x16x32_bf16 v[228:231], v[252:255], a[220:223], v[228:231]\n"
      "v_mfma_f32_16x16x32_bf16 v[232:235], v[116:119], a[224:227], v[232:235]\n"
      "v_mfma_f32_16x16x32_bf16 v[232:235], v[124:127], a[228:231], v[232:235]\n"
      "v_mfma_f32_16x16x32_bf16 v[232:235], v[48:51], a[232:235], v[232:235]\n"
      "v_mfma_f32_16x16x32_bf16 v[232:235], v[252:255], a[236:239], v[232:235]\n"
      "v_mfma_f32_16x16x32_bf16 v[236:239], v[116:119], a[240:243], v[236:239]\n"
      "v_mfma_f32_16x16x32_bf16 v[236:239], v[124:127], a[244:247], v[236:239]\n"
      "v_mfma_f32_16x16x32_bf16 v[236:239], v[48:51], a[248:251], v[236:239]\n"
      "v_mfma_f32_16x16x32_bf16 v[236:239], v[252:255], a[252:255], v[236:239]\n"
      "v_cvt_pk_bf16_f32 v224, v224, v228\n"
      "v_cvt_pk_bf16_f32 v228, v225, v229\n"
      "v_pk_max_i16 v224, v224, 0\n"
      "v_pk_max_i16 v228, v228, 0\n"
      "v_cvt_pk_bf16_f32 v226, v226, v230\n"
      "v_mfma_f32_16x16x32_bf16 v[140:143], v[172:175], v[156:159], 0\n"
      "v_cvt_pk_bf16_f32 v230, v227, v231\n"
      "v_pk_max_i16 v226, v226, 0\n"
      "v_cvt_pk_bf16_f32 v225, v232, v236\n"
      "v_cvt_pk_bf16_f32 v229, v233, v237\n"
      "v_mfma_f32_16x16x32_bf16 v[140:143], v[176:179], v[32:35], v[140:143]\n"
      "v_pk_max_i16 v225, v225, 0\n"
      "v_pk_max_i16 v229, v229, 0\n"
      "v_cvt_pk_bf16_f32 v227, v234, v238\n"
      "v_cvt_pk_bf16_f32 v231, v235, v239\n"
      "v_pk_max_i16 v227, v227, 0\n"
      "v_mfma_f32_16x16x32_bf16 v[88:91], v[108:111], v[156:159], 0\n"
      "v_pk_max_i16 v230, v230, 0\n"
      "v_pk_max_i16 v231, v231, 0\n"
      "v_max_f32 v188, v140, v141\n"
      "v_max3_f32 v188, v188, v142, v143\n"
      "v_mfma_f32_16x16x32_bf16 v[88:91], v[112:115], v[32:35], v[88:91]\n"
      "v_mfma_f32_16x16x32_bf16 v[144:147], v[164:167], v[156:159], 0\n"
      "v_mfma_f32_16x16x32_bf16 v[144:147], v[168:171], v[32:35], v[144:147]\n"
      "v_mfma_f32_16x16x32_bf16 v[236:239], v[40:43], v[156:159], 0\n"
      "v_mfma_f32_16x16x32_bf16 v[236:239], v[44:47], v[32:35], v[236:239]\n"
      "v_mfma_f32_16x16x32_bf16 v[232:235], v[92:95], v[156:159], 0\n"
      "v_mfma_f32_16x16x32_bf16 v[232:235], v[96:99], v[32:35], v[232:235]\n"
      "v_mfma_f32_16x16x32_bf16 v[160:163], v[216:219], v[156:159], 0\n"
      "v_mfma_f32_16x16x32_bf16 v[160:163], v[220:223], v[32:35], v[160:163]\n"
      "v_max3_f32 v188, v188, v88, v89\n"
      "v_max3_f32 v188, v188, v90, v91\n"
      "v_max3_f32 v188, v188, v144, v145\n"
      "v_max3_f32 v188, v188, v146, v147\n"
      "v_max3_f32 v188, v188, v236, v237\n"
      "v_max3_f32 v188, v188, v238, v239\n"
      "v_max3_f32 v188, v188, v232, v233\n"
      "v_mfma_f32_16x16x32_bf16 v[152:155], v[244:247], v[156:159], 0\n"
      "v_max3_f32 v188, v188, v234, v235\n"
      "v_max3_f32 v188, v188, v160, v161\n"
      "v_max3_f32 v188, v188, v162, v163\n"
      "v_mfma_f32_16x16x32_bf16 v[152:155], v[248:251], v[32:35], v[152:155]\n"
      "ds_read_b128 v[124:127], v0 offset:0\n"
      "ds_read_b128 v[48:51], v0 offset:512\n"
      "ds_read_b128 v[116:119], v0 offset:1024\n"
      "ds_read_b128 v[252:255], v0 offset:1536\n"
      "s_add_u32 s56, s48, 8192\n"
      "buffer_store_dwordx2 v[224:225], v2, s[44:47], s56 offen offset:0 nt sc1\n"
      "buffer_store_dwordx2 v[228:229], v2, s[44:47], s56 offen offset:256 nt sc1\n"
      "buffer_store_dwordx2 v[226:227], v2, s[44:47], s56 offen offset:512 nt sc1\n"
      "v_max3_f32 v188, v188, v152, v153\n"
      "v_max3_f32 v188, v188, v154, v155\n"
      "v_mov_b32 v32, v188\n"
      "buffer_store_dwordx2 v[230:231], v2, s[44:47], s56 offen offset:768 nt sc1\n"
      "s_nop 0\n"
      "v_permlane16_swap_b32 v188, v32\n"
      "v_max_f32 v188, v188, v32\n"
      "v_mov_b32 v32, v188\n"
      "s_nop 1\n"
      "v_permlane32_swap_b32 v188, v32\n"
      "v_max_f32 v188, v188, v32\n"
      "v_sub_f32 v140, v140, v188\n"
      "v_sub_f32 v141, v141, v188\n"
      "v_sub_f32 v142, v142, v188\n"
      "s_waitcnt lgkmcnt(3)\n"
      "v_mfma_f32_16x16x32_bf16 v[32:35], a[128:131], v[124:127], 0\n"
      "v_sub_f32 v143, v143, v188\n"
      "v_sub_f32 v88, v88, v188\n"
      "s_waitcnt lgkmcnt(2)\n"
      "v_mfma_f32_16x16x32_bf16 v[32:35], a[132:135], v[48:51], v[32:35]\n"
      "v_exp_f32 v140, v140\n"
      "s_waitcnt lgkmcnt(1)\n"
      "v_mfma_f32_16x16x32_bf16 v[32:35], a[136:139], v[116:119], v[32:35]\n"
      "v_exp_f32 v141, v141\n"
      "s_waitcnt lgkmcnt(0)\n"
      "v_mfma_f32_16x16x32_bf16 v[32:35], a[140:143], v[252:255], v[32:35]\n"
      "v_exp_f32 v142, v142\n"
      "v_mfma_f32_16x16x32_bf16 v[28:31], a[144:147], v[124:127], 0\n"
      "v_exp_f32 v143, v143\n"
      "v_mfma_f32_16x16x32_bf16 v[28:31], a[148:151], v[48:51], v[28:31]\n"
      "v_cvt_pk_bf16_f32 v190, v140, v141\n"
      "v_cvt_pk_bf16_f32 v191, v142, v143\n"
      "v_mfma_f32_16x16x32_bf16 v[28:31], a[152:155], v[116:119], v[28:31]\n"
      "v_sub_f32 v89, v89, v188\n"
      "v_sub_f32 v90, v90, v188\n"
      "v_sub_f32 v91, v91, v188\n"
      "v_sub_f32 v144, v144, v188\n"
      "v_mfma_f32_16x16x32_bf16 v[28:31], a[156:159], v[252:255], v[28:31]\n"
      "v_sub_f32 v145, v145, v188\n"
      "v_sub_f32 v146, v146, v188\n"
      "v_sub_f32 v147, v147, v188\n"
      "v_sub_f32 v236, v236, v188\n"
      "v_sub_f32 v237, v237, v188\n"
      "v_sub_f32 v238, v238, v188\n"
      "v_mfma_f32_16x16x32_bf16 v[184:187], a[160:163], v[124:127], 0\n"
      "v_sub_f32 v239, v239, v188\n"
      "v_sub_f32 v232, v232, v188\n"
      "v_sub_f32 v233, v233, v188\n"
      "v_sub_f32 v234, v234, v188\n"
      "v_sub_f32 v235, v235, v188\n"
      "v_add_f32 v86, v140, v141\n"
      "v_add_f32 v87, v142, v143\n"
      "v_mfma_f32_16x16x32_bf16 v[184:187], a[164:167], v[48:51], v[184:187]\n"
      "v_exp_f32 v88, v88\n"
      "v_exp_f32 v89, v89\n"
      "v_exp_f32 v90, v90\n"
      "v_mfma_f32_16x16x32_bf16 v[184:187], a[168:171], v[116:119], v[184:187]\n"
      "v_exp_f32 v91, v91\n"
      "v_cvt_pk_bf16_f32 v140, v88, v89\n"
      "v_cvt_pk_bf16_f32 v141, v90, v91\n"
      "v_exp_f32 v144, v144\n"
      "v_mfma_f32_16x16x32_bf16 v[184:187], a[172:175], v[252:255], v[184:187]\n"
      "v_exp_f32 v145, v145\n"
      "v_exp_f32 v146, v146\n"
      "v_exp_f32 v147, v147\n"
      "v_cvt_pk_bf16_f32 v142, v144, v145\n"
      "v_mfma_f32_16x16x32_bf16 v[208:211], a[176:179], v[124:127], 0\n"
      "v_cvt_pk_bf16_f32 v143, v146, v147\n"
      "v_exp_f32 v236, v236\n"
      "v_exp_f32 v237, v237\n"
      "v_exp_f32 v238, v238\n"
      "v_mfma_f32_16x16x32_bf16 v[208:211], a[180:183], v[48:51], v[208:211]\n"
      "v_exp_f32 v239, v239\n"
      "v_cvt_pk_bf16_f32 v144, v236, v237\n"
      "v_cvt_pk_bf16_f32 v145, v238, v239\n"
      "v_exp_f32 v232, v232\n"
      "v_mfma_f32_16x16x32_bf16 v[208:211], a[184:187], v[116:119], v[208:211]\n"
      "v_exp_f32 v233, v233\n"
      "v_exp_f32 v234, v234\n"
      "v_exp_f32 v235, v235\n"
      "v_cvt_pk_bf16_f32 v146, v232, v233\n"
      "v_mfma_f32_16x16x32_bf16 v[208:211], a[188:191], v[252:255], v[208:211]\n"
      "ds_read_b128 v[156:159], v10 offset:0\n"
      "v_cvt_pk_bf16_f32 v147, v234, v235\n"
      "v_sub_f32 v160, v160, v188\n"
      "v_sub_f32 v161, v161, v188\n"
      "v_sub_f32 v162, v162, v188\n"
      "v_sub_f32 v163, v163, v188\n"
      "v_sub_f32 v152, v152, v188\n"
      "v_mfma_f32_16x16x4_4b_bf16 v[224:239], v[190:191], v[18:19], 0 cbsz:2 abid:0\n"
      "s_add_u32 m0, s53, 0\n"
      "s_add_u32 s54, s49, 0\n"
      "buffer_load_dwordx4 v1, s[40:43], s54 offen offset:0 lds\n"
      "v_sub_f32 v153, v153, v188\n"
      "v_sub_f32 v154, v154, v188\n"
      "v_sub_f32 v155, v155, v188\n"
      "v_exp_f32 v160, v160\n"
      "v_exp_f32 v161, v161\n"
      "v_exp_f32 v162, v162\n"
      "v_exp_f32 v163, v163\n"
      "v_cvt_pk_bf16_f32 v188, v160, v161\n"
      "v_cvt_pk_bf16_f32 v189, v162, v163\n"
      "v_exp_f32 v152, v152\n"
      "v_exp_f32 v153, v153\n"
      "v_exp_f32 v154, v154\n"
      "v_smfmac_f32_16x16x64_bf16 v[86:89], v[12:15], v[140:147], v5\n"
      "v_exp_f32 v155, v155\n"
      "v_cvt_pk_bf16_f32 v190, v152, v153\n"
      "v_mfma_f32_16x16x32_bf16 v[224:227], v[140:143], v[52:55], v[224:227]\n"
      "buffer_load_dwordx4 v1, s[40:43], s54 offen offset:2048 lds\n"
      "s_add_u32 m0, s53, 896\n"
      "v_cvt_pk_bf16_f32 v191, v154, v155\n"
      "v_cvt_pk_bf16_f32 v152, v32, v33\n"
      "v_cvt_pk_bf16_f32 v153, v34, v35\n"
      "v_cvt_pk_bf16_f32 v154, v28, v29\n"
      "v_mfma_f32_16x16x32_bf16 v[224:227], v[144:147], v[56:59], v[224:227]\n"
      "v_cvt_pk_bf16_f32 v155, v30, v31\n"
      "v_cvt_pk_bf16_f32 v32, v184, v185\n"
      "v_cvt_pk_bf16_f32 v33, v186, v187\n"
      "v_cvt_pk_bf16_f32 v34, v208, v209\n"
      "v_mfma_f32_16x16x32_bf16 v[224:227], v[188:191], v[120:123], v[224:227]\n"
      "buffer_load_dwordx4 v1, s[40:43], s54 offen offset:128 lds\n"
      "v_cvt_pk_bf16_f32 v35, v210, v211\n"
      "v_smfmac_f32_16x16x32_bf16 v[86:89], v[12:13], v[188:191], v5\n"
      "v_mfma_f32_16x16x32_bf16 v[228:231], v[140:143], v[128:131], v[228:231]\n"
      "buffer_load_dwordx4 v1, s[40:43], s54 offen offset:2176 lds\n"
      "v_mfma_f32_16x16x32_bf16 v[228:231], v[144:147], v[132:135], v[228:231]\n"
      "v_mfma_f32_16x16x32_bf16 v[228:231], v[188:191], v[68:71], v[228:231]\n"
      "v_add_u32 v10, 64, v10\n"
      "v_mfma_f32_16x16x32_bf16 v[232:235], v[140:143], v[100:103], v[232:235]\n"
      "v_mfma_f32_16x16x32_bf16 v[232:235], v[144:147], v[104:107], v[232:235]\n"
      "v_mfma_f32_16x16x32_bf16 v[232:235], v[188:191], v[36:39], v[232:235]\n"
      "v_add_f32 v86, v86, v87\n"
      "v_rcp_f32 v87, v86\n"
      "v_mfma_f32_16x16x32_bf16 v[236:239], v[140:143], v[192:195], v[236:239]\n"
      "ds_bpermute_b32 v88, v4, v87\n"
      "v_mfma_f32_16x16x32_bf16 v[236:239], v[144:147], v[196:199], v[236:239]\n"
      "v_mfma_f32_16x16x32_bf16 v[236:239], v[188:191], v[240:243], v[236:239]\n"
      "s_waitcnt lgkmcnt(0)\n"
      "v_mul_f32_dpp v224, v88, v224 row_newbcast:0 row_mask:0xf bank_mask:0xf\n"
      "v_mul_f32_dpp v225, v88, v225 row_newbcast:4 row_mask:0xf bank_mask:0xf\n"
      "v_mul_f32_dpp v226, v88, v226 row_newbcast:8 row_mask:0xf bank_mask:0xf\n"
      "v_mul_f32_dpp v227, v88, v227 row_newbcast:12 row_mask:0xf bank_mask:0xf\n"
      "v_mul_f32_dpp v228, v88, v228 row_newbcast:0 row_mask:0xf bank_mask:0xf\n"
      "v_mul_f32_dpp v229, v88, v229 row_newbcast:4 row_mask:0xf bank_mask:0xf\n"
      "v_mfma_f32_16x16x32_bf16 v[224:227], v[136:139], a[192:195], v[224:227]\n"
      "v_mul_f32_dpp v230, v88, v230 row_newbcast:8 row_mask:0xf bank_mask:0xf\n"
      "v_mul_f32_dpp v231, v88, v231 row_newbcast:12 row_mask:0xf bank_mask:0xf\n"
      "v_mul_f32_dpp v232, v88, v232 row_newbcast:0 row_mask:0xf bank_mask:0xf\n"
      "v_mfma_f32_16x16x32_bf16 v[224:227], v[148:151], a[196:199], v[224:227]\n"
      "v_mul_f32_dpp v233, v88, v233 row_newbcast:4 row_mask:0xf bank_mask:0xf\n"
      "v_mul_f32_dpp v234, v88, v234 row_newbcast:8 row_mask:0xf bank_mask:0xf\n"
      "v_mul_f32_dpp v235, v88, v235 row_newbcast:12 row_mask:0xf bank_mask:0xf\n"
      "v_mul_f32_dpp v236, v88, v236 row_newbcast:0 row_mask:0xf bank_mask:0xf\n"
      "v_mfma_f32_16x16x32_bf16 v[224:227], v[24:27], a[200:203], v[224:227]\n"
      "v_mul_f32_dpp v237, v88, v237 row_newbcast:4 row_mask:0xf bank_mask:0xf\n"
      "v_mul_f32_dpp v238, v88, v238 row_newbcast:8 row_mask:0xf bank_mask:0xf\n"
      "v_mul_f32_dpp v239, v88, v239 row_newbcast:12 row_mask:0xf bank_mask:0xf\n"
      "v_mfma_f32_16x16x32_bf16 v[224:227], v[20:23], a[204:207], v[224:227]\n"
      "v_mfma_f32_16x16x32_bf16 v[228:231], v[136:139], a[208:211], v[228:231]\n"
      "v_mfma_f32_16x16x32_bf16 v[228:231], v[148:151], a[212:215], v[228:231]\n"
      "v_mfma_f32_16x16x32_bf16 v[228:231], v[24:27], a[216:219], v[228:231]\n"
      "v_mfma_f32_16x16x32_bf16 v[228:231], v[20:23], a[220:223], v[228:231]\n"
      "v_mfma_f32_16x16x32_bf16 v[232:235], v[136:139], a[224:227], v[232:235]\n"
      "v_mfma_f32_16x16x32_bf16 v[232:235], v[148:151], a[228:231], v[232:235]\n"
      "v_mfma_f32_16x16x32_bf16 v[232:235], v[24:27], a[232:235], v[232:235]\n"
      "v_mfma_f32_16x16x32_bf16 v[232:235], v[20:23], a[236:239], v[232:235]\n"
      "v_mfma_f32_16x16x32_bf16 v[236:239], v[136:139], a[240:243], v[236:239]\n"
      "v_mfma_f32_16x16x32_bf16 v[236:239], v[148:151], a[244:247], v[236:239]\n"
      "v_mfma_f32_16x16x32_bf16 v[236:239], v[24:27], a[248:251], v[236:239]\n"
      "v_mfma_f32_16x16x32_bf16 v[236:239], v[20:23], a[252:255], v[236:239]\n"
      "v_cvt_pk_bf16_f32 v224, v224, v228\n"
      "v_cvt_pk_bf16_f32 v228, v225, v229\n"
      "v_pk_max_i16 v224, v224, 0\n"
      "v_pk_max_i16 v228, v228, 0\n"
      "v_cvt_pk_bf16_f32 v226, v226, v230\n"
      "v_cvt_pk_bf16_f32 v230, v227, v231\n"
      "v_pk_max_i16 v226, v226, 0\n"
      "v_mfma_f32_16x16x32_bf16 v[148:151], v[172:175], v[152:155], 0\n"
      "v_cvt_pk_bf16_f32 v225, v232, v236\n"
      "v_cvt_pk_bf16_f32 v229, v233, v237\n"
      "v_pk_max_i16 v225, v225, 0\n"
      "v_pk_max_i16 v229, v229, 0\n"
      "v_cvt_pk_bf16_f32 v227, v234, v238\n"
      "v_cvt_pk_bf16_f32 v231, v235, v239\n"
      "v_pk_max_i16 v227, v227, 0\n"
      "v_mfma_f32_16x16x32_bf16 v[148:151], v[176:179], v[32:35], v[148:151]\n"
      "v_pk_max_i16 v230, v230, 0\n"
      "v_pk_max_i16 v231, v231, 0\n"
      "v_mfma_f32_16x16x32_bf16 v[136:139], v[108:111], v[152:155], 0\n"
      "v_mfma_f32_16x16x32_bf16 v[136:139], v[112:115], v[32:35], v[136:139]\n"
      "v_mfma_f32_16x16x32_bf16 v[236:239], v[164:167], v[152:155], 0\n"
      "v_mfma_f32_16x16x32_bf16 v[236:239], v[168:171], v[32:35], v[236:239]\n"
      "v_mfma_f32_16x16x32_bf16 v[160:163], v[40:43], v[152:155], 0\n"
      "v_mfma_f32_16x16x32_bf16 v[160:163], v[44:47], v[32:35], v[160:163]\n"
      "v_max_f32 v208, v148, v149\n"
      "v_max3_f32 v208, v208, v150, v151\n"
      "v_mfma_f32_16x16x32_bf16 v[28:31], v[92:95], v[152:155], 0\n"
      "v_mfma_f32_16x16x32_bf16 v[28:31], v[96:99], v[32:35], v[28:31]\n"
      "v_max3_f32 v208, v208, v136, v137\n"
      "v_max3_f32 v208, v208, v138, v139\n"
      "v_max3_f32 v208, v208, v236, v237\n"
      "v_max3_f32 v208, v208, v238, v239\n"
      "v_max3_f32 v208, v208, v160, v161\n"
      "v_max3_f32 v208, v208, v162, v163\n"
      "v_mfma_f32_16x16x32_bf16 v[24:27], v[216:219], v[152:155], 0\n"
      "v_mfma_f32_16x16x32_bf16 v[24:27], v[220:223], v[32:35], v[24:27]\n"
      "v_max3_f32 v208, v208, v28, v29\n"
      "v_max3_f32 v208, v208, v30, v31\n"
      "v_mfma_f32_16x16x32_bf16 v[20:23], v[244:247], v[152:155], 0\n"
      "v_mfma_f32_16x16x32_bf16 v[20:23], v[248:251], v[32:35], v[20:23]\n"
      "ds_read_b64 v[152:153], v11 offset:4096\n"
      "s_nop 2\n"
      "v_max3_f32 v208, v208, v24, v25\n"
      "v_max3_f32 v208, v208, v26, v27\n"
      "s_nop 1\n"
      "v_max3_f32 v208, v208, v20, v21\n"
      "v_max3_f32 v208, v208, v22, v23\n"
      "v_mov_b32 v32, v208\n"
      "s_nop 1\n"
      "v_permlane16_swap_b32 v208, v32\n"
      "v_max_f32 v208, v208, v32\n"
      "v_mov_b32 v32, v208\n"
      "s_nop 1\n"
      "v_permlane32_swap_b32 v208, v32\n"
      "v_max_f32 v208, v208, v32\n"
      "v_sub_f32 v148, v148, v208\n"
      "v_sub_f32 v149, v149, v208\n"
      "v_sub_f32 v150, v150, v208\n"
      "v_sub_f32 v151, v151, v208\n"
      "v_exp_f32 v148, v148\n"
      "v_exp_f32 v149, v149\n"
      "v_exp_f32 v150, v150\n"
      "v_exp_f32 v151, v151\n"
      "v_cvt_pk_bf16_f32 v234, v148, v149\n"
      "v_cvt_pk_bf16_f32 v235, v150, v151\n"
      "v_sub_f32 v136, v136, v208\n"
      "v_sub_f32 v137, v137, v208\n"
      "v_sub_f32 v138, v138, v208\n"
      "v_sub_f32 v139, v139, v208\n"
      "v_sub_f32 v236, v236, v208\n"
      "v_sub_f32 v237, v237, v208\n"
      "v_sub_f32 v238, v238, v208\n"
      "v_sub_f32 v239, v239, v208\n"
      "v_sub_f32 v160, v160, v208\n"
      "v_sub_f32 v161, v161, v208\n"
      "v_sub_f32 v162, v162, v208\n"
      "v_sub_f32 v163, v163, v208\n"
      "v_sub_f32 v28, v28, v208\n"
      "v_sub_f32 v29, v29, v208\n"
      "v_sub_f32 v30, v30, v208\n"
      "v_sub_f32 v31, v31, v208\n"
      "v_add_f32 v32, v148, v149\n"
      "v_add_f32 v33, v150, v151\n"
      "v_exp_f32 v136, v136\n"
      "v_exp_f32 v137, v137\n"
      "v_exp_f32 v138, v138\n"
      "v_exp_f32 v139, v139\n"
      "v_cvt_pk_bf16_f32 v232, v136, v137\n"
      "v_cvt_pk_bf16_f32 v233, v138, v139\n"
      "v_exp_f32 v236, v236\n"
      "v_mfma_f32_16x16x4_4b_bf16 v[136:151], v[234:235], v[18:19], 0 cbsz:2 abid:0\n"
      "v_exp_f32 v237, v237\n"
      "v_exp_f32 v238, v238\n"
      "v_exp_f32 v239, v239\n"
      "v_cvt_pk_bf16_f32 v234, v236, v237\n"
      "v_cvt_pk_bf16_f32 v235, v238, v239\n"
      "v_exp_f32 v160, v160\n"
      "v_exp_f32 v161, v161\n"
      "v_exp_f32 v162, v162\n"
      "v_exp_f32 v163, v163\n"
      "v_cvt_pk_bf16_f32 v236, v160, v161\n"
      "v_cvt_pk_bf16_f32 v237, v162, v163\n"
      "v_exp_f32 v28, v28\n"
      "v_exp_f32 v29, v29\n"
      "v_exp_f32 v30, v30\n"
      "v_exp_f32 v31, v31\n"
      "v_cvt_pk_bf16_f32 v238, v28, v29\n"
      "v_cvt_pk_bf16_f32 v239, v30, v31\n"
      "v_sub_f32 v24, v24, v208\n"
      "v_sub_f32 v25, v25, v208\n"
      "v_sub_f32 v26, v26, v208\n"
      "v_sub_f32 v27, v27, v208\n"
      "v_sub_f32 v20, v20, v208\n"
      "v_sub_f32 v21, v21, v208\n"
      "v_sub_f32 v22, v22, v208\n"
      "v_sub_f32 v23, v23, v208\n"
      "v_exp_f32 v24, v24\n"
      "v_exp_f32 v25, v25\n"
      "v_exp_f32 v26, v26\n"
      "v_exp_f32 v27, v27\n"
      "v_cvt_pk_bf16_f32 v208, v24, v25\n"
      "v_cvt_pk_bf16_f32 v209, v26, v27\n"
      "v_exp_f32 v20, v20\n"
      "v_exp_f32 v21, v21\n"
      "v_exp_f32 v22, v22\n"
      "v_exp_f32 v23, v23\n"
      "v_cvt_pk_bf16_f32 v210, v20, v21\n"
      "v_cvt_pk_bf16_f32 v211, v22, v23\n"
      "v_smfmac_f32_16x16x64_bf16 v[32:35], v[12:15], v[232:239], v5\n"
      "s_add_u32 s56, s48, 4096\n"
      "buffer_store_dwordx2 v[224:225], v2, s[44:47], s56 offen offset:0 nt sc1\n"
      "v_mfma_f32_16x16x32_bf16 v[136:139], v[232:235], v[52:55], v[136:139]\n"
      "buffer_store_dwordx2 v[228:229], v2, s[44:47], s56 offen offset:256 nt sc1\n"
      "v_mfma_f32_16x16x32_bf16 v[136:139], v[236:239], v[56:59], v[136:139]\n"
      "buffer_store_dwordx2 v[226:227], v2, s[44:47], s56 offen offset:512 nt sc1\n"
      "v_mfma_f32_16x16x32_bf16 v[136:139], v[208:211], v[120:123], v[136:139]\n"
      "buffer_store_dwordx2 v[230:231], v2, s[44:47], s56 offen offset:768 nt sc1\n"
      "v_smfmac_f32_16x16x32_bf16 v[32:35], v[12:13], v[208:211], v5\n"
      "s_waitcnt lgkmcnt(0)\n"
      "v_lshlrev_b32 v228, 16, v152\n"
      "v_and_b32 v229, 0xffff0000, v152\n"
      "v_mfma_f32_16x16x32_bf16 v[140:143], v[232:235], v[128:131], v[140:143]\n"
      "v_mov_b64 v[224:225], 0\n"
      "v_lshlrev_b32 v230, 16, v153\n"
      "v_and_b32 v231, 0xffff0000, v153\n"
      "v_mfma_f32_16x16x32_bf16 v[140:143], v[236:239], v[132:135], v[140:143]\n"
      "v_mov_b64 v[20:21], 0\n"
      "v_add_f32 v32, v32, v33\n"
      "v_rcp_f32 v33, v32\n"
      "v_mfma_f32_16x16x32_bf16 v[140:143], v[208:211], v[68:71], v[140:143]\n"
      "ds_bpermute_b32 v34, v4, v33\n"
      "v_mov_b64 v[28:29], 0\n"
      "v_mfma_f32_16x16x32_bf16 v[144:147], v[232:235], v[100:103], v[144:147]\n"
      "v_mov_b64 v[160:161], 0\n"
      "v_mfma_f32_16x16x32_bf16 v[144:147], v[236:239], v[104:107], v[144:147]\n"
      "v_mov_b64 v[24:25], 0\n"
      "v_mfma_f32_16x16x32_bf16 v[144:147], v[208:211], v[36:39], v[144:147]\n"
      "v_mov_b64 v[32:33], 0\n"
      "v_mfma_f32_16x16x32_bf16 v[148:151], v[232:235], v[192:195], v[148:151]\n"
      "v_mov_b64 v[232:233], 0\n"
      "v_mfma_f32_16x16x32_bf16 v[148:151], v[236:239], v[196:199], v[148:151]\n"
      "v_mfma_f32_16x16x32_bf16 v[148:151], v[208:211], v[240:243], v[148:151]\n"
      "s_waitcnt lgkmcnt(0)\n"
      "v_mul_f32_dpp v136, v34, v136 row_newbcast:0 row_mask:0xf bank_mask:0xf\n"
      "v_mul_f32_dpp v137, v34, v137 row_newbcast:4 row_mask:0xf bank_mask:0xf\n"
      "v_mul_f32_dpp v138, v34, v138 row_newbcast:8 row_mask:0xf bank_mask:0xf\n"
      "v_mul_f32_dpp v139, v34, v139 row_newbcast:12 row_mask:0xf bank_mask:0xf\n"
      "v_mul_f32_dpp v140, v34, v140 row_newbcast:0 row_mask:0xf bank_mask:0xf\n"
      "v_mul_f32_dpp v141, v34, v141 row_newbcast:4 row_mask:0xf bank_mask:0xf\n"
      "v_mfma_f32_16x16x32_bf16 v[136:139], v[124:127], a[192:195], v[136:139]\n"
      "v_mul_f32_dpp v142, v34, v142 row_newbcast:8 row_mask:0xf bank_mask:0xf\n"
      "v_mul_f32_dpp v143, v34, v143 row_newbcast:12 row_mask:0xf bank_mask:0xf\n"
      "v_mul_f32_dpp v144, v34, v144 row_newbcast:0 row_mask:0xf bank_mask:0xf\n"
      "v_mfma_f32_16x16x32_bf16 v[136:139], v[48:51], a[196:199], v[136:139]\n"
      "v_mul_f32_dpp v145, v34, v145 row_newbcast:4 row_mask:0xf bank_mask:0xf\n"
      "v_mul_f32_dpp v146, v34, v146 row_newbcast:8 row_mask:0xf bank_mask:0xf\n"
      "v_mul_f32_dpp v147, v34, v147 row_newbcast:12 row_mask:0xf bank_mask:0xf\n"
      "v_mul_f32_dpp v148, v34, v148 row_newbcast:0 row_mask:0xf bank_mask:0xf\n"
      "v_mfma_f32_16x16x32_bf16 v[136:139], v[116:119], a[200:203], v[136:139]\n"
      "v_mul_f32_dpp v149, v34, v149 row_newbcast:4 row_mask:0xf bank_mask:0xf\n"
      "v_mul_f32_dpp v150, v34, v150 row_newbcast:8 row_mask:0xf bank_mask:0xf\n"
      "v_mul_f32_dpp v151, v34, v151 row_newbcast:12 row_mask:0xf bank_mask:0xf\n"
      "v_add_u32 v11, 0x200, v11\n"
      "v_mfma_f32_16x16x32_bf16 v[136:139], v[252:255], a[204:207], v[136:139]\n"
      "v_mfma_f32_16x16x32_bf16 v[140:143], v[124:127], a[208:211], v[140:143]\n"
      "v_mfma_f32_16x16x32_bf16 v[140:143], v[48:51], a[212:215], v[140:143]\n"
      "v_mfma_f32_16x16x32_bf16 v[140:143], v[116:119], a[216:219], v[140:143]\n"
      "v_mfma_f32_16x16x32_bf16 v[140:143], v[252:255], a[220:223], v[140:143]\n"
      "s_add_u32 s56, s48, 0\n"
      "v_mfma_f32_16x16x32_bf16 v[144:147], v[124:127], a[224:227], v[144:147]\n"
      "v_mfma_f32_16x16x32_bf16 v[144:147], v[48:51], a[228:231], v[144:147]\n"
      "v_mfma_f32_16x16x32_bf16 v[144:147], v[116:119], a[232:235], v[144:147]\n"
      "v_mfma_f32_16x16x32_bf16 v[144:147], v[252:255], a[236:239], v[144:147]\n"
      "v_mfma_f32_16x16x32_bf16 v[148:151], v[124:127], a[240:243], v[148:151]\n"
      "v_mfma_f32_16x16x32_bf16 v[148:151], v[48:51], a[244:247], v[148:151]\n"
      "v_mfma_f32_16x16x32_bf16 v[148:151], v[116:119], a[248:251], v[148:151]\n"
      "v_cvt_pk_bf16_f32 v136, v136, v140\n"
      "v_cvt_pk_bf16_f32 v140, v137, v141\n"
      "v_pk_max_i16 v136, v136, 0\n"
      "v_pk_max_i16 v140, v140, 0\n"
      "v_cvt_pk_bf16_f32 v138, v138, v142\n"
      "v_cvt_pk_bf16_f32 v142, v139, v143\n"
      "v_pk_max_i16 v138, v138, 0\n"
      "v_mfma_f32_16x16x32_bf16 v[148:151], v[252:255], a[252:255], v[148:151]\n"
      "v_pk_max_i16 v142, v142, 0\n"
      "v_smfmac_f32_16x16x64_bf16 v[224:227], v[156:159], v[108:115], v5\n"
      "v_smfmac_f32_16x16x64_bf16 v[20:23], v[156:159], v[164:171], v5\n"
      "v_smfmac_f32_16x16x64_bf16 v[28:31], v[156:159], v[40:47], v5\n"
      "v_smfmac_f32_16x16x64_bf16 v[160:163], v[156:159], v[92:99], v5\n"
      "v_smfmac_f32_16x16x64_bf16 v[24:27], v[156:159], v[216:223], v5\n"
      "v_smfmac_f32_16x16x64_bf16 v[32:35], v[156:159], v[244:251], v5\n"
      "v_smfmac_f32_16x16x64_bf16 v[232:235], v[156:159], v[172:179], v5\n"
      "v_cvt_pk_bf16_f32 v137, v144, v148\n"
      "v_cvt_pk_bf16_f32 v141, v145, v149\n"
      "v_add_f32 v224, v224, v225\n"
      "v_add_f32 v20, v20, v21\n"
      "v_add_f32 v28, v28, v29\n"
      "v_add_f32 v160, v160, v161\n"
      "v_add_f32 v24, v24, v25\n"
      "v_add_f32 v32, v32, v33\n"
      "v_add_f32 v232, v232, v233\n"
      "v_max3_f32 v148, v224, v20, v28\n"
      "v_max3_f32 v148, v148, v160, v24\n"
      "v_max3_f32 v148, v148, v32, v232\n"
      "v_mov_b32 v233, 0\n"
      "v_pk_max_i16 v137, v137, 0\n"
      "v_max_f32_dpp v148, v148, v148 row_ror:8 row_mask:0xf bank_mask:0xf\n"
      "v_pk_max_i16 v141, v141, 0\n"
      "v_cvt_pk_bf16_f32 v139, v146, v150\n"
      "v_max_f32_dpp v148, v148, v148 row_ror:4 row_mask:0xf bank_mask:0xf\n"
      "v_cvt_pk_bf16_f32 v143, v147, v151\n"
      "v_pk_max_i16 v139, v139, 0\n"
      "v_max_f32_dpp v148, v148, v148 row_ror:2 row_mask:0xf bank_mask:0xf\n"
      "v_pk_max_i16 v143, v143, 0\n"
      "buffer_store_dwordx2 v[136:137], v2, s[44:47], s56 offen offset:0 nt sc1\n"
      "v_max_f32_dpp v148, v148, v148 row_ror:1 row_mask:0xf bank_mask:0xf\n"
      "v_sub_f32 v232, v232, v148\n"
      "v_sub_f32 v224, v224, v148\n"
      "v_sub_f32 v20, v20, v148\n"
      "v_exp_f32_dpp v233, v232 quad_perm:[0,1,2,3] row_mask:0xf bank_mask:0x1\n"
      "v_sub_f32 v28, v28, v148\n"
      "v_sub_f32 v160, v160, v148\n"
      "v_mov_b32_dpp v232, v233 quad_perm:[1,1,3,3] row_mask:0xf bank_mask:0xf\n"
      "v_cvt_pk_bf16_f32 v232, v233, v232\n"
      "ds_bpermute_b32 v136, v7, v232 offset:248\n"
      "ds_bpermute_b32 v137, v7, v232 offset:8\n"
      "v_sub_f32 v24, v24, v148\n"
      "v_sub_f32 v32, v32, v148\n"
      "v_exp_f32 v224, v224\n"
      "v_exp_f32 v20, v20\n"
      "v_exp_f32 v28, v28\n"
      "v_exp_f32 v160, v160\n"
      "v_exp_f32 v24, v24\n"
      "v_exp_f32 v32, v32\n"
      "v_cndmask_b32_dpp v225, v28, v224, vcc quad_perm:[0,0,2,2] row_mask:0xf bank_mask:0xf\n"
      "v_cndmask_b32_dpp v21, v160, v20, vcc quad_perm:[0,0,2,2] row_mask:0xf bank_mask:0xf\n"
      "v_cndmask_b32_dpp v25, v32, v24, vcc quad_perm:[0,0,2,2] row_mask:0xf bank_mask:0xf\n"
      "s_not_b64 vcc, vcc\n"
      "v_cndmask_b32_dpp v29, v224, v28, vcc quad_perm:[1,1,3,3] row_mask:0xf bank_mask:0xf\n"
      "v_cndmask_b32_dpp v161, v20, v160, vcc quad_perm:[1,1,3,3] row_mask:0xf bank_mask:0xf\n"
      "v_cvt_pk_bf16_f32 v225, v225, v29\n"
      "v_cvt_pk_bf16_f32 v21, v21, v161\n"
      "ds_bpermute_b32 v148, v6, v225\n"
      "ds_bpermute_b32 v149, v6, v21\n"
      "ds_bpermute_b32 v150, v6, v225 offset:16\n"
      "ds_bpermute_b32 v151, v6, v21 offset:16\n"
      "v_cndmask_b32_dpp v33, v24, v32, vcc quad_perm:[1,1,3,3] row_mask:0xf bank_mask:0xf\n"
      "v_cvt_pk_bf16_f32 v25, v25, v33\n"
      "ds_bpermute_b32 v146, v7, v25\n"
      "ds_bpermute_b32 v147, v7, v25 offset:4\n"
      "buffer_store_dwordx2 v[140:141], v2, s[44:47], s56 offen offset:256 nt sc1\n"
      "buffer_store_dwordx2 v[138:139], v2, s[44:47], s56 offen offset:512 nt sc1\n"
      "s_waitcnt lgkmcnt(6)\n"
      "v_mfma_f32_16x16x4_4b_bf16 v[164:179], v[136:137], v[18:19], 0 cbsz:2 abid:0\n"
      "buffer_store_dwordx2 v[142:143], v2, s[44:47], s56 offen offset:768 nt sc1\n"
      "s_not_b64 vcc, vcc\n"
      "v_add_f32 v224, v224, v20\n"
      "v_add_f32 v28, v28, v160\n"
      "v_add_f32 v24, v24, v32\n"
      "v_add_f32 v224, v224, v28\n"
      "v_add_f32 v24, v24, v233\n"
      "v_add_f32 v142, v224, v24\n"
      "s_add_u32 s56, s48, 24576\n"
      "s_nop 0\n"
      "v_add_f32_dpp v142, v142, v142 row_ror:8 row_mask:0xf bank_mask:0xf\n"
      "s_nop 1\n"
      "v_add_f32_dpp v142, v142, v142 row_ror:4 row_mask:0xf bank_mask:0xf\n"
      "s_nop 1\n"
      "v_add_f32_dpp v142, v142, v142 row_ror:2 row_mask:0xf bank_mask:0xf\n"
      "s_waitcnt lgkmcnt(2)\n"
      "v_smfmac_f32_16x16x64_bf16 v[164:167], v[148:151], v[52:59], v5\n"
      "v_smfmac_f32_16x16x64_bf16 v[168:171], v[148:151], v[128:135], v5\n"
      "v_add_f32_dpp v142, v142, v142 row_ror:1 row_mask:0xf bank_mask:0xf\n"
      "v_smfmac_f32_16x16x64_bf16 v[172:175], v[148:151], v[100:107], v5\n"
      "v_smfmac_f32_16x16x64_bf16 v[176:179], v[148:151], v[192:199], v5\n"
      "v_rcp_f32 v143, v142\n"
      "s_waitcnt lgkmcnt(0)\n"
      "v_smfmac_f32_16x16x32_bf16 v[164:167], v[146:147], v[120:123], v5\n"
      "v_smfmac_f32_16x16x32_bf16 v[168:171], v[146:147], v[68:71], v5\n"
      "s_nop 0\n"
      "v_smfmac_f32_16x16x32_bf16 v[172:175], v[146:147], v[36:39], v5\n"
      "v_smfmac_f32_16x16x32_bf16 v[176:179], v[146:147], v[240:243], v5\n"
      "s_nop 3\n"
      "v_add_f32 v164, v164, v165\n"
      "v_fma_f32 v164, v164, v143, v228\n"
      "v_add_f32 v168, v168, v169\n"
      "v_fma_f32 v168, v168, v143, v229\n"
      "v_cvt_pk_bf16_f32 v164, v164, v168\n"
      "v_pk_max_i16 v164, v164, 0\n"
      "v_add_f32 v172, v172, v173\n"
      "v_fma_f32 v172, v172, v143, v230\n"
      "v_add_f32 v176, v176, v177\n"
      "v_fma_f32 v176, v176, v143, v231\n"
      "v_cvt_pk_bf16_f32 v165, v172, v176\n"
      "v_pk_max_i16 v165, v165, 0\n"
      "buffer_store_dwordx2 v[164:165], v3, s[44:47], s56 offen offset:0 nt sc1\n"
      "s_mov_b32 s48, s49\n"
      "s_add_u32 s49, s49, 13107200\n"
      "s_sub_u32 s50, s50, 1\n"
      "s_cmp_eq_u32 s50, 1\n"
      "s_cselect_b32 s49, 0x80000000, s49\n"
      "s_cmp_lg_u32 s50, 0\n"
      "s_cbranch_scc1 L_safe_%=\n"
      "s_waitcnt vmcnt(0) lgkmcnt(0)\n"
      "L_done_%=:\n"
      "s_endpgm\n"
      ".p2align 12\n"
      :
      : [ka] "s"(__builtin_amdgcn_kernarg_segment_ptr()), [g] "s"(blockIdx.x), [wave] "s"(wave), [ldsb] "s"(ldsb)
      : "v0", "v1", "v2", "v3", "v4", "v5", "v6", "v7", "v8", "v9", "v10", "v11", "v12", "v13", "v14", "v15", "v16", "v17", "v18", "v19", "v20", "v21", "v22", "v23", "v24", "v25", "v26", "v27", "v28", "v29", "v30", "v31", "v32", "v33", "v34", "v35", "v36", "v37", "v38", "v39", "v40", "v41", "v42", "v43", "v44", "v45", "v46", "v47", "v48", "v49", "v50", "v51", "v52", "v53", "v54", "v55", "v56", "v57", "v58", "v59", "v60", "v61", "v62", "v63", "v64", "v65", "v66", "v67", "v68", "v69", "v70", "v71", "v72", "v73", "v74", "v75", "v76", "v77", "v78", "v79", "v80", "v81", "v82", "v83", "v84", "v85", "v86", "v87", "v88", "v89", "v90", "v91", "v92", "v93", "v94", "v95", "v96", "v97", "v98", "v99", "v100", "v101", "v102", "v103", "v104", "v105", "v106", "v107", "v108", "v109", "v110", "v111", "v112", "v113", "v114", "v115", "v116", "v117", "v118", "v119", "v120", "v121", "v122", "v123", "v124", "v125", "v126", "v127", "v128", "v129", "v130", "v131", "v132", "v133", "v134", "v135", "v136", "v137", "v138", "v139", "v140", "v141", "v142", "v143", "v144", "v145", "v146", "v147", "v148", "v149", "v150", "v151", "v152", "v153", "v154", "v155", "v156", "v157", "v158", "v159", "v160", "v161", "v162", "v163", "v164", "v165", "v166", "v167", "v168", "v169", "v170", "v171", "v172", "v173", "v174", "v175", "v176", "v177", "v178", "v179", "v180", "v181", "v182", "v183", "v184", "v185", "v186", "v187", "v188", "v189", "v190", "v191", "v192", "v193", "v194", "v195", "v196", "v197", "v198", "v199", "v200", "v201", "v202", "v203", "v204", "v205", "v206", "v207", "v208", "v209", "v210", "v211", "v212", "v213", "v214", "v215", "v216", "v217", "v218", "v219", "v220", "v221", "v222", "v223", "v224", "v225", "v226", "v227", "v228", "v229", "v230", "v231", "v232", "v233", "v234", "v235", "v236", "v237", "v238", "v239", "v240", "v241", "v242", "v243", "v244", "v245", "v246", "v247", "v248", "v249", "v250", "v251", "v252", "v253", "v254", "v255", "a0", "a1", "a2", "a3", "a4", "a5", "a6", "a7", "a8", "a9", "a10", "a11", "a12", "a13", "a14", "a15", "a16", "a17", "a18", "a19", "a20", "a21", "a22", "a23", "a24", "a25", "a26", "a27", "a28", "a29", "a30", "a31", "a32", "a33", "a34", "a35", "a36", "a37", "a38", "a39", "a40", "a41", "a42", "a43", "a44", "a45", "a46", "a47", "a48", "a49", "a50", "a51", "a52", "a53", "a54", "a55", "a56", "a57", "a58", "a59", "a60", "a61", "a62", "a63", "a64", "a65", "a66", "a67", "a68", "a69", "a70", "a71", "a72", "a73", "a74", "a75", "a76", "a77", "a78", "a79", "a80", "a81", "a82", "a83", "a84", "a85", "a86", "a87", "a88", "a89", "a90", "a91", "a92", "a93", "a94", "a95", "a96", "a97", "a98", "a99", "a100", "a101", "a102", "a103", "a104", "a105", "a106", "a107", "a108", "a109", "a110", "a111", "a112", "a113", "a114", "a115", "a116", "a117", "a118", "a119", "a120", "a121", "a122", "a123", "a124", "a125", "a126", "a127", "a128", "a129", "a130", "a131", "a132", "a133", "a134", "a135", "a136", "a137", "a138", "a139", "a140", "a141", "a142", "a143", "a144", "a145", "a146", "a147", "a148", "a149", "a150", "a151", "a152", "a153", "a154", "a155", "a156", "a157", "a158", "a159", "a160", "a161", "a162", "a163", "a164", "a165", "a166", "a167", "a168", "a169", "a170", "a171", "a172", "a173", "a174", "a175", "a176", "a177", "a178", "a179", "a180", "a181", "a182", "a183", "a184", "a185", "a186", "a187", "a188", "a189", "a190", "a191", "a192", "a193", "a194", "a195", "a196", "a197", "a198", "a199", "a200", "a201", "a202", "a203", "a204", "a205", "a206", "a207", "a208", "a209", "a210", "a211", "a212", "a213", "a214", "a215", "a216", "a217", "a218", "a219", "a220", "a221", "a222", "a223", "a224", "a225", "a226", "a227", "a228", "a229", "a230", "a231", "a232", "a233", "a234", "a235", "a236", "a237", "a238", "a239", "a240", "a241", "a242", "a243", "a244", "a245", "a246", "a247", "a248", "a249", "a250", "a251", "a252", "a253", "a254", "a255", "s4", "s5", "s6", "s7", "s8", "s9", "s10", "s11", "s12", "s13", "s14", "s15", "s16", "s17", "s18", "s19", "s20", "s21", "s22", "s23", "s24", "s25", "s26", "s27", "s28", "s29", "s30", "s31", "s32", "s33", "s38", "s39", "s40", "s41", "s42", "s43", "s44", "s45", "s46", "s47", "s48", "s49", "s50", "s51", "s52", "s53", "s54", "s55", "s56", "s57", "s73", "s74", "s75", "s76", "s77", "s78", "s79", "s90", "s91", "s92", "s93", "s94", "s95", "s100", "s101", "s34", "s35", "vcc", "scc", "m0", "memory");
}

extern "C" void kernel_launch(void* const* d_in, const int* in_sizes, int n_in,
                              void* d_out, int out_size, void* d_ws, size_t ws_size,
                              hipStream_t stream) {
  const uint16_t* e = (const uint16_t*)d_in[0];
  const uint16_t* wq = (const uint16_t*)d_in[1];
  const uint16_t* wk = (const uint16_t*)d_in[2];
  const uint16_t* wv = (const uint16_t*)d_in[3];
  const uint16_t* wres = (const uint16_t*)d_in[4];
  uint16_t* out = (uint16_t*)d_out;
  autoint_kernel<<<256, 256, 0, stream>>>(e, wq, wk, wv, wres, out, (uint8_t*)d_ws);
}
```

```cpp
#include <hip/hip_runtime.h>
#include <stdint.h>

__global__ __launch_bounds__(256, 1)
void autoint_kernel(const uint16_t* __restrict__ e, const uint16_t* __restrict__ wq,
                    const uint16_t* __restrict__ wk, const uint16_t* __restrict__ wv,
                    const uint16_t* __restrict__ wres, uint16_t* out, uint8_t* ws) {
  __shared__ __attribute__((aligned(16))) uint8_t lds[163840];
  const unsigned wave = __builtin_amdgcn_readfirstlane(threadIdx.x >> 6);
  const unsigned ldsb = __builtin_amdgcn_readfirstlane((unsigned)(size_t)lds);
  asm volatile(
      "s_load_dwordx8 s[4:11], %[ka], 0x0\n"
      "s_load_dwordx4 s[12:15], %[ka], 0x20\n"
      "s_load_dwordx2 s[16:17], %[ka], 0x30\n"
      "s_lshl_b32 s22, %[wave], 3\n"
      "s_add_u32 s22, s22, 8\n"
      "s_load_dwordx2 s[20:21], %[ka], s22\n"
      "v_mbcnt_lo_u32_b32 v240, -1, 0\n"
      "v_mbcnt_hi_u32_b32 v240, -1, v240\n"
      "s_getpc_b64 s[50:51]\n"
      "L_ai_pc:\n"
      "s_bfe_u32 s54, %[g], 0x20003\n"
      "s_lshl2_add_u32 s54, s54, %[wave]\n"
      "s_lshl_b32 s54, s54, 13\n"
      "v_lshlrev_b32 v250, 7, v240\n"
      "v_add_u32 v250, s54, v250\n"
      "v_min_u32 v250, L_ai_end-L_ai_pc, v250\n"
      "global_load_dword v17, v250, s[50:51]\n"
      "s_bfe_u32 s19, %[g], 0x10003\n"
      "s_and_b32 s18, %[g], 7\n"
      "s_lshr_b32 s54, %[g], 4\n"
      "s_lshl3_add_u32 s18, s54, s18\n"
      "s_lshl_b32 s18, s18, 2\n"
      "s_add_u32 s18, s18, %[wave]\n"
      "s_mul_i32 s18, s18, 25600\n"
      "s_mov_b32 s46, 32768\n"
      "s_mov_b32 s47, 0x20000\n"
      "v_lshrrev_b32 v234, 3, v240\n"
      "v_and_b32 v235, 7, v240\n"
      "v_lshlrev_b32 v233, 4, v235\n"
      "v_lshl_add_u32 v246, v234, 9, v233\n"
      "v_lshl_add_u32 v236, v234, 10, v233\n"
      "s_lshl_b32 s52, %[wave], 12\n"
      "s_lshl_b32 s55, s19, 14\n"
      "s_add_u32 s52, s52, s55\n"
      "s_lshl_b32 s57, %[wave], 13\n"
      "s_lshl_b32 s55, s19, 7\n"
      "s_add_u32 s57, s57, s55\n"
      "v_lshlrev_b32 v230, 7, v235\n"
      "s_lshr_b32 s23, %[g], 3\n"
      "s_lshl_b32 s23, s23, 10\n"
      "s_sub_u32 s49, s18, 13108224\n"
      "s_ashr_i32 s50, s49, 31\n"
      "s_mov_b32 s42, 104884224\n"
      "s_mov_b32 s43, 0x20000\n"
      "v_and_b32 v241, 15, v240\n"
      "v_lshrrev_b32 v242, 4, v240\n"
      "v_lshrrev_b32 v243, 2, v241\n"
      "v_and_b32 v244, 3, v240\n"
      "s_mul_i32 s53, %[wave], 24576\n"
      "s_add_u32 s53, s53, %[ldsb]\n"
      "s_add_u32 s53, s53, 65536\n"
      "v_and_b32 v248, 2, v234\n"
      "v_xor_b32 v248, v248, v244\n"
      "v_lshlrev_b32 v248, 4, v248\n"
      "v_lshrrev_b32 v249, 5, v240\n"
      "v_lshl_add_u32 v249, v249, 6, v248\n"
      "v_bfe_u32 v248, v240, 2, 3\n"
      "v_lshl_add_u32 v1, v248, 8, v249\n"
      "v_and_b32 v248, 1, v242\n"
      "v_mul_u32_u24 v247, 13106176, v248\n"
      "v_add_u32 v247, v247, v1\n"
      "s_mov_b32 s73, 13128704\n"
      "s_mov_b32 s74, 13127808\n"
      "s_mov_b32 s75, 13124608\n"
      "s_mov_b32 s76, 13123712\n"
      "s_mov_b32 s77, 13120512\n"
      "s_mov_b32 s78, 13119616\n"
      "s_mov_b32 s79, 13132800\n"
      "s_mov_b32 s90, 13116416\n"
      "s_mov_b32 s91, 13115520\n"
      "s_mov_b32 s92, 13112320\n"
      "s_mov_b32 s93, 13111424\n"
      "s_mov_b32 s94, 13108224\n"
      "s_mov_b32 s95, 13107328\n"
      "s_mov_b32 s100, 13312\n"
      "s_mov_b32 s101, 9216\n"
      "s_mov_b32 s34, 5120\n"
      "s_mov_b32 s35, 1024\n"
      "s_mov_b32 s51, 0x3e38aa3b\n"
      "s_lshl_b32 s54, %[wave], 10\n"
      "s_add_u32 s54, s54, %[ldsb]\n"
      "v_lshlrev_b32 v255, 8, v242\n"
      "s_waitcnt lgkmcnt(0)\n"
      "s_mov_b32 s44, s20\n"
      "s_and_b32 s45, s21, 0xffff\n"
      "buffer_load_dword v17, v230, s[44:47], s23 offen\n"
      "s_mov_b32 s44, s8\n s_and_b32 s45, s9, 0xffff\n"
      "buffer_load_dwordx4 v[64:67], v246, s[44:47], s52 offen offset:0\n"
      "s_lshr_b32 s33, %[g], 3\n"
      "s_lshl2_add_u32 s33, s33, %[wave]\n"
      "s_bitcmp1_b32 s33, 6\n"
      "s_cselect_b64 s[24:25], s[14:15], s[4:5]\n"
      "s_and_b32 s33, s33, 63\n"
      "s_lshl_b32 s33, s33, 21\n"
      "s_min_u32 s33, s33, 104857596\n"
      "s_load_dword s32, s[24:25], s33\n"
      "s_add_u32 s40, s4, s49\n"
      "s_addc_u32 s41, s5, s50\n"
      "s_and_b32 s41, s41, 0xffff\n"
      "buffer_load_dwordx4 v[68:71], v246, s[44:47], s52 offen offset:128\n"
      "buffer_load_dwordx4 v[72:75], v246, s[44:47], s52 offen offset:256\n"
      "buffer_load_dwordx4 v[76:79], v246, s[44:47], s52 offen offset:384\n"
      "s_mov_b32 s44, s10\n s_and_b32 s45, s11, 0xffff\n"
      "buffer_load_dwordx4 v[80:83], v246, s[44:47], s52 offen offset:0\n"
      "buffer_load_dwordx4 v[84:87], v246, s[44:47], s52 offen offset:128\n"
      "buffer_load_dwordx4 v[88:91], v246, s[44:47], s52 offen offset:256\n"
      "buffer_load_dwordx4 v[92:95], v246, s[44:47], s52 offen offset:384\n"
      "s_add_u32 m0, s53, 0\n s_mov_b32 s30, 13132800\n"
      "s_nop 0\n"
      "buffer_load_dwordx4 v247, s[40:43], s30 offen offset:0 lds\n"
      "v_lshl_add_u32 v255, v235, 5, v255\n"
      "v_lshrrev_b32 v248, 2, v235\n"
      "v_lshl_add_u32 v255, v248, 4, v255\n"
      "v_and_b32 v248, 1, v234\n"
      "v_lshl_add_u32 v255, v248, 3, v255\n"
      "v_add_u32 v255, s54, v255\n"
      "v_xor_b32 v232, 16, v255\n"
      "v_mov_b32 v238, 0x05040100\n"
      "v_mov_b32 v239, 0x07060302\n"
      "v_xor_b32 v254, v248, v240\n"
      "s_mov_b32 s30, 39345152\n"
      "s_nop 0\n"
      "buffer_load_dwordx4 v247, s[40:43], s30 offen offset:2048 lds\n"
      "v_lshlrev_b32 v254, 4, v254\n"
      "v_add_u32 v254, %[ldsb], v254\n"
      "v_lshlrev_b32 v245, 3, v241\n"
      "v_lshrrev_b32 v248, 3, v241\n"
      "v_lshlrev_b32 v248, 11, v248\n"
      "v_lshl_add_u32 v248, v235, 6, v248\n"
      "v_lshrrev_b32 v249, 1, v241\n"
      "v_and_b32 v249, 2, v249\n"
      "v_xor_b32 v249, v249, v242\n"
      "v_lshl_add_u32 v248, v249, 4, v248\n"
      "s_mov_b32 s30, 13131904\n"
      "s_nop 0\n"
      "buffer_load_dwordx4 v247, s[40:43], s30 offen offset:1024 lds\n"
      "v_add_u32 v0, s53, v248\n"
      "s_lshl_b32 s54, s19, 7\n"
      "v_add_u32 v248, s54, v245\n"
      "v_lshl_add_u32 v2, v242, 10, v248\n"
      "v_lshl_add_u32 v3, v242, 8, v248\n"
      "v_and_b32 v4, 60, v240\n"
      "v_and_b32 v248, 1, v240\n"
      "v_mul_u32_u24 v5, 0xaaaa, v248\n"
      "v_add_u32 v5, 0x4444, v5\n"
      "v_lshlrev_b32 v7, 6, v243\n"
      "s_mov_b32 s30, 39344256\n"
      "s_nop 0\n"
      "buffer_load_dwordx4 v247, s[40:43], s30 offen offset:3072 lds\n"
      "v_lshl_add_u32 v7, v248, 3, v7\n"
      "v_and_b32 v249, 1, v242\n"
      "v_lshl_add_u32 v6, v249, 5, v7\n"
      "v_lshrrev_b32 v250, 1, v242\n"
      "v_lshl_add_u32 v6, v250, 2, v6\n"
      "v_lshl_add_u32 v7, v242, 4, v7\n"
      "v_mov_b32 v8, 1.0\n"
      "s_lshl_b32 s54, %[wave], 14\n"
      "s_add_u32 s54, s54, %[ldsb]\n"
      "v_lshlrev_b32 v16, 10, v242\n"
      "v_lshrrev_b32 v231, 1, v241\n"
      "v_lshl_add_u32 v16, v231, 4, v16\n"
      "v_lshl_add_u32 v16, v248, 2, v16\n"
      "v_add_u32 v16, s54, v16\n"
      "v_lshrrev_b32 v231, 3, v241\n"
      "v_lshl_add_u32 v16, v231, 20, v16\n"
      "v_lshlrev_b32 v9, 9, v242\n"
      "v_add_u32 v9, s54, v9\n"
      "v_lshl_add_u32 v251, v241, 4, v9\n"
      "v_lshl_add_u32 v9, v244, 4, v9\n"
      "s_add_u32 s54, s54, 4096\n"
      "v_lshlrev_b32 v10, 10, v242\n"
      "v_lshl_add_u32 v10, v243, 4, v10\n"
      "v_lshl_add_u32 v10, v248, 9, v10\n"
      "v_add_u32 v10, s54, v10\n"
      "v_lshlrev_b32 v252, 10, v250\n"
      "v_lshl_add_u32 v252, v241, 4, v252\n"
      "v_lshl_add_u32 v252, v249, 3, v252\n"
      "v_add_u32 v252, s54, v252\n"
      "s_add_u32 s54, s54, 4096\n"
      "v_lshlrev_b32 v11, 3, v240\n"
      "v_add_u32 v11, s54, v11\n"
      "v_lshl_add_u32 v253, v242, 9, v245\n"
      "v_add_u32 v253, s54, v253\n"
      "v_mov_b32 v12, 0x3f803f80\n"
      "v_mov_b32 v13, 0x3f803f80\n"
      "v_mov_b32 v14, 0x3f803f80\n"
      "v_mov_b32 v15, 0x3f803f80\n"
      "s_mov_b32 vcc_lo, 0x55555555\n"
      "s_mov_b32 vcc_hi, 0x55555555\n"
      "s_mov_b32 s26, 16384\n"
      "s_mov_b32 s27, 0x20000\n"
      "s_branch L_hs_%=\n"
      "L_hr_%=:\n"
      "s_setpc_b64 vcc\n"
      "L_e5_%=:\n"
      "s_getpc_b64 s[56:57]\n"
      "L_te_%=:\n"
      "s_lshl_b32 s55, %[wave], 12\n"
      "s_add_u32 s55, s55, L_ln_%=-L_te_%=\n"
      "v_mbcnt_lo_u32_b32 v17, -1, 0\n"
      "v_mbcnt_hi_u32_b32 v17, -1, v17\n"
      "v_lshlrev_b32 v17, 6, v17\n"
      "v_add_u32 v17, s55, v17\n"
      "v_min_u32 v17, L_ai_end-L_te_%=, v17\n"
      "global_load_dword v17, v17, s[56:57]\n"
      "s_branch L_task_%=\n"
      "L_hs_%=:\n"
      "s_mov_b32 s38, 0xc000000\n"
      "s_mov_b64 exec, 0\n"
      "s_cmp_eq_u32 %[wave], 0\n"
      "s_cbranch_scc0 L_hn1o0_%=\n"
      "s_call_b64 vcc, L_h0_%=\n"
      "L_hn1o0_%=:\n"
      "s_cmp_eq_u32 %[wave], 1\n"
      "s_cbranch_scc0 L_hn1o1_%=\n"
      "s_call_b64 vcc, L_h1_%=\n"
      "L_hn1o1_%=:\n"
      "s_cmp_eq_u32 %[wave], 2\n"
      "s_cbranch_scc0 L_hn1o2_%=\n"
      "s_call_b64 vcc, L_h2_%=\n"
      "L_hn1o2_%=:\n"
      "s_cmp_eq_u32 %[wave], 3\n"
      "s_cbranch_scc0 L_hn1o3_%=\n"
      "s_nop 0\n"
      "L_hn1o3_%=:\n"
      "s_mov_b64 exec, -1\n"
      "s_mov_b32 vcc_lo, 0x55555555\n"
      "s_mov_b32 vcc_hi, 0x55555555\n"
      "s_add_u32 m0, s53, 20480\n"
      "s_nop 0\n"
      "buffer_load_dwordx4 v1, s[40:43], s73 offen offset:0 lds\n"
      "s_mov_b32 s44, s12\n s_and_b32 s45, s13, 0xffff\n"
      "buffer_load_dwordx4 v1, s[40:43], s73 offen offset:2048 lds\n"
      "s_waitcnt vmcnt(12)\n"
      "v_perm_b32 v116, v68, v64, v238\n"
      "v_perm_b32 v222, v69, v65, v239\n"
      "v_perm_b32 v60, v69, v65, v238\n"
      "s_waitcnt vmcnt(10)\n"
      "v_perm_b32 v193, v79, v75, v238\n"
      "v_perm_b32 v225, v76, v72, v239\n"
      "buffer_load_dwordx4 v1, s[40:43], s74 offen offset:1024 lds\n"
      "buffer_load_dwordx4 v1, s[40:43], s74 offen offset:3072 lds\n"
      "s_mov_b64 exec, 0\n"
      "s_cmp_eq_u32 %[wave], 0\n"
      "s_cbranch_scc0 L_hn2o0_%=\n"
      "s_call_b64 vcc, L_h3_%=\n"
      "s_call_b64 vcc, L_h7_%=\n"
      "s_nop 0\n"
      "L_hn2o0_%=:\n"
      "s_cmp_eq_u32 %[wave], 1\n"
      "s_cbranch_scc0 L_hn2o1_%=\n"
      "s_call_b64 vcc, L_h4_%=\n"
      "s_nop 0\n"
      "s_nop 0\n"
      "L_hn2o1_%=:\n"
      "s_cmp_eq_u32 %[wave], 2\n"
      "s_cbranch_scc0 L_hn2o2_%=\n"
      "s_call_b64 vcc, L_h5_%=\n"
      "s_nop 0\n"
      "s_nop 0\n"
      "L_hn2o2_%=:\n"
      "s_cmp_eq_u32 %[wave], 3\n"
      "s_cbranch_scc0 L_hn2o3_%=\n"
      "s_call_b64 vcc, L_h6_%=\n"
      "s_nop 0\n"
      "s_nop 0\n"
      "L_hn2o3_%=:\n"
      "s_mov_b64 exec, -1\n"
      "s_mov_b32 vcc_lo, 0x55555555\n"
      "s_mov_b32 vcc_hi, 0x55555555\n"
      "v_perm_b32 v50, v70, v66, v239\n"
      "s_waitcnt vmcnt(10)\n"
      "v_perm_b32 v194, v84, v80, v238\n"
      "s_waitcnt vmcnt(8)\n"
      "v_perm_b32 v63, v93, v89, v239\n"
      "v_perm_b32 v195, v92, v88, v238\n"
      "v_perm_b32 v51, v78, v74, v239\n"
      "v_perm_b32 v221, v94, v90, v239\n"
      "v_perm_b32 v226, v85, v81, v238\n"
      "v_perm_b32 v98, v84, v80, v239\n"
      "v_perm_b32 v192, v71, v67, v238\n"
      "v_perm_b32 v61, v77, v73, v238\n"
      "buffer_load_dwordx4 v[228:231], v236, s[44:47], s57 offen offset:0\n"
      "v_perm_b32 v119, v94, v90, v238\n"
      "ds_write_b64 v232, v[50:51] offset:4096\n"
      "v_perm_b32 v118, v86, v82, v238\n"
      "v_perm_b32 v117, v76, v72, v238\n"
      "v_perm_b32 v101, v95, v91, v238\n"
      "ds_write_b64 v255, v[116:117] offset:0\n"
      "buffer_load_dwordx4 v[50:53], v236, s[44:47], s57 offen offset:256\n"
      "v_perm_b32 v227, v93, v89, v238\n"
      "v_perm_b32 v117, v95, v91, v239\n"
      "v_perm_b32 v62, v85, v81, v239\n"
      "v_perm_b32 v100, v87, v83, v238\n"
      "v_perm_b32 v220, v86, v82, v239\n"
      "v_perm_b32 v93, v79, v75, v239\n"
      "ds_write_b64 v232, v[100:101] offset:24576\n"
      "v_perm_b32 v116, v87, v83, v239\n"
      "ds_write_b64 v232, v[118:119] offset:16384\n"
      "v_perm_b32 v75, v78, v74, v238\n"
      "v_perm_b32 v223, v77, v73, v239\n"
      "ds_write_b64 v255, v[222:223] offset:12288\n"
      "ds_write_b64 v255, v[60:61] offset:8192\n"
      "v_perm_b32 v99, v92, v88, v239\n"
      "ds_write_b64 v255, v[98:99] offset:20480\n"
      "ds_write_b64 v232, v[220:221] offset:20480\n"
      "v_perm_b32 v74, v70, v66, v238\n"
      "ds_write_b64 v232, v[74:75] offset:0\n"
      "ds_write_b64 v255, v[226:227] offset:24576\n"
      "ds_write_b64 v255, v[194:195] offset:16384\n"
      "ds_write_b64 v255, v[62:63] offset:28672\n"
      "ds_write_b64 v232, v[116:117] offset:28672\n"
      "v_perm_b32 v224, v68, v64, v239\n"
      "ds_write_b64 v232, v[192:193] offset:8192\n"
      "ds_write_b64 v255, v[224:225] offset:4096\n"
      "v_perm_b32 v92, v71, v67, v239\n"
      "ds_write_b64 v232, v[92:93] offset:12288\n"
      "s_waitcnt lgkmcnt(0)\n"
      "s_barrier\n"
      "buffer_load_dwordx4 v[68:71], v236, s[44:47], s57 offen offset:512\n"
      "buffer_load_dwordx4 v[20:23], v236, s[44:47], s57 offen offset:768\n"
      "ds_read_b128 a[56:59], v254 offset:14336\n"
      "ds_read_b128 a[88:91], v254 offset:22528\n"
      "ds_read_b128 a[104:107], v254 offset:26624\n"
      "ds_read_b128 a[124:127], v254 offset:31744\n"
      "ds_read_b128 a[84:87], v254 offset:21504\n"
      "ds_read_b128 a[0:3], v254 offset:0\n"
      "ds_read_b128 a[48:51], v254 offset:12288\n"
      "ds_read_b128 a[108:111], v254 offset:27648\n"
      "ds_read_b128 a[76:79], v254 offset:19456\n"
      "s_mov_b32 s44, s6\n s_and_b32 s45, s7, 0xffff\n"
      "ds_read_b128 a[60:63], v254 offset:15360\n"
      "ds_read_b128 a[28:31], v254 offset:7168\n"
      "s_waitcnt vmcnt(8)\n"
      "ds_read_b128 v[72:75], v0 offset:0\n"
      "ds_read_b128 a[24:27], v254 offset:6144\n"
      "ds_read_b128 a[8:11], v254 offset:2048\n"
      "ds_read_b128 a[52:55], v254 offset:13312\n"
      "ds_read_b128 a[96:99], v254 offset:24576\n"
      "buffer_load_dwordx4 v[60:63], v246, s[44:47], s52 offen offset:0\n"
      "ds_read_b128 a[116:119], v254 offset:29696\n"
      "buffer_load_dwordx4 v[76:79], v246, s[44:47], s52 offen offset:128\n"
      "ds_read_b128 v[224:227], v0 offset:512\n"
      "ds_read_b128 a[12:15], v254 offset:3072\n"
      "ds_read_b128 a[4:7], v254 offset:1024\n"
      "ds_read_b128 a[72:75], v254 offset:18432\n"
      "ds_read_b128 a[68:71], v254 offset:17408\n"
      "ds_read_b128 v[220:223], v0 offset:1536\n"
      "ds_read_b128 a[40:43], v254 offset:10240\n"
      "s_mov_b32 s30, 65561600\n"
      "ds_read_b128 a[36:39], v254 offset:9216\n"
      "ds_read_b128 v[92:95], v0 offset:1024\n"
      "buffer_load_dwordx4 v[88:91], v246, s[44:47], s52 offen offset:256\n"
      "ds_read_b128 a[64:67], v254 offset:16384\n"
      "buffer_load_dwordx4 v[84:87], v246, s[44:47], s52 offen offset:384\n"
      "ds_read_b128 a[112:115], v254 offset:28672\n"
      "ds_read_b128 a[120:123], v254 offset:30720\n"
      "ds_read_b128 a[100:103], v254 offset:25600\n"
      "ds_read_b128 a[92:95], v254 offset:23552\n"
      "ds_read_b128 a[44:47], v254 offset:11264\n"
      "ds_read_b128 a[20:23], v254 offset:5120\n"
      "s_add_u32 m0, s53, 4096\n"
      "ds_read_b128 a[80:83], v254 offset:20480\n"
      "s_add_u32 s44, s14, s49\n s_addc_u32 s45, s15, s50\n s_and_b32 s45, s45, 0xffff\n s_mov_b32 s46, 13133824\n"
      "ds_read_b128 a[16:19], v254 offset:4096\n"
      "buffer_load_dwordx4 v247, s[40:43], s30 offen offset:0 lds\n"
      "s_waitcnt lgkmcnt(15)\n"
      "v_mfma_f32_16x16x32_bf16 v[116:119], v[72:75], a[96:99], 0\n"
      "s_waitcnt lgkmcnt(5)\n"
      "v_mfma_f32_16x16x32_bf16 v[116:119], v[224:227], a[100:103], v[116:119]\n"
      "v_mfma_f32_16x16x32_bf16 v[116:119], v[92:95], a[104:107], v[116:119]\n"
      "v_mfma_f32_16x16x32_bf16 v[116:119], v[220:223], a[108:111], v[116:119]\n"
      "ds_read_b128 a[32:35], v254 offset:8192\n"
      "v_mfma_f32_16x16x32_bf16 v[64:67], a[0:3], v[72:75], 0\n"
      "v_mfma_f32_16x16x32_bf16 v[64:67], a[4:7], v[224:227], v[64:67]\n"
      "v_mfma_f32_16x16x32_bf16 v[64:67], a[8:11], v[92:95], v[64:67]\n"
      "v_mfma_f32_16x16x32_bf16 v[64:67], a[12:15], v[220:223], v[64:67]\n"
      "s_mov_b32 s30, 91773952\n"
      "buffer_load_dwordx4 v247, s[40:43], s30 offen offset:2048 lds\n"
      "v_mfma_f32_16x16x32_bf16 v[98:101], a[48:51], v[72:75], 0\n"
      "v_mfma_f32_16x16x32_bf16 v[98:101], a[52:55], v[224:227], v[98:101]\n"
      "v_mfma_f32_16x16x32_bf16 v[98:101], a[56:59], v[92:95], v[98:101]\n"
      "v_mfma_f32_16x16x32_bf16 v[98:101], a[60:63], v[220:223], v[98:101]\n"
      "s_waitcnt lgkmcnt(2)\n"
      "v_mfma_f32_16x16x32_bf16 v[136:139], v[72:75], a[80:83], 0\n"
      "v_mfma_f32_16x16x32_bf16 v[136:139], v[224:227], a[84:87], v[136:139]\n"
      "v_mfma_f32_16x16x32_bf16 v[136:139], v[92:95], a[88:91], v[136:139]\n"
      "v_mfma_f32_16x16x32_bf16 v[136:139], v[220:223], a[92:95], v[136:139]\n"
      "v_mfma_f32_16x16x32_bf16 v[204:207], v[72:75], a[112:115], 0\n"
      "s_mov_b32 s30, 65560704\n"
      "v_mfma_f32_16x16x32_bf16 v[204:207], v[224:227], a[116:119], v[204:207]\n"
      "v_mfma_f32_16x16x32_bf16 v[204:207], v[92:95], a[120:123], v[204:207]\n"
      "v_mfma_f32_16x16x32_bf16 v[204:207], v[220:223], a[124:127], v[204:207]\n"
      "buffer_load_dwordx4 v247, s[40:43], s30 offen offset:1024 lds\n"
      "s_waitcnt lgkmcnt(1)\n"
      "v_mfma_f32_16x16x32_bf16 v[104:107], a[16:19], v[72:75], 0\n"
      "v_cvt_pk_bf16_f32 v234, v98, v99\n"
      "v_mfma_f32_16x16x32_bf16 v[104:107], a[20:23], v[224:227], v[104:107]\n"
      "v_mfma_f32_16x16x32_bf16 v[104:107], a[24:27], v[92:95], v[104:107]\n"
      "v_mfma_f32_16x16x32_bf16 v[104:107], a[28:31], v[220:223], v[104:107]\n"
      "s_nop 0\n"
      "v_cvt_pk_bf16_f32 v210, v204, v205\n"
      "s_nop 5\n"
      "v_cvt_pk_bf16_f32 v54, v104, v105\n"
      "s_waitcnt lgkmcnt(0)\n"
      "v_mfma_f32_16x16x32_bf16 v[96:99], a[32:35], v[72:75], 0\n"
      "v_mfma_f32_16x16x32_bf16 v[96:99], a[36:39], v[224:227], v[96:99]\n"
      "v_mfma_f32_16x16x32_bf16 v[96:99], a[40:43], v[92:95], v[96:99]\n"
      "v_mfma_f32_16x16x32_bf16 v[96:99], a[44:47], v[220:223], v[96:99]\n"
      "s_mov_b32 s30, 91773056\n"
      "buffer_load_dwordx4 v247, s[40:43], s30 offen offset:3072 lds\n"
      "s_waitcnt vmcnt(12)\n"
      "ds_read_b128 v[212:215], v0 offset:20480\n"
      "ds_read_b128 v[124:127], v0 offset:20992\n"
      "v_mfma_f32_16x16x32_bf16 v[28:31], v[72:75], a[64:67], 0\n"
      "ds_read_b128 v[188:191], v0 offset:21504\n"
      "ds_read_b128 v[244:247], v0 offset:22016\n"
      "v_mfma_f32_16x16x32_bf16 v[28:31], v[224:227], a[68:71], v[28:31]\n"
      "v_mfma_f32_16x16x32_bf16 v[28:31], v[92:95], a[72:75], v[28:31]\n"
      "v_mfma_f32_16x16x32_bf16 v[28:31], v[220:223], a[76:79], v[28:31]\n"
      "v_cvt_pk_bf16_f32 v233, v98, v99\n"
      "s_add_u32 m0, s53, 16384\n"
      "s_nop 0\n"
      "buffer_load_dwordx4 v1, s[40:43], s75 offen offset:0 nt lds\n"
      "buffer_load_dwordx4 v1, s[40:43], s75 offen offset:2048 nt lds\n"
      "s_waitcnt vmcnt(12)\n"
      "v_perm_b32 v44, v50, v228, v238\n"
      "v_perm_b32 v198, v53, v231, v239\n"
      "v_perm_b32 v104, v52, v230, v238\n"
      "v_perm_b32 v98, v50, v228, v239\n"
      "s_waitcnt lgkmcnt(3)\n"
      "v_mfma_f32_16x16x32_bf16 v[148:151], v[212:215], a[112:115], 0\n"
      "s_waitcnt lgkmcnt(2)\n"
      "v_mfma_f32_16x16x32_bf16 v[148:151], v[124:127], a[116:119], v[148:151]\n"
      "v_perm_b32 v204, v51, v229, v239\n"
      "s_waitcnt lgkmcnt(1)\n"
      "v_mfma_f32_16x16x32_bf16 v[148:151], v[188:191], a[120:123], v[148:151]\n"
      "s_waitcnt lgkmcnt(0)\n"
      "v_mfma_f32_16x16x32_bf16 v[148:151], v[244:247], a[124:127], v[148:151]\n"
      "v_perm_b32 v194, v51, v229, v238\n"
      "v_perm_b32 v50, v52, v230, v239\n"
      "v_mfma_f32_16x16x32_bf16 v[184:187], v[212:215], a[64:67], 0\n"
      "buffer_load_dwordx4 v1, s[40:43], s76 offen offset:1024 nt lds\n"
      "v_mfma_f32_16x16x32_bf16 v[184:187], v[124:127], a[68:71], v[184:187]\n"
      "v_mfma_f32_16x16x32_bf16 v[184:187], v[188:191], a[72:75], v[184:187]\n"
      "s_waitcnt vmcnt(11)\n"
      "v_perm_b32 v51, v22, v70, v239\n"
      "v_perm_b32 v99, v20, v68, v239\n"
      "v_mfma_f32_16x16x32_bf16 v[184:187], v[244:247], a[76:79], v[184:187]\n"
      "ds_write_b64 v232, v[50:51] offset:53248\n"
      "buffer_load_dwordx4 v1, s[40:43], s76 offen offset:3072 nt lds\n"
      "ds_write_b64 v255, v[98:99] offset:53248\n"
      "v_perm_b32 v195, v21, v69, v238\n"
      "s_waitcnt vmcnt(11)\n"
      "v_lshlrev_b32 v228, 16, v60\n"
      "v_lshlrev_b32 v166, 16, v61\n"
      "v_lshlrev_b32 v98, 16, v62\n"
      "v_lshlrev_b32 v236, 16, v63\n"
      "v_mul_f32 v166, s51, v166\n"
      "v_mul_f32 v98, s51, v98\n"
      "v_mfma_f32_16x16x32_bf16 v[48:51], a[16:19], v[212:215], 0\n"
      "v_perm_b32 v105, v22, v70, v238\n"
      "v_and_b32 v82, 0xffff0000, v63\n"
      "v_mfma_f32_16x16x32_bf16 v[48:51], a[20:23], v[124:127], v[48:51]\n"
      "v_perm_b32 v45, v20, v68, v238\n"
      "v_perm_b32 v199, v23, v71, v239\n"
      "v_mfma_f32_16x16x32_bf16 v[48:51], a[24:27], v[188:191], v[48:51]\n"
      "v_and_b32 v230, 0xffff0000, v60\n"
      "v_mul_f32 v230, s51, v230\n"
      "v_mfma_f32_16x16x32_bf16 v[48:51], a[28:31], v[244:247], v[48:51]\n"
      "ds_write_b64 v255, v[44:45] offset:49152\n"
      "ds_write_b64 v255, v[194:195] offset:57344\n"
      "ds_write_b64 v232, v[104:105] offset:49152\n"
      "v_mfma_f32_16x16x32_bf16 v[44:47], a[0:3], v[212:215], 0\n"
      "s_waitcnt vmcnt(9)\n"
      "v_lshlrev_b32 v20, 16, v89\n"
      "v_lshlrev_b32 v104, 16, v91\n"
      "v_mfma_f32_16x16x32_bf16 v[44:47], a[4:7], v[124:127], v[44:47]\n"
      "ds_write_b64 v232, v[198:199] offset:61440\n"
      "v_lshlrev_b32 v99, 16, v78\n"
      "v_lshlrev_b32 v167, 16, v77\n"
      "v_and_b32 v198, 0xffff0000, v90\n"
      "v_and_b32 v164, 0xffff0000, v89\n"
      "v_mul_f32 v198, s51, v198\n"
      "s_waitcnt vmcnt(8)\n"
      "v_lshlrev_b32 v35, 16, v86\n"
      "v_mul_f32 v35, s51, v35\n"
      "v_and_b32 v60, 0xffff0000, v62\n"
      "v_mul_f32 v99, s51, v99\n"
      "v_lshlrev_b32 v229, 16, v76\n"
      "v_mul_f32 v229, s51, v229\n"
      "v_lshlrev_b32 v105, 16, v87\n"
      "v_perm_b32 v68, v53, v231, v238\n"
      "v_and_b32 v199, 0xffff0000, v86\n"
      "v_perm_b32 v205, v21, v69, v239\n"
      "v_and_b32 v231, 0xffff0000, v76\n"
      "v_cvt_pk_bf16_f32 v53, v66, v67\n"
      "v_mul_f32 v167, s51, v167\n"
      "v_mul_f32 v199, s51, v199\n"
      "v_lshlrev_b32 v33, 16, v84\n"
      "v_cvt_pk_bf16_f32 v76, v166, v167\n"
      "v_mul_f32 v231, s51, v231\n"
      "v_and_b32 v62, 0xffff0000, v88\n"
      "v_lshlrev_b32 v34, 16, v90\n"
      "v_and_b32 v67, 0xffff0000, v77\n"
      "v_mul_f32 v34, s51, v34\n"
      "v_and_b32 v165, 0xffff0000, v85\n"
      "v_cvt_pk_bf16_f32 v239, v34, v35\n"
      "v_mul_f32 v228, s51, v228\n"
      "v_and_b32 v66, 0xffff0000, v61\n"
      "v_mul_f32 v20, s51, v20\n"
      "v_mul_f32 v67, s51, v67\n"
      "v_and_b32 v90, 0xffff0000, v91\n"
      "v_cvt_pk_bf16_f32 v89, v198, v199\n"
      "v_mul_f32 v90, s51, v90\n"
      "v_mul_f32 v105, s51, v105\n"
      "v_mul_f32 v104, s51, v104\n"
      "v_mul_f32 v82, s51, v82\n"
      "v_and_b32 v83, 0xffff0000, v79\n"
      "v_mul_f32 v62, s51, v62\n"
      "v_and_b32 v63, 0xffff0000, v84\n"
      "v_and_b32 v91, 0xffff0000, v87\n"
      "v_mul_f32 v91, s51, v91\n"
      "v_mul_f32 v66, s51, v66\n"
      "v_and_b32 v61, 0xffff0000, v78\n"
      "v_mul_f32 v236, s51, v236\n"
      "v_lshlrev_b32 v21, 16, v85\n"
      "v_lshlrev_b32 v237, 16, v79\n"
      "v_mul_f32 v237, s51, v237\n"
      "v_mul_f32 v61, s51, v61\n"
      "v_perm_b32 v69, v23, v71, v238\n"
      "v_mul_f32 v21, s51, v21\n"
      "v_mul_f32 v33, s51, v33\n"
      "v_cvt_pk_bf16_f32 v79, v90, v91\n"
      "v_cvt_pk_bf16_f32 v91, v104, v105\n"
      "v_lshlrev_b32 v32, 16, v88\n"
      "v_cvt_pk_bf16_f32 v238, v98, v99\n"
      "v_mul_f32 v83, s51, v83\n"
      "v_cvt_pk_bf16_f32 v78, v82, v83\n"
      "v_mul_f32 v32, s51, v32\n"
      "v_cvt_pk_bf16_f32 v195, v32, v33\n"
      "v_cvt_pk_bf16_f32 v77, v20, v21\n"
      "v_mul_f32 v63, s51, v63\n"
      "v_cvt_pk_bf16_f32 v90, v236, v237\n"
      "v_cvt_pk_bf16_f32 v194, v228, v229\n"
      "v_mul_f32 v165, s51, v165\n"
      "v_cvt_pk_bf16_f32 v237, v62, v63\n"
      "v_mul_f32 v164, s51, v164\n"
      "v_mul_f32 v60, s51, v60\n"
      "v_cvt_pk_bf16_f32 v88, v60, v61\n"
      "v_cvt_pk_bf16_f32 v61, v164, v165\n"
      "v_cvt_pk_bf16_f32 v60, v66, v67\n"
      "v_cvt_pk_bf16_f32 v236, v230, v231\n"
      "v_mfma_f32_16x16x32_bf16 v[228:231], v[212:215], a[96:99], 0\n"
      "s_add_u32 m0, s53, 12288\n"
      "ds_write_b64 v232, v[90:91] offset:40960\n"
      "ds_write_b64 v255, v[204:205] offset:61440\n"
      "ds_write_b64 v255, v[60:61] offset:45056\n"
      "ds_write_b64 v232, v[238:239] offset:32768\n"
      "ds_write_b64 v255, v[76:77] offset:40960\n"
      "ds_write_b64 v232, v[68:69] offset:57344\n"
      "ds_write_b64 v232, v[88:89] offset:36864\n"
      "ds_write_b64 v232, v[78:79] offset:45056\n"
      "ds_write_b64 v255, v[236:237] offset:36864\n"
      "ds_write_b64 v255, v[194:195] offset:32768\n"
      "buffer_load_dwordx4 v1, s[40:43], s77 offen offset:0 lds\n"
      "v_cvt_pk_bf16_f32 v235, v100, v101\n"
      "v_mfma_f32_16x16x32_bf16 v[236:239], a[48:51], v[212:215], 0\n"
      "v_mfma_f32_16x16x32_bf16 v[236:239], a[52:55], v[124:127], v[236:239]\n"
      "v_mfma_f32_16x16x32_bf16 v[236:239], a[56:59], v[188:191], v[236:239]\n"
      "buffer_load_dwordx4 v1, s[40:43], s77 offen offset:2048 lds\n"
      "v_mfma_f32_16x16x32_bf16 v[236:239], a[60:63], v[244:247], v[236:239]\n"
      "buffer_load_dwordx4 v1, s[40:43], s78 offen offset:1024 lds\n"
      "v_mfma_f32_16x16x32_bf16 v[76:79], v[212:215], a[80:83], 0\n"
      "v_mfma_f32_16x16x32_bf16 v[76:79], v[124:127], a[84:87], v[76:79]\n"
      "v_mfma_f32_16x16x32_bf16 v[76:79], v[188:191], a[88:91], v[76:79]\n"
      "v_mfma_f32_16x16x32_bf16 v[76:79], v[244:247], a[92:95], v[76:79]\n"
      "s_waitcnt lgkmcnt(0)\n"
      "s_barrier\n"
      "ds_read_b128 a[156:159], v254 offset:39936\n"
      "buffer_load_dwordx4 v1, s[40:43], s78 offen offset:3072 lds\n"
      "ds_read_b128 a[144:147], v254 offset:36864\n"
      "v_mfma_f32_16x16x32_bf16 v[164:167], a[32:35], v[212:215], 0\n"
      "ds_read_b128 a[148:151], v254 offset:37888\n"
      "v_mfma_f32_16x16x32_bf16 v[164:167], a[36:39], v[124:127], v[164:167]\n"
      "ds_read_b128 a[152:155], v254 offset:38912\n"
      "v_mfma_f32_16x16x32_bf16 v[164:167], a[40:43], v[188:191], v[164:167]\n"
      "v_mfma_f32_16x16x32_bf16 v[164:167], a[44:47], v[244:247], v[164:167]\n"
      "ds_read_b128 a[180:183], v254 offset:46080\n"
      "v_cvt_pk_bf16_f32 v99, v118, v119\n"
      "v_mfma_f32_16x16x32_bf16 v[228:231], v[124:127], a[100:103], v[228:231]\n"
      "ds_read_b128 a[164:167], v254 offset:41984\n"
      "v_mfma_f32_16x16x32_bf16 v[228:231], v[188:191], a[104:107], v[228:231]\n"
      "ds_read_b128 a[172:175], v254 offset:44032\n"
      "v_mfma_f32_16x16x32_bf16 v[228:231], v[244:247], a[108:111], v[228:231]\n"
      "ds_read_b128 a[176:179], v254 offset:45056\n"
      "v_cvt_pk_bf16_f32 v211, v206, v207\n"
      "v_mfma_f32_16x16x32_bf16 v[44:47], a[8:11], v[188:191], v[44:47]\n"
      "ds_read_b128 a[188:191], v254 offset:48128\n"
      "v_mfma_f32_16x16x32_bf16 v[44:47], a[12:15], v[244:247], v[44:47]\n"
      "ds_read_b128 a[160:163], v254 offset:40960\n"
      "s_waitcnt lgkmcnt(8)\n"
      "v_mfma_f32_16x16x32_bf16 v[88:91], a[144:147], v[72:75], 0\n"
      "ds_read_b128 a[184:187], v254 offset:47104\n"
      "s_waitcnt lgkmcnt(8)\n"
      "v_mfma_f32_16x16x32_bf16 v[88:91], a[148:151], v[224:227], v[88:91]\n"
      "ds_read_b128 a[192:195], v254 offset:49152\n"
      "s_waitcnt lgkmcnt(8)\n"
      "v_mfma_f32_16x16x32_bf16 v[88:91], a[152:155], v[92:95], v[88:91]\n"
      "ds_read_b128 a[204:207], v254 offset:52224\n"
      "s_add_u32 m0, s53, 8192\n"
      "v_mfma_f32_16x16x32_bf16 v[88:91], a[156:159], v[220:223], v[88:91]\n"
      "ds_read_b128 a[248:251], v254 offset:63488\n"
      "v_mfma_f32_16x16x32_bf16 v[68:71], a[144:147], v[212:215], 0\n"
      "ds_read_b128 a[168:171], v254 offset:43008\n"
      "v_cvt_pk_bf16_f32 v82, v28, v29\n"
      "v_mfma_f32_16x16x32_bf16 v[68:71], a[148:151], v[124:127], v[68:71]\n"
      "ds_read_b128 a[216:219], v254 offset:55296\n"
      "v_mfma_f32_16x16x32_bf16 v[68:71], a[152:155], v[188:191], v[68:71]\n"
      "ds_read_b128 a[240:243], v254 offset:61440\n"
      "v_mfma_f32_16x16x32_bf16 v[68:71], a[156:159], v[244:247], v[68:71]\n"
      "ds_read_b128 a[208:211], v254 offset:53248\n"
      "s_waitcnt lgkmcnt(10)\n"
      "v_mfma_f32_16x16x32_bf16 v[84:87], a[176:179], v[72:75], 0\n"
      "ds_read_b128 a[132:135], v254 offset:33792\n"
      "v_mfma_f32_16x16x32_bf16 v[84:87], a[180:183], v[224:227], v[84:87]\n"
      "s_waitcnt lgkmcnt(8)\n"
      "v_mfma_f32_16x16x32_bf16 v[84:87], a[184:187], v[92:95], v[84:87]\n"
      "L_h0_%=:\n"
      "s_cbranch_execz L_hr_%=\n"
      "v_mfma_f32_16x16x32_bf16 v[84:87], a[188:191], v[220:223], v[84:87]\n"
      "ds_read_b128 a[252:255], v254 offset:64512\n"
      "v_cvt_pk_bf16_f32 v52, v64, v65\n"
      "v_mfma_f32_16x16x32_bf16 v[64:67], a[176:179], v[212:215], 0\n"
      "buffer_load_dwordx4 v1, s[40:43], s90 offen offset:0 nt lds\n"
      "v_mfma_f32_16x16x32_bf16 v[64:67], a[180:183], v[124:127], v[64:67]\n"
      "v_cvt_pk_bf16_f32 v255, v90, v91\n"
      "v_mfma_f32_16x16x32_bf16 v[64:67], a[184:187], v[188:191], v[64:67]\n"
      "ds_read_b128 a[128:131], v254 offset:32768\n"
      "v_mfma_f32_16x16x32_bf16 v[64:67], a[188:191], v[244:247], v[64:67]\n"
      "ds_read_b128 a[244:247], v254 offset:62464\n"
      "v_cvt_pk_bf16_f32 v119, v230, v231\n"
      "v_mfma_f32_16x16x32_bf16 v[60:63], a[160:163], v[212:215], 0\n"
      "ds_read_b128 a[224:227], v254 offset:57344\n"
      "v_cvt_pk_bf16_f32 v230, v136, v137\n"
      "v_mfma_f32_16x16x32_bf16 v[60:63], a[164:167], v[124:127], v[60:63]\n"
      "ds_read_b128 a[140:143], v254 offset:35840\n"
      "s_waitcnt lgkmcnt(9)\n"
      "v_mfma_f32_16x16x32_bf16 v[60:63], a[168:171], v[188:191], v[60:63]\n"
      "v_mfma_f32_16x16x32_bf16 v[60:63], a[172:175], v[244:247], v[60:63]\n"
      "ds_read_b128 a[136:139], v254 offset:34816\n"
      "v_mfma_f32_16x16x32_bf16 v[204:207], a[160:163], v[72:75], 0\n"
      "ds_read_b128 a[228:231], v254 offset:58368\n"
      "v_mfma_f32_16x16x32_bf16 v[204:207], a[164:167], v[224:227], v[204:207]\n"
      "v_cvt_pk_bf16_f32 v123, v78, v79\n"
      "v_mfma_f32_16x16x32_bf16 v[204:207], a[168:171], v[92:95], v[204:207]\n"
      "buffer_load_dwordx4 v1, s[40:43], s90 offen offset:2048 nt lds\n"
      "v_mfma_f32_16x16x32_bf16 v[204:207], a[172:175], v[220:223], v[204:207]\n"
      "s_waitcnt lgkmcnt(9)\n"
      "v_mfma_f32_16x16x32_bf16 v[40:43], v[72:75], a[240:243], 0\n"
      "ds_read_b128 a[236:239], v254 offset:60416\n"
      "s_waitcnt lgkmcnt(5)\n"
      "v_mfma_f32_16x16x32_bf16 v[40:43], v[224:227], a[244:247], v[40:43]\n"
      "v_mfma_f32_16x16x32_bf16 v[40:43], v[92:95], a[248:251], v[40:43]\n"
      "ds_read_b128 a[200:203], v254 offset:51200\n"
      "v_cvt_pk_bf16_f32 v199, v88, v89\n"
      "v_mfma_f32_16x16x32_bf16 v[40:43], v[220:223], a[252:255], v[40:43]\n"
      "ds_read_b128 a[196:199], v254 offset:50176\n"
      "v_mfma_f32_16x16x32_bf16 v[88:91], a[128:131], v[72:75], 0\n"
      "v_mfma_f32_16x16x32_bf16 v[88:91], a[132:135], v[224:227], v[88:91]\n"
      "s_waitcnt lgkmcnt(4)\n"
      "v_mfma_f32_16x16x32_bf16 v[88:91], a[136:139], v[92:95], v[88:91]\n"
      "ds_read_b128 a[220:223], v254 offset:56320\n"
      "v_mfma_f32_16x16x32_bf16 v[88:91], a[140:143], v[220:223], v[88:91]\n"
      "buffer_load_dwordx4 v1, s[40:43], s91 offen offset:1024 nt lds\n"
      "v_mfma_f32_16x16x32_bf16 v[32:35], a[128:131], v[212:215], 0\n"
      "ds_read_b128 a[232:235], v254 offset:59392\n"
      "v_mfma_f32_16x16x32_bf16 v[32:35], a[132:135], v[124:127], v[32:35]\n"
      "v_mfma_f32_16x16x32_bf16 v[32:35], a[136:139], v[188:191], v[32:35]\n"
      "s_waitcnt vmcnt(11)\n"
      "ds_read_b128 v[20:23], v0 offset:4096\n"
      "v_mfma_f32_16x16x32_bf16 v[32:35], a[140:143], v[244:247], v[32:35]\n"
      "ds_read_b128 a[212:215], v254 offset:54272\n"
      "ds_read_b128 v[100:103], v0 offset:4608\n"
      "v_mfma_f32_16x16x32_bf16 v[24:27], v[72:75], a[192:195], 0\n"
      "v_cvt_pk_bf16_f32 v198, v88, v89\n"
      "s_waitcnt lgkmcnt(5)\n"
      "v_mfma_f32_16x16x32_bf16 v[24:27], v[224:227], a[196:199], v[24:27]\n"
      "ds_read_b128 v[216:219], v0 offset:5632\n"
      "ds_read_b128 v[112:115], v0 offset:5120\n"
      "v_mfma_f32_16x16x32_bf16 v[24:27], v[92:95], a[200:203], v[24:27]\n"
      "v_cvt_pk_bf16_f32 v231, v138, v139\n"
      "v_mfma_f32_16x16x32_bf16 v[24:27], v[220:223], a[204:207], v[24:27]\n"
      "v_cvt_pk_bf16_f32 v254, v90, v91\n"
      "v_mfma_f32_16x16x32_bf16 v[152:155], v[72:75], a[224:227], 0\n"
      "buffer_load_dwordx4 v1, s[40:43], s91 offen offset:3072 nt lds\n"
      "v_mfma_f32_16x16x32_bf16 v[152:155], v[224:227], a[228:231], v[152:155]\n"
      "s_waitcnt lgkmcnt(5)\n"
      "v_mfma_f32_16x16x32_bf16 v[152:155], v[92:95], a[232:235], v[152:155]\n"
      "v_mfma_f32_16x16x32_bf16 v[152:155], v[220:223], a[236:239], v[152:155]\n"
      "v_mfma_f32_16x16x32_bf16 v[160:163], v[72:75], a[208:211], 0\n"
      "s_waitcnt lgkmcnt(3)\n"
      "v_mfma_f32_16x16x32_bf16 v[160:163], v[224:227], a[212:215], v[160:163]\n"
      "v_cvt_pk_bf16_f32 v140, v164, v165\n"
      "v_mfma_f32_16x16x32_bf16 v[160:163], v[92:95], a[216:219], v[160:163]\n"
      "v_cvt_pk_bf16_f32 v83, v30, v31\n"
      "v_mfma_f32_16x16x32_bf16 v[160:163], v[220:223], a[220:223], v[160:163]\n"
      "v_mfma_f32_16x16x32_bf16 v[194:197], a[176:179], v[20:23], 0\n"
      "s_waitcnt lgkmcnt(2)\n"
      "v_mfma_f32_16x16x32_bf16 v[194:197], a[180:183], v[100:103], v[194:197]\n"
      "s_waitcnt lgkmcnt(0)\n"
      "v_mfma_f32_16x16x32_bf16 v[194:197], a[184:187], v[112:115], v[194:197]\n"
      "v_cvt_pk_bf16_f32 v19, v154, v42\n"
      "v_cvt_pk_bf16_f32 v57, v155, v43\n"
      "v_mfma_f32_16x16x32_bf16 v[194:197], a[188:191], v[216:219], v[194:197]\n"
      "s_barrier\n"
      "v_mfma_f32_16x16x32_bf16 v[78:81], v[20:23], a[192:195], 0\n"
      "v_mfma_f32_16x16x32_bf16 v[78:81], v[100:103], a[196:199], v[78:81]\n"
      "v_mfma_f32_16x16x32_bf16 v[78:81], v[112:115], a[200:203], v[78:81]\n"
      "v_mfma_f32_16x16x32_bf16 v[78:81], v[216:219], a[204:207], v[78:81]\n"
      "ds_write_b64 v253, v[210:211] offset:384\n"
      "v_mfma_f32_16x16x32_bf16 v[28:31], v[20:23], a[80:83], 0\n"
      "v_mfma_f32_16x16x32_bf16 v[28:31], v[100:103], a[84:87], v[28:31]\n"
      "v_cvt_pk_bf16_f32 v55, v106, v107\n"
      "v_mfma_f32_16x16x32_bf16 v[28:31], v[112:115], a[88:91], v[28:31]\n"
      "v_cvt_pk_bf16_f32 v232, v96, v97\n"
      "v_mfma_f32_16x16x32_bf16 v[28:31], v[216:219], a[92:95], v[28:31]\n"
      "ds_write_b64 v252, v[254:255] offset:512\n"
      "v_mfma_f32_16x16x32_bf16 v[104:107], a[160:163], v[20:23], 0\n"
      "v_mfma_f32_16x16x32_bf16 v[104:107], a[164:167], v[100:103], v[104:107]\n"
      "v_mfma_f32_16x16x32_bf16 v[104:107], a[168:171], v[112:115], v[104:107]\n"
      "v_mfma_f32_16x16x32_bf16 v[104:107], a[172:175], v[216:219], v[104:107]\n"
      "ds_write_b64 v253, v[230:231] offset:128\n"
      "v_mfma_f32_16x16x32_bf16 v[224:227], v[20:23], a[96:99], 0\n"
      "s_add_u32 m0, s53, 4096\n"
      "v_mfma_f32_16x16x32_bf16 v[224:227], v[100:103], a[100:103], v[224:227]\n"
      "v_mfma_f32_16x16x32_bf16 v[224:227], v[112:115], a[104:107], v[224:227]\n"
      "v_cvt_pk_bf16_f32 v92, v60, v61\n"
      "v_mfma_f32_16x16x32_bf16 v[224:227], v[216:219], a[108:111], v[224:227]\n"
      "ds_write_b128 v251, v[52:55] offset:0\n"
      "buffer_load_dwordx4 v1, s[40:43], s92 offen offset:0 lds\n"
      "v_cvt_pk_bf16_f32 v89, v34, v35\n"
      "v_mfma_f32_16x16x32_bf16 v[154:157], v[20:23], a[112:115], 0\n"
      "v_mfma_f32_16x16x32_bf16 v[154:157], v[100:103], a[116:119], v[154:157]\n"
      "v_mfma_f32_16x16x32_bf16 v[154:157], v[112:115], a[120:123], v[154:157]\n"
      "v_mfma_f32_16x16x32_bf16 v[154:157], v[216:219], a[124:127], v[154:157]\n"
      "s_nop 0\n"
      "v_cvt_pk_bf16_f32 v254, v224, v225\n"
      "v_cvt_pk_bf16_f32 v54, v184, v185\n"
      "v_mfma_f32_16x16x32_bf16 v[200:203], v[20:23], a[64:67], 0\n"
      "buffer_load_dwordx4 v1, s[40:43], s92 offen offset:2048 lds\n"
      "v_mfma_f32_16x16x32_bf16 v[200:203], v[100:103], a[68:71], v[200:203]\n"
      "v_cvt_pk_bf16_f32 v164, v24, v160\n"
      "v_mfma_f32_16x16x32_bf16 v[200:203], v[112:115], a[72:75], v[200:203]\n"
      "v_cvt_pk_bf16_f32 v56, v27, v163\n"
      "v_cvt_pk_bf16_f32 v230, v148, v149\n"
      "v_mfma_f32_16x16x32_bf16 v[200:203], v[216:219], a[76:79], v[200:203]\n"
      "v_cvt_pk_bf16_f32 v184, v204, v205\n"
      "v_cvt_pk_bf16_f32 v165, v152, v40\n"
      "v_mfma_f32_16x16x32_bf16 v[208:211], a[0:3], v[20:23], 0\n"
      "s_waitcnt vmcnt(10)\n"
      "ds_read_b128 v[172:175], v0 offset:16384\n"
      "ds_read_b128 v[128:131], v0 offset:17408\n"
      "v_mfma_f32_16x16x32_bf16 v[208:211], a[4:7], v[100:103], v[208:211]\n"
      "v_mfma_f32_16x16x32_bf16 v[208:211], a[8:11], v[112:115], v[208:211]\n"
      "buffer_load_dwordx4 v1, s[40:43], s93 offen offset:1024 lds\n"
      "v_mfma_f32_16x16x32_bf16 v[208:211], a[12:15], v[216:219], v[208:211]\n"
      "v_mfma_f32_16x16x32_bf16 v[180:183], v[20:23], a[240:243], 0\n"
      "v_cvt_pk_bf16_f32 v138, v48, v49\n"
      "v_mfma_f32_16x16x32_bf16 v[180:183], v[100:103], a[244:247], v[180:183]\n"
      "v_cvt_pk_bf16_f32 v231, v150, v151\n"
      "v_cvt_pk_bf16_f32 v27, v156, v157\n"
      "v_mfma_f32_16x16x32_bf16 v[180:183], v[112:115], a[248:251], v[180:183]\n"
      "v_mfma_f32_16x16x32_bf16 v[180:183], v[216:219], a[252:255], v[180:183]\n"
      "v_cvt_pk_bf16_f32 v18, v26, v162\n"
      "v_cvt_pk_bf16_f32 v136, v44, v45\n"
      "v_mfma_f32_16x16x32_bf16 v[176:179], a[16:19], v[20:23], 0\n"
      "ds_read_b128 v[36:39], v0 offset:17920\n"
      "ds_read_b128 v[72:75], v0 offset:16896\n"
      "v_mfma_f32_16x16x32_bf16 v[176:179], a[20:23], v[100:103], v[176:179]\n"
      "v_mfma_f32_16x16x32_bf16 v[176:179], a[24:27], v[112:115], v[176:179]\n"
      "v_cvt_pk_bf16_f32 v98, v116, v117\n"
      "v_mfma_f32_16x16x32_bf16 v[176:179], a[28:31], v[216:219], v[176:179]\n"
      "ds_write_b64 v253, v[164:165] offset:4096\n"
      "v_mfma_f32_16x16x32_bf16 v[58:61], a[128:131], v[20:23], 0\n"
      "v_mfma_f32_16x16x32_bf16 v[58:61], a[132:135], v[100:103], v[58:61]\n"
      "v_cvt_pk_bf16_f32 v26, v154, v155\n"
      "v_mfma_f32_16x16x32_bf16 v[58:61], a[136:139], v[112:115], v[58:61]\n"
      "v_mfma_f32_16x16x32_bf16 v[58:61], a[140:143], v[216:219], v[58:61]\n"
      "ds_write_b64 v253, v[82:83] offset:0\n"
      "v_cvt_pk_bf16_f32 v91, v70, v71\n"
      "v_mfma_f32_16x16x32_bf16 v[154:157], a[48:51], v[20:23], 0\n"
      "v_mfma_f32_16x16x32_bf16 v[154:157], a[52:55], v[100:103], v[154:157]\n"
      "v_mfma_f32_16x16x32_bf16 v[154:157], a[56:59], v[112:115], v[154:157]\n"
      "buffer_load_dwordx4 v1, s[40:43], s93 offen offset:3072 lds\n"
      "v_mfma_f32_16x16x32_bf16 v[154:157], a[60:63], v[216:219], v[154:157]\n"
      "ds_write_b64 v253, v[26:27] offset:2432\n"
      "v_mfma_f32_16x16x32_bf16 v[162:165], v[20:23], a[224:227], 0\n"
      "v_cvt_pk_bf16_f32 v185, v84, v85\n"
      "s_add_u32 m0, s53, 0\n"
      "v_mfma_f32_16x16x32_bf16 v[162:165], v[100:103], a[228:231], v[162:165]\n"
      "v_cvt_pk_bf16_f32 v88, v32, v33\n"
      "v_mfma_f32_16x16x32_bf16 v[162:165], v[112:115], a[232:235], v[162:165]\n"
      "v_mfma_f32_16x16x32_bf16 v[162:165], v[216:219], a[236:239], v[162:165]\n"
      "ds_write_b64 v252, v[198:199] offset:0\n"
      "v_cvt_pk_bf16_f32 v26, v154, v155\n"
      "v_mfma_f32_16x16x32_bf16 v[82:85], a[32:35], v[20:23], 0\n"
      "buffer_load_dwordx4 v1, s[40:43], s94 offen offset:0 nt lds\n"
      "v_mfma_f32_16x16x32_bf16 v[82:85], a[36:39], v[100:103], v[82:85]\n"
      "v_mfma_f32_16x16x32_bf16 v[82:85], a[40:43], v[112:115], v[82:85]\n"
      "buffer_load_dwordx4 v1, s[40:43], s94 offen offset:2048 nt lds\n"
      "v_mfma_f32_16x16x32_bf16 v[82:85], a[44:47], v[216:219], v[82:85]\n"
      "v_cvt_pk_bf16_f32 v221, v210, v211\n"
      "v_cvt_pk_bf16_f32 v160, v206, v207\n"
      "v_mfma_f32_16x16x32_bf16 v[240:243], v[20:23], a[208:211], 0\n"
      "v_cvt_pk_bf16_f32 v137, v46, v47\n"
      "v_cvt_pk_bf16_f32 v255, v226, v227\n"
      "v_mfma_f32_16x16x32_bf16 v[240:243], v[100:103], a[212:215], v[240:243]\n"
      "v_cvt_pk_bf16_f32 v71, v165, v183\n"
      "v_cvt_pk_bf16_f32 v27, v156, v157\n"
      "v_mfma_f32_16x16x32_bf16 v[240:243], v[112:115], a[216:219], v[240:243]\n"
      "v_mfma_f32_16x16x32_bf16 v[240:243], v[216:219], a[220:223], v[240:243]\n"
      "ds_write_b64 v253, v[98:99] offset:256\n"
      "s_waitcnt lgkmcnt(8)\n"
      "v_mfma_f32_16x16x32_bf16 v[154:157], a[16:19], v[172:175], 0\n"
      "s_waitcnt lgkmcnt(5)\n"
      "v_mfma_f32_16x16x32_bf16 v[154:157], a[20:23], v[72:75], v[154:157]\n"
      "v_cvt_pk_bf16_f32 v110, v200, v201\n"
      "v_mfma_f32_16x16x32_bf16 v[154:157], a[24:27], v[128:131], v[154:157]\n"
      "v_mfma_f32_16x16x32_bf16 v[154:157], a[28:31], v[36:39], v[154:157]\n"
      "v_mfma_f32_16x16x32_bf16 v[42:45], a[144:147], v[20:23], 0\n"
      "buffer_load_dwordx4 v1, s[40:43], s95 offen offset:1024 nt lds\n"
      "v_mfma_f32_16x16x32_bf16 v[42:45], a[148:151], v[100:103], v[42:45]\n"
      "v_cvt_pk_bf16_f32 v222, v176, v177\n"
      "v_mfma_f32_16x16x32_bf16 v[42:45], a[152:155], v[112:115], v[42:45]\n"
      "v_cvt_pk_bf16_f32 v142, v236, v237\n"
      "v_cvt_pk_bf16_f32 v111, v202, v203\n"
      "v_mfma_f32_16x16x32_bf16 v[42:45], a[156:159], v[216:219], v[42:45]\n"
      "v_cvt_pk_bf16_f32 v183, v194, v195\n"
      "v_cvt_pk_bf16_f32 v95, v66, v67\n"
      "v_mfma_f32_16x16x32_bf16 v[200:203], a[144:147], v[172:175], 0\n"
      "v_cvt_pk_bf16_f32 v205, v164, v182\n"
      "v_cvt_pk_bf16_f32 v113, v153, v41\n"
      "v_mfma_f32_16x16x32_bf16 v[200:203], a[148:151], v[72:75], v[200:203]\n"
      "v_mfma_f32_16x16x32_bf16 v[200:203], a[152:155], v[128:131], v[200:203]\n"
      "v_mfma_f32_16x16x32_bf16 v[200:203], a[156:159], v[36:39], v[200:203]\n"
      "v_cvt_pk_bf16_f32 v139, v50, v51\n"
      "v_cvt_pk_bf16_f32 v93, v62, v63\n"
      "v_mfma_f32_16x16x32_bf16 v[20:23], v[172:175], a[112:115], 0\n"
      "v_cvt_pk_bf16_f32 v143, v238, v239\n"
      "v_cvt_pk_bf16_f32 v164, v28, v29\n"
      "v_mfma_f32_16x16x32_bf16 v[20:23], v[72:75], a[116:119], v[20:23]\n"
      "v_mfma_f32_16x16x32_bf16 v[20:23], v[128:131], a[120:123], v[20:23]\n"
      "v_cvt_pk_bf16_f32 v112, v25, v161\n"
      "v_mfma_f32_16x16x32_bf16 v[20:23], v[36:39], a[124:127], v[20:23]\n"
      "ds_write_b64 v253, v[112:113] offset:4224\n"
      "v_cvt_pk_bf16_f32 v66, v154, v155\n"
      "v_mfma_f32_16x16x32_bf16 v[236:239], a[48:51], v[172:175], 0\n"
      "s_waitcnt vmcnt(11)\n"
      "ds_read_b128 v[48:51], v0 offset:12288\n"
      "ds_read_b128 v[224:227], v0 offset:13824\n"
      "v_mfma_f32_16x16x32_bf16 v[236:239], a[52:55], v[72:75], v[236:239]\n"
      "buffer_load_dwordx4 v1, s[40:43], s95 offen offset:3072 nt lds\n"
      "v_mfma_f32_16x16x32_bf16 v[236:239], a[56:59], v[128:131], v[236:239]\n"
      "v_cvt_pk_bf16_f32 v55, v186, v187\n"
      "v_cvt_pk_bf16_f32 v161, v86, v87\n"
      "v_mfma_f32_16x16x32_bf16 v[236:239], a[60:63], v[36:39], v[236:239]\n"
      "s_add_u32 m0, s53, 20480\n"
      "ds_write_b128 v251, v[232:235] offset:2048\n"
      "s_add_u32 s40, s40, 13107200\n s_addc_u32 s41, s41, 0\n"
      "ds_read_b128 v[216:219], v0 offset:12800\n"
      "v_mfma_f32_16x16x32_bf16 v[112:115], a[160:163], v[172:175], 0\n"
      "v_mfma_f32_16x16x32_bf16 v[112:115], a[164:167], v[72:75], v[112:115]\n"
      "v_mfma_f32_16x16x32_bf16 v[112:115], a[168:171], v[128:131], v[112:115]\n"
      "buffer_load_dwordx4 v1, s[40:43], s73 offen offset:0 lds\n"
      "v_mfma_f32_16x16x32_bf16 v[112:115], a[172:175], v[36:39], v[112:115]\n"
      "v_cvt_pk_bf16_f32 v70, v81, v243\n"
      "v_cvt_pk_bf16_f32 v148, v79, v241\n"
      "v_mfma_f32_16x16x32_bf16 v[232:235], v[172:175], a[96:99], 0\n"
      "v_mfma_f32_16x16x32_bf16 v[232:235], v[72:75], a[100:103], v[232:235]\n"
      "v_cvt_pk_bf16_f32 v149, v163, v181\n"
      "v_cvt_pk_bf16_f32 v90, v68, v69\n"
      "v_mfma_f32_16x16x32_bf16 v[232:235], v[128:131], a[104:107], v[232:235]\n"
      "v_cvt_pk_bf16_f32 v103, v202, v203\n"
      "v_mfma_f32_16x16x32_bf16 v[232:235], v[36:39], a[108:111], v[232:235]\n"
      "ds_write_b64 v253, v[70:71] offset:6528\n"
      "v_mfma_f32_16x16x32_bf16 v[96:99], a[32:35], v[172:175], 0\n"
      "v_cvt_pk_bf16_f32 v203, v44, v45\n"
      "v_cvt_pk_bf16_f32 v67, v156, v157\n"
      "v_mfma_f32_16x16x32_bf16 v[96:99], a[36:39], v[72:75], v[96:99]\n"
      "buffer_load_dwordx4 v1, s[40:43], s74 offen offset:1024 lds\n"
      "v_mfma_f32_16x16x32_bf16 v[96:99], a[40:43], v[128:131], v[96:99]\n"
      "v_mfma_f32_16x16x32_bf16 v[96:99], a[44:47], v[36:39], v[96:99]\n"
      "v_cvt_pk_bf16_f32 v71, v238, v239\n"
      "v_cvt_pk_bf16_f32 v25, v84, v85\n"
      "v_mfma_f32_16x16x32_bf16 v[156:159], v[172:175], a[64:67], 0\n"
      "ds_read_b128 v[132:135], v0 offset:13312\n"
      "v_cvt_pk_bf16_f32 v116, v232, v233\n"
      "v_mfma_f32_16x16x32_bf16 v[156:159], v[72:75], a[68:71], v[156:159]\n"
      "v_cvt_pk_bf16_f32 v28, v106, v107\n"
      "v_cvt_pk_bf16_f32 v94, v64, v65\n"
      "v_mfma_f32_16x16x32_bf16 v[156:159], v[128:131], a[72:75], v[156:159]\n"
      "v_mfma_f32_16x16x32_bf16 v[156:159], v[36:39], a[76:79], v[156:159]\n"
      "ds_write_b64 v253, v[18:19] offset:4352\n"
      "v_mfma_f32_16x16x32_bf16 v[62:65], a[128:131], v[172:175], 0\n"
      "v_cvt_pk_bf16_f32 v141, v166, v167\n"
      "v_cvt_pk_bf16_f32 v238, v78, v240\n"
      "v_mfma_f32_16x16x32_bf16 v[62:65], a[132:135], v[72:75], v[62:65]\n"
      "v_cvt_pk_bf16_f32 v78, v58, v59\n"
      "v_cvt_pk_bf16_f32 v165, v30, v31\n"
      "v_mfma_f32_16x16x32_bf16 v[62:65], a[136:139], v[128:131], v[62:65]\n"
      "v_mfma_f32_16x16x32_bf16 v[62:65], a[140:143], v[36:39], v[62:65]\n"
      "v_cvt_pk_bf16_f32 v53, v158, v159\n"
      "v_cvt_pk_bf16_f32 v118, v228, v229\n"
      "v_mfma_f32_16x16x32_bf16 v[152:155], v[136:139], v[88:91], 0\n"
      "v_mfma_f32_16x16x32_bf16 v[152:155], v[140:143], v[92:95], v[152:155]\n"
      "ds_write_b64 v253, v[110:111] offset:2048\n"
      "s_waitcnt lgkmcnt(7)\n"
      "v_mfma_f32_16x16x32_bf16 v[84:87], v[48:51], a[112:115], 0\n"
      "v_cvt_pk_bf16_f32 v182, v104, v105\n"
      "s_waitcnt lgkmcnt(4)\n"
      "v_mfma_f32_16x16x32_bf16 v[84:87], v[216:219], a[116:119], v[84:87]\n"
      "v_cvt_pk_bf16_f32 v101, v64, v65\n"
      "s_waitcnt lgkmcnt(2)\n"
      "v_mfma_f32_16x16x32_bf16 v[84:87], v[132:135], a[120:123], v[84:87]\n"
      "v_cvt_pk_bf16_f32 v100, v62, v63\n"
      "v_mfma_f32_16x16x32_bf16 v[84:87], v[224:227], a[124:127], v[84:87]\n"
      "ds_write_b64 v252, v[182:183] offset:2304\n"
      "v_cvt_pk_bf16_f32 v69, v98, v99\n"
      "v_mfma_f32_16x16x32_bf16 v[44:47], a[32:35], v[48:51], 0\n"
      "v_mfma_f32_16x16x32_bf16 v[44:47], a[36:39], v[216:219], v[44:47]\n"
      "v_cvt_pk_bf16_f32 v228, v20, v21\n"
      "v_mfma_f32_16x16x32_bf16 v[44:47], a[40:43], v[132:135], v[44:47]\n"
      "v_cvt_pk_bf16_f32 v52, v156, v157\n"
      "v_mfma_f32_16x16x32_bf16 v[44:47], a[44:47], v[224:227], v[44:47]\n"
      "ds_write_b64 v252, v[160:161] offset:2560\n"
      "v_mfma_f32_16x16x32_bf16 v[18:21], v[48:51], a[96:99], 0\n"
      "v_cvt_pk_bf16_f32 v117, v234, v235\n"
      "v_mfma_f32_16x16x32_bf16 v[18:21], v[216:219], a[100:103], v[18:21]\n"
      "v_cvt_pk_bf16_f32 v202, v60, v61\n"
      "v_cvt_pk_bf16_f32 v239, v162, v180\n"
      "v_mfma_f32_16x16x32_bf16 v[18:21], v[132:135], a[104:107], v[18:21]\n"
      "buffer_load_dwordx4 v1, s[40:43], s74 offen offset:3072 lds\n"
      "v_mfma_f32_16x16x32_bf16 v[18:21], v[224:227], a[108:111], v[18:21]\n"
      "ds_write_b64 v253, v[148:149] offset:6272\n"
      "v_cvt_pk_bf16_f32 v104, v112, v113\n"
      "v_mfma_f32_16x16x32_bf16 v[160:163], a[0:3], v[172:175], 0\n"
      "v_mfma_f32_16x16x32_bf16 v[160:163], a[4:7], v[72:75], v[160:163]\n"
      "v_mfma_f32_16x16x32_bf16 v[160:163], a[8:11], v[128:131], v[160:163]\n"
      "v_cvt_pk_bf16_f32 v102, v200, v201\n"
      "v_mfma_f32_16x16x32_bf16 v[160:163], a[12:15], v[36:39], v[160:163]\n"
      "ds_write_b64 v253, v[56:57] offset:4480\n"
      "v_mfma_f32_16x16x32_bf16 v[156:159], v[172:175], a[80:83], 0\n"
      "v_mfma_f32_16x16x32_bf16 v[156:159], v[72:75], a[84:87], v[156:159]\n"
      "v_cvt_pk_bf16_f32 v81, v46, v47\n"
      "v_mfma_f32_16x16x32_bf16 v[156:159], v[128:131], a[88:91], v[156:159]\n"
      "v_mfma_f32_16x16x32_bf16 v[156:159], v[36:39], a[92:95], v[156:159]\n"
      "ds_write_b64 v252, v[184:185] offset:2048\n"
      "ds_read_b128 v[232:235], v9 offset:0\n"
      "v_cvt_pk_bf16_f32 v70, v236, v237\n"
      "v_mfma_f32_16x16x32_bf16 v[60:63], a[176:179], v[172:175], 0\n"
      "v_exp_f32 v155, v155\n"
      "v_mfma_f32_16x16x32_bf16 v[60:63], a[180:183], v[72:75], v[60:63]\n"
      "v_mfma_f32_16x16x32_bf16 v[60:63], a[184:187], v[128:131], v[60:63]\n"
      "v_cvt_pk_bf16_f32 v68, v96, v97\n"
      "v_mfma_f32_16x16x32_bf16 v[60:63], a[188:191], v[36:39], v[60:63]\n"
      "ds_write_b64 v252, v[202:203] offset:768\n"
      "v_mfma_f32_16x16x32_bf16 v[56:59], v[48:51], a[64:67], 0\n"
      "s_waitcnt vmcnt(11)\n"
      "ds_read_b128 v[192:195], v0 offset:8192\n"
      "ds_read_b128 v[184:187], v0 offset:9216\n"
      "v_mfma_f32_16x16x32_bf16 v[56:59], v[216:219], a[68:71], v[56:59]\n"
      "v_cvt_pk_bf16_f32 v204, v80, v242\n"
      "v_cvt_pk_bf16_f32 v120, v156, v157\n"
      "v_mfma_f32_16x16x32_bf16 v[56:59], v[132:135], a[72:75], v[56:59]\n"
      "v_mfma_f32_16x16x32_bf16 v[56:59], v[224:227], a[76:79], v[56:59]\n"
      "ds_read_b128 v[240:243], v0 offset:9728\n"
      "ds_read_b128 v[200:203], v0 offset:8704\n"
      "v_mfma_f32_16x16x32_bf16 v[110:113], a[48:51], v[48:51], 0\n"
      "v_cvt_pk_bf16_f32 v65, v162, v163\n"
      "v_cvt_pk_bf16_f32 v29, v196, v197\n"
      "v_mfma_f32_16x16x32_bf16 v[110:113], a[52:55], v[216:219], v[110:113]\n"
      "v_cvt_pk_bf16_f32 v106, v60, v61\n"
      "v_cvt_pk_bf16_f32 v107, v62, v63\n"
      "v_mfma_f32_16x16x32_bf16 v[110:113], a[56:59], v[132:135], v[110:113]\n"
      "v_cvt_pk_bf16_f32 v64, v160, v161\n"
      "v_mfma_f32_16x16x32_bf16 v[110:113], a[60:63], v[224:227], v[110:113]\n"
      "ds_write_b64 v252, v[28:29] offset:2816\n"
      "v_cvt_pk_bf16_f32 v220, v208, v209\n"
      "v_mfma_f32_16x16x32_bf16 v[180:183], a[16:19], v[48:51], 0\n"
      "v_mfma_f32_16x16x32_bf16 v[180:183], a[20:23], v[216:219], v[180:183]\n"
      "v_mfma_f32_16x16x32_bf16 v[180:183], a[24:27], v[132:135], v[180:183]\n"
      "v_mfma_f32_16x16x32_bf16 v[180:183], a[28:31], v[224:227], v[180:183]\n"
      "ds_read_b128 v[32:35], v10 offset:0\n"
      "v_cvt_pk_bf16_f32 v24, v82, v83\n"
      "v_mfma_f32_16x16x32_bf16 v[160:163], v[64:67], v[88:91], 0\n"
      "v_exp_f32 v152, v152\n"
      "v_mfma_f32_16x16x32_bf16 v[160:163], v[68:71], v[92:95], v[160:163]\n"
      "v_exp_f32 v154, v154\n"
      "v_mfma_f32_16x16x32_bf16 v[96:99], a[0:3], v[48:51], 0\n"
      "v_cvt_pk_bf16_f32 v63, v20, v21\n"
      "v_mov_b64 v[144:145], 0\n"
      "v_mfma_f32_16x16x32_bf16 v[96:99], a[4:7], v[216:219], v[96:99]\n"
      "v_exp_f32 v153, v153\n"
      "v_mfma_f32_16x16x32_bf16 v[96:99], a[8:11], v[132:135], v[96:99]\n"
      "buffer_load_dwordx4 v1, s[40:43], s73 offen offset:2048 lds\n"
      "v_mfma_f32_16x16x32_bf16 v[96:99], a[12:15], v[224:227], v[96:99]\n"
      "ds_write_b64 v253, v[164:165] offset:2176\n"
      "s_waitcnt lgkmcnt(6)\n"
      "v_mfma_f32_16x16x32_bf16 v[166:169], a[48:51], v[192:195], 0\n"
      "s_waitcnt lgkmcnt(3)\n"
      "v_mfma_f32_16x16x32_bf16 v[166:169], a[52:55], v[200:203], v[166:169]\n"
      "v_cvt_pk_bf16_f32 v122, v76, v77\n"
      "v_mfma_f32_16x16x32_bf16 v[166:169], a[56:59], v[184:187], v[166:169]\n"
      "s_nop 0\n"
      "v_cvt_pk_bf16_f32 v77, v98, v99\n"
      "v_cvt_pk_bf16_f32 v223, v178, v179\n"
      "v_mfma_f32_16x16x32_bf16 v[166:169], a[60:63], v[240:243], v[166:169]\n"
      "s_add_u32 m0, s53, 16384\n"
      "ds_write_b64 v253, v[238:239] offset:6144\n"
      "v_cvt_pk_bf16_f32 v79, v42, v43\n"
      "v_mfma_f32_16x16x32_bf16 v[28:31], a[0:3], v[192:195], 0\n"
      "ds_read_b128 v[236:239], v9 offset:2048\n"
      "v_cvt_pk_bf16_f32 v121, v158, v159\n"
      "v_mfma_f32_16x16x32_bf16 v[28:31], a[4:7], v[200:203], v[28:31]\n"
      "v_mfma_f32_16x16x32_bf16 v[28:31], a[8:11], v[184:187], v[28:31]\n"
      "buffer_load_dwordx4 v1, s[40:43], s76 offen offset:1024 nt lds\n"
      "v_mfma_f32_16x16x32_bf16 v[28:31], a[12:15], v[240:243], v[28:31]\n"
      "ds_write_b64 v252, v[78:79] offset:256\n"
      "v_mfma_f32_16x16x32_bf16 v[208:211], a[32:35], v[192:195], 0\n"
      "v_cvt_pk_bf16_f32 v76, v96, v97\n"
      "v_mfma_f32_16x16x32_bf16 v[208:211], a[36:39], v[200:203], v[208:211]\n"
      "v_cvt_pk_bf16_f32 v82, v110, v111\n"
      "v_cvt_pk_bf16_f32 v80, v44, v45\n"
      "v_mfma_f32_16x16x32_bf16 v[208:211], a[40:43], v[184:187], v[208:211]\n"
      "v_mfma_f32_16x16x32_bf16 v[208:211], a[44:47], v[240:243], v[208:211]\n"
      "ds_write_b128 v251, v[24:27] offset:2304\n"
      "v_cvt_pk_bf16_f32 v20, v28, v29\n"
      "v_exp_f32 v160, v160\n"
      "v_mfma_f32_16x16x32_bf16 v[156:159], v[192:195], a[96:99], 0\n"
      "v_exp_f32 v163, v163\n"
      "v_mfma_f32_16x16x32_bf16 v[156:159], v[200:203], a[100:103], v[156:159]\n"
      "v_mfma_f32_16x16x32_bf16 v[156:159], v[184:187], a[104:107], v[156:159]\n"
      "v_exp_f32 v161, v161\n"
      "v_mfma_f32_16x16x32_bf16 v[156:159], v[240:243], a[108:111], v[156:159]\n"
      "v_mfma_f32_16x16x32_bf16 v[176:179], a[16:19], v[192:195], 0\n"
      "v_cvt_pk_bf16_f32 v25, v210, v211\n"
      "v_mfma_f32_16x16x32_bf16 v[176:179], a[20:23], v[200:203], v[176:179]\n"
      "v_cvt_pk_bf16_f32 v26, v166, v167\n"
      "v_cvt_pk_bf16_f32 v105, v114, v115\n"
      "v_mfma_f32_16x16x32_bf16 v[176:179], a[24:27], v[184:187], v[176:179]\n"
      "v_mfma_f32_16x16x32_bf16 v[176:179], a[28:31], v[240:243], v[176:179]\n"
      "v_cvt_pk_bf16_f32 v79, v182, v183\n"
      "v_cvt_pk_bf16_f32 v21, v30, v31\n"
      "v_mfma_f32_16x16x32_bf16 v[44:47], v[192:195], a[112:115], 0\n"
      "v_cvt_pk_bf16_f32 v62, v18, v19\n"
      "v_mfma_f32_16x16x32_bf16 v[44:47], v[200:203], a[116:119], v[44:47]\n"
      "s_waitcnt vmcnt(9)\n"
      "ds_read_b128 v[40:43], v0 offset:4096\n"
      "ds_read_b128 v[96:99], v0 offset:5632\n"
      "v_mfma_f32_16x16x32_bf16 v[44:47], v[184:187], a[120:123], v[44:47]\n"
      "v_mfma_f32_16x16x32_bf16 v[44:47], v[240:243], a[124:127], v[44:47]\n"
      "ds_write_b64 v253, v[254:255] offset:2304\n"
      "s_waitcnt lgkmcnt(5)\n"
      "v_smfmac_f32_16x16x64_bf16 v[144:147], v[32:35], v[232:239], v5\n"
      "ds_read_b128 v[108:111], v0 offset:4608\n"
      "v_cvt_pk_bf16_f32 v229, v22, v23\n"
      "v_mfma_f32_16x16x32_bf16 v[196:199], v[192:195], a[80:83], 0\n"
      "ds_read_b128 v[164:167], v0 offset:5120\n"
      "v_cvt_pk_bf16_f32 v24, v208, v209\n"
      "v_mfma_f32_16x16x32_bf16 v[196:199], v[200:203], a[84:87], v[196:199]\n"
      "buffer_load_dwordx4 v1, s[40:43], s76 offen offset:3072 nt lds\n"
      "v_mfma_f32_16x16x32_bf16 v[196:199], v[184:187], a[88:91], v[196:199]\n"
      "v_cvt_pk_bf16_f32 v22, v176, v177\n"
      "v_mfma_f32_16x16x32_bf16 v[196:199], v[240:243], a[92:95], v[196:199]\n"
      "v_cvt_pk_bf16_f32 v237, v46, v47\n"
      "v_mfma_f32_16x16x32_bf16 v[208:211], v[192:195], a[64:67], 0\n"
      "v_exp_f32 v162, v162\n"
      "v_mfma_f32_16x16x32_bf16 v[208:211], v[200:203], a[68:71], v[208:211]\n"
      "v_cvt_pk_bf16_f32 v176, v160, v161\n"
      "v_cvt_pk_bf16_f32 v23, v178, v179\n"
      "v_mfma_f32_16x16x32_bf16 v[208:211], v[184:187], a[72:75], v[208:211]\n"
      "v_mov_b64 v[192:193], 0\n"
      "v_cvt_pk_bf16_f32 v60, v156, v157\n"
      "v_mfma_f32_16x16x32_bf16 v[208:211], v[240:243], a[76:79], v[208:211]\n"
      "ds_write_b64 v253, v[204:205] offset:6400\n"
      "v_mov_b64 v[28:29], 0\n"
      "v_mfma_f32_16x16x32_bf16 v[232:235], v[48:51], a[80:83], 0\n"
      "v_mfma_f32_16x16x32_bf16 v[232:235], v[216:219], a[84:87], v[232:235]\n"
      "v_cvt_pk_bf16_f32 v61, v158, v159\n"
      "v_cvt_pk_bf16_f32 v177, v162, v163\n"
      "v_mfma_f32_16x16x32_bf16 v[232:235], v[132:135], a[88:91], v[232:235]\n"
      "v_mov_b64 v[202:203], 0\n"
      "v_mfma_f32_16x16x32_bf16 v[232:235], v[224:227], a[92:95], v[232:235]\n"
      "v_cvt_pk_bf16_f32 v115, v58, v59\n"
      "v_add_f32 v144, v144, v145\n"
      "v_smfmac_f32_16x16x64_bf16 v[202:205], v[32:35], v[136:143], v5\n"
      "s_waitcnt lgkmcnt(5)\n"
      "v_mfma_f32_16x16x32_bf16 v[160:163], a[32:35], v[40:43], 0\n"
      "v_cvt_pk_bf16_f32 v179, v154, v155\n"
      "s_waitcnt lgkmcnt(2)\n"
      "v_mfma_f32_16x16x32_bf16 v[160:163], a[36:39], v[108:111], v[160:163]\n"
      "s_waitcnt lgkmcnt(1)\n"
      "v_mfma_f32_16x16x32_bf16 v[160:163], a[40:43], v[164:167], v[160:163]\n"
      "v_cvt_pk_bf16_f32 v27, v168, v169\n"
      "v_mfma_f32_16x16x32_bf16 v[160:163], a[44:47], v[96:99], v[160:163]\n"
      "v_mov_b32 v145, 0\n"
      "v_cvt_pk_bf16_f32 v187, v234, v235\n"
      "v_mfma_f32_16x16x32_bf16 v[252:255], v[40:43], a[112:115], 0\n"
      "v_mfma_f32_16x16x32_bf16 v[252:255], v[108:111], a[116:119], v[252:255]\n"
      "v_add_u32 v10, 64, v10\n"
      "v_mfma_f32_16x16x32_bf16 v[252:255], v[164:167], a[120:123], v[252:255]\n"
      "v_cvt_pk_bf16_f32 v78, v180, v181\n"
      "v_cvt_pk_bf16_f32 v83, v112, v113\n"
      "v_mfma_f32_16x16x32_bf16 v[252:255], v[96:99], a[124:127], v[252:255]\n"
      "v_exp_f32_dpp v145, v144 quad_perm:[0,1,2,3] row_mask:0xf bank_mask:0x1\n"
      "v_mfma_f32_16x16x32_bf16 v[154:157], v[76:79], v[88:91], 0\n"
      "v_cvt_pk_bf16_f32 v239, v86, v87\n"
      "v_mfma_f32_16x16x32_bf16 v[154:157], v[80:83], v[92:95], v[154:157]\n"
      "v_cvt_pk_bf16_f32 v114, v56, v57\n"
      "v_smfmac_f32_16x16x64_bf16 v[192:195], v[32:35], v[64:71], v5\n"
      "buffer_load_dwordx4 v1, s[40:43], s75 offen offset:0 nt lds\n"
      "v_mfma_f32_16x16x32_bf16 v[168:171], a[48:51], v[40:43], 0\n"
      "v_cvt_pk_bf16_f32 v234, v252, v253\n"
      "v_cvt_pk_bf16_f32 v112, v208, v209\n"
      "v_mfma_f32_16x16x32_bf16 v[168:171], a[52:55], v[108:111], v[168:171]\n"
      "s_nop 0\n"
      "v_exp_f32 v157, v157\n"
      "v_mfma_f32_16x16x32_bf16 v[168:171], a[56:59], v[164:167], v[168:171]\n"
      "v_cvt_pk_bf16_f32 v185, v198, v199\n"
      "v_cvt_pk_bf16_f32 v184, v196, v197\n"
      "v_mfma_f32_16x16x32_bf16 v[168:171], a[60:63], v[96:99], v[168:171]\n"
      "v_mov_b64 v[146:147], 0\n"
      "v_mfma_f32_16x16x32_bf16 v[56:59], v[20:23], v[88:91], 0\n"
      "v_cvt_pk_bf16_f32 v236, v44, v45\n"
      "v_mfma_f32_16x16x32_bf16 v[56:59], v[24:27], v[92:95], v[56:59]\n"
      "ds_write_b128 v251, v[220:223] offset:256\n"
      "v_cvt_pk_bf16_f32 v208, v160, v161\n"
      "v_cvt_pk_bf16_f32 v209, v162, v163\n"
      "v_mfma_f32_16x16x32_bf16 v[160:163], v[40:43], a[80:83], 0\n"
      "v_cvt_pk_bf16_f32 v113, v210, v211\n"
      "v_cvt_pk_bf16_f32 v238, v84, v85\n"
      "v_mfma_f32_16x16x32_bf16 v[160:163], v[108:111], a[84:87], v[160:163]\n"
      "v_cvt_pk_bf16_f32 v210, v168, v169\n"
      "v_cvt_pk_bf16_f32 v235, v254, v255\n"
      "v_mfma_f32_16x16x32_bf16 v[160:163], v[164:167], a[88:91], v[160:163]\n"
      "v_mfma_f32_16x16x32_bf16 v[160:163], v[96:99], a[92:95], v[160:163]\n"
      "v_exp_f32 v56, v56\n"
      "v_mfma_f32_16x16x32_bf16 v[194:197], v[40:43], a[64:67], 0\n"
      "v_exp_f32 v58, v58\n"
      "v_mfma_f32_16x16x32_bf16 v[194:197], v[108:111], a[68:71], v[194:197]\n"
      "v_exp_f32 v59, v59\n"
      "v_mfma_f32_16x16x32_bf16 v[194:197], v[164:167], a[72:75], v[194:197]\n"
      "v_mfma_f32_16x16x32_bf16 v[194:197], v[96:99], a[76:79], v[194:197]\n"
      "v_exp_f32 v154, v154\n"
      "v_mfma_f32_16x16x32_bf16 v[84:87], a[0:3], v[40:43], 0\n"
      "v_cvt_pk_bf16_f32 v186, v232, v233\n"
      "v_add_f32 v192, v192, v193\n"
      "v_mfma_f32_16x16x32_bf16 v[84:87], a[4:7], v[108:111], v[84:87]\n"
      "v_add_f32 v202, v202, v203\n"
      "v_mfma_f32_16x16x32_bf16 v[84:87], a[8:11], v[164:167], v[84:87]\n"
      "buffer_load_dwordx4 v1, s[40:43], s75 offen offset:2048 nt lds\n"
      "v_mfma_f32_16x16x32_bf16 v[84:87], a[12:15], v[96:99], v[84:87]\n"
      "v_cvt_pk_bf16_f32 v178, v152, v153\n"
      "v_smfmac_f32_16x16x64_bf16 v[28:31], v[32:35], v[76:83], v5\n"
      "s_waitcnt vmcnt(8)\n"
      "ds_read_b128 v[44:47], v0 offset:0\n"
      "v_mfma_f32_16x16x32_bf16 v[180:183], v[40:43], a[96:99], 0\n"
      "L_h1_%=:\n"
      "s_cbranch_execz L_hr_%=\n"
      "ds_read_b128 v[248:251], v0 offset:512\n"
      "ds_read_b128 v[252:255], v0 offset:1024\n"
      "v_mfma_f32_16x16x32_bf16 v[180:183], v[108:111], a[100:103], v[180:183]\n"
      "v_exp_f32 v57, v57\n"
      "v_mfma_f32_16x16x32_bf16 v[180:183], v[164:167], a[104:107], v[180:183]\n"
      "ds_read_b128 v[240:243], v0 offset:1536\n"
      "v_cvt_pk_bf16_f32 v211, v170, v171\n"
      "v_mfma_f32_16x16x32_bf16 v[180:183], v[96:99], a[108:111], v[180:183]\n"
      "v_exp_f32 v156, v156\n"
      "v_smfmac_f32_16x16x64_bf16 v[146:149], v[32:35], v[20:27], v5\n"
      "v_add_f32 v28, v28, v29\n"
      "v_mfma_f32_16x16x32_bf16 v[168:171], a[16:19], v[40:43], 0\n"
      "v_mfma_f32_16x16x32_bf16 v[168:171], a[20:23], v[108:111], v[168:171]\n"
      "v_exp_f32 v202, v202\n"
      "v_mfma_f32_16x16x32_bf16 v[168:171], a[24:27], v[164:167], v[168:171]\n"
      "v_exp_f32 v28, v28\n"
      "v_mfma_f32_16x16x32_bf16 v[168:171], a[28:31], v[96:99], v[168:171]\n"
      "v_cvt_pk_bf16_f32 v99, v156, v157\n"
      "s_waitcnt lgkmcnt(3)\n"
      "v_mfma_f32_16x16x32_bf16 v[108:111], v[44:47], a[112:115], 0\n"
      "s_waitcnt lgkmcnt(2)\n"
      "v_mfma_f32_16x16x32_bf16 v[108:111], v[248:251], a[116:119], v[108:111]\n"
      "v_cvt_pk_bf16_f32 v97, v58, v59\n"
      "s_waitcnt lgkmcnt(1)\n"
      "v_mfma_f32_16x16x32_bf16 v[108:111], v[252:255], a[120:123], v[108:111]\n"
      "s_waitcnt lgkmcnt(0)\n"
      "v_mfma_f32_16x16x32_bf16 v[108:111], v[240:243], a[124:127], v[108:111]\n"
      "v_cvt_pk_bf16_f32 v204, v84, v85\n"
      "v_cvt_pk_bf16_f32 v205, v86, v87\n"
      "v_mfma_f32_16x16x32_bf16 v[40:43], a[32:35], v[44:47], 0\n"
      "v_mfma_f32_16x16x32_bf16 v[40:43], a[36:39], v[248:251], v[40:43]\n"
      "v_exp_f32 v192, v192\n"
      "v_mfma_f32_16x16x32_bf16 v[40:43], a[40:43], v[252:255], v[40:43]\n"
      "v_mfma_f32_16x16x32_bf16 v[40:43], a[44:47], v[240:243], v[40:43]\n"
      "v_cvt_pk_bf16_f32 v206, v168, v169\n"
      "v_cvt_pk_bf16_f32 v207, v170, v171\n"
      "v_mfma_f32_16x16x32_bf16 v[164:167], a[48:51], v[44:47], 0\n"
      "s_add_u32 m0, s53, 12288\n"
      "v_exp_f32 v155, v155\n"
      "v_mfma_f32_16x16x32_bf16 v[164:167], a[52:55], v[248:251], v[164:167]\n"
      "s_nop 1\n"
      "v_cvt_pk_bf16_f32 v169, v42, v43\n"
      "v_mfma_f32_16x16x32_bf16 v[164:167], a[56:59], v[252:255], v[164:167]\n"
      "v_cvt_pk_bf16_f32 v98, v154, v155\n"
      "v_cvt_pk_bf16_f32 v232, v108, v109\n"
      "v_mfma_f32_16x16x32_bf16 v[164:167], a[60:63], v[240:243], v[164:167]\n"
      "v_cvt_pk_bf16_f32 v96, v56, v57\n"
      "v_cvt_pk_bf16_f32 v233, v110, v111\n"
      "v_mfma_f32_16x16x32_bf16 v[156:159], a[16:19], v[44:47], 0\n"
      "buffer_load_dwordx4 v1, s[40:43], s77 offen offset:0 lds\n"
      "v_mfma_f32_16x16x32_bf16 v[156:159], a[20:23], v[248:251], v[156:159]\n"
      "v_mov_b32_dpp v144, v145 quad_perm:[1,1,3,3] row_mask:0xf bank_mask:0xf\n"
      "v_cvt_pk_bf16_f32 v111, v196, v197\n"
      "v_mfma_f32_16x16x32_bf16 v[156:159], a[24:27], v[252:255], v[156:159]\n"
      "v_cvt_pk_bf16_f32 v59, v182, v183\n"
      "v_mfma_f32_16x16x32_bf16 v[156:159], a[28:31], v[240:243], v[156:159]\n"
      "v_cvt_pk_bf16_f32 v168, v40, v41\n"
      "v_cvt_pk_bf16_f32 v110, v194, v195\n"
      "v_mfma_f32_16x16x32_bf16 v[196:199], a[0:3], v[44:47], 0\n"
      "v_cvt_pk_bf16_f32 v58, v180, v181\n"
      "v_mfma_f32_16x16x32_bf16 v[196:199], a[4:7], v[248:251], v[196:199]\n"
      "v_mfma_f32_16x16x32_bf16 v[196:199], a[8:11], v[252:255], v[196:199]\n"
      "v_cvt_pk_bf16_f32 v183, v162, v163\n"
      "v_cvt_pk_bf16_f32 v182, v160, v161\n"
      "v_mfma_f32_16x16x32_bf16 v[196:199], a[12:15], v[240:243], v[196:199]\n"
      "v_cvt_pk_bf16_f32 v171, v166, v167\n"
      "v_cvt_pk_bf16_f32 v166, v156, v157\n"
      "v_mfma_f32_16x16x32_bf16 v[160:163], v[44:47], a[64:67], 0\n"
      "v_cvt_pk_bf16_f32 v167, v158, v159\n"
      "v_cvt_pk_bf16_f32 v170, v164, v165\n"
      "v_mfma_f32_16x16x32_bf16 v[160:163], v[248:251], a[68:71], v[160:163]\n"
      "v_mov_b64 v[156:157], 0\n"
      "v_cvt_pk_bf16_f32 v144, v145, v144\n"
      "v_mfma_f32_16x16x32_bf16 v[160:163], v[252:255], a[72:75], v[160:163]\n"
      "v_cndmask_b32_dpp v193, v202, v192, vcc quad_perm:[0,0,2,2] row_mask:0xf bank_mask:0xf\n"
      "v_cvt_pk_bf16_f32 v164, v196, v197\n"
      "v_mfma_f32_16x16x32_bf16 v[160:163], v[240:243], a[76:79], v[160:163]\n"
      "v_mov_b64 v[84:85], 0\n"
      "v_cvt_pk_bf16_f32 v165, v198, v199\n"
      "v_smfmac_f32_16x16x64_bf16 v[156:159], v[32:35], v[204:211], v5\n"
      "ds_read2_b32 v[222:223], v16 offset0:128 offset1:130\n"
      "ds_read2_b32 v[220:221], v16 offset0:0 offset1:2\n"
      "v_smfmac_f32_16x16x64_bf16 v[84:87], v[32:35], v[164:171], v5\n"
      "s_nop 1\n"
      "v_cvt_pk_bf16_f32 v108, v160, v161\n"
      "v_mfma_f32_16x16x32_bf16 v[196:199], v[44:47], a[80:83], 0\n"
      "v_add_f32 v146, v146, v147\n"
      "v_cvt_pk_bf16_f32 v109, v162, v163\n"
      "v_mfma_f32_16x16x32_bf16 v[196:199], v[248:251], a[84:87], v[196:199]\n"
      "v_add_f32 v156, v156, v157\n"
      "v_add_f32 v84, v84, v85\n"
      "v_mfma_f32_16x16x32_bf16 v[196:199], v[252:255], a[88:91], v[196:199]\n"
      "v_exp_f32 v146, v146\n"
      "v_mfma_f32_16x16x32_bf16 v[196:199], v[240:243], a[92:95], v[196:199]\n"
      "buffer_load_dwordx4 v1, s[40:43], s77 offen offset:2048 lds\n"
      "v_mfma_f32_16x16x32_bf16 v[158:161], v[44:47], a[96:99], 0\n"
      "v_exp_f32 v84, v84\n"
      "v_mfma_f32_16x16x32_bf16 v[158:161], v[248:251], a[100:103], v[158:161]\n"
      "v_exp_f32 v156, v156\n"
      "v_mfma_f32_16x16x32_bf16 v[158:161], v[252:255], a[104:107], v[158:161]\n"
      "s_nop 0\n"
      "v_cndmask_b32_dpp v157, v28, v156, vcc quad_perm:[0,0,2,2] row_mask:0xf bank_mask:0xf\n"
      "v_cndmask_b32_dpp v85, v146, v84, vcc quad_perm:[0,0,2,2] row_mask:0xf bank_mask:0xf\n"
      "v_mfma_f32_16x16x32_bf16 v[158:161], v[240:243], a[108:111], v[158:161]\n"
      "s_not_b64 vcc, vcc\n"
      "v_cndmask_b32_dpp v203, v192, v202, vcc quad_perm:[1,1,3,3] row_mask:0xf bank_mask:0xf\n"
      "v_mov_b64 v[248:249], 0\n"
      "v_cndmask_b32_dpp v147, v84, v146, vcc quad_perm:[1,1,3,3] row_mask:0xf bank_mask:0xf\n"
      "v_cvt_pk_bf16_f32 v193, v193, v203\n"
      "v_mfma_f32_16x16x32_bf16 v[148:151], v[164:167], v[88:91], 0\n"
      "v_cndmask_b32_dpp v29, v156, v28, vcc quad_perm:[1,1,3,3] row_mask:0xf bank_mask:0xf\n"
      "v_cvt_pk_bf16_f32 v180, v196, v197\n"
      "v_mfma_f32_16x16x32_bf16 v[148:151], v[168:171], v[92:95], v[148:151]\n"
      "s_waitcnt lgkmcnt(0)\n"
      "v_mov_b32_dpp v44, v220 row_ror:8 row_mask:0xf bank_mask:0xf\n"
      "v_add_f32 v192, v192, v202\n"
      "v_mov_b64 v[250:251], 0\n"
      "v_mfma_f32_16x16x32_bf16 v[240:243], v[164:167], v[100:103], 0\n"
      "v_add_f32 v84, v84, v156\n"
      "v_cvt_pk_bf16_f32 v157, v157, v29\n"
      "v_mfma_f32_16x16x32_bf16 v[240:243], v[168:171], v[104:107], v[240:243]\n"
      "v_add_f32 v192, v192, v145\n"
      "v_mov_b32_dpp v45, v221 row_ror:8 row_mask:0xf bank_mask:0xf\n"
      "v_mov_b32_dpp v47, v223 row_ror:8 row_mask:0xf bank_mask:0xf\n"
      "v_mfma_f32_16x16x32_bf16 v[152:155], v[204:207], v[88:91], 0\n"
      "v_cvt_pk_bf16_f32 v56, v158, v159\n"
      "v_add_f32 v146, v146, v28\n"
      "v_mfma_f32_16x16x32_bf16 v[152:155], v[208:211], v[92:95], v[152:155]\n"
      "v_cvt_pk_bf16_f32 v85, v85, v147\n"
      "v_exp_f32 v242, v242\n"
      "v_add_f32 v84, v84, v146\n"
      "v_mov_b32_dpp v46, v222 row_ror:8 row_mask:0xf bank_mask:0xf\n"
      "v_smfmac_f32_16x16x64_bf16 v[248:251], v[220:223], v[88:95], v5\n"
      "ds_bpermute_b32 v43, v6, v157 offset:16\n"
      "v_smfmac_f32_16x16x64_bf16 v[248:251], v[44:47], v[100:107], v5\n"
      "ds_bpermute_b32 v255, v7, v193 offset:4\n"
      "v_exp_f32 v240, v240\n"
      "v_mfma_f32_16x16x32_bf16 v[32:35], v[76:79], v[100:103], 0\n"
      "v_exp_f32 v243, v243\n"
      "v_mfma_f32_16x16x32_bf16 v[32:35], v[80:83], v[104:107], v[32:35]\n"
      "v_exp_f32 v148, v148\n"
      "v_mfma_f32_16x16x32_bf16 v[200:203], v[204:207], v[100:103], 0\n"
      "ds_bpermute_b32 v41, v6, v157\n"
      "v_mfma_f32_16x16x32_bf16 v[200:203], v[208:211], v[104:107], v[200:203]\n"
      "ds_bpermute_b32 v254, v7, v193\n"
      "v_add_f32 v90, v84, v192\n"
      "v_mfma_f32_16x16x32_bf16 v[156:159], v[136:139], v[100:103], 0\n"
      "s_nop 0\n"
      "v_exp_f32 v35, v35\n"
      "v_mfma_f32_16x16x32_bf16 v[156:159], v[140:143], v[104:107], v[156:159]\n"
      "v_exp_f32 v34, v34\n"
      "v_mfma_f32_16x16x32_bf16 v[192:195], v[20:23], v[100:103], 0\n"
      "ds_bpermute_b32 v40, v6, v85\n"
      "v_mfma_f32_16x16x32_bf16 v[192:195], v[24:27], v[104:107], v[192:195]\n"
      "v_add_f32 v250, v250, v251\n"
      "v_exp_f32 v201, v201\n"
      "v_exp_f32 v149, v149\n"
      "v_cvt_pk_bf16_f32 v57, v160, v161\n"
      "v_mfma_f32_16x16x32_bf16 v[92:95], a[128:131], v[48:51], 0\n"
      "ds_bpermute_b32 v42, v6, v85 offset:16\n"
      "v_mfma_f32_16x16x32_bf16 v[92:95], a[132:135], v[216:219], v[92:95]\n"
      "v_exp_f32 v158, v158\n"
      "v_mfma_f32_16x16x32_bf16 v[92:95], a[136:139], v[132:135], v[92:95]\n"
      "v_exp_f32 v150, v150\n"
      "v_mfma_f32_16x16x32_bf16 v[92:95], a[140:143], v[224:227], v[92:95]\n"
      "v_exp_f32 v192, v192\n"
      "v_exp_f32 v155, v155\n"
      "v_exp_f32 v153, v153\n"
      "v_add_f32 v248, v248, v249\n"
      "v_exp_f32 v248, v248\n"
      "v_exp_f32 v250, v250\n"
      "v_mfma_f32_16x16x32_bf16 v[86:89], v[64:67], v[100:103], 0\n"
      "v_exp_f32 v152, v152\n"
      "v_mfma_f32_16x16x32_bf16 v[86:89], v[68:71], v[104:107], v[86:89]\n"
      "v_exp_f32 v195, v195\n"
      "v_exp_f32 v154, v154\n"
      "v_mfma_f32_16x16x32_bf16 v[100:103], a[144:147], v[48:51], 0\n"
      "v_exp_f32 v157, v157\n"
      "v_mfma_f32_16x16x32_bf16 v[100:103], a[148:151], v[216:219], v[100:103]\n"
      "v_exp_f32 v241, v241\n"
      "v_mfma_f32_16x16x32_bf16 v[100:103], a[152:155], v[132:135], v[100:103]\n"
      "v_exp_f32 v203, v203\n"
      "v_mfma_f32_16x16x32_bf16 v[100:103], a[156:159], v[224:227], v[100:103]\n"
      "v_cvt_pk_bf16_f32 v181, v198, v199\n"
      "v_exp_f32 v89, v89\n"
      "v_exp_f32 v200, v200\n"
      "v_exp_f32 v194, v194\n"
      "v_exp_f32 v193, v193\n"
      "v_cvt_pk_bf16_f32 v29, v94, v95\n"
      "v_cvt_pk_bf16_f32 v196, v240, v241\n"
      "v_exp_f32 v202, v202\n"
      "v_exp_f32 v33, v33\n"
      "v_cvt_pk_bf16_f32 v31, v102, v103\n"
      "v_cvt_pk_bf16_f32 v197, v242, v243\n"
      "v_exp_f32 v159, v159\n"
      "v_cvt_pk_bf16_f32 v199, v202, v203\n"
      "v_cvt_pk_bf16_f32 v30, v100, v101\n"
      "v_exp_f32 v86, v86\n"
      "v_add_f32 v100, v248, v250\n"
      "v_cvt_pk_bf16_f32 v95, v154, v155\n"
      "v_exp_f32 v151, v151\n"
      "v_cvt_pk_bf16_f32 v198, v200, v201\n"
      "v_cvt_pk_bf16_f32 v28, v92, v93\n"
      "v_cvt_pk_bf16_f32 v162, v248, v250\n"
      "v_cvt_pk_bf16_f32 v200, v192, v193\n"
      "v_cvt_pk_bf16_f32 v94, v152, v153\n"
      "v_cvt_pk_bf16_f32 v203, v34, v35\n"
      "v_cvt_pk_bf16_f32 v163, v248, v250\n"
      "v_mfma_f32_16x16x32_bf16 v[240:243], a[176:179], v[48:51], 0\n"
      "v_cvt_pk_bf16_f32 v201, v194, v195\n"
      "v_add_f32 v101, v248, v250\n"
      "v_mfma_f32_16x16x32_bf16 v[240:243], a[180:183], v[216:219], v[240:243]\n"
      "v_permlane16_swap_b32 v162, v163\n"
      "v_mfma_f32_16x16x32_bf16 v[240:243], a[184:187], v[132:135], v[240:243]\n"
      "v_exp_f32 v88, v88\n"
      "v_mfma_f32_16x16x32_bf16 v[240:243], a[188:191], v[224:227], v[240:243]\n"
      "ds_read_b64 v[18:19], v11 offset:0\n"
      "v_permlane16_swap_b32 v100, v101\n"
      "v_mov_b64 v[248:249], v[100:101]\n"
      "v_mfma_f32_16x16x32_bf16 v[152:155], a[160:163], v[48:51], 0\n"
      "v_exp_f32 v32, v32\n"
      "v_mfma_f32_16x16x32_bf16 v[152:155], a[164:167], v[216:219], v[152:155]\n"
      "v_cvt_pk_bf16_f32 v202, v32, v33\n"
      "v_cvt_pk_bf16_f32 v193, v88, v89\n"
      "v_mfma_f32_16x16x32_bf16 v[152:155], a[168:171], v[132:135], v[152:155]\n"
      "v_exp_f32 v156, v156\n"
      "v_mfma_f32_16x16x32_bf16 v[152:155], a[172:175], v[224:227], v[152:155]\n"
      "v_cvt_pk_bf16_f32 v92, v148, v149\n"
      "v_cvt_pk_bf16_f32 v93, v150, v151\n"
      "v_cvt_pk_bf16_f32 v194, v156, v157\n"
      "v_smfmac_f32_16x16x64_bf16 v[248:251], v[12:15], v[196:203], v5\n"
      "ds_bpermute_b32 v253, v7, v144 offset:8\n"
      "v_smfmac_f32_16x16x64_bf16 v[100:103], v[12:15], v[92:99], v5\n"
      "ds_bpermute_b32 v252, v7, v144 offset:248\n"
      "v_cvt_pk_bf16_f32 v195, v158, v159\n"
      "v_cvt_pk_bf16_f32 v35, v242, v243\n"
      "v_exp_f32 v87, v87\n"
      "v_cvt_pk_bf16_f32 v32, v152, v153\n"
      "v_cvt_pk_bf16_f32 v33, v154, v155\n"
      "v_smfmac_f32_16x16x32_bf16 v[100:103], v[12:13], v[176:179], v5\n"
      "v_cvt_pk_bf16_f32 v192, v86, v87\n"
      "v_cvt_pk_bf16_f32 v34, v240, v241\n"
      "s_waitcnt lgkmcnt(2)\n"
      "v_mfma_f32_16x16x4_4b_bf16 v[146:161], v[162:163], v[18:19], 0 cbsz:2 abid:0\n"
      "s_nop 3\n"
      "v_add_f32 v100, v100, v101\n"
      "v_rcp_f32 v101, v100\n"
      "v_smfmac_f32_16x16x32_bf16 v[248:251], v[12:13], v[192:195], v5\n"
      "ds_read_b64 v[88:89], v11 offset:4096\n"
      "ds_bpermute_b32 v102, v4, v101\n"
      "s_nop 0\n"
      "v_mfma_f32_16x16x32_bf16 v[150:153], v[92:95], v[180:183], v[150:153]\n"
      "v_mfma_f32_16x16x32_bf16 v[150:153], v[96:99], v[184:187], v[150:153]\n"
      "v_add_f32_dpp v90, v90, v90 row_ror:8 row_mask:0xf bank_mask:0xf\n"
      "v_mfma_f32_16x16x32_bf16 v[150:153], v[176:179], v[120:123], v[150:153]\n"
      "s_nop 0\n"
      "v_add_f32_dpp v90, v90, v90 row_ror:4 row_mask:0xf bank_mask:0xf\n"
      "v_mfma_f32_16x16x32_bf16 v[146:149], v[92:95], v[108:111], v[146:149]\n"
      "buffer_load_dwordx4 v1, s[40:43], s78 offen offset:1024 lds\n"
      "v_mfma_f32_16x16x32_bf16 v[146:149], v[96:99], v[112:115], v[146:149]\n"
      "v_add_f32_dpp v90, v90, v90 row_ror:2 row_mask:0xf bank_mask:0xf\n"
      "v_mfma_f32_16x16x32_bf16 v[146:149], v[176:179], v[52:55], v[146:149]\n"
      "s_nop 0\n"
      "v_add_f32_dpp v90, v90, v90 row_ror:1 row_mask:0xf bank_mask:0xf\n"
      "v_mfma_f32_16x16x32_bf16 v[158:161], v[92:95], v[232:235], v[158:161]\n"
      "v_rcp_f32 v91, v90\n"
      "v_mfma_f32_16x16x32_bf16 v[158:161], v[96:99], v[236:239], v[158:161]\n"
      "v_mfma_f32_16x16x32_bf16 v[158:161], v[176:179], v[228:231], v[158:161]\n"
      "s_waitcnt lgkmcnt(0)\n"
      "s_nop 0\n"
      "v_mul_f32_dpp v149, v102, v149 row_newbcast:12 row_mask:0xf bank_mask:0xf\n"
      "v_mfma_f32_16x16x32_bf16 v[154:157], v[92:95], v[56:59], v[154:157]\n"
      "v_mul_f32_dpp v153, v102, v153 row_newbcast:12 row_mask:0xf bank_mask:0xf\n"
      "v_mul_f32_dpp v150, v102, v150 row_newbcast:0 row_mask:0xf bank_mask:0xf\n"
      "v_mfma_f32_16x16x32_bf16 v[154:157], v[96:99], v[60:63], v[154:157]\n"
      "v_mul_f32_dpp v151, v102, v151 row_newbcast:4 row_mask:0xf bank_mask:0xf\n"
      "v_mul_f32_dpp v152, v102, v152 row_newbcast:8 row_mask:0xf bank_mask:0xf\n"
      "v_mfma_f32_16x16x32_bf16 v[154:157], v[176:179], v[116:119], v[154:157]\n"
      "v_add_u32 v11, 0x200, v11\n"
      "v_mul_f32_dpp v146, v102, v146 row_newbcast:0 row_mask:0xf bank_mask:0xf\n"
      "v_mfma_f32_16x16x32_bf16 v[150:153], v[212:215], a[208:211], v[150:153]\n"
      "v_mul_f32_dpp v148, v102, v148 row_newbcast:8 row_mask:0xf bank_mask:0xf\n"
      "v_mul_f32_dpp v147, v102, v147 row_newbcast:4 row_mask:0xf bank_mask:0xf\n"
      "v_mfma_f32_16x16x32_bf16 v[150:153], v[124:127], a[212:215], v[150:153]\n"
      "v_mfma_f32_16x16x32_bf16 v[150:153], v[188:191], a[216:219], v[150:153]\n"
      "v_maximum3_f32 v8, v8, v90, v91\n"
      "v_mul_f32_dpp v161, v102, v161 row_newbcast:12 row_mask:0xf bank_mask:0xf\n"
      "v_mfma_f32_16x16x32_bf16 v[150:153], v[244:247], a[220:223], v[150:153]\n"
      "v_mul_f32_dpp v155, v102, v155 row_newbcast:4 row_mask:0xf bank_mask:0xf\n"
      "v_mul_f32_dpp v156, v102, v156 row_newbcast:8 row_mask:0xf bank_mask:0xf\n"
      "v_mfma_f32_16x16x32_bf16 v[146:149], v[212:215], a[192:195], v[146:149]\n"
      "v_maximum3_f32 v8, v8, v100, v101\n"
      "v_mul_f32_dpp v157, v102, v157 row_newbcast:12 row_mask:0xf bank_mask:0xf\n"
      "v_mfma_f32_16x16x32_bf16 v[146:149], v[124:127], a[196:199], v[146:149]\n"
      "v_mul_f32_dpp v154, v102, v154 row_newbcast:0 row_mask:0xf bank_mask:0xf\n"
      "v_mfma_f32_16x16x32_bf16 v[146:149], v[188:191], a[200:203], v[146:149]\n"
      "v_mul_f32_dpp v159, v102, v159 row_newbcast:4 row_mask:0xf bank_mask:0xf\n"
      "v_mul_f32_dpp v160, v102, v160 row_newbcast:8 row_mask:0xf bank_mask:0xf\n"
      "v_mfma_f32_16x16x32_bf16 v[146:149], v[244:247], a[204:207], v[146:149]\n"
      "v_mul_f32_dpp v158, v102, v158 row_newbcast:0 row_mask:0xf bank_mask:0xf\n"
      "v_add_f32 v248, v248, v249\n"
      "v_mfma_f32_16x16x32_bf16 v[154:157], v[212:215], a[224:227], v[154:157]\n"
      "v_rcp_f32 v249, v248\n"
      "v_mfma_f32_16x16x32_bf16 v[154:157], v[124:127], a[228:231], v[154:157]\n"
      "v_mfma_f32_16x16x32_bf16 v[154:157], v[188:191], a[232:235], v[154:157]\n"
      "ds_bpermute_b32 v250, v4, v249 offset:128\n"
      "v_mfma_f32_16x16x32_bf16 v[154:157], v[244:247], a[236:239], v[154:157]\n"
      "v_maximum3_f32 v8, v8, v248, v249\n"
      "v_cvt_pk_bf16_f32 v146, v146, v150\n"
      "v_mfma_f32_16x16x4_4b_bf16 v[92:107], v[162:163], v[18:19], 0 cbsz:2 abid:2\n"
      "v_cvt_pk_bf16_f32 v150, v147, v151\n"
      "v_pk_max_i16 v150, v150, 0\n"
      "buffer_load_dwordx4 v1, s[40:43], s78 offen offset:3072 lds\n"
      "v_cvt_pk_bf16_f32 v148, v148, v152\n"
      "v_mfma_f32_16x16x32_bf16 v[158:161], v[212:215], a[240:243], v[158:161]\n"
      "v_mfma_f32_16x16x32_bf16 v[158:161], v[124:127], a[244:247], v[158:161]\n"
      "v_mfma_f32_16x16x32_bf16 v[158:161], v[188:191], a[248:251], v[158:161]\n"
      "v_mfma_f32_16x16x32_bf16 v[158:161], v[244:247], a[252:255], v[158:161]\n"
      "v_cvt_pk_bf16_f32 v152, v149, v153\n"
      "v_pk_max_i16 v146, v146, 0\n"
      "v_mfma_f32_16x16x32_bf16 v[92:95], v[196:199], v[108:111], v[92:95]\n"
      "v_mfma_f32_16x16x32_bf16 v[92:95], v[200:203], v[112:115], v[92:95]\n"
      "v_and_b32 v213, 0xffff0000, v88\n"
      "v_add_u32 v16, 64, v16\n"
      "v_mfma_f32_16x16x32_bf16 v[92:95], v[192:195], v[52:55], v[92:95]\n"
      "s_nop 0\n"
      "v_cvt_pk_bf16_f32 v153, v157, v161\n"
      "v_pk_max_i16 v152, v152, 0\n"
      "v_mfma_f32_16x16x32_bf16 v[100:103], v[196:199], v[56:59], v[100:103]\n"
      "v_cvt_pk_bf16_f32 v149, v156, v160\n"
      "v_pk_max_i16 v149, v149, 0\n"
      "v_mfma_f32_16x16x32_bf16 v[100:103], v[200:203], v[60:63], v[100:103]\n"
      "v_cvt_pk_bf16_f32 v147, v154, v158\n"
      "v_mfma_f32_16x16x32_bf16 v[100:103], v[192:195], v[116:119], v[100:103]\n"
      "ds_read_b128 v[240:243], v0 offset:9216\n"
      "ds_read_b128 v[160:163], v0 offset:8192\n"
      "v_mfma_f32_16x16x32_bf16 v[104:107], v[196:199], v[232:235], v[104:107]\n"
      "s_add_u32 m0, s53, 8192\n"
      "ds_read_b128 v[124:127], v0 offset:9728\n"
      "v_cvt_pk_bf16_f32 v151, v155, v159\n"
      "v_mfma_f32_16x16x32_bf16 v[104:107], v[200:203], v[236:239], v[104:107]\n"
      "s_waitcnt lgkmcnt(3)\n"
      "v_mul_f32_dpp v95, v250, v95 row_newbcast:12 row_mask:0xf bank_mask:0xf\n"
      "v_mul_f32_dpp v92, v250, v92 row_newbcast:0 row_mask:0xf bank_mask:0xf\n"
      "v_mfma_f32_16x16x32_bf16 v[104:107], v[192:195], v[228:231], v[104:107]\n"
      "ds_read_b128 v[84:87], v0 offset:8704\n"
      "v_pk_max_i16 v148, v148, 0\n"
      "v_mfma_f32_16x16x32_bf16 v[96:99], v[196:199], v[180:183], v[96:99]\n"
      "v_mfma_f32_16x16x32_bf16 v[96:99], v[200:203], v[184:187], v[96:99]\n"
      "v_pk_max_i16 v151, v151, 0\n"
      "v_pk_max_i16 v153, v153, 0\n"
      "v_mfma_f32_16x16x32_bf16 v[96:99], v[192:195], v[120:123], v[96:99]\n"
      "v_and_b32 v215, 0xffff0000, v89\n"
      "v_lshlrev_b32 v212, 16, v88\n"
      "v_mfma_f32_16x16x4_4b_bf16 v[188:203], v[252:253], v[18:19], 0 cbsz:2 abid:0\n"
      "buffer_store_dwordx2 v[152:153], v2, s[44:47], s73 offen offset:768 nt sc1\n"
      "v_mul_f32_dpp v103, v250, v103 row_newbcast:12 row_mask:0xf bank_mask:0xf\n"
      "v_mul_f32_dpp v93, v250, v93 row_newbcast:4 row_mask:0xf bank_mask:0xf\n"
      "v_mul_f32_dpp v101, v250, v101 row_newbcast:4 row_mask:0xf bank_mask:0xf\n"
      "v_mul_f32_dpp v100, v250, v100 row_newbcast:0 row_mask:0xf bank_mask:0xf\n"
      "v_mfma_f32_16x16x32_bf16 v[176:179], v[20:23], v[28:31], 0\n"
      "v_mul_f32_dpp v96, v250, v96 row_newbcast:0 row_mask:0xf bank_mask:0xf\n"
      "v_mul_f32_dpp v106, v250, v106 row_newbcast:8 row_mask:0xf bank_mask:0xf\n"
      "v_mfma_f32_16x16x32_bf16 v[176:179], v[24:27], v[32:35], v[176:179]\n"
      "v_mul_f32_dpp v98, v250, v98 row_newbcast:8 row_mask:0xf bank_mask:0xf\n"
      "v_mul_f32_dpp v99, v250, v99 row_newbcast:12 row_mask:0xf bank_mask:0xf\n"
      "v_smfmac_f32_16x16x64_bf16 v[196:199], v[40:43], v[56:63], v5\n"
      "v_smfmac_f32_16x16x64_bf16 v[192:195], v[40:43], v[180:187], v5\n"
      "s_waitcnt lgkmcnt(0)\n"
      "buffer_load_dwordx4 v1, s[40:43], s91 offen offset:3072 nt lds\n"
      "v_smfmac_f32_16x16x64_bf16 v[188:191], v[40:43], v[108:115], v5\n"
      "v_mul_f32_dpp v94, v250, v94 row_newbcast:8 row_mask:0xf bank_mask:0xf\n"
      "v_smfmac_f32_16x16x64_bf16 v[200:203], v[40:43], v[232:239], v5\n"
      "v_mul_f32_dpp v107, v250, v107 row_newbcast:12 row_mask:0xf bank_mask:0xf\n"
      "v_mfma_f32_16x16x32_bf16 v[244:247], v[204:207], v[28:31], 0\n"
      "v_mul_f32_dpp v102, v250, v102 row_newbcast:8 row_mask:0xf bank_mask:0xf\n"
      "v_mul_f32_dpp v97, v250, v97 row_newbcast:4 row_mask:0xf bank_mask:0xf\n"
      "v_mfma_f32_16x16x32_bf16 v[244:247], v[208:211], v[32:35], v[244:247]\n"
      "v_lshlrev_b32 v214, 16, v89\n"
      "v_mul_f32_dpp v104, v250, v104 row_newbcast:0 row_mask:0xf bank_mask:0xf\n"
      "v_smfmac_f32_16x16x32_bf16 v[200:203], v[254:255], v[228:231], v5\n"
      "v_mul_f32_dpp v105, v250, v105 row_newbcast:4 row_mask:0xf bank_mask:0xf\n"
      "v_smfmac_f32_16x16x32_bf16 v[196:199], v[254:255], v[116:119], v5\n"
      "v_pk_max_i16 v147, v147, 0\n"
      "v_smfmac_f32_16x16x32_bf16 v[188:191], v[254:255], v[52:55], v5\n"
      "buffer_load_dwordx4 v1, s[40:43], s91 offen offset:1024 nt lds\n"
      "v_smfmac_f32_16x16x32_bf16 v[192:195], v[254:255], v[120:123], v5\n"
      "buffer_store_dwordx2 v[146:147], v2, s[44:47], s73 offen offset:0 nt sc1\n"
      "v_mfma_f32_16x16x32_bf16 v[152:155], v[64:67], v[28:31], 0\n"
      "v_exp_f32 v179, v179\n"
      "v_mfma_f32_16x16x32_bf16 v[152:155], v[68:71], v[32:35], v[152:155]\n"
      "v_exp_f32 v246, v246\n"
      "v_mfma_f32_16x16x32_bf16 v[104:107], v[172:175], a[240:243], v[104:107]\n"
      "v_exp_f32 v244, v244\n"
      "v_mfma_f32_16x16x32_bf16 v[104:107], v[72:75], a[244:247], v[104:107]\n"
      "v_add_f32 v188, v188, v189\n"
      "v_add_f32 v196, v196, v197\n"
      "v_mfma_f32_16x16x32_bf16 v[104:107], v[128:131], a[248:251], v[104:107]\n"
      "v_add_f32 v192, v192, v193\n"
      "v_fma_f32 v192, v192, v91, v213\n"
      "v_mfma_f32_16x16x32_bf16 v[104:107], v[36:39], a[252:255], v[104:107]\n"
      "v_exp_f32 v177, v177\n"
      "v_mfma_f32_16x16x32_bf16 v[252:255], v[136:139], v[28:31], 0\n"
      "v_fma_f32 v188, v188, v91, v212\n"
      "v_fma_f32 v196, v196, v91, v214\n"
      "v_mfma_f32_16x16x32_bf16 v[252:255], v[140:143], v[32:35], v[252:255]\n"
      "v_exp_f32 v152, v152\n"
      "v_mfma_f32_16x16x32_bf16 v[96:99], v[172:175], a[208:211], v[96:99]\n"
      "v_add_f32 v200, v200, v201\n"
      "v_fma_f32 v200, v200, v91, v215\n"
      "v_mfma_f32_16x16x32_bf16 v[96:99], v[72:75], a[212:215], v[96:99]\n"
      "v_cvt_pk_bf16_f32 v189, v196, v200\n"
      "v_cvt_pk_bf16_f32 v188, v188, v192\n"
      "v_mfma_f32_16x16x32_bf16 v[96:99], v[128:131], a[216:219], v[96:99]\n"
      "v_exp_f32 v255, v255\n"
      "v_mfma_f32_16x16x32_bf16 v[96:99], v[36:39], a[220:223], v[96:99]\n"
      "v_exp_f32 v155, v155\n"
      "v_mfma_f32_16x16x32_bf16 v[88:91], a[144:147], v[160:163], 0\n"
      "v_exp_f32 v247, v247\n"
      "v_mfma_f32_16x16x32_bf16 v[88:91], a[148:151], v[84:87], v[88:91]\n"
      "v_exp_f32 v245, v245\n"
      "v_mfma_f32_16x16x32_bf16 v[88:91], a[152:155], v[240:243], v[88:91]\n"
      "v_exp_f32 v153, v153\n"
      "v_mfma_f32_16x16x32_bf16 v[88:91], a[156:159], v[124:127], v[88:91]\n"
      "v_exp_f32 v176, v176\n"
      "v_mfma_f32_16x16x32_bf16 v[156:159], v[164:167], v[28:31], 0\n"
      "v_exp_f32 v178, v178\n"
      "v_mfma_f32_16x16x32_bf16 v[156:159], v[168:171], v[32:35], v[156:159]\n"
      "v_cvt_pk_bf16_f32 v196, v176, v177\n"
      "v_mfma_f32_16x16x32_bf16 v[40:43], v[76:79], v[28:31], 0\n"
      "v_cvt_pk_bf16_f32 v197, v178, v179\n"
      "v_cvt_pk_bf16_f32 v194, v244, v245\n"
      "v_mfma_f32_16x16x32_bf16 v[40:43], v[80:83], v[32:35], v[40:43]\n"
      "buffer_store_dwordx2 v[148:149], v2, s[44:47], s73 offen offset:512 nt sc1\n"
      "v_mfma_f32_16x16x32_bf16 v[92:95], v[172:175], a[192:195], v[92:95]\n"
      "v_exp_f32 v252, v252\n"
      "v_mfma_f32_16x16x32_bf16 v[92:95], v[72:75], a[196:199], v[92:95]\n"
      "v_exp_f32 v253, v253\n"
      "v_mfma_f32_16x16x32_bf16 v[92:95], v[128:131], a[200:203], v[92:95]\n"
      "v_exp_f32 v159, v159\n"
      "v_mfma_f32_16x16x32_bf16 v[92:95], v[36:39], a[204:207], v[92:95]\n"
      "v_exp_f32 v43, v43\n"
      "v_mfma_f32_16x16x32_bf16 v[212:215], a[160:163], v[160:163], 0\n"
      "buffer_load_dwordx4 v1, s[40:43], s90 offen offset:2048 nt lds\n"
      "v_mfma_f32_16x16x32_bf16 v[212:215], a[164:167], v[84:87], v[212:215]\n"
      "v_exp_f32 v42, v42\n"
      "v_mfma_f32_16x16x32_bf16 v[212:215], a[168:171], v[240:243], v[212:215]\n"
      "v_exp_f32 v157, v157\n"
      "v_mfma_f32_16x16x32_bf16 v[212:215], a[172:175], v[124:127], v[212:215]\n"
      "v_exp_f32 v40, v40\n"
      "v_mfma_f32_16x16x32_bf16 v[100:103], v[172:175], a[224:227], v[100:103]\n"
      "v_exp_f32 v254, v254\n"
      "v_mfma_f32_16x16x32_bf16 v[100:103], v[72:75], a[228:231], v[100:103]\n"
      "v_exp_f32 v158, v158\n"
      "v_mfma_f32_16x16x32_bf16 v[100:103], v[128:131], a[232:235], v[100:103]\n"
      "v_exp_f32 v156, v156\n"
      "v_mfma_f32_16x16x32_bf16 v[100:103], v[36:39], a[236:239], v[100:103]\n"
      "v_exp_f32 v41, v41\n"
      "v_mfma_f32_16x16x32_bf16 v[172:175], a[128:131], v[160:163], 0\n"
      "v_cvt_pk_bf16_f32 v192, v156, v157\n"
      "v_mov_b64 v[128:129], 0\n"
      "v_mfma_f32_16x16x32_bf16 v[172:175], a[132:135], v[84:87], v[172:175]\n"
      "v_cvt_pk_bf16_f32 v176, v152, v153\n"
      "v_cvt_pk_bf16_f32 v178, v252, v253\n"
      "v_mfma_f32_16x16x32_bf16 v[172:175], a[136:139], v[240:243], v[172:175]\n"
      "v_mov_b64 v[130:131], 0\n"
      "v_cvt_pk_bf16_f32 v198, v40, v41\n"
      "v_mfma_f32_16x16x32_bf16 v[172:175], a[140:143], v[124:127], v[172:175]\n"
      "v_exp_f32 v154, v154\n"
      "v_mfma_f32_16x16x32_bf16 v[72:75], a[176:179], v[160:163], 0\n"
      "v_cvt_pk_bf16_f32 v195, v246, v247\n"
      "v_cvt_pk_bf16_f32 v252, v212, v213\n"
      "v_mfma_f32_16x16x32_bf16 v[72:75], a[180:183], v[84:87], v[72:75]\n"
      "v_cvt_pk_bf16_f32 v179, v254, v255\n"
      "v_cvt_pk_bf16_f32 v250, v88, v89\n"
      "v_mfma_f32_16x16x32_bf16 v[72:75], a[184:187], v[240:243], v[72:75]\n"
      "v_cvt_pk_bf16_f32 v249, v174, v175\n"
      "v_cvt_pk_bf16_f32 v251, v90, v91\n"
      "v_mfma_f32_16x16x32_bf16 v[72:75], a[188:191], v[124:127], v[72:75]\n"
      "v_cvt_pk_bf16_f32 v248, v172, v173\n"
      "v_pk_max_i16 v189, v189, 0\n"
      "v_cvt_pk_bf16_f32 v92, v92, v96\n"
      "v_cvt_pk_bf16_f32 v94, v94, v98\n"
      "v_cvt_pk_bf16_f32 v96, v93, v97\n"
      "v_cvt_pk_bf16_f32 v253, v214, v215\n"
      "v_mfma_f32_16x16x32_bf16 v[172:175], v[136:139], v[248:251], 0\n"
      "s_nop 0\n"
      "v_cvt_pk_bf16_f32 v255, v74, v75\n"
      "v_cvt_pk_bf16_f32 v254, v72, v73\n"
      "v_smfmac_f32_16x16x64_bf16 v[128:131], v[220:223], v[28:35], v5\n"
      "buffer_load_dwordx4 v1, s[40:43], s90 offen offset:0 nt lds\n"
      "v_smfmac_f32_16x16x64_bf16 v[128:131], v[44:47], v[248:255], v5\n"
      "buffer_store_dwordx2 v[150:151], v2, s[44:47], s73 offen offset:256 nt sc1\n"
      "v_pk_max_i16 v188, v188, 0\n"
      "v_cvt_pk_bf16_f32 v199, v42, v43\n"
      "v_mfma_f32_16x16x32_bf16 v[200:203], v[76:79], v[248:251], 0\n"
      "v_pk_max_i16 v92, v92, 0\n"
      "v_cvt_pk_bf16_f32 v177, v154, v155\n"
      "v_mfma_f32_16x16x32_bf16 v[200:203], v[80:83], v[252:255], v[200:203]\n"
      "s_nop 0\n"
      "v_add_f32 v130, v130, v131\n"
      "v_exp_f32 v130, v130\n"
      "v_mfma_f32_16x16x32_bf16 v[172:175], v[140:143], v[252:255], v[172:175]\n"
      "v_add_f32 v128, v128, v129\n"
      "v_exp_f32 v128, v128\n"
      "v_mfma_f32_16x16x32_bf16 v[150:153], v[204:207], v[248:251], 0\n"
      "v_cvt_pk_bf16_f32 v193, v158, v159\n"
      "v_add_f32 v213, v128, v130\n"
      "v_mfma_f32_16x16x32_bf16 v[150:153], v[208:211], v[252:255], v[150:153]\n"
      "v_add_f32 v212, v128, v130\n"
      "v_cvt_pk_bf16_f32 v155, v128, v130\n"
      "v_mfma_f32_16x16x32_bf16 v[244:247], v[64:67], v[248:251], 0\n"
      "v_permlane16_swap_b32 v212, v213\n"
      "v_mfma_f32_16x16x32_bf16 v[244:247], v[68:71], v[252:255], v[244:247]\n"
      "v_cvt_pk_bf16_f32 v154, v128, v130\n"
      "v_mov_b64 v[148:149], v[212:213]\n"
      "v_smfmac_f32_16x16x64_bf16 v[212:215], v[12:15], v[192:199], v5\n"
      "v_cvt_pk_bf16_f32 v98, v95, v99\n"
      "v_mfma_f32_16x16x32_bf16 v[128:131], v[164:167], v[248:251], 0\n"
      "s_add_u32 m0, s53, 4096\n"
      "v_exp_f32 v203, v203\n"
      "v_mfma_f32_16x16x32_bf16 v[128:131], v[168:171], v[252:255], v[128:131]\n"
      "v_permlane16_swap_b32 v154, v155\n"
      "v_smfmac_f32_16x16x32_bf16 v[212:215], v[12:13], v[176:179], v5\n"
      "v_pk_max_i16 v94, v94, 0\n"
      "v_mfma_f32_16x16x4_4b_bf16 v[28:43], v[154:155], v[18:19], 0 cbsz:2 abid:0\n"
      "v_exp_f32 v151, v151\n"
      "v_cvt_pk_bf16_f32 v97, v101, v105\n"
      "v_cvt_pk_bf16_f32 v95, v102, v106\n"
      "v_cvt_pk_bf16_f32 v93, v100, v104\n"
      "v_exp_f32 v130, v130\n"
      "v_cvt_pk_bf16_f32 v99, v103, v107\n"
      "v_mfma_f32_16x16x32_bf16 v[144:147], v[20:23], v[248:251], 0\n"
      "ds_read_b128 v[100:103], v0 offset:4096\n"
      "v_add_f32 v212, v212, v213\n"
      "v_mfma_f32_16x16x32_bf16 v[144:147], v[24:27], v[252:255], v[144:147]\n"
      "v_rcp_f32 v213, v212\n"
      "v_mfma_f32_16x16x32_bf16 v[28:31], v[192:195], v[108:111], v[28:31]\n"
      "ds_bpermute_b32 v214, v4, v213\n"
      "v_mfma_f32_16x16x32_bf16 v[28:31], v[196:199], v[112:115], v[28:31]\n"
      "v_exp_f32 v201, v201\n"
      "v_mfma_f32_16x16x32_bf16 v[28:31], v[176:179], v[52:55], v[28:31]\n"
      "buffer_store_dwordx2 v[188:189], v3, s[44:47], s79 offen offset:0 nt sc1\n"
      "ds_read_b128 v[72:75], v0 offset:5632\n"
      "v_mfma_f32_16x16x32_bf16 v[32:35], v[192:195], v[180:183], v[32:35]\n"
      "ds_read_b128 v[156:159], v0 offset:4608\n"
      "ds_read_b128 v[88:91], v0 offset:5120\n"
      "v_mfma_f32_16x16x32_bf16 v[32:35], v[196:199], v[184:187], v[32:35]\n"
      "v_exp_f32 v202, v202\n"
      "v_mfma_f32_16x16x32_bf16 v[32:35], v[176:179], v[120:123], v[32:35]\n"
      "v_exp_f32 v147, v147\n"
      "v_mfma_f32_16x16x32_bf16 v[40:43], v[192:195], v[232:235], v[40:43]\n"
      "v_exp_f32 v131, v131\n"
      "v_mfma_f32_16x16x32_bf16 v[40:43], v[196:199], v[236:239], v[40:43]\n"
      "v_exp_f32 v129, v129\n"
      "v_mfma_f32_16x16x32_bf16 v[40:43], v[176:179], v[228:231], v[40:43]\n"
      "v_exp_f32 v150, v150\n"
      "v_mfma_f32_16x16x32_bf16 v[36:39], v[192:195], v[56:59], v[36:39]\n"
      "v_exp_f32 v128, v128\n"
      "v_mfma_f32_16x16x32_bf16 v[36:39], v[196:199], v[60:63], v[36:39]\n"
      "v_cvt_pk_bf16_f32 v248, v128, v129\n"
      "v_cvt_pk_bf16_f32 v249, v130, v131\n"
      "v_mfma_f32_16x16x32_bf16 v[36:39], v[176:179], v[116:119], v[36:39]\n"
      "s_waitcnt lgkmcnt(3)\n"
      "v_mul_f32_dpp v34, v214, v34 row_newbcast:8 row_mask:0xf bank_mask:0xf\n"
      "v_mul_f32_dpp v28, v214, v28 row_newbcast:0 row_mask:0xf bank_mask:0xf\n"
      "v_mfma_f32_16x16x32_bf16 v[176:179], a[160:163], v[100:103], 0\n"
      "v_mul_f32_dpp v32, v214, v32 row_newbcast:0 row_mask:0xf bank_mask:0xf\n"
      "s_waitcnt lgkmcnt(1)\n"
      "v_mfma_f32_16x16x32_bf16 v[176:179], a[164:167], v[156:159], v[176:179]\n"
      "s_waitcnt lgkmcnt(0)\n"
      "buffer_load_dwordx4 v1, s[40:43], s92 offen offset:2048 lds\n"
      "v_mfma_f32_16x16x32_bf16 v[176:179], a[168:171], v[88:91], v[176:179]\n"
      "v_exp_f32 v246, v246\n"
      "v_mfma_f32_16x16x32_bf16 v[176:179], a[172:175], v[72:75], v[176:179]\n"
      "v_cvt_pk_bf16_f32 v250, v150, v151\n"
      "v_mul_f32_dpp v38, v214, v38 row_newbcast:8 row_mask:0xf bank_mask:0xf\n"
      "v_mfma_f32_16x16x32_bf16 v[128:131], a[176:179], v[100:103], 0\n"
      "v_mul_f32_dpp v42, v214, v42 row_newbcast:8 row_mask:0xf bank_mask:0xf\n"
      "v_mul_f32_dpp v29, v214, v29 row_newbcast:4 row_mask:0xf bank_mask:0xf\n"
      "v_mfma_f32_16x16x32_bf16 v[128:131], a[180:183], v[156:159], v[128:131]\n"
      "v_mul_f32_dpp v40, v214, v40 row_newbcast:0 row_mask:0xf bank_mask:0xf\n"
      "v_mul_f32_dpp v35, v214, v35 row_newbcast:12 row_mask:0xf bank_mask:0xf\n"
      "v_mfma_f32_16x16x32_bf16 v[128:131], a[184:187], v[88:91], v[128:131]\n"
      "L_h2_%=:\n"
      "s_cbranch_execz L_hr_%=\n"
      "v_exp_f32 v172, v172\n"
      "v_mfma_f32_16x16x32_bf16 v[128:131], a[188:191], v[72:75], v[128:131]\n"
      "v_pk_max_i16 v95, v95, 0\n"
      "v_mul_f32_dpp v36, v214, v36 row_newbcast:0 row_mask:0xf bank_mask:0xf\n"
      "v_mfma_f32_16x16x32_bf16 v[196:199], a[144:147], v[100:103], 0\n"
      "v_mul_f32_dpp v33, v214, v33 row_newbcast:4 row_mask:0xf bank_mask:0xf\n"
      "v_maximum3_f32 v8, v8, v212, v213\n"
      "v_mfma_f32_16x16x32_bf16 v[196:199], a[148:151], v[156:159], v[196:199]\n"
      "v_pk_max_i16 v98, v98, 0\n"
      "v_mul_f32_dpp v39, v214, v39 row_newbcast:12 row_mask:0xf bank_mask:0xf\n"
      "v_mfma_f32_16x16x32_bf16 v[196:199], a[152:155], v[88:91], v[196:199]\n"
      "v_mul_f32_dpp v43, v214, v43 row_newbcast:12 row_mask:0xf bank_mask:0xf\n"
      "v_mul_f32_dpp v37, v214, v37 row_newbcast:4 row_mask:0xf bank_mask:0xf\n"
      "v_mfma_f32_16x16x32_bf16 v[196:199], a[156:159], v[72:75], v[196:199]\n"
      "v_mul_f32_dpp v31, v214, v31 row_newbcast:12 row_mask:0xf bank_mask:0xf\n"
      "v_mul_f32_dpp v30, v214, v30 row_newbcast:8 row_mask:0xf bank_mask:0xf\n"
      "v_mfma_f32_16x16x32_bf16 v[32:35], v[48:51], a[208:211], v[32:35]\n"
      "v_exp_f32 v247, v247\n"
      "v_mfma_f32_16x16x32_bf16 v[32:35], v[216:219], a[212:215], v[32:35]\n"
      "v_exp_f32 v174, v174\n"
      "v_mfma_f32_16x16x32_bf16 v[32:35], v[132:135], a[216:219], v[32:35]\n"
      "v_exp_f32 v200, v200\n"
      "v_mfma_f32_16x16x32_bf16 v[32:35], v[224:227], a[220:223], v[32:35]\n"
      "v_exp_f32 v152, v152\n"
      "v_mfma_f32_16x16x32_bf16 v[28:31], v[48:51], a[192:195], v[28:31]\n"
      "buffer_load_dwordx4 v1, s[40:43], s93 offen offset:1024 lds\n"
      "v_mfma_f32_16x16x32_bf16 v[28:31], v[216:219], a[196:199], v[28:31]\n"
      "v_exp_f32 v146, v146\n"
      "v_mfma_f32_16x16x32_bf16 v[28:31], v[132:135], a[200:203], v[28:31]\n"
      "v_mul_f32_dpp v41, v214, v41 row_newbcast:4 row_mask:0xf bank_mask:0xf\n"
      "v_cvt_pk_bf16_f32 v215, v198, v199\n"
      "v_mfma_f32_16x16x32_bf16 v[28:31], v[224:227], a[204:207], v[28:31]\n"
      "buffer_store_dwordx2 v[94:95], v2, s[44:47], s75 offen offset:512 nt sc1\n"
      "v_cvt_pk_bf16_f32 v214, v196, v197\n"
      "v_mfma_f32_16x16x32_bf16 v[40:43], v[48:51], a[240:243], v[40:43]\n"
      "v_exp_f32 v244, v244\n"
      "v_mfma_f32_16x16x32_bf16 v[40:43], v[216:219], a[244:247], v[40:43]\n"
      "v_exp_f32 v173, v173\n"
      "v_mfma_f32_16x16x32_bf16 v[40:43], v[132:135], a[248:251], v[40:43]\n"
      "v_cvt_pk_bf16_f32 v254, v200, v201\n"
      "v_cvt_pk_bf16_f32 v30, v30, v34\n"
      "v_mfma_f32_16x16x32_bf16 v[40:43], v[224:227], a[252:255], v[40:43]\n"
      "v_exp_f32 v144, v144\n"
      "v_mfma_f32_16x16x32_bf16 v[36:39], v[48:51], a[224:227], v[36:39]\n"
      "v_exp_f32 v175, v175\n"
      "v_mfma_f32_16x16x32_bf16 v[36:39], v[216:219], a[228:231], v[36:39]\n"
      "v_exp_f32 v145, v145\n"
      "v_mfma_f32_16x16x32_bf16 v[36:39], v[132:135], a[232:235], v[36:39]\n"
      "v_exp_f32 v153, v153\n"
      "v_mfma_f32_16x16x32_bf16 v[36:39], v[224:227], a[236:239], v[36:39]\n"
      "v_cvt_pk_bf16_f32 v251, v152, v153\n"
      "v_pk_max_i16 v99, v99, 0\n"
      "v_mfma_f32_16x16x32_bf16 v[224:227], a[128:131], v[100:103], 0\n"
      "v_cvt_pk_bf16_f32 v51, v174, v175\n"
      "v_cvt_pk_bf16_f32 v255, v202, v203\n"
      "v_mfma_f32_16x16x32_bf16 v[224:227], a[132:135], v[156:159], v[224:227]\n"
      "ds_read_b128 v[104:107], v0 offset:512\n"
      "v_cvt_pk_bf16_f32 v253, v146, v147\n"
      "v_mfma_f32_16x16x32_bf16 v[224:227], a[136:139], v[88:91], v[224:227]\n"
      "v_pk_max_i16 v93, v93, 0\n"
      "v_cvt_pk_bf16_f32 v252, v144, v145\n"
      "v_mfma_f32_16x16x32_bf16 v[224:227], a[140:143], v[72:75], v[224:227]\n"
      "v_exp_f32 v245, v245\n"
      "v_smfmac_f32_16x16x64_bf16 v[148:151], v[12:15], v[248:255], v5\n"
      "buffer_store_dwordx2 v[92:93], v2, s[44:47], s75 offen offset:0 nt sc1\n"
      "v_cvt_pk_bf16_f32 v50, v172, v173\n"
      "v_mfma_f32_16x16x4_4b_bf16 v[188:203], v[154:155], v[18:19], 0 cbsz:2 abid:2\n"
      "v_cvt_pk_bf16_f32 v49, v246, v247\n"
      "v_cvt_pk_bf16_f32 v48, v244, v245\n"
      "v_cvt_pk_bf16_f32 v34, v31, v35\n"
      "v_cvt_pk_bf16_f32 v35, v39, v43\n"
      "v_cvt_pk_bf16_f32 v28, v28, v32\n"
      "v_cvt_pk_bf16_f32 v212, v224, v225\n"
      "v_smfmac_f32_16x16x32_bf16 v[148:151], v[12:13], v[48:51], v5\n"
      "ds_read_b128 v[132:135], v0 offset:1536\n"
      "v_cvt_pk_bf16_f32 v217, v178, v179\n"
      "v_cvt_pk_bf16_f32 v219, v130, v131\n"
      "v_cvt_pk_bf16_f32 v213, v226, v227\n"
      "v_mfma_f32_16x16x32_bf16 v[188:191], v[248:251], v[108:111], v[188:191]\n"
      "v_pk_max_i16 v34, v34, 0\n"
      "v_mov_b64 v[152:153], 0\n"
      "v_mfma_f32_16x16x32_bf16 v[188:191], v[252:255], v[112:115], v[188:191]\n"
      "v_cvt_pk_bf16_f32 v216, v176, v177\n"
      "v_add_f32 v148, v148, v149\n"
      "v_mfma_f32_16x16x32_bf16 v[188:191], v[48:51], v[52:55], v[188:191]\n"
      "v_mov_b64 v[154:155], 0\n"
      "v_cvt_pk_bf16_f32 v218, v128, v129\n"
      "v_mfma_f32_16x16x32_bf16 v[224:227], v[76:79], v[212:215], 0\n"
      "buffer_load_dwordx4 v1, s[40:43], s93 offen offset:3072 lds\n"
      "v_mfma_f32_16x16x32_bf16 v[224:227], v[80:83], v[216:219], v[224:227]\n"
      "v_rcp_f32 v149, v148\n"
      "v_mfma_f32_16x16x32_bf16 v[176:179], v[20:23], v[212:215], 0\n"
      "ds_read_b128 v[172:175], v0 offset:1024\n"
      "v_cvt_pk_bf16_f32 v31, v38, v42\n"
      "v_mfma_f32_16x16x32_bf16 v[176:179], v[24:27], v[216:219], v[176:179]\n"
      "v_cvt_pk_bf16_f32 v32, v29, v33\n"
      "v_cvt_pk_bf16_f32 v33, v37, v41\n"
      "v_smfmac_f32_16x16x64_bf16 v[152:155], v[220:223], v[212:219], v5\n"
      "ds_read_b128 v[144:147], v0 offset:0\n"
      "v_cvt_pk_bf16_f32 v29, v36, v40\n"
      "v_mfma_f32_16x16x32_bf16 v[192:195], v[248:251], v[180:183], v[192:195]\n"
      "ds_bpermute_b32 v150, v4, v149 offset:128\n"
      "v_mfma_f32_16x16x32_bf16 v[192:195], v[252:255], v[184:187], v[192:195]\n"
      "v_exp_f32 v179, v179\n"
      "v_mfma_f32_16x16x32_bf16 v[192:195], v[48:51], v[120:123], v[192:195]\n"
      "v_exp_f32 v225, v225\n"
      "v_mfma_f32_16x16x32_bf16 v[92:95], v[204:207], v[212:215], 0\n"
      "v_exp_f32 v224, v224\n"
      "v_mfma_f32_16x16x32_bf16 v[92:95], v[208:211], v[216:219], v[92:95]\n"
      "v_exp_f32 v226, v226\n"
      "v_mfma_f32_16x16x32_bf16 v[128:131], v[64:67], v[212:215], 0\n"
      "v_exp_f32 v176, v176\n"
      "v_mfma_f32_16x16x32_bf16 v[128:131], v[68:71], v[216:219], v[128:131]\n"
      "buffer_load_dwordx4 v1, s[40:43], s92 offen offset:0 lds\n"
      "v_mfma_f32_16x16x32_bf16 v[196:199], v[248:251], v[56:59], v[196:199]\n"
      "v_exp_f32 v177, v177\n"
      "v_mfma_f32_16x16x32_bf16 v[196:199], v[252:255], v[60:63], v[196:199]\n"
      "v_exp_f32 v178, v178\n"
      "v_mfma_f32_16x16x32_bf16 v[196:199], v[48:51], v[116:119], v[196:199]\n"
      "v_exp_f32 v227, v227\n"
      "v_mfma_f32_16x16x32_bf16 v[200:203], v[248:251], v[232:235], v[200:203]\n"
      "v_cvt_pk_bf16_f32 v221, v178, v179\n"
      "v_cvt_pk_bf16_f32 v220, v176, v177\n"
      "v_mfma_f32_16x16x32_bf16 v[200:203], v[252:255], v[236:239], v[200:203]\n"
      "v_exp_f32 v93, v93\n"
      "v_mfma_f32_16x16x32_bf16 v[200:203], v[48:51], v[228:231], v[200:203]\n"
      "s_waitcnt lgkmcnt(0)\n"
      "v_mul_f32_dpp v196, v150, v196 row_newbcast:0 row_mask:0xf bank_mask:0xf\n"
      "v_mul_f32_dpp v191, v150, v191 row_newbcast:12 row_mask:0xf bank_mask:0xf\n"
      "v_mfma_f32_16x16x32_bf16 v[36:39], v[164:167], v[212:215], 0\n"
      "v_mul_f32_dpp v192, v150, v192 row_newbcast:0 row_mask:0xf bank_mask:0xf\n"
      "v_mul_f32_dpp v188, v150, v188 row_newbcast:0 row_mask:0xf bank_mask:0xf\n"
      "v_mfma_f32_16x16x32_bf16 v[36:39], v[168:171], v[216:219], v[36:39]\n"
      "s_nop 0\n"
      "v_mul_f32_dpp v201, v150, v201 row_newbcast:4 row_mask:0xf bank_mask:0xf\n"
      "v_mul_f32_dpp v194, v150, v194 row_newbcast:8 row_mask:0xf bank_mask:0xf\n"
      "v_mfma_f32_16x16x32_bf16 v[176:179], v[136:139], v[212:215], 0\n"
      "v_mul_f32_dpp v200, v150, v200 row_newbcast:0 row_mask:0xf bank_mask:0xf\n"
      "v_mfma_f32_16x16x32_bf16 v[176:179], v[140:143], v[216:219], v[176:179]\n"
      "v_exp_f32 v92, v92\n"
      "v_mfma_f32_16x16x32_bf16 v[216:219], a[160:163], v[144:147], 0\n"
      "v_cvt_pk_bf16_f32 v222, v224, v225\n"
      "v_mul_f32_dpp v197, v150, v197 row_newbcast:4 row_mask:0xf bank_mask:0xf\n"
      "v_mfma_f32_16x16x32_bf16 v[216:219], a[164:167], v[104:107], v[216:219]\n"
      "v_exp_f32 v37, v37\n"
      "v_mfma_f32_16x16x32_bf16 v[216:219], a[168:171], v[172:175], v[216:219]\n"
      "v_exp_f32 v95, v95\n"
      "v_mfma_f32_16x16x32_bf16 v[216:219], a[172:175], v[132:135], v[216:219]\n"
      "v_exp_f32 v129, v129\n"
      "v_mfma_f32_16x16x32_bf16 v[252:255], a[144:147], v[144:147], 0\n"
      "v_exp_f32 v176, v176\n"
      "v_mfma_f32_16x16x32_bf16 v[252:255], a[148:151], v[104:107], v[252:255]\n"
      "v_exp_f32 v94, v94\n"
      "v_mfma_f32_16x16x32_bf16 v[252:255], a[152:155], v[172:175], v[252:255]\n"
      "v_exp_f32 v38, v38\n"
      "v_mfma_f32_16x16x32_bf16 v[252:255], a[156:159], v[132:135], v[252:255]\n"
      "v_cvt_pk_bf16_f32 v223, v226, v227\n"
      "v_cvt_pk_bf16_f32 v251, v218, v219\n"
      "v_mfma_f32_16x16x32_bf16 v[48:51], a[176:179], v[144:147], 0\n"
      "v_exp_f32 v39, v39\n"
      "v_mfma_f32_16x16x32_bf16 v[48:51], a[180:183], v[104:107], v[48:51]\n"
      "v_cvt_pk_bf16_f32 v250, v216, v217\n"
      "v_mul_f32_dpp v198, v150, v198 row_newbcast:8 row_mask:0xf bank_mask:0xf\n"
      "v_mfma_f32_16x16x32_bf16 v[48:51], a[184:187], v[172:175], v[48:51]\n"
      "v_cvt_pk_bf16_f32 v248, v252, v253\n"
      "v_mul_f32_dpp v190, v150, v190 row_newbcast:8 row_mask:0xf bank_mask:0xf\n"
      "v_mfma_f32_16x16x32_bf16 v[48:51], a[188:191], v[132:135], v[48:51]\n"
      "v_mul_f32_dpp v189, v150, v189 row_newbcast:4 row_mask:0xf bank_mask:0xf\n"
      "v_cvt_pk_bf16_f32 v249, v254, v255\n"
      "v_mfma_f32_16x16x32_bf16 v[224:227], a[128:131], v[144:147], 0\n"
      "v_exp_f32 v131, v131\n"
      "v_mfma_f32_16x16x32_bf16 v[224:227], a[132:135], v[104:107], v[224:227]\n"
      "s_add_u32 m0, s53, 0\n"
      "v_exp_f32 v36, v36\n"
      "v_mfma_f32_16x16x32_bf16 v[224:227], a[136:139], v[172:175], v[224:227]\n"
      "v_cvt_pk_bf16_f32 v252, v48, v49\n"
      "v_cvt_pk_bf16_f32 v253, v50, v51\n"
      "v_mfma_f32_16x16x32_bf16 v[224:227], a[140:143], v[132:135], v[224:227]\n"
      "v_exp_f32 v177, v177\n"
      "v_mfma_f32_16x16x32_bf16 v[188:191], v[160:163], a[192:195], v[188:191]\n"
      "v_mul_f32_dpp v199, v150, v199 row_newbcast:12 row_mask:0xf bank_mask:0xf\n"
      "v_mul_f32_dpp v203, v150, v203 row_newbcast:12 row_mask:0xf bank_mask:0xf\n"
      "v_mfma_f32_16x16x32_bf16 v[188:191], v[84:87], a[196:199], v[188:191]\n"
      "v_cvt_pk_bf16_f32 v217, v38, v39\n"
      "v_cvt_pk_bf16_f32 v218, v92, v93\n"
      "v_mfma_f32_16x16x32_bf16 v[188:191], v[240:243], a[200:203], v[188:191]\n"
      "v_cvt_pk_bf16_f32 v247, v226, v227\n"
      "v_cvt_pk_bf16_f32 v246, v224, v225\n"
      "v_mfma_f32_16x16x32_bf16 v[188:191], v[124:127], a[204:207], v[188:191]\n"
      "v_mul_f32_dpp v195, v150, v195 row_newbcast:12 row_mask:0xf bank_mask:0xf\n"
      "v_mul_f32_dpp v193, v150, v193 row_newbcast:4 row_mask:0xf bank_mask:0xf\n"
      "v_smfmac_f32_16x16x64_bf16 v[152:155], v[44:47], v[246:253], v5\n"
      "buffer_load_dwordx4 v1, s[40:43], s95 offen offset:3072 nt lds\n"
      "v_mfma_f32_16x16x32_bf16 v[196:199], v[160:163], a[224:227], v[196:199]\n"
      "v_mul_f32_dpp v202, v150, v202 row_newbcast:8 row_mask:0xf bank_mask:0xf\n"
      "v_cvt_pk_bf16_f32 v219, v94, v95\n"
      "v_mfma_f32_16x16x32_bf16 v[196:199], v[84:87], a[228:231], v[196:199]\n"
      "v_exp_f32 v128, v128\n"
      "v_mfma_f32_16x16x32_bf16 v[196:199], v[240:243], a[232:235], v[196:199]\n"
      "v_cvt_pk_bf16_f32 v224, v128, v129\n"
      "v_add_f32 v152, v152, v153\n"
      "v_mfma_f32_16x16x32_bf16 v[196:199], v[124:127], a[236:239], v[196:199]\n"
      "v_add_f32 v154, v154, v155\n"
      "v_cvt_pk_bf16_f32 v216, v36, v37\n"
      "v_mfma_f32_16x16x32_bf16 v[192:195], v[160:163], a[208:211], v[192:195]\n"
      "v_exp_f32 v152, v152\n"
      "v_mfma_f32_16x16x32_bf16 v[192:195], v[84:87], a[212:215], v[192:195]\n"
      "v_exp_f32 v179, v179\n"
      "v_mfma_f32_16x16x32_bf16 v[192:195], v[240:243], a[216:219], v[192:195]\n"
      "v_exp_f32 v178, v178\n"
      "v_mfma_f32_16x16x32_bf16 v[192:195], v[124:127], a[220:223], v[192:195]\n"
      "v_exp_f32 v154, v154\n"
      "v_mfma_f32_16x16x32_bf16 v[200:203], v[160:163], a[240:243], v[200:203]\n"
      "v_cvt_pk_bf16_f32 v94, v152, v154\n"
      "v_add_f32 v160, v152, v154\n"
      "v_mfma_f32_16x16x32_bf16 v[200:203], v[84:87], a[244:247], v[200:203]\n"
      "v_add_f32 v161, v152, v154\n"
      "v_cvt_pk_bf16_f32 v95, v152, v154\n"
      "v_mfma_f32_16x16x32_bf16 v[200:203], v[240:243], a[248:251], v[200:203]\n"
      "s_nop 0\n"
      "v_permlane16_swap_b32 v94, v95\n"
      "v_mfma_f32_16x16x32_bf16 v[200:203], v[124:127], a[252:255], v[200:203]\n"
      "v_permlane16_swap_b32 v160, v161\n"
      "v_mov_b64 v[128:129], v[160:161]\n"
      "v_pk_max_i16 v32, v32, 0\n"
      "v_smfmac_f32_16x16x64_bf16 v[160:163], v[12:15], v[216:223], v5\n"
      "buffer_store_dwordx2 v[98:99], v2, s[44:47], s75 offen offset:768 nt sc1\n"
      "v_cvt_pk_bf16_f32 v188, v188, v192\n"
      "v_mfma_f32_16x16x4_4b_bf16 v[36:51], v[94:95], v[18:19], 0 cbsz:2 abid:0\n"
      "v_cvt_pk_bf16_f32 v227, v178, v179\n"
      "v_exp_f32 v130, v130\n"
      "v_cvt_pk_bf16_f32 v226, v176, v177\n"
      "v_cvt_pk_bf16_f32 v225, v130, v131\n"
      "v_pk_max_i16 v29, v29, 0\n"
      "s_nop 0\n"
      "v_smfmac_f32_16x16x32_bf16 v[160:163], v[12:13], v[224:227], v5\n"
      "buffer_load_dwordx4 v1, s[40:43], s94 offen offset:2048 nt lds\n"
      "v_pk_max_i16 v28, v28, 0\n"
      "s_nop 0\n"
      "v_mfma_f32_16x16x32_bf16 v[40:43], v[216:219], v[180:183], v[40:43]\n"
      "v_pk_max_i16 v97, v97, 0\n"
      "v_cvt_pk_bf16_f32 v192, v189, v193\n"
      "v_mfma_f32_16x16x32_bf16 v[40:43], v[220:223], v[184:187], v[40:43]\n"
      "s_not_b64 vcc, vcc\n"
      "v_pk_max_i16 v96, v96, 0\n"
      "v_add_f32 v160, v160, v161\n"
      "v_mfma_f32_16x16x32_bf16 v[40:43], v[224:227], v[120:123], v[40:43]\n"
      "v_rcp_f32 v161, v160\n"
      "v_mfma_f32_16x16x32_bf16 v[36:39], v[216:219], v[108:111], v[36:39]\n"
      "ds_bpermute_b32 v162, v4, v161\n"
      "v_mfma_f32_16x16x32_bf16 v[36:39], v[220:223], v[112:115], v[36:39]\n"
      "v_maximum3_f32 v8, v8, v148, v149\n"
      "v_mfma_f32_16x16x32_bf16 v[36:39], v[224:227], v[52:55], v[36:39]\n"
      "v_pk_max_i16 v188, v188, 0\n"
      "v_mfma_f32_16x16x32_bf16 v[48:51], v[216:219], v[232:235], v[48:51]\n"
      "v_mfma_f32_16x16x32_bf16 v[48:51], v[220:223], v[236:239], v[48:51]\n"
      "v_cvt_pk_bf16_f32 v190, v190, v194\n"
      "v_cvt_pk_bf16_f32 v194, v191, v195\n"
      "v_mfma_f32_16x16x32_bf16 v[48:51], v[224:227], v[228:231], v[48:51]\n"
      "buffer_store_dwordx2 v[96:97], v2, s[44:47], s75 offen offset:256 nt sc1\n"
      "v_cvt_pk_bf16_f32 v195, v199, v203\n"
      "v_mfma_f32_16x16x32_bf16 v[44:47], v[216:219], v[56:59], v[44:47]\n"
      "buffer_load_dwordx4 v1, s[40:43], s95 offen offset:1024 nt lds\n"
      "v_mfma_f32_16x16x32_bf16 v[44:47], v[220:223], v[60:63], v[44:47]\n"
      "v_cvt_pk_bf16_f32 v189, v196, v200\n"
      "v_maximum3_f32 v8, v8, v160, v161\n"
      "v_mfma_f32_16x16x32_bf16 v[44:47], v[224:227], v[116:119], v[44:47]\n"
      "v_cvt_pk_bf16_f32 v193, v197, v201\n"
      "v_cvt_pk_bf16_f32 v191, v198, v202\n"
      "v_mfma_f32_16x16x4_4b_bf16 v[212:227], v[94:95], v[18:19], 0 cbsz:2 abid:2\n"
      "s_waitcnt lgkmcnt(0)\n"
      "v_mul_f32_dpp v38, v162, v38 row_newbcast:8 row_mask:0xf bank_mask:0xf\n"
      "v_mul_f32_dpp v39, v162, v39 row_newbcast:12 row_mask:0xf bank_mask:0xf\n"
      "v_mul_f32_dpp v37, v162, v37 row_newbcast:4 row_mask:0xf bank_mask:0xf\n"
      "v_mul_f32_dpp v36, v162, v36 row_newbcast:0 row_mask:0xf bank_mask:0xf\n"
      "v_pk_max_i16 v192, v192, 0\n"
      "v_mul_f32_dpp v46, v162, v46 row_newbcast:8 row_mask:0xf bank_mask:0xf\n"
      "v_mul_f32_dpp v47, v162, v47 row_newbcast:12 row_mask:0xf bank_mask:0xf\n"
      "v_mul_f32_dpp v41, v162, v41 row_newbcast:4 row_mask:0xf bank_mask:0xf\n"
      "v_mfma_f32_16x16x32_bf16 v[36:39], v[100:103], a[192:195], v[36:39]\n"
      "v_mul_f32_dpp v45, v162, v45 row_newbcast:4 row_mask:0xf bank_mask:0xf\n"
      "v_mul_f32_dpp v50, v162, v50 row_newbcast:8 row_mask:0xf bank_mask:0xf\n"
      "v_mfma_f32_16x16x32_bf16 v[36:39], v[156:159], a[196:199], v[36:39]\n"
      "v_mul_f32_dpp v49, v162, v49 row_newbcast:4 row_mask:0xf bank_mask:0xf\n"
      "v_mul_f32_dpp v44, v162, v44 row_newbcast:0 row_mask:0xf bank_mask:0xf\n"
      "v_mfma_f32_16x16x32_bf16 v[36:39], v[88:91], a[200:203], v[36:39]\n"
      "v_mul_f32_dpp v51, v162, v51 row_newbcast:12 row_mask:0xf bank_mask:0xf\n"
      "v_mul_f32_dpp v48, v162, v48 row_newbcast:0 row_mask:0xf bank_mask:0xf\n"
      "v_mfma_f32_16x16x32_bf16 v[36:39], v[72:75], a[204:207], v[36:39]\n"
      "v_pk_max_i16 v193, v193, 0\n"
      "v_pk_max_i16 v190, v190, 0\n"
      "v_mfma_f32_16x16x32_bf16 v[48:51], v[100:103], a[240:243], v[48:51]\n"
      "v_mfma_f32_16x16x32_bf16 v[48:51], v[156:159], a[244:247], v[48:51]\n"
      "v_mul_f32_dpp v43, v162, v43 row_newbcast:12 row_mask:0xf bank_mask:0xf\n"
      "v_pk_max_i16 v35, v35, 0\n"
      "v_mfma_f32_16x16x32_bf16 v[48:51], v[88:91], a[248:251], v[48:51]\n"
      "v_pk_max_i16 v30, v30, 0\n"
      "v_pk_max_i16 v195, v195, 0\n"
      "v_mfma_f32_16x16x32_bf16 v[48:51], v[72:75], a[252:255], v[48:51]\n"
      "v_pk_max_i16 v189, v189, 0\n"
      "v_add_u32 v9, 64, v9\n"
      "v_mfma_f32_16x16x32_bf16 v[44:47], v[100:103], a[224:227], v[44:47]\n"
      "v_mul_f32_dpp v42, v162, v42 row_newbcast:8 row_mask:0xf bank_mask:0xf\n"
      "v_mul_f32_dpp v40, v162, v40 row_newbcast:0 row_mask:0xf bank_mask:0xf\n"
      "v_mfma_f32_16x16x32_bf16 v[44:47], v[156:159], a[228:231], v[44:47]\n"
      "v_pk_max_i16 v31, v31, 0\n"
      "v_mfma_f32_16x16x32_bf16 v[44:47], v[88:91], a[232:235], v[44:47]\n"
      "s_add_u32 s44, s44, 13107200\n s_addc_u32 s45, s45, 0\n"
      "v_mfma_f32_16x16x32_bf16 v[44:47], v[72:75], a[236:239], v[44:47]\n"
      "v_pk_max_i16 v33, v33, 0\n"
      "v_pk_max_i16 v191, v191, 0\n"
      "v_mfma_f32_16x16x32_bf16 v[40:43], v[100:103], a[208:211], v[40:43]\n"
      "v_pk_max_i16 v194, v194, 0\n"
      "v_mfma_f32_16x16x32_bf16 v[40:43], v[156:159], a[212:215], v[40:43]\n"
      "buffer_load_dwordx4 v1, s[40:43], s94 offen offset:0 nt lds\n"
      "v_mfma_f32_16x16x32_bf16 v[40:43], v[88:91], a[216:219], v[40:43]\n"
      "s_add_u32 s40, s40, 13107200\n s_addc_u32 s41, s41, 0\n"
      "v_mfma_f32_16x16x32_bf16 v[40:43], v[72:75], a[220:223], v[40:43]\n"
      "s_nop 0\n"
      "L_task_%=:\n"
      "v_mfma_f32_16x16x32_bf16 v[240:243], v[136:139], v[246:249], 0\n"
      "s_waitcnt vmcnt(29)\n"
      "ds_read_b128 v[92:95], v0 offset:20480\n"
      "ds_read_b128 v[96:99], v0 offset:21504\n"
      "v_mfma_f32_16x16x32_bf16 v[240:243], v[140:143], v[250:253], v[240:243]\n"
      "buffer_store_dwordx2 v[28:29], v2, s[44:47], s100 offen offset:0 nt sc1\n"
      "ds_read_b128 v[88:91], v0 offset:20992\n"
      "v_mfma_f32_16x16x32_bf16 v[156:159], v[76:79], v[246:249], 0\n"
      "s_waitcnt vmcnt(14)\n"
      "ds_read_b128 v[160:163], v0 offset:8192\n"
      "ds_read_b128 v[200:203], v0 offset:9728\n"
      "v_mfma_f32_16x16x32_bf16 v[156:159], v[80:83], v[250:253], v[156:159]\n"
      "ds_read_b128 v[84:87], v0 offset:9216\n"
      "ds_read_b128 v[148:151], v0 offset:8704\n"
      "v_mfma_f32_16x16x32_bf16 v[100:103], v[64:67], v[246:249], 0\n"
      "ds_read_b128 v[124:127], v0 offset:16384\n"
      "ds_read_b128 v[72:75], v0 offset:17408\n"
      "v_mfma_f32_16x16x32_bf16 v[100:103], v[68:71], v[250:253], v[100:103]\n"
      "s_nop 1\n"
      "v_exp_f32 v158, v158\n"
      "v_mfma_f32_16x16x32_bf16 v[64:67], v[164:167], v[246:249], 0\n"
      "v_cvt_pk_bf16_f32 v36, v36, v40\n"
      "v_mfma_f32_16x16x32_bf16 v[64:67], v[168:171], v[250:253], v[64:67]\n"
      "v_exp_f32 v157, v157\n"
      "v_mfma_f32_16x16x32_bf16 v[168:171], v[204:207], v[246:249], 0\n"
      "v_exp_f32 v103, v103\n"
      "v_mfma_f32_16x16x32_bf16 v[168:171], v[208:211], v[250:253], v[168:171]\n"
      "v_exp_f32 v102, v102\n"
      "v_mfma_f32_16x16x32_bf16 v[152:155], v[20:23], v[246:249], 0\n"
      "v_exp_f32 v242, v242\n"
      "v_mfma_f32_16x16x32_bf16 v[152:155], v[24:27], v[250:253], v[152:155]\n"
      "s_waitcnt lgkmcnt(5)\n"
      "v_mfma_f32_16x16x32_bf16 v[80:83], a[32:35], v[160:163], 0\n"
      "s_waitcnt lgkmcnt(2)\n"
      "v_mfma_f32_16x16x32_bf16 v[80:83], a[36:39], v[148:151], v[80:83]\n"
      "v_mfma_f32_16x16x32_bf16 v[80:83], a[40:43], v[84:87], v[80:83]\n"
      "v_cvt_pk_bf16_f32 v40, v37, v41\n"
      "v_mfma_f32_16x16x32_bf16 v[80:83], a[44:47], v[200:203], v[80:83]\n"
      "v_exp_f32 v159, v159\n"
      "v_mfma_f32_16x16x32_bf16 v[140:143], a[48:51], v[160:163], 0\n"
      "v_exp_f32 v153, v153\n"
      "v_mfma_f32_16x16x32_bf16 v[140:143], a[52:55], v[148:151], v[140:143]\n"
      "v_cvt_pk_bf16_f32 v211, v158, v159\n"
      "v_mfma_f32_16x16x32_bf16 v[140:143], a[56:59], v[84:87], v[140:143]\n"
      "v_exp_f32 v101, v101\n"
      "v_mfma_f32_16x16x32_bf16 v[140:143], a[60:63], v[200:203], v[140:143]\n"
      "ds_read_b128 v[176:179], v0 offset:22016\n"
      "v_mfma_f32_16x16x32_bf16 v[164:167], v[160:163], a[80:83], 0\n"
      "v_exp_f32 v243, v243\n"
      "v_mfma_f32_16x16x32_bf16 v[164:167], v[148:151], a[84:87], v[164:167]\n"
      "v_exp_f32 v66, v66\n"
      "v_mfma_f32_16x16x32_bf16 v[164:167], v[84:87], a[88:91], v[164:167]\n"
      "v_mfma_f32_16x16x32_bf16 v[164:167], v[200:203], a[92:95], v[164:167]\n"
      "buffer_store_dwordx2 v[190:191], v2, s[44:47], s101 offen offset:512 nt sc1\n"
      "v_mfma_f32_16x16x32_bf16 v[76:79], v[160:163], a[64:67], 0\n"
      "ds_read_b128 v[196:199], v0 offset:17920\n"
      "v_mfma_f32_16x16x32_bf16 v[76:79], v[148:151], a[68:71], v[76:79]\n"
      "v_mfma_f32_16x16x32_bf16 v[76:79], v[84:87], a[72:75], v[76:79]\n"
      "v_mfma_f32_16x16x32_bf16 v[76:79], v[200:203], a[76:79], v[76:79]\n"
      "s_add_u32 m0, s53, 20480\n"
      "v_exp_f32 v64, v64\n"
      "v_mfma_f32_16x16x32_bf16 v[136:139], v[160:163], a[112:115], 0\n"
      "v_exp_f32 v156, v156\n"
      "v_mfma_f32_16x16x32_bf16 v[136:139], v[148:151], a[116:119], v[136:139]\n"
      "v_cvt_pk_bf16_f32 v210, v156, v157\n"
      "v_mfma_f32_16x16x32_bf16 v[136:139], v[84:87], a[120:123], v[136:139]\n"
      "v_cvt_pk_bf16_f32 v38, v38, v42\n"
      "v_mfma_f32_16x16x32_bf16 v[136:139], v[200:203], a[124:127], v[136:139]\n"
      "v_exp_f32 v154, v154\n"
      "v_mfma_f32_16x16x32_bf16 v[68:71], a[0:3], v[160:163], 0\n"
      "v_exp_f32 v241, v241\n"
      "v_mfma_f32_16x16x32_bf16 v[68:71], a[4:7], v[148:151], v[68:71]\n"
      "v_exp_f32 v152, v152\n"
      "v_mfma_f32_16x16x32_bf16 v[68:71], a[8:11], v[84:87], v[68:71]\n"
      "v_exp_f32 v240, v240\n"
      "v_mfma_f32_16x16x32_bf16 v[68:71], a[12:15], v[200:203], v[68:71]\n"
      "v_cvt_pk_bf16_f32 v41, v45, v49\n"
      "v_pk_max_i16 v41, v41, 0\n"
      "v_mfma_f32_16x16x32_bf16 v[252:255], a[16:19], v[160:163], 0\n"
      "s_waitcnt lgkmcnt(1)\n"
      "buffer_load_dwordx4 v1, s[40:43], s74 offen offset:1024 lds\n"
      "v_mfma_f32_16x16x32_bf16 v[252:255], a[20:23], v[148:151], v[252:255]\n"
      "v_exp_f32 v155, v155\n"
      "v_mfma_f32_16x16x32_bf16 v[252:255], a[24:27], v[84:87], v[252:255]\n"
      "v_exp_f32 v65, v65\n"
      "v_mfma_f32_16x16x32_bf16 v[252:255], a[28:31], v[200:203], v[252:255]\n"
      "v_cvt_pk_bf16_f32 v209, v154, v155\n"
      "v_cvt_pk_bf16_f32 v208, v152, v153\n"
      "v_mfma_f32_16x16x32_bf16 v[152:155], v[160:163], a[96:99], 0\n"
      "v_cvt_pk_bf16_f32 v42, v39, v43\n"
      "v_cvt_pk_bf16_f32 v19, v102, v103\n"
      "v_mfma_f32_16x16x32_bf16 v[152:155], v[148:151], a[100:103], v[152:155]\n"
      "v_exp_f32 v100, v100\n"
      "v_mfma_f32_16x16x32_bf16 v[152:155], v[84:87], a[104:107], v[152:155]\n"
      "ds_read2_b32 v[160:161], v16 offset0:0 offset1:2\n"
      "v_mfma_f32_16x16x32_bf16 v[152:155], v[200:203], a[108:111], v[152:155]\n"
      "buffer_store_dwordx2 v[32:33], v2, s[44:47], s100 offen offset:256 nt sc1\n"
      "v_cvt_pk_bf16_f32 v25, v82, v83\n"
      "v_mfma_f32_16x16x32_bf16 v[148:151], a[176:179], v[92:95], 0\n"
      "v_exp_f32 v168, v168\n"
      "v_mfma_f32_16x16x32_bf16 v[148:151], a[180:183], v[88:91], v[148:151]\n"
      "v_cvt_pk_bf16_f32 v21, v242, v243\n"
      "v_mfma_f32_16x16x32_bf16 v[148:151], a[184:187], v[96:99], v[148:151]\n"
      "v_exp_f32 v170, v170\n"
      "v_mfma_f32_16x16x32_bf16 v[148:151], a[188:191], v[176:179], v[148:151]\n"
      "v_exp_f32 v67, v67\n"
      "v_mfma_f32_16x16x32_bf16 v[200:203], a[160:163], v[92:95], 0\n"
      "v_mfma_f32_16x16x32_bf16 v[200:203], a[164:167], v[88:91], v[200:203]\n"
      "v_exp_f32 v171, v171\n"
      "v_mfma_f32_16x16x32_bf16 v[200:203], a[168:171], v[96:99], v[200:203]\n"
      "v_mfma_f32_16x16x32_bf16 v[200:203], a[172:175], v[176:179], v[200:203]\n"
      "v_cvt_pk_bf16_f32 v205, v66, v67\n"
      "v_cvt_pk_bf16_f32 v204, v64, v65\n"
      "v_mfma_f32_16x16x32_bf16 v[64:67], a[32:35], v[92:95], 0\n"
      "v_mfma_f32_16x16x32_bf16 v[64:67], a[36:39], v[88:91], v[64:67]\n"
      "v_cvt_pk_bf16_f32 v18, v100, v101\n"
      "v_cvt_pk_bf16_f32 v207, v170, v171\n"
      "v_mfma_f32_16x16x32_bf16 v[64:67], a[40:43], v[96:99], v[64:67]\n"
      "v_mfma_f32_16x16x32_bf16 v[64:67], a[44:47], v[176:179], v[64:67]\n"
      "v_exp_f32 v169, v169\n"
      "v_mfma_f32_16x16x32_bf16 v[248:251], a[0:3], v[92:95], 0\n"
      "v_mfma_f32_16x16x32_bf16 v[248:251], a[4:7], v[88:91], v[248:251]\n"
      "v_cvt_pk_bf16_f32 v206, v168, v169\n"
      "v_mfma_f32_16x16x32_bf16 v[248:251], a[8:11], v[96:99], v[248:251]\n"
      "v_cvt_pk_bf16_f32 v244, v200, v201\n"
      "v_mfma_f32_16x16x32_bf16 v[248:251], a[12:15], v[176:179], v[248:251]\n"
      "ds_read_b128 v[100:103], v0 offset:16896\n"
      "v_cvt_pk_bf16_f32 v20, v240, v241\n"
      "v_smfmac_f32_16x16x64_bf16 v[128:131], v[12:15], v[204:211], v5\n"
      "v_mfma_f32_16x16x32_bf16 v[224:227], v[204:207], v[232:235], v[224:227]\n"
      "v_mfma_f32_16x16x32_bf16 v[224:227], v[208:211], v[236:239], v[224:227]\n"
      "v_cvt_pk_bf16_f32 v43, v47, v51\n"
      "v_cvt_pk_bf16_f32 v245, v202, v203\n"
      "v_mfma_f32_16x16x32_bf16 v[224:227], v[18:21], v[228:231], v[224:227]\n"
      "buffer_load_dwordx4 v1, s[40:43], s73 offen offset:2048 lds\n"
      "v_smfmac_f32_16x16x32_bf16 v[128:131], v[12:13], v[18:21], v5\n"
      "buffer_store_dwordx2 v[192:193], v2, s[44:47], s101 offen offset:256 nt sc1\n"
      "v_mfma_f32_16x16x32_bf16 v[232:235], a[16:19], v[92:95], 0\n"
      "v_mfma_f32_16x16x32_bf16 v[232:235], a[20:23], v[88:91], v[232:235]\n"
      "ds_read_b128 v[168:171], v9 offset:2048\n"
      "v_mfma_f32_16x16x32_bf16 v[232:235], a[24:27], v[96:99], v[232:235]\n"
      "ds_read2_b32 v[162:163], v16 offset0:128 offset1:130\n"
      "v_mfma_f32_16x16x32_bf16 v[232:235], a[28:31], v[176:179], v[232:235]\n"
      "v_cvt_pk_bf16_f32 v37, v44, v48\n"
      "v_cvt_pk_bf16_f32 v24, v80, v81\n"
      "v_mfma_f32_16x16x32_bf16 v[220:223], v[204:207], v[56:59], v[220:223]\n"
      "v_mfma_f32_16x16x32_bf16 v[220:223], v[208:211], v[60:63], v[220:223]\n"
      "s_waitcnt vmcnt(12)\n"
      "ds_read_b128 v[80:83], v0 offset:4096\n"
      "v_mfma_f32_16x16x32_bf16 v[220:223], v[18:21], v[116:119], v[220:223]\n"
      "buffer_load_dwordx4 v1, s[40:43], s74 offen offset:3072 lds\n"
      "v_mfma_f32_16x16x32_bf16 v[212:215], v[204:207], v[108:111], v[212:215]\n"
      "v_mfma_f32_16x16x32_bf16 v[212:215], v[208:211], v[112:115], v[212:215]\n"
      "ds_read_b128 v[116:119], v0 offset:5632\n"
      "v_add_f32 v128, v128, v129\n"
      "v_mfma_f32_16x16x32_bf16 v[212:215], v[18:21], v[52:55], v[212:215]\n"
      "v_cvt_pk_bf16_f32 v22, v252, v253\n"
      "v_cvt_pk_bf16_f32 v236, v136, v137\n"
      "v_mfma_f32_16x16x32_bf16 v[200:203], a[16:19], v[124:127], 0\n"
      "v_cvt_pk_bf16_f32 v136, v248, v249\n"
      "s_waitcnt lgkmcnt(4)\n"
      "v_mfma_f32_16x16x32_bf16 v[200:203], a[20:23], v[100:103], v[200:203]\n"
      "ds_read_b128 v[108:111], v0 offset:4608\n"
      "v_mfma_f32_16x16x32_bf16 v[200:203], a[24:27], v[72:75], v[200:203]\n"
      "v_rcp_f32 v129, v128\n"
      "v_mfma_f32_16x16x32_bf16 v[200:203], a[28:31], v[196:199], v[200:203]\n"
      "ds_bpermute_b32 v130, v4, v129 offset:128\n"
      "v_mfma_f32_16x16x32_bf16 v[216:219], v[204:207], v[180:183], v[216:219]\n"
      "v_mfma_f32_16x16x32_bf16 v[216:219], v[208:211], v[184:187], v[216:219]\n"
      "v_cvt_pk_bf16_f32 v184, v164, v165\n"
      "v_cvt_pk_bf16_f32 v27, v142, v143\n"
      "v_mfma_f32_16x16x32_bf16 v[216:219], v[18:21], v[120:123], v[216:219]\n"
      "v_mfma_f32_16x16x32_bf16 v[56:59], a[128:131], v[124:127], 0\n"
      "v_cvt_pk_bf16_f32 v237, v138, v139\n"
      "v_pk_max_i16 v38, v38, 0\n"
      "v_mfma_f32_16x16x32_bf16 v[56:59], a[132:135], v[100:103], v[56:59]\n"
      "ds_read_b128 v[52:55], v10 offset:0\n"
      "v_cvt_pk_bf16_f32 v185, v166, v167\n"
      "v_mfma_f32_16x16x32_bf16 v[56:59], a[136:139], v[72:75], v[56:59]\n"
      "v_cvt_pk_bf16_f32 v21, v70, v71\n"
      "v_mfma_f32_16x16x32_bf16 v[56:59], a[140:143], v[196:199], v[56:59]\n"
      "buffer_store_dwordx2 v[30:31], v2, s[44:47], s100 offen offset:512 nt sc1\n"
      "ds_read_b128 v[180:183], v0 offset:5120\n"
      "v_mfma_f32_16x16x32_bf16 v[208:211], a[48:51], v[92:95], 0\n"
      "v_mfma_f32_16x16x32_bf16 v[208:211], a[52:55], v[88:91], v[208:211]\n"
      "s_waitcnt lgkmcnt(2)\n"
      "v_mul_f32_dpp v219, v130, v219 row_newbcast:12 row_mask:0xf bank_mask:0xf\n"
      "v_mul_f32_dpp v216, v130, v216 row_newbcast:0 row_mask:0xf bank_mask:0xf\n"
      "v_mfma_f32_16x16x32_bf16 v[208:211], a[56:59], v[96:99], v[208:211]\n"
      "v_cvt_pk_bf16_f32 v20, v68, v69\n"
      "v_mul_f32_dpp v220, v130, v220 row_newbcast:0 row_mask:0xf bank_mask:0xf\n"
      "v_mfma_f32_16x16x32_bf16 v[208:211], a[60:63], v[176:179], v[208:211]\n"
      "v_cvt_pk_bf16_f32 v246, v148, v149\n"
      "v_mfma_f32_16x16x32_bf16 v[112:115], a[128:131], v[92:95], 0\n"
      "v_cvt_pk_bf16_f32 v60, v152, v153\n"
      "v_mfma_f32_16x16x32_bf16 v[112:115], a[132:135], v[88:91], v[112:115]\n"
      "v_mfma_f32_16x16x32_bf16 v[112:115], a[136:139], v[96:99], v[112:115]\n"
      "v_mfma_f32_16x16x32_bf16 v[112:115], a[140:143], v[176:179], v[112:115]\n"
      "v_pk_max_i16 v36, v36, 0\n"
      "v_cvt_pk_bf16_f32 v247, v150, v151\n"
      "v_mfma_f32_16x16x32_bf16 v[148:151], a[16:19], v[80:83], 0\n"
      "v_mfma_f32_16x16x32_bf16 v[148:151], a[20:23], v[108:111], v[148:151]\n"
      "s_waitcnt lgkmcnt(0)\n"
      "v_mfma_f32_16x16x32_bf16 v[148:151], a[24:27], v[180:183], v[148:151]\n"
      "ds_read_b128 v[164:167], v9 offset:0\n"
      "v_mul_f32_dpp v217, v130, v217 row_newbcast:4 row_mask:0xf bank_mask:0xf\n"
      "v_mfma_f32_16x16x32_bf16 v[148:151], a[28:31], v[116:119], v[148:151]\n"
      "v_mul_f32_dpp v214, v130, v214 row_newbcast:8 row_mask:0xf bank_mask:0xf\n"
      "v_mfma_f32_16x16x32_bf16 v[68:71], a[144:147], v[92:95], 0\n"
      "buffer_load_dwordx4 v1, s[40:43], s73 offen offset:0 lds\n"
      "v_mfma_f32_16x16x32_bf16 v[68:71], a[148:151], v[88:91], v[68:71]\n"
      "v_cvt_pk_bf16_f32 v142, v208, v209\n"
      "v_cvt_pk_bf16_f32 v26, v140, v141\n"
      "v_mfma_f32_16x16x32_bf16 v[68:71], a[152:155], v[96:99], v[68:71]\n"
      "v_mfma_f32_16x16x32_bf16 v[68:71], a[156:159], v[176:179], v[68:71]\n"
      "v_cvt_pk_bf16_f32 v39, v46, v50\n"
      "v_mfma_f32_16x16x32_bf16 v[48:51], a[32:35], v[80:83], 0\n"
      "v_mfma_f32_16x16x32_bf16 v[48:51], a[36:39], v[108:111], v[48:51]\n"
      "v_mul_f32_dpp v225, v130, v225 row_newbcast:4 row_mask:0xf bank_mask:0xf\n"
      "v_cvt_pk_bf16_f32 v139, v234, v235\n"
      "v_mfma_f32_16x16x32_bf16 v[48:51], a[40:43], v[180:183], v[48:51]\n"
      "s_add_u32 m0, s53, 16384\n"
      "v_cvt_pk_bf16_f32 v240, v112, v113\n"
      "v_mov_b64 v[152:153], 0\n"
      "v_mfma_f32_16x16x32_bf16 v[48:51], a[44:47], v[116:119], v[48:51]\n"
      "buffer_store_dwordx2 v[194:195], v2, s[44:47], s101 offen offset:768 nt sc1\n"
      "v_cvt_pk_bf16_f32 v23, v254, v255\n"
      "v_mfma_f32_16x16x32_bf16 v[120:123], a[32:35], v[124:127], 0\n"
      "v_cvt_pk_bf16_f32 v61, v154, v155\n"
      "v_cvt_pk_bf16_f32 v241, v114, v115\n"
      "v_mfma_f32_16x16x32_bf16 v[120:123], a[36:39], v[100:103], v[120:123]\n"
      "v_mfma_f32_16x16x32_bf16 v[120:123], a[40:43], v[72:75], v[120:123]\n"
      "v_mfma_f32_16x16x32_bf16 v[120:123], a[44:47], v[196:199], v[120:123]\n"
      "v_mov_b64 v[230:231], 0\n"
      "v_cvt_pk_bf16_f32 v208, v48, v49\n"
      "v_mfma_f32_16x16x32_bf16 v[28:31], v[80:83], a[112:115], 0\n"
      "v_mfma_f32_16x16x32_bf16 v[28:31], v[108:111], a[116:119], v[28:31]\n"
      "v_mfma_f32_16x16x32_bf16 v[28:31], v[180:183], a[120:123], v[28:31]\n"
      "v_cvt_pk_bf16_f32 v207, v150, v151\n"
      "v_mul_f32_dpp v212, v130, v212 row_newbcast:0 row_mask:0xf bank_mask:0xf\n"
      "v_mfma_f32_16x16x32_bf16 v[28:31], v[116:119], a[124:127], v[28:31]\n"
      "v_mul_f32_dpp v218, v130, v218 row_newbcast:8 row_mask:0xf bank_mask:0xf\n"
      "v_cvt_pk_bf16_f32 v143, v210, v211\n"
      "v_mfma_f32_16x16x32_bf16 v[44:47], a[0:3], v[80:83], 0\n"
      "L_h3_%=:\n"
      "s_cbranch_execz L_hr_%=\n"
      "v_mul_f32_dpp v221, v130, v221 row_newbcast:4 row_mask:0xf bank_mask:0xf\n"
      "v_mul_f32_dpp v224, v130, v224 row_newbcast:0 row_mask:0xf bank_mask:0xf\n"
      "v_mfma_f32_16x16x32_bf16 v[44:47], a[4:7], v[108:111], v[44:47]\n"
      "buffer_load_dwordx4 v1, s[40:43], s76 offen offset:3072 nt lds\n"
      "v_mfma_f32_16x16x32_bf16 v[44:47], a[8:11], v[180:183], v[44:47]\n"
      "v_mul_f32_dpp v215, v130, v215 row_newbcast:12 row_mask:0xf bank_mask:0xf\n"
      "v_cvt_pk_bf16_f32 v141, v66, v67\n"
      "v_mfma_f32_16x16x32_bf16 v[44:47], a[12:15], v[116:119], v[44:47]\n"
      "v_mfma_f32_16x16x32_bf16 v[154:157], a[48:51], v[80:83], 0\n"
      "v_pk_max_i16 v40, v40, 0\n"
      "v_cvt_pk_bf16_f32 v242, v68, v69\n"
      "v_mfma_f32_16x16x32_bf16 v[154:157], a[52:55], v[108:111], v[154:157]\n"
      "v_cvt_pk_bf16_f32 v138, v232, v233\n"
      "v_mfma_f32_16x16x32_bf16 v[154:157], a[56:59], v[180:183], v[154:157]\n"
      "v_cvt_pk_bf16_f32 v243, v70, v71\n"
      "v_mfma_f32_16x16x32_bf16 v[154:157], a[60:63], v[116:119], v[154:157]\n"
      "buffer_store_dwordx2 v[34:35], v2, s[44:47], s100 offen offset:768 nt sc1\n"
      "v_mul_f32_dpp v226, v130, v226 row_newbcast:8 row_mask:0xf bank_mask:0xf\n"
      "v_mfma_f32_16x16x32_bf16 v[112:115], a[0:3], v[124:127], 0\n"
      "v_mul_f32_dpp v213, v130, v213 row_newbcast:4 row_mask:0xf bank_mask:0xf\n"
      "v_mul_f32_dpp v223, v130, v223 row_newbcast:12 row_mask:0xf bank_mask:0xf\n"
      "v_mfma_f32_16x16x32_bf16 v[112:115], a[4:7], v[100:103], v[112:115]\n"
      "v_cvt_pk_bf16_f32 v66, v200, v201\n"
      "v_cvt_pk_bf16_f32 v235, v30, v31\n"
      "v_mfma_f32_16x16x32_bf16 v[112:115], a[8:11], v[72:75], v[112:115]\n"
      "v_mfma_f32_16x16x32_bf16 v[112:115], a[12:15], v[196:199], v[112:115]\n"
      "v_cvt_pk_bf16_f32 v67, v202, v203\n"
      "v_cvt_pk_bf16_f32 v68, v120, v121\n"
      "v_mfma_f32_16x16x32_bf16 v[252:255], v[80:83], a[96:99], 0\n"
      "v_cvt_pk_bf16_f32 v211, v156, v157\n"
      "v_mfma_f32_16x16x32_bf16 v[252:255], v[108:111], a[100:103], v[252:255]\n"
      "v_mul_f32_dpp v227, v130, v227 row_newbcast:12 row_mask:0xf bank_mask:0xf\n"
      "v_cvt_pk_bf16_f32 v140, v64, v65\n"
      "v_mfma_f32_16x16x32_bf16 v[252:255], v[180:183], a[104:107], v[252:255]\n"
      "v_cvt_pk_bf16_f32 v137, v250, v251\n"
      "v_cvt_pk_bf16_f32 v205, v46, v47\n"
      "v_mfma_f32_16x16x32_bf16 v[252:255], v[116:119], a[108:111], v[252:255]\n"
      "v_cvt_pk_bf16_f32 v64, v112, v113\n"
      "v_mfma_f32_16x16x32_bf16 v[248:251], a[48:51], v[124:127], 0\n"
      "ds_read_b128 v[192:195], v0 offset:12288\n"
      "v_cvt_pk_bf16_f32 v69, v122, v123\n"
      "v_mfma_f32_16x16x32_bf16 v[248:251], a[52:55], v[100:103], v[248:251]\n"
      "v_pk_max_i16 v42, v42, 0\n"
      "v_mfma_f32_16x16x32_bf16 v[248:251], a[56:59], v[72:75], v[248:251]\n"
      "v_mfma_f32_16x16x32_bf16 v[248:251], a[60:63], v[196:199], v[248:251]\n"
      "v_cvt_pk_bf16_f32 v234, v28, v29\n"
      "v_mul_f32_dpp v222, v130, v222 row_newbcast:8 row_mask:0xf bank_mask:0xf\n"
      "v_mfma_f32_16x16x32_bf16 v[32:35], v[136:139], v[240:243], 0\n"
      "v_mfma_f32_16x16x32_bf16 v[32:35], v[140:143], v[244:247], v[32:35]\n"
      "v_cvt_pk_bf16_f32 v210, v154, v155\n"
      "v_cvt_pk_bf16_f32 v206, v148, v149\n"
      "v_mfma_f32_16x16x32_bf16 v[216:219], v[144:147], a[208:211], v[216:219]\n"
      "v_cvt_pk_bf16_f32 v29, v58, v59\n"
      "v_mfma_f32_16x16x32_bf16 v[216:219], v[104:107], a[212:215], v[216:219]\n"
      "v_mfma_f32_16x16x32_bf16 v[216:219], v[172:175], a[216:219], v[216:219]\n"
      "v_cvt_pk_bf16_f32 v65, v114, v115\n"
      "v_mfma_f32_16x16x32_bf16 v[216:219], v[132:135], a[220:223], v[216:219]\n"
      "v_exp_f32 v33, v33\n"
      "v_mfma_f32_16x16x32_bf16 v[212:215], v[144:147], a[192:195], v[212:215]\n"
      "v_mov_b64 v[120:121], 0\n"
      "v_mfma_f32_16x16x32_bf16 v[212:215], v[104:107], a[196:199], v[212:215]\n"
      "v_cvt_pk_bf16_f32 v70, v248, v249\n"
      "v_cvt_pk_bf16_f32 v209, v50, v51\n"
      "v_mfma_f32_16x16x32_bf16 v[212:215], v[172:175], a[200:203], v[212:215]\n"
      "v_exp_f32 v34, v34\n"
      "v_mfma_f32_16x16x32_bf16 v[212:215], v[132:135], a[204:207], v[212:215]\n"
      "buffer_load_dwordx4 v1, s[40:43], s75 offen offset:0 nt lds\n"
      "v_mfma_f32_16x16x32_bf16 v[220:223], v[144:147], a[224:227], v[220:223]\n"
      "v_mfma_f32_16x16x32_bf16 v[220:223], v[104:107], a[228:231], v[220:223]\n"
      "ds_read_b128 v[48:51], v0 offset:13824\n"
      "v_cvt_pk_bf16_f32 v28, v56, v57\n"
      "v_mfma_f32_16x16x32_bf16 v[220:223], v[172:175], a[232:235], v[220:223]\n"
      "v_cvt_pk_bf16_f32 v204, v44, v45\n"
      "v_mov_b64 v[248:249], 0\n"
      "v_mfma_f32_16x16x32_bf16 v[220:223], v[132:135], a[236:239], v[220:223]\n"
      "v_mfma_f32_16x16x32_bf16 v[224:227], v[144:147], a[240:243], v[224:227]\n"
      "s_waitcnt vmcnt(13)\n"
      "ds_read_b128 v[44:47], v0 offset:0\n"
      "ds_read_b128 v[148:151], v0 offset:1024\n"
      "v_mfma_f32_16x16x32_bf16 v[224:227], v[104:107], a[244:247], v[224:227]\n"
      "ds_read_b128 v[200:203], v0 offset:12800\n"
      "v_cvt_pk_bf16_f32 v214, v214, v218\n"
      "v_mfma_f32_16x16x32_bf16 v[224:227], v[172:175], a[248:251], v[224:227]\n"
      "v_cvt_pk_bf16_f32 v113, v78, v79\n"
      "v_cvt_pk_bf16_f32 v212, v212, v216\n"
      "v_mfma_f32_16x16x32_bf16 v[224:227], v[132:135], a[252:255], v[224:227]\n"
      "v_cvt_pk_bf16_f32 v71, v250, v251\n"
      "s_waitcnt lgkmcnt(5)\n"
      "v_smfmac_f32_16x16x64_bf16 v[230:233], v[52:55], v[164:171], v5\n"
      "ds_read_b128 v[164:167], v0 offset:1536\n"
      "v_cvt_pk_bf16_f32 v218, v215, v219\n"
      "v_smfmac_f32_16x16x64_bf16 v[248:251], v[52:55], v[20:27], v5\n"
      "buffer_store_dwordx2 v[188:189], v2, s[44:47], s101 offen offset:0 nt sc1\n"
      "v_smfmac_f32_16x16x64_bf16 v[120:123], v[52:55], v[204:211], v5\n"
      "ds_read_b128 v[104:107], v0 offset:13312\n"
      "v_cvt_pk_bf16_f32 v59, v254, v255\n"
      "v_mfma_f32_16x16x32_bf16 v[132:135], v[80:83], a[64:67], 0\n"
      "v_mfma_f32_16x16x32_bf16 v[132:135], v[108:111], a[68:71], v[132:135]\n"
      "ds_read_b128 v[168:171], v0 offset:512\n"
      "v_add_f32 v230, v230, v231\n"
      "v_mfma_f32_16x16x32_bf16 v[132:135], v[180:183], a[72:75], v[132:135]\n"
      "v_mfma_f32_16x16x32_bf16 v[132:135], v[116:119], a[76:79], v[132:135]\n"
      "v_cvt_pk_bf16_f32 v216, v213, v217\n"
      "v_cvt_pk_bf16_f32 v215, v222, v226\n"
      "v_mfma_f32_16x16x32_bf16 v[154:157], v[80:83], a[80:83], 0\n"
      "v_pk_max_i16 v39, v39, 0\n"
      "v_mov_b64 v[144:145], 0\n"
      "v_mfma_f32_16x16x32_bf16 v[154:157], v[108:111], a[84:87], v[154:157]\n"
      "v_cvt_pk_bf16_f32 v217, v221, v225\n"
      "v_mfma_f32_16x16x32_bf16 v[154:157], v[180:183], a[88:91], v[154:157]\n"
      "v_cvt_pk_bf16_f32 v111, v134, v135\n"
      "v_cvt_pk_bf16_f32 v219, v223, v227\n"
      "v_mfma_f32_16x16x32_bf16 v[154:157], v[116:119], a[92:95], v[154:157]\n"
      "v_pk_max_i16 v37, v37, 0\n"
      "v_cvt_pk_bf16_f32 v112, v76, v77\n"
      "v_mfma_f32_16x16x32_bf16 v[80:83], v[64:67], v[240:243], 0\n"
      "v_maximum3_f32 v8, v8, v128, v129\n"
      "v_pk_max_i16 v43, v43, 0\n"
      "v_mfma_f32_16x16x32_bf16 v[80:83], v[68:71], v[244:247], v[80:83]\n"
      "v_pk_max_i16 v219, v219, 0\n"
      "s_waitcnt lgkmcnt(5)\n"
      "v_mfma_f32_16x16x32_bf16 v[172:175], v[44:47], a[80:83], 0\n"
      "s_waitcnt lgkmcnt(0)\n"
      "v_mfma_f32_16x16x32_bf16 v[172:175], v[168:171], a[84:87], v[172:175]\n"
      "v_cvt_pk_bf16_f32 v213, v220, v224\n"
      "v_mfma_f32_16x16x32_bf16 v[172:175], v[148:151], a[88:91], v[172:175]\n"
      "v_mfma_f32_16x16x32_bf16 v[172:175], v[164:167], a[92:95], v[172:175]\n"
      "buffer_store_dwordx2 v[42:43], v2, s[44:47], s34 offen offset:768 nt sc1\n"
      "v_add_f32 v248, v248, v249\n"
      "v_mfma_f32_16x16x32_bf16 v[76:79], v[44:47], a[112:115], 0\n"
      "v_exp_f32 v80, v80\n"
      "v_mfma_f32_16x16x32_bf16 v[76:79], v[168:171], a[116:119], v[76:79]\n"
      "v_cvt_pk_bf16_f32 v182, v154, v155\n"
      "v_pk_max_i16 v212, v212, 0\n"
      "v_mfma_f32_16x16x32_bf16 v[76:79], v[148:151], a[120:123], v[76:79]\n"
      "v_exp_f32 v82, v82\n"
      "v_mfma_f32_16x16x32_bf16 v[76:79], v[164:167], a[124:127], v[76:79]\n"
      "v_cvt_pk_bf16_f32 v180, v172, v173\n"
      "v_mov_b64 v[146:147], 0\n"
      "v_mfma_f32_16x16x32_bf16 v[224:227], v[44:47], a[96:99], 0\n"
      "v_mfma_f32_16x16x32_bf16 v[224:227], v[168:171], a[100:103], v[224:227]\n"
      "buffer_load_dwordx4 v1, s[40:43], s75 offen offset:2048 nt lds\n"
      "v_mfma_f32_16x16x32_bf16 v[224:227], v[148:151], a[104:107], v[224:227]\n"
      "v_cvt_pk_bf16_f32 v110, v132, v133\n"
      "v_mfma_f32_16x16x32_bf16 v[224:227], v[164:167], a[108:111], v[224:227]\n"
      "v_mov_b64 v[116:117], 0\n"
      "v_cvt_pk_bf16_f32 v232, v76, v77\n"
      "v_smfmac_f32_16x16x64_bf16 v[152:155], v[52:55], v[64:71], v5\n"
      "v_cvt_pk_bf16_f32 v233, v78, v79\n"
      "v_smfmac_f32_16x16x64_bf16 v[116:119], v[52:55], v[136:143], v5\n"
      "v_mov_b32 v231, 0\n"
      "v_mfma_f32_16x16x32_bf16 v[132:135], v[44:47], a[64:67], 0\n"
      "v_exp_f32 v248, v248\n"
      "v_mfma_f32_16x16x32_bf16 v[132:135], v[168:171], a[68:71], v[132:135]\n"
      "v_exp_f32 v81, v81\n"
      "v_mfma_f32_16x16x32_bf16 v[132:135], v[148:151], a[72:75], v[132:135]\n"
      "v_exp_f32_dpp v231, v230 quad_perm:[0,1,2,3] row_mask:0xf bank_mask:0x1\n"
      "v_mfma_f32_16x16x32_bf16 v[132:135], v[164:167], a[76:79], v[132:135]\n"
      "v_exp_f32 v32, v32\n"
      "v_mfma_f32_16x16x32_bf16 v[76:79], a[32:35], v[44:47], 0\n"
      "v_cvt_pk_bf16_f32 v58, v252, v253\n"
      "v_mfma_f32_16x16x32_bf16 v[76:79], a[36:39], v[168:171], v[76:79]\n"
      "v_cvt_pk_bf16_f32 v183, v156, v157\n"
      "v_mfma_f32_16x16x32_bf16 v[76:79], a[40:43], v[148:151], v[76:79]\n"
      "v_mfma_f32_16x16x32_bf16 v[76:79], a[44:47], v[164:167], v[76:79]\n"
      "buffer_store_dwordx2 v[40:41], v2, s[44:47], s34 offen offset:256 nt sc1\n"
      "v_mov_b32_dpp v230, v231 quad_perm:[1,1,3,3] row_mask:0xf bank_mask:0xf\n"
      "v_mfma_f32_16x16x32_bf16 v[252:255], a[16:19], v[44:47], 0\n"
      "v_cvt_pk_bf16_f32 v128, v80, v81\n"
      "v_add_f32 v152, v152, v153\n"
      "v_mfma_f32_16x16x32_bf16 v[252:255], a[20:23], v[168:171], v[252:255]\n"
      "v_add_f32 v116, v116, v117\n"
      "v_add_f32 v120, v120, v121\n"
      "v_mfma_f32_16x16x32_bf16 v[252:255], a[24:27], v[148:151], v[252:255]\n"
      "v_cvt_pk_bf16_f32 v109, v134, v135\n"
      "v_pk_max_i16 v217, v217, 0\n"
      "v_mfma_f32_16x16x32_bf16 v[252:255], a[28:31], v[164:167], v[252:255]\n"
      "v_exp_f32 v35, v35\n"
      "v_mfma_f32_16x16x32_bf16 v[220:223], v[204:207], v[240:243], 0\n"
      "v_cvt_pk_bf16_f32 v57, v226, v227\n"
      "v_cvt_pk_bf16_f32 v230, v231, v230\n"
      "v_mfma_f32_16x16x32_bf16 v[220:223], v[208:211], v[244:247], v[220:223]\n"
      "v_exp_f32 v83, v83\n"
      "v_mfma_f32_16x16x32_bf16 v[154:157], a[48:51], v[44:47], 0\n"
      "v_exp_f32 v116, v116\n"
      "v_mfma_f32_16x16x32_bf16 v[154:157], a[52:55], v[168:171], v[154:157]\n"
      "v_cvt_pk_bf16_f32 v181, v174, v175\n"
      "v_pk_max_i16 v218, v218, 0\n"
      "v_mfma_f32_16x16x32_bf16 v[154:157], a[56:59], v[148:151], v[154:157]\n"
      "v_cvt_pk_bf16_f32 v108, v132, v133\n"
      "v_cvt_pk_bf16_f32 v130, v32, v33\n"
      "v_mfma_f32_16x16x32_bf16 v[154:157], a[60:63], v[164:167], v[154:157]\n"
      "v_exp_f32 v222, v222\n"
      "v_mfma_f32_16x16x32_bf16 v[132:135], a[0:3], v[44:47], 0\n"
      "v_mov_b64 v[172:173], 0\n"
      "v_pk_max_i16 v214, v214, 0\n"
      "v_mfma_f32_16x16x32_bf16 v[132:135], a[4:7], v[168:171], v[132:135]\n"
      "v_cvt_pk_bf16_f32 v56, v224, v225\n"
      "v_mov_b64 v[224:225], 0\n"
      "v_mfma_f32_16x16x32_bf16 v[132:135], a[8:11], v[148:151], v[132:135]\n"
      "v_exp_f32 v223, v223\n"
      "v_mfma_f32_16x16x32_bf16 v[132:135], a[12:15], v[164:167], v[132:135]\n"
      "v_exp_f32 v152, v152\n"
      "v_mfma_f32_16x16x32_bf16 v[40:43], a[48:51], v[192:195], 0\n"
      "v_cvt_pk_bf16_f32 v169, v78, v79\n"
      "v_cndmask_b32_dpp v153, v116, v152, vcc quad_perm:[0,0,2,2] row_mask:0xf bank_mask:0xf\n"
      "v_mfma_f32_16x16x32_bf16 v[40:43], a[52:55], v[200:203], v[40:43]\n"
      "v_cvt_pk_bf16_f32 v171, v156, v157\n"
      "v_cvt_pk_bf16_f32 v166, v252, v253\n"
      "v_mfma_f32_16x16x32_bf16 v[40:43], a[56:59], v[104:107], v[40:43]\n"
      "v_cvt_pk_bf16_f32 v131, v34, v35\n"
      "v_cvt_pk_bf16_f32 v170, v154, v155\n"
      "v_mfma_f32_16x16x32_bf16 v[40:43], a[60:63], v[48:51], v[40:43]\n"
      "v_cvt_pk_bf16_f32 v129, v82, v83\n"
      "v_cvt_pk_bf16_f32 v165, v134, v135\n"
      "v_mfma_f32_16x16x32_bf16 v[148:151], a[0:3], v[192:195], 0\n"
      "v_mfma_f32_16x16x32_bf16 v[148:151], a[4:7], v[200:203], v[148:151]\n"
      "v_mov_b32_dpp v44, v160 row_ror:8 row_mask:0xf bank_mask:0xf\n"
      "v_mfma_f32_16x16x32_bf16 v[148:151], a[8:11], v[104:107], v[148:151]\n"
      "v_exp_f32 v221, v221\n"
      "v_mfma_f32_16x16x32_bf16 v[148:151], a[12:15], v[48:51], v[148:151]\n"
      "buffer_store_dwordx2 v[36:37], v2, s[44:47], s34 offen offset:0 nt sc1\n"
      "v_pk_max_i16 v213, v213, 0\n"
      "v_mfma_f32_16x16x32_bf16 v[32:35], a[16:19], v[192:195], 0\n"
      "v_cvt_pk_bf16_f32 v82, v40, v41\n"
      "v_cvt_pk_bf16_f32 v167, v254, v255\n"
      "v_mfma_f32_16x16x32_bf16 v[32:35], a[20:23], v[200:203], v[32:35]\n"
      "v_cvt_pk_bf16_f32 v168, v76, v77\n"
      "v_cvt_pk_bf16_f32 v164, v132, v133\n"
      "v_mfma_f32_16x16x32_bf16 v[32:35], a[24:27], v[104:107], v[32:35]\n"
      "buffer_load_dwordx4 v1, s[40:43], s76 offen offset:1024 nt lds\n"
      "v_mfma_f32_16x16x32_bf16 v[32:35], a[28:31], v[48:51], v[32:35]\n"
      "v_cvt_pk_bf16_f32 v76, v148, v149\n"
      "v_pk_max_i16 v216, v216, 0\n"
      "v_mfma_f32_16x16x32_bf16 v[132:135], a[32:35], v[192:195], 0\n"
      "v_exp_f32 v120, v120\n"
      "v_mfma_f32_16x16x32_bf16 v[132:135], a[36:39], v[200:203], v[132:135]\n"
      "v_mov_b32_dpp v45, v161 row_ror:8 row_mask:0xf bank_mask:0xf\n"
      "v_cvt_pk_bf16_f32 v251, v222, v223\n"
      "v_mfma_f32_16x16x32_bf16 v[132:135], a[40:43], v[104:107], v[132:135]\n"
      "v_exp_f32 v220, v220\n"
      "v_mfma_f32_16x16x32_bf16 v[132:135], a[44:47], v[48:51], v[132:135]\n"
      "v_cvt_pk_bf16_f32 v77, v150, v151\n"
      "s_add_u32 m0, s53, 12288\n"
      "v_smfmac_f32_16x16x64_bf16 v[224:227], v[52:55], v[164:171], v5\n"
      "v_cvt_pk_bf16_f32 v78, v32, v33\n"
      "v_mfma_f32_16x16x32_bf16 v[252:255], v[164:167], v[240:243], 0\n"
      "v_cvt_pk_bf16_f32 v83, v42, v43\n"
      "v_pk_max_i16 v215, v215, 0\n"
      "v_mfma_f32_16x16x32_bf16 v[252:255], v[168:171], v[244:247], v[252:255]\n"
      "v_cvt_pk_bf16_f32 v79, v34, v35\n"
      "v_cvt_pk_bf16_f32 v81, v134, v135\n"
      "v_mfma_f32_16x16x32_bf16 v[40:43], v[20:23], v[240:243], 0\n"
      "v_add_f32 v224, v224, v225\n"
      "v_cvt_pk_bf16_f32 v80, v132, v133\n"
      "v_mfma_f32_16x16x32_bf16 v[40:43], v[24:27], v[244:247], v[40:43]\n"
      "v_exp_f32 v224, v224\n"
      "v_smfmac_f32_16x16x64_bf16 v[172:175], v[52:55], v[76:83], v5\n"
      "buffer_load_dwordx4 v1, s[40:43], s77 offen offset:0 lds\n"
      "v_mfma_f32_16x16x32_bf16 v[52:55], a[176:179], v[124:127], 0\n"
      "v_exp_f32 v253, v253\n"
      "v_mfma_f32_16x16x32_bf16 v[52:55], a[180:183], v[100:103], v[52:55]\n"
      "v_cvt_pk_bf16_f32 v250, v220, v221\n"
      "v_mov_b32_dpp v47, v163 row_ror:8 row_mask:0xf bank_mask:0xf\n"
      "v_mfma_f32_16x16x32_bf16 v[52:55], a[184:187], v[72:75], v[52:55]\n"
      "v_exp_f32 v252, v252\n"
      "v_mfma_f32_16x16x32_bf16 v[52:55], a[188:191], v[196:199], v[52:55]\n"
      "v_exp_f32 v41, v41\n"
      "v_mfma_f32_16x16x32_bf16 v[220:223], a[160:163], v[124:127], 0\n"
      "v_exp_f32 v254, v254\n"
      "v_mfma_f32_16x16x32_bf16 v[220:223], a[164:167], v[100:103], v[220:223]\n"
      "v_add_f32 v172, v172, v173\n"
      "v_cndmask_b32_dpp v225, v248, v224, vcc quad_perm:[0,0,2,2] row_mask:0xf bank_mask:0xf\n"
      "v_mfma_f32_16x16x32_bf16 v[220:223], a[168:171], v[72:75], v[220:223]\n"
      "v_exp_f32 v43, v43\n"
      "v_mfma_f32_16x16x32_bf16 v[220:223], a[172:175], v[196:199], v[220:223]\n"
      "v_cvt_pk_bf16_f32 v35, v54, v55\n"
      "v_mfma_f32_16x16x32_bf16 v[132:135], a[144:147], v[124:127], 0\n"
      "v_exp_f32 v172, v172\n"
      "v_mfma_f32_16x16x32_bf16 v[132:135], a[148:151], v[100:103], v[132:135]\n"
      "v_cvt_pk_bf16_f32 v34, v52, v53\n"
      "v_cndmask_b32_dpp v121, v172, v120, vcc quad_perm:[0,0,2,2] row_mask:0xf bank_mask:0xf\n"
      "v_mfma_f32_16x16x32_bf16 v[132:135], a[152:155], v[72:75], v[132:135]\n"
      "s_not_b64 vcc, vcc\n"
      "v_cndmask_b32_dpp v249, v224, v248, vcc quad_perm:[1,1,3,3] row_mask:0xf bank_mask:0xf\n"
      "v_add_f32 v224, v224, v120\n"
      "v_mfma_f32_16x16x32_bf16 v[132:135], a[156:159], v[196:199], v[132:135]\n"
      "v_add_f32 v248, v248, v172\n"
      "v_cndmask_b32_dpp v117, v152, v116, vcc quad_perm:[1,1,3,3] row_mask:0xf bank_mask:0xf\n"
      "v_mfma_f32_16x16x32_bf16 v[52:55], v[76:79], v[240:243], 0\n"
      "v_cvt_pk_bf16_f32 v153, v153, v117\n"
      "v_cvt_pk_bf16_f32 v32, v220, v221\n"
      "v_mfma_f32_16x16x32_bf16 v[52:55], v[80:83], v[244:247], v[52:55]\n"
      "buffer_store_dwordx2 v[38:39], v2, s[44:47], s34 offen offset:512 nt sc1\n"
      "v_add_f32 v152, v152, v116\n"
      "v_mfma_f32_16x16x32_bf16 v[116:119], v[192:195], a[96:99], 0\n"
      "v_add_f32 v224, v224, v248\n"
      "v_cvt_pk_bf16_f32 v225, v225, v249\n"
      "v_mfma_f32_16x16x32_bf16 v[116:119], v[200:203], a[100:103], v[116:119]\n"
      "v_add_f32 v152, v152, v231\n"
      "v_cvt_pk_bf16_f32 v30, v132, v133\n"
      "v_mfma_f32_16x16x32_bf16 v[116:119], v[104:107], a[104:107], v[116:119]\n"
      "v_cvt_pk_bf16_f32 v33, v222, v223\n"
      "v_cvt_pk_bf16_f32 v31, v134, v135\n"
      "v_mfma_f32_16x16x32_bf16 v[116:119], v[48:51], a[108:111], v[116:119]\n"
      "ds_bpermute_b32 v174, v6, v225 offset:16\n"
      "v_mfma_f32_16x16x32_bf16 v[220:223], v[20:23], v[28:31], 0\n"
      "v_exp_f32 v54, v54\n"
      "v_mfma_f32_16x16x32_bf16 v[220:223], v[24:27], v[32:35], v[220:223]\n"
      "v_cvt_pk_bf16_f32 v248, v252, v253\n"
      "v_cndmask_b32_dpp v173, v120, v172, vcc quad_perm:[1,1,3,3] row_mask:0xf bank_mask:0xf\n"
      "v_mfma_f32_16x16x32_bf16 v[36:39], v[76:79], v[28:31], 0\n"
      "v_cvt_pk_bf16_f32 v121, v121, v173\n"
      "v_add_f32 v18, v224, v152\n"
      "v_mfma_f32_16x16x32_bf16 v[36:39], v[80:83], v[32:35], v[36:39]\n"
      "ds_bpermute_b32 v173, v6, v121\n"
      "v_mfma_f32_16x16x32_bf16 v[132:135], v[192:195], a[80:83], 0\n"
      "v_exp_f32 v40, v40\n"
      "v_mfma_f32_16x16x32_bf16 v[132:135], v[200:203], a[84:87], v[132:135]\n"
      "v_exp_f32 v52, v52\n"
      "v_mfma_f32_16x16x32_bf16 v[132:135], v[104:107], a[88:91], v[132:135]\n"
      "v_add_f32_dpp v18, v18, v18 row_ror:8 row_mask:0xf bank_mask:0xf\n"
      "v_mov_b32_dpp v46, v162 row_ror:8 row_mask:0xf bank_mask:0xf\n"
      "v_mfma_f32_16x16x32_bf16 v[132:135], v[48:51], a[92:95], v[132:135]\n"
      "v_exp_f32 v42, v42\n"
      "v_smfmac_f32_16x16x64_bf16 v[144:147], v[160:163], v[240:247], v5\n"
      "ds_bpermute_b32 v158, v7, v230 offset:248\n"
      "v_smfmac_f32_16x16x64_bf16 v[144:147], v[44:47], v[28:35], v5\n"
      "ds_bpermute_b32 v175, v6, v121 offset:16\n"
      "v_mfma_f32_16x16x32_bf16 v[120:123], v[204:207], v[28:31], 0\n"
      "v_exp_f32 v39, v39\n"
      "v_mfma_f32_16x16x32_bf16 v[120:123], v[208:211], v[32:35], v[120:123]\n"
      "v_exp_f32 v223, v223\n"
      "v_mfma_f32_16x16x32_bf16 v[148:151], v[164:167], v[28:31], 0\n"
      "v_exp_f32 v255, v255\n"
      "v_mfma_f32_16x16x32_bf16 v[148:151], v[168:171], v[32:35], v[148:151]\n"
      "v_cvt_pk_bf16_f32 v187, v134, v135\n"
      "v_cvt_pk_bf16_f32 v186, v132, v133\n"
      "v_mfma_f32_16x16x32_bf16 v[244:247], v[64:67], v[28:31], 0\n"
      "buffer_load_dwordx4 v1, s[40:43], s78 offen offset:1024 lds\n"
      "v_mfma_f32_16x16x32_bf16 v[244:247], v[68:71], v[32:35], v[244:247]\n"
      "buffer_store_dwordx2 v[218:219], v2, s[44:47], s35 offen offset:768 nt sc1\n"
      "v_cvt_pk_bf16_f32 v249, v254, v255\n"
      "v_mfma_f32_16x16x32_bf16 v[132:135], v[92:95], a[64:67], 0\n"
      "v_exp_f32 v151, v151\n"
      "v_mfma_f32_16x16x32_bf16 v[132:135], v[88:91], a[68:71], v[132:135]\n"
      "v_exp_f32 v149, v149\n"
      "v_mfma_f32_16x16x32_bf16 v[132:135], v[96:99], a[72:75], v[132:135]\n"
      "v_exp_f32 v122, v122\n"
      "v_mfma_f32_16x16x32_bf16 v[132:135], v[176:179], a[76:79], v[132:135]\n"
      "v_exp_f32 v53, v53\n"
      "v_mfma_f32_16x16x32_bf16 v[240:243], v[136:139], v[28:31], 0\n"
      "v_exp_f32 v245, v245\n"
      "v_mfma_f32_16x16x32_bf16 v[240:243], v[140:143], v[32:35], v[240:243]\n"
      "v_cvt_pk_bf16_f32 v253, v42, v43\n"
      "v_cvt_pk_bf16_f32 v62, v116, v117\n"
      "v_mfma_f32_16x16x32_bf16 v[32:35], v[192:195], a[112:115], 0\n"
      "v_exp_f32 v55, v55\n"
      "v_mfma_f32_16x16x32_bf16 v[32:35], v[200:203], a[116:119], v[32:35]\n"
      "ds_bpermute_b32 v159, v7, v230 offset:8\n"
      "v_mfma_f32_16x16x32_bf16 v[32:35], v[104:107], a[120:123], v[32:35]\n"
      "v_cvt_pk_bf16_f32 v254, v52, v53\n"
      "v_cvt_pk_bf16_f32 v63, v118, v119\n"
      "v_mfma_f32_16x16x32_bf16 v[32:35], v[48:51], a[124:127], v[32:35]\n"
      "v_exp_f32 v120, v120\n"
      "v_mfma_f32_16x16x32_bf16 v[28:31], v[192:195], a[64:67], 0\n"
      "v_exp_f32 v222, v222\n"
      "v_mfma_f32_16x16x32_bf16 v[28:31], v[200:203], a[68:71], v[28:31]\n"
      "v_exp_f32 v150, v150\n"
      "v_mfma_f32_16x16x32_bf16 v[28:31], v[104:107], a[72:75], v[28:31]\n"
      "v_exp_f32 v246, v246\n"
      "v_mfma_f32_16x16x32_bf16 v[28:31], v[48:51], a[76:79], v[28:31]\n"
      "ds_bpermute_b32 v172, v6, v225\n"
      "v_mfma_f32_16x16x32_bf16 v[116:119], v[124:127], a[64:67], 0\n"
      "v_exp_f32 v38, v38\n"
      "v_mfma_f32_16x16x32_bf16 v[116:119], v[100:103], a[68:71], v[116:119]\n"
      "v_exp_f32 v123, v123\n"
      "v_mfma_f32_16x16x32_bf16 v[116:119], v[72:75], a[72:75], v[116:119]\n"
      "v_exp_f32 v148, v148\n"
      "v_mfma_f32_16x16x32_bf16 v[116:119], v[196:199], a[76:79], v[116:119]\n"
      "v_exp_f32 v121, v121\n"
      "v_mfma_f32_16x16x32_bf16 v[224:227], v[124:127], a[112:115], 0\n"
      "v_cvt_pk_bf16_f32 v115, v30, v31\n"
      "v_cvt_pk_bf16_f32 v114, v28, v29\n"
      "v_mfma_f32_16x16x32_bf16 v[224:227], v[100:103], a[116:119], v[224:227]\n"
      "v_cvt_pk_bf16_f32 v29, v150, v151\n"
      "v_cvt_pk_bf16_f32 v255, v54, v55\n"
      "v_mfma_f32_16x16x32_bf16 v[224:227], v[72:75], a[120:123], v[224:227]\n"
      "v_exp_f32 v221, v221\n"
      "v_mfma_f32_16x16x32_bf16 v[224:227], v[196:199], a[124:127], v[224:227]\n"
      "buffer_store_dwordx2 v[216:217], v2, s[44:47], s35 offen offset:256 nt sc1\n"
      "v_add_f32 v146, v146, v147\n"
      "v_mfma_f32_16x16x32_bf16 v[154:157], v[124:127], a[80:83], 0\n"
      "v_cvt_pk_bf16_f32 v28, v148, v149\n"
      "v_cvt_pk_bf16_f32 v30, v120, v121\n"
      "v_mfma_f32_16x16x32_bf16 v[154:157], v[100:103], a[84:87], v[154:157]\n"
      "v_exp_f32 v241, v241\n"
      "v_mfma_f32_16x16x32_bf16 v[154:157], v[72:75], a[88:91], v[154:157]\n"
      "v_exp_f32 v242, v242\n"
      "v_mfma_f32_16x16x32_bf16 v[154:157], v[196:199], a[92:95], v[154:157]\n"
      "v_cvt_pk_bf16_f32 v229, v226, v227\n"
      "v_cvt_pk_bf16_f32 v252, v40, v41\n"
      "v_mfma_f32_16x16x32_bf16 v[40:43], v[92:95], a[112:115], 0\n"
      "v_exp_f32 v247, v247\n"
      "v_mfma_f32_16x16x32_bf16 v[40:43], v[88:91], a[116:119], v[40:43]\n"
      "v_exp_f32 v36, v36\n"
      "v_mfma_f32_16x16x32_bf16 v[40:43], v[96:99], a[120:123], v[40:43]\n"
      "v_exp_f32 v244, v244\n"
      "v_mfma_f32_16x16x32_bf16 v[40:43], v[176:179], a[124:127], v[40:43]\n"
      "v_exp_f32 v37, v37\n"
      "v_mfma_f32_16x16x32_bf16 v[148:151], v[124:127], a[96:99], 0\n"
      "v_cvt_pk_bf16_f32 v31, v122, v123\n"
      "v_cvt_pk_bf16_f32 v52, v116, v117\n"
      "v_mfma_f32_16x16x32_bf16 v[148:151], v[100:103], a[100:103], v[148:151]\n"
      "v_cvt_pk_bf16_f32 v239, v34, v35\n"
      "v_cvt_pk_bf16_f32 v228, v224, v225\n"
      "v_mfma_f32_16x16x32_bf16 v[148:151], v[72:75], a[104:107], v[148:151]\n"
      "v_exp_f32 v243, v243\n"
      "v_mfma_f32_16x16x32_bf16 v[148:151], v[196:199], a[108:111], v[148:151]\n"
      "v_cvt_pk_bf16_f32 v55, v134, v135\n"
      "v_cvt_pk_bf16_f32 v54, v132, v133\n"
      "v_mfma_f32_16x16x32_bf16 v[216:219], a[176:179], v[192:195], 0\n"
      "v_cvt_pk_bf16_f32 v53, v118, v119\n"
      "v_cvt_pk_bf16_f32 v230, v40, v41\n"
      "v_mfma_f32_16x16x32_bf16 v[216:219], a[180:183], v[200:203], v[216:219]\n"
      "v_cvt_pk_bf16_f32 v121, v156, v157\n"
      "v_cvt_pk_bf16_f32 v120, v154, v155\n"
      "v_mfma_f32_16x16x32_bf16 v[216:219], a[184:187], v[104:107], v[216:219]\n"
      "v_exp_f32 v220, v220\n"
      "v_mfma_f32_16x16x32_bf16 v[216:219], a[188:191], v[48:51], v[216:219]\n"
      "v_exp_f32 v240, v240\n"
      "v_mfma_f32_16x16x32_bf16 v[224:227], v[92:95], a[96:99], 0\n"
      "v_cvt_pk_bf16_f32 v134, v240, v241\n"
      "v_mfma_f32_16x16x32_bf16 v[224:227], v[88:91], a[100:103], v[224:227]\n"
      "v_cvt_pk_bf16_f32 v135, v242, v243\n"
      "v_cvt_pk_bf16_f32 v231, v42, v43\n"
      "v_mfma_f32_16x16x32_bf16 v[224:227], v[96:99], a[104:107], v[224:227]\n"
      "v_cvt_pk_bf16_f32 v132, v244, v245\n"
      "v_cvt_pk_bf16_f32 v42, v216, v217\n"
      "v_mfma_f32_16x16x32_bf16 v[224:227], v[176:179], a[108:111], v[224:227]\n"
      "buffer_store_dwordx2 v[212:213], v2, s[44:47], s35 offen offset:0 nt sc1\n"
      "v_cvt_pk_bf16_f32 v133, v246, v247\n"
      "v_mfma_f32_16x16x32_bf16 v[240:243], a[144:147], v[192:195], 0\n"
      "v_cvt_pk_bf16_f32 v238, v32, v33\n"
      "v_add_f32 v144, v144, v145\n"
      "v_mfma_f32_16x16x32_bf16 v[240:243], a[148:151], v[200:203], v[240:243]\n"
      "v_exp_f32 v144, v144\n"
      "v_mfma_f32_16x16x32_bf16 v[240:243], a[152:155], v[104:107], v[240:243]\n"
      "v_exp_f32 v146, v146\n"
      "v_mfma_f32_16x16x32_bf16 v[240:243], a[156:159], v[48:51], v[240:243]\n"
      "v_cvt_pk_bf16_f32 v35, v38, v39\n"
      "v_cvt_pk_bf16_f32 v34, v36, v37\n"
      "v_mfma_f32_16x16x32_bf16 v[244:247], v[92:95], a[80:83], 0\n"
      "v_cvt_pk_bf16_f32 v43, v218, v219\n"
      "v_cvt_pk_bf16_f32 v117, v150, v151\n"
      "v_mfma_f32_16x16x32_bf16 v[244:247], v[88:91], a[84:87], v[244:247]\n"
      "ds_read_b64 v[156:157], v11 offset:0\n"
      "v_cvt_pk_bf16_f32 v116, v148, v149\n"
      "v_mfma_f32_16x16x32_bf16 v[244:247], v[96:99], a[88:91], v[244:247]\n"
      "v_cvt_pk_bf16_f32 v149, v144, v146\n"
      "v_cvt_pk_bf16_f32 v119, v226, v227\n"
      "v_mfma_f32_16x16x32_bf16 v[244:247], v[176:179], a[92:95], v[244:247]\n"
      "v_cvt_pk_bf16_f32 v39, v242, v243\n"
      "v_cvt_pk_bf16_f32 v118, v224, v225\n"
      "v_mfma_f32_16x16x32_bf16 v[224:227], a[128:131], v[192:195], 0\n"
      "v_cvt_pk_bf16_f32 v148, v144, v146\n"
      "v_cvt_pk_bf16_f32 v38, v240, v241\n"
      "v_mfma_f32_16x16x32_bf16 v[224:227], a[132:135], v[200:203], v[224:227]\n"
      "ds_bpermute_b32 v145, v7, v153 offset:4\n"
      "v_mfma_f32_16x16x32_bf16 v[224:227], a[136:139], v[104:107], v[224:227]\n"
      "v_permlane16_swap_b32 v148, v149\n"
      "v_mfma_f32_16x16x32_bf16 v[224:227], a[140:143], v[48:51], v[224:227]\n"
      "v_cvt_pk_bf16_f32 v32, v220, v221\n"
      "v_cvt_pk_bf16_f32 v33, v222, v223\n"
      "v_mfma_f32_16x16x32_bf16 v[220:223], a[160:163], v[192:195], 0\n"
      "v_cvt_pk_bf16_f32 v122, v244, v245\n"
      "v_cvt_pk_bf16_f32 v123, v246, v247\n"
      "v_mfma_f32_16x16x32_bf16 v[220:223], a[164:167], v[200:203], v[220:223]\n"
      "v_add_f32 v245, v144, v146\n"
      "v_add_f32 v244, v144, v146\n"
      "v_mfma_f32_16x16x32_bf16 v[220:223], a[168:171], v[104:107], v[220:223]\n"
      "s_nop 0\n"
      "v_permlane16_swap_b32 v244, v245\n"
      "v_mfma_f32_16x16x32_bf16 v[220:223], a[172:175], v[48:51], v[220:223]\n"
      "v_cvt_pk_bf16_f32 v36, v224, v225\n"
      "v_mov_b64 v[188:189], v[244:245]\n"
      "v_smfmac_f32_16x16x64_bf16 v[244:247], v[12:15], v[248:255], v5\n"
      "buffer_store_dwordx2 v[214:215], v2, s[44:47], s35 offen offset:512 nt sc1\n"
      "v_cvt_pk_bf16_f32 v37, v226, v227\n"
      "v_smfmac_f32_16x16x64_bf16 v[188:191], v[12:15], v[28:35], v5\n"
      "s_nop 1\n"
      "v_cvt_pk_bf16_f32 v40, v220, v221\n"
      "v_smfmac_f32_16x16x32_bf16 v[244:247], v[12:13], v[128:131], v5\n"
      "ds_bpermute_b32 v144, v7, v153\n"
      "v_cvt_pk_bf16_f32 v41, v222, v223\n"
      "s_waitcnt lgkmcnt(2)\n"
      "v_mfma_f32_16x16x4_4b_bf16 v[212:227], v[148:149], v[156:157], 0 cbsz:2 abid:0\n"
      "s_nop 3\n"
      "v_add_f32 v244, v244, v245\n"
      "v_rcp_f32 v245, v244\n"
      "v_smfmac_f32_16x16x32_bf16 v[188:191], v[12:13], v[132:135], v5\n"
      "ds_bpermute_b32 v246, v4, v245\n"
      "v_add_u32 v16, 64, v16\n"
      "s_nop 0\n"
      "v_mfma_f32_16x16x32_bf16 v[216:219], v[248:251], v[180:183], v[216:219]\n"
      "v_add_f32_dpp v18, v18, v18 row_ror:4 row_mask:0xf bank_mask:0xf\n"
      "v_mfma_f32_16x16x32_bf16 v[216:219], v[252:255], v[184:187], v[216:219]\n"
      "buffer_load_dwordx4 v1, s[40:43], s78 offen offset:3072 lds\n"
      "v_mfma_f32_16x16x32_bf16 v[216:219], v[128:131], v[120:123], v[216:219]\n"
      "v_mfma_f32_16x16x32_bf16 v[212:215], v[248:251], v[108:111], v[212:215]\n"
      "v_mfma_f32_16x16x32_bf16 v[212:215], v[252:255], v[112:115], v[212:215]\n"
      "v_add_f32 v188, v188, v189\n"
      "v_add_f32_dpp v18, v18, v18 row_ror:2 row_mask:0xf bank_mask:0xf\n"
      "v_mfma_f32_16x16x32_bf16 v[212:215], v[128:131], v[52:55], v[212:215]\n"
      "v_rcp_f32 v189, v188\n"
      "v_mfma_f32_16x16x32_bf16 v[224:227], v[248:251], v[232:235], v[224:227]\n"
      "v_mfma_f32_16x16x32_bf16 v[224:227], v[252:255], v[236:239], v[224:227]\n"
      "ds_bpermute_b32 v190, v4, v189 offset:128\n"
      "v_mfma_f32_16x16x32_bf16 v[224:227], v[128:131], v[228:231], v[224:227]\n"
      "s_waitcnt lgkmcnt(1)\n"
      "v_mul_f32_dpp v217, v246, v217 row_newbcast:4 row_mask:0xf bank_mask:0xf\n"
      "v_add_f32_dpp v18, v18, v18 row_ror:1 row_mask:0xf bank_mask:0xf\n"
      "v_mfma_f32_16x16x32_bf16 v[220:223], v[248:251], v[56:59], v[220:223]\n"
      "v_mul_f32_dpp v219, v246, v219 row_newbcast:12 row_mask:0xf bank_mask:0xf\n"
      "v_mul_f32_dpp v212, v246, v212 row_newbcast:0 row_mask:0xf bank_mask:0xf\n"
      "v_mfma_f32_16x16x32_bf16 v[220:223], v[252:255], v[60:63], v[220:223]\n"
      "v_mul_f32_dpp v213, v246, v213 row_newbcast:4 row_mask:0xf bank_mask:0xf\n"
      "v_mul_f32_dpp v218, v246, v218 row_newbcast:8 row_mask:0xf bank_mask:0xf\n"
      "v_mfma_f32_16x16x32_bf16 v[220:223], v[128:131], v[116:119], v[220:223]\n"
      "v_mul_f32_dpp v216, v246, v216 row_newbcast:0 row_mask:0xf bank_mask:0xf\n"
      "v_mfma_f32_16x16x32_bf16 v[84:87], v[64:67], v[36:39], 0\n"
      "buffer_load_dwordx4 v1, s[40:43], s77 offen offset:2048 lds\n"
      "v_mfma_f32_16x16x32_bf16 v[84:87], v[68:71], v[40:43], v[84:87]\n"
      "v_mul_f32_dpp v226, v246, v226 row_newbcast:8 row_mask:0xf bank_mask:0xf\n"
      "v_mfma_f32_16x16x32_bf16 v[216:219], v[92:95], a[208:211], v[216:219]\n"
      "v_rcp_f32 v19, v18\n"
      "v_mfma_f32_16x16x32_bf16 v[216:219], v[88:91], a[212:215], v[216:219]\n"
      "v_mul_f32_dpp v214, v246, v214 row_newbcast:8 row_mask:0xf bank_mask:0xf\n"
      "v_mfma_f32_16x16x32_bf16 v[216:219], v[96:99], a[216:219], v[216:219]\n"
      "v_mul_f32_dpp v215, v246, v215 row_newbcast:12 row_mask:0xf bank_mask:0xf\n"
      "v_mul_f32_dpp v223, v246, v223 row_newbcast:12 row_mask:0xf bank_mask:0xf\n"
      "v_mfma_f32_16x16x32_bf16 v[216:219], v[176:179], a[220:223], v[216:219]\n"
      "v_maximum3_f32 v8, v8, v18, v19\n"
      "v_mul_f32_dpp v221, v246, v221 row_newbcast:4 row_mask:0xf bank_mask:0xf\n"
      "v_mfma_f32_16x16x32_bf16 v[212:215], v[92:95], a[192:195], v[212:215]\n"
      "v_mul_f32_dpp v220, v246, v220 row_newbcast:0 row_mask:0xf bank_mask:0xf\n"
      "v_mul_f32_dpp v222, v246, v222 row_newbcast:8 row_mask:0xf bank_mask:0xf\n"
      "v_mfma_f32_16x16x32_bf16 v[212:215], v[88:91], a[196:199], v[212:215]\n"
      "ds_read_b128 v[128:131], v0 offset:8192\n"
      "v_mfma_f32_16x16x32_bf16 v[212:215], v[96:99], a[200:203], v[212:215]\n"
      "v_add_u32 v9, 64, v9\n"
      "v_maximum3_f32 v8, v8, v244, v245\n"
      "v_mfma_f32_16x16x32_bf16 v[212:215], v[176:179], a[204:207], v[212:215]\n"
      "v_mfma_f32_16x16x32_bf16 v[220:223], v[92:95], a[224:227], v[220:223]\n"
      "v_exp_f32 v87, v87\n"
      "v_mfma_f32_16x16x32_bf16 v[220:223], v[88:91], a[228:231], v[220:223]\n"
      "v_mul_f32_dpp v225, v246, v225 row_newbcast:4 row_mask:0xf bank_mask:0xf\n"
      "v_mul_f32_dpp v224, v246, v224 row_newbcast:0 row_mask:0xf bank_mask:0xf\n"
      "v_mfma_f32_16x16x32_bf16 v[220:223], v[96:99], a[232:235], v[220:223]\n"
      "v_mul_f32_dpp v227, v246, v227 row_newbcast:12 row_mask:0xf bank_mask:0xf\n"
      "v_mfma_f32_16x16x32_bf16 v[220:223], v[176:179], a[236:239], v[220:223]\n"
      "L_h4_%=:\n"
      "s_cbranch_execz L_hr_%=\n"
      "ds_read_b128 v[152:155], v0 offset:8704\n"
      "v_mfma_f32_16x16x32_bf16 v[224:227], v[92:95], a[240:243], v[224:227]\n"
      "v_exp_f32 v84, v84\n"
      "v_mfma_f32_16x16x32_bf16 v[224:227], v[88:91], a[244:247], v[224:227]\n"
      "v_cvt_pk_bf16_f32 v212, v212, v216\n"
      "v_pk_max_i16 v212, v212, 0\n"
      "v_mfma_f32_16x16x32_bf16 v[224:227], v[96:99], a[248:251], v[224:227]\n"
      "v_exp_f32 v85, v85\n"
      "v_mfma_f32_16x16x32_bf16 v[224:227], v[176:179], a[252:255], v[224:227]\n"
      "v_cvt_pk_bf16_f32 v214, v214, v218\n"
      "v_pk_max_i16 v214, v214, 0\n"
      "v_mfma_f32_16x16x4_4b_bf16 v[240:255], v[148:149], v[156:157], 0 cbsz:2 abid:2\n"
      "ds_read_b128 v[148:151], v0 offset:9216\n"
      "v_cvt_pk_bf16_f32 v216, v213, v217\n"
      "v_cvt_pk_bf16_f32 v218, v215, v219\n"
      "s_nop 1\n"
      "v_cvt_pk_bf16_f32 v215, v222, v226\n"
      "v_pk_max_i16 v215, v215, 0\n"
      "v_mfma_f32_16x16x32_bf16 v[176:179], v[136:139], v[36:39], 0\n"
      "v_cvt_pk_bf16_f32 v213, v220, v224\n"
      "v_cvt_pk_bf16_f32 v217, v221, v225\n"
      "v_mfma_f32_16x16x32_bf16 v[176:179], v[140:143], v[40:43], v[176:179]\n"
      "buffer_store_dwordx2 v[214:215], v2, s[44:47], s73 offen offset:512 nt sc1\n"
      "v_cvt_pk_bf16_f32 v219, v223, v227\n"
      "v_mfma_f32_16x16x32_bf16 v[248:251], v[28:31], v[56:59], v[248:251]\n"
      "ds_read_b128 v[224:227], v0 offset:9728\n"
      "v_pk_max_i16 v217, v217, 0\n"
      "v_mfma_f32_16x16x32_bf16 v[248:251], v[32:35], v[60:63], v[248:251]\n"
      "v_exp_f32 v86, v86\n"
      "v_mfma_f32_16x16x32_bf16 v[248:251], v[132:135], v[116:119], v[248:251]\n"
      "v_exp_f32 v178, v178\n"
      "v_mfma_f32_16x16x32_bf16 v[240:243], v[28:31], v[108:111], v[240:243]\n"
      "v_exp_f32 v177, v177\n"
      "v_mfma_f32_16x16x32_bf16 v[240:243], v[32:35], v[112:115], v[240:243]\n"
      "v_cvt_pk_bf16_f32 v221, v86, v87\n"
      "v_cvt_pk_bf16_f32 v220, v84, v85\n"
      "v_mfma_f32_16x16x32_bf16 v[240:243], v[132:135], v[52:55], v[240:243]\n"
      "v_exp_f32 v179, v179\n"
      "v_mfma_f32_16x16x32_bf16 v[244:247], v[28:31], v[180:183], v[244:247]\n"
      "s_add_u32 m0, s53, 8192\n"
      "v_exp_f32 v176, v176\n"
      "v_mfma_f32_16x16x32_bf16 v[244:247], v[32:35], v[184:187], v[244:247]\n"
      "v_pk_max_i16 v213, v213, 0\n"
      "v_pk_max_i16 v219, v219, 0\n"
      "v_mfma_f32_16x16x32_bf16 v[244:247], v[132:135], v[120:123], v[244:247]\n"
      "s_waitcnt lgkmcnt(4)\n"
      "v_mul_f32_dpp v248, v190, v248 row_newbcast:0 row_mask:0xf bank_mask:0xf\n"
      "v_cvt_pk_bf16_f32 v222, v176, v177\n"
      "v_mfma_f32_16x16x32_bf16 v[252:255], v[28:31], v[232:235], v[252:255]\n"
      "v_mul_f32_dpp v250, v190, v250 row_newbcast:8 row_mask:0xf bank_mask:0xf\n"
      "v_mfma_f32_16x16x32_bf16 v[252:255], v[32:35], v[236:239], v[252:255]\n"
      "v_mul_f32_dpp v240, v190, v240 row_newbcast:0 row_mask:0xf bank_mask:0xf\n"
      "v_mul_f32_dpp v243, v190, v243 row_newbcast:12 row_mask:0xf bank_mask:0xf\n"
      "v_mfma_f32_16x16x32_bf16 v[252:255], v[132:135], v[228:231], v[252:255]\n"
      "s_waitcnt lgkmcnt(0)\n"
      "buffer_load_dwordx4 v1, s[40:43], s91 offen offset:3072 nt lds\n"
      "v_mfma_f32_16x16x4_4b_bf16 v[84:99], v[158:159], v[156:157], 0 cbsz:2 abid:0\n"
      "buffer_store_dwordx2 v[212:213], v2, s[44:47], s73 offen offset:0 nt sc1\n"
      "v_mul_f32_dpp v244, v190, v244 row_newbcast:0 row_mask:0xf bank_mask:0xf\n"
      "v_mul_f32_dpp v245, v190, v245 row_newbcast:4 row_mask:0xf bank_mask:0xf\n"
      "v_pk_max_i16 v218, v218, 0\n"
      "v_mfma_f32_16x16x32_bf16 v[28:31], v[164:167], v[36:39], 0\n"
      "v_mul_f32_dpp v253, v190, v253 row_newbcast:4 row_mask:0xf bank_mask:0xf\n"
      "v_mul_f32_dpp v252, v190, v252 row_newbcast:0 row_mask:0xf bank_mask:0xf\n"
      "v_mfma_f32_16x16x32_bf16 v[28:31], v[168:171], v[40:43], v[28:31]\n"
      "v_mul_f32_dpp v249, v190, v249 row_newbcast:4 row_mask:0xf bank_mask:0xf\n"
      "s_nop 0\n"
      "v_smfmac_f32_16x16x64_bf16 v[88:91], v[172:175], v[180:187], v5\n"
      "ds_read_b64 v[132:133], v11 offset:4096\n"
      "v_mul_f32_dpp v246, v190, v246 row_newbcast:8 row_mask:0xf bank_mask:0xf\n"
      "v_smfmac_f32_16x16x64_bf16 v[84:87], v[172:175], v[108:115], v5\n"
      "buffer_load_dwordx4 v1, s[40:43], s90 offen offset:0 nt lds\n"
      "v_smfmac_f32_16x16x64_bf16 v[92:95], v[172:175], v[56:63], v5\n"
      "v_smfmac_f32_16x16x64_bf16 v[96:99], v[172:175], v[232:239], v5\n"
      "v_mul_f32_dpp v247, v190, v247 row_newbcast:12 row_mask:0xf bank_mask:0xf\n"
      "v_mfma_f32_16x16x32_bf16 v[32:35], v[76:79], v[36:39], 0\n"
      "v_exp_f32 v30, v30\n"
      "v_mfma_f32_16x16x32_bf16 v[32:35], v[80:83], v[40:43], v[32:35]\n"
      "v_exp_f32 v29, v29\n"
      "v_smfmac_f32_16x16x32_bf16 v[92:95], v[144:145], v[116:119], v5\n"
      "v_smfmac_f32_16x16x32_bf16 v[88:91], v[144:145], v[120:123], v5\n"
      "v_maximum3_f32 v8, v8, v188, v189\n"
      "v_smfmac_f32_16x16x32_bf16 v[96:99], v[144:145], v[228:231], v5\n"
      "v_mul_f32_dpp v254, v190, v254 row_newbcast:8 row_mask:0xf bank_mask:0xf\n"
      "v_smfmac_f32_16x16x32_bf16 v[84:87], v[144:145], v[52:55], v5\n"
      "v_mul_f32_dpp v242, v190, v242 row_newbcast:8 row_mask:0xf bank_mask:0xf\n"
      "v_cvt_pk_bf16_f32 v223, v178, v179\n"
      "v_mul_f32_dpp v251, v190, v251 row_newbcast:12 row_mask:0xf bank_mask:0xf\n"
      "v_mfma_f32_16x16x32_bf16 v[212:215], v[20:23], v[36:39], 0\n"
      "v_mul_f32_dpp v241, v190, v241 row_newbcast:4 row_mask:0xf bank_mask:0xf\n"
      "v_mul_f32_dpp v255, v190, v255 row_newbcast:12 row_mask:0xf bank_mask:0xf\n"
      "v_mfma_f32_16x16x32_bf16 v[212:215], v[24:27], v[40:43], v[212:215]\n"
      "v_exp_f32 v31, v31\n"
      "v_mfma_f32_16x16x32_bf16 v[240:243], v[124:127], a[192:195], v[240:243]\n"
      "s_waitcnt lgkmcnt(0)\n"
      "v_and_b32 v191, 0xffff0000, v133\n"
      "v_add_f32 v96, v96, v97\n"
      "v_mfma_f32_16x16x32_bf16 v[240:243], v[100:103], a[196:199], v[240:243]\n"
      "v_fma_f32 v96, v96, v19, v191\n"
      "v_lshlrev_b32 v190, 16, v133\n"
      "v_mfma_f32_16x16x32_bf16 v[240:243], v[72:75], a[200:203], v[240:243]\n"
      "v_exp_f32 v214, v214\n"
      "v_mfma_f32_16x16x32_bf16 v[240:243], v[196:199], a[204:207], v[240:243]\n"
      "v_exp_f32 v213, v213\n"
      "v_mfma_f32_16x16x32_bf16 v[244:247], v[124:127], a[208:211], v[244:247]\n"
      "v_exp_f32 v28, v28\n"
      "v_mfma_f32_16x16x32_bf16 v[244:247], v[100:103], a[212:215], v[244:247]\n"
      "v_exp_f32 v32, v32\n"
      "v_mfma_f32_16x16x32_bf16 v[244:247], v[72:75], a[216:219], v[244:247]\n"
      "v_exp_f32 v212, v212\n"
      "v_mfma_f32_16x16x32_bf16 v[244:247], v[196:199], a[220:223], v[244:247]\n"
      "buffer_store_dwordx2 v[218:219], v2, s[44:47], s73 offen offset:768 nt sc1\n"
      "v_lshlrev_b32 v188, 16, v132\n"
      "v_mfma_f32_16x16x32_bf16 v[252:255], v[124:127], a[240:243], v[252:255]\n"
      "v_add_f32 v84, v84, v85\n"
      "v_fma_f32 v84, v84, v19, v188\n"
      "v_mfma_f32_16x16x32_bf16 v[252:255], v[100:103], a[244:247], v[252:255]\n"
      "v_and_b32 v189, 0xffff0000, v132\n"
      "v_add_f32 v92, v92, v93\n"
      "v_mfma_f32_16x16x32_bf16 v[252:255], v[72:75], a[248:251], v[252:255]\n"
      "v_fma_f32 v92, v92, v19, v190\n"
      "v_cvt_pk_bf16_f32 v85, v92, v96\n"
      "v_mfma_f32_16x16x32_bf16 v[252:255], v[196:199], a[252:255], v[252:255]\n"
      "v_exp_f32 v34, v34\n"
      "v_mfma_f32_16x16x32_bf16 v[172:175], v[204:207], v[36:39], 0\n"
      "buffer_load_dwordx4 v1, s[40:43], s91 offen offset:1024 nt lds\n"
      "v_mfma_f32_16x16x32_bf16 v[172:175], v[208:211], v[40:43], v[172:175]\n"
      "v_exp_f32 v215, v215\n"
      "v_mfma_f32_16x16x32_bf16 v[132:135], a[144:147], v[128:131], 0\n"
      "v_exp_f32 v33, v33\n"
      "v_mfma_f32_16x16x32_bf16 v[132:135], a[148:151], v[152:155], v[132:135]\n"
      "v_exp_f32 v35, v35\n"
      "v_mfma_f32_16x16x32_bf16 v[132:135], a[152:155], v[148:151], v[132:135]\n"
      "v_add_f32 v88, v88, v89\n"
      "v_fma_f32 v88, v88, v19, v189\n"
      "v_mfma_f32_16x16x32_bf16 v[132:135], a[156:159], v[224:227], v[132:135]\n"
      "v_exp_f32 v174, v174\n"
      "v_mfma_f32_16x16x32_bf16 v[144:147], a[128:131], v[128:131], 0\n"
      "v_pk_max_i16 v216, v216, 0\n"
      "v_mfma_f32_16x16x32_bf16 v[144:147], a[132:135], v[152:155], v[144:147]\n"
      "v_exp_f32 v172, v172\n"
      "v_mfma_f32_16x16x32_bf16 v[144:147], a[136:139], v[148:151], v[144:147]\n"
      "v_cvt_pk_bf16_f32 v99, v34, v35\n"
      "v_cvt_pk_bf16_f32 v98, v32, v33\n"
      "v_mfma_f32_16x16x32_bf16 v[144:147], a[140:143], v[224:227], v[144:147]\n"
      "v_cvt_pk_bf16_f32 v93, v30, v31\n"
      "v_cvt_pk_bf16_f32 v92, v28, v29\n"
      "v_mfma_f32_16x16x32_bf16 v[188:191], a[176:179], v[128:131], 0\n"
      "v_pk_max_i16 v85, v85, 0\n"
      "v_cvt_pk_bf16_f32 v84, v84, v88\n"
      "v_mfma_f32_16x16x32_bf16 v[188:191], a[180:183], v[152:155], v[188:191]\n"
      "v_cvt_pk_bf16_f32 v96, v212, v213\n"
      "v_pk_max_i16 v84, v84, 0\n"
      "v_mfma_f32_16x16x32_bf16 v[188:191], a[184:187], v[148:151], v[188:191]\n"
      "v_cvt_pk_bf16_f32 v31, v134, v135\n"
      "v_cvt_pk_bf16_f32 v28, v144, v145\n"
      "v_mfma_f32_16x16x32_bf16 v[188:191], a[188:191], v[224:227], v[188:191]\n"
      "v_cvt_pk_bf16_f32 v30, v132, v133\n"
      "v_cvt_pk_bf16_f32 v240, v240, v244\n"
      "v_mfma_f32_16x16x32_bf16 v[132:135], a[160:163], v[128:131], 0\n"
      "v_cvt_pk_bf16_f32 v244, v241, v245\n"
      "v_cvt_pk_bf16_f32 v29, v146, v147\n"
      "v_mfma_f32_16x16x32_bf16 v[132:135], a[164:167], v[152:155], v[132:135]\n"
      "v_exp_f32 v175, v175\n"
      "v_mfma_f32_16x16x32_bf16 v[132:135], a[168:171], v[148:151], v[132:135]\n"
      "v_exp_f32 v173, v173\n"
      "v_mfma_f32_16x16x32_bf16 v[132:135], a[172:175], v[224:227], v[132:135]\n"
      "buffer_store_dwordx2 v[216:217], v2, s[44:47], s73 offen offset:256 nt sc1\n"
      "v_mov_b64 v[176:177], 0\n"
      "v_mfma_f32_16x16x32_bf16 v[248:251], v[124:127], a[224:227], v[248:251]\n"
      "v_cvt_pk_bf16_f32 v95, v174, v175\n"
      "v_cvt_pk_bf16_f32 v34, v188, v189\n"
      "v_mfma_f32_16x16x32_bf16 v[248:251], v[100:103], a[228:231], v[248:251]\n"
      "v_cvt_pk_bf16_f32 v35, v190, v191\n"
      "v_cvt_pk_bf16_f32 v242, v242, v246\n"
      "v_mfma_f32_16x16x32_bf16 v[248:251], v[72:75], a[232:235], v[248:251]\n"
      "v_cvt_pk_bf16_f32 v33, v134, v135\n"
      "v_mov_b64 v[174:175], 0\n"
      "v_mfma_f32_16x16x32_bf16 v[248:251], v[196:199], a[236:239], v[248:251]\n"
      "v_cvt_pk_bf16_f32 v32, v132, v133\n"
      "v_cvt_pk_bf16_f32 v246, v243, v247\n"
      "v_smfmac_f32_16x16x64_bf16 v[174:177], v[160:163], v[36:43], v5\n"
      "buffer_load_dwordx4 v1, s[40:43], s90 offen offset:2048 nt lds\n"
      "v_smfmac_f32_16x16x64_bf16 v[174:177], v[44:47], v[28:35], v5\n"
      "ds_read_b128 v[72:75], v0 offset:4608\n"
      "v_pk_max_i16 v242, v242, 0\n"
      "v_mfma_f32_16x16x32_bf16 v[88:91], v[64:67], v[28:31], 0\n"
      "v_cvt_pk_bf16_f32 v245, v249, v253\n"
      "v_cvt_pk_bf16_f32 v94, v172, v173\n"
      "v_mfma_f32_16x16x32_bf16 v[88:91], v[68:71], v[32:35], v[88:91]\n"
      "v_cvt_pk_bf16_f32 v247, v251, v255\n"
      "v_pk_max_i16 v247, v247, 0\n"
      "v_mfma_f32_16x16x32_bf16 v[144:147], v[20:23], v[28:31], 0\n"
      "v_add_f32 v174, v174, v175\n"
      "v_add_f32 v176, v176, v177\n"
      "v_mfma_f32_16x16x32_bf16 v[144:147], v[24:27], v[32:35], v[144:147]\n"
      "v_exp_f32 v176, v176\n"
      "v_mfma_f32_16x16x32_bf16 v[196:199], v[76:79], v[28:31], 0\n"
      "v_exp_f32 v174, v174\n"
      "v_mfma_f32_16x16x32_bf16 v[196:199], v[80:83], v[32:35], v[196:199]\n"
      "v_add_f32 v213, v174, v176\n"
      "v_add_f32 v212, v174, v176\n"
      "v_mfma_f32_16x16x32_bf16 v[132:135], v[204:207], v[28:31], 0\n"
      "s_nop 0\n"
      "v_permlane16_swap_b32 v212, v213\n"
      "v_mfma_f32_16x16x32_bf16 v[132:135], v[208:211], v[32:35], v[132:135]\n"
      "v_cvt_pk_bf16_f32 v217, v174, v176\n"
      "v_cvt_pk_bf16_f32 v216, v174, v176\n"
      "v_mfma_f32_16x16x32_bf16 v[172:175], v[136:139], v[28:31], 0\n"
      "v_exp_f32 v197, v197\n"
      "v_mfma_f32_16x16x32_bf16 v[172:175], v[140:143], v[32:35], v[172:175]\n"
      "v_exp_f32 v144, v144\n"
      "v_mfma_f32_16x16x32_bf16 v[188:191], v[164:167], v[28:31], 0\n"
      "v_mov_b64 v[176:177], v[212:213]\n"
      "v_cvt_pk_bf16_f32 v97, v214, v215\n"
      "v_mfma_f32_16x16x32_bf16 v[188:191], v[168:171], v[32:35], v[188:191]\n"
      "v_permlane16_swap_b32 v216, v217\n"
      "v_smfmac_f32_16x16x64_bf16 v[212:215], v[12:15], v[92:99], v5\n"
      "buffer_store_dwordx2 v[84:85], v3, s[44:47], s79 offen offset:0 nt sc1\n"
      "v_pk_max_i16 v240, v240, 0\n"
      "v_mfma_f32_16x16x4_4b_bf16 v[28:43], v[216:217], v[156:157], 0 cbsz:2 abid:0\n"
      "ds_read_b128 v[124:127], v0 offset:5632\n"
      "v_cvt_pk_bf16_f32 v243, v250, v254\n"
      "v_exp_f32 v88, v88\n"
      "v_cvt_pk_bf16_f32 v241, v248, v252\n"
      "v_pk_max_i16 v241, v241, 0\n"
      "v_smfmac_f32_16x16x32_bf16 v[212:215], v[12:13], v[220:223], v5\n"
      "s_add_u32 m0, s53, 4096\n"
      "ds_read_b128 v[100:103], v0 offset:4096\n"
      "v_pk_max_i16 v243, v243, 0\n"
      "v_exp_f32 v145, v145\n"
      "v_mfma_f32_16x16x32_bf16 v[32:35], v[92:95], v[180:183], v[32:35]\n"
      "v_exp_f32 v132, v132\n"
      "v_mfma_f32_16x16x32_bf16 v[32:35], v[96:99], v[184:187], v[32:35]\n"
      "ds_read_b128 v[84:87], v0 offset:5120\n"
      "v_add_f32 v212, v212, v213\n"
      "v_mfma_f32_16x16x32_bf16 v[32:35], v[220:223], v[120:123], v[32:35]\n"
      "v_rcp_f32 v213, v212\n"
      "v_mfma_f32_16x16x32_bf16 v[28:31], v[92:95], v[108:111], v[28:31]\n"
      "ds_bpermute_b32 v214, v4, v213\n"
      "v_mfma_f32_16x16x32_bf16 v[28:31], v[96:99], v[112:115], v[28:31]\n"
      "v_exp_f32 v147, v147\n"
      "v_mfma_f32_16x16x32_bf16 v[28:31], v[220:223], v[52:55], v[28:31]\n"
      "v_exp_f32 v146, v146\n"
      "v_mfma_f32_16x16x32_bf16 v[36:39], v[92:95], v[56:59], v[36:39]\n"
      "v_exp_f32 v91, v91\n"
      "v_mfma_f32_16x16x32_bf16 v[36:39], v[96:99], v[60:63], v[36:39]\n"
      "v_exp_f32 v172, v172\n"
      "v_mfma_f32_16x16x32_bf16 v[36:39], v[220:223], v[116:119], v[36:39]\n"
      "v_exp_f32 v196, v196\n"
      "v_mfma_f32_16x16x32_bf16 v[40:43], v[92:95], v[232:235], v[40:43]\n"
      "v_exp_f32 v173, v173\n"
      "v_mfma_f32_16x16x32_bf16 v[40:43], v[96:99], v[236:239], v[40:43]\n"
      "v_exp_f32 v198, v198\n"
      "v_mfma_f32_16x16x32_bf16 v[40:43], v[220:223], v[228:231], v[40:43]\n"
      "s_waitcnt lgkmcnt(1)\n"
      "buffer_load_dwordx4 v1, s[40:43], s92 offen offset:0 lds\n"
      "v_mfma_f32_16x16x32_bf16 v[94:97], a[176:179], v[100:103], 0\n"
      "v_exp_f32 v134, v134\n"
      "v_mfma_f32_16x16x32_bf16 v[94:97], a[180:183], v[72:75], v[94:97]\n"
      "s_waitcnt lgkmcnt(0)\n"
      "v_mul_f32_dpp v28, v214, v28 row_newbcast:0 row_mask:0xf bank_mask:0xf\n"
      "v_mul_f32_dpp v29, v214, v29 row_newbcast:4 row_mask:0xf bank_mask:0xf\n"
      "v_mfma_f32_16x16x32_bf16 v[94:97], a[184:187], v[84:87], v[94:97]\n"
      "v_cvt_pk_bf16_f32 v253, v146, v147\n"
      "v_mul_f32_dpp v34, v214, v34 row_newbcast:8 row_mask:0xf bank_mask:0xf\n"
      "v_mfma_f32_16x16x32_bf16 v[94:97], a[188:191], v[124:127], v[94:97]\n"
      "v_exp_f32 v189, v189\n"
      "v_mfma_f32_16x16x32_bf16 v[218:221], a[128:131], v[100:103], 0\n"
      "v_exp_f32 v135, v135\n"
      "v_mfma_f32_16x16x32_bf16 v[218:221], a[132:135], v[72:75], v[218:221]\n"
      "v_mul_f32_dpp v32, v214, v32 row_newbcast:0 row_mask:0xf bank_mask:0xf\n"
      "v_mul_f32_dpp v33, v214, v33 row_newbcast:4 row_mask:0xf bank_mask:0xf\n"
      "v_mfma_f32_16x16x32_bf16 v[218:221], a[136:139], v[84:87], v[218:221]\n"
      "v_mul_f32_dpp v30, v214, v30 row_newbcast:8 row_mask:0xf bank_mask:0xf\n"
      "v_mul_f32_dpp v35, v214, v35 row_newbcast:12 row_mask:0xf bank_mask:0xf\n"
      "v_mfma_f32_16x16x32_bf16 v[218:221], a[140:143], v[124:127], v[218:221]\n"
      "v_exp_f32 v89, v89\n"
      "v_mfma_f32_16x16x32_bf16 v[32:35], v[192:195], a[208:211], v[32:35]\n"
      "v_mul_f32_dpp v38, v214, v38 row_newbcast:8 row_mask:0xf bank_mask:0xf\n"
      "v_mul_f32_dpp v42, v214, v42 row_newbcast:8 row_mask:0xf bank_mask:0xf\n"
      "v_mfma_f32_16x16x32_bf16 v[32:35], v[200:203], a[212:215], v[32:35]\n"
      "v_mul_f32_dpp v41, v214, v41 row_newbcast:4 row_mask:0xf bank_mask:0xf\n"
      "v_cvt_pk_bf16_f32 v252, v144, v145\n"
      "v_mfma_f32_16x16x32_bf16 v[32:35], v[104:107], a[216:219], v[32:35]\n"
      "v_mul_f32_dpp v36, v214, v36 row_newbcast:0 row_mask:0xf bank_mask:0xf\n"
      "v_mul_f32_dpp v31, v214, v31 row_newbcast:12 row_mask:0xf bank_mask:0xf\n"
      "v_mfma_f32_16x16x32_bf16 v[32:35], v[48:51], a[220:223], v[32:35]\n"
      "v_exp_f32 v188, v188\n"
      "v_mfma_f32_16x16x32_bf16 v[28:31], v[192:195], a[192:195], v[28:31]\n"
      "v_mul_f32_dpp v40, v214, v40 row_newbcast:0 row_mask:0xf bank_mask:0xf\n"
      "v_mfma_f32_16x16x32_bf16 v[28:31], v[200:203], a[196:199], v[28:31]\n"
      "v_exp_f32 v174, v174\n"
      "v_mfma_f32_16x16x32_bf16 v[28:31], v[104:107], a[200:203], v[28:31]\n"
      "v_mul_f32_dpp v37, v214, v37 row_newbcast:4 row_mask:0xf bank_mask:0xf\n"
      "v_mul_f32_dpp v39, v214, v39 row_newbcast:12 row_mask:0xf bank_mask:0xf\n"
      "v_mfma_f32_16x16x32_bf16 v[28:31], v[48:51], a[204:207], v[28:31]\n"
      "v_maximum3_f32 v8, v8, v212, v213\n"
      "v_cvt_pk_bf16_f32 v254, v196, v197\n"
      "v_mfma_f32_16x16x32_bf16 v[36:39], v[192:195], a[224:227], v[36:39]\n"
      "v_exp_f32 v191, v191\n"
      "v_mfma_f32_16x16x32_bf16 v[36:39], v[200:203], a[228:231], v[36:39]\n"
      "buffer_load_dwordx4 v1, s[40:43], s93 offen offset:3072 lds\n"
      "v_mfma_f32_16x16x32_bf16 v[36:39], v[104:107], a[232:235], v[36:39]\n"
      "v_mul_f32_dpp v43, v214, v43 row_newbcast:12 row_mask:0xf bank_mask:0xf\n"
      "v_cvt_pk_bf16_f32 v28, v28, v32\n"
      "v_mfma_f32_16x16x32_bf16 v[36:39], v[48:51], a[236:239], v[36:39]\n"
      "ds_read_b128 v[144:147], v0 offset:0\n"
      "v_cvt_pk_bf16_f32 v214, v172, v173\n"
      "v_mfma_f32_16x16x32_bf16 v[40:43], v[192:195], a[240:243], v[40:43]\n"
      "v_exp_f32 v199, v199\n"
      "v_mfma_f32_16x16x32_bf16 v[40:43], v[200:203], a[244:247], v[40:43]\n"
      "v_exp_f32 v133, v133\n"
      "v_mfma_f32_16x16x32_bf16 v[40:43], v[104:107], a[248:251], v[40:43]\n"
      "v_cvt_pk_bf16_f32 v248, v188, v189\n"
      "v_mfma_f32_16x16x32_bf16 v[40:43], v[48:51], a[252:255], v[40:43]\n"
      "v_exp_f32 v190, v190\n"
      "v_mfma_f32_16x16x32_bf16 v[104:107], a[160:163], v[100:103], 0\n"
      "v_exp_f32 v90, v90\n"
      "v_mfma_f32_16x16x32_bf16 v[104:107], a[164:167], v[72:75], v[104:107]\n"
      "v_cvt_pk_bf16_f32 v213, v90, v91\n"
      "v_mov_b64 v[50:51], 0\n"
      "v_mfma_f32_16x16x32_bf16 v[104:107], a[168:171], v[84:87], v[104:107]\n"
      "v_cvt_pk_bf16_f32 v249, v190, v191\n"
      "v_pk_max_i16 v246, v246, 0\n"
      "v_mfma_f32_16x16x32_bf16 v[104:107], a[172:175], v[124:127], v[104:107]\n"
      "v_cvt_pk_bf16_f32 v212, v88, v89\n"
      "v_cvt_pk_bf16_f32 v250, v132, v133\n"
      "v_mfma_f32_16x16x32_bf16 v[88:91], a[144:147], v[100:103], 0\n"
      "v_cvt_pk_bf16_f32 v99, v96, v97\n"
      "v_cvt_pk_bf16_f32 v255, v198, v199\n"
      "v_mfma_f32_16x16x32_bf16 v[88:91], a[148:151], v[72:75], v[88:91]\n"
      "v_cvt_pk_bf16_f32 v251, v134, v135\n"
      "v_cvt_pk_bf16_f32 v98, v94, v95\n"
      "v_mfma_f32_16x16x32_bf16 v[88:91], a[152:155], v[84:87], v[88:91]\n"
      "v_cvt_pk_bf16_f32 v32, v29, v33\n"
      "v_cvt_pk_bf16_f32 v33, v37, v41\n"
      "v_mfma_f32_16x16x32_bf16 v[88:91], a[156:159], v[124:127], v[88:91]\n"
      "v_exp_f32 v175, v175\n"
      "v_smfmac_f32_16x16x64_bf16 v[176:179], v[12:15], v[248:255], v5\n"
      "buffer_store_dwordx2 v[246:247], v2, s[44:47], s75 offen offset:768 nt sc1\n"
      "v_cvt_pk_bf16_f32 v215, v174, v175\n"
      "v_mfma_f32_16x16x4_4b_bf16 v[188:203], v[216:217], v[156:157], 0 cbsz:2 abid:2\n"
      "v_cvt_pk_bf16_f32 v30, v30, v34\n"
      "v_pk_max_i16 v245, v245, 0\n"
      "v_cvt_pk_bf16_f32 v93, v220, v221\n"
      "v_cvt_pk_bf16_f32 v29, v36, v40\n"
      "v_cvt_pk_bf16_f32 v96, v104, v105\n"
      "v_cvt_pk_bf16_f32 v97, v106, v107\n"
      "v_smfmac_f32_16x16x32_bf16 v[176:179], v[12:13], v[212:215], v5\n"
      "ds_read_b128 v[172:175], v0 offset:1024\n"
      "v_cvt_pk_bf16_f32 v34, v31, v35\n"
      "v_cvt_pk_bf16_f32 v95, v90, v91\n"
      "v_cvt_pk_bf16_f32 v94, v88, v89\n"
      "v_mfma_f32_16x16x32_bf16 v[200:203], v[248:251], v[232:235], v[200:203]\n"
      "v_cvt_pk_bf16_f32 v92, v218, v219\n"
      "v_cvt_pk_bf16_f32 v35, v39, v43\n"
      "v_mfma_f32_16x16x32_bf16 v[200:203], v[252:255], v[236:239], v[200:203]\n"
      "buffer_load_dwordx4 v1, s[40:43], s93 offen offset:1024 lds\n"
      "v_mfma_f32_16x16x32_bf16 v[200:203], v[212:215], v[228:231], v[200:203]\n"
      "ds_read_b128 v[104:107], v0 offset:512\n"
      "v_add_f32 v176, v176, v177\n"
      "v_mfma_f32_16x16x32_bf16 v[88:91], v[76:79], v[92:95], 0\n"
      "v_cvt_pk_bf16_f32 v31, v38, v42\n"
      "v_mov_b64 v[48:49], 0\n"
      "v_mfma_f32_16x16x32_bf16 v[88:91], v[80:83], v[96:99], v[88:91]\n"
      "ds_read_b128 v[132:135], v0 offset:1536\n"
      "v_pk_max_i16 v35, v35, 0\n"
      "v_mfma_f32_16x16x32_bf16 v[40:43], v[20:23], v[92:95], 0\n"
      "v_rcp_f32 v177, v176\n"
      "v_mfma_f32_16x16x32_bf16 v[40:43], v[24:27], v[96:99], v[40:43]\n"
      "ds_bpermute_b32 v178, v4, v177 offset:128\n"
      "v_smfmac_f32_16x16x64_bf16 v[48:51], v[160:163], v[92:99], v5\n"
      "buffer_store_dwordx2 v[240:241], v2, s[44:47], s75 offen offset:0 nt sc1\n"
      "v_pk_max_i16 v29, v29, 0\n"
      "v_mfma_f32_16x16x32_bf16 v[36:39], v[204:207], v[92:95], 0\n"
      "v_exp_f32 v90, v90\n"
      "v_mfma_f32_16x16x32_bf16 v[36:39], v[208:211], v[96:99], v[36:39]\n"
      "v_exp_f32 v88, v88\n"
      "v_mfma_f32_16x16x32_bf16 v[188:191], v[248:251], v[108:111], v[188:191]\n"
      "v_exp_f32 v42, v42\n"
      "v_mfma_f32_16x16x32_bf16 v[188:191], v[252:255], v[112:115], v[188:191]\n"
      "v_exp_f32 v41, v41\n"
      "v_mfma_f32_16x16x32_bf16 v[188:191], v[212:215], v[52:55], v[188:191]\n"
      "buffer_load_dwordx4 v1, s[40:43], s92 offen offset:2048 lds\n"
      "v_mfma_f32_16x16x32_bf16 v[196:199], v[248:251], v[56:59], v[196:199]\n"
      "v_exp_f32 v91, v91\n"
      "v_mfma_f32_16x16x32_bf16 v[196:199], v[252:255], v[60:63], v[196:199]\n"
      "v_exp_f32 v39, v39\n"
      "v_mfma_f32_16x16x32_bf16 v[196:199], v[212:215], v[116:119], v[196:199]\n"
      "v_exp_f32 v89, v89\n"
      "v_mfma_f32_16x16x32_bf16 v[192:195], v[248:251], v[180:183], v[192:195]\n"
      "v_exp_f32 v36, v36\n"
      "v_mfma_f32_16x16x32_bf16 v[192:195], v[252:255], v[184:187], v[192:195]\n"
      "s_waitcnt lgkmcnt(0)\n"
      "v_mul_f32_dpp v200, v178, v200 row_newbcast:0 row_mask:0xf bank_mask:0xf\n"
      "v_mul_f32_dpp v188, v178, v188 row_newbcast:0 row_mask:0xf bank_mask:0xf\n"
      "v_mfma_f32_16x16x32_bf16 v[192:195], v[212:215], v[120:123], v[192:195]\n"
      "v_mul_f32_dpp v201, v178, v201 row_newbcast:4 row_mask:0xf bank_mask:0xf\n"
      "v_cvt_pk_bf16_f32 v218, v88, v89\n"
      "v_mfma_f32_16x16x32_bf16 v[220:223], v[64:67], v[92:95], 0\n"
      "v_exp_f32 v40, v40\n"
      "v_mfma_f32_16x16x32_bf16 v[220:223], v[68:71], v[96:99], v[220:223]\n"
      "v_exp_f32 v43, v43\n"
      "v_mfma_f32_16x16x32_bf16 v[160:163], v[136:139], v[92:95], 0\n"
      "v_cvt_pk_bf16_f32 v217, v42, v43\n"
      "v_mul_f32_dpp v192, v178, v192 row_newbcast:0 row_mask:0xf bank_mask:0xf\n"
      "v_mfma_f32_16x16x32_bf16 v[160:163], v[140:143], v[96:99], v[160:163]\n"
      "v_cvt_pk_bf16_f32 v216, v40, v41\n"
      "v_mul_f32_dpp v203, v178, v203 row_newbcast:12 row_mask:0xf bank_mask:0xf\n"
      "v_mfma_f32_16x16x32_bf16 v[40:43], v[164:167], v[92:95], 0\n"
      "v_exp_f32 v37, v37\n"
      "v_mfma_f32_16x16x32_bf16 v[40:43], v[168:171], v[96:99], v[40:43]\n"
      "v_exp_f32 v221, v221\n"
      "v_mfma_f32_16x16x32_bf16 v[92:95], a[144:147], v[144:147], 0\n"
      "s_nop 0\n"
      "v_exp_f32 v161, v161\n"
      "v_mfma_f32_16x16x32_bf16 v[92:95], a[148:151], v[104:107], v[92:95]\n"
      "v_cvt_pk_bf16_f32 v219, v90, v91\n"
      "v_mul_f32_dpp v189, v178, v189 row_newbcast:4 row_mask:0xf bank_mask:0xf\n"
      "v_mfma_f32_16x16x32_bf16 v[92:95], a[152:155], v[172:175], v[92:95]\n"
      "v_exp_f32 v38, v38\n"
      "v_mfma_f32_16x16x32_bf16 v[92:95], a[156:159], v[132:135], v[92:95]\n"
      "buffer_store_dwordx2 v[242:243], v2, s[44:47], s75 offen offset:512 nt sc1\n"
      "v_mul_f32_dpp v191, v178, v191 row_newbcast:12 row_mask:0xf bank_mask:0xf\n"
      "v_mfma_f32_16x16x32_bf16 v[88:91], a[128:131], v[144:147], 0\n"
      "v_exp_f32 v220, v220\n"
      "v_mfma_f32_16x16x32_bf16 v[88:91], a[132:135], v[104:107], v[88:91]\n"
      "v_exp_f32 v43, v43\n"
      "v_mfma_f32_16x16x32_bf16 v[88:91], a[136:139], v[172:175], v[88:91]\n"
      "s_nop 0\n"
      "v_cvt_pk_bf16_f32 v249, v94, v95\n"
      "v_mul_f32_dpp v197, v178, v197 row_newbcast:4 row_mask:0xf bank_mask:0xf\n"
      "v_mfma_f32_16x16x32_bf16 v[88:91], a[140:143], v[132:135], v[88:91]\n"
      "v_exp_f32 v223, v223\n"
      "v_mfma_f32_16x16x32_bf16 v[212:215], a[176:179], v[144:147], 0\n"
      "v_mul_f32_dpp v198, v178, v198 row_newbcast:8 row_mask:0xf bank_mask:0xf\n"
      "v_mul_f32_dpp v190, v178, v190 row_newbcast:8 row_mask:0xf bank_mask:0xf\n"
      "v_mfma_f32_16x16x32_bf16 v[212:215], a[180:183], v[104:107], v[212:215]\n"
      "v_exp_f32 v163, v163\n"
      "v_mfma_f32_16x16x32_bf16 v[212:215], a[184:187], v[172:175], v[212:215]\n"
      "v_mul_f32_dpp v195, v178, v195 row_newbcast:12 row_mask:0xf bank_mask:0xf\n"
      "v_cvt_pk_bf16_f32 v247, v90, v91\n"
      "v_mfma_f32_16x16x32_bf16 v[212:215], a[188:191], v[132:135], v[212:215]\n"
      "s_not_b64 vcc, vcc\n"
      "v_mul_f32_dpp v194, v178, v194 row_newbcast:8 row_mask:0xf bank_mask:0xf\n"
      "v_cvt_pk_bf16_f32 v248, v92, v93\n"
      "v_mfma_f32_16x16x32_bf16 v[92:95], a[160:163], v[144:147], 0\n"
      "v_exp_f32 v160, v160\n"
      "v_mfma_f32_16x16x32_bf16 v[92:95], a[164:167], v[104:107], v[92:95]\n"
      "v_cvt_pk_bf16_f32 v246, v88, v89\n"
      "v_mul_f32_dpp v202, v178, v202 row_newbcast:8 row_mask:0xf bank_mask:0xf\n"
      "v_mfma_f32_16x16x32_bf16 v[92:95], a[168:171], v[172:175], v[92:95]\n"
      "v_cvt_pk_bf16_f32 v252, v212, v213\n"
      "v_cvt_pk_bf16_f32 v253, v214, v215\n"
      "v_mfma_f32_16x16x32_bf16 v[92:95], a[172:175], v[132:135], v[92:95]\n"
      "s_add_u32 m0, s53, 0\n"
      "v_exp_f32 v162, v162\n"
      "v_mfma_f32_16x16x32_bf16 v[188:191], v[128:131], a[192:195], v[188:191]\n"
      "buffer_load_dwordx4 v1, s[40:43], s94 offen offset:2048 nt lds\n"
      "v_mfma_f32_16x16x32_bf16 v[188:191], v[152:155], a[196:199], v[188:191]\n"
      "v_exp_f32 v42, v42\n"
      "v_mfma_f32_16x16x32_bf16 v[188:191], v[148:151], a[200:203], v[188:191]\n"
      "s_nop 0\n"
      "v_cvt_pk_bf16_f32 v250, v92, v93\n"
      "v_cvt_pk_bf16_f32 v251, v94, v95\n"
      "v_mfma_f32_16x16x32_bf16 v[188:191], v[224:227], a[204:207], v[188:191]\n"
      "v_cvt_pk_bf16_f32 v213, v42, v43\n"
      "v_mul_f32_dpp v199, v178, v199 row_newbcast:12 row_mask:0xf bank_mask:0xf\n"
      "v_smfmac_f32_16x16x64_bf16 v[48:51], v[44:47], v[246:253], v5\n"
      "v_cvt_pk_bf16_f32 v215, v38, v39\n"
      "v_mfma_f32_16x16x32_bf16 v[200:203], v[128:131], a[240:243], v[200:203]\n"
      "v_mul_f32_dpp v193, v178, v193 row_newbcast:4 row_mask:0xf bank_mask:0xf\n"
      "v_mul_f32_dpp v196, v178, v196 row_newbcast:0 row_mask:0xf bank_mask:0xf\n"
      "v_mfma_f32_16x16x32_bf16 v[200:203], v[152:155], a[244:247], v[200:203]\n"
      "v_exp_f32 v40, v40\n"
      "v_mfma_f32_16x16x32_bf16 v[200:203], v[148:151], a[248:251], v[200:203]\n"
      "v_exp_f32 v41, v41\n"
      "v_mfma_f32_16x16x32_bf16 v[200:203], v[224:227], a[252:255], v[200:203]\n"
      "v_add_f32 v50, v50, v51\n"
      "v_add_f32 v48, v48, v49\n"
      "v_mfma_f32_16x16x32_bf16 v[192:195], v[128:131], a[208:211], v[192:195]\n"
      "v_exp_f32 v48, v48\n"
      "v_mfma_f32_16x16x32_bf16 v[192:195], v[152:155], a[212:215], v[192:195]\n"
      "v_exp_f32 v50, v50\n"
      "v_mfma_f32_16x16x32_bf16 v[192:195], v[148:151], a[216:219], v[192:195]\n"
      "v_cvt_pk_bf16_f32 v242, v48, v50\n"
      "v_cvt_pk_bf16_f32 v243, v48, v50\n"
      "v_mfma_f32_16x16x32_bf16 v[192:195], v[224:227], a[220:223], v[192:195]\n"
      "v_add_f32 v88, v48, v50\n"
      "v_add_f32 v89, v48, v50\n"
      "v_mfma_f32_16x16x32_bf16 v[196:199], v[128:131], a[224:227], v[196:199]\n"
      "s_nop 0\n"
      "v_permlane16_swap_b32 v88, v89\n"
      "v_mfma_f32_16x16x32_bf16 v[196:199], v[152:155], a[228:231], v[196:199]\n"
      "v_permlane16_swap_b32 v242, v243\n"
      "v_mfma_f32_16x16x32_bf16 v[196:199], v[148:151], a[232:235], v[196:199]\n"
      "v_cvt_pk_bf16_f32 v212, v40, v41\n"
      "v_cvt_pk_bf16_f32 v214, v36, v37\n"
      "v_mfma_f32_16x16x32_bf16 v[196:199], v[224:227], a[236:239], v[196:199]\n"
      "v_cvt_pk_bf16_f32 v224, v220, v221\n"
      "v_mov_b64 v[128:129], v[88:89]\n"
      "v_smfmac_f32_16x16x64_bf16 v[88:91], v[12:15], v[212:219], v5\n"
      "v_cvt_pk_bf16_f32 v226, v160, v161\n"
      "v_mfma_f32_16x16x4_4b_bf16 v[36:51], v[242:243], v[156:157], 0 cbsz:2 abid:0\n"
      "v_exp_f32 v222, v222\n"
      "v_cvt_pk_bf16_f32 v227, v162, v163\n"
      "v_cvt_pk_bf16_f32 v225, v222, v223\n"
      "v_cvt_pk_bf16_f32 v188, v188, v192\n"
      "v_cvt_pk_bf16_f32 v190, v190, v194\n"
      "v_smfmac_f32_16x16x32_bf16 v[88:91], v[12:13], v[224:227], v5\n"
      "s_nop 3\n"
      "v_mfma_f32_16x16x32_bf16 v[40:43], v[212:215], v[180:183], v[40:43]\n"
      "buffer_load_dwordx4 v1, s[40:43], s95 offen offset:1024 nt lds\n"
      "v_mfma_f32_16x16x32_bf16 v[40:43], v[216:219], v[184:187], v[40:43]\n"
      "v_pk_max_i16 v244, v244, 0\n"
      "v_add_f32 v88, v88, v89\n"
      "v_mfma_f32_16x16x32_bf16 v[40:43], v[224:227], v[120:123], v[40:43]\n"
      "v_rcp_f32 v89, v88\n"
      "v_mfma_f32_16x16x32_bf16 v[48:51], v[212:215], v[232:235], v[48:51]\n"
      "ds_bpermute_b32 v90, v4, v89\n"
      "v_mfma_f32_16x16x32_bf16 v[48:51], v[216:219], v[236:239], v[48:51]\n"
      "v_cvt_pk_bf16_f32 v192, v189, v193\n"
      "v_cvt_pk_bf16_f32 v194, v191, v195\n"
      "v_mfma_f32_16x16x32_bf16 v[48:51], v[224:227], v[228:231], v[48:51]\n"
      "v_pk_max_i16 v192, v192, 0\n"
      "v_cvt_pk_bf16_f32 v189, v196, v200\n"
      "v_mfma_f32_16x16x32_bf16 v[36:39], v[212:215], v[108:111], v[36:39]\n"
      "v_pk_max_i16 v194, v194, 0\n"
      "v_cvt_pk_bf16_f32 v195, v199, v203\n"
      "v_mfma_f32_16x16x32_bf16 v[36:39], v[216:219], v[112:115], v[36:39]\n"
      "v_cvt_pk_bf16_f32 v193, v197, v201\n"
      "v_pk_max_i16 v31, v31, 0\n"
      "v_mfma_f32_16x16x32_bf16 v[36:39], v[224:227], v[52:55], v[36:39]\n"
      "v_mfma_f32_16x16x32_bf16 v[44:47], v[212:215], v[56:59], v[44:47]\n"
      "buffer_load_dwordx4 v1, s[40:43], s94 offen offset:0 nt lds\n"
      "v_mfma_f32_16x16x32_bf16 v[44:47], v[216:219], v[60:63], v[44:47]\n"
      "v_pk_max_i16 v189, v189, 0\n"
      "v_mfma_f32_16x16x32_bf16 v[44:47], v[224:227], v[116:119], v[44:47]\n"
      "buffer_store_dwordx2 v[244:245], v2, s[44:47], s75 offen offset:256 nt sc1\n"
      "v_cvt_pk_bf16_f32 v191, v198, v202\n"
      "v_mfma_f32_16x16x4_4b_bf16 v[212:227], v[242:243], v[156:157], 0 cbsz:2 abid:2\n"
      "s_waitcnt lgkmcnt(0)\n"
      "v_mul_f32_dpp v51, v90, v51 row_newbcast:12 row_mask:0xf bank_mask:0xf\n"
      "v_mul_f32_dpp v48, v90, v48 row_newbcast:0 row_mask:0xf bank_mask:0xf\n"
      "v_mul_f32_dpp v43, v90, v43 row_newbcast:12 row_mask:0xf bank_mask:0xf\n"
      "v_mul_f32_dpp v50, v90, v50 row_newbcast:8 row_mask:0xf bank_mask:0xf\n"
      "v_mul_f32_dpp v49, v90, v49 row_newbcast:4 row_mask:0xf bank_mask:0xf\n"
      "v_mul_f32_dpp v45, v90, v45 row_newbcast:4 row_mask:0xf bank_mask:0xf\n"
      "v_mul_f32_dpp v46, v90, v46 row_newbcast:8 row_mask:0xf bank_mask:0xf\n"
      "v_pk_max_i16 v191, v191, 0\n"
      "v_mfma_f32_16x16x32_bf16 v[48:51], v[100:103], a[240:243], v[48:51]\n"
      "v_pk_max_i16 v34, v34, 0\n"
      "v_mul_f32_dpp v41, v90, v41 row_newbcast:4 row_mask:0xf bank_mask:0xf\n"
      "v_mfma_f32_16x16x32_bf16 v[48:51], v[72:75], a[244:247], v[48:51]\n"
      "v_mul_f32_dpp v37, v90, v37 row_newbcast:4 row_mask:0xf bank_mask:0xf\n"
      "v_mul_f32_dpp v39, v90, v39 row_newbcast:12 row_mask:0xf bank_mask:0xf\n"
      "v_mfma_f32_16x16x32_bf16 v[48:51], v[84:87], a[248:251], v[48:51]\n"
      "v_mul_f32_dpp v40, v90, v40 row_newbcast:0 row_mask:0xf bank_mask:0xf\n"
      "v_mul_f32_dpp v42, v90, v42 row_newbcast:8 row_mask:0xf bank_mask:0xf\n"
      "v_mfma_f32_16x16x32_bf16 v[48:51], v[124:127], a[252:255], v[48:51]\n"
      "v_pk_max_i16 v33, v33, 0\n"
      "v_mfma_f32_16x16x32_bf16 v[40:43], v[100:103], a[208:211], v[40:43]\n"
      "v_maximum3_f32 v8, v8, v176, v177\n"
      "v_pk_max_i16 v30, v30, 0\n"
      "v_mfma_f32_16x16x32_bf16 v[40:43], v[72:75], a[212:215], v[40:43]\n"
      "v_mul_f32_dpp v44, v90, v44 row_newbcast:0 row_mask:0xf bank_mask:0xf\n"
      "v_pk_max_i16 v28, v28, 0\n"
      "v_mfma_f32_16x16x32_bf16 v[40:43], v[84:87], a[216:219], v[40:43]\n"
      "v_mul_f32_dpp v47, v90, v47 row_newbcast:12 row_mask:0xf bank_mask:0xf\n"
      "v_maximum3_f32 v8, v8, v88, v89\n"
      "v_mfma_f32_16x16x32_bf16 v[40:43], v[124:127], a[220:223], v[40:43]\n"
      "v_pk_max_i16 v190, v190, 0\n"
      "v_add_u32 v10, 64, v10\n"
      "v_mfma_f32_16x16x32_bf16 v[44:47], v[100:103], a[224:227], v[44:47]\n"
      "v_mul_f32_dpp v36, v90, v36 row_newbcast:0 row_mask:0xf bank_mask:0xf\n"
      "v_mul_f32_dpp v38, v90, v38 row_newbcast:8 row_mask:0xf bank_mask:0xf\n"
      "v_mfma_f32_16x16x32_bf16 v[44:47], v[72:75], a[228:231], v[44:47]\n"
      "L_h5_%=:\n"
      "s_cbranch_execz L_hr_%=\n"
      "v_add_u32 v11, 0x200, v11\n"
      "v_mfma_f32_16x16x32_bf16 v[44:47], v[84:87], a[232:235], v[44:47]\n"
      "s_add_u32 s44, s44, 13107200\n s_addc_u32 s45, s45, 0\n"
      "v_pk_max_i16 v193, v193, 0\n"
      "v_mfma_f32_16x16x32_bf16 v[44:47], v[124:127], a[236:239], v[44:47]\n"
      "v_pk_max_i16 v195, v195, 0\n"
      "v_pk_max_i16 v32, v32, 0\n"
      "v_mfma_f32_16x16x32_bf16 v[36:39], v[100:103], a[192:195], v[36:39]\n"
      "v_pk_max_i16 v188, v188, 0\n"
      "v_mfma_f32_16x16x32_bf16 v[36:39], v[72:75], a[196:199], v[36:39]\n"
      "buffer_load_dwordx4 v1, s[40:43], s95 offen offset:3072 nt lds\n"
      "v_mfma_f32_16x16x32_bf16 v[36:39], v[84:87], a[200:203], v[36:39]\n"
      "s_add_u32 s40, s40, 13107200\n s_addc_u32 s41, s41, 0\n"
      "s_add_u32 s38, s38, s38\n"
      "v_mfma_f32_16x16x32_bf16 v[36:39], v[124:127], a[204:207], v[36:39]\n"
      "s_cbranch_scc0 L_task_%=\n"
      "s_cmp_lg_u32 s38, 0\n"
      "s_cbranch_scc1 L_e5_%=\n"
      "L_ln_%=:\n"
      "v_mfma_f32_16x16x32_bf16 v[88:91], v[204:207], v[246:249], 0\n"
      "s_waitcnt vmcnt(40)\n"
      "ds_read_b128 v[92:95], v0 offset:20480\n"
      "ds_read_b128 v[84:87], v0 offset:21504\n"
      "v_mfma_f32_16x16x32_bf16 v[88:91], v[208:211], v[250:253], v[88:91]\n"
      "s_waitcnt vmcnt(30)\n"
      "ds_read_b128 v[72:75], v0 offset:16384\n"
      "ds_read_b128 v[204:207], v0 offset:17920\n"
      "v_mfma_f32_16x16x32_bf16 v[124:127], v[164:167], v[246:249], 0\n"
      "ds_read_b128 v[148:151], v0 offset:16896\n"
      "ds_read_b128 v[164:167], v0 offset:17408\n"
      "v_mfma_f32_16x16x32_bf16 v[124:127], v[168:171], v[250:253], v[124:127]\n"
      "buffer_store_dwordx2 v[28:29], v2, s[44:47], s100 offen offset:0 nt sc1\n"
      "v_mfma_f32_16x16x32_bf16 v[208:211], v[76:79], v[246:249], 0\n"
      "v_mfma_f32_16x16x32_bf16 v[208:211], v[80:83], v[250:253], v[208:211]\n"
      "ds_read_b128 v[76:79], v0 offset:20992\n"
      "ds_read_b128 v[80:83], v0 offset:22016\n"
      "v_mfma_f32_16x16x32_bf16 v[156:159], v[64:67], v[246:249], 0\n"
      "v_cvt_pk_bf16_f32 v38, v38, v42\n"
      "v_cvt_pk_bf16_f32 v36, v36, v40\n"
      "v_mfma_f32_16x16x32_bf16 v[156:159], v[68:71], v[250:253], v[156:159]\n"
      "v_exp_f32 v89, v89\n"
      "v_mfma_f32_16x16x32_bf16 v[240:243], v[20:23], v[246:249], 0\n"
      "v_exp_f32 v124, v124\n"
      "v_mfma_f32_16x16x32_bf16 v[240:243], v[24:27], v[250:253], v[240:243]\n"
      "v_exp_f32 v91, v91\n"
      "v_mfma_f32_16x16x32_bf16 v[176:179], v[136:139], v[246:249], 0\n"
      "v_mfma_f32_16x16x32_bf16 v[176:179], v[140:143], v[250:253], v[176:179]\n"
      "buffer_store_dwordx2 v[190:191], v2, s[44:47], s101 offen offset:512 nt sc1\n"
      "s_waitcnt lgkmcnt(5)\n"
      "v_mfma_f32_16x16x32_bf16 v[140:143], a[32:35], v[72:75], 0\n"
      "s_waitcnt lgkmcnt(3)\n"
      "v_mfma_f32_16x16x32_bf16 v[140:143], a[36:39], v[148:151], v[140:143]\n"
      "s_waitcnt lgkmcnt(2)\n"
      "v_mfma_f32_16x16x32_bf16 v[140:143], a[40:43], v[164:167], v[140:143]\n"
      "v_cvt_pk_bf16_f32 v42, v39, v43\n"
      "v_mfma_f32_16x16x32_bf16 v[140:143], a[44:47], v[204:207], v[140:143]\n"
      "v_exp_f32 v240, v240\n"
      "v_mfma_f32_16x16x32_bf16 v[168:171], a[144:147], v[72:75], 0\n"
      "v_mfma_f32_16x16x32_bf16 v[168:171], a[148:151], v[148:151], v[168:171]\n"
      "v_exp_f32 v211, v211\n"
      "v_mfma_f32_16x16x32_bf16 v[168:171], a[152:155], v[164:167], v[168:171]\n"
      "v_exp_f32 v179, v179\n"
      "v_mfma_f32_16x16x32_bf16 v[168:171], a[156:159], v[204:207], v[168:171]\n"
      "v_exp_f32 v156, v156\n"
      "v_mfma_f32_16x16x32_bf16 v[160:163], a[32:35], v[92:95], 0\n"
      "v_pk_max_i16 v36, v36, 0\n"
      "s_waitcnt lgkmcnt(1)\n"
      "v_mfma_f32_16x16x32_bf16 v[160:163], a[36:39], v[76:79], v[160:163]\n"
      "v_mfma_f32_16x16x32_bf16 v[160:163], a[40:43], v[84:87], v[160:163]\n"
      "s_waitcnt lgkmcnt(0)\n"
      "v_mfma_f32_16x16x32_bf16 v[160:163], a[44:47], v[80:83], v[160:163]\n"
      "v_exp_f32 v208, v208\n"
      "v_mfma_f32_16x16x32_bf16 v[20:23], a[176:179], v[92:95], 0\n"
      "v_exp_f32 v127, v127\n"
      "v_mfma_f32_16x16x32_bf16 v[20:23], a[180:183], v[76:79], v[20:23]\n"
      "v_exp_f32 v157, v157\n"
      "v_mfma_f32_16x16x32_bf16 v[20:23], a[184:187], v[84:87], v[20:23]\n"
      "v_exp_f32 v243, v243\n"
      "v_mfma_f32_16x16x32_bf16 v[20:23], a[188:191], v[80:83], v[20:23]\n"
      "v_cvt_pk_bf16_f32 v100, v156, v157\n"
      "v_pk_max_i16 v42, v42, 0\n"
      "v_mfma_f32_16x16x32_bf16 v[196:199], a[48:51], v[72:75], 0\n"
      "v_exp_f32 v159, v159\n"
      "v_mfma_f32_16x16x32_bf16 v[196:199], a[52:55], v[148:151], v[196:199]\n"
      "v_exp_f32 v210, v210\n"
      "v_mfma_f32_16x16x32_bf16 v[196:199], a[56:59], v[164:167], v[196:199]\n"
      "v_exp_f32 v158, v158\n"
      "v_mfma_f32_16x16x32_bf16 v[196:199], a[60:63], v[204:207], v[196:199]\n"
      "v_exp_f32 v126, v126\n"
      "v_mfma_f32_16x16x32_bf16 v[26:29], a[0:3], v[92:95], 0\n"
      "v_mfma_f32_16x16x32_bf16 v[26:29], a[4:7], v[76:79], v[26:29]\n"
      "v_exp_f32 v241, v241\n"
      "v_mfma_f32_16x16x32_bf16 v[26:29], a[8:11], v[84:87], v[26:29]\n"
      "v_cvt_pk_bf16_f32 v101, v158, v159\n"
      "v_cvt_pk_bf16_f32 v40, v37, v41\n"
      "v_mfma_f32_16x16x32_bf16 v[26:29], a[12:15], v[80:83], v[26:29]\n"
      "buffer_store_dwordx2 v[192:193], v2, s[44:47], s101 offen offset:256 nt sc1\n"
      "v_cvt_pk_bf16_f32 v39, v46, v50\n"
      "v_mfma_f32_16x16x32_bf16 v[244:247], a[16:19], v[92:95], 0\n"
      "v_cvt_pk_bf16_f32 v252, v240, v241\n"
      "v_pk_max_i16 v39, v39, 0\n"
      "v_mfma_f32_16x16x32_bf16 v[244:247], a[20:23], v[76:79], v[244:247]\n"
      "v_exp_f32 v209, v209\n"
      "v_mfma_f32_16x16x32_bf16 v[244:247], a[24:27], v[84:87], v[244:247]\n"
      "v_exp_f32 v178, v178\n"
      "v_mfma_f32_16x16x32_bf16 v[244:247], a[28:31], v[80:83], v[244:247]\n"
      "v_exp_f32 v177, v177\n"
      "v_mfma_f32_16x16x32_bf16 v[152:155], a[128:131], v[92:95], 0\n"
      "v_mfma_f32_16x16x32_bf16 v[152:155], a[132:135], v[76:79], v[152:155]\n"
      "v_exp_f32 v176, v176\n"
      "v_mfma_f32_16x16x32_bf16 v[152:155], a[136:139], v[84:87], v[152:155]\n"
      "v_exp_f32 v88, v88\n"
      "v_mfma_f32_16x16x32_bf16 v[152:155], a[140:143], v[80:83], v[152:155]\n"
      "v_exp_f32 v90, v90\n"
      "v_mfma_f32_16x16x32_bf16 v[156:159], a[48:51], v[92:95], 0\n"
      "v_cvt_pk_bf16_f32 v251, v90, v91\n"
      "v_cvt_pk_bf16_f32 v254, v208, v209\n"
      "v_mfma_f32_16x16x32_bf16 v[156:159], a[52:55], v[76:79], v[156:159]\n"
      "v_mfma_f32_16x16x32_bf16 v[156:159], a[56:59], v[84:87], v[156:159]\n"
      "v_cvt_pk_bf16_f32 v68, v160, v161\n"
      "v_mfma_f32_16x16x32_bf16 v[156:159], a[60:63], v[80:83], v[156:159]\n"
      "v_cvt_pk_bf16_f32 v103, v178, v179\n"
      "v_cvt_pk_bf16_f32 v255, v210, v211\n"
      "v_mfma_f32_16x16x32_bf16 v[136:139], a[176:179], v[72:75], 0\n"
      "v_exp_f32 v242, v242\n"
      "v_mfma_f32_16x16x32_bf16 v[136:139], a[180:183], v[148:151], v[136:139]\n"
      "v_cvt_pk_bf16_f32 v241, v142, v143\n"
      "v_cvt_pk_bf16_f32 v250, v88, v89\n"
      "v_mfma_f32_16x16x32_bf16 v[136:139], a[184:187], v[164:167], v[136:139]\n"
      "v_cvt_pk_bf16_f32 v240, v140, v141\n"
      "v_cvt_pk_bf16_f32 v102, v176, v177\n"
      "v_mfma_f32_16x16x32_bf16 v[136:139], a[188:191], v[204:207], v[136:139]\n"
      "v_exp_f32 v125, v125\n"
      "v_mfma_f32_16x16x32_bf16 v[88:91], a[128:131], v[72:75], 0\n"
      "v_mfma_f32_16x16x32_bf16 v[88:91], a[132:135], v[148:151], v[88:91]\n"
      "v_cvt_pk_bf16_f32 v249, v126, v127\n"
      "v_mfma_f32_16x16x32_bf16 v[88:91], a[136:139], v[164:167], v[88:91]\n"
      "v_mfma_f32_16x16x32_bf16 v[88:91], a[140:143], v[204:207], v[88:91]\n"
      "buffer_store_dwordx2 v[30:31], v2, s[44:47], s100 offen offset:512 nt sc1\n"
      "v_cvt_pk_bf16_f32 v253, v242, v243\n"
      "v_mfma_f32_16x16x32_bf16 v[200:203], a[160:163], v[92:95], 0\n"
      "s_waitcnt vmcnt(17)\n"
      "ds_read_b128 v[190:193], v0 offset:8192\n"
      "v_mfma_f32_16x16x32_bf16 v[200:203], a[164:167], v[76:79], v[200:203]\n"
      "v_cvt_pk_bf16_f32 v69, v162, v163\n"
      "v_mfma_f32_16x16x32_bf16 v[200:203], a[168:171], v[84:87], v[200:203]\n"
      "v_cvt_pk_bf16_f32 v248, v124, v125\n"
      "v_mfma_f32_16x16x32_bf16 v[200:203], a[172:175], v[80:83], v[200:203]\n"
      "ds_read_b128 v[124:127], v9 offset:2048\n"
      "v_pk_max_i16 v40, v40, 0\n"
      "v_smfmac_f32_16x16x64_bf16 v[128:131], v[12:15], v[248:255], v5\n"
      "ds_read_b128 v[140:143], v0 offset:9216\n"
      "v_cvt_pk_bf16_f32 v243, v198, v199\n"
      "v_mfma_f32_16x16x32_bf16 v[220:223], v[248:251], v[56:59], v[220:223]\n"
      "ds_read_b128 v[208:211], v0 offset:9728\n"
      "v_mfma_f32_16x16x32_bf16 v[220:223], v[252:255], v[60:63], v[220:223]\n"
      "v_mfma_f32_16x16x32_bf16 v[220:223], v[100:103], v[116:119], v[220:223]\n"
      "v_cvt_pk_bf16_f32 v31, v22, v23\n"
      "v_smfmac_f32_16x16x32_bf16 v[128:131], v[12:13], v[100:103], v5\n"
      "buffer_store_dwordx2 v[194:195], v2, s[44:47], s101 offen offset:768 nt sc1\n"
      "v_mfma_f32_16x16x32_bf16 v[216:219], v[248:251], v[180:183], v[216:219]\n"
      "v_cvt_pk_bf16_f32 v65, v28, v29\n"
      "v_cvt_pk_bf16_f32 v43, v47, v51\n"
      "v_mfma_f32_16x16x32_bf16 v[216:219], v[252:255], v[184:187], v[216:219]\n"
      "v_cvt_pk_bf16_f32 v41, v45, v49\n"
      "v_mfma_f32_16x16x32_bf16 v[216:219], v[100:103], v[120:123], v[216:219]\n"
      "ds_read_b128 v[176:179], v0 offset:8704\n"
      "v_add_f32 v128, v128, v129\n"
      "v_mfma_f32_16x16x32_bf16 v[224:227], v[248:251], v[232:235], v[224:227]\n"
      "v_mfma_f32_16x16x32_bf16 v[224:227], v[252:255], v[236:239], v[224:227]\n"
      "v_rcp_f32 v129, v128\n"
      "v_mfma_f32_16x16x32_bf16 v[224:227], v[100:103], v[228:231], v[224:227]\n"
      "ds_bpermute_b32 v130, v4, v129 offset:128\n"
      "v_mfma_f32_16x16x32_bf16 v[232:235], a[144:147], v[92:95], 0\n"
      "v_mfma_f32_16x16x32_bf16 v[232:235], a[148:151], v[76:79], v[232:235]\n"
      "v_mfma_f32_16x16x32_bf16 v[232:235], a[152:155], v[84:87], v[232:235]\n"
      "ds_read_b128 v[116:119], v10 offset:0\n"
      "v_mfma_f32_16x16x32_bf16 v[232:235], a[156:159], v[80:83], v[232:235]\n"
      "ds_read_b128 v[120:123], v9 offset:0\n"
      "v_mfma_f32_16x16x32_bf16 v[212:215], v[248:251], v[108:111], v[212:215]\n"
      "v_cvt_pk_bf16_f32 v37, v44, v48\n"
      "v_mov_b64 v[44:45], 0\n"
      "v_mfma_f32_16x16x32_bf16 v[212:215], v[252:255], v[112:115], v[212:215]\n"
      "v_mfma_f32_16x16x32_bf16 v[212:215], v[100:103], v[52:55], v[212:215]\n"
      "v_cvt_pk_bf16_f32 v24, v152, v153\n"
      "v_mfma_f32_16x16x32_bf16 v[100:103], a[16:19], v[72:75], 0\n"
      "v_mfma_f32_16x16x32_bf16 v[100:103], a[20:23], v[148:151], v[100:103]\n"
      "v_cvt_pk_bf16_f32 v64, v26, v27\n"
      "v_cvt_pk_bf16_f32 v28, v200, v201\n"
      "v_mfma_f32_16x16x32_bf16 v[100:103], a[24:27], v[164:167], v[100:103]\n"
      "s_waitcnt lgkmcnt(2)\n"
      "v_mul_f32_dpp v216, v130, v216 row_newbcast:0 row_mask:0xf bank_mask:0xf\n"
      "v_mul_f32_dpp v217, v130, v217 row_newbcast:4 row_mask:0xf bank_mask:0xf\n"
      "v_mfma_f32_16x16x32_bf16 v[100:103], a[28:31], v[204:207], v[100:103]\n"
      "v_cvt_pk_bf16_f32 v67, v246, v247\n"
      "v_mul_f32_dpp v214, v130, v214 row_newbcast:8 row_mask:0xf bank_mask:0xf\n"
      "v_mfma_f32_16x16x32_bf16 v[252:255], a[160:163], v[72:75], 0\n"
      "v_mul_f32_dpp v212, v130, v212 row_newbcast:0 row_mask:0xf bank_mask:0xf\n"
      "v_cvt_pk_bf16_f32 v30, v20, v21\n"
      "v_mfma_f32_16x16x32_bf16 v[252:255], a[164:167], v[148:151], v[252:255]\n"
      "v_mul_f32_dpp v225, v130, v225 row_newbcast:4 row_mask:0xf bank_mask:0xf\n"
      "v_mfma_f32_16x16x32_bf16 v[252:255], a[168:171], v[164:167], v[252:255]\n"
      "v_pk_max_i16 v37, v37, 0\n"
      "v_mul_f32_dpp v223, v130, v223 row_newbcast:12 row_mask:0xf bank_mask:0xf\n"
      "v_mfma_f32_16x16x32_bf16 v[252:255], a[172:175], v[204:207], v[252:255]\n"
      "buffer_store_dwordx2 v[36:37], v2, s[44:47], s34 offen offset:0 nt sc1\n"
      "v_cvt_pk_bf16_f32 v238, v100, v101\n"
      "v_mfma_f32_16x16x32_bf16 v[48:51], a[0:3], v[72:75], 0\n"
      "v_cvt_pk_bf16_f32 v29, v202, v203\n"
      "v_mfma_f32_16x16x32_bf16 v[48:51], a[4:7], v[148:151], v[48:51]\n"
      "v_mul_f32_dpp v219, v130, v219 row_newbcast:12 row_mask:0xf bank_mask:0xf\n"
      "v_mfma_f32_16x16x32_bf16 v[48:51], a[8:11], v[164:167], v[48:51]\n"
      "v_mul_f32_dpp v213, v130, v213 row_newbcast:4 row_mask:0xf bank_mask:0xf\n"
      "v_mul_f32_dpp v227, v130, v227 row_newbcast:12 row_mask:0xf bank_mask:0xf\n"
      "v_mfma_f32_16x16x32_bf16 v[48:51], a[12:15], v[204:207], v[48:51]\n"
      "v_cvt_pk_bf16_f32 v58, v136, v137\n"
      "v_cvt_pk_bf16_f32 v27, v234, v235\n"
      "v_mfma_f32_16x16x32_bf16 v[228:231], v[190:193], a[96:99], 0\n"
      "v_mul_f32_dpp v224, v130, v224 row_newbcast:0 row_mask:0xf bank_mask:0xf\n"
      "v_mfma_f32_16x16x32_bf16 v[228:231], v[176:179], a[100:103], v[228:231]\n"
      "v_mov_b64 v[246:247], 0\n"
      "v_pk_max_i16 v38, v38, 0\n"
      "v_mfma_f32_16x16x32_bf16 v[228:231], v[140:143], a[104:107], v[228:231]\n"
      "v_pk_max_i16 v41, v41, 0\n"
      "v_mfma_f32_16x16x32_bf16 v[228:231], v[208:211], a[108:111], v[228:231]\n"
      "v_mul_f32_dpp v220, v130, v220 row_newbcast:0 row_mask:0xf bank_mask:0xf\n"
      "v_mfma_f32_16x16x32_bf16 v[184:187], v[190:193], a[80:83], 0\n"
      "v_mul_f32_dpp v222, v130, v222 row_newbcast:8 row_mask:0xf bank_mask:0xf\n"
      "v_mfma_f32_16x16x32_bf16 v[184:187], v[176:179], a[84:87], v[184:187]\n"
      "v_mfma_f32_16x16x32_bf16 v[184:187], v[140:143], a[88:91], v[184:187]\n"
      "v_mov_b64 v[152:153], 0\n"
      "v_mfma_f32_16x16x32_bf16 v[184:187], v[208:211], a[92:95], v[184:187]\n"
      "v_cvt_pk_bf16_f32 v71, v158, v159\n"
      "v_mfma_f32_16x16x32_bf16 v[20:23], v[190:193], a[112:115], 0\n"
      "v_cvt_pk_bf16_f32 v57, v254, v255\n"
      "v_mul_f32_dpp v218, v130, v218 row_newbcast:8 row_mask:0xf bank_mask:0xf\n"
      "v_mfma_f32_16x16x32_bf16 v[20:23], v[176:179], a[116:119], v[20:23]\n"
      "v_mul_f32_dpp v215, v130, v215 row_newbcast:12 row_mask:0xf bank_mask:0xf\n"
      "v_cvt_pk_bf16_f32 v136, v228, v229\n"
      "v_mfma_f32_16x16x32_bf16 v[20:23], v[140:143], a[120:123], v[20:23]\n"
      "v_cvt_pk_bf16_f32 v236, v48, v49\n"
      "v_mfma_f32_16x16x32_bf16 v[20:23], v[208:211], a[124:127], v[20:23]\n"
      "v_cvt_pk_bf16_f32 v55, v170, v171\n"
      "v_mul_f32_dpp v226, v130, v226 row_newbcast:8 row_mask:0xf bank_mask:0xf\n"
      "v_mfma_f32_16x16x32_bf16 v[212:215], v[144:147], a[192:195], v[212:215]\n"
      "v_cvt_pk_bf16_f32 v66, v244, v245\n"
      "v_cvt_pk_bf16_f32 v237, v50, v51\n"
      "v_mfma_f32_16x16x32_bf16 v[212:215], v[104:107], a[196:199], v[212:215]\n"
      "v_mfma_f32_16x16x32_bf16 v[212:215], v[172:175], a[200:203], v[212:215]\n"
      "v_cvt_pk_bf16_f32 v239, v102, v103\n"
      "v_mfma_f32_16x16x32_bf16 v[212:215], v[132:135], a[204:207], v[212:215]\n"
      "v_cvt_pk_bf16_f32 v53, v90, v91\n"
      "v_cvt_pk_bf16_f32 v242, v196, v197\n"
      "v_mfma_f32_16x16x32_bf16 v[248:251], a[32:35], v[190:193], 0\n"
      "v_cvt_pk_bf16_f32 v26, v232, v233\n"
      "v_mfma_f32_16x16x32_bf16 v[248:251], a[36:39], v[176:179], v[248:251]\n"
      "v_mfma_f32_16x16x32_bf16 v[248:251], a[40:43], v[140:143], v[248:251]\n"
      "v_mfma_f32_16x16x32_bf16 v[248:251], a[44:47], v[208:211], v[248:251]\n"
      "buffer_store_dwordx2 v[40:41], v2, s[44:47], s34 offen offset:256 nt sc1\n"
      "v_maximum3_f32 v8, v8, v128, v129\n"
      "v_mfma_f32_16x16x32_bf16 v[224:227], v[144:147], a[240:243], v[224:227]\n"
      "v_mfma_f32_16x16x32_bf16 v[224:227], v[104:107], a[244:247], v[224:227]\n"
      "v_mfma_f32_16x16x32_bf16 v[224:227], v[172:175], a[248:251], v[224:227]\n"
      "v_mfma_f32_16x16x32_bf16 v[224:227], v[132:135], a[252:255], v[224:227]\n"
      "ds_read_b128 v[232:235], v0 offset:12288\n"
      "v_cvt_pk_bf16_f32 v70, v156, v157\n"
      "v_mfma_f32_16x16x32_bf16 v[216:219], v[144:147], a[208:211], v[216:219]\n"
      "s_waitcnt vmcnt(13)\n"
      "ds_read_b128 v[156:159], v0 offset:4096\n"
      "ds_read_b128 v[48:51], v0 offset:5632\n"
      "v_mfma_f32_16x16x32_bf16 v[216:219], v[104:107], a[212:215], v[216:219]\n"
      "v_cvt_pk_bf16_f32 v108, v248, v249\n"
      "v_mul_f32_dpp v221, v130, v221 row_newbcast:4 row_mask:0xf bank_mask:0xf\n"
      "v_mfma_f32_16x16x32_bf16 v[216:219], v[172:175], a[216:219], v[216:219]\n"
      "v_mfma_f32_16x16x32_bf16 v[216:219], v[132:135], a[220:223], v[216:219]\n"
      "v_cvt_pk_bf16_f32 v25, v154, v155\n"
      "v_mfma_f32_16x16x32_bf16 v[100:103], a[0:3], v[190:193], 0\n"
      "ds_read2_b32 v[112:113], v16 offset0:0 offset1:2\n"
      "v_mfma_f32_16x16x32_bf16 v[100:103], a[4:7], v[176:179], v[100:103]\n"
      "v_mfma_f32_16x16x32_bf16 v[100:103], a[8:11], v[140:143], v[100:103]\n"
      "ds_read_b128 v[194:197], v0 offset:5120\n"
      "v_mfma_f32_16x16x32_bf16 v[100:103], a[12:15], v[208:211], v[100:103]\n"
      "s_waitcnt lgkmcnt(6)\n"
      "v_smfmac_f32_16x16x64_bf16 v[152:155], v[116:119], v[236:243], v5\n"
      "buffer_store_dwordx2 v[38:39], v2, s[44:47], s34 offen offset:512 nt sc1\n"
      "v_mfma_f32_16x16x32_bf16 v[128:131], v[236:239], v[24:27], 0\n"
      "v_mfma_f32_16x16x32_bf16 v[128:131], v[240:243], v[28:31], v[128:131]\n"
      "ds_read_b128 v[180:183], v0 offset:13824\n"
      "v_cvt_pk_bf16_f32 v52, v88, v89\n"
      "v_mfma_f32_16x16x32_bf16 v[88:91], v[64:67], v[24:27], 0\n"
      "v_cvt_pk_bf16_f32 v214, v214, v218\n"
      "v_mfma_f32_16x16x32_bf16 v[88:91], v[68:71], v[28:31], v[88:91]\n"
      "s_waitcnt lgkmcnt(6)\n"
      "v_smfmac_f32_16x16x64_bf16 v[246:249], v[116:119], v[120:127], v5\n"
      "ds_read_b128 v[120:123], v0 offset:4608\n"
      "v_mfma_f32_16x16x32_bf16 v[200:203], a[48:51], v[190:193], 0\n"
      "v_cvt_pk_bf16_f32 v229, v22, v23\n"
      "v_cvt_pk_bf16_f32 v109, v250, v251\n"
      "v_mfma_f32_16x16x32_bf16 v[200:203], a[52:55], v[176:179], v[200:203]\n"
      "v_cvt_pk_bf16_f32 v59, v138, v139\n"
      "v_cvt_pk_bf16_f32 v212, v212, v216\n"
      "v_mfma_f32_16x16x32_bf16 v[200:203], a[56:59], v[140:143], v[200:203]\n"
      "v_cvt_pk_bf16_f32 v216, v213, v217\n"
      "v_add_f32 v152, v152, v153\n"
      "v_mfma_f32_16x16x32_bf16 v[200:203], a[60:63], v[208:211], v[200:203]\n"
      "v_cvt_pk_bf16_f32 v218, v215, v219\n"
      "v_cvt_pk_bf16_f32 v41, v186, v187\n"
      "v_mfma_f32_16x16x32_bf16 v[220:223], v[144:147], a[224:227], v[220:223]\n"
      "v_mfma_f32_16x16x32_bf16 v[220:223], v[104:107], a[228:231], v[220:223]\n"
      "v_cvt_pk_bf16_f32 v40, v184, v185\n"
      "v_mfma_f32_16x16x32_bf16 v[220:223], v[172:175], a[232:235], v[220:223]\n"
      "v_exp_f32 v129, v129\n"
      "v_mfma_f32_16x16x32_bf16 v[220:223], v[132:135], a[236:239], v[220:223]\n"
      "buffer_store_dwordx2 v[188:189], v2, s[44:47], s101 offen offset:0 nt sc1\n"
      "s_waitcnt lgkmcnt(5)\n"
      "v_mfma_f32_16x16x32_bf16 v[36:39], a[0:3], v[156:159], 0\n"
      "v_cvt_pk_bf16_f32 v111, v202, v203\n"
      "s_waitcnt lgkmcnt(0)\n"
      "v_mfma_f32_16x16x32_bf16 v[36:39], a[4:7], v[120:123], v[36:39]\n"
      "v_mfma_f32_16x16x32_bf16 v[36:39], a[8:11], v[194:197], v[36:39]\n"
      "v_exp_f32 v89, v89\n"
      "v_mfma_f32_16x16x32_bf16 v[36:39], a[12:15], v[48:51], v[36:39]\n"
      "v_cvt_pk_bf16_f32 v105, v102, v103\n"
      "v_mfma_f32_16x16x32_bf16 v[124:127], v[156:159], a[80:83], 0\n"
      "v_pk_max_i16 v216, v216, 0\n"
      "v_cvt_pk_bf16_f32 v215, v222, v226\n"
      "v_mfma_f32_16x16x32_bf16 v[124:127], v[120:123], a[84:87], v[124:127]\n"
      "v_mfma_f32_16x16x32_bf16 v[124:127], v[194:197], a[88:91], v[124:127]\n"
      "v_cvt_pk_bf16_f32 v219, v223, v227\n"
      "v_mfma_f32_16x16x32_bf16 v[124:127], v[48:51], a[92:95], v[124:127]\n"
      "v_pk_max_i16 v219, v219, 0\n"
      "v_cvt_pk_bf16_f32 v54, v168, v169\n"
      "v_mfma_f32_16x16x32_bf16 v[172:175], v[190:193], a[64:67], 0\n"
      "v_cvt_pk_bf16_f32 v110, v200, v201\n"
      "v_mfma_f32_16x16x32_bf16 v[172:175], v[176:179], a[68:71], v[172:175]\n"
      "v_mfma_f32_16x16x32_bf16 v[172:175], v[140:143], a[72:75], v[172:175]\n"
      "v_mfma_f32_16x16x32_bf16 v[172:175], v[208:211], a[76:79], v[172:175]\n"
      "v_exp_f32 v130, v130\n"
      "v_mfma_f32_16x16x32_bf16 v[200:203], a[48:51], v[156:159], 0\n"
      "v_mfma_f32_16x16x32_bf16 v[200:203], a[52:55], v[120:123], v[200:203]\n"
      "v_cvt_pk_bf16_f32 v213, v220, v224\n"
      "v_cvt_pk_bf16_f32 v185, v38, v39\n"
      "v_mfma_f32_16x16x32_bf16 v[200:203], a[56:59], v[194:197], v[200:203]\n"
      "s_nop 1\n"
      "v_cvt_pk_bf16_f32 v144, v172, v173\n"
      "v_mfma_f32_16x16x32_bf16 v[200:203], a[60:63], v[48:51], v[200:203]\n"
      "v_exp_f32 v88, v88\n"
      "v_mfma_f32_16x16x32_bf16 v[168:171], a[16:19], v[190:193], 0\n"
      "ds_read_b128 v[248:251], v0 offset:13312\n"
      "v_pk_max_i16 v213, v213, 0\n"
      "v_mfma_f32_16x16x32_bf16 v[168:171], a[20:23], v[176:179], v[168:171]\n"
      "v_exp_f32 v91, v91\n"
      "v_mfma_f32_16x16x32_bf16 v[168:171], a[24:27], v[140:143], v[168:171]\n"
      "v_cvt_pk_bf16_f32 v56, v252, v253\n"
      "v_cvt_pk_bf16_f32 v39, v126, v127\n"
      "v_mfma_f32_16x16x32_bf16 v[168:171], a[28:31], v[208:211], v[168:171]\n"
      "ds_read_b128 v[252:255], v0 offset:12800\n"
      "v_mfma_f32_16x16x32_bf16 v[176:179], a[16:19], v[156:159], 0\n"
      "v_cvt_pk_bf16_f32 v191, v202, v203\n"
      "v_mfma_f32_16x16x32_bf16 v[176:179], a[20:23], v[120:123], v[176:179]\n"
      "v_mfma_f32_16x16x32_bf16 v[176:179], a[24:27], v[194:197], v[176:179]\n"
      "v_cvt_pk_bf16_f32 v38, v124, v125\n"
      "v_cvt_pk_bf16_f32 v217, v221, v225\n"
      "v_mfma_f32_16x16x32_bf16 v[176:179], a[28:31], v[48:51], v[176:179]\n"
      "buffer_store_dwordx2 v[34:35], v2, s[44:47], s100 offen offset:768 nt sc1\n"
      "v_mfma_f32_16x16x32_bf16 v[208:211], a[32:35], v[156:159], 0\n"
      "v_cvt_pk_bf16_f32 v190, v200, v201\n"
      "v_cvt_pk_bf16_f32 v106, v168, v169\n"
      "v_mfma_f32_16x16x32_bf16 v[208:211], a[36:39], v[120:123], v[208:211]\n"
      "v_mfma_f32_16x16x32_bf16 v[208:211], a[40:43], v[194:197], v[208:211]\n"
      "v_mfma_f32_16x16x32_bf16 v[208:211], a[44:47], v[48:51], v[208:211]\n"
      "s_nop 0\n"
      "v_cvt_pk_bf16_f32 v187, v178, v179\n"
      "v_mov_b64 v[138:139], 0\n"
      "v_mfma_f32_16x16x32_bf16 v[140:143], a[0:3], v[232:235], 0\n"
      "v_cvt_pk_bf16_f32 v126, v88, v89\n"
      "s_waitcnt lgkmcnt(0)\n"
      "v_mfma_f32_16x16x32_bf16 v[140:143], a[4:7], v[252:255], v[140:143]\n"
      "v_cvt_pk_bf16_f32 v186, v176, v177\n"
      "v_mfma_f32_16x16x32_bf16 v[140:143], a[8:11], v[248:251], v[140:143]\n"
      "v_exp_f32 v90, v90\n"
      "v_mfma_f32_16x16x32_bf16 v[140:143], a[12:15], v[180:183], v[140:143]\n"
      "v_pk_max_i16 v43, v43, 0\n"
      "v_pk_max_i16 v212, v212, 0\n"
      "v_mfma_f32_16x16x32_bf16 v[132:135], v[232:235], a[80:83], 0\n"
      "v_mfma_f32_16x16x32_bf16 v[132:135], v[252:255], a[84:87], v[132:135]\n"
      "v_exp_f32 v128, v128\n"
      "v_mfma_f32_16x16x32_bf16 v[132:135], v[248:251], a[88:91], v[132:135]\n"
      "v_mfma_f32_16x16x32_bf16 v[132:135], v[180:183], a[92:95], v[132:135]\n"
      "v_cvt_pk_bf16_f32 v124, v128, v129\n"
      "v_smfmac_f32_16x16x64_bf16 v[44:47], v[116:119], v[64:71], v5\n"
      "buffer_store_dwordx2 v[42:43], v2, s[44:47], s34 offen offset:768 nt sc1\n"
      "v_mfma_f32_16x16x32_bf16 v[200:203], a[16:19], v[232:235], 0\n"
      "v_exp_f32 v152, v152\n"
      "v_mfma_f32_16x16x32_bf16 v[200:203], a[20:23], v[252:255], v[200:203]\n"
      "v_cvt_pk_bf16_f32 v104, v100, v101\n"
      "v_add_f32 v246, v246, v247\n"
      "v_mfma_f32_16x16x32_bf16 v[200:203], a[24:27], v[248:251], v[200:203]\n"
      "v_cvt_pk_bf16_f32 v43, v134, v135\n"
      "v_add_f32 v44, v44, v45\n"
      "v_mfma_f32_16x16x32_bf16 v[200:203], a[28:31], v[180:183], v[200:203]\n"
      "v_pk_max_i16 v217, v217, 0\n"
      "v_cvt_pk_bf16_f32 v107, v170, v171\n"
      "v_mfma_f32_16x16x32_bf16 v[224:227], a[48:51], v[232:235], 0\n"
      "v_exp_f32 v131, v131\n"
      "v_mfma_f32_16x16x32_bf16 v[224:227], a[52:55], v[252:255], v[224:227]\n"
      "v_mov_b32 v247, 0\n"
      "v_mfma_f32_16x16x32_bf16 v[224:227], a[56:59], v[248:251], v[224:227]\n"
      "v_cvt_pk_bf16_f32 v189, v210, v211\n"
      "v_cvt_pk_bf16_f32 v188, v208, v209\n"
      "v_mfma_f32_16x16x32_bf16 v[224:227], a[60:63], v[180:183], v[224:227]\n"
      "v_cvt_pk_bf16_f32 v184, v36, v37\n"
      "v_cvt_pk_bf16_f32 v170, v200, v201\n"
      "v_mfma_f32_16x16x32_bf16 v[208:211], v[156:159], a[96:99], 0\n"
      "s_waitcnt vmcnt(11)\n"
      "ds_read_b128 v[34:37], v0 offset:0\n"
      "v_cvt_pk_bf16_f32 v127, v90, v91\n"
      "v_mfma_f32_16x16x32_bf16 v[208:211], v[120:123], a[100:103], v[208:211]\n"
      "ds_read_b128 v[176:179], v0 offset:1536\n"
      "v_cvt_pk_bf16_f32 v125, v130, v131\n"
      "v_mfma_f32_16x16x32_bf16 v[208:211], v[194:197], a[104:107], v[208:211]\n"
      "ds_read_b128 v[220:223], v0 offset:1024\n"
      "v_cvt_pk_bf16_f32 v228, v20, v21\n"
      "v_mfma_f32_16x16x32_bf16 v[208:211], v[48:51], a[108:111], v[208:211]\n"
      "ds_read2_b32 v[114:115], v16 offset0:128 offset1:130\n"
      "v_cvt_pk_bf16_f32 v171, v202, v203\n"
      "v_mfma_f32_16x16x32_bf16 v[88:91], v[156:159], a[64:67], 0\n"
      "v_cvt_pk_bf16_f32 v42, v132, v133\n"
      "v_mfma_f32_16x16x32_bf16 v[88:91], v[120:123], a[68:71], v[88:91]\n"
      "v_exp_f32_dpp v247, v246 quad_perm:[0,1,2,3] row_mask:0xf bank_mask:0x1\n"
      "v_mfma_f32_16x16x32_bf16 v[88:91], v[194:197], a[72:75], v[88:91]\n"
      "v_cvt_pk_bf16_f32 v137, v230, v231\n"
      "v_mov_b64 v[20:21], 0\n"
      "v_mfma_f32_16x16x32_bf16 v[88:91], v[48:51], a[76:79], v[88:91]\n"
      "buffer_store_dwordx2 v[212:213], v2, s[44:47], s35 offen offset:0 nt sc1\n"
      "ds_read_b128 v[128:131], v0 offset:512\n"
      "v_mfma_f32_16x16x32_bf16 v[100:103], v[156:159], a[112:115], 0\n"
      "v_mfma_f32_16x16x32_bf16 v[100:103], v[120:123], a[116:119], v[100:103]\n"
      "v_cvt_pk_bf16_f32 v168, v140, v141\n"
      "v_cvt_pk_bf16_f32 v145, v174, v175\n"
      "v_mfma_f32_16x16x32_bf16 v[100:103], v[194:197], a[120:123], v[100:103]\n"
      "v_cvt_pk_bf16_f32 v174, v224, v225\n"
      "v_mfma_f32_16x16x32_bf16 v[100:103], v[48:51], a[124:127], v[100:103]\n"
      "v_pk_max_i16 v218, v218, 0\n"
      "v_smfmac_f32_16x16x64_bf16 v[138:141], v[116:119], v[184:191], v5\n"
      "v_mfma_f32_16x16x32_bf16 v[120:123], v[104:107], v[24:27], 0\n"
      "v_pk_max_i16 v215, v215, 0\n"
      "v_cvt_pk_bf16_f32 v135, v210, v211\n"
      "v_mfma_f32_16x16x32_bf16 v[120:123], v[108:111], v[28:31], v[120:123]\n"
      "s_waitcnt lgkmcnt(1)\n"
      "v_mov_b32_dpp v194, v114 row_ror:8 row_mask:0xf bank_mask:0xf\n"
      "v_mov_b32_dpp v246, v247 quad_perm:[1,1,3,3] row_mask:0xf bank_mask:0xf\n"
      "v_smfmac_f32_16x16x64_bf16 v[20:23], v[116:119], v[104:111], v5\n"
      "v_add_f32 v138, v138, v139\n"
      "v_mfma_f32_16x16x32_bf16 v[48:51], a[32:35], v[34:37], 0\n"
      "s_waitcnt lgkmcnt(0)\n"
      "v_mfma_f32_16x16x32_bf16 v[48:51], a[36:39], v[128:131], v[48:51]\n"
      "v_mfma_f32_16x16x32_bf16 v[48:51], a[40:43], v[220:223], v[48:51]\n"
      "v_cvt_pk_bf16_f32 v246, v247, v246\n"
      "v_cvt_pk_bf16_f32 v175, v226, v227\n"
      "v_mfma_f32_16x16x32_bf16 v[48:51], a[44:47], v[176:179], v[48:51]\n"
      "v_exp_f32 v44, v44\n"
      "v_mfma_f32_16x16x32_bf16 v[200:203], a[48:51], v[34:37], 0\n"
      "v_exp_f32 v121, v121\n"
      "v_mfma_f32_16x16x32_bf16 v[200:203], a[52:55], v[128:131], v[200:203]\n"
      "v_cvt_pk_bf16_f32 v169, v142, v143\n"
      "v_cndmask_b32_dpp v153, v44, v152, vcc quad_perm:[0,0,2,2] row_mask:0xf bank_mask:0xf\n"
      "v_mfma_f32_16x16x32_bf16 v[200:203], a[56:59], v[220:223], v[200:203]\n"
      "v_exp_f32 v120, v120\n"
      "v_mfma_f32_16x16x32_bf16 v[200:203], a[60:63], v[176:179], v[200:203]\n"
      "v_cvt_pk_bf16_f32 v227, v102, v103\n"
      "v_pk_max_i16 v214, v214, 0\n"
      "v_mfma_f32_16x16x32_bf16 v[156:159], a[16:19], v[34:37], 0\n"
      "v_cvt_pk_bf16_f32 v134, v208, v209\n"
      "v_cvt_pk_bf16_f32 v226, v100, v101\n"
      "v_mfma_f32_16x16x32_bf16 v[156:159], a[20:23], v[128:131], v[156:159]\n"
      "v_mfma_f32_16x16x32_bf16 v[156:159], a[24:27], v[220:223], v[156:159]\n"
      "v_cvt_pk_bf16_f32 v143, v90, v91\n"
      "v_cvt_pk_bf16_f32 v142, v88, v89\n"
      "v_mfma_f32_16x16x32_bf16 v[156:159], a[28:31], v[176:179], v[156:159]\n"
      "buffer_store_dwordx2 v[214:215], v2, s[44:47], s35 offen offset:512 nt sc1\n"
      "v_mov_b32_dpp v192, v112 row_ror:8 row_mask:0xf bank_mask:0xf\n"
      "v_mfma_f32_16x16x32_bf16 v[88:91], a[0:3], v[34:37], 0\n"
      "v_add_f32 v20, v20, v21\n"
      "v_mfma_f32_16x16x32_bf16 v[88:91], a[4:7], v[128:131], v[88:91]\n"
      "v_cvt_pk_bf16_f32 v214, v200, v201\n"
      "v_cvt_pk_bf16_f32 v213, v50, v51\n"
      "v_mfma_f32_16x16x32_bf16 v[88:91], a[8:11], v[220:223], v[88:91]\n"
      "v_mfma_f32_16x16x32_bf16 v[88:91], a[12:15], v[176:179], v[88:91]\n"
      "v_cvt_pk_bf16_f32 v215, v202, v203\n"
      "v_cvt_pk_bf16_f32 v211, v158, v159\n"
      "v_mfma_f32_16x16x32_bf16 v[100:103], v[34:37], a[112:115], 0\n"
      "v_cvt_pk_bf16_f32 v212, v48, v49\n"
      "v_mfma_f32_16x16x32_bf16 v[100:103], v[128:131], a[116:119], v[100:103]\n"
      "v_mov_b32_dpp v195, v115 row_ror:8 row_mask:0xf bank_mask:0xf\n"
      "v_mfma_f32_16x16x32_bf16 v[100:103], v[220:223], a[120:123], v[100:103]\n"
      "ds_bpermute_b32 v158, v7, v246 offset:248\n"
      "v_mfma_f32_16x16x32_bf16 v[100:103], v[176:179], a[124:127], v[100:103]\n"
      "v_cvt_pk_bf16_f32 v208, v88, v89\n"
      "v_cvt_pk_bf16_f32 v209, v90, v91\n"
      "v_mfma_f32_16x16x32_bf16 v[200:203], a[32:35], v[232:235], 0\n"
      "v_exp_f32 v122, v122\n"
      "v_mfma_f32_16x16x32_bf16 v[200:203], a[36:39], v[252:255], v[200:203]\n"
      "v_cvt_pk_bf16_f32 v210, v156, v157\n"
      "v_mfma_f32_16x16x32_bf16 v[200:203], a[40:43], v[248:251], v[200:203]\n"
      "s_nop 0\n"
      "v_cvt_pk_bf16_f32 v225, v102, v103\n"
      "v_mfma_f32_16x16x32_bf16 v[200:203], a[44:47], v[180:183], v[200:203]\n"
      "v_cvt_pk_bf16_f32 v224, v100, v101\n"
      "v_mov_b64 v[88:89], 0\n"
      "v_mfma_f32_16x16x32_bf16 v[100:103], v[34:37], a[96:99], 0\n"
      "v_exp_f32 v123, v123\n"
      "v_mfma_f32_16x16x32_bf16 v[100:103], v[128:131], a[100:103], v[100:103]\n"
      "ds_bpermute_b32 v159, v7, v246 offset:8\n"
      "v_mfma_f32_16x16x32_bf16 v[100:103], v[220:223], a[104:107], v[100:103]\n"
      "v_mfma_f32_16x16x32_bf16 v[100:103], v[176:179], a[108:111], v[100:103]\n"
      "buffer_store_dwordx2 v[32:33], v2, s[44:47], s100 offen offset:256 nt sc1\n"
      "v_cvt_pk_bf16_f32 v173, v202, v203\n"
      "v_smfmac_f32_16x16x64_bf16 v[88:91], v[116:119], v[208:215], v5\n"
      "v_cvt_pk_bf16_f32 v172, v200, v201\n"
      "v_mfma_f32_16x16x32_bf16 v[48:51], v[232:235], a[112:115], 0\n"
      "v_exp_f32 v138, v138\n"
      "v_mfma_f32_16x16x32_bf16 v[48:51], v[252:255], a[116:119], v[48:51]\n"
      "v_mov_b32_dpp v193, v113 row_ror:8 row_mask:0xf bank_mask:0xf\n"
      "v_mov_b64 v[200:201], 0\n"
      "v_mfma_f32_16x16x32_bf16 v[48:51], v[248:251], a[120:123], v[48:51]\n"
      "v_cvt_pk_bf16_f32 v133, v102, v103\n"
      "v_cvt_pk_bf16_f32 v132, v100, v101\n"
      "v_mfma_f32_16x16x32_bf16 v[48:51], v[180:183], a[124:127], v[48:51]\n"
      "v_exp_f32 v20, v20\n"
      "v_smfmac_f32_16x16x64_bf16 v[200:203], v[116:119], v[168:175], v5\n"
      "v_add_f32 v88, v88, v89\n"
      "v_mfma_f32_16x16x32_bf16 v[100:103], v[34:37], a[80:83], 0\n"
      "v_mov_b64 v[116:117], 0\n"
      "v_mov_b64 v[118:119], 0\n"
      "v_mfma_f32_16x16x32_bf16 v[100:103], v[128:131], a[84:87], v[100:103]\n"
      "s_nop 0\n"
      "v_cvt_pk_bf16_f32 v230, v48, v49\n"
      "v_cvt_pk_bf16_f32 v231, v50, v51\n"
      "v_mfma_f32_16x16x32_bf16 v[100:103], v[220:223], a[88:91], v[100:103]\n"
      "v_exp_f32 v88, v88\n"
      "v_mfma_f32_16x16x32_bf16 v[100:103], v[176:179], a[92:95], v[100:103]\n"
      "v_add_f32 v200, v200, v201\n"
      "v_cndmask_b32_dpp v89, v20, v88, vcc quad_perm:[0,0,2,2] row_mask:0xf bank_mask:0xf\n"
      "v_mfma_f32_16x16x32_bf16 v[196:199], v[34:37], a[64:67], 0\n"
      "v_exp_f32 v200, v200\n"
      "v_mfma_f32_16x16x32_bf16 v[196:199], v[128:131], a[68:71], v[196:199]\n"
      "v_cvt_pk_bf16_f32 v49, v122, v123\n"
      "v_cndmask_b32_dpp v139, v200, v138, vcc quad_perm:[0,0,2,2] row_mask:0xf bank_mask:0xf\n"
      "v_mfma_f32_16x16x32_bf16 v[196:199], v[220:223], a[72:75], v[196:199]\n"
      "L_h6_%=:\n"
      "s_cbranch_execz L_hr_%=\n"
      "v_cvt_pk_bf16_f32 v37, v102, v103\n"
      "v_cvt_pk_bf16_f32 v48, v120, v121\n"
      "v_mfma_f32_16x16x32_bf16 v[196:199], v[176:179], a[76:79], v[196:199]\n"
      "buffer_store_dwordx2 v[216:217], v2, s[44:47], s35 offen offset:256 nt sc1\n"
      "v_cvt_pk_bf16_f32 v36, v100, v101\n"
      "v_mfma_f32_16x16x32_bf16 v[120:123], v[184:187], v[24:27], 0\n"
      "s_not_b64 vcc, vcc\n"
      "v_cndmask_b32_dpp v45, v152, v44, vcc quad_perm:[1,1,3,3] row_mask:0xf bank_mask:0xf\n"
      "v_cndmask_b32_dpp v201, v138, v200, vcc quad_perm:[1,1,3,3] row_mask:0xf bank_mask:0xf\n"
      "v_mfma_f32_16x16x32_bf16 v[120:123], v[188:191], v[28:31], v[120:123]\n"
      "v_cvt_pk_bf16_f32 v153, v153, v45\n"
      "v_cvt_pk_bf16_f32 v140, v196, v197\n"
      "v_mfma_f32_16x16x32_bf16 v[32:35], v[208:211], v[52:55], 0\n"
      "v_cndmask_b32_dpp v21, v88, v20, vcc quad_perm:[1,1,3,3] row_mask:0xf bank_mask:0xf\n"
      "v_add_f32 v88, v88, v138\n"
      "v_mfma_f32_16x16x32_bf16 v[32:35], v[212:215], v[56:59], v[32:35]\n"
      "v_add_f32 v152, v152, v44\n"
      "v_cvt_pk_bf16_f32 v139, v139, v201\n"
      "v_mfma_f32_16x16x32_bf16 v[100:103], v[168:171], v[24:27], 0\n"
      "v_cvt_pk_bf16_f32 v89, v89, v21\n"
      "v_add_f32 v20, v20, v200\n"
      "v_mfma_f32_16x16x32_bf16 v[100:103], v[172:175], v[28:31], v[100:103]\n"
      "v_add_f32 v152, v152, v247\n"
      "v_cvt_pk_bf16_f32 v141, v198, v199\n"
      "v_smfmac_f32_16x16x64_bf16 v[116:119], v[112:115], v[24:31], v5\n"
      "ds_bpermute_b32 v161, v6, v139\n"
      "v_smfmac_f32_16x16x64_bf16 v[116:119], v[192:195], v[52:59], v5\n"
      "ds_bpermute_b32 v19, v7, v153 offset:4\n"
      "v_mfma_f32_16x16x32_bf16 v[200:203], v[208:211], v[24:27], 0\n"
      "v_exp_f32 v122, v122\n"
      "v_mfma_f32_16x16x32_bf16 v[200:203], v[212:215], v[28:31], v[200:203]\n"
      "v_exp_f32 v102, v102\n"
      "v_mfma_f32_16x16x32_bf16 v[176:179], v[232:235], a[64:67], 0\n"
      "v_exp_f32 v101, v101\n"
      "v_mfma_f32_16x16x32_bf16 v[176:179], v[252:255], a[68:71], v[176:179]\n"
      "ds_bpermute_b32 v162, v6, v89 offset:16\n"
      "v_mfma_f32_16x16x32_bf16 v[176:179], v[248:251], a[72:75], v[176:179]\n"
      "v_exp_f32 v120, v120\n"
      "v_mfma_f32_16x16x32_bf16 v[176:179], v[180:183], a[76:79], v[176:179]\n"
      "v_exp_f32 v35, v35\n"
      "v_mfma_f32_16x16x32_bf16 v[244:247], v[168:171], v[52:55], 0\n"
      "v_exp_f32 v200, v200\n"
      "v_mfma_f32_16x16x32_bf16 v[244:247], v[172:175], v[56:59], v[244:247]\n"
      "v_exp_f32 v203, v203\n"
      "v_mfma_f32_16x16x32_bf16 v[24:27], v[236:239], v[52:55], 0\n"
      "v_exp_f32 v100, v100\n"
      "v_mfma_f32_16x16x32_bf16 v[24:27], v[240:243], v[56:59], v[24:27]\n"
      "v_exp_f32 v202, v202\n"
      "v_add_f32 v88, v88, v20\n"
      "v_mfma_f32_16x16x32_bf16 v[28:31], v[104:107], v[52:55], 0\n"
      "v_exp_f32 v121, v121\n"
      "v_mfma_f32_16x16x32_bf16 v[28:31], v[108:111], v[56:59], v[28:31]\n"
      "v_exp_f32 v244, v244\n"
      "v_mfma_f32_16x16x32_bf16 v[196:199], a[128:131], v[232:235], 0\n"
      "s_nop 0\n"
      "v_exp_f32 v24, v24\n"
      "v_mfma_f32_16x16x32_bf16 v[196:199], a[132:135], v[252:255], v[196:199]\n"
      "v_exp_f32 v27, v27\n"
      "v_mfma_f32_16x16x32_bf16 v[196:199], a[136:139], v[248:251], v[196:199]\n"
      "s_nop 0\n"
      "v_exp_f32 v30, v30\n"
      "v_mfma_f32_16x16x32_bf16 v[196:199], a[140:143], v[180:183], v[196:199]\n"
      "v_exp_f32 v201, v201\n"
      "v_mfma_f32_16x16x32_bf16 v[128:131], v[72:75], a[96:99], 0\n"
      "v_cvt_pk_bf16_f32 v147, v178, v179\n"
      "v_cvt_pk_bf16_f32 v50, v100, v101\n"
      "v_mfma_f32_16x16x32_bf16 v[128:131], v[148:151], a[100:103], v[128:131]\n"
      "v_cvt_pk_bf16_f32 v44, v200, v201\n"
      "v_cvt_pk_bf16_f32 v46, v120, v121\n"
      "v_mfma_f32_16x16x32_bf16 v[128:131], v[164:167], a[104:107], v[128:131]\n"
      "ds_bpermute_b32 v163, v6, v139 offset:16\n"
      "v_mfma_f32_16x16x32_bf16 v[128:131], v[204:207], a[108:111], v[128:131]\n"
      "buffer_store_dwordx2 v[218:219], v2, s[44:47], s35 offen offset:768 nt sc1\n"
      "v_exp_f32 v25, v25\n"
      "v_mfma_f32_16x16x32_bf16 v[20:23], v[184:187], v[52:55], 0\n"
      "v_exp_f32 v28, v28\n"
      "v_mfma_f32_16x16x32_bf16 v[20:23], v[188:191], v[56:59], v[20:23]\n"
      "v_exp_f32 v123, v123\n"
      "v_mfma_f32_16x16x32_bf16 v[220:223], v[64:67], v[52:55], 0\n"
      "v_add_f32 v118, v118, v119\n"
      "v_add_f32 v116, v116, v117\n"
      "v_mfma_f32_16x16x32_bf16 v[220:223], v[68:71], v[56:59], v[220:223]\n"
      "v_cvt_pk_bf16_f32 v146, v176, v177\n"
      "v_cvt_pk_bf16_f32 v45, v202, v203\n"
      "v_cvt_pk_bf16_f32 v47, v122, v123\n"
      "v_mfma_f32_16x16x32_bf16 v[52:55], v[92:95], a[96:99], 0\n"
      "v_exp_f32 v21, v21\n"
      "v_mfma_f32_16x16x32_bf16 v[52:55], v[76:79], a[100:103], v[52:55]\n"
      "ds_bpermute_b32 v160, v6, v89\n"
      "v_mfma_f32_16x16x32_bf16 v[52:55], v[84:87], a[104:107], v[52:55]\n"
      "v_exp_f32 v116, v116\n"
      "v_mfma_f32_16x16x32_bf16 v[52:55], v[80:83], a[108:111], v[52:55]\n"
      "v_exp_f32 v31, v31\n"
      "v_mfma_f32_16x16x32_bf16 v[154:157], a[176:179], v[232:235], 0\n"
      "v_exp_f32 v247, v247\n"
      "v_mfma_f32_16x16x32_bf16 v[154:157], a[180:183], v[252:255], v[154:157]\n"
      "v_exp_f32 v246, v246\n"
      "v_mfma_f32_16x16x32_bf16 v[154:157], a[184:187], v[248:251], v[154:157]\n"
      "v_exp_f32 v245, v245\n"
      "v_mfma_f32_16x16x32_bf16 v[154:157], a[188:191], v[180:183], v[154:157]\n"
      "v_exp_f32 v222, v222\n"
      "v_mfma_f32_16x16x32_bf16 v[176:179], v[92:95], a[80:83], 0\n"
      "v_exp_f32 v220, v220\n"
      "v_mfma_f32_16x16x32_bf16 v[176:179], v[76:79], a[84:87], v[176:179]\n"
      "v_cvt_pk_bf16_f32 v122, v244, v245\n"
      "v_cvt_pk_bf16_f32 v216, v196, v197\n"
      "v_mfma_f32_16x16x32_bf16 v[176:179], v[84:87], a[88:91], v[176:179]\n"
      "v_exp_f32 v103, v103\n"
      "v_mfma_f32_16x16x32_bf16 v[176:179], v[80:83], a[92:95], v[176:179]\n"
      "v_cvt_pk_bf16_f32 v51, v102, v103\n"
      "v_add_f32 v200, v88, v152\n"
      "v_mfma_f32_16x16x32_bf16 v[100:103], v[232:235], a[96:99], 0\n"
      "v_exp_f32 v118, v118\n"
      "v_mfma_f32_16x16x32_bf16 v[100:103], v[252:255], a[100:103], v[100:103]\n"
      "v_exp_f32 v26, v26\n"
      "v_mfma_f32_16x16x32_bf16 v[100:103], v[248:251], a[104:107], v[100:103]\n"
      "v_cvt_pk_bf16_f32 v123, v246, v247\n"
      "v_cvt_pk_bf16_f32 v246, v176, v177\n"
      "v_mfma_f32_16x16x32_bf16 v[100:103], v[180:183], a[108:111], v[100:103]\n"
      "v_cvt_pk_bf16_f32 v247, v178, v179\n"
      "v_cvt_pk_bf16_f32 v217, v198, v199\n"
      "v_mfma_f32_16x16x32_bf16 v[176:179], v[72:75], a[64:67], 0\n"
      "ds_bpermute_b32 v18, v7, v153\n"
      "v_mfma_f32_16x16x32_bf16 v[176:179], v[148:151], a[68:71], v[176:179]\n"
      "v_exp_f32 v34, v34\n"
      "v_mfma_f32_16x16x32_bf16 v[176:179], v[164:167], a[72:75], v[176:179]\n"
      "v_exp_f32 v22, v22\n"
      "v_mfma_f32_16x16x32_bf16 v[176:179], v[204:207], a[76:79], v[176:179]\n"
      "v_cvt_pk_bf16_f32 v138, v100, v101\n"
      "v_cvt_pk_bf16_f32 v60, v128, v129\n"
      "v_mfma_f32_16x16x32_bf16 v[196:199], v[72:75], a[80:83], 0\n"
      "v_cvt_pk_bf16_f32 v63, v54, v55\n"
      "v_cvt_pk_bf16_f32 v62, v52, v53\n"
      "v_mfma_f32_16x16x32_bf16 v[196:199], v[148:151], a[84:87], v[196:199]\n"
      "v_add_f32 v101, v116, v118\n"
      "v_cvt_pk_bf16_f32 v139, v102, v103\n"
      "v_mfma_f32_16x16x32_bf16 v[196:199], v[164:167], a[88:91], v[196:199]\n"
      "v_exp_f32 v32, v32\n"
      "v_mfma_f32_16x16x32_bf16 v[196:199], v[204:207], a[92:95], v[196:199]\n"
      "v_cvt_pk_bf16_f32 v61, v130, v131\n"
      "v_exp_f32 v223, v223\n"
      "v_mfma_f32_16x16x32_bf16 v[52:55], v[92:95], a[64:67], 0\n"
      "v_cvt_pk_bf16_f32 v89, v178, v179\n"
      "v_add_f32 v100, v116, v118\n"
      "v_mfma_f32_16x16x32_bf16 v[52:55], v[76:79], a[68:71], v[52:55]\n"
      "v_cvt_pk_bf16_f32 v203, v116, v118\n"
      "v_cvt_pk_bf16_f32 v88, v176, v177\n"
      "v_mfma_f32_16x16x32_bf16 v[52:55], v[84:87], a[72:75], v[52:55]\n"
      "v_cvt_pk_bf16_f32 v202, v116, v118\n"
      "v_cvt_pk_bf16_f32 v128, v24, v25\n"
      "v_mfma_f32_16x16x32_bf16 v[52:55], v[80:83], a[76:79], v[52:55]\n"
      "v_exp_f32 v23, v23\n"
      "v_mfma_f32_16x16x32_bf16 v[116:119], a[160:163], v[232:235], 0\n"
      "v_exp_f32 v221, v221\n"
      "v_mfma_f32_16x16x32_bf16 v[116:119], a[164:167], v[252:255], v[116:119]\n"
      "v_cvt_pk_bf16_f32 v131, v222, v223\n"
      "v_cvt_pk_bf16_f32 v130, v220, v221\n"
      "v_mfma_f32_16x16x32_bf16 v[116:119], a[168:171], v[248:251], v[116:119]\n"
      "v_cvt_pk_bf16_f32 v244, v196, v197\n"
      "v_cvt_pk_bf16_f32 v223, v156, v157\n"
      "v_mfma_f32_16x16x32_bf16 v[116:119], a[172:175], v[180:183], v[116:119]\n"
      "v_cvt_pk_bf16_f32 v91, v54, v55\n"
      "v_cvt_pk_bf16_f32 v222, v154, v155\n"
      "v_mfma_f32_16x16x32_bf16 v[176:179], a[144:147], v[232:235], 0\n"
      "v_cvt_pk_bf16_f32 v121, v30, v31\n"
      "v_cvt_pk_bf16_f32 v90, v52, v53\n"
      "v_mfma_f32_16x16x32_bf16 v[176:179], a[148:151], v[252:255], v[176:179]\n"
      "v_exp_f32 v33, v33\n"
      "v_mfma_f32_16x16x32_bf16 v[176:179], a[152:155], v[248:251], v[176:179]\n"
      "v_cvt_pk_bf16_f32 v245, v198, v199\n"
      "v_cvt_pk_bf16_f32 v220, v116, v117\n"
      "v_mfma_f32_16x16x32_bf16 v[176:179], a[156:159], v[180:183], v[176:179]\n"
      "ds_read_b64 v[156:157], v11 offset:0\n"
      "v_cvt_pk_bf16_f32 v221, v118, v119\n"
      "v_mfma_f32_16x16x32_bf16 v[52:55], v[72:75], a[112:115], 0\n"
      "v_exp_f32 v20, v20\n"
      "v_mfma_f32_16x16x32_bf16 v[52:55], v[148:151], a[116:119], v[52:55]\n"
      "v_permlane16_swap_b32 v100, v101\n"
      "v_mfma_f32_16x16x32_bf16 v[52:55], v[164:167], a[120:123], v[52:55]\n"
      "v_cvt_pk_bf16_f32 v118, v20, v21\n"
      "v_cvt_pk_bf16_f32 v119, v22, v23\n"
      "v_mfma_f32_16x16x32_bf16 v[52:55], v[204:207], a[124:127], v[52:55]\n"
      "v_permlane16_swap_b32 v202, v203\n"
      "v_mfma_f32_16x16x32_bf16 v[20:23], v[92:95], a[112:115], 0\n"
      "v_cvt_pk_bf16_f32 v116, v32, v33\n"
      "v_cvt_pk_bf16_f32 v117, v34, v35\n"
      "v_mfma_f32_16x16x32_bf16 v[20:23], v[76:79], a[116:119], v[20:23]\n"
      "v_exp_f32 v29, v29\n"
      "v_mfma_f32_16x16x32_bf16 v[20:23], v[84:87], a[120:123], v[20:23]\n"
      "v_cvt_pk_bf16_f32 v219, v178, v179\n"
      "v_cvt_pk_bf16_f32 v218, v176, v177\n"
      "v_mfma_f32_16x16x32_bf16 v[20:23], v[80:83], a[124:127], v[20:23]\n"
      "v_cvt_pk_bf16_f32 v196, v52, v53\n"
      "v_cvt_pk_bf16_f32 v120, v28, v29\n"
      "v_mov_b64 v[152:153], v[100:101]\n"
      "v_smfmac_f32_16x16x64_bf16 v[100:103], v[12:15], v[44:51], v5\n"
      "v_cvt_pk_bf16_f32 v129, v26, v27\n"
      "v_smfmac_f32_16x16x64_bf16 v[152:155], v[12:15], v[116:123], v5\n"
      "s_nop 1\n"
      "v_cvt_pk_bf16_f32 v199, v22, v23\n"
      "v_cvt_pk_bf16_f32 v198, v20, v21\n"
      "v_smfmac_f32_16x16x32_bf16 v[100:103], v[12:13], v[124:127], v5\n"
      "v_cvt_pk_bf16_f32 v197, v54, v55\n"
      "s_waitcnt lgkmcnt(0)\n"
      "v_mfma_f32_16x16x4_4b_bf16 v[20:35], v[202:203], v[156:157], 0 cbsz:2 abid:0\n"
      "s_nop 4\n"
      "v_add_f32 v100, v100, v101\n"
      "v_add_f32_dpp v200, v200, v200 row_ror:8 row_mask:0xf bank_mask:0xf\n"
      "v_smfmac_f32_16x16x32_bf16 v[152:155], v[12:13], v[128:131], v5\n"
      "s_nop 1\n"
      "v_mfma_f32_16x16x32_bf16 v[20:23], v[44:47], v[140:143], v[20:23]\n"
      "v_rcp_f32 v101, v100\n"
      "v_mfma_f32_16x16x32_bf16 v[20:23], v[48:51], v[144:147], v[20:23]\n"
      "ds_bpermute_b32 v102, v4, v101\n"
      "v_mfma_f32_16x16x32_bf16 v[20:23], v[124:127], v[88:91], v[20:23]\n"
      "v_mfma_f32_16x16x32_bf16 v[28:31], v[44:47], v[132:135], v[28:31]\n"
      "v_mfma_f32_16x16x32_bf16 v[28:31], v[48:51], v[136:139], v[28:31]\n"
      "v_mfma_f32_16x16x32_bf16 v[28:31], v[124:127], v[60:63], v[28:31]\n"
      "v_mfma_f32_16x16x32_bf16 v[24:27], v[44:47], v[36:39], v[24:27]\n"
      "v_add_f32_dpp v200, v200, v200 row_ror:4 row_mask:0xf bank_mask:0xf\n"
      "v_add_f32 v152, v152, v153\n"
      "v_mfma_f32_16x16x32_bf16 v[24:27], v[48:51], v[40:43], v[24:27]\n"
      "v_mfma_f32_16x16x32_bf16 v[24:27], v[124:127], v[244:247], v[24:27]\n"
      "v_rcp_f32 v153, v152\n"
      "v_mfma_f32_16x16x32_bf16 v[32:35], v[44:47], v[224:227], v[32:35]\n"
      "v_mfma_f32_16x16x32_bf16 v[32:35], v[48:51], v[228:231], v[32:35]\n"
      "s_waitcnt lgkmcnt(0)\n"
      "v_mul_f32_dpp v23, v102, v23 row_newbcast:12 row_mask:0xf bank_mask:0xf\n"
      "v_mfma_f32_16x16x32_bf16 v[32:35], v[124:127], v[196:199], v[32:35]\n"
      "s_nop 1\n"
      "v_mul_f32_dpp v24, v102, v24 row_newbcast:0 row_mask:0xf bank_mask:0xf\n"
      "v_mfma_f32_16x16x32_bf16 v[176:179], v[64:67], v[216:219], 0\n"
      "v_mul_f32_dpp v27, v102, v27 row_newbcast:12 row_mask:0xf bank_mask:0xf\n"
      "v_mfma_f32_16x16x32_bf16 v[176:179], v[68:71], v[220:223], v[176:179]\n"
      "v_mul_f32_dpp v21, v102, v21 row_newbcast:4 row_mask:0xf bank_mask:0xf\n"
      "v_mfma_f32_16x16x32_bf16 v[124:127], v[236:239], v[216:219], 0\n"
      "v_mfma_f32_16x16x32_bf16 v[124:127], v[240:243], v[220:223], v[124:127]\n"
      "v_mul_f32_dpp v30, v102, v30 row_newbcast:8 row_mask:0xf bank_mask:0xf\n"
      "v_mul_f32_dpp v25, v102, v25 row_newbcast:4 row_mask:0xf bank_mask:0xf\n"
      "v_mfma_f32_16x16x32_bf16 v[96:99], v[104:107], v[216:219], 0\n"
      "v_mul_f32_dpp v28, v102, v28 row_newbcast:0 row_mask:0xf bank_mask:0xf\n"
      "v_mul_f32_dpp v26, v102, v26 row_newbcast:8 row_mask:0xf bank_mask:0xf\n"
      "v_mfma_f32_16x16x32_bf16 v[96:99], v[108:111], v[220:223], v[96:99]\n"
      "v_mul_f32_dpp v32, v102, v32 row_newbcast:0 row_mask:0xf bank_mask:0xf\n"
      "v_mul_f32_dpp v22, v102, v22 row_newbcast:8 row_mask:0xf bank_mask:0xf\n"
      "v_mfma_f32_16x16x32_bf16 v[24:27], v[92:95], a[208:211], v[24:27]\n"
      "v_exp_f32 v126, v126\n"
      "v_mfma_f32_16x16x32_bf16 v[24:27], v[76:79], a[212:215], v[24:27]\n"
      "v_mul_f32_dpp v35, v102, v35 row_newbcast:12 row_mask:0xf bank_mask:0xf\n"
      "v_mul_f32_dpp v31, v102, v31 row_newbcast:12 row_mask:0xf bank_mask:0xf\n"
      "v_mfma_f32_16x16x32_bf16 v[24:27], v[84:87], a[216:219], v[24:27]\n"
      "v_mul_f32_dpp v29, v102, v29 row_newbcast:4 row_mask:0xf bank_mask:0xf\n"
      "v_mfma_f32_16x16x32_bf16 v[24:27], v[80:83], a[220:223], v[24:27]\n"
      "v_add_f32_dpp v200, v200, v200 row_ror:2 row_mask:0xf bank_mask:0xf\n"
      "v_mul_f32_dpp v33, v102, v33 row_newbcast:4 row_mask:0xf bank_mask:0xf\n"
      "v_mfma_f32_16x16x32_bf16 v[28:31], v[92:95], a[224:227], v[28:31]\n"
      "v_exp_f32 v179, v179\n"
      "v_mfma_f32_16x16x32_bf16 v[28:31], v[76:79], a[228:231], v[28:31]\n"
      "v_mul_f32_dpp v20, v102, v20 row_newbcast:0 row_mask:0xf bank_mask:0xf\n"
      "v_mul_f32_dpp v34, v102, v34 row_newbcast:8 row_mask:0xf bank_mask:0xf\n"
      "v_mfma_f32_16x16x32_bf16 v[28:31], v[84:87], a[232:235], v[28:31]\n"
      "v_exp_f32 v125, v125\n"
      "v_mfma_f32_16x16x32_bf16 v[28:31], v[80:83], a[236:239], v[28:31]\n"
      "v_add_f32_dpp v200, v200, v200 row_ror:1 row_mask:0xf bank_mask:0xf\n"
      "v_mfma_f32_16x16x32_bf16 v[20:23], v[92:95], a[192:195], v[20:23]\n"
      "v_rcp_f32 v201, v200\n"
      "v_mfma_f32_16x16x32_bf16 v[20:23], v[76:79], a[196:199], v[20:23]\n"
      "v_exp_f32 v124, v124\n"
      "v_mfma_f32_16x16x32_bf16 v[20:23], v[84:87], a[200:203], v[20:23]\n"
      "v_exp_f32 v127, v127\n"
      "v_mfma_f32_16x16x32_bf16 v[20:23], v[80:83], a[204:207], v[20:23]\n"
      "v_exp_f32 v97, v97\n"
      "v_mfma_f32_16x16x32_bf16 v[32:35], v[92:95], a[240:243], v[32:35]\n"
      "v_exp_f32 v178, v178\n"
      "v_mfma_f32_16x16x32_bf16 v[32:35], v[76:79], a[244:247], v[32:35]\n"
      "v_maximum3_f32 v8, v8, v200, v201\n"
      "v_mfma_f32_16x16x32_bf16 v[32:35], v[84:87], a[248:251], v[32:35]\n"
      "v_exp_f32 v99, v99\n"
      "v_mfma_f32_16x16x32_bf16 v[32:35], v[80:83], a[252:255], v[32:35]\n"
      "v_exp_f32 v176, v176\n"
      "v_mfma_f32_16x16x4_4b_bf16 v[44:59], v[202:203], v[156:157], 0 cbsz:2 abid:2\n"
      "ds_bpermute_b32 v154, v4, v153 offset:128\n"
      "v_cvt_pk_bf16_f32 v95, v178, v179\n"
      "v_maximum3_f32 v8, v8, v100, v101\n"
      "v_exp_f32 v177, v177\n"
      "v_cvt_pk_bf16_f32 v22, v22, v26\n"
      "v_mfma_f32_16x16x32_bf16 v[100:103], v[184:187], v[216:219], 0\n"
      "v_exp_f32 v98, v98\n"
      "v_mfma_f32_16x16x32_bf16 v[100:103], v[188:191], v[220:223], v[100:103]\n"
      "v_cvt_pk_bf16_f32 v20, v20, v24\n"
      "v_cvt_pk_bf16_f32 v24, v21, v25\n"
      "v_mfma_f32_16x16x32_bf16 v[48:51], v[116:119], v[36:39], v[48:51]\n"
      "v_cvt_pk_bf16_f32 v21, v28, v32\n"
      "v_cvt_pk_bf16_f32 v93, v126, v127\n"
      "v_mfma_f32_16x16x32_bf16 v[48:51], v[120:123], v[40:43], v[48:51]\n"
      "v_cvt_pk_bf16_f32 v26, v23, v27\n"
      "v_cvt_pk_bf16_f32 v27, v31, v35\n"
      "v_mfma_f32_16x16x32_bf16 v[48:51], v[128:131], v[244:247], v[48:51]\n"
      "v_pk_max_i16 v27, v27, 0\n"
      "v_cvt_pk_bf16_f32 v25, v29, v33\n"
      "v_mfma_f32_16x16x32_bf16 v[56:59], v[116:119], v[224:227], v[56:59]\n"
      "v_maximum3_f32 v8, v8, v152, v153\n"
      "v_mfma_f32_16x16x32_bf16 v[56:59], v[120:123], v[228:231], v[56:59]\n"
      "v_exp_f32 v102, v102\n"
      "v_mfma_f32_16x16x32_bf16 v[56:59], v[128:131], v[196:199], v[56:59]\n"
      "v_exp_f32 v100, v100\n"
      "v_mfma_f32_16x16x32_bf16 v[44:47], v[116:119], v[140:143], v[44:47]\n"
      "v_cvt_pk_bf16_f32 v94, v176, v177\n"
      "v_mfma_f32_16x16x32_bf16 v[44:47], v[120:123], v[144:147], v[44:47]\n"
      "s_waitcnt lgkmcnt(0)\n"
      "v_mul_f32_dpp v48, v154, v48 row_newbcast:0 row_mask:0xf bank_mask:0xf\n"
      "v_cvt_pk_bf16_f32 v92, v124, v125\n"
      "v_mfma_f32_16x16x32_bf16 v[44:47], v[128:131], v[88:91], v[44:47]\n"
      "ds_read_b128 v[80:83], v0 offset:8704\n"
      "v_mul_f32_dpp v59, v154, v59 row_newbcast:12 row_mask:0xf bank_mask:0xf\n"
      "v_mfma_f32_16x16x32_bf16 v[52:55], v[116:119], v[132:135], v[52:55]\n"
      "v_mfma_f32_16x16x32_bf16 v[52:55], v[120:123], v[136:139], v[52:55]\n"
      "v_cvt_pk_bf16_f32 v23, v30, v34\n"
      "v_pk_max_i16 v23, v23, 0\n"
      "v_mfma_f32_16x16x32_bf16 v[52:55], v[128:131], v[60:63], v[52:55]\n"
      "ds_read_b64 v[202:203], v11 offset:4096\n"
      "v_mul_f32_dpp v46, v154, v46 row_newbcast:8 row_mask:0xf bank_mask:0xf\n"
      "v_mfma_f32_16x16x32_bf16 v[28:31], v[168:171], v[216:219], 0\n"
      "v_mul_f32_dpp v49, v154, v49 row_newbcast:4 row_mask:0xf bank_mask:0xf\n"
      "v_mfma_f32_16x16x32_bf16 v[28:31], v[172:175], v[220:223], v[28:31]\n"
      "v_mul_f32_dpp v50, v154, v50 row_newbcast:8 row_mask:0xf bank_mask:0xf\n"
      "v_mul_f32_dpp v57, v154, v57 row_newbcast:4 row_mask:0xf bank_mask:0xf\n"
      "v_mfma_f32_16x16x4_4b_bf16 v[116:131], v[158:159], v[156:157], 0 cbsz:2 abid:0\n"
      "v_mul_f32_dpp v53, v154, v53 row_newbcast:4 row_mask:0xf bank_mask:0xf\n"
      "v_pk_max_i16 v22, v22, 0\n"
      "v_exp_f32 v96, v96\n"
      "v_mul_f32_dpp v45, v154, v45 row_newbcast:4 row_mask:0xf bank_mask:0xf\n"
      "v_mul_f32_dpp v55, v154, v55 row_newbcast:12 row_mask:0xf bank_mask:0xf\n"
      "v_mul_f32_dpp v44, v154, v44 row_newbcast:0 row_mask:0xf bank_mask:0xf\n"
      "v_pk_max_i16 v21, v21, 0\n"
      "v_mfma_f32_16x16x32_bf16 v[32:35], v[208:211], v[216:219], 0\n"
      "v_mul_f32_dpp v51, v154, v51 row_newbcast:12 row_mask:0xf bank_mask:0xf\n"
      "v_mul_f32_dpp v47, v154, v47 row_newbcast:12 row_mask:0xf bank_mask:0xf\n"
      "v_mfma_f32_16x16x32_bf16 v[32:35], v[212:215], v[220:223], v[32:35]\n"
      "v_mul_f32_dpp v54, v154, v54 row_newbcast:8 row_mask:0xf bank_mask:0xf\n"
      "v_mul_f32_dpp v58, v154, v58 row_newbcast:8 row_mask:0xf bank_mask:0xf\n"
      "v_smfmac_f32_16x16x64_bf16 v[116:119], v[160:163], v[140:147], v5\n"
      "v_mul_f32_dpp v52, v154, v52 row_newbcast:0 row_mask:0xf bank_mask:0xf\n"
      "v_mfma_f32_16x16x32_bf16 v[44:47], v[72:75], a[192:195], v[44:47]\n"
      "v_pk_max_i16 v24, v24, 0\n"
      "v_pk_max_i16 v25, v25, 0\n"
      "v_mfma_f32_16x16x32_bf16 v[44:47], v[148:151], a[196:199], v[44:47]\n"
      "v_exp_f32 v103, v103\n"
      "v_mfma_f32_16x16x32_bf16 v[44:47], v[164:167], a[200:203], v[44:47]\n"
      "v_mul_f32_dpp v56, v154, v56 row_newbcast:0 row_mask:0xf bank_mask:0xf\n"
      "v_pk_max_i16 v26, v26, 0\n"
      "v_mfma_f32_16x16x32_bf16 v[44:47], v[204:207], a[204:207], v[44:47]\n"
      "v_exp_f32 v29, v29\n"
      "v_mfma_f32_16x16x32_bf16 v[48:51], v[72:75], a[208:211], v[48:51]\n"
      "v_mov_b64 v[178:179], 0\n"
      "v_mfma_f32_16x16x32_bf16 v[48:51], v[148:151], a[212:215], v[48:51]\n"
      "v_exp_f32 v34, v34\n"
      "v_mfma_f32_16x16x32_bf16 v[48:51], v[164:167], a[216:219], v[48:51]\n"
      "v_mov_b64 v[176:177], 0\n"
      "v_mfma_f32_16x16x32_bf16 v[48:51], v[204:207], a[220:223], v[48:51]\n"
      "v_exp_f32 v28, v28\n"
      "v_smfmac_f32_16x16x64_bf16 v[176:179], v[112:115], v[216:223], v5\n"
      "buffer_store_dwordx2 v[26:27], v2, s[44:47], s73 offen offset:768 nt sc1\n"
      "ds_read_b128 v[84:87], v0 offset:9216\n"
      "v_smfmac_f32_16x16x64_bf16 v[120:123], v[160:163], v[36:43], v5\n"
      "buffer_store_dwordx2 v[24:25], v2, s[44:47], s73 offen offset:256 nt sc1\n"
      "ds_read_b128 v[76:79], v0 offset:9728\n"
      "v_smfmac_f32_16x16x64_bf16 v[128:131], v[160:163], v[224:231], v5\n"
      "ds_read_b128 v[152:155], v0 offset:8192\n"
      "v_smfmac_f32_16x16x64_bf16 v[124:127], v[160:163], v[132:139], v5\n"
      "v_cvt_pk_bf16_f32 v44, v44, v48\n"
      "v_mfma_f32_16x16x32_bf16 v[52:55], v[72:75], a[224:227], v[52:55]\n"
      "v_cvt_pk_bf16_f32 v48, v45, v49\n"
      "v_cvt_pk_bf16_f32 v219, v102, v103\n"
      "v_mfma_f32_16x16x32_bf16 v[52:55], v[148:151], a[228:231], v[52:55]\n"
      "v_exp_f32 v33, v33\n"
      "v_mfma_f32_16x16x32_bf16 v[52:55], v[164:167], a[232:235], v[52:55]\n"
      "v_cvt_pk_bf16_f32 v46, v46, v50\n"
      "v_cvt_pk_bf16_f32 v50, v47, v51\n"
      "v_mfma_f32_16x16x32_bf16 v[52:55], v[204:207], a[236:239], v[52:55]\n"
      "v_pk_max_i16 v20, v20, 0\n"
      "v_mfma_f32_16x16x32_bf16 v[56:59], v[72:75], a[240:243], v[56:59]\n"
      "v_exp_f32 v101, v101\n"
      "v_mfma_f32_16x16x32_bf16 v[56:59], v[148:151], a[244:247], v[56:59]\n"
      "v_exp_f32 v35, v35\n"
      "v_mfma_f32_16x16x32_bf16 v[56:59], v[164:167], a[248:251], v[56:59]\n"
      "v_exp_f32 v32, v32\n"
      "v_mfma_f32_16x16x32_bf16 v[56:59], v[204:207], a[252:255], v[56:59]\n"
      "v_pk_max_i16 v46, v46, 0\n"
      "v_pk_max_i16 v48, v48, 0\n"
      "v_smfmac_f32_16x16x32_bf16 v[128:131], v[18:19], v[196:199], v5\n"
      "buffer_store_dwordx2 v[22:23], v2, s[44:47], s73 offen offset:512 nt sc1\n"
      "s_waitcnt lgkmcnt(0)\n"
      "v_mfma_f32_16x16x32_bf16 v[164:167], a[144:147], v[152:155], 0\n"
      "v_mfma_f32_16x16x32_bf16 v[164:167], a[148:151], v[80:83], v[164:167]\n"
      "v_cvt_pk_bf16_f32 v218, v100, v101\n"
      "v_cvt_pk_bf16_f32 v47, v54, v58\n"
      "v_mfma_f32_16x16x32_bf16 v[164:167], a[152:155], v[84:87], v[164:167]\n"
      "v_cvt_pk_bf16_f32 v49, v53, v57\n"
      "v_pk_max_i16 v49, v49, 0\n"
      "v_mfma_f32_16x16x32_bf16 v[164:167], a[156:159], v[76:79], v[164:167]\n"
      "v_pk_max_i16 v44, v44, 0\n"
      "v_cvt_pk_bf16_f32 v51, v55, v59\n"
      "v_mfma_f32_16x16x32_bf16 v[204:207], a[128:131], v[152:155], 0\n"
      "v_exp_f32 v31, v31\n"
      "v_mfma_f32_16x16x32_bf16 v[204:207], a[132:135], v[80:83], v[204:207]\n"
      "v_pk_max_i16 v51, v51, 0\n"
      "v_and_b32 v75, 0xffff0000, v203\n"
      "v_mfma_f32_16x16x32_bf16 v[204:207], a[136:139], v[84:87], v[204:207]\n"
      "v_cvt_pk_bf16_f32 v222, v28, v29\n"
      "v_pk_max_i16 v50, v50, 0\n"
      "v_mfma_f32_16x16x32_bf16 v[204:207], a[140:143], v[76:79], v[204:207]\n"
      "buffer_store_dwordx2 v[50:51], v2, s[44:47], s75 offen offset:768 nt sc1\n"
      "v_cvt_pk_bf16_f32 v221, v98, v99\n"
      "v_mfma_f32_16x16x32_bf16 v[148:151], a[160:163], v[152:155], 0\n"
      "v_lshlrev_b32 v74, 16, v203\n"
      "v_and_b32 v73, 0xffff0000, v202\n"
      "v_mfma_f32_16x16x32_bf16 v[148:151], a[164:167], v[80:83], v[148:151]\n"
      "v_exp_f32 v30, v30\n"
      "v_mfma_f32_16x16x32_bf16 v[148:151], a[168:171], v[84:87], v[148:151]\n"
      "v_cvt_pk_bf16_f32 v220, v96, v97\n"
      "v_cvt_pk_bf16_f32 v45, v52, v56\n"
      "v_mfma_f32_16x16x32_bf16 v[148:151], a[172:175], v[76:79], v[148:151]\n"
      "v_lshlrev_b32 v72, 16, v202\n"
      "v_cvt_pk_bf16_f32 v217, v34, v35\n"
      "v_mfma_f32_16x16x32_bf16 v[96:99], a[176:179], v[152:155], 0\n"
      "v_add_f32 v128, v128, v129\n"
      "v_cvt_pk_bf16_f32 v223, v30, v31\n"
      "v_mfma_f32_16x16x32_bf16 v[96:99], a[180:183], v[80:83], v[96:99]\n"
      "v_cvt_pk_bf16_f32 v24, v204, v205\n"
      "v_cvt_pk_bf16_f32 v26, v164, v165\n"
      "v_mfma_f32_16x16x32_bf16 v[96:99], a[184:187], v[84:87], v[96:99]\n"
      "v_cvt_pk_bf16_f32 v25, v206, v207\n"
      "v_cvt_pk_bf16_f32 v27, v166, v167\n"
      "v_mfma_f32_16x16x32_bf16 v[96:99], a[188:191], v[76:79], v[96:99]\n"
      "v_cvt_pk_bf16_f32 v28, v148, v149\n"
      "v_fma_f32 v128, v128, v201, v75\n"
      "v_smfmac_f32_16x16x32_bf16 v[116:119], v[18:19], v[88:91], v5\n"
      "ds_read_b128 v[164:167], v0 offset:4608\n"
      "v_cvt_pk_bf16_f32 v216, v32, v33\n"
      "v_smfmac_f32_16x16x32_bf16 v[124:127], v[18:19], v[60:63], v5\n"
      "ds_read_b128 v[204:207], v0 offset:5120\n"
      "v_pk_max_i16 v47, v47, 0\n"
      "v_smfmac_f32_16x16x32_bf16 v[120:123], v[18:19], v[244:247], v5\n"
      "v_cvt_pk_bf16_f32 v29, v150, v151\n"
      "v_cvt_pk_bf16_f32 v31, v98, v99\n"
      "v_cvt_pk_bf16_f32 v30, v96, v97\n"
      "v_mfma_f32_16x16x32_bf16 v[52:55], v[64:67], v[24:27], 0\n"
      "s_nop 0\n"
      "v_add_f32 v124, v124, v125\n"
      "v_add_f32 v116, v116, v117\n"
      "v_mfma_f32_16x16x32_bf16 v[52:55], v[68:71], v[28:31], v[52:55]\n"
      "v_add_f32 v120, v120, v121\n"
      "v_fma_f32 v124, v124, v201, v74\n"
      "v_smfmac_f32_16x16x64_bf16 v[176:179], v[192:195], v[24:31], v5\n"
      "buffer_store_dwordx2 v[20:21], v2, s[44:47], s73 offen offset:0 nt sc1\n"
      "v_fma_f32 v120, v120, v201, v73\n"
      "v_mfma_f32_16x16x32_bf16 v[96:99], v[236:239], v[24:27], 0\n"
      "ds_read_b128 v[148:151], v0 offset:5632\n"
      "ds_read_b128 v[158:161], v0 offset:4096\n"
      "v_mfma_f32_16x16x32_bf16 v[96:99], v[240:243], v[28:31], v[96:99]\n"
      "buffer_store_dwordx2 v[48:49], v2, s[44:47], s75 offen offset:256 nt sc1\n"
      "v_fma_f32 v116, v116, v201, v72\n"
      "v_mfma_f32_16x16x32_bf16 v[56:59], v[208:211], v[24:27], 0\n"
      "v_add_f32 v178, v178, v179\n"
      "v_cvt_pk_bf16_f32 v117, v124, v128\n"
      "v_mfma_f32_16x16x32_bf16 v[56:59], v[212:215], v[28:31], v[56:59]\n"
      "v_exp_f32 v178, v178\n"
      "v_mfma_f32_16x16x32_bf16 v[124:127], v[168:171], v[24:27], 0\n"
      "v_cvt_pk_bf16_f32 v116, v116, v120\n"
      "v_add_f32 v176, v176, v177\n"
      "v_mfma_f32_16x16x32_bf16 v[124:127], v[172:175], v[28:31], v[124:127]\n"
      "v_exp_f32 v176, v176\n"
      "v_mfma_f32_16x16x32_bf16 v[48:51], v[184:187], v[24:27], 0\n"
      "v_cvt_pk_bf16_f32 v19, v176, v178\n"
      "v_cvt_pk_bf16_f32 v18, v176, v178\n"
      "v_mfma_f32_16x16x32_bf16 v[48:51], v[188:191], v[28:31], v[48:51]\n"
      "v_add_f32 v101, v176, v178\n"
      "v_pk_max_i16 v116, v116, 0\n"
      "v_mfma_f32_16x16x32_bf16 v[118:121], v[104:107], v[24:27], 0\n"
      "v_permlane16_swap_b32 v18, v19\n"
      "v_mfma_f32_16x16x32_bf16 v[118:121], v[108:111], v[28:31], v[118:121]\n"
      "v_exp_f32 v59, v59\n"
      "v_mfma_f32_16x16x4_4b_bf16 v[20:35], v[18:19], v[156:157], 0 cbsz:2 abid:0\n"
      "v_add_f32 v100, v176, v178\n"
      "v_pk_max_i16 v117, v117, 0\n"
      "v_exp_f32 v54, v54\n"
      "v_permlane16_swap_b32 v100, v101\n"
      "v_mov_b64 v[200:201], v[100:101]\n"
      "s_nop 0\n"
      "v_smfmac_f32_16x16x64_bf16 v[100:103], v[12:15], v[216:223], v5\n"
      "s_nop 2\n"
      "v_mfma_f32_16x16x32_bf16 v[28:31], v[216:219], v[132:135], v[28:31]\n"
      "v_exp_f32 v121, v121\n"
      "v_mfma_f32_16x16x32_bf16 v[28:31], v[220:223], v[136:139], v[28:31]\n"
      "v_exp_f32 v49, v49\n"
      "v_mfma_f32_16x16x32_bf16 v[28:31], v[92:95], v[60:63], v[28:31]\n"
      "v_exp_f32 v97, v97\n"
      "v_smfmac_f32_16x16x32_bf16 v[100:103], v[12:13], v[92:95], v5\n"
      "v_pk_max_i16 v45, v45, 0\n"
      "v_exp_f32 v118, v118\n"
      "v_mfma_f32_16x16x32_bf16 v[20:23], v[216:219], v[140:143], v[20:23]\n"
      "v_exp_f32 v120, v120\n"
      "v_mfma_f32_16x16x32_bf16 v[20:23], v[220:223], v[144:147], v[20:23]\n"
      "v_exp_f32 v50, v50\n"
      "v_mfma_f32_16x16x32_bf16 v[20:23], v[92:95], v[88:91], v[20:23]\n"
      "buffer_store_dwordx2 v[116:117], v3, s[44:47], s79 offen offset:0 nt sc1\n"
      "v_add_f32 v100, v100, v101\n"
      "v_mfma_f32_16x16x32_bf16 v[32:35], v[216:219], v[224:227], v[32:35]\n"
      "v_rcp_f32 v101, v100\n"
      "v_mfma_f32_16x16x32_bf16 v[32:35], v[220:223], v[228:231], v[32:35]\n"
      "ds_bpermute_b32 v102, v4, v101\n"
      "v_mfma_f32_16x16x32_bf16 v[32:35], v[92:95], v[196:199], v[32:35]\n"
      "s_waitcnt lgkmcnt(1)\n"
      "v_mfma_f32_16x16x32_bf16 v[128:131], a[160:163], v[158:161], 0\n"
      "v_exp_f32 v127, v127\n"
      "v_mfma_f32_16x16x32_bf16 v[128:131], a[164:167], v[164:167], v[128:131]\n"
      "v_exp_f32 v119, v119\n"
      "v_mfma_f32_16x16x32_bf16 v[128:131], a[168:171], v[204:207], v[128:131]\n"
      "v_exp_f32 v57, v57\n"
      "v_mfma_f32_16x16x32_bf16 v[128:131], a[172:175], v[148:151], v[128:131]\n"
      "v_exp_f32 v58, v58\n"
      "v_mfma_f32_16x16x32_bf16 v[24:27], v[216:219], v[36:39], v[24:27]\n"
      "v_exp_f32 v55, v55\n"
      "v_mfma_f32_16x16x32_bf16 v[24:27], v[220:223], v[40:43], v[24:27]\n"
      "v_exp_f32 v99, v99\n"
      "v_mfma_f32_16x16x32_bf16 v[24:27], v[92:95], v[244:247], v[24:27]\n"
      "v_exp_f32 v124, v124\n"
      "v_mfma_f32_16x16x32_bf16 v[220:223], a[176:179], v[158:161], 0\n"
      "v_exp_f32 v56, v56\n"
      "v_mfma_f32_16x16x32_bf16 v[220:223], a[180:183], v[164:167], v[220:223]\n"
      "s_waitcnt lgkmcnt(0)\n"
      "v_mul_f32_dpp v33, v102, v33 row_newbcast:4 row_mask:0xf bank_mask:0xf\n"
      "v_mul_f32_dpp v35, v102, v35 row_newbcast:12 row_mask:0xf bank_mask:0xf\n"
      "v_mfma_f32_16x16x32_bf16 v[220:223], a[184:187], v[204:207], v[220:223]\n"
      "v_mul_f32_dpp v34, v102, v34 row_newbcast:8 row_mask:0xf bank_mask:0xf\n"
      "v_mul_f32_dpp v32, v102, v32 row_newbcast:0 row_mask:0xf bank_mask:0xf\n"
      "v_mfma_f32_16x16x32_bf16 v[220:223], a[188:191], v[148:151], v[220:223]\n"
      "v_mul_f32_dpp v24, v102, v24 row_newbcast:0 row_mask:0xf bank_mask:0xf\n"
      "v_mul_f32_dpp v26, v102, v26 row_newbcast:8 row_mask:0xf bank_mask:0xf\n"
      "v_mfma_f32_16x16x32_bf16 v[92:95], a[128:131], v[158:161], 0\n"
      "v_mul_f32_dpp v30, v102, v30 row_newbcast:8 row_mask:0xf bank_mask:0xf\n"
      "v_mul_f32_dpp v29, v102, v29 row_newbcast:4 row_mask:0xf bank_mask:0xf\n"
      "v_mfma_f32_16x16x32_bf16 v[92:95], a[132:135], v[164:167], v[92:95]\n"
      "v_mul_f32_dpp v28, v102, v28 row_newbcast:0 row_mask:0xf bank_mask:0xf\n"
      "v_mul_f32_dpp v31, v102, v31 row_newbcast:12 row_mask:0xf bank_mask:0xf\n"
      "v_mfma_f32_16x16x32_bf16 v[92:95], a[136:139], v[204:207], v[92:95]\n"
      "v_exp_f32 v51, v51\n"
      "v_mfma_f32_16x16x32_bf16 v[92:95], a[140:143], v[148:151], v[92:95]\n"
      "buffer_store_dwordx2 v[46:47], v2, s[44:47], s75 offen offset:512 nt sc1\n"
      "v_cvt_pk_bf16_f32 v219, v50, v51\n"
      "v_mfma_f32_16x16x32_bf16 v[28:31], v[232:235], a[224:227], v[28:31]\n"
      "v_mul_f32_dpp v25, v102, v25 row_newbcast:4 row_mask:0xf bank_mask:0xf\n"
      "v_mul_f32_dpp v27, v102, v27 row_newbcast:12 row_mask:0xf bank_mask:0xf\n"
      "v_mfma_f32_16x16x32_bf16 v[28:31], v[252:255], a[228:231], v[28:31]\n"
      "v_exp_f32 v48, v48\n"
      "v_mfma_f32_16x16x32_bf16 v[28:31], v[248:251], a[232:235], v[28:31]\n"
      "v_mul_f32_dpp v20, v102, v20 row_newbcast:0 row_mask:0xf bank_mask:0xf\n"
      "v_cvt_pk_bf16_f32 v217, v58, v59\n"
      "v_mfma_f32_16x16x32_bf16 v[28:31], v[180:183], a[236:239], v[28:31]\n"
      "buffer_store_dwordx2 v[44:45], v2, s[44:47], s75 offen offset:0 nt sc1\n"
      "v_cvt_pk_bf16_f32 v218, v48, v49\n"
      "v_mfma_f32_16x16x32_bf16 v[32:35], v[232:235], a[240:243], v[32:35]\n"
      "v_cvt_pk_bf16_f32 v216, v56, v57\n"
      "v_mul_f32_dpp v23, v102, v23 row_newbcast:12 row_mask:0xf bank_mask:0xf\n"
      "v_mfma_f32_16x16x32_bf16 v[32:35], v[252:255], a[244:247], v[32:35]\n"
      "v_cvt_pk_bf16_f32 v122, v220, v221\n"
      "v_mfma_f32_16x16x32_bf16 v[32:35], v[248:251], a[248:251], v[32:35]\n"
      "s_add_u32 s44, s44, 13107200\n s_addc_u32 s45, s45, 0\n"
      "v_mov_b64 v[178:179], 0\n"
      "v_mfma_f32_16x16x32_bf16 v[32:35], v[180:183], a[252:255], v[32:35]\n"
      "v_mul_f32_dpp v21, v102, v21 row_newbcast:4 row_mask:0xf bank_mask:0xf\n"
      "v_maximum3_f32 v8, v8, v100, v101\n"
      "v_mfma_f32_16x16x32_bf16 v[48:51], a[144:147], v[158:161], 0\n"
      "v_mul_f32_dpp v22, v102, v22 row_newbcast:8 row_mask:0xf bank_mask:0xf\n"
      "v_cvt_pk_bf16_f32 v123, v222, v223\n"
      "v_mfma_f32_16x16x32_bf16 v[48:51], a[148:151], v[164:167], v[48:51]\n"
      "v_exp_f32 v96, v96\n"
      "v_mfma_f32_16x16x32_bf16 v[48:51], a[152:155], v[204:207], v[48:51]\n"
      "L_h7_%=:\n"
      "s_cbranch_execz L_hr_%=\n"
      "v_exp_f32 v53, v53\n"
      "v_mfma_f32_16x16x32_bf16 v[48:51], a[156:159], v[148:151], v[48:51]\n"
      "v_exp_f32 v125, v125\n"
      "v_mfma_f32_16x16x32_bf16 v[24:27], v[232:235], a[208:211], v[24:27]\n"
      "v_exp_f32 v98, v98\n"
      "v_mfma_f32_16x16x32_bf16 v[24:27], v[252:255], a[212:215], v[24:27]\n"
      "v_exp_f32 v126, v126\n"
      "v_mfma_f32_16x16x32_bf16 v[24:27], v[248:251], a[216:219], v[24:27]\n"
      "v_exp_f32 v52, v52\n"
      "v_mfma_f32_16x16x32_bf16 v[24:27], v[180:183], a[220:223], v[24:27]\n"
      "v_cvt_pk_bf16_f32 v221, v120, v121\n"
      "v_cvt_pk_bf16_f32 v117, v94, v95\n"
      "v_mfma_f32_16x16x32_bf16 v[20:23], v[232:235], a[192:195], v[20:23]\n"
      "v_cvt_pk_bf16_f32 v75, v54, v55\n"
      "v_cvt_pk_bf16_f32 v223, v126, v127\n"
      "v_mfma_f32_16x16x32_bf16 v[20:23], v[252:255], a[196:199], v[20:23]\n"
      "v_cvt_pk_bf16_f32 v222, v124, v125\n"
      "v_cvt_pk_bf16_f32 v73, v98, v99\n"
      "v_mfma_f32_16x16x32_bf16 v[20:23], v[248:251], a[200:203], v[20:23]\n"
      "v_cvt_pk_bf16_f32 v220, v118, v119\n"
      "v_cvt_pk_bf16_f32 v120, v128, v129\n"
      "v_mfma_f32_16x16x32_bf16 v[20:23], v[180:183], a[204:207], v[20:23]\n"
      "v_cvt_pk_bf16_f32 v74, v52, v53\n"
      "v_cvt_pk_bf16_f32 v119, v50, v51\n"
      "v_smfmac_f32_16x16x64_bf16 v[200:203], v[12:15], v[216:223], v5\n"
      "ds_read_b128 v[180:183], v0 offset:1024\n"
      "v_cvt_pk_bf16_f32 v118, v48, v49\n"
      "v_mfma_f32_16x16x4_4b_bf16 v[44:59], v[18:19], v[156:157], 0 cbsz:2 abid:2\n"
      "ds_read_b128 v[232:235], v0 offset:0\n"
      "v_cvt_pk_bf16_f32 v116, v92, v93\n"
      "v_cvt_pk_bf16_f32 v72, v96, v97\n"
      "v_cvt_pk_bf16_f32 v20, v20, v24\n"
      "v_cvt_pk_bf16_f32 v22, v22, v26\n"
      "v_cvt_pk_bf16_f32 v26, v23, v27\n"
      "v_smfmac_f32_16x16x32_bf16 v[200:203], v[12:13], v[72:75], v5\n"
      "ds_read_b128 v[124:127], v0 offset:1536\n"
      "v_cvt_pk_bf16_f32 v121, v130, v131\n"
      "v_pk_max_i16 v22, v22, 0\n"
      "v_cvt_pk_bf16_f32 v24, v21, v25\n"
      "v_mfma_f32_16x16x32_bf16 v[96:99], v[104:107], v[116:119], 0\n"
      "v_cvt_pk_bf16_f32 v21, v28, v32\n"
      "v_pk_max_i16 v20, v20, 0\n"
      "v_mfma_f32_16x16x32_bf16 v[96:99], v[108:111], v[120:123], v[96:99]\n"
      "v_pk_max_i16 v26, v26, 0\n"
      "v_cvt_pk_bf16_f32 v27, v31, v35\n"
      "v_mfma_f32_16x16x32_bf16 v[100:103], v[236:239], v[116:119], 0\n"
      "v_cvt_pk_bf16_f32 v25, v29, v33\n"
      "v_mov_b64 v[176:177], 0\n"
      "v_mfma_f32_16x16x32_bf16 v[100:103], v[240:243], v[120:123], v[100:103]\n"
      "v_pk_max_i16 v27, v27, 0\n"
      "v_cvt_pk_bf16_f32 v23, v30, v34\n"
      "v_mfma_f32_16x16x32_bf16 v[248:251], v[184:187], v[116:119], 0\n"
      "ds_read_b128 v[128:131], v0 offset:512\n"
      "v_add_f32 v200, v200, v201\n"
      "v_mfma_f32_16x16x32_bf16 v[248:251], v[188:191], v[120:123], v[248:251]\n"
      "v_exp_f32 v97, v97\n"
      "v_mfma_f32_16x16x32_bf16 v[52:55], v[216:219], v[132:135], v[52:55]\n"
      "v_exp_f32 v100, v100\n"
      "v_mfma_f32_16x16x32_bf16 v[52:55], v[220:223], v[136:139], v[52:55]\n"
      "v_rcp_f32 v201, v200\n"
      "v_mfma_f32_16x16x32_bf16 v[52:55], v[72:75], v[60:63], v[52:55]\n"
      "v_exp_f32 v98, v98\n"
      "v_mfma_f32_16x16x32_bf16 v[48:51], v[216:219], v[36:39], v[48:51]\n"
      "ds_bpermute_b32 v202, v4, v201 offset:128\n"
      "v_mfma_f32_16x16x32_bf16 v[48:51], v[220:223], v[40:43], v[48:51]\n"
      "v_exp_f32 v101, v101\n"
      "v_mfma_f32_16x16x32_bf16 v[48:51], v[72:75], v[244:247], v[48:51]\n"
      "v_exp_f32 v102, v102\n"
      "v_mfma_f32_16x16x32_bf16 v[28:31], v[168:171], v[116:119], 0\n"
      "v_pk_max_i16 v23, v23, 0\n"
      "v_pk_max_i16 v25, v25, 0\n"
      "v_mfma_f32_16x16x32_bf16 v[28:31], v[172:175], v[120:123], v[28:31]\n"
      "v_exp_f32 v248, v248\n"
      "v_mfma_f32_16x16x32_bf16 v[56:59], v[216:219], v[224:227], v[56:59]\n"
      "v_exp_f32 v251, v251\n"
      "v_mfma_f32_16x16x32_bf16 v[56:59], v[220:223], v[228:231], v[56:59]\n"
      "v_exp_f32 v99, v99\n"
      "v_mfma_f32_16x16x32_bf16 v[56:59], v[72:75], v[196:199], v[56:59]\n"
      "v_pk_max_i16 v24, v24, 0\n"
      "v_pk_max_i16 v21, v21, 0\n"
      "v_mfma_f32_16x16x32_bf16 v[44:47], v[216:219], v[140:143], v[44:47]\n"
      "v_exp_f32 v249, v249\n"
      "v_mfma_f32_16x16x32_bf16 v[44:47], v[220:223], v[144:147], v[44:47]\n"
      "v_exp_f32 v31, v31\n"
      "v_mfma_f32_16x16x32_bf16 v[44:47], v[72:75], v[88:91], v[44:47]\n"
      "buffer_store_dwordx2 v[20:21], v2, s[44:47], s100 offen offset:0 nt sc1\n"
      "v_cvt_pk_bf16_f32 v218, v248, v249\n"
      "v_mfma_f32_16x16x32_bf16 v[32:35], v[208:211], v[116:119], 0\n"
      "s_waitcnt lgkmcnt(0)\n"
      "v_mul_f32_dpp v51, v202, v51 row_newbcast:12 row_mask:0xf bank_mask:0xf\n"
      "v_mul_f32_dpp v53, v202, v53 row_newbcast:4 row_mask:0xf bank_mask:0xf\n"
      "v_mfma_f32_16x16x32_bf16 v[32:35], v[212:215], v[120:123], v[32:35]\n"
      "v_exp_f32 v250, v250\n"
      "v_mfma_f32_16x16x32_bf16 v[18:21], a[176:179], v[232:235], 0\n"
      "v_mul_f32_dpp v48, v202, v48 row_newbcast:0 row_mask:0xf bank_mask:0xf\n"
      "v_cvt_pk_bf16_f32 v219, v250, v251\n"
      "v_mfma_f32_16x16x32_bf16 v[18:21], a[180:183], v[128:131], v[18:21]\n"
      "v_mul_f32_dpp v49, v202, v49 row_newbcast:4 row_mask:0xf bank_mask:0xf\n"
      "v_mul_f32_dpp v50, v202, v50 row_newbcast:8 row_mask:0xf bank_mask:0xf\n"
      "v_mfma_f32_16x16x32_bf16 v[18:21], a[184:187], v[180:183], v[18:21]\n"
      "v_exp_f32 v28, v28\n"
      "v_mfma_f32_16x16x32_bf16 v[18:21], a[188:191], v[124:127], v[18:21]\n"
      "v_exp_f32 v33, v33\n"
      "v_smfmac_f32_16x16x64_bf16 v[176:179], v[112:115], v[116:123], v5\n"
      "buffer_store_dwordx2 v[22:23], v2, s[44:47], s100 offen offset:512 nt sc1\n"
      "v_mul_f32_dpp v52, v202, v52 row_newbcast:0 row_mask:0xf bank_mask:0xf\n"
      "v_mfma_f32_16x16x32_bf16 v[112:115], a[144:147], v[232:235], 0\n"
      "v_exp_f32 v32, v32\n"
      "v_mfma_f32_16x16x32_bf16 v[112:115], a[148:151], v[128:131], v[112:115]\n"
      "v_exp_f32 v29, v29\n"
      "v_mfma_f32_16x16x32_bf16 v[112:115], a[152:155], v[180:183], v[112:115]\n"
      "v_cvt_pk_bf16_f32 v255, v20, v21\n"
      "v_mul_f32_dpp v57, v202, v57 row_newbcast:4 row_mask:0xf bank_mask:0xf\n"
      "v_mfma_f32_16x16x32_bf16 v[112:115], a[156:159], v[124:127], v[112:115]\n"
      "v_exp_f32 v96, v96\n"
      "v_mfma_f32_16x16x32_bf16 v[220:223], a[128:131], v[232:235], 0\n"
      "v_exp_f32 v103, v103\n"
      "v_mfma_f32_16x16x32_bf16 v[220:223], a[132:135], v[128:131], v[220:223]\n"
      "v_mul_f32_dpp v47, v202, v47 row_newbcast:12 row_mask:0xf bank_mask:0xf\n"
      "v_cvt_pk_bf16_f32 v254, v18, v19\n"
      "v_mfma_f32_16x16x32_bf16 v[220:223], a[136:139], v[180:183], v[220:223]\n"
      "v_exp_f32 v35, v35\n"
      "v_mfma_f32_16x16x32_bf16 v[220:223], a[140:143], v[124:127], v[220:223]\n"
      "v_mul_f32_dpp v54, v202, v54 row_newbcast:8 row_mask:0xf bank_mask:0xf\n"
      "v_mul_f32_dpp v46, v202, v46 row_newbcast:8 row_mask:0xf bank_mask:0xf\n"
      "v_mfma_f32_16x16x32_bf16 v[20:23], v[64:67], v[116:119], 0\n"
      "v_mul_f32_dpp v59, v202, v59 row_newbcast:12 row_mask:0xf bank_mask:0xf\n"
      "v_mul_f32_dpp v44, v202, v44 row_newbcast:0 row_mask:0xf bank_mask:0xf\n"
      "v_mfma_f32_16x16x32_bf16 v[20:23], v[68:71], v[120:123], v[20:23]\n"
      "v_cvt_pk_bf16_f32 v251, v114, v115\n"
      "v_cvt_pk_bf16_f32 v250, v112, v113\n"
      "v_mfma_f32_16x16x32_bf16 v[120:123], a[160:163], v[232:235], 0\n"
      "v_mul_f32_dpp v45, v202, v45 row_newbcast:4 row_mask:0xf bank_mask:0xf\n"
      "v_cvt_pk_bf16_f32 v117, v102, v103\n"
      "v_mfma_f32_16x16x32_bf16 v[120:123], a[164:167], v[128:131], v[120:123]\n"
      "v_cvt_pk_bf16_f32 v249, v222, v223\n"
      "v_cvt_pk_bf16_f32 v116, v100, v101\n"
      "v_mfma_f32_16x16x32_bf16 v[120:123], a[168:171], v[180:183], v[120:123]\n"
      "v_exp_f32 v30, v30\n"
      "v_mfma_f32_16x16x32_bf16 v[120:123], a[172:175], v[124:127], v[120:123]\n"
      "v_exp_f32 v22, v22\n"
      "v_mfma_f32_16x16x32_bf16 v[48:51], v[152:155], a[208:211], v[48:51]\n"
      "v_mul_f32_dpp v55, v202, v55 row_newbcast:12 row_mask:0xf bank_mask:0xf\n"
      "v_cvt_pk_bf16_f32 v248, v220, v221\n"
      "v_mfma_f32_16x16x32_bf16 v[48:51], v[80:83], a[212:215], v[48:51]\n"
      "v_exp_f32 v34, v34\n"
      "v_mfma_f32_16x16x32_bf16 v[48:51], v[84:87], a[216:219], v[48:51]\n"
      "s_nop 0\n"
      "v_cvt_pk_bf16_f32 v252, v120, v121\n"
      "v_cvt_pk_bf16_f32 v253, v122, v123\n"
      "v_mfma_f32_16x16x32_bf16 v[48:51], v[76:79], a[220:223], v[48:51]\n"
      "v_cvt_pk_bf16_f32 v220, v96, v97\n"
      "v_mul_f32_dpp v56, v202, v56 row_newbcast:0 row_mask:0xf bank_mask:0xf\n"
      "v_mfma_f32_16x16x32_bf16 v[120:123], v[184:187], v[248:251], 0\n"
      "v_mul_f32_dpp v58, v202, v58 row_newbcast:8 row_mask:0xf bank_mask:0xf\n"
      "v_cvt_pk_bf16_f32 v221, v98, v99\n"
      "v_mfma_f32_16x16x32_bf16 v[120:123], v[188:191], v[252:255], v[120:123]\n"
      "v_cvt_pk_bf16_f32 v223, v30, v31\n"
      "v_cvt_pk_bf16_f32 v222, v28, v29\n"
      "v_smfmac_f32_16x16x64_bf16 v[176:179], v[192:195], v[248:255], v5\n"
      "buffer_store_dwordx2 v[24:25], v2, s[44:47], s100 offen offset:256 nt sc1\n"
      "v_cvt_pk_bf16_f32 v216, v32, v33\n"
      "v_mfma_f32_16x16x32_bf16 v[192:195], v[236:239], v[248:251], 0\n"
      "v_exp_f32 v20, v20\n"
      "v_mfma_f32_16x16x32_bf16 v[192:195], v[240:243], v[252:255], v[192:195]\n"
      "v_exp_f32 v23, v23\n"
      "v_mfma_f32_16x16x32_bf16 v[52:55], v[152:155], a[224:227], v[52:55]\n"
      "s_nop 0\n"
      "v_add_f32 v176, v176, v177\n"
      "v_add_f32 v178, v178, v179\n"
      "v_mfma_f32_16x16x32_bf16 v[52:55], v[80:83], a[228:231], v[52:55]\n"
      "v_exp_f32 v178, v178\n"
      "v_mfma_f32_16x16x32_bf16 v[52:55], v[84:87], a[232:235], v[52:55]\n"
      "v_exp_f32 v123, v123\n"
      "v_mfma_f32_16x16x32_bf16 v[52:55], v[76:79], a[236:239], v[52:55]\n"
      "v_exp_f32 v176, v176\n"
      "v_mfma_f32_16x16x32_bf16 v[236:239], v[64:67], v[248:251], 0\n"
      "v_cvt_pk_bf16_f32 v189, v176, v178\n"
      "v_add_f32 v240, v176, v178\n"
      "v_mfma_f32_16x16x32_bf16 v[236:239], v[68:71], v[252:255], v[236:239]\n"
      "v_cvt_pk_bf16_f32 v119, v22, v23\n"
      "v_cvt_pk_bf16_f32 v188, v176, v178\n"
      "v_mfma_f32_16x16x32_bf16 v[44:47], v[152:155], a[192:195], v[44:47]\n"
      "v_exp_f32 v121, v121\n"
      "v_mfma_f32_16x16x32_bf16 v[44:47], v[80:83], a[196:199], v[44:47]\n"
      "v_permlane16_swap_b32 v188, v189\n"
      "v_mfma_f32_16x16x32_bf16 v[44:47], v[84:87], a[200:203], v[44:47]\n"
      "v_cvt_pk_bf16_f32 v217, v34, v35\n"
      "v_add_f32 v241, v176, v178\n"
      "v_mfma_f32_16x16x32_bf16 v[44:47], v[76:79], a[204:207], v[44:47]\n"
      "v_exp_f32 v236, v236\n"
      "v_mfma_f32_16x16x32_bf16 v[92:95], v[168:171], v[248:251], 0\n"
      "v_permlane16_swap_b32 v240, v241\n"
      "v_mfma_f32_16x16x32_bf16 v[92:95], v[172:175], v[252:255], v[92:95]\n"
      "v_exp_f32 v21, v21\n"
      "v_mfma_f32_16x16x32_bf16 v[56:59], v[152:155], a[240:243], v[56:59]\n"
      "v_exp_f32 v120, v120\n"
      "v_mfma_f32_16x16x32_bf16 v[56:59], v[80:83], a[244:247], v[56:59]\n"
      "v_exp_f32 v194, v194\n"
      "v_mfma_f32_16x16x32_bf16 v[56:59], v[84:87], a[248:251], v[56:59]\n"
      "v_exp_f32 v237, v237\n"
      "v_mfma_f32_16x16x32_bf16 v[56:59], v[76:79], a[252:255], v[56:59]\n"
      "v_cvt_pk_bf16_f32 v170, v120, v121\n"
      "v_mov_b64 v[76:77], v[240:241]\n"
      "v_smfmac_f32_16x16x64_bf16 v[240:243], v[12:15], v[216:223], v5\n"
      "buffer_store_dwordx2 v[26:27], v2, s[44:47], s100 offen offset:768 nt sc1\n"
      "v_cvt_pk_bf16_f32 v118, v20, v21\n"
      "v_mfma_f32_16x16x32_bf16 v[84:87], v[208:211], v[248:251], 0\n"
      "v_exp_f32 v239, v239\n"
      "v_mfma_f32_16x16x32_bf16 v[84:87], v[212:215], v[252:255], v[84:87]\n"
      "v_cvt_pk_bf16_f32 v44, v44, v48\n"
      "v_cvt_pk_bf16_f32 v48, v45, v49\n"
      "v_cvt_pk_bf16_f32 v49, v53, v57\n"
      "v_cvt_pk_bf16_f32 v46, v46, v50\n"
      "v_smfmac_f32_16x16x32_bf16 v[240:243], v[12:13], v[116:119], v5\n"
      "v_pk_max_i16 v49, v49, 0\n"
      "v_mfma_f32_16x16x4_4b_bf16 v[20:35], v[188:189], v[156:157], 0 cbsz:2 abid:0\n"
      "v_cvt_pk_bf16_f32 v50, v47, v51\n"
      "v_cvt_pk_bf16_f32 v47, v54, v58\n"
      "v_pk_max_i16 v47, v47, 0\n"
      "v_cvt_pk_bf16_f32 v51, v55, v59\n"
      "v_cvt_pk_bf16_f32 v45, v52, v56\n"
      "v_exp_f32 v122, v122\n"
      "v_pk_max_i16 v44, v44, 0\n"
      "v_mfma_f32_16x16x32_bf16 v[212:215], v[104:107], v[248:251], 0\n"
      "v_pk_max_i16 v45, v45, 0\n"
      "v_pk_max_i16 v46, v46, 0\n"
      "v_mfma_f32_16x16x32_bf16 v[212:215], v[108:111], v[252:255], v[212:215]\n"
      "buffer_store_dwordx2 v[44:45], v2, s[44:47], s101 offen offset:0 nt sc1\n"
      "v_add_f32 v240, v240, v241\n"
      "v_mfma_f32_16x16x32_bf16 v[32:35], v[216:219], v[224:227], v[32:35]\n"
      "v_rcp_f32 v241, v240\n"
      "v_mfma_f32_16x16x32_bf16 v[32:35], v[220:223], v[228:231], v[32:35]\n"
      "ds_bpermute_b32 v242, v4, v241\n"
      "v_mfma_f32_16x16x32_bf16 v[32:35], v[116:119], v[196:199], v[32:35]\n"
      "v_exp_f32 v84, v84\n"
      "v_mfma_f32_16x16x32_bf16 v[20:23], v[216:219], v[140:143], v[20:23]\n"
      "v_exp_f32 v214, v214\n"
      "v_mfma_f32_16x16x32_bf16 v[20:23], v[220:223], v[144:147], v[20:23]\n"
      "v_exp_f32 v87, v87\n"
      "v_mfma_f32_16x16x32_bf16 v[20:23], v[116:119], v[88:91], v[20:23]\n"
      "v_exp_f32 v213, v213\n"
      "v_mfma_f32_16x16x32_bf16 v[24:27], v[216:219], v[36:39], v[24:27]\n"
      "v_exp_f32 v85, v85\n"
      "v_mfma_f32_16x16x32_bf16 v[24:27], v[220:223], v[40:43], v[24:27]\n"
      "v_exp_f32 v95, v95\n"
      "v_mfma_f32_16x16x32_bf16 v[24:27], v[116:119], v[244:247], v[24:27]\n"
      "buffer_store_dwordx2 v[46:47], v2, s[44:47], s101 offen offset:512 nt sc1\n"
      "v_pk_max_i16 v48, v48, 0\n"
      "v_mfma_f32_16x16x32_bf16 v[28:31], v[216:219], v[132:135], v[28:31]\n"
      "v_exp_f32 v86, v86\n"
      "v_mfma_f32_16x16x32_bf16 v[28:31], v[220:223], v[136:139], v[28:31]\n"
      "v_exp_f32 v215, v215\n"
      "v_mfma_f32_16x16x32_bf16 v[28:31], v[116:119], v[60:63], v[28:31]\n"
      "s_waitcnt lgkmcnt(0)\n"
      "v_mul_f32_dpp v21, v242, v21 row_newbcast:4 row_mask:0xf bank_mask:0xf\n"
      "v_mul_f32_dpp v24, v242, v24 row_newbcast:0 row_mask:0xf bank_mask:0xf\n"
      "v_mfma_f32_16x16x4_4b_bf16 v[104:119], v[188:189], v[156:157], 0 cbsz:2 abid:2\n"
      "v_mul_f32_dpp v26, v242, v26 row_newbcast:8 row_mask:0xf bank_mask:0xf\n"
      "v_cvt_pk_bf16_f32 v173, v214, v215\n"
      "v_mul_f32_dpp v27, v242, v27 row_newbcast:12 row_mask:0xf bank_mask:0xf\n"
      "v_mul_f32_dpp v32, v242, v32 row_newbcast:0 row_mask:0xf bank_mask:0xf\n"
      "v_mul_f32_dpp v23, v242, v23 row_newbcast:12 row_mask:0xf bank_mask:0xf\n"
      "v_mul_f32_dpp v31, v242, v31 row_newbcast:12 row_mask:0xf bank_mask:0xf\n"
      "v_mul_f32_dpp v25, v242, v25 row_newbcast:4 row_mask:0xf bank_mask:0xf\n"
      "v_mul_f32_dpp v35, v242, v35 row_newbcast:12 row_mask:0xf bank_mask:0xf\n"
      "v_cvt_pk_bf16_f32 v169, v86, v87\n"
      "v_mfma_f32_16x16x32_bf16 v[24:27], v[158:161], a[208:211], v[24:27]\n"
      "v_mul_f32_dpp v34, v242, v34 row_newbcast:8 row_mask:0xf bank_mask:0xf\n"
      "v_mul_f32_dpp v20, v242, v20 row_newbcast:0 row_mask:0xf bank_mask:0xf\n"
      "v_mfma_f32_16x16x32_bf16 v[24:27], v[164:167], a[212:215], v[24:27]\n"
      "v_exp_f32 v94, v94\n"
      "v_mfma_f32_16x16x32_bf16 v[24:27], v[204:207], a[216:219], v[24:27]\n"
      "v_exp_f32 v92, v92\n"
      "v_mfma_f32_16x16x32_bf16 v[24:27], v[148:151], a[220:223], v[24:27]\n"
      "buffer_store_dwordx2 v[48:49], v2, s[44:47], s101 offen offset:256 nt sc1\n"
      "v_mul_f32_dpp v22, v242, v22 row_newbcast:8 row_mask:0xf bank_mask:0xf\n"
      "v_mul_f32_dpp v29, v242, v29 row_newbcast:4 row_mask:0xf bank_mask:0xf\n"
      "v_cvt_pk_bf16_f32 v175, v94, v95\n"
      "v_mfma_f32_16x16x32_bf16 v[20:23], v[158:161], a[192:195], v[20:23]\n"
      "v_exp_f32 v212, v212\n"
      "v_mfma_f32_16x16x32_bf16 v[20:23], v[164:167], a[196:199], v[20:23]\n"
      "v_cvt_pk_bf16_f32 v172, v212, v213\n"
      "v_mul_f32_dpp v33, v242, v33 row_newbcast:4 row_mask:0xf bank_mask:0xf\n"
      "v_mfma_f32_16x16x32_bf16 v[20:23], v[204:207], a[200:203], v[20:23]\n"
      "v_exp_f32 v93, v93\n"
      "v_mfma_f32_16x16x32_bf16 v[20:23], v[148:151], a[204:207], v[20:23]\n"
      "v_cvt_pk_bf16_f32 v171, v122, v123\n"
      "v_cvt_pk_bf16_f32 v174, v92, v93\n"
      "v_pk_max_i16 v51, v51, 0\n"
      "v_mfma_f32_16x16x32_bf16 v[32:35], v[158:161], a[240:243], v[32:35]\n"
      "v_exp_f32 v192, v192\n"
      "v_mfma_f32_16x16x32_bf16 v[32:35], v[164:167], a[244:247], v[32:35]\n"
      "v_cvt_pk_bf16_f32 v168, v84, v85\n"
      "v_mul_f32_dpp v28, v242, v28 row_newbcast:0 row_mask:0xf bank_mask:0xf\n"
      "v_mfma_f32_16x16x32_bf16 v[32:35], v[204:207], a[248:251], v[32:35]\n"
      "v_exp_f32 v238, v238\n"
      "v_mfma_f32_16x16x32_bf16 v[32:35], v[148:151], a[252:255], v[32:35]\n"
      "v_mul_f32_dpp v30, v242, v30 row_newbcast:8 row_mask:0xf bank_mask:0xf\n"
      "v_pk_max_i16 v50, v50, 0\n"
      "v_smfmac_f32_16x16x64_bf16 v[76:79], v[12:15], v[168:175], v5\n"
      "buffer_store_dwordx2 v[50:51], v2, s[44:47], s101 offen offset:768 nt sc1\n"
      "v_cvt_pk_bf16_f32 v94, v236, v237\n"
      "v_mfma_f32_16x16x32_bf16 v[28:31], v[158:161], a[224:227], v[28:31]\n"
      "v_exp_f32 v193, v193\n"
      "v_mfma_f32_16x16x32_bf16 v[28:31], v[164:167], a[228:231], v[28:31]\n"
      "v_cvt_pk_bf16_f32 v92, v192, v193\n"
      "v_cvt_pk_bf16_f32 v95, v238, v239\n"
      "v_mfma_f32_16x16x32_bf16 v[28:31], v[204:207], a[232:235], v[28:31]\n"
      "v_exp_f32 v195, v195\n"
      "v_mfma_f32_16x16x32_bf16 v[28:31], v[148:151], a[236:239], v[28:31]\n"
      "v_cvt_pk_bf16_f32 v93, v194, v195\n"
      "v_cvt_pk_bf16_f32 v22, v22, v26\n"
      "v_cvt_pk_bf16_f32 v26, v23, v27\n"
      "v_pk_max_i16 v26, v26, 0\n"
      "v_smfmac_f32_16x16x32_bf16 v[76:79], v[12:13], v[92:95], v5\n"
      "s_nop 2\n"
      "v_cvt_pk_bf16_f32 v27, v31, v35\n"
      "v_pk_max_i16 v27, v27, 0\n"
      "v_mfma_f32_16x16x32_bf16 v[104:107], v[168:171], v[140:143], v[104:107]\n"
      "s_nop 1\n"
      "v_add_f32 v76, v76, v77\n"
      "v_mfma_f32_16x16x32_bf16 v[104:107], v[172:175], v[144:147], v[104:107]\n"
      "v_rcp_f32 v77, v76\n"
      "v_mfma_f32_16x16x32_bf16 v[104:107], v[92:95], v[88:91], v[104:107]\n"
      "buffer_store_dwordx2 v[26:27], v2, s[44:47], s34 offen offset:768 nt sc1\n"
      "ds_bpermute_b32 v78, v4, v77 offset:128\n"
      "v_mfma_f32_16x16x32_bf16 v[108:111], v[168:171], v[36:39], v[108:111]\n"
      "v_cvt_pk_bf16_f32 v20, v20, v24\n"
      "v_cvt_pk_bf16_f32 v24, v21, v25\n"
      "v_mfma_f32_16x16x32_bf16 v[108:111], v[172:175], v[40:43], v[108:111]\n"
      "v_mfma_f32_16x16x32_bf16 v[108:111], v[92:95], v[244:247], v[108:111]\n"
      "v_cvt_pk_bf16_f32 v25, v29, v33\n"
      "v_pk_max_i16 v20, v20, 0\n"
      "v_mfma_f32_16x16x32_bf16 v[116:119], v[168:171], v[224:227], v[116:119]\n"
      "v_pk_max_i16 v25, v25, 0\n"
      "v_pk_max_i16 v24, v24, 0\n"
      "v_mfma_f32_16x16x32_bf16 v[116:119], v[172:175], v[228:231], v[116:119]\n"
      "v_mfma_f32_16x16x32_bf16 v[116:119], v[92:95], v[196:199], v[116:119]\n"
      "buffer_store_dwordx2 v[24:25], v2, s[44:47], s34 offen offset:256 nt sc1\n"
      "v_pk_max_i16 v22, v22, 0\n"
      "v_mfma_f32_16x16x32_bf16 v[112:115], v[168:171], v[132:135], v[112:115]\n"
      "v_cvt_pk_bf16_f32 v23, v30, v34\n"
      "v_cvt_pk_bf16_f32 v21, v28, v32\n"
      "v_mfma_f32_16x16x32_bf16 v[112:115], v[172:175], v[136:139], v[112:115]\n"
      "v_mfma_f32_16x16x32_bf16 v[112:115], v[92:95], v[60:63], v[112:115]\n"
      "s_waitcnt lgkmcnt(0)\n"
      "v_mul_f32_dpp v110, v78, v110 row_newbcast:8 row_mask:0xf bank_mask:0xf\n"
      "v_mul_f32_dpp v109, v78, v109 row_newbcast:4 row_mask:0xf bank_mask:0xf\n"
      "v_mul_f32_dpp v111, v78, v111 row_newbcast:12 row_mask:0xf bank_mask:0xf\n"
      "v_mul_f32_dpp v108, v78, v108 row_newbcast:0 row_mask:0xf bank_mask:0xf\n"
      "v_mul_f32_dpp v106, v78, v106 row_newbcast:8 row_mask:0xf bank_mask:0xf\n"
      "v_pk_max_i16 v23, v23, 0\n"
      "v_mfma_f32_16x16x32_bf16 v[108:111], v[232:235], a[208:211], v[108:111]\n"
      "v_mul_f32_dpp v104, v78, v104 row_newbcast:0 row_mask:0xf bank_mask:0xf\n"
      "v_mfma_f32_16x16x32_bf16 v[108:111], v[128:131], a[212:215], v[108:111]\n"
      "v_mul_f32_dpp v105, v78, v105 row_newbcast:4 row_mask:0xf bank_mask:0xf\n"
      "v_pk_max_i16 v21, v21, 0\n"
      "v_mfma_f32_16x16x32_bf16 v[108:111], v[180:183], a[216:219], v[108:111]\n"
      "v_mul_f32_dpp v118, v78, v118 row_newbcast:8 row_mask:0xf bank_mask:0xf\n"
      "v_mul_f32_dpp v107, v78, v107 row_newbcast:12 row_mask:0xf bank_mask:0xf\n"
      "v_mfma_f32_16x16x32_bf16 v[108:111], v[124:127], a[220:223], v[108:111]\n"
      "buffer_store_dwordx2 v[22:23], v2, s[44:47], s34 offen offset:512 nt sc1\n"
      "v_mul_f32_dpp v116, v78, v116 row_newbcast:0 row_mask:0xf bank_mask:0xf\n"
      "v_mfma_f32_16x16x32_bf16 v[104:107], v[232:235], a[192:195], v[104:107]\n"
      "v_mul_f32_dpp v114, v78, v114 row_newbcast:8 row_mask:0xf bank_mask:0xf\n"
      "v_mul_f32_dpp v113, v78, v113 row_newbcast:4 row_mask:0xf bank_mask:0xf\n"
      "v_mfma_f32_16x16x32_bf16 v[104:107], v[128:131], a[196:199], v[104:107]\n"
      "v_mul_f32_dpp v119, v78, v119 row_newbcast:12 row_mask:0xf bank_mask:0xf\n"
      "v_mul_f32_dpp v117, v78, v117 row_newbcast:4 row_mask:0xf bank_mask:0xf\n"
      "v_mfma_f32_16x16x32_bf16 v[104:107], v[180:183], a[200:203], v[104:107]\n"
      "v_mfma_f32_16x16x32_bf16 v[104:107], v[124:127], a[204:207], v[104:107]\n"
      "buffer_store_dwordx2 v[20:21], v2, s[44:47], s34 offen offset:0 nt sc1\n"
      "v_mul_f32_dpp v115, v78, v115 row_newbcast:12 row_mask:0xf bank_mask:0xf\n"
      "v_mfma_f32_16x16x32_bf16 v[116:119], v[232:235], a[240:243], v[116:119]\n"
      "v_mul_f32_dpp v112, v78, v112 row_newbcast:0 row_mask:0xf bank_mask:0xf\n"
      "v_mfma_f32_16x16x32_bf16 v[116:119], v[128:131], a[244:247], v[116:119]\n"
      "v_mfma_f32_16x16x32_bf16 v[116:119], v[180:183], a[248:251], v[116:119]\n"
      "v_mfma_f32_16x16x32_bf16 v[116:119], v[124:127], a[252:255], v[116:119]\n"
      "s_nop 0\n"
      "v_cvt_pk_bf16_f32 v106, v106, v110\n"
      "v_mfma_f32_16x16x32_bf16 v[112:115], v[232:235], a[224:227], v[112:115]\n"
      "v_pk_max_i16 v106, v106, 0\n"
      "v_mfma_f32_16x16x32_bf16 v[112:115], v[128:131], a[228:231], v[112:115]\n"
      "v_cvt_pk_bf16_f32 v110, v107, v111\n"
      "v_mfma_f32_16x16x32_bf16 v[112:115], v[180:183], a[232:235], v[112:115]\n"
      "v_mfma_f32_16x16x32_bf16 v[112:115], v[124:127], a[236:239], v[112:115]\n"
      "s_nop 7\n"
      "v_cvt_pk_bf16_f32 v107, v114, v118\n"
      "v_pk_max_i16 v107, v107, 0\n"
      "buffer_store_dwordx2 v[106:107], v2, s[44:47], s35 offen offset:512 nt sc1\n"
      "v_cvt_pk_bf16_f32 v104, v104, v108\n"
      "v_cvt_pk_bf16_f32 v108, v105, v109\n"
      "v_cvt_pk_bf16_f32 v109, v113, v117\n"
      "v_pk_max_i16 v108, v108, 0\n"
      "v_cvt_pk_bf16_f32 v111, v115, v119\n"
      "v_pk_max_i16 v111, v111, 0\n"
      "v_pk_max_i16 v109, v109, 0\n"
      "v_maximum3_f32 v8, v8, v200, v201\n"
      "buffer_store_dwordx2 v[108:109], v2, s[44:47], s35 offen offset:256 nt sc1\n"
      "v_maximum3_f32 v8, v8, v240, v241\n"
      "v_pk_max_i16 v110, v110, 0\n"
      "s_not_b64 vcc, vcc\n"
      "v_cvt_pk_bf16_f32 v105, v112, v116\n"
      "v_pk_max_i16 v105, v105, 0\n"
      "buffer_store_dwordx2 v[110:111], v2, s[44:47], s35 offen offset:768 nt sc1\n"
      "v_pk_max_i16 v104, v104, 0\n"
      "v_maximum3_f32 v8, v8, v76, v77\n"
      "buffer_store_dwordx2 v[104:105], v2, s[44:47], s35 offen offset:0 nt sc1\n"
      "L_end_%=:\n"
      "s_waitcnt vmcnt(0) lgkmcnt(0)\n"
      "v_cmp_nge_f32 vcc, 0x53800000, v8\n"
      "s_nop 4\n"
      "s_cbranch_vccnz L_ai_end\n"
      "s_endpgm\n"
      "L_ai_end:\n"
      "s_mov_b32 s40, s4\n"
      "s_and_b32 s41, s5, 0xffff\n"
      "s_mov_b32 s42, 104857600\n"
      "s_mov_b32 s44, s14\n"
      "s_and_b32 s45, s15, 0xffff\n"
      "s_mov_b32 s46, 104857600\n"
      "s_mov_b32 s48, s18\n"
      "s_add_u32 s49, s18, 13107200\n"
      "s_mov_b32 s50, 8\n"
      "s_mov_b32 vcc_lo, 0x55555555\n"
      "s_mov_b32 vcc_hi, 0x55555555\n"
      "v_subrev_u32 v9, 448, v9\n"
      "v_subrev_u32 v10, 448, v10\n"
      "v_subrev_u32 v11, 3584, v11\n"
      "s_add_u32 s52, s48, 0\n"
      "s_add_u32 m0, s53, 0\n"
      "s_nop 0\n"
      "buffer_load_dwordx4 v1, s[40:43], s52 offen offset:0 lds\n"
      "buffer_load_dwordx4 v1, s[40:43], s52 offen offset:2048 lds\n"
      "s_add_u32 m0, s53, 896\n"
      "s_nop 0\n"
      "buffer_load_dwordx4 v1, s[40:43], s52 offen offset:128 lds\n"
      "buffer_load_dwordx4 v1, s[40:43], s52 offen offset:2176 lds\n"
      "s_add_u32 s52, s48, 4096\n"
      "s_add_u32 m0, s53, 4096\n"
      "s_nop 0\n"
      "buffer_load_dwordx4 v1, s[40:43], s52 offen offset:0 lds\n"
      "buffer_load_dwordx4 v1, s[40:43], s52 offen offset:2048 lds\n"
      "s_add_u32 m0, s53, 4992\n"
      "s_nop 0\n"
      "buffer_load_dwordx4 v1, s[40:43], s52 offen offset:128 lds\n"
      "buffer_load_dwordx4 v1, s[40:43], s52 offen offset:2176 lds\n"
      "s_add_u32 s52, s48, 8192\n"
      "s_add_u32 m0, s53, 8192\n"
      "s_nop 0\n"
      "buffer_load_dwordx4 v1, s[40:43], s52 offen offset:0 lds\n"
      "buffer_load_dwordx4 v1, s[40:43], s52 offen offset:2048 lds\n"
      "s_add_u32 m0, s53, 9088\n"
      "s_nop 0\n"
      "buffer_load_dwordx4 v1, s[40:43], s52 offen offset:128 lds\n"
      "buffer_load_dwordx4 v1, s[40:43], s52 offen offset:2176 lds\n"
      "s_add_u32 s52, s48, 12288\n"
      "s_add_u32 m0, s53, 12288\n"
      "s_nop 0\n"
      "buffer_load_dwordx4 v1, s[40:43], s52 offen offset:0 lds\n"
      "buffer_load_dwordx4 v1, s[40:43], s52 offen offset:2048 lds\n"
      "s_add_u32 m0, s53, 13184\n"
      "s_nop 0\n"
      "buffer_load_dwordx4 v1, s[40:43], s52 offen offset:128 lds\n"
      "buffer_load_dwordx4 v1, s[40:43], s52 offen offset:2176 lds\n"
      "s_add_u32 s52, s48, 16384\n"
      "s_add_u32 m0, s53, 16384\n"
      "s_nop 0\n"
      "buffer_load_dwordx4 v1, s[40:43], s52 offen offset:0 lds\n"
      "buffer_load_dwordx4 v1, s[40:43], s52 offen offset:2048 lds\n"
      "s_add_u32 m0, s53, 17280\n"
      "s_nop 0\n"
      "buffer_load_dwordx4 v1, s[40:43], s52 offen offset:128 lds\n"
      "buffer_load_dwordx4 v1, s[40:43], s52 offen offset:2176 lds\n"
      "s_add_u32 s52, s48, 20480\n"
      "s_add_u32 m0, s53, 20480\n"
      "s_nop 0\n"
      "buffer_load_dwordx4 v1, s[40:43], s52 offen offset:0 lds\n"
      "buffer_load_dwordx4 v1, s[40:43], s52 offen offset:2048 lds\n"
      "s_add_u32 m0, s53, 21376\n"
      "s_nop 0\n"
      "buffer_load_dwordx4 v1, s[40:43], s52 offen offset:128 lds\n"
      "buffer_load_dwordx4 v1, s[40:43], s52 offen offset:2176 lds\n"
      "s_waitcnt vmcnt(0)\n"
      "L_safe_%=:\n"
      "s_waitcnt vmcnt(45)\n"
      "ds_read_b128 v[148:151], v0 offset:12288\n"
      "ds_read_b128 v[136:139], v0 offset:12800\n"
      "ds_read_b128 v[140:143], v0 offset:13312\n"
      "ds_read_b128 v[144:147], v0 offset:13824\n"
      "s_waitcnt vmcnt(41)\n"
      "ds_read_b128 v[252:255], v0 offset:16384\n"
      "ds_read_b128 v[48:51], v0 offset:16896\n"
      "ds_read_b128 v[124:127], v0 offset:17408\n"
      "ds_read_b128 v[116:119], v0 offset:17920\n"
      "s_waitcnt lgkmcnt(7)\n"
      "v_mfma_f32_16x16x32_bf16 v[216:219], a[0:3], v[148:151], 0\n"
      "s_waitcnt lgkmcnt(6)\n"
      "v_mfma_f32_16x16x32_bf16 v[216:219], a[4:7], v[136:139], v[216:219]\n"
      "s_waitcnt lgkmcnt(5)\n"
      "v_mfma_f32_16x16x32_bf16 v[216:219], a[8:11], v[140:143], v[216:219]\n"
      "s_waitcnt lgkmcnt(4)\n"
      "v_mfma_f32_16x16x32_bf16 v[216:219], a[12:15], v[144:147], v[216:219]\n"
      "v_mfma_f32_16x16x32_bf16 v[96:99], a[16:19], v[148:151], 0\n"
      "v_mfma_f32_16x16x32_bf16 v[96:99], a[20:23], v[136:139], v[96:99]\n"
      "v_mfma_f32_16x16x32_bf16 v[96:99], a[24:27], v[140:143], v[96:99]\n"
      "v_mfma_f32_16x16x32_bf16 v[96:99], a[28:31], v[144:147], v[96:99]\n"
      "v_mfma_f32_16x16x32_bf16 v[24:27], a[32:35], v[148:151], 0\n"
      "v_mfma_f32_16x16x32_bf16 v[24:27], a[36:39], v[136:139], v[24:27]\n"
      "v_mfma_f32_16x16x32_bf16 v[24:27], a[40:43], v[140:143], v[24:27]\n"
      "v_mfma_f32_16x16x32_bf16 v[24:27], a[44:47], v[144:147], v[24:27]\n"
      "v_cvt_pk_bf16_f32 v92, v216, v217\n"
      "v_cvt_pk_bf16_f32 v93, v218, v219\n"
      "v_mfma_f32_16x16x32_bf16 v[216:219], a[48:51], v[148:151], 0\n"
      "v_mfma_f32_16x16x32_bf16 v[216:219], a[52:55], v[136:139], v[216:219]\n"
      "v_cvt_pk_bf16_f32 v94, v96, v97\n"
      "v_cvt_pk_bf16_f32 v95, v98, v99\n"
      "v_mfma_f32_16x16x32_bf16 v[216:219], a[56:59], v[140:143], v[216:219]\n"
      "v_mfma_f32_16x16x32_bf16 v[216:219], a[60:63], v[144:147], v[216:219]\n"
      "v_cvt_pk_bf16_f32 v96, v24, v25\n"
      "v_cvt_pk_bf16_f32 v97, v26, v27\n"
      "v_mfma_f32_16x16x32_bf16 v[248:251], v[148:151], a[64:67], 0\n"
      "v_mfma_f32_16x16x32_bf16 v[248:251], v[136:139], a[68:71], v[248:251]\n"
      "v_mfma_f32_16x16x32_bf16 v[248:251], v[140:143], a[72:75], v[248:251]\n"
      "v_mfma_f32_16x16x32_bf16 v[248:251], v[144:147], a[76:79], v[248:251]\n"
      "v_mfma_f32_16x16x32_bf16 v[24:27], v[148:151], a[80:83], 0\n"
      "v_mfma_f32_16x16x32_bf16 v[24:27], v[136:139], a[84:87], v[24:27]\n"
      "v_cvt_pk_bf16_f32 v98, v216, v217\n"
      "v_cvt_pk_bf16_f32 v99, v218, v219\n"
      "v_mfma_f32_16x16x32_bf16 v[24:27], v[140:143], a[88:91], v[24:27]\n"
      "v_mfma_f32_16x16x32_bf16 v[24:27], v[144:147], a[92:95], v[24:27]\n"
      "v_mfma_f32_16x16x32_bf16 v[216:219], v[148:151], a[96:99], 0\n"
      "v_mfma_f32_16x16x32_bf16 v[216:219], v[136:139], a[100:103], v[216:219]\n"
      "v_cvt_pk_bf16_f32 v58, v248, v249\n"
      "v_cvt_pk_bf16_f32 v59, v250, v251\n"
      "v_mfma_f32_16x16x32_bf16 v[216:219], v[140:143], a[104:107], v[216:219]\n"
      "v_mfma_f32_16x16x32_bf16 v[216:219], v[144:147], a[108:111], v[216:219]\n"
      "v_mfma_f32_16x16x32_bf16 v[248:251], v[148:151], a[112:115], 0\n"
      "v_mfma_f32_16x16x32_bf16 v[248:251], v[136:139], a[116:119], v[248:251]\n"
      "v_cvt_pk_bf16_f32 v134, v24, v25\n"
      "v_cvt_pk_bf16_f32 v135, v26, v27\n"
      "v_mfma_f32_16x16x32_bf16 v[248:251], v[140:143], a[120:123], v[248:251]\n"
      "v_mfma_f32_16x16x32_bf16 v[248:251], v[144:147], a[124:127], v[248:251]\n"
      "s_waitcnt vmcnt(37)\n"
      "ds_read_b128 v[24:27], v0 offset:20480\n"
      "ds_read_b128 v[152:155], v0 offset:20992\n"
      "ds_read_b128 v[20:23], v0 offset:21504\n"
      "ds_read_b128 v[156:159], v0 offset:22016\n"
      "v_cvt_pk_bf16_f32 v106, v216, v217\n"
      "v_cvt_pk_bf16_f32 v107, v218, v219\n"
      "s_waitcnt lgkmcnt(7)\n"
      "v_mfma_f32_16x16x32_bf16 v[220:223], a[0:3], v[252:255], 0\n"
      "s_waitcnt lgkmcnt(6)\n"
      "v_mfma_f32_16x16x32_bf16 v[220:223], a[4:7], v[48:51], v[220:223]\n"
      "v_cvt_pk_bf16_f32 v198, v248, v249\n"
      "v_cvt_pk_bf16_f32 v199, v250, v251\n"
      "s_waitcnt lgkmcnt(5)\n"
      "v_mfma_f32_16x16x32_bf16 v[220:223], a[8:11], v[124:127], v[220:223]\n"
      "s_waitcnt lgkmcnt(4)\n"
      "v_mfma_f32_16x16x32_bf16 v[220:223], a[12:15], v[116:119], v[220:223]\n"
      "v_mfma_f32_16x16x32_bf16 v[52:55], a[16:19], v[252:255], 0\n"
      "v_mfma_f32_16x16x32_bf16 v[52:55], a[20:23], v[48:51], v[52:55]\n"
      "v_mfma_f32_16x16x32_bf16 v[52:55], a[24:27], v[124:127], v[52:55]\n"
      "v_mfma_f32_16x16x32_bf16 v[52:55], a[28:31], v[116:119], v[52:55]\n"
      "v_mfma_f32_16x16x32_bf16 v[244:247], a[32:35], v[252:255], 0\n"
      "v_mfma_f32_16x16x32_bf16 v[244:247], a[36:39], v[48:51], v[244:247]\n"
      "v_mfma_f32_16x16x32_bf16 v[244:247], a[40:43], v[124:127], v[244:247]\n"
      "v_mfma_f32_16x16x32_bf16 v[244:247], a[44:47], v[116:119], v[244:247]\n"
      "v_cvt_pk_bf16_f32 v216, v220, v221\n"
      "v_cvt_pk_bf16_f32 v217, v222, v223\n"
      "v_mfma_f32_16x16x32_bf16 v[248:251], a[48:51], v[252:255], 0\n"
      "v_mfma_f32_16x16x32_bf16 v[248:251], a[52:55], v[48:51], v[248:251]\n"
      "v_cvt_pk_bf16_f32 v218, v52, v53\n"
      "v_cvt_pk_bf16_f32 v219, v54, v55\n"
      "v_mfma_f32_16x16x32_bf16 v[248:251], a[56:59], v[124:127], v[248:251]\n"
      "v_mfma_f32_16x16x32_bf16 v[248:251], a[60:63], v[116:119], v[248:251]\n"
      "v_cvt_pk_bf16_f32 v220, v244, v245\n"
      "v_cvt_pk_bf16_f32 v221, v246, v247\n"
      "v_mfma_f32_16x16x32_bf16 v[244:247], v[252:255], a[64:67], 0\n"
      "v_mfma_f32_16x16x32_bf16 v[244:247], v[48:51], a[68:71], v[244:247]\n"
      "v_mfma_f32_16x16x32_bf16 v[244:247], v[124:127], a[72:75], v[244:247]\n"
      "v_mfma_f32_16x16x32_bf16 v[244:247], v[116:119], a[76:79], v[244:247]\n"
      "v_mfma_f32_16x16x32_bf16 v[52:55], v[252:255], a[80:83], 0\n"
      "v_mfma_f32_16x16x32_bf16 v[52:55], v[48:51], a[84:87], v[52:55]\n"
      "v_cvt_pk_bf16_f32 v222, v248, v249\n"
      "v_cvt_pk_bf16_f32 v223, v250, v251\n"
      "v_mfma_f32_16x16x32_bf16 v[52:55], v[124:127], a[88:91], v[52:55]\n"
      "v_mfma_f32_16x16x32_bf16 v[52:55], v[116:119], a[92:95], v[52:55]\n"
      "v_mfma_f32_16x16x32_bf16 v[248:251], v[252:255], a[96:99], 0\n"
      "v_mfma_f32_16x16x32_bf16 v[248:251], v[48:51], a[100:103], v[248:251]\n"
      "v_cvt_pk_bf16_f32 v120, v244, v245\n"
      "v_cvt_pk_bf16_f32 v121, v246, v247\n"
      "v_mfma_f32_16x16x32_bf16 v[248:251], v[124:127], a[104:107], v[248:251]\n"
      "v_mfma_f32_16x16x32_bf16 v[248:251], v[116:119], a[108:111], v[248:251]\n"
      "v_mfma_f32_16x16x32_bf16 v[244:247], v[252:255], a[112:115], 0\n"
      "v_mfma_f32_16x16x32_bf16 v[244:247], v[48:51], a[116:119], v[244:247]\n"
      "v_cvt_pk_bf16_f32 v68, v52, v53\n"
      "v_cvt_pk_bf16_f32 v69, v54, v55\n"
      "v_mfma_f32_16x16x32_bf16 v[244:247], v[124:127], a[120:123], v[244:247]\n"
      "v_mfma_f32_16x16x32_bf16 v[244:247], v[116:119], a[124:127], v[244:247]\n"
      "s_waitcnt vmcnt(25)\n"
      "ds_read_b128 v[52:55], v0 offset:8192\n"
      "ds_read_b128 v[128:131], v0 offset:8704\n"
      "ds_read_b128 v[168:171], v0 offset:9216\n"
      "ds_read_b128 v[164:167], v0 offset:9728\n"
      "v_cvt_pk_bf16_f32 v36, v248, v249\n"
      "v_cvt_pk_bf16_f32 v37, v250, v251\n"
      "s_waitcnt lgkmcnt(7)\n"
      "v_mfma_f32_16x16x32_bf16 v[248:251], a[0:3], v[24:27], 0\n"
      "s_add_u32 m0, s53, 12288\n"
      "s_add_u32 s54, s49, 12288\n"
      "buffer_load_dwordx4 v1, s[40:43], s54 offen offset:0 lds\n"
      "v_cvt_pk_bf16_f32 v240, v244, v245\n"
      "v_cvt_pk_bf16_f32 v241, v246, v247\n"
      "s_waitcnt lgkmcnt(6)\n"
      "v_mfma_f32_16x16x32_bf16 v[248:251], a[4:7], v[152:155], v[248:251]\n"
      "s_waitcnt lgkmcnt(5)\n"
      "v_mfma_f32_16x16x32_bf16 v[248:251], a[8:11], v[20:23], v[248:251]\n"
      "buffer_load_dwordx4 v1, s[40:43], s54 offen offset:2048 lds\n"
      "s_add_u32 m0, s53, 13184\n"
      "s_waitcnt lgkmcnt(4)\n"
      "v_mfma_f32_16x16x32_bf16 v[248:251], a[12:15], v[156:159], v[248:251]\n"
      "v_mfma_f32_16x16x32_bf16 v[40:43], a[16:19], v[24:27], 0\n"
      "buffer_load_dwordx4 v1, s[40:43], s54 offen offset:128 lds\n"
      "v_mfma_f32_16x16x32_bf16 v[40:43], a[20:23], v[152:155], v[40:43]\n"
      "v_mfma_f32_16x16x32_bf16 v[40:43], a[24:27], v[20:23], v[40:43]\n"
      "buffer_load_dwordx4 v1, s[40:43], s54 offen offset:2176 lds\n"
      "v_mfma_f32_16x16x32_bf16 v[40:43], a[28:31], v[156:159], v[40:43]\n"
      "v_mfma_f32_16x16x32_bf16 v[100:103], a[32:35], v[24:27], 0\n"
      "v_mfma_f32_16x16x32_bf16 v[100:103], a[36:39], v[152:155], v[100:103]\n"
      "v_cvt_pk_bf16_f32 v244, v248, v249\n"
      "v_cvt_pk_bf16_f32 v245, v250, v251\n"
      "v_mfma_f32_16x16x32_bf16 v[100:103], a[40:43], v[20:23], v[100:103]\n"
      "v_mfma_f32_16x16x32_bf16 v[100:103], a[44:47], v[156:159], v[100:103]\n"
      "v_mfma_f32_16x16x32_bf16 v[44:47], a[48:51], v[24:27], 0\n"
      "v_mfma_f32_16x16x32_bf16 v[44:47], a[52:55], v[152:155], v[44:47]\n"
      "v_cvt_pk_bf16_f32 v246, v40, v41\n"
      "v_cvt_pk_bf16_f32 v247, v42, v43\n"
      "v_mfma_f32_16x16x32_bf16 v[44:47], a[56:59], v[20:23], v[44:47]\n"
      "v_mfma_f32_16x16x32_bf16 v[44:47], a[60:63], v[156:159], v[44:47]\n"
      "v_mfma_f32_16x16x32_bf16 v[40:43], v[24:27], a[64:67], 0\n"
      "v_mfma_f32_16x16x32_bf16 v[40:43], v[152:155], a[68:71], v[40:43]\n"
      "v_cvt_pk_bf16_f32 v248, v100, v101\n"
      "v_cvt_pk_bf16_f32 v249, v102, v103\n"
      "v_mfma_f32_16x16x32_bf16 v[40:43], v[20:23], a[72:75], v[40:43]\n"
      "v_mfma_f32_16x16x32_bf16 v[40:43], v[156:159], a[76:79], v[40:43]\n"
      "v_mfma_f32_16x16x32_bf16 v[100:103], v[24:27], a[80:83], 0\n"
      "v_mfma_f32_16x16x32_bf16 v[100:103], v[152:155], a[84:87], v[100:103]\n"
      "v_cvt_pk_bf16_f32 v250, v44, v45\n"
      "v_cvt_pk_bf16_f32 v251, v46, v47\n"
      "v_mfma_f32_16x16x32_bf16 v[100:103], v[20:23], a[88:91], v[100:103]\n"
      "v_mfma_f32_16x16x32_bf16 v[100:103], v[156:159], a[92:95], v[100:103]\n"
      "v_mfma_f32_16x16x32_bf16 v[44:47], v[24:27], a[96:99], 0\n"
      "v_mfma_f32_16x16x32_bf16 v[44:47], v[152:155], a[100:103], v[44:47]\n"
      "v_cvt_pk_bf16_f32 v122, v40, v41\n"
      "v_cvt_pk_bf16_f32 v123, v42, v43\n"
      "v_mfma_f32_16x16x32_bf16 v[44:47], v[20:23], a[104:107], v[44:47]\n"
      "v_mfma_f32_16x16x32_bf16 v[44:47], v[156:159], a[108:111], v[44:47]\n"
      "v_mfma_f32_16x16x32_bf16 v[40:43], v[24:27], a[112:115], 0\n"
      "v_mfma_f32_16x16x32_bf16 v[40:43], v[152:155], a[116:119], v[40:43]\n"
      "v_cvt_pk_bf16_f32 v70, v100, v101\n"
      "v_cvt_pk_bf16_f32 v71, v102, v103\n"
      "v_mfma_f32_16x16x32_bf16 v[40:43], v[20:23], a[120:123], v[40:43]\n"
      "v_mfma_f32_16x16x32_bf16 v[40:43], v[156:159], a[124:127], v[40:43]\n"
      "s_waitcnt vmcnt(21)\n"
      "ds_read_b128 v[100:103], v0 offset:4096\n"
      "ds_read_b128 v[108:111], v0 offset:4608\n"
      "ds_read_b128 v[176:179], v0 offset:5120\n"
      "ds_read_b128 v[172:175], v0 offset:5632\n"
      "v_cvt_pk_bf16_f32 v38, v44, v45\n"
      "v_cvt_pk_bf16_f32 v39, v46, v47\n"
      "s_waitcnt lgkmcnt(7)\n"
      "v_mfma_f32_16x16x32_bf16 v[44:47], a[0:3], v[52:55], 0\n"
      "s_add_u32 m0, s53, 16384\n"
      "s_add_u32 s54, s49, 16384\n"
      "buffer_load_dwordx4 v1, s[40:43], s54 offen offset:0 nt lds\n"
      "v_cvt_pk_bf16_f32 v242, v40, v41\n"
      "v_cvt_pk_bf16_f32 v243, v42, v43\n"
      "s_waitcnt lgkmcnt(6)\n"
      "v_mfma_f32_16x16x32_bf16 v[44:47], a[4:7], v[128:131], v[44:47]\n"
      "s_waitcnt lgkmcnt(5)\n"
      "v_mfma_f32_16x16x32_bf16 v[44:47], a[8:11], v[168:171], v[44:47]\n"
      "buffer_load_dwordx4 v1, s[40:43], s54 offen offset:2048 nt lds\n"
      "s_add_u32 m0, s53, 17280\n"
      "s_waitcnt lgkmcnt(4)\n"
      "v_mfma_f32_16x16x32_bf16 v[44:47], a[12:15], v[164:167], v[44:47]\n"
      "v_mfma_f32_16x16x32_bf16 v[112:115], a[16:19], v[52:55], 0\n"
      "buffer_load_dwordx4 v1, s[40:43], s54 offen offset:128 nt lds\n"
      "v_mfma_f32_16x16x32_bf16 v[112:115], a[20:23], v[128:131], v[112:115]\n"
      "v_mfma_f32_16x16x32_bf16 v[112:115], a[24:27], v[168:171], v[112:115]\n"
      "buffer_load_dwordx4 v1, s[40:43], s54 offen offset:2176 nt lds\n"
      "v_mfma_f32_16x16x32_bf16 v[112:115], a[28:31], v[164:167], v[112:115]\n"
      "v_mfma_f32_16x16x32_bf16 v[192:195], a[32:35], v[52:55], 0\n"
      "v_mfma_f32_16x16x32_bf16 v[192:195], a[36:39], v[128:131], v[192:195]\n"
      "v_cvt_pk_bf16_f32 v40, v44, v45\n"
      "v_cvt_pk_bf16_f32 v41, v46, v47\n"
      "v_mfma_f32_16x16x32_bf16 v[192:195], a[40:43], v[168:171], v[192:195]\n"
      "v_mfma_f32_16x16x32_bf16 v[192:195], a[44:47], v[164:167], v[192:195]\n"
      "v_mfma_f32_16x16x32_bf16 v[224:227], a[48:51], v[52:55], 0\n"
      "v_mfma_f32_16x16x32_bf16 v[224:227], a[52:55], v[128:131], v[224:227]\n"
      "v_cvt_pk_bf16_f32 v42, v112, v113\n"
      "v_cvt_pk_bf16_f32 v43, v114, v115\n"
      "v_mfma_f32_16x16x32_bf16 v[224:227], a[56:59], v[168:171], v[224:227]\n"
      "v_mfma_f32_16x16x32_bf16 v[224:227], a[60:63], v[164:167], v[224:227]\n"
      "v_mfma_f32_16x16x32_bf16 v[32:35], v[52:55], a[64:67], 0\n"
      "v_mfma_f32_16x16x32_bf16 v[32:35], v[128:131], a[68:71], v[32:35]\n"
      "v_cvt_pk_bf16_f32 v44, v192, v193\n"
      "v_cvt_pk_bf16_f32 v45, v194, v195\n"
      "v_mfma_f32_16x16x32_bf16 v[32:35], v[168:171], a[72:75], v[32:35]\n"
      "v_mfma_f32_16x16x32_bf16 v[32:35], v[164:167], a[76:79], v[32:35]\n"
      "v_mfma_f32_16x16x32_bf16 v[160:163], v[52:55], a[80:83], 0\n"
      "v_mfma_f32_16x16x32_bf16 v[160:163], v[128:131], a[84:87], v[160:163]\n"
      "v_cvt_pk_bf16_f32 v46, v224, v225\n"
      "v_cvt_pk_bf16_f32 v47, v226, v227\n"
      "v_mfma_f32_16x16x32_bf16 v[160:163], v[168:171], a[88:91], v[160:163]\n"
      "v_mfma_f32_16x16x32_bf16 v[160:163], v[164:167], a[92:95], v[160:163]\n"
      "v_mfma_f32_16x16x32_bf16 v[112:115], v[52:55], a[96:99], 0\n"
      "v_mfma_f32_16x16x32_bf16 v[112:115], v[128:131], a[100:103], v[112:115]\n"
      "v_cvt_pk_bf16_f32 v56, v32, v33\n"
      "v_cvt_pk_bf16_f32 v57, v34, v35\n"
      "v_mfma_f32_16x16x32_bf16 v[112:115], v[168:171], a[104:107], v[112:115]\n"
      "v_mfma_f32_16x16x32_bf16 v[112:115], v[164:167], a[108:111], v[112:115]\n"
      "v_mfma_f32_16x16x32_bf16 v[192:195], v[52:55], a[112:115], 0\n"
      "v_mfma_f32_16x16x32_bf16 v[192:195], v[128:131], a[116:119], v[192:195]\n"
      "v_cvt_pk_bf16_f32 v132, v160, v161\n"
      "v_cvt_pk_bf16_f32 v133, v162, v163\n"
      "v_mfma_f32_16x16x32_bf16 v[192:195], v[168:171], a[120:123], v[192:195]\n"
      "v_mfma_f32_16x16x32_bf16 v[192:195], v[164:167], a[124:127], v[192:195]\n"
      "s_waitcnt vmcnt(17)\n"
      "ds_read_b128 v[224:227], v0 offset:0\n"
      "ds_read_b128 v[32:35], v0 offset:512\n"
      "ds_read_b128 v[160:163], v0 offset:1024\n"
      "ds_read_b128 v[28:31], v0 offset:1536\n"
      "v_cvt_pk_bf16_f32 v104, v112, v113\n"
      "v_cvt_pk_bf16_f32 v105, v114, v115\n"
      "s_waitcnt lgkmcnt(7)\n"
      "v_mfma_f32_16x16x32_bf16 v[168:171], a[0:3], v[100:103], 0\n"
      "s_add_u32 m0, s53, 20480\n"
      "s_add_u32 s54, s49, 20480\n"
      "buffer_load_dwordx4 v1, s[40:43], s54 offen offset:0 lds\n"
      "v_cvt_pk_bf16_f32 v196, v192, v193\n"
      "v_cvt_pk_bf16_f32 v197, v194, v195\n"
      "s_waitcnt lgkmcnt(6)\n"
      "v_mfma_f32_16x16x32_bf16 v[168:171], a[4:7], v[108:111], v[168:171]\n"
      "s_waitcnt lgkmcnt(5)\n"
      "v_mfma_f32_16x16x32_bf16 v[168:171], a[8:11], v[176:179], v[168:171]\n"
      "buffer_load_dwordx4 v1, s[40:43], s54 offen offset:2048 lds\n"
      "s_add_u32 m0, s53, 21376\n"
      "s_waitcnt lgkmcnt(4)\n"
      "v_mfma_f32_16x16x32_bf16 v[168:171], a[12:15], v[172:175], v[168:171]\n"
      "v_mfma_f32_16x16x32_bf16 v[112:115], a[16:19], v[100:103], 0\n"
      "buffer_load_dwordx4 v1, s[40:43], s54 offen offset:128 lds\n"
      "v_mfma_f32_16x16x32_bf16 v[112:115], a[20:23], v[108:111], v[112:115]\n"
      "v_mfma_f32_16x16x32_bf16 v[112:115], a[24:27], v[176:179], v[112:115]\n"
      "buffer_load_dwordx4 v1, s[40:43], s54 offen offset:2176 lds\n"
      "v_mfma_f32_16x16x32_bf16 v[112:115], a[28:31], v[172:175], v[112:115]\n"
      "v_mfma_f32_16x16x32_bf16 v[192:195], a[32:35], v[100:103], 0\n"
      "v_mfma_f32_16x16x32_bf16 v[192:195], a[36:39], v[108:111], v[192:195]\n"
      "v_cvt_pk_bf16_f32 v164, v168, v169\n"
      "v_cvt_pk_bf16_f32 v165, v170, v171\n"
      "v_mfma_f32_16x16x32_bf16 v[192:195], a[40:43], v[176:179], v[192:195]\n"
      "v_mfma_f32_16x16x32_bf16 v[192:195], a[44:47], v[172:175], v[192:195]\n"
      "v_mfma_f32_16x16x32_bf16 v[52:55], a[48:51], v[100:103], 0\n"
      "v_mfma_f32_16x16x32_bf16 v[52:55], a[52:55], v[108:111], v[52:55]\n"
      "v_cvt_pk_bf16_f32 v166, v112, v113\n"
      "v_cvt_pk_bf16_f32 v167, v114, v115\n"
      "v_mfma_f32_16x16x32_bf16 v[52:55], a[56:59], v[176:179], v[52:55]\n"
      "v_mfma_f32_16x16x32_bf16 v[52:55], a[60:63], v[172:175], v[52:55]\n"
      "v_mfma_f32_16x16x32_bf16 v[112:115], v[100:103], a[64:67], 0\n"
      "v_mfma_f32_16x16x32_bf16 v[112:115], v[108:111], a[68:71], v[112:115]\n"
      "v_cvt_pk_bf16_f32 v168, v192, v193\n"
      "v_cvt_pk_bf16_f32 v169, v194, v195\n"
      "v_mfma_f32_16x16x32_bf16 v[112:115], v[176:179], a[72:75], v[112:115]\n"
      "v_mfma_f32_16x16x32_bf16 v[112:115], v[172:175], a[76:79], v[112:115]\n"
      "v_mfma_f32_16x16x32_bf16 v[192:195], v[100:103], a[80:83], 0\n"
      "v_mfma_f32_16x16x32_bf16 v[192:195], v[108:111], a[84:87], v[192:195]\n"
      "v_cvt_pk_bf16_f32 v170, v52, v53\n"
      "v_cvt_pk_bf16_f32 v171, v54, v55\n"
      "v_mfma_f32_16x16x32_bf16 v[192:195], v[176:179], a[88:91], v[192:195]\n"
      "v_mfma_f32_16x16x32_bf16 v[192:195], v[172:175], a[92:95], v[192:195]\n"
      "v_mfma_f32_16x16x32_bf16 v[88:91], v[100:103], a[96:99], 0\n"
      "v_mfma_f32_16x16x32_bf16 v[88:91], v[108:111], a[100:103], v[88:91]\n"
      "v_cvt_pk_bf16_f32 v54, v112, v113\n"
      "v_cvt_pk_bf16_f32 v55, v114, v115\n"
      "v_mfma_f32_16x16x32_bf16 v[88:91], v[176:179], a[104:107], v[88:91]\n"
      "v_mfma_f32_16x16x32_bf16 v[88:91], v[172:175], a[108:111], v[88:91]\n"
      "v_mfma_f32_16x16x32_bf16 v[112:115], v[100:103], a[112:115], 0\n"
      "v_mfma_f32_16x16x32_bf16 v[112:115], v[108:111], a[116:119], v[112:115]\n"
      "v_cvt_pk_bf16_f32 v130, v192, v193\n"
      "v_cvt_pk_bf16_f32 v131, v194, v195\n"
      "v_mfma_f32_16x16x32_bf16 v[112:115], v[176:179], a[120:123], v[112:115]\n"
      "v_mfma_f32_16x16x32_bf16 v[112:115], v[172:175], a[124:127], v[112:115]\n"
      "ds_read_b128 v[172:175], v9 offset:0\n"
      "ds_read_b128 v[176:179], v9 offset:2048\n"
      "v_cvt_pk_bf16_f32 v102, v88, v89\n"
      "v_cvt_pk_bf16_f32 v103, v90, v91\n"
      "s_waitcnt lgkmcnt(5)\n"
      "v_mfma_f32_16x16x32_bf16 v[88:91], a[0:3], v[224:227], 0\n"
      "s_waitcnt lgkmcnt(4)\n"
      "v_mfma_f32_16x16x32_bf16 v[88:91], a[4:7], v[32:35], v[88:91]\n"
      "s_waitcnt lgkmcnt(3)\n"
      "v_mfma_f32_16x16x32_bf16 v[88:91], a[8:11], v[160:163], v[88:91]\n"
      "s_waitcnt lgkmcnt(2)\n"
      "v_mfma_f32_16x16x32_bf16 v[88:91], a[12:15], v[28:31], v[88:91]\n"
      "v_cvt_pk_bf16_f32 v194, v112, v113\n"
      "v_cvt_pk_bf16_f32 v195, v114, v115\n"
      "v_mfma_f32_16x16x32_bf16 v[112:115], a[16:19], v[224:227], 0\n"
      "v_mfma_f32_16x16x32_bf16 v[112:115], a[20:23], v[32:35], v[112:115]\n"
      "v_mfma_f32_16x16x32_bf16 v[112:115], a[24:27], v[160:163], v[112:115]\n"
      "v_mfma_f32_16x16x32_bf16 v[112:115], a[28:31], v[28:31], v[112:115]\n"
      "v_mfma_f32_16x16x32_bf16 v[190:193], a[32:35], v[224:227], 0\n"
      "v_mfma_f32_16x16x32_bf16 v[190:193], a[36:39], v[32:35], v[190:193]\n"
      "v_cvt_pk_bf16_f32 v108, v88, v89\n"
      "v_cvt_pk_bf16_f32 v109, v90, v91\n"
      "v_mfma_f32_16x16x32_bf16 v[190:193], a[40:43], v[160:163], v[190:193]\n"
      "v_mfma_f32_16x16x32_bf16 v[190:193], a[44:47], v[28:31], v[190:193]\n"
      "v_mfma_f32_16x16x32_bf16 v[88:91], a[48:51], v[224:227], 0\n"
      "v_mfma_f32_16x16x32_bf16 v[88:91], a[52:55], v[32:35], v[88:91]\n"
      "v_cvt_pk_bf16_f32 v110, v112, v113\n"
      "v_cvt_pk_bf16_f32 v111, v114, v115\n"
      "v_mfma_f32_16x16x32_bf16 v[88:91], a[56:59], v[160:163], v[88:91]\n"
      "v_mfma_f32_16x16x32_bf16 v[88:91], a[60:63], v[28:31], v[88:91]\n"
      "v_add_u32 v9, 64, v9\n"
      "v_mfma_f32_16x16x32_bf16 v[228:231], v[224:227], a[64:67], 0\n"
      "v_cvt_pk_bf16_f32 v112, v190, v191\n"
      "v_cvt_pk_bf16_f32 v113, v192, v193\n"
      "v_mfma_f32_16x16x32_bf16 v[228:231], v[32:35], a[68:71], v[228:231]\n"
      "v_mfma_f32_16x16x32_bf16 v[228:231], v[160:163], a[72:75], v[228:231]\n"
      "v_mfma_f32_16x16x32_bf16 v[228:231], v[28:31], a[76:79], v[228:231]\n"
      "v_mfma_f32_16x16x32_bf16 v[190:193], v[224:227], a[80:83], 0\n"
      "v_cvt_pk_bf16_f32 v114, v88, v89\n"
      "v_cvt_pk_bf16_f32 v115, v90, v91\n"
      "v_mfma_f32_16x16x32_bf16 v[190:193], v[32:35], a[84:87], v[190:193]\n"
      "v_mfma_f32_16x16x32_bf16 v[190:193], v[160:163], a[88:91], v[190:193]\n"
      "v_mfma_f32_16x16x32_bf16 v[190:193], v[28:31], a[92:95], v[190:193]\n"
      "v_mfma_f32_16x16x32_bf16 v[88:91], v[224:227], a[96:99], 0\n"
      "v_mfma_f32_16x16x32_bf16 v[88:91], v[32:35], a[100:103], v[88:91]\n"
      "v_cvt_pk_bf16_f32 v52, v228, v229\n"
      "v_cvt_pk_bf16_f32 v53, v230, v231\n"
      "v_mfma_f32_16x16x32_bf16 v[88:91], v[160:163], a[104:107], v[88:91]\n"
      "v_mfma_f32_16x16x32_bf16 v[88:91], v[28:31], a[108:111], v[88:91]\n"
      "v_mfma_f32_16x16x32_bf16 v[228:231], v[224:227], a[112:115], 0\n"
      "v_mfma_f32_16x16x32_bf16 v[228:231], v[32:35], a[116:119], v[228:231]\n"
      "v_cvt_pk_bf16_f32 v128, v190, v191\n"
      "v_cvt_pk_bf16_f32 v129, v192, v193\n"
      "v_mfma_f32_16x16x32_bf16 v[228:231], v[160:163], a[120:123], v[228:231]\n"
      "v_mfma_f32_16x16x32_bf16 v[228:231], v[28:31], a[124:127], v[228:231]\n"
      "ds_read_b64 v[18:19], v11 offset:0\n"
      "v_mfma_f32_16x16x32_bf16 v[28:31], a[128:131], v[24:27], 0\n"
      "v_cvt_pk_bf16_f32 v100, v88, v89\n"
      "v_cvt_pk_bf16_f32 v101, v90, v91\n"
      "v_mfma_f32_16x16x32_bf16 v[28:31], a[132:135], v[152:155], v[28:31]\n"
      "v_mfma_f32_16x16x32_bf16 v[28:31], a[136:139], v[20:23], v[28:31]\n"
      "v_mfma_f32_16x16x32_bf16 v[28:31], a[140:143], v[156:159], v[28:31]\n"
      "v_mfma_f32_16x16x32_bf16 v[88:91], a[144:147], v[24:27], 0\n"
      "v_cvt_pk_bf16_f32 v192, v228, v229\n"
      "v_cvt_pk_bf16_f32 v193, v230, v231\n"
      "v_mfma_f32_16x16x32_bf16 v[88:91], a[148:151], v[152:155], v[88:91]\n"
      "v_mfma_f32_16x16x32_bf16 v[88:91], a[152:155], v[20:23], v[88:91]\n"
      "v_mfma_f32_16x16x32_bf16 v[88:91], a[156:159], v[156:159], v[88:91]\n"
      "v_mfma_f32_16x16x32_bf16 v[228:231], a[160:163], v[24:27], 0\n"
      "v_mfma_f32_16x16x32_bf16 v[228:231], a[164:167], v[152:155], v[228:231]\n"
      "v_cvt_pk_bf16_f32 v160, v28, v29\n"
      "v_cvt_pk_bf16_f32 v161, v30, v31\n"
      "v_mfma_f32_16x16x32_bf16 v[228:231], a[168:171], v[20:23], v[228:231]\n"
      "v_mfma_f32_16x16x32_bf16 v[228:231], a[172:175], v[156:159], v[228:231]\n"
      "v_mfma_f32_16x16x32_bf16 v[32:35], a[176:179], v[24:27], 0\n"
      "v_mfma_f32_16x16x32_bf16 v[32:35], a[180:183], v[152:155], v[32:35]\n"
      "v_cvt_pk_bf16_f32 v162, v88, v89\n"
      "v_cvt_pk_bf16_f32 v163, v90, v91\n"
      "v_mfma_f32_16x16x32_bf16 v[32:35], a[184:187], v[20:23], v[32:35]\n"
      "v_mfma_f32_16x16x32_bf16 v[32:35], a[188:191], v[156:159], v[32:35]\n"
      "s_waitcnt lgkmcnt(2)\n"
      "v_mfma_f32_16x16x32_bf16 v[28:31], v[172:175], v[160:163], 0\n"
      "v_cvt_pk_bf16_f32 v88, v228, v229\n"
      "v_cvt_pk_bf16_f32 v89, v230, v231\n"
      "s_nop 3\n"
      "v_cvt_pk_bf16_f32 v90, v32, v33\n"
      "v_cvt_pk_bf16_f32 v91, v34, v35\n"
      "s_waitcnt lgkmcnt(1)\n"
      "s_nop 0\n"
      "v_mfma_f32_16x16x32_bf16 v[28:31], v[176:179], v[88:91], v[28:31]\n"
      "v_mfma_f32_16x16x32_bf16 v[228:231], v[108:111], v[160:163], 0\n"
      "v_mfma_f32_16x16x32_bf16 v[228:231], v[112:115], v[88:91], v[228:231]\n"
      "v_mfma_f32_16x16x32_bf16 v[32:35], v[164:167], v[160:163], 0\n"
      "v_mfma_f32_16x16x32_bf16 v[32:35], v[168:171], v[88:91], v[32:35]\n"
      "v_mfma_f32_16x16x32_bf16 v[224:227], v[40:43], v[160:163], 0\n"
      "v_mfma_f32_16x16x32_bf16 v[224:227], v[44:47], v[88:91], v[224:227]\n"
      "v_mfma_f32_16x16x32_bf16 v[200:203], v[92:95], v[160:163], 0\n"
      "v_mfma_f32_16x16x32_bf16 v[200:203], v[96:99], v[88:91], v[200:203]\n"
      "v_max_f32 v190, v28, v29\n"
      "v_max3_f32 v190, v190, v30, v31\n"
      "v_max3_f32 v190, v190, v228, v229\n"
      "v_max3_f32 v190, v190, v230, v231\n"
      "v_max3_f32 v190, v190, v32, v33\n"
      "v_mfma_f32_16x16x32_bf16 v[180:183], v[216:219], v[160:163], 0\n"
      "v_max3_f32 v190, v190, v34, v35\n"
      "v_max3_f32 v190, v190, v224, v225\n"
      "v_max3_f32 v190, v190, v226, v227\n"
      "v_max3_f32 v190, v190, v200, v201\n"
      "v_max3_f32 v190, v190, v202, v203\n"
      "v_mfma_f32_16x16x32_bf16 v[180:183], v[220:223], v[88:91], v[180:183]\n"
      "v_mfma_f32_16x16x32_bf16 v[208:211], v[244:247], v[160:163], 0\n"
      "v_mfma_f32_16x16x32_bf16 v[208:211], v[248:251], v[88:91], v[208:211]\n"
      "v_mfma_f32_16x16x32_bf16 v[184:187], a[128:131], v[252:255], 0\n"
      "v_mfma_f32_16x16x32_bf16 v[184:187], a[132:135], v[48:51], v[184:187]\n"
      "v_mfma_f32_16x16x32_bf16 v[184:187], a[136:139], v[124:127], v[184:187]\n"
      "v_mfma_f32_16x16x32_bf16 v[184:187], a[140:143], v[116:119], v[184:187]\n"
      "v_mfma_f32_16x16x32_bf16 v[88:91], a[144:147], v[252:255], 0\n"
      "v_mfma_f32_16x16x32_bf16 v[88:91], a[148:151], v[48:51], v[88:91]\n"
      "v_max3_f32 v190, v190, v180, v181\n"
      "v_max3_f32 v190, v190, v182, v183\n"
      "v_max3_f32 v190, v190, v208, v209\n"
      "v_max3_f32 v190, v190, v210, v211\n"
      "v_mov_b32 v162, v190\n"
      "v_mfma_f32_16x16x32_bf16 v[88:91], a[152:155], v[124:127], v[88:91]\n"
      "v_cvt_pk_bf16_f32 v160, v184, v185\n"
      "v_permlane16_swap_b32 v190, v162\n"
      "v_max_f32 v190, v190, v162\n"
      "v_mov_b32 v162, v190\n"
      "v_cvt_pk_bf16_f32 v161, v186, v187\n"
      "v_mfma_f32_16x16x32_bf16 v[88:91], a[156:159], v[116:119], v[88:91]\n"
      "v_permlane32_swap_b32 v190, v162\n"
      "v_max_f32 v190, v190, v162\n"
      "v_sub_f32 v28, v28, v190\n"
      "v_sub_f32 v29, v29, v190\n"
      "v_sub_f32 v30, v30, v190\n"
      "v_sub_f32 v31, v31, v190\n"
      "v_exp_f32 v28, v28\n"
      "v_mfma_f32_16x16x32_bf16 v[184:187], a[160:163], v[252:255], 0\n"
      "v_exp_f32 v29, v29\n"
      "v_exp_f32 v30, v30\n"
      "v_exp_f32 v31, v31\n"
      "v_cvt_pk_bf16_f32 v162, v28, v29\n"
      "v_cvt_pk_bf16_f32 v163, v30, v31\n"
      "v_mfma_f32_16x16x32_bf16 v[184:187], a[164:167], v[48:51], v[184:187]\n"
      "v_sub_f32 v228, v228, v190\n"
      "v_sub_f32 v229, v229, v190\n"
      "v_sub_f32 v230, v230, v190\n"
      "v_sub_f32 v231, v231, v190\n"
      "v_sub_f32 v32, v32, v190\n"
      "v_sub_f32 v33, v33, v190\n"
      "v_sub_f32 v34, v34, v190\n"
      "v_sub_f32 v35, v35, v190\n"
      "v_mfma_f32_16x16x32_bf16 v[184:187], a[168:171], v[124:127], v[184:187]\n"
      "v_sub_f32 v224, v224, v190\n"
      "v_sub_f32 v225, v225, v190\n"
      "v_sub_f32 v226, v226, v190\n"
      "v_sub_f32 v227, v227, v190\n"
      "v_sub_f32 v200, v200, v190\n"
      "v_sub_f32 v201, v201, v190\n"
      "v_sub_f32 v202, v202, v190\n"
      "v_sub_f32 v203, v203, v190\n"
      "v_mfma_f32_16x16x32_bf16 v[184:187], a[172:175], v[116:119], v[184:187]\n"
      "v_add_f32 v188, v28, v29\n"
      "v_add_f32 v189, v30, v31\n"
      "v_exp_f32 v228, v228\n"
      "v_exp_f32 v229, v229\n"
      "v_exp_f32 v230, v230\n"
      "v_cvt_pk_bf16_f32 v28, v228, v229\n"
      "v_mfma_f32_16x16x32_bf16 v[84:87], a[176:179], v[252:255], 0\n"
      "v_exp_f32 v231, v231\n"
      "v_exp_f32 v32, v32\n"
      "v_cvt_pk_bf16_f32 v29, v230, v231\n"
      "v_exp_f32 v33, v33\n"
      "v_mfma_f32_16x16x32_bf16 v[84:87], a[180:183], v[48:51], v[84:87]\n"
      "v_exp_f32 v34, v34\n"
      "v_exp_f32 v35, v35\n"
      "v_cvt_pk_bf16_f32 v30, v32, v33\n"
      "v_cvt_pk_bf16_f32 v31, v34, v35\n"
      "v_exp_f32 v224, v224\n"
      "v_sub_f32 v180, v180, v190\n"
      "v_mfma_f32_16x16x32_bf16 v[84:87], a[184:187], v[124:127], v[84:87]\n"
      "v_exp_f32 v225, v225\n"
      "v_exp_f32 v226, v226\n"
      "v_exp_f32 v227, v227\n"
      "v_cvt_pk_bf16_f32 v32, v224, v225\n"
      "v_cvt_pk_bf16_f32 v33, v226, v227\n"
      "v_mfma_f32_16x16x32_bf16 v[84:87], a[188:191], v[116:119], v[84:87]\n"
      "v_exp_f32 v200, v200\n"
      "v_exp_f32 v201, v201\n"
      "v_exp_f32 v202, v202\n"
      "v_exp_f32 v203, v203\n"
      "s_waitcnt lgkmcnt(0)\n"
      "v_mfma_f32_16x16x4_4b_bf16 v[224:239], v[162:163], v[18:19], 0 cbsz:2 abid:0\n"
      "v_cvt_pk_bf16_f32 v34, v200, v201\n"
      "v_cvt_pk_bf16_f32 v35, v202, v203\n"
      "v_sub_f32 v181, v181, v190\n"
      "v_sub_f32 v182, v182, v190\n"
      "v_sub_f32 v183, v183, v190\n"
      "v_sub_f32 v208, v208, v190\n"
      "v_sub_f32 v209, v209, v190\n"
      "v_sub_f32 v210, v210, v190\n"
      "v_sub_f32 v211, v211, v190\n"
      "v_exp_f32 v180, v180\n"
      "v_exp_f32 v181, v181\n"
      "v_exp_f32 v182, v182\n"
      "v_exp_f32 v183, v183\n"
      "v_cvt_pk_bf16_f32 v200, v180, v181\n"
      "v_cvt_pk_bf16_f32 v201, v182, v183\n"
      "v_exp_f32 v208, v208\n"
      "v_exp_f32 v209, v209\n"
      "v_smfmac_f32_16x16x64_bf16 v[188:191], v[12:15], v[28:35], v5\n"
      "v_exp_f32 v210, v210\n"
      "v_exp_f32 v211, v211\n"
      "v_mfma_f32_16x16x32_bf16 v[224:227], v[28:31], v[52:55], v[224:227]\n"
      "v_cvt_pk_bf16_f32 v202, v208, v209\n"
      "v_cvt_pk_bf16_f32 v203, v210, v211\n"
      "v_cvt_pk_bf16_f32 v162, v88, v89\n"
      "v_cvt_pk_bf16_f32 v163, v90, v91\n"
      "v_mfma_f32_16x16x32_bf16 v[224:227], v[32:35], v[56:59], v[224:227]\n"
      "v_cvt_pk_bf16_f32 v88, v184, v185\n"
      "v_cvt_pk_bf16_f32 v89, v186, v187\n"
      "v_cvt_pk_bf16_f32 v90, v84, v85\n"
      "v_mfma_f32_16x16x32_bf16 v[224:227], v[200:203], v[120:123], v[224:227]\n"
      "v_cvt_pk_bf16_f32 v91, v86, v87\n"
      "v_smfmac_f32_16x16x32_bf16 v[188:191], v[12:13], v[200:203], v5\n"
      "v_mfma_f32_16x16x32_bf16 v[228:231], v[28:31], v[128:131], v[228:231]\n"
      "v_mfma_f32_16x16x32_bf16 v[228:231], v[32:35], v[132:135], v[228:231]\n"
      "v_mfma_f32_16x16x32_bf16 v[228:231], v[200:203], v[68:71], v[228:231]\n"
      "v_mfma_f32_16x16x32_bf16 v[232:235], v[28:31], v[100:103], v[232:235]\n"
      "v_mfma_f32_16x16x32_bf16 v[232:235], v[32:35], v[104:107], v[232:235]\n"
      "v_mfma_f32_16x16x32_bf16 v[232:235], v[200:203], v[36:39], v[232:235]\n"
      "v_mfma_f32_16x16x32_bf16 v[236:239], v[28:31], v[192:195], v[236:239]\n"
      "v_mfma_f32_16x16x32_bf16 v[236:239], v[32:35], v[196:199], v[236:239]\n"
      "v_add_f32 v188, v188, v189\n"
      "v_rcp_f32 v189, v188\n"
      "v_mfma_f32_16x16x32_bf16 v[236:239], v[200:203], v[240:243], v[236:239]\n"
      "ds_bpermute_b32 v190, v4, v189\n"
      "s_waitcnt lgkmcnt(0)\n"
      "v_mul_f32_dpp v224, v190, v224 row_newbcast:0 row_mask:0xf bank_mask:0xf\n"
      "v_mul_f32_dpp v225, v190, v225 row_newbcast:4 row_mask:0xf bank_mask:0xf\n"
      "v_mul_f32_dpp v226, v190, v226 row_newbcast:8 row_mask:0xf bank_mask:0xf\n"
      "v_mul_f32_dpp v227, v190, v227 row_newbcast:12 row_mask:0xf bank_mask:0xf\n"
      "v_mul_f32_dpp v228, v190, v228 row_newbcast:0 row_mask:0xf bank_mask:0xf\n"
      "v_mul_f32_dpp v229, v190, v229 row_newbcast:4 row_mask:0xf bank_mask:0xf\n"
      "v_mfma_f32_16x16x32_bf16 v[224:227], v[24:27], a[192:195], v[224:227]\n"
      "v_mul_f32_dpp v230, v190, v230 row_newbcast:8 row_mask:0xf bank_mask:0xf\n"
      "v_mul_f32_dpp v231, v190, v231 row_newbcast:12 row_mask:0xf bank_mask:0xf\n"
      "v_mfma_f32_16x16x32_bf16 v[224:227], v[152:155], a[196:199], v[224:227]\n"
      "v_mul_f32_dpp v232, v190, v232 row_newbcast:0 row_mask:0xf bank_mask:0xf\n"
      "v_mul_f32_dpp v233, v190, v233 row_newbcast:4 row_mask:0xf bank_mask:0xf\n"
      "v_mul_f32_dpp v234, v190, v234 row_newbcast:8 row_mask:0xf bank_mask:0xf\n"
      "v_mfma_f32_16x16x32_bf16 v[224:227], v[20:23], a[200:203], v[224:227]\n"
      "v_mul_f32_dpp v235, v190, v235 row_newbcast:12 row_mask:0xf bank_mask:0xf\n"
      "v_mul_f32_dpp v236, v190, v236 row_newbcast:0 row_mask:0xf bank_mask:0xf\n"
      "v_mul_f32_dpp v237, v190, v237 row_newbcast:4 row_mask:0xf bank_mask:0xf\n"
      "v_mfma_f32_16x16x32_bf16 v[224:227], v[156:159], a[204:207], v[224:227]\n"
      "v_mul_f32_dpp v238, v190, v238 row_newbcast:8 row_mask:0xf bank_mask:0xf\n"
      "v_mul_f32_dpp v239, v190, v239 row_newbcast:12 row_mask:0xf bank_mask:0xf\n"
      "v_mfma_f32_16x16x32_bf16 v[228:231], v[24:27], a[208:211], v[228:231]\n"
      "v_mfma_f32_16x16x32_bf16 v[228:231], v[152:155], a[212:215], v[228:231]\n"
      "v_mfma_f32_16x16x32_bf16 v[228:231], v[20:23], a[216:219], v[228:231]\n"
      "v_mfma_f32_16x16x32_bf16 v[228:231], v[156:159], a[220:223], v[228:231]\n"
      "v_mfma_f32_16x16x32_bf16 v[232:235], v[24:27], a[224:227], v[232:235]\n"
      "v_mfma_f32_16x16x32_bf16 v[232:235], v[152:155], a[228:231], v[232:235]\n"
      "v_mfma_f32_16x16x32_bf16 v[232:235], v[20:23], a[232:235], v[232:235]\n"
      "v_mfma_f32_16x16x32_bf16 v[232:235], v[156:159], a[236:239], v[232:235]\n"
      "v_mfma_f32_16x16x32_bf16 v[236:239], v[24:27], a[240:243], v[236:239]\n"
      "v_mfma_f32_16x16x32_bf16 v[236:239], v[152:155], a[244:247], v[236:239]\n"
      "v_mfma_f32_16x16x32_bf16 v[236:239], v[20:23], a[248:251], v[236:239]\n"
      "v_mfma_f32_16x16x32_bf16 v[236:239], v[156:159], a[252:255], v[236:239]\n"
      "v_cvt_pk_bf16_f32 v224, v224, v228\n"
      "v_cvt_pk_bf16_f32 v228, v225, v229\n"
      "v_pk_max_i16 v224, v224, 0\n"
      "v_pk_max_i16 v228, v228, 0\n"
      "v_mfma_f32_16x16x32_bf16 v[20:23], v[172:175], v[160:163], 0\n"
      "v_cvt_pk_bf16_f32 v226, v226, v230\n"
      "v_cvt_pk_bf16_f32 v230, v227, v231\n"
      "v_pk_max_i16 v226, v226, 0\n"
      "v_cvt_pk_bf16_f32 v225, v232, v236\n"
      "v_mfma_f32_16x16x32_bf16 v[20:23], v[176:179], v[88:91], v[20:23]\n"
      "v_cvt_pk_bf16_f32 v229, v233, v237\n"
      "v_pk_max_i16 v225, v225, 0\n"
      "v_pk_max_i16 v229, v229, 0\n"
      "v_cvt_pk_bf16_f32 v227, v234, v238\n"
      "v_mfma_f32_16x16x32_bf16 v[24:27], v[108:111], v[160:163], 0\n"
      "v_cvt_pk_bf16_f32 v231, v235, v239\n"
      "v_pk_max_i16 v227, v227, 0\n"
      "v_pk_max_i16 v230, v230, 0\n"
      "v_max_f32 v186, v20, v21\n"
      "v_mfma_f32_16x16x32_bf16 v[24:27], v[112:115], v[88:91], v[24:27]\n"
      "v_max3_f32 v186, v186, v22, v23\n"
      "v_pk_max_i16 v231, v231, 0\n"
      "v_mfma_f32_16x16x32_bf16 v[28:31], v[164:167], v[160:163], 0\n"
      "v_mfma_f32_16x16x32_bf16 v[28:31], v[168:171], v[88:91], v[28:31]\n"
      "v_mfma_f32_16x16x32_bf16 v[236:239], v[40:43], v[160:163], 0\n"
      "v_mfma_f32_16x16x32_bf16 v[236:239], v[44:47], v[88:91], v[236:239]\n"
      "v_mfma_f32_16x16x32_bf16 v[232:235], v[92:95], v[160:163], 0\n"
      "v_mfma_f32_16x16x32_bf16 v[232:235], v[96:99], v[88:91], v[232:235]\n"
      "v_max3_f32 v186, v186, v24, v25\n"
      "v_max3_f32 v186, v186, v26, v27\n"
      "v_mfma_f32_16x16x32_bf16 v[156:159], v[216:219], v[160:163], 0\n"
      "v_mfma_f32_16x16x32_bf16 v[156:159], v[220:223], v[88:91], v[156:159]\n"
      "v_max3_f32 v186, v186, v28, v29\n"
      "v_max3_f32 v186, v186, v30, v31\n"
      "v_max3_f32 v186, v186, v236, v237\n"
      "v_max3_f32 v186, v186, v238, v239\n"
      "v_max3_f32 v186, v186, v232, v233\n"
      "v_max3_f32 v186, v186, v234, v235\n"
      "v_mfma_f32_16x16x32_bf16 v[152:155], v[244:247], v[160:163], 0\n"
      "v_mfma_f32_16x16x32_bf16 v[152:155], v[248:251], v[88:91], v[152:155]\n"
      "v_max3_f32 v186, v186, v156, v157\n"
      "v_max3_f32 v186, v186, v158, v159\n"
      "v_mfma_f32_16x16x32_bf16 v[160:163], a[128:131], v[148:151], 0\n"
      "v_mfma_f32_16x16x32_bf16 v[160:163], a[132:135], v[136:139], v[160:163]\n"
      "s_add_u32 s56, s48, 20480\n"
      "buffer_store_dwordx2 v[224:225], v2, s[44:47], s56 offen offset:0 nt sc1\n"
      "buffer_store_dwordx2 v[228:229], v2, s[44:47], s56 offen offset:256 nt sc1\n"
      "v_mfma_f32_16x16x32_bf16 v[160:163], a[136:139], v[140:143], v[160:163]\n"
      "v_max3_f32 v186, v186, v152, v153\n"
      "v_max3_f32 v186, v186, v154, v155\n"
      "v_mov_b32 v88, v186\n"
      "buffer_store_dwordx2 v[226:227], v2, s[44:47], s56 offen offset:512 nt sc1\n"
      "v_mfma_f32_16x16x32_bf16 v[160:163], a[140:143], v[144:147], v[160:163]\n"
      "v_permlane16_swap_b32 v186, v88\n"
      "v_max_f32 v186, v186, v88\n"
      "v_mfma_f32_16x16x32_bf16 v[32:35], a[144:147], v[148:151], 0\n"
      "v_mov_b32 v88, v186\n"
      "buffer_store_dwordx2 v[230:231], v2, s[44:47], s56 offen offset:768 nt sc1\n"
      "v_mfma_f32_16x16x32_bf16 v[32:35], a[148:151], v[136:139], v[32:35]\n"
      "v_permlane32_swap_b32 v186, v88\n"
      "v_max_f32 v186, v186, v88\n"
      "v_sub_f32 v20, v20, v186\n"
      "v_sub_f32 v21, v21, v186\n"
      "v_sub_f32 v22, v22, v186\n"
      "v_sub_f32 v23, v23, v186\n"
      "v_sub_f32 v24, v24, v186\n"
      "v_mfma_f32_16x16x32_bf16 v[32:35], a[152:155], v[140:143], v[32:35]\n"
      "v_exp_f32 v20, v20\n"
      "v_exp_f32 v21, v21\n"
      "v_exp_f32 v22, v22\n"
      "v_cvt_pk_bf16_f32 v190, v20, v21\n"
      "v_mfma_f32_16x16x32_bf16 v[32:35], a[156:159], v[144:147], v[32:35]\n"
      "v_exp_f32 v23, v23\n"
      "v_sub_f32 v25, v25, v186\n"
      "v_cvt_pk_bf16_f32 v191, v22, v23\n"
      "v_sub_f32 v26, v26, v186\n"
      "v_sub_f32 v27, v27, v186\n"
      "v_sub_f32 v28, v28, v186\n"
      "v_mfma_f32_16x16x32_bf16 v[88:91], a[160:163], v[148:151], 0\n"
      "v_sub_f32 v29, v29, v186\n"
      "v_sub_f32 v30, v30, v186\n"
      "v_sub_f32 v31, v31, v186\n"
      "v_sub_f32 v236, v236, v186\n"
      "v_sub_f32 v237, v237, v186\n"
      "v_sub_f32 v238, v238, v186\n"
      "v_sub_f32 v239, v239, v186\n"
      "v_mfma_f32_16x16x32_bf16 v[88:91], a[164:167], v[136:139], v[88:91]\n"
      "v_sub_f32 v232, v232, v186\n"
      "v_sub_f32 v233, v233, v186\n"
      "v_sub_f32 v234, v234, v186\n"
      "v_sub_f32 v235, v235, v186\n"
      "v_add_f32 v184, v20, v21\n"
      "v_add_f32 v185, v22, v23\n"
      "v_mfma_f32_16x16x32_bf16 v[88:91], a[168:171], v[140:143], v[88:91]\n"
      "v_exp_f32 v24, v24\n"
      "v_exp_f32 v25, v25\n"
      "v_exp_f32 v26, v26\n"
      "v_exp_f32 v27, v27\n"
      "v_mfma_f32_16x16x32_bf16 v[88:91], a[172:175], v[144:147], v[88:91]\n"
      "v_cvt_pk_bf16_f32 v20, v24, v25\n"
      "v_cvt_pk_bf16_f32 v21, v26, v27\n"
      "v_exp_f32 v28, v28\n"
      "v_exp_f32 v29, v29\n"
      "v_mfma_f32_16x16x32_bf16 v[208:211], a[176:179], v[148:151], 0\n"
      "v_exp_f32 v30, v30\n"
      "v_exp_f32 v31, v31\n"
      "v_cvt_pk_bf16_f32 v22, v28, v29\n"
      "v_cvt_pk_bf16_f32 v23, v30, v31\n"
      "v_exp_f32 v236, v236\n"
      "v_mfma_f32_16x16x32_bf16 v[208:211], a[180:183], v[136:139], v[208:211]\n"
      "v_exp_f32 v237, v237\n"
      "v_exp_f32 v238, v238\n"
      "v_exp_f32 v239, v239\n"
      "v_cvt_pk_bf16_f32 v24, v236, v237\n"
      "v_mfma_f32_16x16x32_bf16 v[208:211], a[184:187], v[140:143], v[208:211]\n"
      "v_cvt_pk_bf16_f32 v25, v238, v239\n"
      "v_exp_f32 v232, v232\n"
      "v_exp_f32 v233, v233\n"
      "v_exp_f32 v234, v234\n"
      "v_mfma_f32_16x16x32_bf16 v[208:211], a[188:191], v[144:147], v[208:211]\n"
      "v_exp_f32 v235, v235\n"
      "v_cvt_pk_bf16_f32 v26, v232, v233\n"
      "v_cvt_pk_bf16_f32 v27, v234, v235\n"
      "v_sub_f32 v156, v156, v186\n"
      "v_sub_f32 v157, v157, v186\n"
      "v_sub_f32 v158, v158, v186\n"
      "v_mfma_f32_16x16x4_4b_bf16 v[224:239], v[190:191], v[18:19], 0 cbsz:2 abid:0\n"
      "v_sub_f32 v159, v159, v186\n"
      "v_sub_f32 v152, v152, v186\n"
      "v_sub_f32 v153, v153, v186\n"
      "v_sub_f32 v154, v154, v186\n"
      "v_sub_f32 v155, v155, v186\n"
      "v_exp_f32 v156, v156\n"
      "v_exp_f32 v157, v157\n"
      "v_exp_f32 v158, v158\n"
      "v_exp_f32 v159, v159\n"
      "v_cvt_pk_bf16_f32 v28, v156, v157\n"
      "v_cvt_pk_bf16_f32 v29, v158, v159\n"
      "v_exp_f32 v152, v152\n"
      "v_exp_f32 v153, v153\n"
      "v_cvt_pk_bf16_f32 v156, v160, v161\n"
      "v_smfmac_f32_16x16x64_bf16 v[184:187], v[12:15], v[20:27], v5\n"
      "v_exp_f32 v154, v154\n"
      "v_exp_f32 v155, v155\n"
      "v_mfma_f32_16x16x32_bf16 v[224:227], v[20:23], v[52:55], v[224:227]\n"
      "v_cvt_pk_bf16_f32 v30, v152, v153\n"
      "v_cvt_pk_bf16_f32 v31, v154, v155\n"
      "v_cvt_pk_bf16_f32 v157, v162, v163\n"
      "v_mfma_f32_16x16x32_bf16 v[224:227], v[24:27], v[56:59], v[224:227]\n"
      "v_cvt_pk_bf16_f32 v158, v32, v33\n"
      "v_cvt_pk_bf16_f32 v159, v34, v35\n"
      "v_cvt_pk_bf16_f32 v32, v88, v89\n"
      "v_mfma_f32_16x16x32_bf16 v[224:227], v[28:31], v[120:123], v[224:227]\n"
      "v_cvt_pk_bf16_f32 v33, v90, v91\n"
      "v_cvt_pk_bf16_f32 v34, v208, v209\n"
      "v_cvt_pk_bf16_f32 v35, v210, v211\n"
      "v_smfmac_f32_16x16x32_bf16 v[184:187], v[12:13], v[28:31], v5\n"
      "v_mfma_f32_16x16x32_bf16 v[228:231], v[20:23], v[128:131], v[228:231]\n"
      "v_mfma_f32_16x16x32_bf16 v[228:231], v[24:27], v[132:135], v[228:231]\n"
      "v_mfma_f32_16x16x32_bf16 v[228:231], v[28:31], v[68:71], v[228:231]\n"
      "v_mfma_f32_16x16x32_bf16 v[232:235], v[20:23], v[100:103], v[232:235]\n"
      "v_mfma_f32_16x16x32_bf16 v[232:235], v[24:27], v[104:107], v[232:235]\n"
      "v_mfma_f32_16x16x32_bf16 v[232:235], v[28:31], v[36:39], v[232:235]\n"
      "v_mfma_f32_16x16x32_bf16 v[236:239], v[20:23], v[192:195], v[236:239]\n"
      "v_mfma_f32_16x16x32_bf16 v[236:239], v[24:27], v[196:199], v[236:239]\n"
      "v_add_f32 v184, v184, v185\n"
      "v_rcp_f32 v185, v184\n"
      "v_mfma_f32_16x16x32_bf16 v[236:239], v[28:31], v[240:243], v[236:239]\n"
      "ds_bpermute_b32 v186, v4, v185\n"
      "s_waitcnt lgkmcnt(0)\n"
      "v_mul_f32_dpp v224, v186, v224 row_newbcast:0 row_mask:0xf bank_mask:0xf\n"
      "v_mul_f32_dpp v225, v186, v225 row_newbcast:4 row_mask:0xf bank_mask:0xf\n"
      "v_mul_f32_dpp v226, v186, v226 row_newbcast:8 row_mask:0xf bank_mask:0xf\n"
      "v_mul_f32_dpp v227, v186, v227 row_newbcast:12 row_mask:0xf bank_mask:0xf\n"
      "v_mul_f32_dpp v228, v186, v228 row_newbcast:0 row_mask:0xf bank_mask:0xf\n"
      "v_mul_f32_dpp v229, v186, v229 row_newbcast:4 row_mask:0xf bank_mask:0xf\n"
      "v_mfma_f32_16x16x32_bf16 v[224:227], v[252:255], a[192:195], v[224:227]\n"
      "v_mul_f32_dpp v230, v186, v230 row_newbcast:8 row_mask:0xf bank_mask:0xf\n"
      "v_mul_f32_dpp v231, v186, v231 row_newbcast:12 row_mask:0xf bank_mask:0xf\n"
      "v_mfma_f32_16x16x32_bf16 v[224:227], v[48:51], a[196:199], v[224:227]\n"
      "v_mul_f32_dpp v232, v186, v232 row_newbcast:0 row_mask:0xf bank_mask:0xf\n"
      "v_mul_f32_dpp v233, v186, v233 row_newbcast:4 row_mask:0xf bank_mask:0xf\n"
      "v_mul_f32_dpp v234, v186, v234 row_newbcast:8 row_mask:0xf bank_mask:0xf\n"
      "v_mfma_f32_16x16x32_bf16 v[224:227], v[124:127], a[200:203], v[224:227]\n"
      "v_mul_f32_dpp v235, v186, v235 row_newbcast:12 row_mask:0xf bank_mask:0xf\n"
      "v_mul_f32_dpp v236, v186, v236 row_newbcast:0 row_mask:0xf bank_mask:0xf\n"
      "v_mul_f32_dpp v237, v186, v237 row_newbcast:4 row_mask:0xf bank_mask:0xf\n"
      "v_mul_f32_dpp v238, v186, v238 row_newbcast:8 row_mask:0xf bank_mask:0xf\n"
      "v_mfma_f32_16x16x32_bf16 v[224:227], v[116:119], a[204:207], v[224:227]\n"
      "v_mul_f32_dpp v239, v186, v239 row_newbcast:12 row_mask:0xf bank_mask:0xf\n"
      "v_mfma_f32_16x16x32_bf16 v[228:231], v[252:255], a[208:211], v[228:231]\n"
      "v_mfma_f32_16x16x32_bf16 v[228:231], v[48:51], a[212:215], v[228:231]\n"
      "v_mfma_f32_16x16x32_bf16 v[228:231], v[124:127], a[216:219], v[228:231]\n"
      "v_mfma_f32_16x16x32_bf16 v[228:231], v[116:119], a[220:223], v[228:231]\n"
      "v_mfma_f32_16x16x32_bf16 v[232:235], v[252:255], a[224:227], v[232:235]\n"
      "v_mfma_f32_16x16x32_bf16 v[232:235], v[48:51], a[228:231], v[232:235]\n"
      "v_mfma_f32_16x16x32_bf16 v[232:235], v[124:127], a[232:235], v[232:235]\n"
      "v_mfma_f32_16x16x32_bf16 v[232:235], v[116:119], a[236:239], v[232:235]\n"
      "v_mfma_f32_16x16x32_bf16 v[236:239], v[252:255], a[240:243], v[236:239]\n"
      "v_mfma_f32_16x16x32_bf16 v[236:239], v[48:51], a[244:247], v[236:239]\n"
      "v_mfma_f32_16x16x32_bf16 v[236:239], v[124:127], a[248:251], v[236:239]\n"
      "v_mfma_f32_16x16x32_bf16 v[236:239], v[116:119], a[252:255], v[236:239]\n"
      "v_cvt_pk_bf16_f32 v224, v224, v228\n"
      "v_cvt_pk_bf16_f32 v228, v225, v229\n"
      "v_pk_max_i16 v224, v224, 0\n"
      "v_pk_max_i16 v228, v228, 0\n"
      "v_cvt_pk_bf16_f32 v226, v226, v230\n"
      "v_mfma_f32_16x16x32_bf16 v[20:23], v[172:175], v[156:159], 0\n"
      "v_cvt_pk_bf16_f32 v230, v227, v231\n"
      "v_pk_max_i16 v226, v226, 0\n"
      "v_cvt_pk_bf16_f32 v225, v232, v236\n"
      "v_cvt_pk_bf16_f32 v229, v233, v237\n"
      "v_mfma_f32_16x16x32_bf16 v[20:23], v[176:179], v[32:35], v[20:23]\n"
      "v_pk_max_i16 v225, v225, 0\n"
      "v_pk_max_i16 v229, v229, 0\n"
      "v_cvt_pk_bf16_f32 v227, v234, v238\n"
      "v_cvt_pk_bf16_f32 v231, v235, v239\n"
      "v_pk_max_i16 v227, v227, 0\n"
      "v_mfma_f32_16x16x32_bf16 v[88:91], v[108:111], v[156:159], 0\n"
      "v_pk_max_i16 v230, v230, 0\n"
      "v_pk_max_i16 v231, v231, 0\n"
      "v_max_f32 v190, v20, v21\n"
      "v_max3_f32 v190, v190, v22, v23\n"
      "v_mfma_f32_16x16x32_bf16 v[88:91], v[112:115], v[32:35], v[88:91]\n"
      "v_mfma_f32_16x16x32_bf16 v[24:27], v[164:167], v[156:159], 0\n"
      "v_mfma_f32_16x16x32_bf16 v[24:27], v[168:171], v[32:35], v[24:27]\n"
      "v_mfma_f32_16x16x32_bf16 v[236:239], v[40:43], v[156:159], 0\n"
      "v_mfma_f32_16x16x32_bf16 v[236:239], v[44:47], v[32:35], v[236:239]\n"
      "v_mfma_f32_16x16x32_bf16 v[232:235], v[92:95], v[156:159], 0\n"
      "v_mfma_f32_16x16x32_bf16 v[232:235], v[96:99], v[32:35], v[232:235]\n"
      "v_mfma_f32_16x16x32_bf16 v[160:163], v[216:219], v[156:159], 0\n"
      "v_mfma_f32_16x16x32_bf16 v[160:163], v[220:223], v[32:35], v[160:163]\n"
      "v_max3_f32 v190, v190, v88, v89\n"
      "v_max3_f32 v190, v190, v90, v91\n"
      "v_max3_f32 v190, v190, v24, v25\n"
      "v_max3_f32 v190, v190, v26, v27\n"
      "v_max3_f32 v190, v190, v236, v237\n"
      "v_max3_f32 v190, v190, v238, v239\n"
      "v_max3_f32 v190, v190, v232, v233\n"
      "v_mfma_f32_16x16x32_bf16 v[152:155], v[244:247], v[156:159], 0\n"
      "v_max3_f32 v190, v190, v234, v235\n"
      "v_max3_f32 v190, v190, v160, v161\n"
      "v_max3_f32 v190, v190, v162, v163\n"
      "v_mfma_f32_16x16x32_bf16 v[152:155], v[248:251], v[32:35], v[152:155]\n"
      "ds_read_b128 v[116:119], v0 offset:8192\n"
      "ds_read_b128 v[124:127], v0 offset:8704\n"
      "ds_read_b128 v[48:51], v0 offset:9216\n"
      "ds_read_b128 v[252:255], v0 offset:9728\n"
      "s_add_u32 s56, s48, 16384\n"
      "buffer_store_dwordx2 v[224:225], v2, s[44:47], s56 offen offset:0 nt sc1\n"
      "buffer_store_dwordx2 v[228:229], v2, s[44:47], s56 offen offset:256 nt sc1\n"
      "buffer_store_dwordx2 v[226:227], v2, s[44:47], s56 offen offset:512 nt sc1\n"
      "v_max3_f32 v190, v190, v152, v153\n"
      "v_max3_f32 v190, v190, v154, v155\n"
      "v_mov_b32 v32, v190\n"
      "buffer_store_dwordx2 v[230:231], v2, s[44:47], s56 offen offset:768 nt sc1\n"
      "s_nop 0\n"
      "v_permlane16_swap_b32 v190, v32\n"
      "v_max_f32 v190, v190, v32\n"
      "v_mov_b32 v32, v190\n"
      "s_nop 1\n"
      "v_permlane32_swap_b32 v190, v32\n"
      "v_max_f32 v190, v190, v32\n"
      "v_sub_f32 v20, v20, v190\n"
      "v_sub_f32 v21, v21, v190\n"
      "v_sub_f32 v22, v22, v190\n"
      "s_waitcnt lgkmcnt(3)\n"
      "v_mfma_f32_16x16x32_bf16 v[32:35], a[128:131], v[116:119], 0\n"
      "v_sub_f32 v23, v23, v190\n"
      "v_sub_f32 v88, v88, v190\n"
      "s_waitcnt lgkmcnt(2)\n"
      "v_mfma_f32_16x16x32_bf16 v[32:35], a[132:135], v[124:127], v[32:35]\n"
      "v_exp_f32 v20, v20\n"
      "s_waitcnt lgkmcnt(1)\n"
      "v_mfma_f32_16x16x32_bf16 v[32:35], a[136:139], v[48:51], v[32:35]\n"
      "v_exp_f32 v21, v21\n"
      "s_waitcnt lgkmcnt(0)\n"
      "v_mfma_f32_16x16x32_bf16 v[32:35], a[140:143], v[252:255], v[32:35]\n"
      "v_exp_f32 v22, v22\n"
      "v_mfma_f32_16x16x32_bf16 v[28:31], a[144:147], v[116:119], 0\n"
      "v_exp_f32 v23, v23\n"
      "v_mfma_f32_16x16x32_bf16 v[28:31], a[148:151], v[124:127], v[28:31]\n"
      "v_cvt_pk_bf16_f32 v156, v20, v21\n"
      "v_cvt_pk_bf16_f32 v157, v22, v23\n"
      "v_mfma_f32_16x16x32_bf16 v[28:31], a[152:155], v[48:51], v[28:31]\n"
      "v_sub_f32 v89, v89, v190\n"
      "v_sub_f32 v90, v90, v190\n"
      "v_sub_f32 v91, v91, v190\n"
      "v_sub_f32 v24, v24, v190\n"
      "v_mfma_f32_16x16x32_bf16 v[28:31], a[156:159], v[252:255], v[28:31]\n"
      "v_sub_f32 v25, v25, v190\n"
      "v_sub_f32 v26, v26, v190\n"
      "v_sub_f32 v27, v27, v190\n"
      "v_sub_f32 v236, v236, v190\n"
      "v_sub_f32 v237, v237, v190\n"
      "v_sub_f32 v238, v238, v190\n"
      "v_mfma_f32_16x16x32_bf16 v[184:187], a[160:163], v[116:119], 0\n"
      "v_sub_f32 v239, v239, v190\n"
      "v_sub_f32 v232, v232, v190\n"
      "v_sub_f32 v233, v233, v190\n"
      "v_sub_f32 v234, v234, v190\n"
      "v_sub_f32 v235, v235, v190\n"
      "v_add_f32 v86, v20, v21\n"
      "v_add_f32 v87, v22, v23\n"
      "v_mfma_f32_16x16x32_bf16 v[184:187], a[164:167], v[124:127], v[184:187]\n"
      "v_exp_f32 v88, v88\n"
      "v_exp_f32 v89, v89\n"
      "v_exp_f32 v90, v90\n"
      "v_mfma_f32_16x16x32_bf16 v[184:187], a[168:171], v[48:51], v[184:187]\n"
      "v_exp_f32 v91, v91\n"
      "v_cvt_pk_bf16_f32 v20, v88, v89\n"
      "v_cvt_pk_bf16_f32 v21, v90, v91\n"
      "v_exp_f32 v24, v24\n"
      "v_mfma_f32_16x16x32_bf16 v[184:187], a[172:175], v[252:255], v[184:187]\n"
      "v_exp_f32 v25, v25\n"
      "v_exp_f32 v26, v26\n"
      "v_exp_f32 v27, v27\n"
      "v_cvt_pk_bf16_f32 v22, v24, v25\n"
      "v_mfma_f32_16x16x32_bf16 v[208:211], a[176:179], v[116:119], 0\n"
      "v_cvt_pk_bf16_f32 v23, v26, v27\n"
      "v_exp_f32 v236, v236\n"
      "v_exp_f32 v237, v237\n"
      "v_exp_f32 v238, v238\n"
      "v_mfma_f32_16x16x32_bf16 v[208:211], a[180:183], v[124:127], v[208:211]\n"
      "v_exp_f32 v239, v239\n"
      "v_cvt_pk_bf16_f32 v24, v236, v237\n"
      "v_cvt_pk_bf16_f32 v25, v238, v239\n"
      "v_exp_f32 v232, v232\n"
      "v_mfma_f32_16x16x32_bf16 v[208:211], a[184:187], v[48:51], v[208:211]\n"
      "v_exp_f32 v233, v233\n"
      "v_exp_f32 v234, v234\n"
      "v_exp_f32 v235, v235\n"
      "v_cvt_pk_bf16_f32 v26, v232, v233\n"
      "v_mfma_f32_16x16x32_bf16 v[208:211], a[188:191], v[252:255], v[208:211]\n"
      "v_cvt_pk_bf16_f32 v27, v234, v235\n"
      "v_sub_f32 v160, v160, v190\n"
      "v_sub_f32 v161, v161, v190\n"
      "v_sub_f32 v162, v162, v190\n"
      "v_sub_f32 v163, v163, v190\n"
      "v_sub_f32 v152, v152, v190\n"
      "v_mfma_f32_16x16x4_4b_bf16 v[224:239], v[156:157], v[18:19], 0 cbsz:2 abid:0\n"
      "s_add_u32 m0, s53, 8192\n"
      "s_add_u32 s54, s49, 8192\n"
      "buffer_load_dwordx4 v1, s[40:43], s54 offen offset:0 nt lds\n"
      "v_sub_f32 v153, v153, v190\n"
      "v_sub_f32 v154, v154, v190\n"
      "v_sub_f32 v155, v155, v190\n"
      "v_exp_f32 v160, v160\n"
      "v_exp_f32 v161, v161\n"
      "v_exp_f32 v162, v162\n"
      "v_exp_f32 v163, v163\n"
      "v_cvt_pk_bf16_f32 v188, v160, v161\n"
      "v_cvt_pk_bf16_f32 v189, v162, v163\n"
      "v_exp_f32 v152, v152\n"
      "v_exp_f32 v153, v153\n"
      "v_exp_f32 v154, v154\n"
      "v_smfmac_f32_16x16x64_bf16 v[86:89], v[12:15], v[20:27], v5\n"
      "v_exp_f32 v155, v155\n"
      "v_cvt_pk_bf16_f32 v190, v152, v153\n"
      "v_mfma_f32_16x16x32_bf16 v[224:227], v[20:23], v[52:55], v[224:227]\n"
      "buffer_load_dwordx4 v1, s[40:43], s54 offen offset:2048 nt lds\n"
      "s_add_u32 m0, s53, 9088\n"
      "v_cvt_pk_bf16_f32 v191, v154, v155\n"
      "v_cvt_pk_bf16_f32 v156, v32, v33\n"
      "v_cvt_pk_bf16_f32 v157, v34, v35\n"
      "v_mfma_f32_16x16x32_bf16 v[224:227], v[24:27], v[56:59], v[224:227]\n"
      "v_cvt_pk_bf16_f32 v158, v28, v29\n"
      "v_cvt_pk_bf16_f32 v159, v30, v31\n"
      "v_cvt_pk_bf16_f32 v32, v184, v185\n"
      "v_mfma_f32_16x16x32_bf16 v[224:227], v[188:191], v[120:123], v[224:227]\n"
      "buffer_load_dwordx4 v1, s[40:43], s54 offen offset:128 nt lds\n"
      "v_cvt_pk_bf16_f32 v33, v186, v187\n"
      "v_cvt_pk_bf16_f32 v34, v208, v209\n"
      "v_cvt_pk_bf16_f32 v35, v210, v211\n"
      "v_smfmac_f32_16x16x32_bf16 v[86:89], v[12:13], v[188:191], v5\n"
      "v_mfma_f32_16x16x32_bf16 v[228:231], v[20:23], v[128:131], v[228:231]\n"
      "buffer_load_dwordx4 v1, s[40:43], s54 offen offset:2176 nt lds\n"
      "v_mfma_f32_16x16x32_bf16 v[228:231], v[24:27], v[132:135], v[228:231]\n"
      "v_mfma_f32_16x16x32_bf16 v[228:231], v[188:191], v[68:71], v[228:231]\n"
      "v_mfma_f32_16x16x32_bf16 v[232:235], v[20:23], v[100:103], v[232:235]\n"
      "v_mfma_f32_16x16x32_bf16 v[232:235], v[24:27], v[104:107], v[232:235]\n"
      "v_mfma_f32_16x16x32_bf16 v[232:235], v[188:191], v[36:39], v[232:235]\n"
      "v_mfma_f32_16x16x32_bf16 v[236:239], v[20:23], v[192:195], v[236:239]\n"
      "v_add_f32 v86, v86, v87\n"
      "v_rcp_f32 v87, v86\n"
      "v_mfma_f32_16x16x32_bf16 v[236:239], v[24:27], v[196:199], v[236:239]\n"
      "ds_bpermute_b32 v88, v4, v87\n"
      "v_mfma_f32_16x16x32_bf16 v[236:239], v[188:191], v[240:243], v[236:239]\n"
      "s_waitcnt lgkmcnt(0)\n"
      "v_mul_f32_dpp v224, v88, v224 row_newbcast:0 row_mask:0xf bank_mask:0xf\n"
      "v_mul_f32_dpp v225, v88, v225 row_newbcast:4 row_mask:0xf bank_mask:0xf\n"
      "v_mul_f32_dpp v226, v88, v226 row_newbcast:8 row_mask:0xf bank_mask:0xf\n"
      "v_mul_f32_dpp v227, v88, v227 row_newbcast:12 row_mask:0xf bank_mask:0xf\n"
      "v_mul_f32_dpp v228, v88, v228 row_newbcast:0 row_mask:0xf bank_mask:0xf\n"
      "v_mul_f32_dpp v229, v88, v229 row_newbcast:4 row_mask:0xf bank_mask:0xf\n"
      "v_mfma_f32_16x16x32_bf16 v[224:227], v[148:151], a[192:195], v[224:227]\n"
      "v_mul_f32_dpp v230, v88, v230 row_newbcast:8 row_mask:0xf bank_mask:0xf\n"
      "v_mul_f32_dpp v231, v88, v231 row_newbcast:12 row_mask:0xf bank_mask:0xf\n"
      "v_mfma_f32_16x16x32_bf16 v[224:227], v[136:139], a[196:199], v[224:227]\n"
      "v_mul_f32_dpp v232, v88, v232 row_newbcast:0 row_mask:0xf bank_mask:0xf\n"
      "v_mul_f32_dpp v233, v88, v233 row_newbcast:4 row_mask:0xf bank_mask:0xf\n"
      "v_mul_f32_dpp v234, v88, v234 row_newbcast:8 row_mask:0xf bank_mask:0xf\n"
      "v_mfma_f32_16x16x32_bf16 v[224:227], v[140:143], a[200:203], v[224:227]\n"
      "v_mul_f32_dpp v235, v88, v235 row_newbcast:12 row_mask:0xf bank_mask:0xf\n"
      "v_mul_f32_dpp v236, v88, v236 row_newbcast:0 row_mask:0xf bank_mask:0xf\n"
      "v_mul_f32_dpp v237, v88, v237 row_newbcast:4 row_mask:0xf bank_mask:0xf\n"
      "v_mul_f32_dpp v238, v88, v238 row_newbcast:8 row_mask:0xf bank_mask:0xf\n"
      "v_mfma_f32_16x16x32_bf16 v[224:227], v[144:147], a[204:207], v[224:227]\n"
      "v_mul_f32_dpp v239, v88, v239 row_newbcast:12 row_mask:0xf bank_mask:0xf\n"
      "v_mfma_f32_16x16x32_bf16 v[228:231], v[148:151], a[208:211], v[228:231]\n"
      "v_mfma_f32_16x16x32_bf16 v[228:231], v[136:139], a[212:215], v[228:231]\n"
      "v_mfma_f32_16x16x32_bf16 v[228:231], v[140:143], a[216:219], v[228:231]\n"
      "v_mfma_f32_16x16x32_bf16 v[228:231], v[144:147], a[220:223], v[228:231]\n"
      "v_mfma_f32_16x16x32_bf16 v[232:235], v[148:151], a[224:227], v[232:235]\n"
      "v_mfma_f32_16x16x32_bf16 v[232:235], v[136:139], a[228:231], v[232:235]\n"
      "v_mfma_f32_16x16x32_bf16 v[232:235], v[140:143], a[232:235], v[232:235]\n"
      "v_mfma_f32_16x16x32_bf16 v[232:235], v[144:147], a[236:239], v[232:235]\n"
      "v_mfma_f32_16x16x32_bf16 v[236:239], v[148:151], a[240:243], v[236:239]\n"
      "v_mfma_f32_16x16x32_bf16 v[236:239], v[136:139], a[244:247], v[236:239]\n"
      "v_mfma_f32_16x16x32_bf16 v[236:239], v[140:143], a[248:251], v[236:239]\n"
      "v_mfma_f32_16x16x32_bf16 v[236:239], v[144:147], a[252:255], v[236:239]\n"
      "v_cvt_pk_bf16_f32 v224, v224, v228\n"
      "v_cvt_pk_bf16_f32 v228, v225, v229\n"
      "v_pk_max_i16 v224, v224, 0\n"
      "v_pk_max_i16 v228, v228, 0\n"
      "v_cvt_pk_bf16_f32 v226, v226, v230\n"
      "v_mfma_f32_16x16x32_bf16 v[140:143], v[172:175], v[156:159], 0\n"
      "v_cvt_pk_bf16_f32 v230, v227, v231\n"
      "v_pk_max_i16 v226, v226, 0\n"
      "v_cvt_pk_bf16_f32 v225, v232, v236\n"
      "v_cvt_pk_bf16_f32 v229, v233, v237\n"
      "v_mfma_f32_16x16x32_bf16 v[140:143], v[176:179], v[32:35], v[140:143]\n"
      "v_pk_max_i16 v225, v225, 0\n"
      "v_pk_max_i16 v229, v229, 0\n"
      "v_cvt_pk_bf16_f32 v227, v234, v238\n"
      "v_cvt_pk_bf16_f32 v231, v235, v239\n"
      "v_pk_max_i16 v227, v227, 0\n"
      "v_mfma_f32_16x16x32_bf16 v[88:91], v[108:111], v[156:159], 0\n"
      "v_pk_max_i16 v230, v230, 0\n"
      "v_pk_max_i16 v231, v231, 0\n"
      "v_max_f32 v188, v140, v141\n"
      "v_max3_f32 v188, v188, v142, v143\n"
      "v_mfma_f32_16x16x32_bf16 v[88:91], v[112:115], v[32:35], v[88:91]\n"
      "v_mfma_f32_16x16x32_bf16 v[144:147], v[164:167], v[156:159], 0\n"
      "v_mfma_f32_16x16x32_bf16 v[144:147], v[168:171], v[32:35], v[144:147]\n"
      "v_mfma_f32_16x16x32_bf16 v[236:239], v[40:43], v[156:159], 0\n"
      "v_mfma_f32_16x16x32_bf16 v[236:239], v[44:47], v[32:35], v[236:239]\n"
      "v_mfma_f32_16x16x32_bf16 v[232:235], v[92:95], v[156:159], 0\n"
      "v_mfma_f32_16x16x32_bf16 v[232:235], v[96:99], v[32:35], v[232:235]\n"
      "v_mfma_f32_16x16x32_bf16 v[160:163], v[216:219], v[156:159], 0\n"
      "v_mfma_f32_16x16x32_bf16 v[160:163], v[220:223], v[32:35], v[160:163]\n"
      "v_max3_f32 v188, v188, v88, v89\n"
      "v_max3_f32 v188, v188, v90, v91\n"
      "v_max3_f32 v188, v188, v144, v145\n"
      "v_max3_f32 v188, v188, v146, v147\n"
      "v_max3_f32 v188, v188, v236, v237\n"
      "v_max3_f32 v188, v188, v238, v239\n"
      "v_max3_f32 v188, v188, v232, v233\n"
      "v_mfma_f32_16x16x32_bf16 v[152:155], v[244:247], v[156:159], 0\n"
      "v_max3_f32 v188, v188, v234, v235\n"
      "v_max3_f32 v188, v188, v160, v161\n"
      "v_max3_f32 v188, v188, v162, v163\n"
      "v_mfma_f32_16x16x32_bf16 v[152:155], v[248:251], v[32:35], v[152:155]\n"
      "ds_read_b128 v[136:139], v0 offset:4096\n"
      "ds_read_b128 v[148:151], v0 offset:4608\n"
      "ds_read_b128 v[24:27], v0 offset:5120\n"
      "ds_read_b128 v[20:23], v0 offset:5632\n"
      "s_add_u32 s56, s48, 12288\n"
      "buffer_store_dwordx2 v[224:225], v2, s[44:47], s56 offen offset:0 nt sc1\n"
      "buffer_store_dwordx2 v[228:229], v2, s[44:47], s56 offen offset:256 nt sc1\n"
      "buffer_store_dwordx2 v[226:227], v2, s[44:47], s56 offen offset:512 nt sc1\n"
      "v_max3_f32 v188, v188, v152, v153\n"
      "v_max3_f32 v188, v188, v154, v155\n"
      "v_mov_b32 v32, v188\n"
      "buffer_store_dwordx2 v[230:231], v2, s[44:47], s56 offen offset:768 nt sc1\n"
      "s_nop 0\n"
      "v_permlane16_swap_b32 v188, v32\n"
      "v_max_f32 v188, v188, v32\n"
      "v_mov_b32 v32, v188\n"
      "s_nop 1\n"
      "v_permlane32_swap_b32 v188, v32\n"
      "v_max_f32 v188, v188, v32\n"
      "v_sub_f32 v140, v140, v188\n"
      "v_sub_f32 v141, v141, v188\n"
      "v_sub_f32 v142, v142, v188\n"
      "s_waitcnt lgkmcnt(3)\n"
      "v_mfma_f32_16x16x32_bf16 v[32:35], a[128:131], v[136:139], 0\n"
      "v_sub_f32 v143, v143, v188\n"
      "v_sub_f32 v88, v88, v188\n"
      "s_waitcnt lgkmcnt(2)\n"
      "v_mfma_f32_16x16x32_bf16 v[32:35], a[132:135], v[148:151], v[32:35]\n"
      "v_exp_f32 v140, v140\n"
      "s_waitcnt lgkmcnt(1)\n"
      "v_mfma_f32_16x16x32_bf16 v[32:35], a[136:139], v[24:27], v[32:35]\n"
      "v_exp_f32 v141, v141\n"
      "s_waitcnt lgkmcnt(0)\n"
      "v_mfma_f32_16x16x32_bf16 v[32:35], a[140:143], v[20:23], v[32:35]\n"
      "v_exp_f32 v142, v142\n"
      "v_mfma_f32_16x16x32_bf16 v[28:31], a[144:147], v[136:139], 0\n"
      "v_exp_f32 v143, v143\n"
      "v_mfma_f32_16x16x32_bf16 v[28:31], a[148:151], v[148:151], v[28:31]\n"
      "v_cvt_pk_bf16_f32 v156, v140, v141\n"
      "v_cvt_pk_bf16_f32 v157, v142, v143\n"
      "v_mfma_f32_16x16x32_bf16 v[28:31], a[152:155], v[24:27], v[28:31]\n"
      "v_sub_f32 v89, v89, v188\n"
      "v_sub_f32 v90, v90, v188\n"
      "v_sub_f32 v91, v91, v188\n"
      "v_sub_f32 v144, v144, v188\n"
      "v_mfma_f32_16x16x32_bf16 v[28:31], a[156:159], v[20:23], v[28:31]\n"
      "v_sub_f32 v145, v145, v188\n"
      "v_sub_f32 v146, v146, v188\n"
      "v_sub_f32 v147, v147, v188\n"
      "v_sub_f32 v236, v236, v188\n"
      "v_sub_f32 v237, v237, v188\n"
      "v_sub_f32 v238, v238, v188\n"
      "v_mfma_f32_16x16x32_bf16 v[184:187], a[160:163], v[136:139], 0\n"
      "v_sub_f32 v239, v239, v188\n"
      "v_sub_f32 v232, v232, v188\n"
      "v_sub_f32 v233, v233, v188\n"
      "v_sub_f32 v234, v234, v188\n"
      "v_sub_f32 v235, v235, v188\n"
      "v_add_f32 v86, v140, v141\n"
      "v_add_f32 v87, v142, v143\n"
      "v_mfma_f32_16x16x32_bf16 v[184:187], a[164:167], v[148:151], v[184:187]\n"
      "v_exp_f32 v88, v88\n"
      "v_exp_f32 v89, v89\n"
      "v_exp_f32 v90, v90\n"
      "v_mfma_f32_16x16x32_bf16 v[184:187], a[168:171], v[24:27], v[184:187]\n"
      "v_exp_f32 v91, v91\n"
      "v_cvt_pk_bf16_f32 v140, v88, v89\n"
      "v_cvt_pk_bf16_f32 v141, v90, v91\n"
      "v_exp_f32 v144, v144\n"
      "v_mfma_f32_16x16x32_bf16 v[184:187], a[172:175], v[20:23], v[184:187]\n"
      "v_exp_f32 v145, v145\n"
      "v_exp_f32 v146, v146\n"
      "v_exp_f32 v147, v147\n"
      "v_cvt_pk_bf16_f32 v142, v144, v145\n"
      "v_mfma_f32_16x16x32_bf16 v[208:211], a[176:179], v[136:139], 0\n"
      "v_cvt_pk_bf16_f32 v143, v146, v147\n"
      "v_exp_f32 v236, v236\n"
      "v_exp_f32 v237, v237\n"
      "v_exp_f32 v238, v238\n"
      "v_mfma_f32_16x16x32_bf16 v[208:211], a[180:183], v[148:151], v[208:211]\n"
      "v_exp_f32 v239, v239\n"
      "v_cvt_pk_bf16_f32 v144, v236, v237\n"
      "v_cvt_pk_bf16_f32 v145, v238, v239\n"
      "v_exp_f32 v232, v232\n"
      "v_mfma_f32_16x16x32_bf16 v[208:211], a[184:187], v[24:27], v[208:211]\n"
      "v_exp_f32 v233, v233\n"
      "v_exp_f32 v234, v234\n"
      "v_exp_f32 v235, v235\n"
      "v_cvt_pk_bf16_f32 v146, v232, v233\n"
      "v_mfma_f32_16x16x32_bf16 v[208:211], a[188:191], v[20:23], v[208:211]\n"
      "v_cvt_pk_bf16_f32 v147, v234, v235\n"
      "v_sub_f32 v160, v160, v188\n"
      "v_sub_f32 v161, v161, v188\n"
      "v_sub_f32 v162, v162, v188\n"
      "v_sub_f32 v163, v163, v188\n"
      "v_sub_f32 v152, v152, v188\n"
      "v_mfma_f32_16x16x4_4b_bf16 v[224:239], v[156:157], v[18:19], 0 cbsz:2 abid:0\n"
      "s_add_u32 m0, s53, 4096\n"
      "s_add_u32 s54, s49, 4096\n"
      "buffer_load_dwordx4 v1, s[40:43], s54 offen offset:0 lds\n"
      "v_sub_f32 v153, v153, v188\n"
      "v_sub_f32 v154, v154, v188\n"
      "v_sub_f32 v155, v155, v188\n"
      "v_exp_f32 v160, v160\n"
      "v_exp_f32 v161, v161\n"
      "v_exp_f32 v162, v162\n"
      "v_exp_f32 v163, v163\n"
      "v_cvt_pk_bf16_f32 v188, v160, v161\n"
      "v_cvt_pk_bf16_f32 v189, v162, v163\n"
      "v_exp_f32 v152, v152\n"
      "v_exp_f32 v153, v153\n"
      "v_exp_f32 v154, v154\n"
      "v_smfmac_f32_16x16x64_bf16 v[86:89], v[12:15], v[140:147], v5\n"
      "v_exp_f32 v155, v155\n"
      "v_cvt_pk_bf16_f32 v190, v152, v153\n"
      "v_mfma_f32_16x16x32_bf16 v[224:227], v[140:143], v[52:55], v[224:227]\n"
      "buffer_load_dwordx4 v1, s[40:43], s54 offen offset:2048 lds\n"
      "s_add_u32 m0, s53, 4992\n"
      "v_cvt_pk_bf16_f32 v191, v154, v155\n"
      "v_cvt_pk_bf16_f32 v156, v32, v33\n"
      "v_cvt_pk_bf16_f32 v157, v34, v35\n"
      "v_mfma_f32_16x16x32_bf16 v[224:227], v[144:147], v[56:59], v[224:227]\n"
      "v_cvt_pk_bf16_f32 v158, v28, v29\n"
      "v_cvt_pk_bf16_f32 v159, v30, v31\n"
      "v_cvt_pk_bf16_f32 v32, v184, v185\n"
      "v_cvt_pk_bf16_f32 v33, v186, v187\n"
      "v_mfma_f32_16x16x32_bf16 v[224:227], v[188:191], v[120:123], v[224:227]\n"
      "buffer_load_dwordx4 v1, s[40:43], s54 offen offset:128 lds\n"
      "v_cvt_pk_bf16_f32 v34, v208, v209\n"
      "v_cvt_pk_bf16_f32 v35, v210, v211\n"
      "v_smfmac_f32_16x16x32_bf16 v[86:89], v[12:13], v[188:191], v5\n"
      "v_mfma_f32_16x16x32_bf16 v[228:231], v[140:143], v[128:131], v[228:231]\n"
      "buffer_load_dwordx4 v1, s[40:43], s54 offen offset:2176 lds\n"
      "v_mfma_f32_16x16x32_bf16 v[228:231], v[144:147], v[132:135], v[228:231]\n"
      "v_mfma_f32_16x16x32_bf16 v[228:231], v[188:191], v[68:71], v[228:231]\n"
      "v_mfma_f32_16x16x32_bf16 v[232:235], v[140:143], v[100:103], v[232:235]\n"
      "v_mfma_f32_16x16x32_bf16 v[232:235], v[144:147], v[104:107], v[232:235]\n"
      "v_mfma_f32_16x16x32_bf16 v[232:235], v[188:191], v[36:39], v[232:235]\n"
      "v_mfma_f32_16x16x32_bf16 v[236:239], v[140:143], v[192:195], v[236:239]\n"
      "v_add_f32 v86, v86, v87\n"
      "v_rcp_f32 v87, v86\n"
      "v_mfma_f32_16x16x32_bf16 v[236:239], v[144:147], v[196:199], v[236:239]\n"
      "ds_bpermute_b32 v88, v4, v87\n"
      "v_mfma_f32_16x16x32_bf16 v[236:239], v[188:191], v[240:243], v[236:239]\n"
      "s_waitcnt lgkmcnt(0)\n"
      "v_mul_f32_dpp v224, v88, v224 row_newbcast:0 row_mask:0xf bank_mask:0xf\n"
      "v_mul_f32_dpp v225, v88, v225 row_newbcast:4 row_mask:0xf bank_mask:0xf\n"
      "v_mul_f32_dpp v226, v88, v226 row_newbcast:8 row_mask:0xf bank_mask:0xf\n"
      "v_mul_f32_dpp v227, v88, v227 row_newbcast:12 row_mask:0xf bank_mask:0xf\n"
      "v_mul_f32_dpp v228, v88, v228 row_newbcast:0 row_mask:0xf bank_mask:0xf\n"
      "v_mul_f32_dpp v229, v88, v229 row_newbcast:4 row_mask:0xf bank_mask:0xf\n"
      "v_mfma_f32_16x16x32_bf16 v[224:227], v[116:119], a[192:195], v[224:227]\n"
      "v_mul_f32_dpp v230, v88, v230 row_newbcast:8 row_mask:0xf bank_mask:0xf\n"
      "v_mul_f32_dpp v231, v88, v231 row_newbcast:12 row_mask:0xf bank_mask:0xf\n"
      "v_mfma_f32_16x16x32_bf16 v[224:227], v[124:127], a[196:199], v[224:227]\n"
      "v_mul_f32_dpp v232, v88, v232 row_newbcast:0 row_mask:0xf bank_mask:0xf\n"
      "v_mul_f32_dpp v233, v88, v233 row_newbcast:4 row_mask:0xf bank_mask:0xf\n"
      "v_mul_f32_dpp v234, v88, v234 row_newbcast:8 row_mask:0xf bank_mask:0xf\n"
      "v_mul_f32_dpp v235, v88, v235 row_newbcast:12 row_mask:0xf bank_mask:0xf\n"
      "v_mfma_f32_16x16x32_bf16 v[224:227], v[48:51], a[200:203], v[224:227]\n"
      "v_mul_f32_dpp v236, v88, v236 row_newbcast:0 row_mask:0xf bank_mask:0xf\n"
      "v_mul_f32_dpp v237, v88, v237 row_newbcast:4 row_mask:0xf bank_mask:0xf\n"
      "v_mul_f32_dpp v238, v88, v238 row_newbcast:8 row_mask:0xf bank_mask:0xf\n"
      "v_mfma_f32_16x16x32_bf16 v[224:227], v[252:255], a[204:207], v[224:227]\n"
      "v_mul_f32_dpp v239, v88, v239 row_newbcast:12 row_mask:0xf bank_mask:0xf\n"
      "v_mfma_f32_16x16x32_bf16 v[228:231], v[116:119], a[208:211], v[228:231]\n"
      "v_mfma_f32_16x16x32_bf16 v[228:231], v[124:127], a[212:215], v[228:231]\n"
      "v_mfma_f32_16x16x32_bf16 v[228:231], v[48:51], a[216:219], v[228:231]\n"
      "v_mfma_f32_16x16x32_bf16 v[228:231], v[252:255], a[220:223], v[228:231]\n"
      "v_mfma_f32_16x16x32_bf16 v[232:235], v[116:119], a[224:227], v[232:235]\n"
      "v_mfma_f32_16x16x32_bf16 v[232:235], v[124:127], a[228:231], v[232:235]\n"
      "v_mfma_f32_16x16x32_bf16 v[232:235], v[48:51], a[232:235], v[232:235]\n"
      "v_mfma_f32_16x16x32_bf16 v[232:235], v[252:255], a[236:239], v[232:235]\n"
      "v_mfma_f32_16x16x32_bf16 v[236:239], v[116:119], a[240:243], v[236:239]\n"
      "v_mfma_f32_16x16x32_bf16 v[236:239], v[124:127], a[244:247], v[236:239]\n"
      "v_mfma_f32_16x16x32_bf16 v[236:239], v[48:51], a[248:251], v[236:239]\n"
      "v_mfma_f32_16x16x32_bf16 v[236:239], v[252:255], a[252:255], v[236:239]\n"
      "v_cvt_pk_bf16_f32 v224, v224, v228\n"
      "v_cvt_pk_bf16_f32 v228, v225, v229\n"
      "v_pk_max_i16 v224, v224, 0\n"
      "v_pk_max_i16 v228, v228, 0\n"
      "v_cvt_pk_bf16_f32 v226, v226, v230\n"
      "v_mfma_f32_16x16x32_bf16 v[140:143], v[172:175], v[156:159], 0\n"
      "v_cvt_pk_bf16_f32 v230, v227, v231\n"
      "v_pk_max_i16 v226, v226, 0\n"
      "v_cvt_pk_bf16_f32 v225, v232, v236\n"
      "v_cvt_pk_bf16_f32 v229, v233, v237\n"
      "v_mfma_f32_16x16x32_bf16 v[140:143], v[176:179], v[32:35], v[140:143]\n"
      "v_pk_max_i16 v225, v225, 0\n"
      "v_pk_max_i16 v229, v229, 0\n"
      "v_cvt_pk_bf16_f32 v227, v234, v238\n"
      "v_cvt_pk_bf16_f32 v231, v235, v239\n"
      "v_pk_max_i16 v227, v227, 0\n"
      "v_mfma_f32_16x16x32_bf16 v[88:91], v[108:111], v[156:159], 0\n"
      "v_pk_max_i16 v230, v230, 0\n"
      "v_pk_max_i16 v231, v231, 0\n"
      "v_max_f32 v188, v140, v141\n"
      "v_max3_f32 v188, v188, v142, v143\n"
      "v_mfma_f32_16x16x32_bf16 v[88:91], v[112:115], v[32:35], v[88:91]\n"
      "v_mfma_f32_16x16x32_bf16 v[144:147], v[164:167], v[156:159], 0\n"
      "v_mfma_f32_16x16x32_bf16 v[144:147], v[168:171], v[32:35], v[144:147]\n"
      "v_mfma_f32_16x16x32_bf16 v[236:239], v[40:43], v[156:159], 0\n"
      "v_mfma_f32_16x16x32_bf16 v[236:239], v[44:47], v[32:35], v[236:239]\n"
      "v_mfma_f32_16x16x32_bf16 v[232:235], v[92:95], v[156:159], 0\n"
      "v_mfma_f32_16x16x32_bf16 v[232:235], v[96:99], v[32:35], v[232:235]\n"
      "v_mfma_f32_16x16x32_bf16 v[160:163], v[216:219], v[156:159], 0\n"
      "v_mfma_f32_16x16x32_bf16 v[160:163], v[220:223], v[32:35], v[160:163]\n"
      "v_max3_f32 v188, v188, v88, v89\n"
      "v_max3_f32 v188, v188, v90, v91\n"
      "v_max3_f32 v188, v188, v144, v145\n"
      "v_max3_f32 v188, v188, v146, v147\n"
      "v_max3_f32 v188, v188, v236, v237\n"
      "v_max3_f32 v188, v188, v238, v239\n"
      "v_max3_f32 v188, v188, v232, v233\n"
      "v_mfma_f32_16x16x32_bf16 v[152:155], v[244:247], v[156:159], 0\n"
      "v_max3_f32 v188, v188, v234, v235\n"
      "v_max3_f32 v188, v188, v160, v161\n"
      "v_max3_f32 v188, v188, v162, v163\n"
      "v_mfma_f32_16x16x32_bf16 v[152:155], v[248:251], v[32:35], v[152:155]\n"
      "ds_read_b128 v[124:127], v0 offset:0\n"
      "ds_read_b128 v[48:51], v0 offset:512\n"
      "ds_read_b128 v[116:119], v0 offset:1024\n"
      "ds_read_b128 v[252:255], v0 offset:1536\n"
      "s_add_u32 s56, s48, 8192\n"
      "buffer_store_dwordx2 v[224:225], v2, s[44:47], s56 offen offset:0 nt sc1\n"
      "buffer_store_dwordx2 v[228:229], v2, s[44:47], s56 offen offset:256 nt sc1\n"
      "buffer_store_dwordx2 v[226:227], v2, s[44:47], s56 offen offset:512 nt sc1\n"
      "v_max3_f32 v188, v188, v152, v153\n"
      "v_max3_f32 v188, v188, v154, v155\n"
      "v_mov_b32 v32, v188\n"
      "buffer_store_dwordx2 v[230:231], v2, s[44:47], s56 offen offset:768 nt sc1\n"
      "s_nop 0\n"
      "v_permlane16_swap_b32 v188, v32\n"
      "v_max_f32 v188, v188, v32\n"
      "v_mov_b32 v32, v188\n"
      "s_nop 1\n"
      "v_permlane32_swap_b32 v188, v32\n"
      "v_max_f32 v188, v188, v32\n"
      "v_sub_f32 v140, v140, v188\n"
      "v_sub_f32 v141, v141, v188\n"
      "v_sub_f32 v142, v142, v188\n"
      "s_waitcnt lgkmcnt(3)\n"
      "v_mfma_f32_16x16x32_bf16 v[32:35], a[128:131], v[124:127], 0\n"
      "v_sub_f32 v143, v143, v188\n"
      "v_sub_f32 v88, v88, v188\n"
      "s_waitcnt lgkmcnt(2)\n"
      "v_mfma_f32_16x16x32_bf16 v[32:35], a[132:135], v[48:51], v[32:35]\n"
      "v_exp_f32 v140, v140\n"
      "s_waitcnt lgkmcnt(1)\n"
      "v_mfma_f32_16x16x32_bf16 v[32:35], a[136:139], v[116:119], v[32:35]\n"
      "v_exp_f32 v141, v141\n"
      "s_waitcnt lgkmcnt(0)\n"
      "v_mfma_f32_16x16x32_bf16 v[32:35], a[140:143], v[252:255], v[32:35]\n"
      "v_exp_f32 v142, v142\n"
      "v_mfma_f32_16x16x32_bf16 v[28:31], a[144:147], v[124:127], 0\n"
      "v_exp_f32 v143, v143\n"
      "v_mfma_f32_16x16x32_bf16 v[28:31], a[148:151], v[48:51], v[28:31]\n"
      "v_cvt_pk_bf16_f32 v190, v140, v141\n"
      "v_cvt_pk_bf16_f32 v191, v142, v143\n"
      "v_mfma_f32_16x16x32_bf16 v[28:31], a[152:155], v[116:119], v[28:31]\n"
      "v_sub_f32 v89, v89, v188\n"
      "v_sub_f32 v90, v90, v188\n"
      "v_sub_f32 v91, v91, v188\n"
      "v_sub_f32 v144, v144, v188\n"
      "v_mfma_f32_16x16x32_bf16 v[28:31], a[156:159], v[252:255], v[28:31]\n"
      "v_sub_f32 v145, v145, v188\n"
      "v_sub_f32 v146, v146, v188\n"
      "v_sub_f32 v147, v147, v188\n"
      "v_sub_f32 v236, v236, v188\n"
      "v_sub_f32 v237, v237, v188\n"
      "v_sub_f32 v238, v238, v188\n"
      "v_mfma_f32_16x16x32_bf16 v[184:187], a[160:163], v[124:127], 0\n"
      "v_sub_f32 v239, v239, v188\n"
      "v_sub_f32 v232, v232, v188\n"
      "v_sub_f32 v233, v233, v188\n"
      "v_sub_f32 v234, v234, v188\n"
      "v_sub_f32 v235, v235, v188\n"
      "v_add_f32 v86, v140, v141\n"
      "v_add_f32 v87, v142, v143\n"
      "v_mfma_f32_16x16x32_bf16 v[184:187], a[164:167], v[48:51], v[184:187]\n"
      "v_exp_f32 v88, v88\n"
      "v_exp_f32 v89, v89\n"
      "v_exp_f32 v90, v90\n"
      "v_mfma_f32_16x16x32_bf16 v[184:187], a[168:171], v[116:119], v[184:187]\n"
      "v_exp_f32 v91, v91\n"
      "v_cvt_pk_bf16_f32 v140, v88, v89\n"
      "v_cvt_pk_bf16_f32 v141, v90, v91\n"
      "v_exp_f32 v144, v144\n"
      "v_mfma_f32_16x16x32_bf16 v[184:187], a[172:175], v[252:255], v[184:187]\n"
      "v_exp_f32 v145, v145\n"
      "v_exp_f32 v146, v146\n"
      "v_exp_f32 v147, v147\n"
      "v_cvt_pk_bf16_f32 v142, v144, v145\n"
      "v_mfma_f32_16x16x32_bf16 v[208:211], a[176:179], v[124:127], 0\n"
      "v_cvt_pk_bf16_f32 v143, v146, v147\n"
      "v_exp_f32 v236, v236\n"
      "v_exp_f32 v237, v237\n"
      "v_exp_f32 v238, v238\n"
      "v_mfma_f32_16x16x32_bf16 v[208:211], a[180:183], v[48:51], v[208:211]\n"
      "v_exp_f32 v239, v239\n"
      "v_cvt_pk_bf16_f32 v144, v236, v237\n"
      "v_cvt_pk_bf16_f32 v145, v238, v239\n"
      "v_exp_f32 v232, v232\n"
      "v_mfma_f32_16x16x32_bf16 v[208:211], a[184:187], v[116:119], v[208:211]\n"
      "v_exp_f32 v233, v233\n"
      "v_exp_f32 v234, v234\n"
      "v_exp_f32 v235, v235\n"
      "v_cvt_pk_bf16_f32 v146, v232, v233\n"
      "v_mfma_f32_16x16x32_bf16 v[208:211], a[188:191], v[252:255], v[208:211]\n"
      "ds_read_b128 v[156:159], v10 offset:0\n"
      "v_cvt_pk_bf16_f32 v147, v234, v235\n"
      "v_sub_f32 v160, v160, v188\n"
      "v_sub_f32 v161, v161, v188\n"
      "v_sub_f32 v162, v162, v188\n"
      "v_sub_f32 v163, v163, v188\n"
      "v_sub_f32 v152, v152, v188\n"
      "v_mfma_f32_16x16x4_4b_bf16 v[224:239], v[190:191], v[18:19], 0 cbsz:2 abid:0\n"
      "s_add_u32 m0, s53, 0\n"
      "s_add_u32 s54, s49, 0\n"
      "buffer_load_dwordx4 v1, s[40:43], s54 offen offset:0 nt lds\n"
      "v_sub_f32 v153, v153, v188\n"
      "v_sub_f32 v154, v154, v188\n"
      "v_sub_f32 v155, v155, v188\n"
      "v_exp_f32 v160, v160\n"
      "v_exp_f32 v161, v161\n"
      "v_exp_f32 v162, v162\n"
      "v_exp_f32 v163, v163\n"
      "v_cvt_pk_bf16_f32 v188, v160, v161\n"
      "v_cvt_pk_bf16_f32 v189, v162, v163\n"
      "v_exp_f32 v152, v152\n"
      "v_exp_f32 v153, v153\n"
      "v_exp_f32 v154, v154\n"
      "v_smfmac_f32_16x16x64_bf16 v[86:89], v[12:15], v[140:147], v5\n"
      "v_exp_f32 v155, v155\n"
      "v_cvt_pk_bf16_f32 v190, v152, v153\n"
      "v_mfma_f32_16x16x32_bf16 v[224:227], v[140:143], v[52:55], v[224:227]\n"
      "buffer_load_dwordx4 v1, s[40:43], s54 offen offset:2048 nt lds\n"
      "s_add_u32 m0, s53, 896\n"
      "v_cvt_pk_bf16_f32 v191, v154, v155\n"
      "v_cvt_pk_bf16_f32 v152, v32, v33\n"
      "v_cvt_pk_bf16_f32 v153, v34, v35\n"
      "v_cvt_pk_bf16_f32 v154, v28, v29\n"
      "v_mfma_f32_16x16x32_bf16 v[224:227], v[144:147], v[56:59], v[224:227]\n"
      "v_cvt_pk_bf16_f32 v155, v30, v31\n"
      "v_cvt_pk_bf16_f32 v32, v184, v185\n"
      "v_cvt_pk_bf16_f32 v33, v186, v187\n"
      "v_cvt_pk_bf16_f32 v34, v208, v209\n"
      "v_mfma_f32_16x16x32_bf16 v[224:227], v[188:191], v[120:123], v[224:227]\n"
      "buffer_load_dwordx4 v1, s[40:43], s54 offen offset:128 nt lds\n"
      "v_cvt_pk_bf16_f32 v35, v210, v211\n"
      "v_smfmac_f32_16x16x32_bf16 v[86:89], v[12:13], v[188:191], v5\n"
      "v_mfma_f32_16x16x32_bf16 v[228:231], v[140:143], v[128:131], v[228:231]\n"
      "buffer_load_dwordx4 v1, s[40:43], s54 offen offset:2176 nt lds\n"
      "v_mfma_f32_16x16x32_bf16 v[228:231], v[144:147], v[132:135], v[228:231]\n"
      "v_mfma_f32_16x16x32_bf16 v[228:231], v[188:191], v[68:71], v[228:231]\n"
      "v_add_u32 v10, 64, v10\n"
      "v_mfma_f32_16x16x32_bf16 v[232:235], v[140:143], v[100:103], v[232:235]\n"
      "v_mfma_f32_16x16x32_bf16 v[232:235], v[144:147], v[104:107], v[232:235]\n"
      "v_mfma_f32_16x16x32_bf16 v[232:235], v[188:191], v[36:39], v[232:235]\n"
      "v_add_f32 v86, v86, v87\n"
      "v_rcp_f32 v87, v86\n"
      "v_mfma_f32_16x16x32_bf16 v[236:239], v[140:143], v[192:195], v[236:239]\n"
      "ds_bpermute_b32 v88, v4, v87\n"
      "v_mfma_f32_16x16x32_bf16 v[236:239], v[144:147], v[196:199], v[236:239]\n"
      "v_mfma_f32_16x16x32_bf16 v[236:239], v[188:191], v[240:243], v[236:239]\n"
      "s_waitcnt lgkmcnt(0)\n"
      "v_mul_f32_dpp v224, v88, v224 row_newbcast:0 row_mask:0xf bank_mask:0xf\n"
      "v_mul_f32_dpp v225, v88, v225 row_newbcast:4 row_mask:0xf bank_mask:0xf\n"
      "v_mul_f32_dpp v226, v88, v226 row_newbcast:8 row_mask:0xf bank_mask:0xf\n"
      "v_mul_f32_dpp v227, v88, v227 row_newbcast:12 row_mask:0xf bank_mask:0xf\n"
      "v_mul_f32_dpp v228, v88, v228 row_newbcast:0 row_mask:0xf bank_mask:0xf\n"
      "v_mul_f32_dpp v229, v88, v229 row_newbcast:4 row_mask:0xf bank_mask:0xf\n"
      "v_mfma_f32_16x16x32_bf16 v[224:227], v[136:139], a[192:195], v[224:227]\n"
      "v_mul_f32_dpp v230, v88, v230 row_newbcast:8 row_mask:0xf bank_mask:0xf\n"
      "v_mul_f32_dpp v231, v88, v231 row_newbcast:12 row_mask:0xf bank_mask:0xf\n"
      "v_mul_f32_dpp v232, v88, v232 row_newbcast:0 row_mask:0xf bank_mask:0xf\n"
      "v_mfma_f32_16x16x32_bf16 v[224:227], v[148:151], a[196:199], v[224:227]\n"
      "v_mul_f32_dpp v233, v88, v233 row_newbcast:4 row_mask:0xf bank_mask:0xf\n"
      "v_mul_f32_dpp v234, v88, v234 row_newbcast:8 row_mask:0xf bank_mask:0xf\n"
      "v_mul_f32_dpp v235, v88, v235 row_newbcast:12 row_mask:0xf bank_mask:0xf\n"
      "v_mul_f32_dpp v236, v88, v236 row_newbcast:0 row_mask:0xf bank_mask:0xf\n"
      "v_mfma_f32_16x16x32_bf16 v[224:227], v[24:27], a[200:203], v[224:227]\n"
      "v_mul_f32_dpp v237, v88, v237 row_newbcast:4 row_mask:0xf bank_mask:0xf\n"
      "v_mul_f32_dpp v238, v88, v238 row_newbcast:8 row_mask:0xf bank_mask:0xf\n"
      "v_mul_f32_dpp v239, v88, v239 row_newbcast:12 row_mask:0xf bank_mask:0xf\n"
      "v_mfma_f32_16x16x32_bf16 v[224:227], v[20:23], a[204:207], v[224:227]\n"
      "v_mfma_f32_16x16x32_bf16 v[228:231], v[136:139], a[208:211], v[228:231]\n"
      "v_mfma_f32_16x16x32_bf16 v[228:231], v[148:151], a[212:215], v[228:231]\n"
      "v_mfma_f32_16x16x32_bf16 v[228:231], v[24:27], a[216:219], v[228:231]\n"
      "v_mfma_f32_16x16x32_bf16 v[228:231], v[20:23], a[220:223], v[228:231]\n"
      "v_mfma_f32_16x16x32_bf16 v[232:235], v[136:139], a[224:227], v[232:235]\n"
      "v_mfma_f32_16x16x32_bf16 v[232:235], v[148:151], a[228:231], v[232:235]\n"
      "v_mfma_f32_16x16x32_bf16 v[232:235], v[24:27], a[232:235], v[232:235]\n"
      "v_mfma_f32_16x16x32_bf16 v[232:235], v[20:23], a[236:239], v[232:235]\n"
      "v_mfma_f32_16x16x32_bf16 v[236:239], v[136:139], a[240:243], v[236:239]\n"
      "v_mfma_f32_16x16x32_bf16 v[236:239], v[148:151], a[244:247], v[236:239]\n"
      "v_mfma_f32_16x16x32_bf16 v[236:239], v[24:27], a[248:251], v[236:239]\n"
      "v_mfma_f32_16x16x32_bf16 v[236:239], v[20:23], a[252:255], v[236:239]\n"
      "v_cvt_pk_bf16_f32 v224, v224, v228\n"
      "v_cvt_pk_bf16_f32 v228, v225, v229\n"
      "v_pk_max_i16 v224, v224, 0\n"
      "v_pk_max_i16 v228, v228, 0\n"
      "v_cvt_pk_bf16_f32 v226, v226, v230\n"
      "v_cvt_pk_bf16_f32 v230, v227, v231\n"
      "v_pk_max_i16 v226, v226, 0\n"
      "v_mfma_f32_16x16x32_bf16 v[148:151], v[172:175], v[152:155], 0\n"
      "v_cvt_pk_bf16_f32 v225, v232, v236\n"
      "v_cvt_pk_bf16_f32 v229, v233, v237\n"
      "v_pk_max_i16 v225, v225, 0\n"
      "v_pk_max_i16 v229, v229, 0\n"
      "v_cvt_pk_bf16_f32 v227, v234, v238\n"
      "v_cvt_pk_bf16_f32 v231, v235, v239\n"
      "v_pk_max_i16 v227, v227, 0\n"
      "v_mfma_f32_16x16x32_bf16 v[148:151], v[176:179], v[32:35], v[148:151]\n"
      "v_pk_max_i16 v230, v230, 0\n"
      "v_pk_max_i16 v231, v231, 0\n"
      "v_mfma_f32_16x16x32_bf16 v[136:139], v[108:111], v[152:155], 0\n"
      "v_mfma_f32_16x16x32_bf16 v[136:139], v[112:115], v[32:35], v[136:139]\n"
      "v_mfma_f32_16x16x32_bf16 v[236:239], v[164:167], v[152:155], 0\n"
      "v_mfma_f32_16x16x32_bf16 v[236:239], v[168:171], v[32:35], v[236:239]\n"
      "v_mfma_f32_16x16x32_bf16 v[160:163], v[40:43], v[152:155], 0\n"
      "v_mfma_f32_16x16x32_bf16 v[160:163], v[44:47], v[32:35], v[160:163]\n"
      "v_max_f32 v208, v148, v149\n"
      "v_max3_f32 v208, v208, v150, v151\n"
      "v_mfma_f32_16x16x32_bf16 v[28:31], v[92:95], v[152:155], 0\n"
      "v_mfma_f32_16x16x32_bf16 v[28:31], v[96:99], v[32:35], v[28:31]\n"
      "v_max3_f32 v208, v208, v136, v137\n"
      "v_max3_f32 v208, v208, v138, v139\n"
      "v_max3_f32 v208, v208, v236, v237\n"
      "v_max3_f32 v208, v208, v238, v239\n"
      "v_max3_f32 v208, v208, v160, v161\n"
      "v_max3_f32 v208, v208, v162, v163\n"
      "v_mfma_f32_16x16x32_bf16 v[24:27], v[216:219], v[152:155], 0\n"
      "v_mfma_f32_16x16x32_bf16 v[24:27], v[220:223], v[32:35], v[24:27]\n"
      "v_max3_f32 v208, v208, v28, v29\n"
      "v_max3_f32 v208, v208, v30, v31\n"
      "v_mfma_f32_16x16x32_bf16 v[20:23], v[244:247], v[152:155], 0\n"
      "v_mfma_f32_16x16x32_bf16 v[20:23], v[248:251], v[32:35], v[20:23]\n"
      "ds_read_b64 v[152:153], v11 offset:4096\n"
      "s_nop 2\n"
      "v_max3_f32 v208, v208, v24, v25\n"
      "v_max3_f32 v208, v208, v26, v27\n"
      "s_nop 1\n"
      "v_max3_f32 v208, v208, v20, v21\n"
      "v_max3_f32 v208, v208, v22, v23\n"
      "v_mov_b32 v32, v208\n"
      "s_nop 1\n"
      "v_permlane16_swap_b32 v208, v32\n"
      "v_max_f32 v208, v208, v32\n"
      "v_mov_b32 v32, v208\n"
      "s_nop 1\n"
      "v_permlane32_swap_b32 v208, v32\n"
      "v_max_f32 v208, v208, v32\n"
      "v_sub_f32 v148, v148, v208\n"
      "v_sub_f32 v149, v149, v208\n"
      "v_sub_f32 v150, v150, v208\n"
      "v_sub_f32 v151, v151, v208\n"
      "v_exp_f32 v148, v148\n"
      "v_exp_f32 v149, v149\n"
      "v_exp_f32 v150, v150\n"
      "v_exp_f32 v151, v151\n"
      "v_cvt_pk_bf16_f32 v234, v148, v149\n"
      "v_cvt_pk_bf16_f32 v235, v150, v151\n"
      "v_sub_f32 v136, v136, v208\n"
      "v_sub_f32 v137, v137, v208\n"
      "v_sub_f32 v138, v138, v208\n"
      "v_sub_f32 v139, v139, v208\n"
      "v_sub_f32 v236, v236, v208\n"
      "v_sub_f32 v237, v237, v208\n"
      "v_sub_f32 v238, v238, v208\n"
      "v_sub_f32 v239, v239, v208\n"
      "v_sub_f32 v160, v160, v208\n"
      "v_sub_f32 v161, v161, v208\n"
      "v_sub_f32 v162, v162, v208\n"
      "v_sub_f32 v163, v163, v208\n"
      "v_sub_f32 v28, v28, v208\n"
      "v_sub_f32 v29, v29, v208\n"
      "v_sub_f32 v30, v30, v208\n"
      "v_sub_f32 v31, v31, v208\n"
      "v_add_f32 v32, v148, v149\n"
      "v_add_f32 v33, v150, v151\n"
      "v_exp_f32 v136, v136\n"
      "v_exp_f32 v137, v137\n"
      "v_exp_f32 v138, v138\n"
      "v_exp_f32 v139, v139\n"
      "v_cvt_pk_bf16_f32 v232, v136, v137\n"
      "v_cvt_pk_bf16_f32 v233, v138, v139\n"
      "v_exp_f32 v236, v236\n"
      "v_mfma_f32_16x16x4_4b_bf16 v[136:151], v[234:235], v[18:19], 0 cbsz:2 abid:0\n"
      "v_exp_f32 v237, v237\n"
      "v_exp_f32 v238, v238\n"
      "v_exp_f32 v239, v239\n"
      "v_cvt_pk_bf16_f32 v234, v236, v237\n"
      "v_cvt_pk_bf16_f32 v235, v238, v239\n"
      "v_exp_f32 v160, v160\n"
      "v_exp_f32 v161, v161\n"
      "v_exp_f32 v162, v162\n"
      "v_exp_f32 v163, v163\n"
      "v_cvt_pk_bf16_f32 v236, v160, v161\n"
      "v_cvt_pk_bf16_f32 v237, v162, v163\n"
      "v_exp_f32 v28, v28\n"
      "v_exp_f32 v29, v29\n"
      "v_exp_f32 v30, v30\n"
      "v_exp_f32 v31, v31\n"
      "v_cvt_pk_bf16_f32 v238, v28, v29\n"
      "v_cvt_pk_bf16_f32 v239, v30, v31\n"
      "v_sub_f32 v24, v24, v208\n"
      "v_sub_f32 v25, v25, v208\n"
      "v_sub_f32 v26, v26, v208\n"
      "v_sub_f32 v27, v27, v208\n"
      "v_sub_f32 v20, v20, v208\n"
      "v_sub_f32 v21, v21, v208\n"
      "v_sub_f32 v22, v22, v208\n"
      "v_sub_f32 v23, v23, v208\n"
      "v_exp_f32 v24, v24\n"
      "v_exp_f32 v25, v25\n"
      "v_exp_f32 v26, v26\n"
      "v_exp_f32 v27, v27\n"
      "v_cvt_pk_bf16_f32 v208, v24, v25\n"
      "v_cvt_pk_bf16_f32 v209, v26, v27\n"
      "v_exp_f32 v20, v20\n"
      "v_exp_f32 v21, v21\n"
      "v_exp_f32 v22, v22\n"
      "v_exp_f32 v23, v23\n"
      "v_cvt_pk_bf16_f32 v210, v20, v21\n"
      "v_cvt_pk_bf16_f32 v211, v22, v23\n"
      "v_smfmac_f32_16x16x64_bf16 v[32:35], v[12:15], v[232:239], v5\n"
      "s_add_u32 s56, s48, 4096\n"
      "buffer_store_dwordx2 v[224:225], v2, s[44:47], s56 offen offset:0 nt sc1\n"
      "v_mfma_f32_16x16x32_bf16 v[136:139], v[232:235], v[52:55], v[136:139]\n"
      "buffer_store_dwordx2 v[228:229], v2, s[44:47], s56 offen offset:256 nt sc1\n"
      "v_mfma_f32_16x16x32_bf16 v[136:139], v[236:239], v[56:59], v[136:139]\n"
      "buffer_store_dwordx2 v[226:227], v2, s[44:47], s56 offen offset:512 nt sc1\n"
      "v_mfma_f32_16x16x32_bf16 v[136:139], v[208:211], v[120:123], v[136:139]\n"
      "buffer_store_dwordx2 v[230:231], v2, s[44:47], s56 offen offset:768 nt sc1\n"
      "v_smfmac_f32_16x16x32_bf16 v[32:35], v[12:13], v[208:211], v5\n"
      "s_waitcnt lgkmcnt(0)\n"
      "v_lshlrev_b32 v228, 16, v152\n"
      "v_and_b32 v229, 0xffff0000, v152\n"
      "v_mfma_f32_16x16x32_bf16 v[140:143], v[232:235], v[128:131], v[140:143]\n"
      "v_mov_b64 v[224:225], 0\n"
      "v_lshlrev_b32 v230, 16, v153\n"
      "v_and_b32 v231, 0xffff0000, v153\n"
      "v_mfma_f32_16x16x32_bf16 v[140:143], v[236:239], v[132:135], v[140:143]\n"
      "v_mov_b64 v[20:21], 0\n"
      "v_add_f32 v32, v32, v33\n"
      "v_rcp_f32 v33, v32\n"
      "v_mfma_f32_16x16x32_bf16 v[140:143], v[208:211], v[68:71], v[140:143]\n"
      "ds_bpermute_b32 v34, v4, v33\n"
      "v_mov_b64 v[28:29], 0\n"
      "v_mfma_f32_16x16x32_bf16 v[144:147], v[232:235], v[100:103], v[144:147]\n"
      "v_mov_b64 v[160:161], 0\n"
      "v_mfma_f32_16x16x32_bf16 v[144:147], v[236:239], v[104:107], v[144:147]\n"
      "v_mov_b64 v[24:25], 0\n"
      "v_mfma_f32_16x16x32_bf16 v[144:147], v[208:211], v[36:39], v[144:147]\n"
      "v_mov_b64 v[32:33], 0\n"
      "v_mfma_f32_16x16x32_bf16 v[148:151], v[232:235], v[192:195], v[148:151]\n"
      "v_mov_b64 v[232:233], 0\n"
      "v_mfma_f32_16x16x32_bf16 v[148:151], v[236:239], v[196:199], v[148:151]\n"
      "v_mfma_f32_16x16x32_bf16 v[148:151], v[208:211], v[240:243], v[148:151]\n"
      "s_waitcnt lgkmcnt(0)\n"
      "v_mul_f32_dpp v136, v34, v136 row_newbcast:0 row_mask:0xf bank_mask:0xf\n"
      "v_mul_f32_dpp v137, v34, v137 row_newbcast:4 row_mask:0xf bank_mask:0xf\n"
      "v_mul_f32_dpp v138, v34, v138 row_newbcast:8 row_mask:0xf bank_mask:0xf\n"
      "v_mul_f32_dpp v139, v34, v139 row_newbcast:12 row_mask:0xf bank_mask:0xf\n"
      "v_mul_f32_dpp v140, v34, v140 row_newbcast:0 row_mask:0xf bank_mask:0xf\n"
      "v_mul_f32_dpp v141, v34, v141 row_newbcast:4 row_mask:0xf bank_mask:0xf\n"
      "v_mfma_f32_16x16x32_bf16 v[136:139], v[124:127], a[192:195], v[136:139]\n"
      "v_mul_f32_dpp v142, v34, v142 row_newbcast:8 row_mask:0xf bank_mask:0xf\n"
      "v_mul_f32_dpp v143, v34, v143 row_newbcast:12 row_mask:0xf bank_mask:0xf\n"
      "v_mul_f32_dpp v144, v34, v144 row_newbcast:0 row_mask:0xf bank_mask:0xf\n"
      "v_mfma_f32_16x16x32_bf16 v[136:139], v[48:51], a[196:199], v[136:139]\n"
      "v_mul_f32_dpp v145, v34, v145 row_newbcast:4 row_mask:0xf bank_mask:0xf\n"
      "v_mul_f32_dpp v146, v34, v146 row_newbcast:8 row_mask:0xf bank_mask:0xf\n"
      "v_mul_f32_dpp v147, v34, v147 row_newbcast:12 row_mask:0xf bank_mask:0xf\n"
      "v_mul_f32_dpp v148, v34, v148 row_newbcast:0 row_mask:0xf bank_mask:0xf\n"
      "v_mfma_f32_16x16x32_bf16 v[136:139], v[116:119], a[200:203], v[136:139]\n"
      "v_mul_f32_dpp v149, v34, v149 row_newbcast:4 row_mask:0xf bank_mask:0xf\n"
      "v_mul_f32_dpp v150, v34, v150 row_newbcast:8 row_mask:0xf bank_mask:0xf\n"
      "v_mul_f32_dpp v151, v34, v151 row_newbcast:12 row_mask:0xf bank_mask:0xf\n"
      "v_add_u32 v11, 0x200, v11\n"
      "v_mfma_f32_16x16x32_bf16 v[136:139], v[252:255], a[204:207], v[136:139]\n"
      "v_mfma_f32_16x16x32_bf16 v[140:143], v[124:127], a[208:211], v[140:143]\n"
      "v_mfma_f32_16x16x32_bf16 v[140:143], v[48:51], a[212:215], v[140:143]\n"
      "v_mfma_f32_16x16x32_bf16 v[140:143], v[116:119], a[216:219], v[140:143]\n"
      "v_mfma_f32_16x16x32_bf16 v[140:143], v[252:255], a[220:223], v[140:143]\n"
      "s_add_u32 s56, s48, 0\n"
      "v_mfma_f32_16x16x32_bf16 v[144:147], v[124:127], a[224:227], v[144:147]\n"
      "v_mfma_f32_16x16x32_bf16 v[144:147], v[48:51], a[228:231], v[144:147]\n"
      "v_mfma_f32_16x16x32_bf16 v[144:147], v[116:119], a[232:235], v[144:147]\n"
      "v_mfma_f32_16x16x32_bf16 v[144:147], v[252:255], a[236:239], v[144:147]\n"
      "v_mfma_f32_16x16x32_bf16 v[148:151], v[124:127], a[240:243], v[148:151]\n"
      "v_mfma_f32_16x16x32_bf16 v[148:151], v[48:51], a[244:247], v[148:151]\n"
      "v_mfma_f32_16x16x32_bf16 v[148:151], v[116:119], a[248:251], v[148:151]\n"
      "v_cvt_pk_bf16_f32 v136, v136, v140\n"
      "v_cvt_pk_bf16_f32 v140, v137, v141\n"
      "v_pk_max_i16 v136, v136, 0\n"
      "v_pk_max_i16 v140, v140, 0\n"
      "v_cvt_pk_bf16_f32 v138, v138, v142\n"
      "v_cvt_pk_bf16_f32 v142, v139, v143\n"
      "v_pk_max_i16 v138, v138, 0\n"
      "v_mfma_f32_16x16x32_bf16 v[148:151], v[252:255], a[252:255], v[148:151]\n"
      "v_pk_max_i16 v142, v142, 0\n"
      "v_smfmac_f32_16x16x64_bf16 v[224:227], v[156:159], v[108:115], v5\n"
      "v_smfmac_f32_16x16x64_bf16 v[20:23], v[156:159], v[164:171], v5\n"
      "v_smfmac_f32_16x16x64_bf16 v[28:31], v[156:159], v[40:47], v5\n"
      "v_smfmac_f32_16x16x64_bf16 v[160:163], v[156:159], v[92:99], v5\n"
      "v_smfmac_f32_16x16x64_bf16 v[24:27], v[156:159], v[216:223], v5\n"
      "v_smfmac_f32_16x16x64_bf16 v[32:35], v[156:159], v[244:251], v5\n"
      "v_smfmac_f32_16x16x64_bf16 v[232:235], v[156:159], v[172:179], v5\n"
      "v_cvt_pk_bf16_f32 v137, v144, v148\n"
      "v_cvt_pk_bf16_f32 v141, v145, v149\n"
      "v_add_f32 v224, v224, v225\n"
      "v_add_f32 v20, v20, v21\n"
      "v_add_f32 v28, v28, v29\n"
      "v_add_f32 v160, v160, v161\n"
      "v_add_f32 v24, v24, v25\n"
      "v_add_f32 v32, v32, v33\n"
      "v_add_f32 v232, v232, v233\n"
      "v_max3_f32 v148, v224, v20, v28\n"
      "v_max3_f32 v148, v148, v160, v24\n"
      "v_max3_f32 v148, v148, v32, v232\n"
      "v_mov_b32 v233, 0\n"
      "v_pk_max_i16 v137, v137, 0\n"
      "v_max_f32_dpp v148, v148, v148 row_ror:8 row_mask:0xf bank_mask:0xf\n"
      "v_pk_max_i16 v141, v141, 0\n"
      "v_cvt_pk_bf16_f32 v139, v146, v150\n"
      "v_max_f32_dpp v148, v148, v148 row_ror:4 row_mask:0xf bank_mask:0xf\n"
      "v_cvt_pk_bf16_f32 v143, v147, v151\n"
      "v_pk_max_i16 v139, v139, 0\n"
      "v_max_f32_dpp v148, v148, v148 row_ror:2 row_mask:0xf bank_mask:0xf\n"
      "v_pk_max_i16 v143, v143, 0\n"
      "buffer_store_dwordx2 v[136:137], v2, s[44:47], s56 offen offset:0 nt sc1\n"
      "v_max_f32_dpp v148, v148, v148 row_ror:1 row_mask:0xf bank_mask:0xf\n"
      "v_sub_f32 v232, v232, v148\n"
      "v_sub_f32 v224, v224, v148\n"
      "v_sub_f32 v20, v20, v148\n"
      "v_exp_f32_dpp v233, v232 quad_perm:[0,1,2,3] row_mask:0xf bank_mask:0x1\n"
      "v_sub_f32 v28, v28, v148\n"
      "v_sub_f32 v160, v160, v148\n"
      "v_mov_b32_dpp v232, v233 quad_perm:[1,1,3,3] row_mask:0xf bank_mask:0xf\n"
      "v_cvt_pk_bf16_f32 v232, v233, v232\n"
      "ds_bpermute_b32 v136, v7, v232 offset:248\n"
      "ds_bpermute_b32 v137, v7, v232 offset:8\n"
      "v_sub_f32 v24, v24, v148\n"
      "v_sub_f32 v32, v32, v148\n"
      "v_exp_f32 v224, v224\n"
      "v_exp_f32 v20, v20\n"
      "v_exp_f32 v28, v28\n"
      "v_exp_f32 v160, v160\n"
      "v_exp_f32 v24, v24\n"
      "v_exp_f32 v32, v32\n"
      "v_cndmask_b32_dpp v225, v28, v224, vcc quad_perm:[0,0,2,2] row_mask:0xf bank_mask:0xf\n"
      "v_cndmask_b32_dpp v21, v160, v20, vcc quad_perm:[0,0,2,2] row_mask:0xf bank_mask:0xf\n"
      "v_cndmask_b32_dpp v25, v32, v24, vcc quad_perm:[0,0,2,2] row_mask:0xf bank_mask:0xf\n"
      "s_not_b64 vcc, vcc\n"
      "v_cndmask_b32_dpp v29, v224, v28, vcc quad_perm:[1,1,3,3] row_mask:0xf bank_mask:0xf\n"
      "v_cndmask_b32_dpp v161, v20, v160, vcc quad_perm:[1,1,3,3] row_mask:0xf bank_mask:0xf\n"
      "v_cvt_pk_bf16_f32 v225, v225, v29\n"
      "v_cvt_pk_bf16_f32 v21, v21, v161\n"
      "ds_bpermute_b32 v148, v6, v225\n"
      "ds_bpermute_b32 v149, v6, v21\n"
      "ds_bpermute_b32 v150, v6, v225 offset:16\n"
      "ds_bpermute_b32 v151, v6, v21 offset:16\n"
      "v_cndmask_b32_dpp v33, v24, v32, vcc quad_perm:[1,1,3,3] row_mask:0xf bank_mask:0xf\n"
      "v_cvt_pk_bf16_f32 v25, v25, v33\n"
      "ds_bpermute_b32 v146, v7, v25\n"
      "ds_bpermute_b32 v147, v7, v25 offset:4\n"
      "buffer_store_dwordx2 v[140:141], v2, s[44:47], s56 offen offset:256 nt sc1\n"
      "buffer_store_dwordx2 v[138:139], v2, s[44:47], s56 offen offset:512 nt sc1\n"
      "s_waitcnt lgkmcnt(6)\n"
      "v_mfma_f32_16x16x4_4b_bf16 v[164:179], v[136:137], v[18:19], 0 cbsz:2 abid:0\n"
      "buffer_store_dwordx2 v[142:143], v2, s[44:47], s56 offen offset:768 nt sc1\n"
      "s_not_b64 vcc, vcc\n"
      "v_add_f32 v224, v224, v20\n"
      "v_add_f32 v28, v28, v160\n"
      "v_add_f32 v24, v24, v32\n"
      "v_add_f32 v224, v224, v28\n"
      "v_add_f32 v24, v24, v233\n"
      "v_add_f32 v142, v224, v24\n"
      "s_add_u32 s56, s48, 24576\n"
      "s_nop 0\n"
      "v_add_f32_dpp v142, v142, v142 row_ror:8 row_mask:0xf bank_mask:0xf\n"
      "s_nop 1\n"
      "v_add_f32_dpp v142, v142, v142 row_ror:4 row_mask:0xf bank_mask:0xf\n"
      "s_nop 1\n"
      "v_add_f32_dpp v142, v142, v142 row_ror:2 row_mask:0xf bank_mask:0xf\n"
      "s_waitcnt lgkmcnt(2)\n"
      "v_smfmac_f32_16x16x64_bf16 v[164:167], v[148:151], v[52:59], v5\n"
      "v_smfmac_f32_16x16x64_bf16 v[168:171], v[148:151], v[128:135], v5\n"
      "v_add_f32_dpp v142, v142, v142 row_ror:1 row_mask:0xf bank_mask:0xf\n"
      "v_smfmac_f32_16x16x64_bf16 v[172:175], v[148:151], v[100:107], v5\n"
      "v_smfmac_f32_16x16x64_bf16 v[176:179], v[148:151], v[192:199], v5\n"
      "v_rcp_f32 v143, v142\n"
      "s_waitcnt lgkmcnt(0)\n"
      "v_smfmac_f32_16x16x32_bf16 v[164:167], v[146:147], v[120:123], v5\n"
      "v_smfmac_f32_16x16x32_bf16 v[168:171], v[146:147], v[68:71], v5\n"
      "s_nop 0\n"
      "v_smfmac_f32_16x16x32_bf16 v[172:175], v[146:147], v[36:39], v5\n"
      "v_smfmac_f32_16x16x32_bf16 v[176:179], v[146:147], v[240:243], v5\n"
      "s_nop 3\n"
      "v_add_f32 v164, v164, v165\n"
      "v_fma_f32 v164, v164, v143, v228\n"
      "v_add_f32 v168, v168, v169\n"
      "v_fma_f32 v168, v168, v143, v229\n"
      "v_cvt_pk_bf16_f32 v164, v164, v168\n"
      "v_pk_max_i16 v164, v164, 0\n"
      "v_add_f32 v172, v172, v173\n"
      "v_fma_f32 v172, v172, v143, v230\n"
      "v_add_f32 v176, v176, v177\n"
      "v_fma_f32 v176, v176, v143, v231\n"
      "v_cvt_pk_bf16_f32 v165, v172, v176\n"
      "v_pk_max_i16 v165, v165, 0\n"
      "buffer_store_dwordx2 v[164:165], v3, s[44:47], s56 offen offset:0 nt sc1\n"
      "s_mov_b32 s48, s49\n"
      "s_add_u32 s49, s49, 13107200\n"
      "s_sub_u32 s50, s50, 1\n"
      "s_cmp_eq_u32 s50, 1\n"
      "s_cselect_b32 s49, 0x80000000, s49\n"
      "s_cmp_lg_u32 s50, 0\n"
      "s_cbranch_scc1 L_safe_%=\n"
      "s_waitcnt vmcnt(0) lgkmcnt(0)\n"
      "L_done_%=:\n"
      "s_endpgm\n"
      ".p2align 12\n"
      :
      : [ka] "s"(__builtin_amdgcn_kernarg_segment_ptr()), [g] "s"(blockIdx.x), [wave] "s"(wave), [ldsb] "s"(ldsb)
      : "v0", "v1", "v2", "v3", "v4", "v5", "v6", "v7", "v8", "v9", "v10", "v11", "v12", "v13", "v14", "v15", "v16", "v17", "v18", "v19", "v20", "v21", "v22", "v23", "v24", "v25", "v26", "v27", "v28", "v29", "v30", "v31", "v32", "v33", "v34", "v35", "v36", "v37", "v38", "v39", "v40", "v41", "v42", "v43", "v44", "v45", "v46", "v47", "v48", "v49", "v50", "v51", "v52", "v53", "v54", "v55", "v56", "v57", "v58", "v59", "v60", "v61", "v62", "v63", "v64", "v65", "v66", "v67", "v68", "v69", "v70", "v71", "v72", "v73", "v74", "v75", "v76", "v77", "v78", "v79", "v80", "v81", "v82", "v83", "v84", "v85", "v86", "v87", "v88", "v89", "v90", "v91", "v92", "v93", "v94", "v95", "v96", "v97", "v98", "v99", "v100", "v101", "v102", "v103", "v104", "v105", "v106", "v107", "v108", "v109", "v110", "v111", "v112", "v113", "v114", "v115", "v116", "v117", "v118", "v119", "v120", "v121", "v122", "v123", "v124", "v125", "v126", "v127", "v128", "v129", "v130", "v131", "v132", "v133", "v134", "v135", "v136", "v137", "v138", "v139", "v140", "v141", "v142", "v143", "v144", "v145", "v146", "v147", "v148", "v149", "v150", "v151", "v152", "v153", "v154", "v155", "v156", "v157", "v158", "v159", "v160", "v161", "v162", "v163", "v164", "v165", "v166", "v167", "v168", "v169", "v170", "v171", "v172", "v173", "v174", "v175", "v176", "v177", "v178", "v179", "v180", "v181", "v182", "v183", "v184", "v185", "v186", "v187", "v188", "v189", "v190", "v191", "v192", "v193", "v194", "v195", "v196", "v197", "v198", "v199", "v200", "v201", "v202", "v203", "v204", "v205", "v206", "v207", "v208", "v209", "v210", "v211", "v212", "v213", "v214", "v215", "v216", "v217", "v218", "v219", "v220", "v221", "v222", "v223", "v224", "v225", "v226", "v227", "v228", "v229", "v230", "v231", "v232", "v233", "v234", "v235", "v236", "v237", "v238", "v239", "v240", "v241", "v242", "v243", "v244", "v245", "v246", "v247", "v248", "v249", "v250", "v251", "v252", "v253", "v254", "v255", "a0", "a1", "a2", "a3", "a4", "a5", "a6", "a7", "a8", "a9", "a10", "a11", "a12", "a13", "a14", "a15", "a16", "a17", "a18", "a19", "a20", "a21", "a22", "a23", "a24", "a25", "a26", "a27", "a28", "a29", "a30", "a31", "a32", "a33", "a34", "a35", "a36", "a37", "a38", "a39", "a40", "a41", "a42", "a43", "a44", "a45", "a46", "a47", "a48", "a49", "a50", "a51", "a52", "a53", "a54", "a55", "a56", "a57", "a58", "a59", "a60", "a61", "a62", "a63", "a64", "a65", "a66", "a67", "a68", "a69", "a70", "a71", "a72", "a73", "a74", "a75", "a76", "a77", "a78", "a79", "a80", "a81", "a82", "a83", "a84", "a85", "a86", "a87", "a88", "a89", "a90", "a91", "a92", "a93", "a94", "a95", "a96", "a97", "a98", "a99", "a100", "a101", "a102", "a103", "a104", "a105", "a106", "a107", "a108", "a109", "a110", "a111", "a112", "a113", "a114", "a115", "a116", "a117", "a118", "a119", "a120", "a121", "a122", "a123", "a124", "a125", "a126", "a127", "a128", "a129", "a130", "a131", "a132", "a133", "a134", "a135", "a136", "a137", "a138", "a139", "a140", "a141", "a142", "a143", "a144", "a145", "a146", "a147", "a148", "a149", "a150", "a151", "a152", "a153", "a154", "a155", "a156", "a157", "a158", "a159", "a160", "a161", "a162", "a163", "a164", "a165", "a166", "a167", "a168", "a169", "a170", "a171", "a172", "a173", "a174", "a175", "a176", "a177", "a178", "a179", "a180", "a181", "a182", "a183", "a184", "a185", "a186", "a187", "a188", "a189", "a190", "a191", "a192", "a193", "a194", "a195", "a196", "a197", "a198", "a199", "a200", "a201", "a202", "a203", "a204", "a205", "a206", "a207", "a208", "a209", "a210", "a211", "a212", "a213", "a214", "a215", "a216", "a217", "a218", "a219", "a220", "a221", "a222", "a223", "a224", "a225", "a226", "a227", "a228", "a229", "a230", "a231", "a232", "a233", "a234", "a235", "a236", "a237", "a238", "a239", "a240", "a241", "a242", "a243", "a244", "a245", "a246", "a247", "a248", "a249", "a250", "a251", "a252", "a253", "a254", "a255", "s4", "s5", "s6", "s7", "s8", "s9", "s10", "s11", "s12", "s13", "s14", "s15", "s16", "s17", "s18", "s19", "s20", "s21", "s22", "s23", "s24", "s25", "s26", "s27", "s28", "s29", "s30", "s31", "s32", "s33", "s38", "s39", "s40", "s41", "s42", "s43", "s44", "s45", "s46", "s47", "s48", "s49", "s50", "s51", "s52", "s53", "s54", "s55", "s56", "s57", "s73", "s74", "s75", "s76", "s77", "s78", "s79", "s90", "s91", "s92", "s93", "s94", "s95", "s100", "s101", "s34", "s35", "vcc", "scc", "m0", "memory");
}

extern "C" void kernel_launch(void* const* d_in, const int* in_sizes, int n_in,
                              void* d_out, int out_size, void* d_ws, size_t ws_size,
                              hipStream_t stream) {
  const uint16_t* e = (const uint16_t*)d_in[0];
  const uint16_t* wq = (const uint16_t*)d_in[1];
  const uint16_t* wk = (const uint16_t*)d_in[2];
  const uint16_t* wv = (const uint16_t*)d_in[3];
  const uint16_t* wres = (const uint16_t*)d_in[4];
  uint16_t* out = (uint16_t*)d_out;
  autoint_kernel<<<256, 256, 0, stream>>>(e, wq, wk, wv, wres, out, (uint8_t*)d_ws);
}
```

```cpp
#include <hip/hip_runtime.h>
#include <stdint.h>

__global__ __launch_bounds__(256, 1)
void autoint_kernel(const uint16_t* __restrict__ e, const uint16_t* __restrict__ wq,
                    const uint16_t* __restrict__ wk, const uint16_t* __restrict__ wv,
                    const uint16_t* __restrict__ wres, uint16_t* out, uint8_t* ws) {
  __shared__ __attribute__((aligned(16))) uint8_t lds[163840];
  const unsigned wave = __builtin_amdgcn_readfirstlane(threadIdx.x >> 6);
  const unsigned ldsb = __builtin_amdgcn_readfirstlane((unsigned)(size_t)lds);
  asm volatile(
      "s_load_dwordx8 s[4:11], %[ka], 0x0\n"
      "s_load_dwordx4 s[12:15], %[ka], 0x20\n"
      "s_load_dwordx2 s[16:17], %[ka], 0x30\n"
      "s_lshl_b32 s22, %[wave], 3\n"
      "s_add_u32 s22, s22, 8\n"
      "s_load_dwordx2 s[20:21], %[ka], s22\n"
      "v_mbcnt_lo_u32_b32 v240, -1, 0\n"
      "v_mbcnt_hi_u32_b32 v240, -1, v240\n"
      "s_getpc_b64 s[50:51]\n"
      "L_ai_pc:\n"
      "s_bfe_u32 s54, %[g], 0x20003\n"
      "s_lshl2_add_u32 s54, s54, %[wave]\n"
      "s_lshl_b32 s54, s54, 13\n"
      "v_lshlrev_b32 v250, 7, v240\n"
      "v_add_u32 v250, s54, v250\n"
      "v_min_u32 v250, L_ai_end-L_ai_pc, v250\n"
      "global_load_dword v17, v250, s[50:51]\n"
      "s_bfe_u32 s19, %[g], 0x10003\n"
      "s_and_b32 s18, %[g], 7\n"
      "s_lshr_b32 s54, %[g], 4\n"
      "s_lshl4_add_u32 s18, s18, s54\n"
      "s_lshl_b32 s18, s18, 2\n"
      "s_add_u32 s18, s18, %[wave]\n"
      "s_mul_i32 s18, s18, 25600\n"
      "s_mov_b32 s46, 32768\n"
      "s_mov_b32 s47, 0x20000\n"
      "v_lshrrev_b32 v234, 3, v240\n"
      "v_and_b32 v235, 7, v240\n"
      "v_lshlrev_b32 v233, 4, v235\n"
      "v_lshl_add_u32 v246, v234, 9, v233\n"
      "v_lshl_add_u32 v236, v234, 10, v233\n"
      "s_lshl_b32 s52, %[wave], 12\n"
      "s_lshl_b32 s55, s19, 14\n"
      "s_add_u32 s52, s52, s55\n"
      "s_lshl_b32 s57, %[wave], 13\n"
      "s_lshl_b32 s55, s19, 7\n"
      "s_add_u32 s57, s57, s55\n"
      "v_lshlrev_b32 v230, 7, v235\n"
      "s_lshr_b32 s23, %[g], 3\n"
      "s_lshl_b32 s23, s23, 10\n"
      "s_sub_u32 s49, s18, 13108224\n"
      "s_ashr_i32 s50, s49, 31\n"
      "s_mov_b32 s42, 104884224\n"
      "s_mov_b32 s43, 0x20000\n"
      "v_and_b32 v241, 15, v240\n"
      "v_lshrrev_b32 v242, 4, v240\n"
      "v_lshrrev_b32 v243, 2, v241\n"
      "v_and_b32 v244, 3, v240\n"
      "s_mul_i32 s53, %[wave], 24576\n"
      "s_add_u32 s53, s53, %[ldsb]\n"
      "s_add_u32 s53, s53, 65536\n"
      "v_and_b32 v248, 2, v234\n"
      "v_xor_b32 v248, v248, v244\n"
      "v_lshlrev_b32 v248, 4, v248\n"
      "v_lshrrev_b32 v249, 5, v240\n"
      "v_lshl_add_u32 v249, v249, 6, v248\n"
      "v_bfe_u32 v248, v240, 2, 3\n"
      "v_lshl_add_u32 v1, v248, 8, v249\n"
      "v_and_b32 v248, 1, v242\n"
      "v_mul_u32_u24 v247, 13106176, v248\n"
      "v_add_u32 v247, v247, v1\n"
      "s_mov_b32 s73, 13128704\n"
      "s_mov_b32 s74, 13127808\n"
      "s_mov_b32 s75, 13124608\n"
      "s_mov_b32 s76, 13123712\n"
      "s_mov_b32 s77, 13120512\n"
      "s_mov_b32 s78, 13119616\n"
      "s_mov_b32 s79, 13132800\n"
      "s_mov_b32 s90, 13116416\n"
      "s_mov_b32 s91, 13115520\n"
      "s_mov_b32 s92, 13112320\n"
      "s_mov_b32 s93, 13111424\n"
      "s_mov_b32 s94, 13108224\n"
      "s_mov_b32 s95, 13107328\n"
      "s_mov_b32 s100, 13312\n"
      "s_mov_b32 s101, 9216\n"
      "s_mov_b32 s34, 5120\n"
      "s_mov_b32 s35, 1024\n"
      "s_mov_b32 s51, 0x3e38aa3b\n"
      "s_lshl_b32 s54, %[wave], 10\n"
      "s_add_u32 s54, s54, %[ldsb]\n"
      "v_lshlrev_b32 v255, 8, v242\n"
      "s_waitcnt lgkmcnt(0)\n"
      "s_mov_b32 s44, s20\n"
      "s_and_b32 s45, s21, 0xffff\n"
      "buffer_load_dword v17, v230, s[44:47], s23 offen\n"
      "s_mov_b32 s44, s8\n s_and_b32 s45, s9, 0xffff\n"
      "buffer_load_dwordx4 v[64:67], v246, s[44:47], s52 offen offset:0\n"
      "s_lshr_b32 s33, %[g], 3\n"
      "s_lshl2_add_u32 s33, s33, %[wave]\n"
      "s_bitcmp1_b32 s33, 6\n"
      "s_cselect_b64 s[24:25], s[14:15], s[4:5]\n"
      "s_and_b32 s33, s33, 63\n"
      "s_lshl_b32 s33, s33, 21\n"
      "s_min_u32 s33, s33, 104857596\n"
      "s_load_dword s32, s[24:25], s33\n"
      "s_add_u32 s40, s4, s49\n"
      "s_addc_u32 s41, s5, s50\n"
      "s_and_b32 s41, s41, 0xffff\n"
      "buffer_load_dwordx4 v[68:71], v246, s[44:47], s52 offen offset:128\n"
      "buffer_load_dwordx4 v[72:75], v246, s[44:47], s52 offen offset:256\n"
      "buffer_load_dwordx4 v[76:79], v246, s[44:47], s52 offen offset:384\n"
      "s_mov_b32 s44, s10\n s_and_b32 s45, s11, 0xffff\n"
      "buffer_load_dwordx4 v[80:83], v246, s[44:47], s52 offen offset:0\n"
      "buffer_load_dwordx4 v[84:87], v246, s[44:47], s52 offen offset:128\n"
      "buffer_load_dwordx4 v[88:91], v246, s[44:47], s52 offen offset:256\n"
      "buffer_load_dwordx4 v[92:95], v246, s[44:47], s52 offen offset:384\n"
      "s_add_u32 m0, s53, 0\n s_mov_b32 s30, 13132800\n"
      "s_nop 0\n"
      "buffer_load_dwordx4 v247, s[40:43], s30 offen offset:0 lds\n"
      "v_lshl_add_u32 v255, v235, 5, v255\n"
      "v_lshrrev_b32 v248, 2, v235\n"
      "v_lshl_add_u32 v255, v248, 4, v255\n"
      "v_and_b32 v248, 1, v234\n"
      "v_lshl_add_u32 v255, v248, 3, v255\n"
      "v_add_u32 v255, s54, v255\n"
      "v_xor_b32 v232, 16, v255\n"
      "v_mov_b32 v238, 0x05040100\n"
      "v_mov_b32 v239, 0x07060302\n"
      "v_xor_b32 v254, v248, v240\n"
      "s_mov_b32 s30, 39345152\n"
      "s_nop 0\n"
      "buffer_load_dwordx4 v247, s[40:43], s30 offen offset:2048 lds\n"
      "v_lshlrev_b32 v254, 4, v254\n"
      "v_add_u32 v254, %[ldsb], v254\n"
      "v_lshlrev_b32 v245, 3, v241\n"
      "v_lshrrev_b32 v248, 3, v241\n"
      "v_lshlrev_b32 v248, 11, v248\n"
      "v_lshl_add_u32 v248, v235, 6, v248\n"
      "v_lshrrev_b32 v249, 1, v241\n"
      "v_and_b32 v249, 2, v249\n"
      "v_xor_b32 v249, v249, v242\n"
      "v_lshl_add_u32 v248, v249, 4, v248\n"
      "s_mov_b32 s30, 13131904\n"
      "s_nop 0\n"
      "buffer_load_dwordx4 v247, s[40:43], s30 offen offset:1024 lds\n"
      "v_add_u32 v0, s53, v248\n"
      "s_lshl_b32 s54, s19, 7\n"
      "v_add_u32 v248, s54, v245\n"
      "v_lshl_add_u32 v2, v242, 10, v248\n"
      "v_lshl_add_u32 v3, v242, 8, v248\n"
      "v_and_b32 v4, 60, v240\n"
      "v_and_b32 v248, 1, v240\n"
      "v_mul_u32_u24 v5, 0xaaaa, v248\n"
      "v_add_u32 v5, 0x4444, v5\n"
      "v_lshlrev_b32 v7, 6, v243\n"
      "s_mov_b32 s30, 39344256\n"
      "s_nop 0\n"
      "buffer_load_dwordx4 v247, s[40:43], s30 offen offset:3072 lds\n"
      "v_lshl_add_u32 v7, v248, 3, v7\n"
      "v_and_b32 v249, 1, v242\n"
      "v_lshl_add_u32 v6, v249, 5, v7\n"
      "v_lshrrev_b32 v250, 1, v242\n"
      "v_lshl_add_u32 v6, v250, 2, v6\n"
      "v_lshl_add_u32 v7, v242, 4, v7\n"
      "v_mov_b32 v8, 1.0\n"
      "s_lshl_b32 s54, %[wave], 14\n"
      "s_add_u32 s54, s54, %[ldsb]\n"
      "v_lshlrev_b32 v16, 10, v242\n"
      "v_lshrrev_b32 v231, 1, v241\n"
      "v_lshl_add_u32 v16, v231, 4, v16\n"
      "v_lshl_add_u32 v16, v248, 2, v16\n"
      "v_add_u32 v16, s54, v16\n"
      "v_lshrrev_b32 v231, 3, v241\n"
      "v_lshl_add_u32 v16, v231, 20, v16\n"
      "v_lshlrev_b32 v9, 9, v242\n"
      "v_add_u32 v9, s54, v9\n"
      "v_lshl_add_u32 v251, v241, 4, v9\n"
      "v_lshl_add_u32 v9, v244, 4, v9\n"
      "s_add_u32 s54, s54, 4096\n"
      "v_lshlrev_b32 v10, 10, v242\n"
      "v_lshl_add_u32 v10, v243, 4, v10\n"
      "v_lshl_add_u32 v10, v248, 9, v10\n"
      "v_add_u32 v10, s54, v10\n"
      "v_lshlrev_b32 v252, 10, v250\n"
      "v_lshl_add_u32 v252, v241, 4, v252\n"
      "v_lshl_add_u32 v252, v249, 3, v252\n"
      "v_add_u32 v252, s54, v252\n"
      "s_add_u32 s54, s54, 4096\n"
      "v_lshlrev_b32 v11, 3, v240\n"
      "v_add_u32 v11, s54, v11\n"
      "v_lshl_add_u32 v253, v242, 9, v245\n"
      "v_add_u32 v253, s54, v253\n"
      "v_mov_b32 v12, 0x3f803f80\n"
      "v_mov_b32 v13, 0x3f803f80\n"
      "v_mov_b32 v14, 0x3f803f80\n"
      "v_mov_b32 v15, 0x3f803f80\n"
      "s_mov_b32 vcc_lo, 0x55555555\n"
      "s_mov_b32 vcc_hi, 0x55555555\n"
      "s_mov_b32 s26, 16384\n"
      "s_mov_b32 s27, 0x20000\n"
      "s_branch L_hs_%=\n"
      "L_hr_%=:\n"
      "s_setpc_b64 vcc\n"
      "L_e5_%=:\n"
      "s_getpc_b64 s[56:57]\n"
      "L_te_%=:\n"
      "s_lshl_b32 s55, %[wave], 12\n"
      "s_add_u32 s55, s55, L_ln_%=-L_te_%=\n"
      "v_mbcnt_lo_u32_b32 v17, -1, 0\n"
      "v_mbcnt_hi_u32_b32 v17, -1, v17\n"
      "v_lshlrev_b32 v17, 6, v17\n"
      "v_add_u32 v17, s55, v17\n"
      "v_min_u32 v17, L_ai_end-L_te_%=, v17\n"
      "global_load_dword v17, v17, s[56:57]\n"
      "s_branch L_task_%=\n"
      "L_hs_%=:\n"
      "s_mov_b32 s38, 0xc000000\n"
      "s_mov_b64 exec, 0\n"
      "s_cmp_eq_u32 %[wave], 0\n"
      "s_cbranch_scc0 L_hn1o0_%=\n"
      "s_call_b64 vcc, L_h0_%=\n"
      "L_hn1o0_%=:\n"
      "s_cmp_eq_u32 %[wave], 1\n"
      "s_cbranch_scc0 L_hn1o1_%=\n"
      "s_call_b64 vcc, L_h1_%=\n"
      "L_hn1o1_%=:\n"
      "s_cmp_eq_u32 %[wave], 2\n"
      "s_cbranch_scc0 L_hn1o2_%=\n"
      "s_call_b64 vcc, L_h2_%=\n"
      "L_hn1o2_%=:\n"
      "s_cmp_eq_u32 %[wave], 3\n"
      "s_cbranch_scc0 L_hn1o3_%=\n"
      "s_nop 0\n"
      "L_hn1o3_%=:\n"
      "s_mov_b64 exec, -1\n"
      "s_mov_b32 vcc_lo, 0x55555555\n"
      "s_mov_b32 vcc_hi, 0x55555555\n"
      "s_add_u32 m0, s53, 20480\n"
      "s_nop 0\n"
      "buffer_load_dwordx4 v1, s[40:43], s73 offen offset:0 lds\n"
      "s_mov_b32 s44, s12\n s_and_b32 s45, s13, 0xffff\n"
      "buffer_load_dwordx4 v1, s[40:43], s73 offen offset:2048 lds\n"
      "s_waitcnt vmcnt(12)\n"
      "v_perm_b32 v116, v68, v64, v238\n"
      "v_perm_b32 v222, v69, v65, v239\n"
      "v_perm_b32 v60, v69, v65, v238\n"
      "s_waitcnt vmcnt(10)\n"
      "v_perm_b32 v193, v79, v75, v238\n"
      "v_perm_b32 v225, v76, v72, v239\n"
      "buffer_load_dwordx4 v1, s[40:43], s74 offen offset:1024 lds\n"
      "buffer_load_dwordx4 v1, s[40:43], s74 offen offset:3072 lds\n"
      "s_mov_b64 exec, 0\n"
      "s_cmp_eq_u32 %[wave], 0\n"
      "s_cbranch_scc0 L_hn2o0_%=\n"
      "s_call_b64 vcc, L_h3_%=\n"
      "s_call_b64 vcc, L_h7_%=\n"
      "s_nop 0\n"
      "L_hn2o0_%=:\n"
      "s_cmp_eq_u32 %[wave], 1\n"
      "s_cbranch_scc0 L_hn2o1_%=\n"
      "s_call_b64 vcc, L_h4_%=\n"
      "s_nop 0\n"
      "s_nop 0\n"
      "L_hn2o1_%=:\n"
      "s_cmp_eq_u32 %[wave], 2\n"
      "s_cbranch_scc0 L_hn2o2_%=\n"
      "s_call_b64 vcc, L_h5_%=\n"
      "s_nop 0\n"
      "s_nop 0\n"
      "L_hn2o2_%=:\n"
      "s_cmp_eq_u32 %[wave], 3\n"
      "s_cbranch_scc0 L_hn2o3_%=\n"
      "s_call_b64 vcc, L_h6_%=\n"
      "s_nop 0\n"
      "s_nop 0\n"
      "L_hn2o3_%=:\n"
      "s_mov_b64 exec, -1\n"
      "s_mov_b32 vcc_lo, 0x55555555\n"
      "s_mov_b32 vcc_hi, 0x55555555\n"
      "v_perm_b32 v50, v70, v66, v239\n"
      "s_waitcnt vmcnt(10)\n"
      "v_perm_b32 v194, v84, v80, v238\n"
      "s_waitcnt vmcnt(8)\n"
      "v_perm_b32 v63, v93, v89, v239\n"
      "v_perm_b32 v195, v92, v88, v238\n"
      "v_perm_b32 v51, v78, v74, v239\n"
      "v_perm_b32 v221, v94, v90, v239\n"
      "v_perm_b32 v226, v85, v81, v238\n"
      "v_perm_b32 v98, v84, v80, v239\n"
      "v_perm_b32 v192, v71, v67, v238\n"
      "v_perm_b32 v61, v77, v73, v238\n"
      "buffer_load_dwordx4 v[228:231], v236, s[44:47], s57 offen offset:0\n"
      "v_perm_b32 v119, v94, v90, v238\n"
      "ds_write_b64 v232, v[50:51] offset:4096\n"
      "v_perm_b32 v118, v86, v82, v238\n"
      "v_perm_b32 v117, v76, v72, v238\n"
      "v_perm_b32 v101, v95, v91, v238\n"
      "ds_write_b64 v255, v[116:117] offset:0\n"
      "buffer_load_dwordx4 v[50:53], v236, s[44:47], s57 offen offset:256\n"
      "v_perm_b32 v227, v93, v89, v238\n"
      "v_perm_b32 v117, v95, v91, v239\n"
      "v_perm_b32 v62, v85, v81, v239\n"
      "v_perm_b32 v100, v87, v83, v238\n"
      "v_perm_b32 v220, v86, v82, v239\n"
      "v_perm_b32 v93, v79, v75, v239\n"
      "ds_write_b64 v232, v[100:101] offset:24576\n"
      "v_perm_b32 v116, v87, v83, v239\n"
      "ds_write_b64 v232, v[118:119] offset:16384\n"
      "v_perm_b32 v75, v78, v74, v238\n"
      "v_perm_b32 v223, v77, v73, v239\n"
      "ds_write_b64 v255, v[222:223] offset:12288\n"
      "ds_write_b64 v255, v[60:61] offset:8192\n"
      "v_perm_b32 v99, v92, v88, v239\n"
      "ds_write_b64 v255, v[98:99] offset:20480\n"
      "ds_write_b64 v232, v[220:221] offset:20480\n"
      "v_perm_b32 v74, v70, v66, v238\n"
      "ds_write_b64 v232, v[74:75] offset:0\n"
      "ds_write_b64 v255, v[226:227] offset:24576\n"
      "ds_write_b64 v255, v[194:195] offset:16384\n"
      "ds_write_b64 v255, v[62:63] offset:28672\n"
      "ds_write_b64 v232, v[116:117] offset:28672\n"
      "v_perm_b32 v224, v68, v64, v239\n"
      "ds_write_b64 v232, v[192:193] offset:8192\n"
      "ds_write_b64 v255, v[224:225] offset:4096\n"
      "v_perm_b32 v92, v71, v67, v239\n"
      "ds_write_b64 v232, v[92:93] offset:12288\n"
      "s_waitcnt lgkmcnt(0)\n"
      "s_barrier\n"
      "buffer_load_dwordx4 v[68:71], v236, s[44:47], s57 offen offset:512\n"
      "buffer_load_dwordx4 v[20:23], v236, s[44:47], s57 offen offset:768\n"
      "ds_read_b128 a[56:59], v254 offset:14336\n"
      "ds_read_b128 a[88:91], v254 offset:22528\n"
      "ds_read_b128 a[104:107], v254 offset:26624\n"
      "ds_read_b128 a[124:127], v254 offset:31744\n"
      "ds_read_b128 a[84:87], v254 offset:21504\n"
      "ds_read_b128 a[0:3], v254 offset:0\n"
      "ds_read_b128 a[48:51], v254 offset:12288\n"
      "ds_read_b128 a[108:111], v254 offset:27648\n"
      "ds_read_b128 a[76:79], v254 offset:19456\n"
      "s_mov_b32 s44, s6\n s_and_b32 s45, s7, 0xffff\n"
      "ds_read_b128 a[60:63], v254 offset:15360\n"
      "ds_read_b128 a[28:31], v254 offset:7168\n"
      "s_waitcnt vmcnt(8)\n"
      "ds_read_b128 v[72:75], v0 offset:0\n"
      "ds_read_b128 a[24:27], v254 offset:6144\n"
      "ds_read_b128 a[8:11], v254 offset:2048\n"
      "ds_read_b128 a[52:55], v254 offset:13312\n"
      "ds_read_b128 a[96:99], v254 offset:24576\n"
      "buffer_load_dwordx4 v[60:63], v246, s[44:47], s52 offen offset:0\n"
      "ds_read_b128 a[116:119], v254 offset:29696\n"
      "buffer_load_dwordx4 v[76:79], v246, s[44:47], s52 offen offset:128\n"
      "ds_read_b128 v[224:227], v0 offset:512\n"
      "ds_read_b128 a[12:15], v254 offset:3072\n"
      "ds_read_b128 a[4:7], v254 offset:1024\n"
      "ds_read_b128 a[72:75], v254 offset:18432\n"
      "ds_read_b128 a[68:71], v254 offset:17408\n"
      "ds_read_b128 v[220:223], v0 offset:1536\n"
      "ds_read_b128 a[40:43], v254 offset:10240\n"
      "s_mov_b32 s30, 65561600\n"
      "ds_read_b128 a[36:39], v254 offset:9216\n"
      "ds_read_b128 v[92:95], v0 offset:1024\n"
      "buffer_load_dwordx4 v[88:91], v246, s[44:47], s52 offen offset:256\n"
      "ds_read_b128 a[64:67], v254 offset:16384\n"
      "buffer_load_dwordx4 v[84:87], v246, s[44:47], s52 offen offset:384\n"
      "ds_read_b128 a[112:115], v254 offset:28672\n"
      "ds_read_b128 a[120:123], v254 offset:30720\n"
      "ds_read_b128 a[100:103], v254 offset:25600\n"
      "ds_read_b128 a[92:95], v254 offset:23552\n"
      "ds_read_b128 a[44:47], v254 offset:11264\n"
      "ds_read_b128 a[20:23], v254 offset:5120\n"
      "s_add_u32 m0, s53, 4096\n"
      "ds_read_b128 a[80:83], v254 offset:20480\n"
      "s_add_u32 s44, s14, s49\n s_addc_u32 s45, s15, s50\n s_and_b32 s45, s45, 0xffff\n s_mov_b32 s46, 13133824\n"
      "ds_read_b128 a[16:19], v254 offset:4096\n"
      "buffer_load_dwordx4 v247, s[40:43], s30 offen offset:0 lds\n"
      "s_waitcnt lgkmcnt(15)\n"
      "v_mfma_f32_16x16x32_bf16 v[116:119], v[72:75], a[96:99], 0\n"
      "s_waitcnt lgkmcnt(5)\n"
      "v_mfma_f32_16x16x32_bf16 v[116:119], v[224:227], a[100:103], v[116:119]\n"
      "v_mfma_f32_16x16x32_bf16 v[116:119], v[92:95], a[104:107], v[116:119]\n"
      "v_mfma_f32_16x16x32_bf16 v[116:119], v[220:223], a[108:111], v[116:119]\n"
      "ds_read_b128 a[32:35], v254 offset:8192\n"
      "v_mfma_f32_16x16x32_bf16 v[64:67], a[0:3], v[72:75], 0\n"
      "v_mfma_f32_16x16x32_bf16 v[64:67], a[4:7], v[224:227], v[64:67]\n"
      "v_mfma_f32_16x16x32_bf16 v[64:67], a[8:11], v[92:95], v[64:67]\n"
      "v_mfma_f32_16x16x32_bf16 v[64:67], a[12:15], v[220:223], v[64:67]\n"
      "s_mov_b32 s30, 91773952\n"
      "buffer_load_dwordx4 v247, s[40:43], s30 offen offset:2048 lds\n"
      "v_mfma_f32_16x16x32_bf16 v[98:101], a[48:51], v[72:75], 0\n"
      "v_mfma_f32_16x16x32_bf16 v[98:101], a[52:55], v[224:227], v[98:101]\n"
      "v_mfma_f32_16x16x32_bf16 v[98:101], a[56:59], v[92:95], v[98:101]\n"
      "v_mfma_f32_16x16x32_bf16 v[98:101], a[60:63], v[220:223], v[98:101]\n"
      "s_waitcnt lgkmcnt(2)\n"
      "v_mfma_f32_16x16x32_bf16 v[136:139], v[72:75], a[80:83], 0\n"
      "v_mfma_f32_16x16x32_bf16 v[136:139], v[224:227], a[84:87], v[136:139]\n"
      "v_mfma_f32_16x16x32_bf16 v[136:139], v[92:95], a[88:91], v[136:139]\n"
      "v_mfma_f32_16x16x32_bf16 v[136:139], v[220:223], a[92:95], v[136:139]\n"
      "v_mfma_f32_16x16x32_bf16 v[204:207], v[72:75], a[112:115], 0\n"
      "s_mov_b32 s30, 65560704\n"
      "v_mfma_f32_16x16x32_bf16 v[204:207], v[224:227], a[116:119], v[204:207]\n"
      "v_mfma_f32_16x16x32_bf16 v[204:207], v[92:95], a[120:123], v[204:207]\n"
      "v_mfma_f32_16x16x32_bf16 v[204:207], v[220:223], a[124:127], v[204:207]\n"
      "buffer_load_dwordx4 v247, s[40:43], s30 offen offset:1024 lds\n"
      "s_waitcnt lgkmcnt(1)\n"
      "v_mfma_f32_16x16x32_bf16 v[104:107], a[16:19], v[72:75], 0\n"
      "v_cvt_pk_bf16_f32 v234, v98, v99\n"
      "v_mfma_f32_16x16x32_bf16 v[104:107], a[20:23], v[224:227], v[104:107]\n"
      "v_mfma_f32_16x16x32_bf16 v[104:107], a[24:27], v[92:95], v[104:107]\n"
      "v_mfma_f32_16x16x32_bf16 v[104:107], a[28:31], v[220:223], v[104:107]\n"
      "s_nop 0\n"
      "v_cvt_pk_bf16_f32 v210, v204, v205\n"
      "s_nop 5\n"
      "v_cvt_pk_bf16_f32 v54, v104, v105\n"
      "s_waitcnt lgkmcnt(0)\n"
      "v_mfma_f32_16x16x32_bf16 v[96:99], a[32:35], v[72:75], 0\n"
      "v_mfma_f32_16x16x32_bf16 v[96:99], a[36:39], v[224:227], v[96:99]\n"
      "v_mfma_f32_16x16x32_bf16 v[96:99], a[40:43], v[92:95], v[96:99]\n"
      "v_mfma_f32_16x16x32_bf16 v[96:99], a[44:47], v[220:223], v[96:99]\n"
      "s_mov_b32 s30, 91773056\n"
      "buffer_load_dwordx4 v247, s[40:43], s30 offen offset:3072 lds\n"
      "s_waitcnt vmcnt(12)\n"
      "ds_read_b128 v[212:215], v0 offset:20480\n"
      "ds_read_b128 v[124:127], v0 offset:20992\n"
      "v_mfma_f32_16x16x32_bf16 v[28:31], v[72:75], a[64:67], 0\n"
      "ds_read_b128 v[188:191], v0 offset:21504\n"
      "ds_read_b128 v[244:247], v0 offset:22016\n"
      "v_mfma_f32_16x16x32_bf16 v[28:31], v[224:227], a[68:71], v[28:31]\n"
      "v_mfma_f32_16x16x32_bf16 v[28:31], v[92:95], a[72:75], v[28:31]\n"
      "v_mfma_f32_16x16x32_bf16 v[28:31], v[220:223], a[76:79], v[28:31]\n"
      "v_cvt_pk_bf16_f32 v233, v98, v99\n"
      "s_add_u32 m0, s53, 16384\n"
      "s_nop 0\n"
      "buffer_load_dwordx4 v1, s[40:43], s75 offen offset:0 lds\n"
      "buffer_load_dwordx4 v1, s[40:43], s75 offen offset:2048 lds\n"
      "s_waitcnt vmcnt(12)\n"
      "v_perm_b32 v44, v50, v228, v238\n"
      "v_perm_b32 v198, v53, v231, v239\n"
      "v_perm_b32 v104, v52, v230, v238\n"
      "v_perm_b32 v98, v50, v228, v239\n"
      "s_waitcnt lgkmcnt(3)\n"
      "v_mfma_f32_16x16x32_bf16 v[148:151], v[212:215], a[112:115], 0\n"
      "s_waitcnt lgkmcnt(2)\n"
      "v_mfma_f32_16x16x32_bf16 v[148:151], v[124:127], a[116:119], v[148:151]\n"
      "v_perm_b32 v204, v51, v229, v239\n"
      "s_waitcnt lgkmcnt(1)\n"
      "v_mfma_f32_16x16x32_bf16 v[148:151], v[188:191], a[120:123], v[148:151]\n"
      "s_waitcnt lgkmcnt(0)\n"
      "v_mfma_f32_16x16x32_bf16 v[148:151], v[244:247], a[124:127], v[148:151]\n"
      "v_perm_b32 v194, v51, v229, v238\n"
      "v_perm_b32 v50, v52, v230, v239\n"
      "v_mfma_f32_16x16x32_bf16 v[184:187], v[212:215], a[64:67], 0\n"
      "buffer_load_dwordx4 v1, s[40:43], s76 offen offset:1024 lds\n"
      "v_mfma_f32_16x16x32_bf16 v[184:187], v[124:127], a[68:71], v[184:187]\n"
      "v_mfma_f32_16x16x32_bf16 v[184:187], v[188:191], a[72:75], v[184:187]\n"
      "s_waitcnt vmcnt(11)\n"
      "v_perm_b32 v51, v22, v70, v239\n"
      "v_perm_b32 v99, v20, v68, v239\n"
      "v_mfma_f32_16x16x32_bf16 v[184:187], v[244:247], a[76:79], v[184:187]\n"
      "ds_write_b64 v232, v[50:51] offset:53248\n"
      "buffer_load_dwordx4 v1, s[40:43], s76 offen offset:3072 lds\n"
      "ds_write_b64 v255, v[98:99] offset:53248\n"
      "v_perm_b32 v195, v21, v69, v238\n"
      "s_waitcnt vmcnt(11)\n"
      "v_lshlrev_b32 v228, 16, v60\n"
      "v_lshlrev_b32 v166, 16, v61\n"
      "v_lshlrev_b32 v98, 16, v62\n"
      "v_lshlrev_b32 v236, 16, v63\n"
      "v_mul_f32 v166, s51, v166\n"
      "v_mul_f32 v98, s51, v98\n"
      "v_mfma_f32_16x16x32_bf16 v[48:51], a[16:19], v[212:215], 0\n"
      "v_perm_b32 v105, v22, v70, v238\n"
      "v_and_b32 v82, 0xffff0000, v63\n"
      "v_mfma_f32_16x16x32_bf16 v[48:51], a[20:23], v[124:127], v[48:51]\n"
      "v_perm_b32 v45, v20, v68, v238\n"
      "v_perm_b32 v199, v23, v71, v239\n"
      "v_mfma_f32_16x16x32_bf16 v[48:51], a[24:27], v[188:191], v[48:51]\n"
      "v_and_b32 v230, 0xffff0000, v60\n"
      "v_mul_f32 v230, s51, v230\n"
      "v_mfma_f32_16x16x32_bf16 v[48:51], a[28:31], v[244:247], v[48:51]\n"
      "ds_write_b64 v255, v[44:45] offset:49152\n"
      "ds_write_b64 v255, v[194:195] offset:57344\n"
      "ds_write_b64 v232, v[104:105] offset:49152\n"
      "v_mfma_f32_16x16x32_bf16 v[44:47], a[0:3], v[212:215], 0\n"
      "s_waitcnt vmcnt(9)\n"
      "v_lshlrev_b32 v20, 16, v89\n"
      "v_lshlrev_b32 v104, 16, v91\n"
      "v_mfma_f32_16x16x32_bf16 v[44:47], a[4:7], v[124:127], v[44:47]\n"
      "ds_write_b64 v232, v[198:199] offset:61440\n"
      "v_lshlrev_b32 v99, 16, v78\n"
      "v_lshlrev_b32 v167, 16, v77\n"
      "v_and_b32 v198, 0xffff0000, v90\n"
      "v_and_b32 v164, 0xffff0000, v89\n"
      "v_mul_f32 v198, s51, v198\n"
      "s_waitcnt vmcnt(8)\n"
      "v_lshlrev_b32 v35, 16, v86\n"
      "v_mul_f32 v35, s51, v35\n"
      "v_and_b32 v60, 0xffff0000, v62\n"
      "v_mul_f32 v99, s51, v99\n"
      "v_lshlrev_b32 v229, 16, v76\n"
      "v_mul_f32 v229, s51, v229\n"
      "v_lshlrev_b32 v105, 16, v87\n"
      "v_perm_b32 v68, v53, v231, v238\n"
      "v_and_b32 v199, 0xffff0000, v86\n"
      "v_perm_b32 v205, v21, v69, v239\n"
      "v_and_b32 v231, 0xffff0000, v76\n"
      "v_cvt_pk_bf16_f32 v53, v66, v67\n"
      "v_mul_f32 v167, s51, v167\n"
      "v_mul_f32 v199, s51, v199\n"
      "v_lshlrev_b32 v33, 16, v84\n"
      "v_cvt_pk_bf16_f32 v76, v166, v167\n"
      "v_mul_f32 v231, s51, v231\n"
      "v_and_b32 v62, 0xffff0000, v88\n"
      "v_lshlrev_b32 v34, 16, v90\n"
      "v_and_b32 v67, 0xffff0000, v77\n"
      "v_mul_f32 v34, s51, v34\n"
      "v_and_b32 v165, 0xffff0000, v85\n"
      "v_cvt_pk_bf16_f32 v239, v34, v35\n"
      "v_mul_f32 v228, s51, v228\n"
      "v_and_b32 v66, 0xffff0000, v61\n"
      "v_mul_f32 v20, s51, v20\n"
      "v_mul_f32 v67, s51, v67\n"
      "v_and_b32 v90, 0xffff0000, v91\n"
      "v_cvt_pk_bf16_f32 v89, v198, v199\n"
      "v_mul_f32 v90, s51, v90\n"
      "v_mul_f32 v105, s51, v105\n"
      "v_mul_f32 v104, s51, v104\n"
      "v_mul_f32 v82, s51, v82\n"
      "v_and_b32 v83, 0xffff0000, v79\n"
      "v_mul_f32 v62, s51, v62\n"
      "v_and_b32 v63, 0xffff0000, v84\n"
      "v_and_b32 v91, 0xffff0000, v87\n"
      "v_mul_f32 v91, s51, v91\n"
      "v_mul_f32 v66, s51, v66\n"
      "v_and_b32 v61, 0xffff0000, v78\n"
      "v_mul_f32 v236, s51, v236\n"
      "v_lshlrev_b32 v21, 16, v85\n"
      "v_lshlrev_b32 v237, 16, v79\n"
      "v_mul_f32 v237, s51, v237\n"
      "v_mul_f32 v61, s51, v61\n"
      "v_perm_b32 v69, v23, v71, v238\n"
      "v_mul_f32 v21, s51, v21\n"
      "v_mul_f32 v33, s51, v33\n"
      "v_cvt_pk_bf16_f32 v79, v90, v91\n"
      "v_cvt_pk_bf16_f32 v91, v104, v105\n"
      "v_lshlrev_b32 v32, 16, v88\n"
      "v_cvt_pk_bf16_f32 v238, v98, v99\n"
      "v_mul_f32 v83, s51, v83\n"
      "v_cvt_pk_bf16_f32 v78, v82, v83\n"
      "v_mul_f32 v32, s51, v32\n"
      "v_cvt_pk_bf16_f32 v195, v32, v33\n"
      "v_cvt_pk_bf16_f32 v77, v20, v21\n"
      "v_mul_f32 v63, s51, v63\n"
      "v_cvt_pk_bf16_f32 v90, v236, v237\n"
      "v_cvt_pk_bf16_f32 v194, v228, v229\n"
      "v_mul_f32 v165, s51, v165\n"
      "v_cvt_pk_bf16_f32 v237, v62, v63\n"
      "v_mul_f32 v164, s51, v164\n"
      "v_mul_f32 v60, s51, v60\n"
      "v_cvt_pk_bf16_f32 v88, v60, v61\n"
      "v_cvt_pk_bf16_f32 v61, v164, v165\n"
      "v_cvt_pk_bf16_f32 v60, v66, v67\n"
      "v_cvt_pk_bf16_f32 v236, v230, v231\n"
      "v_mfma_f32_16x16x32_bf16 v[228:231], v[212:215], a[96:99], 0\n"
      "s_add_u32 m0, s53, 12288\n"
      "ds_write_b64 v232, v[90:91] offset:40960\n"
      "ds_write_b64 v255, v[204:205] offset:61440\n"
      "ds_write_b64 v255, v[60:61] offset:45056\n"
      "ds_write_b64 v232, v[238:239] offset:32768\n"
      "ds_write_b64 v255, v[76:77] offset:40960\n"
      "ds_write_b64 v232, v[68:69] offset:57344\n"
      "ds_write_b64 v232, v[88:89] offset:36864\n"
      "ds_write_b64 v232, v[78:79] offset:45056\n"
      "ds_write_b64 v255, v[236:237] offset:36864\n"
      "ds_write_b64 v255, v[194:195] offset:32768\n"
      "buffer_load_dwordx4 v1, s[40:43], s77 offen offset:0 lds\n"
      "v_cvt_pk_bf16_f32 v235, v100, v101\n"
      "v_mfma_f32_16x16x32_bf16 v[236:239], a[48:51], v[212:215], 0\n"
      "v_mfma_f32_16x16x32_bf16 v[236:239], a[52:55], v[124:127], v[236:239]\n"
      "v_mfma_f32_16x16x32_bf16 v[236:239], a[56:59], v[188:191], v[236:239]\n"
      "buffer_load_dwordx4 v1, s[40:43], s77 offen offset:2048 lds\n"
      "v_mfma_f32_16x16x32_bf16 v[236:239], a[60:63], v[244:247], v[236:239]\n"
      "buffer_load_dwordx4 v1, s[40:43], s78 offen offset:1024 lds\n"
      "v_mfma_f32_16x16x32_bf16 v[76:79], v[212:215], a[80:83], 0\n"
      "v_mfma_f32_16x16x32_bf16 v[76:79], v[124:127], a[84:87], v[76:79]\n"
      "v_mfma_f32_16x16x32_bf16 v[76:79], v[188:191], a[88:91], v[76:79]\n"
      "v_mfma_f32_16x16x32_bf16 v[76:79], v[244:247], a[92:95], v[76:79]\n"
      "s_waitcnt lgkmcnt(0)\n"
      "s_barrier\n"
      "ds_read_b128 a[156:159], v254 offset:39936\n"
      "buffer_load_dwordx4 v1, s[40:43], s78 offen offset:3072 lds\n"
      "ds_read_b128 a[144:147], v254 offset:36864\n"
      "v_mfma_f32_16x16x32_bf16 v[164:167], a[32:35], v[212:215], 0\n"
      "ds_read_b128 a[148:151], v254 offset:37888\n"
      "v_mfma_f32_16x16x32_bf16 v[164:167], a[36:39], v[124:127], v[164:167]\n"
      "ds_read_b128 a[152:155], v254 offset:38912\n"
      "v_mfma_f32_16x16x32_bf16 v[164:167], a[40:43], v[188:191], v[164:167]\n"
      "v_mfma_f32_16x16x32_bf16 v[164:167], a[44:47], v[244:247], v[164:167]\n"
      "ds_read_b128 a[180:183], v254 offset:46080\n"
      "v_cvt_pk_bf16_f32 v99, v118, v119\n"
      "v_mfma_f32_16x16x32_bf16 v[228:231], v[124:127], a[100:103], v[228:231]\n"
      "ds_read_b128 a[164:167], v254 offset:41984\n"
      "v_mfma_f32_16x16x32_bf16 v[228:231], v[188:191], a[104:107], v[228:231]\n"
      "ds_read_b128 a[172:175], v254 offset:44032\n"
      "v_mfma_f32_16x16x32_bf16 v[228:231], v[244:247], a[108:111], v[228:231]\n"
      "ds_read_b128 a[176:179], v254 offset:45056\n"
      "v_cvt_pk_bf16_f32 v211, v206, v207\n"
      "v_mfma_f32_16x16x32_bf16 v[44:47], a[8:11], v[188:191], v[44:47]\n"
      "ds_read_b128 a[188:191], v254 offset:48128\n"
      "v_mfma_f32_16x16x32_bf16 v[44:47], a[12:15], v[244:247], v[44:47]\n"
      "ds_read_b128 a[160:163], v254 offset:40960\n"
      "s_waitcnt lgkmcnt(8)\n"
      "v_mfma_f32_16x16x32_bf16 v[88:91], a[144:147], v[72:75], 0\n"
      "ds_read_b128 a[184:187], v254 offset:47104\n"
      "s_waitcnt lgkmcnt(8)\n"
      "v_mfma_f32_16x16x32_bf16 v[88:91], a[148:151], v[224:227], v[88:91]\n"
      "ds_read_b128 a[192:195], v254 offset:49152\n"
      "s_waitcnt lgkmcnt(8)\n"
      "v_mfma_f32_16x16x32_bf16 v[88:91], a[152:155], v[92:95], v[88:91]\n"
      "ds_read_b128 a[204:207], v254 offset:52224\n"
      "s_add_u32 m0, s53, 8192\n"
      "v_mfma_f32_16x16x32_bf16 v[88:91], a[156:159], v[220:223], v[88:91]\n"
      "ds_read_b128 a[248:251], v254 offset:63488\n"
      "v_mfma_f32_16x16x32_bf16 v[68:71], a[144:147], v[212:215], 0\n"
      "ds_read_b128 a[168:171], v254 offset:43008\n"
      "v_cvt_pk_bf16_f32 v82, v28, v29\n"
      "v_mfma_f32_16x16x32_bf16 v[68:71], a[148:151], v[124:127], v[68:71]\n"
      "ds_read_b128 a[216:219], v254 offset:55296\n"
      "v_mfma_f32_16x16x32_bf16 v[68:71], a[152:155], v[188:191], v[68:71]\n"
      "ds_read_b128 a[240:243], v254 offset:61440\n"
      "v_mfma_f32_16x16x32_bf16 v[68:71], a[156:159], v[244:247], v[68:71]\n"
      "ds_read_b128 a[208:211], v254 offset:53248\n"
      "s_waitcnt lgkmcnt(10)\n"
      "v_mfma_f32_16x16x32_bf16 v[84:87], a[176:179], v[72:75], 0\n"
      "ds_read_b128 a[132:135], v254 offset:33792\n"
      "v_mfma_f32_16x16x32_bf16 v[84:87], a[180:183], v[224:227], v[84:87]\n"
      "s_waitcnt lgkmcnt(8)\n"
      "v_mfma_f32_16x16x32_bf16 v[84:87], a[184:187], v[92:95], v[84:87]\n"
      "L_h0_%=:\n"
      "s_cbranch_execz L_hr_%=\n"
      "v_mfma_f32_16x16x32_bf16 v[84:87], a[188:191], v[220:223], v[84:87]\n"
      "ds_read_b128 a[252:255], v254 offset:64512\n"
      "v_cvt_pk_bf16_f32 v52, v64, v65\n"
      "v_mfma_f32_16x16x32_bf16 v[64:67], a[176:179], v[212:215], 0\n"
      "buffer_load_dwordx4 v1, s[40:43], s90 offen offset:0 lds\n"
      "v_mfma_f32_16x16x32_bf16 v[64:67], a[180:183], v[124:127], v[64:67]\n"
      "v_cvt_pk_bf16_f32 v255, v90, v91\n"
      "v_mfma_f32_16x16x32_bf16 v[64:67], a[184:187], v[188:191], v[64:67]\n"
      "ds_read_b128 a[128:131], v254 offset:32768\n"
      "v_mfma_f32_16x16x32_bf16 v[64:67], a[188:191], v[244:247], v[64:67]\n"
      "ds_read_b128 a[244:247], v254 offset:62464\n"
      "v_cvt_pk_bf16_f32 v119, v230, v231\n"
      "v_mfma_f32_16x16x32_bf16 v[60:63], a[160:163], v[212:215], 0\n"
      "ds_read_b128 a[224:227], v254 offset:57344\n"
      "v_cvt_pk_bf16_f32 v230, v136, v137\n"
      "v_mfma_f32_16x16x32_bf16 v[60:63], a[164:167], v[124:127], v[60:63]\n"
      "ds_read_b128 a[140:143], v254 offset:35840\n"
      "s_waitcnt lgkmcnt(9)\n"
      "v_mfma_f32_16x16x32_bf16 v[60:63], a[168:171], v[188:191], v[60:63]\n"
      "v_mfma_f32_16x16x32_bf16 v[60:63], a[172:175], v[244:247], v[60:63]\n"
      "ds_read_b128 a[136:139], v254 offset:34816\n"
      "v_mfma_f32_16x16x32_bf16 v[204:207], a[160:163], v[72:75], 0\n"
      "ds_read_b128 a[228:231], v254 offset:58368\n"
      "v_mfma_f32_16x16x32_bf16 v[204:207], a[164:167], v[224:227], v[204:207]\n"
      "v_cvt_pk_bf16_f32 v123, v78, v79\n"
      "v_mfma_f32_16x16x32_bf16 v[204:207], a[168:171], v[92:95], v[204:207]\n"
      "buffer_load_dwordx4 v1, s[40:43], s90 offen offset:2048 lds\n"
      "v_mfma_f32_16x16x32_bf16 v[204:207], a[172:175], v[220:223], v[204:207]\n"
      "s_waitcnt lgkmcnt(9)\n"
      "v_mfma_f32_16x16x32_bf16 v[40:43], v[72:75], a[240:243], 0\n"
      "ds_read_b128 a[236:239], v254 offset:60416\n"
      "s_waitcnt lgkmcnt(5)\n"
      "v_mfma_f32_16x16x32_bf16 v[40:43], v[224:227], a[244:247], v[40:43]\n"
      "v_mfma_f32_16x16x32_bf16 v[40:43], v[92:95], a[248:251], v[40:43]\n"
      "ds_read_b128 a[200:203], v254 offset:51200\n"
      "v_cvt_pk_bf16_f32 v199, v88, v89\n"
      "v_mfma_f32_16x16x32_bf16 v[40:43], v[220:223], a[252:255], v[40:43]\n"
      "ds_read_b128 a[196:199], v254 offset:50176\n"
      "v_mfma_f32_16x16x32_bf16 v[88:91], a[128:131], v[72:75], 0\n"
      "v_mfma_f32_16x16x32_bf16 v[88:91], a[132:135], v[224:227], v[88:91]\n"
      "s_waitcnt lgkmcnt(4)\n"
      "v_mfma_f32_16x16x32_bf16 v[88:91], a[136:139], v[92:95], v[88:91]\n"
      "ds_read_b128 a[220:223], v254 offset:56320\n"
      "v_mfma_f32_16x16x32_bf16 v[88:91], a[140:143], v[220:223], v[88:91]\n"
      "buffer_load_dwordx4 v1, s[40:43], s91 offen offset:1024 lds\n"
      "v_mfma_f32_16x16x32_bf16 v[32:35], a[128:131], v[212:215], 0\n"
      "ds_read_b128 a[232:235], v254 offset:59392\n"
      "v_mfma_f32_16x16x32_bf16 v[32:35], a[132:135], v[124:127], v[32:35]\n"
      "v_mfma_f32_16x16x32_bf16 v[32:35], a[136:139], v[188:191], v[32:35]\n"
      "s_waitcnt vmcnt(11)\n"
      "ds_read_b128 v[20:23], v0 offset:4096\n"
      "v_mfma_f32_16x16x32_bf16 v[32:35], a[140:143], v[244:247], v[32:35]\n"
      "ds_read_b128 a[212:215], v254 offset:54272\n"
      "ds_read_b128 v[100:103], v0 offset:4608\n"
      "v_mfma_f32_16x16x32_bf16 v[24:27], v[72:75], a[192:195], 0\n"
      "v_cvt_pk_bf16_f32 v198, v88, v89\n"
      "s_waitcnt lgkmcnt(5)\n"
      "v_mfma_f32_16x16x32_bf16 v[24:27], v[224:227], a[196:199], v[24:27]\n"
      "ds_read_b128 v[216:219], v0 offset:5632\n"
      "ds_read_b128 v[112:115], v0 offset:5120\n"
      "v_mfma_f32_16x16x32_bf16 v[24:27], v[92:95], a[200:203], v[24:27]\n"
      "v_cvt_pk_bf16_f32 v231, v138, v139\n"
      "v_mfma_f32_16x16x32_bf16 v[24:27], v[220:223], a[204:207], v[24:27]\n"
      "v_cvt_pk_bf16_f32 v254, v90, v91\n"
      "v_mfma_f32_16x16x32_bf16 v[152:155], v[72:75], a[224:227], 0\n"
      "buffer_load_dwordx4 v1, s[40:43], s91 offen offset:3072 lds\n"
      "v_mfma_f32_16x16x32_bf16 v[152:155], v[224:227], a[228:231], v[152:155]\n"
      "s_waitcnt lgkmcnt(5)\n"
      "v_mfma_f32_16x16x32_bf16 v[152:155], v[92:95], a[232:235], v[152:155]\n"
      "v_mfma_f32_16x16x32_bf16 v[152:155], v[220:223], a[236:239], v[152:155]\n"
      "v_mfma_f32_16x16x32_bf16 v[160:163], v[72:75], a[208:211], 0\n"
      "s_waitcnt lgkmcnt(3)\n"
      "v_mfma_f32_16x16x32_bf16 v[160:163], v[224:227], a[212:215], v[160:163]\n"
      "v_cvt_pk_bf16_f32 v140, v164, v165\n"
      "v_mfma_f32_16x16x32_bf16 v[160:163], v[92:95], a[216:219], v[160:163]\n"
      "v_cvt_pk_bf16_f32 v83, v30, v31\n"
      "v_mfma_f32_16x16x32_bf16 v[160:163], v[220:223], a[220:223], v[160:163]\n"
      "v_mfma_f32_16x16x32_bf16 v[194:197], a[176:179], v[20:23], 0\n"
      "s_waitcnt lgkmcnt(2)\n"
      "v_mfma_f32_16x16x32_bf16 v[194:197], a[180:183], v[100:103], v[194:197]\n"
      "s_waitcnt lgkmcnt(0)\n"
      "v_mfma_f32_16x16x32_bf16 v[194:197], a[184:187], v[112:115], v[194:197]\n"
      "v_cvt_pk_bf16_f32 v19, v154, v42\n"
      "v_cvt_pk_bf16_f32 v57, v155, v43\n"
      "v_mfma_f32_16x16x32_bf16 v[194:197], a[188:191], v[216:219], v[194:197]\n"
      "s_barrier\n"
      "v_mfma_f32_16x16x32_bf16 v[78:81], v[20:23], a[192:195], 0\n"
      "v_mfma_f32_16x16x32_bf16 v[78:81], v[100:103], a[196:199], v[78:81]\n"
      "v_mfma_f32_16x16x32_bf16 v[78:81], v[112:115], a[200:203], v[78:81]\n"
      "v_mfma_f32_16x16x32_bf16 v[78:81], v[216:219], a[204:207], v[78:81]\n"
      "ds_write_b64 v253, v[210:211] offset:384\n"
      "v_mfma_f32_16x16x32_bf16 v[28:31], v[20:23], a[80:83], 0\n"
      "v_mfma_f32_16x16x32_bf16 v[28:31], v[100:103], a[84:87], v[28:31]\n"
      "v_cvt_pk_bf16_f32 v55, v106, v107\n"
      "v_mfma_f32_16x16x32_bf16 v[28:31], v[112:115], a[88:91], v[28:31]\n"
      "v_cvt_pk_bf16_f32 v232, v96, v97\n"
      "v_mfma_f32_16x16x32_bf16 v[28:31], v[216:219], a[92:95], v[28:31]\n"
      "ds_write_b64 v252, v[254:255] offset:512\n"
      "v_mfma_f32_16x16x32_bf16 v[104:107], a[160:163], v[20:23], 0\n"
      "v_mfma_f32_16x16x32_bf16 v[104:107], a[164:167], v[100:103], v[104:107]\n"
      "v_mfma_f32_16x16x32_bf16 v[104:107], a[168:171], v[112:115], v[104:107]\n"
      "v_mfma_f32_16x16x32_bf16 v[104:107], a[172:175], v[216:219], v[104:107]\n"
      "ds_write_b64 v253, v[230:231] offset:128\n"
      "v_mfma_f32_16x16x32_bf16 v[224:227], v[20:23], a[96:99], 0\n"
      "s_add_u32 m0, s53, 4096\n"
      "v_mfma_f32_16x16x32_bf16 v[224:227], v[100:103], a[100:103], v[224:227]\n"
      "v_mfma_f32_16x16x32_bf16 v[224:227], v[112:115], a[104:107], v[224:227]\n"
      "v_cvt_pk_bf16_f32 v92, v60, v61\n"
      "v_mfma_f32_16x16x32_bf16 v[224:227], v[216:219], a[108:111], v[224:227]\n"
      "ds_write_b128 v251, v[52:55] offset:0\n"
      "buffer_load_dwordx4 v1, s[40:43], s92 offen offset:0 lds\n"
      "v_cvt_pk_bf16_f32 v89, v34, v35\n"
      "v_mfma_f32_16x16x32_bf16 v[154:157], v[20:23], a[112:115], 0\n"
      "v_mfma_f32_16x16x32_bf16 v[154:157], v[100:103], a[116:119], v[154:157]\n"
      "v_mfma_f32_16x16x32_bf16 v[154:157], v[112:115], a[120:123], v[154:157]\n"
      "v_mfma_f32_16x16x32_bf16 v[154:157], v[216:219], a[124:127], v[154:157]\n"
      "s_nop 0\n"
      "v_cvt_pk_bf16_f32 v254, v224, v225\n"
      "v_cvt_pk_bf16_f32 v54, v184, v185\n"
      "v_mfma_f32_16x16x32_bf16 v[200:203], v[20:23], a[64:67], 0\n"
      "buffer_load_dwordx4 v1, s[40:43], s92 offen offset:2048 lds\n"
      "v_mfma_f32_16x16x32_bf16 v[200:203], v[100:103], a[68:71], v[200:203]\n"
      "v_cvt_pk_bf16_f32 v164, v24, v160\n"
      "v_mfma_f32_16x16x32_bf16 v[200:203], v[112:115], a[72:75], v[200:203]\n"
      "v_cvt_pk_bf16_f32 v56, v27, v163\n"
      "v_cvt_pk_bf16_f32 v230, v148, v149\n"
      "v_mfma_f32_16x16x32_bf16 v[200:203], v[216:219], a[76:79], v[200:203]\n"
      "v_cvt_pk_bf16_f32 v184, v204, v205\n"
      "v_cvt_pk_bf16_f32 v165, v152, v40\n"
      "v_mfma_f32_16x16x32_bf16 v[208:211], a[0:3], v[20:23], 0\n"
      "s_waitcnt vmcnt(10)\n"
      "ds_read_b128 v[172:175], v0 offset:16384\n"
      "ds_read_b128 v[128:131], v0 offset:17408\n"
      "v_mfma_f32_16x16x32_bf16 v[208:211], a[4:7], v[100:103], v[208:211]\n"
      "v_mfma_f32_16x16x32_bf16 v[208:211], a[8:11], v[112:115], v[208:211]\n"
      "buffer_load_dwordx4 v1, s[40:43], s93 offen offset:1024 lds\n"
      "v_mfma_f32_16x16x32_bf16 v[208:211], a[12:15], v[216:219], v[208:211]\n"
      "v_mfma_f32_16x16x32_bf16 v[180:183], v[20:23], a[240:243], 0\n"
      "v_cvt_pk_bf16_f32 v138, v48, v49\n"
      "v_mfma_f32_16x16x32_bf16 v[180:183], v[100:103], a[244:247], v[180:183]\n"
      "v_cvt_pk_bf16_f32 v231, v150, v151\n"
      "v_cvt_pk_bf16_f32 v27, v156, v157\n"
      "v_mfma_f32_16x16x32_bf16 v[180:183], v[112:115], a[248:251], v[180:183]\n"
      "v_mfma_f32_16x16x32_bf16 v[180:183], v[216:219], a[252:255], v[180:183]\n"
      "v_cvt_pk_bf16_f32 v18, v26, v162\n"
      "v_cvt_pk_bf16_f32 v136, v44, v45\n"
      "v_mfma_f32_16x16x32_bf16 v[176:179], a[16:19], v[20:23], 0\n"
      "ds_read_b128 v[36:39], v0 offset:17920\n"
      "ds_read_b128 v[72:75], v0 offset:16896\n"
      "v_mfma_f32_16x16x32_bf16 v[176:179], a[20:23], v[100:103], v[176:179]\n"
      "v_mfma_f32_16x16x32_bf16 v[176:179], a[24:27], v[112:115], v[176:179]\n"
      "v_cvt_pk_bf16_f32 v98, v116, v117\n"
      "v_mfma_f32_16x16x32_bf16 v[176:179], a[28:31], v[216:219], v[176:179]\n"
      "ds_write_b64 v253, v[164:165] offset:4096\n"
      "v_mfma_f32_16x16x32_bf16 v[58:61], a[128:131], v[20:23], 0\n"
      "v_mfma_f32_16x16x32_bf16 v[58:61], a[132:135], v[100:103], v[58:61]\n"
      "v_cvt_pk_bf16_f32 v26, v154, v155\n"
      "v_mfma_f32_16x16x32_bf16 v[58:61], a[136:139], v[112:115], v[58:61]\n"
      "v_mfma_f32_16x16x32_bf16 v[58:61], a[140:143], v[216:219], v[58:61]\n"
      "ds_write_b64 v253, v[82:83] offset:0\n"
      "v_cvt_pk_bf16_f32 v91, v70, v71\n"
      "v_mfma_f32_16x16x32_bf16 v[154:157], a[48:51], v[20:23], 0\n"
      "v_mfma_f32_16x16x32_bf16 v[154:157], a[52:55], v[100:103], v[154:157]\n"
      "v_mfma_f32_16x16x32_bf16 v[154:157], a[56:59], v[112:115], v[154:157]\n"
      "buffer_load_dwordx4 v1, s[40:43], s93 offen offset:3072 lds\n"
      "v_mfma_f32_16x16x32_bf16 v[154:157], a[60:63], v[216:219], v[154:157]\n"
      "ds_write_b64 v253, v[26:27] offset:2432\n"
      "v_mfma_f32_16x16x32_bf16 v[162:165], v[20:23], a[224:227], 0\n"
      "v_cvt_pk_bf16_f32 v185, v84, v85\n"
      "s_add_u32 m0, s53, 0\n"
      "v_mfma_f32_16x16x32_bf16 v[162:165], v[100:103], a[228:231], v[162:165]\n"
      "v_cvt_pk_bf16_f32 v88, v32, v33\n"
      "v_mfma_f32_16x16x32_bf16 v[162:165], v[112:115], a[232:235], v[162:165]\n"
      "v_mfma_f32_16x16x32_bf16 v[162:165], v[216:219], a[236:239], v[162:165]\n"
      "ds_write_b64 v252, v[198:199] offset:0\n"
      "v_cvt_pk_bf16_f32 v26, v154, v155\n"
      "v_mfma_f32_16x16x32_bf16 v[82:85], a[32:35], v[20:23], 0\n"
      "buffer_load_dwordx4 v1, s[40:43], s94 offen offset:0 lds\n"
      "v_mfma_f32_16x16x32_bf16 v[82:85], a[36:39], v[100:103], v[82:85]\n"
      "v_mfma_f32_16x16x32_bf16 v[82:85], a[40:43], v[112:115], v[82:85]\n"
      "buffer_load_dwordx4 v1, s[40:43], s94 offen offset:2048 lds\n"
      "v_mfma_f32_16x16x32_bf16 v[82:85], a[44:47], v[216:219], v[82:85]\n"
      "v_cvt_pk_bf16_f32 v221, v210, v211\n"
      "v_cvt_pk_bf16_f32 v160, v206, v207\n"
      "v_mfma_f32_16x16x32_bf16 v[240:243], v[20:23], a[208:211], 0\n"
      "v_cvt_pk_bf16_f32 v137, v46, v47\n"
      "v_cvt_pk_bf16_f32 v255, v226, v227\n"
      "v_mfma_f32_16x16x32_bf16 v[240:243], v[100:103], a[212:215], v[240:243]\n"
      "v_cvt_pk_bf16_f32 v71, v165, v183\n"
      "v_cvt_pk_bf16_f32 v27, v156, v157\n"
      "v_mfma_f32_16x16x32_bf16 v[240:243], v[112:115], a[216:219], v[240:243]\n"
      "v_mfma_f32_16x16x32_bf16 v[240:243], v[216:219], a[220:223], v[240:243]\n"
      "ds_write_b64 v253, v[98:99] offset:256\n"
      "s_waitcnt lgkmcnt(8)\n"
      "v_mfma_f32_16x16x32_bf16 v[154:157], a[16:19], v[172:175], 0\n"
      "s_waitcnt lgkmcnt(5)\n"
      "v_mfma_f32_16x16x32_bf16 v[154:157], a[20:23], v[72:75], v[154:157]\n"
      "v_cvt_pk_bf16_f32 v110, v200, v201\n"
      "v_mfma_f32_16x16x32_bf16 v[154:157], a[24:27], v[128:131], v[154:157]\n"
      "v_mfma_f32_16x16x32_bf16 v[154:157], a[28:31], v[36:39], v[154:157]\n"
      "v_mfma_f32_16x16x32_bf16 v[42:45], a[144:147], v[20:23], 0\n"
      "buffer_load_dwordx4 v1, s[40:43], s95 offen offset:1024 lds\n"
      "v_mfma_f32_16x16x32_bf16 v[42:45], a[148:151], v[100:103], v[42:45]\n"
      "v_cvt_pk_bf16_f32 v222, v176, v177\n"
      "v_mfma_f32_16x16x32_bf16 v[42:45], a[152:155], v[112:115], v[42:45]\n"
      "v_cvt_pk_bf16_f32 v142, v236, v237\n"
      "v_cvt_pk_bf16_f32 v111, v202, v203\n"
      "v_mfma_f32_16x16x32_bf16 v[42:45], a[156:159], v[216:219], v[42:45]\n"
      "v_cvt_pk_bf16_f32 v183, v194, v195\n"
      "v_cvt_pk_bf16_f32 v95, v66, v67\n"
      "v_mfma_f32_16x16x32_bf16 v[200:203], a[144:147], v[172:175], 0\n"
      "v_cvt_pk_bf16_f32 v205, v164, v182\n"
      "v_cvt_pk_bf16_f32 v113, v153, v41\n"
      "v_mfma_f32_16x16x32_bf16 v[200:203], a[148:151], v[72:75], v[200:203]\n"
      "v_mfma_f32_16x16x32_bf16 v[200:203], a[152:155], v[128:131], v[200:203]\n"
      "v_mfma_f32_16x16x32_bf16 v[200:203], a[156:159], v[36:39], v[200:203]\n"
      "v_cvt_pk_bf16_f32 v139, v50, v51\n"
      "v_cvt_pk_bf16_f32 v93, v62, v63\n"
      "v_mfma_f32_16x16x32_bf16 v[20:23], v[172:175], a[112:115], 0\n"
      "v_cvt_pk_bf16_f32 v143, v238, v239\n"
      "v_cvt_pk_bf16_f32 v164, v28, v29\n"
      "v_mfma_f32_16x16x32_bf16 v[20:23], v[72:75], a[116:119], v[20:23]\n"
      "v_mfma_f32_16x16x32_bf16 v[20:23], v[128:131], a[120:123], v[20:23]\n"
      "v_cvt_pk_bf16_f32 v112, v25, v161\n"
      "v_mfma_f32_16x16x32_bf16 v[20:23], v[36:39], a[124:127], v[20:23]\n"
      "ds_write_b64 v253, v[112:113] offset:4224\n"
      "v_cvt_pk_bf16_f32 v66, v154, v155\n"
      "v_mfma_f32_16x16x32_bf16 v[236:239], a[48:51], v[172:175], 0\n"
      "s_waitcnt vmcnt(11)\n"
      "ds_read_b128 v[48:51], v0 offset:12288\n"
      "ds_read_b128 v[224:227], v0 offset:13824\n"
      "v_mfma_f32_16x16x32_bf16 v[236:239], a[52:55], v[72:75], v[236:239]\n"
      "buffer_load_dwordx4 v1, s[40:43], s95 offen offset:3072 lds\n"
      "v_mfma_f32_16x16x32_bf16 v[236:239], a[56:59], v[128:131], v[236:239]\n"
      "v_cvt_pk_bf16_f32 v55, v186, v187\n"
      "v_cvt_pk_bf16_f32 v161, v86, v87\n"
      "v_mfma_f32_16x16x32_bf16 v[236:239], a[60:63], v[36:39], v[236:239]\n"
      "s_add_u32 m0, s53, 20480\n"
      "ds_write_b128 v251, v[232:235] offset:2048\n"
      "s_add_u32 s40, s40, 13107200\n s_addc_u32 s41, s41, 0\n"
      "ds_read_b128 v[216:219], v0 offset:12800\n"
      "v_mfma_f32_16x16x32_bf16 v[112:115], a[160:163], v[172:175], 0\n"
      "v_mfma_f32_16x16x32_bf16 v[112:115], a[164:167], v[72:75], v[112:115]\n"
      "v_mfma_f32_16x16x32_bf16 v[112:115], a[168:171], v[128:131], v[112:115]\n"
      "buffer_load_dwordx4 v1, s[40:43], s73 offen offset:0 lds\n"
      "v_mfma_f32_16x16x32_bf16 v[112:115], a[172:175], v[36:39], v[112:115]\n"
      "v_cvt_pk_bf16_f32 v70, v81, v243\n"
      "v_cvt_pk_bf16_f32 v148, v79, v241\n"
      "v_mfma_f32_16x16x32_bf16 v[232:235], v[172:175], a[96:99], 0\n"
      "v_mfma_f32_16x16x32_bf16 v[232:235], v[72:75], a[100:103], v[232:235]\n"
      "v_cvt_pk_bf16_f32 v149, v163, v181\n"
      "v_cvt_pk_bf16_f32 v90, v68, v69\n"
      "v_mfma_f32_16x16x32_bf16 v[232:235], v[128:131], a[104:107], v[232:235]\n"
      "v_cvt_pk_bf16_f32 v103, v202, v203\n"
      "v_mfma_f32_16x16x32_bf16 v[232:235], v[36:39], a[108:111], v[232:235]\n"
      "ds_write_b64 v253, v[70:71] offset:6528\n"
      "v_mfma_f32_16x16x32_bf16 v[96:99], a[32:35], v[172:175], 0\n"
      "v_cvt_pk_bf16_f32 v203, v44, v45\n"
      "v_cvt_pk_bf16_f32 v67, v156, v157\n"
      "v_mfma_f32_16x16x32_bf16 v[96:99], a[36:39], v[72:75], v[96:99]\n"
      "buffer_load_dwordx4 v1, s[40:43], s74 offen offset:1024 lds\n"
      "v_mfma_f32_16x16x32_bf16 v[96:99], a[40:43], v[128:131], v[96:99]\n"
      "v_mfma_f32_16x16x32_bf16 v[96:99], a[44:47], v[36:39], v[96:99]\n"
      "v_cvt_pk_bf16_f32 v71, v238, v239\n"
      "v_cvt_pk_bf16_f32 v25, v84, v85\n"
      "v_mfma_f32_16x16x32_bf16 v[156:159], v[172:175], a[64:67], 0\n"
      "ds_read_b128 v[132:135], v0 offset:13312\n"
      "v_cvt_pk_bf16_f32 v116, v232, v233\n"
      "v_mfma_f32_16x16x32_bf16 v[156:159], v[72:75], a[68:71], v[156:159]\n"
      "v_cvt_pk_bf16_f32 v28, v106, v107\n"
      "v_cvt_pk_bf16_f32 v94, v64, v65\n"
      "v_mfma_f32_16x16x32_bf16 v[156:159], v[128:131], a[72:75], v[156:159]\n"
      "v_mfma_f32_16x16x32_bf16 v[156:159], v[36:39], a[76:79], v[156:159]\n"
      "ds_write_b64 v253, v[18:19] offset:4352\n"
      "v_mfma_f32_16x16x32_bf16 v[62:65], a[128:131], v[172:175], 0\n"
      "v_cvt_pk_bf16_f32 v141, v166, v167\n"
      "v_cvt_pk_bf16_f32 v238, v78, v240\n"
      "v_mfma_f32_16x16x32_bf16 v[62:65], a[132:135], v[72:75], v[62:65]\n"
      "v_cvt_pk_bf16_f32 v78, v58, v59\n"
      "v_cvt_pk_bf16_f32 v165, v30, v31\n"
      "v_mfma_f32_16x16x32_bf16 v[62:65], a[136:139], v[128:131], v[62:65]\n"
      "v_mfma_f32_16x16x32_bf16 v[62:65], a[140:143], v[36:39], v[62:65]\n"
      "v_cvt_pk_bf16_f32 v53, v158, v159\n"
      "v_cvt_pk_bf16_f32 v118, v228, v229\n"
      "v_mfma_f32_16x16x32_bf16 v[152:155], v[136:139], v[88:91], 0\n"
      "v_mfma_f32_16x16x32_bf16 v[152:155], v[140:143], v[92:95], v[152:155]\n"
      "ds_write_b64 v253, v[110:111] offset:2048\n"
      "s_waitcnt lgkmcnt(7)\n"
      "v_mfma_f32_16x16x32_bf16 v[84:87], v[48:51], a[112:115], 0\n"
      "v_cvt_pk_bf16_f32 v182, v104, v105\n"
      "s_waitcnt lgkmcnt(4)\n"
      "v_mfma_f32_16x16x32_bf16 v[84:87], v[216:219], a[116:119], v[84:87]\n"
      "v_cvt_pk_bf16_f32 v101, v64, v65\n"
      "s_waitcnt lgkmcnt(2)\n"
      "v_mfma_f32_16x16x32_bf16 v[84:87], v[132:135], a[120:123], v[84:87]\n"
      "v_cvt_pk_bf16_f32 v100, v62, v63\n"
      "v_mfma_f32_16x16x32_bf16 v[84:87], v[224:227], a[124:127], v[84:87]\n"
      "ds_write_b64 v252, v[182:183] offset:2304\n"
      "v_cvt_pk_bf16_f32 v69, v98, v99\n"
      "v_mfma_f32_16x16x32_bf16 v[44:47], a[32:35], v[48:51], 0\n"
      "v_mfma_f32_16x16x32_bf16 v[44:47], a[36:39], v[216:219], v[44:47]\n"
      "v_cvt_pk_bf16_f32 v228, v20, v21\n"
      "v_mfma_f32_16x16x32_bf16 v[44:47], a[40:43], v[132:135], v[44:47]\n"
      "v_cvt_pk_bf16_f32 v52, v156, v157\n"
      "v_mfma_f32_16x16x32_bf16 v[44:47], a[44:47], v[224:227], v[44:47]\n"
      "ds_write_b64 v252, v[160:161] offset:2560\n"
      "v_mfma_f32_16x16x32_bf16 v[18:21], v[48:51], a[96:99], 0\n"
      "v_cvt_pk_bf16_f32 v117, v234, v235\n"
      "v_mfma_f32_16x16x32_bf16 v[18:21], v[216:219], a[100:103], v[18:21]\n"
      "v_cvt_pk_bf16_f32 v202, v60, v61\n"
      "v_cvt_pk_bf16_f32 v239, v162, v180\n"
      "v_mfma_f32_16x16x32_bf16 v[18:21], v[132:135], a[104:107], v[18:21]\n"
      "buffer_load_dwordx4 v1, s[40:43], s74 offen offset:3072 lds\n"
      "v_mfma_f32_16x16x32_bf16 v[18:21], v[224:227], a[108:111], v[18:21]\n"
      "ds_write_b64 v253, v[148:149] offset:6272\n"
      "v_cvt_pk_bf16_f32 v104, v112, v113\n"
      "v_mfma_f32_16x16x32_bf16 v[160:163], a[0:3], v[172:175], 0\n"
      "v_mfma_f32_16x16x32_bf16 v[160:163], a[4:7], v[72:75], v[160:163]\n"
      "v_mfma_f32_16x16x32_bf16 v[160:163], a[8:11], v[128:131], v[160:163]\n"
      "v_cvt_pk_bf16_f32 v102, v200, v201\n"
      "v_mfma_f32_16x16x32_bf16 v[160:163], a[12:15], v[36:39], v[160:163]\n"
      "ds_write_b64 v253, v[56:57] offset:4480\n"
      "v_mfma_f32_16x16x32_bf16 v[156:159], v[172:175], a[80:83], 0\n"
      "v_mfma_f32_16x16x32_bf16 v[156:159], v[72:75], a[84:87], v[156:159]\n"
      "v_cvt_pk_bf16_f32 v81, v46, v47\n"
      "v_mfma_f32_16x16x32_bf16 v[156:159], v[128:131], a[88:91], v[156:159]\n"
      "v_mfma_f32_16x16x32_bf16 v[156:159], v[36:39], a[92:95], v[156:159]\n"
      "ds_write_b64 v252, v[184:185] offset:2048\n"
      "ds_read_b128 v[232:235], v9 offset:0\n"
      "v_cvt_pk_bf16_f32 v70, v236, v237\n"
      "v_mfma_f32_16x16x32_bf16 v[60:63], a[176:179], v[172:175], 0\n"
      "v_exp_f32 v155, v155\n"
      "v_mfma_f32_16x16x32_bf16 v[60:63], a[180:183], v[72:75], v[60:63]\n"
      "v_mfma_f32_16x16x32_bf16 v[60:63], a[184:187], v[128:131], v[60:63]\n"
      "v_cvt_pk_bf16_f32 v68, v96, v97\n"
      "v_mfma_f32_16x16x32_bf16 v[60:63], a[188:191], v[36:39], v[60:63]\n"
      "ds_write_b64 v252, v[202:203] offset:768\n"
      "v_mfma_f32_16x16x32_bf16 v[56:59], v[48:51], a[64:67], 0\n"
      "s_waitcnt vmcnt(11)\n"
      "ds_read_b128 v[192:195], v0 offset:8192\n"
      "ds_read_b128 v[184:187], v0 offset:9216\n"
      "v_mfma_f32_16x16x32_bf16 v[56:59], v[216:219], a[68:71], v[56:59]\n"
      "v_cvt_pk_bf16_f32 v204, v80, v242\n"
      "v_cvt_pk_bf16_f32 v120, v156, v157\n"
      "v_mfma_f32_16x16x32_bf16 v[56:59], v[132:135], a[72:75], v[56:59]\n"
      "v_mfma_f32_16x16x32_bf16 v[56:59], v[224:227], a[76:79], v[56:59]\n"
      "ds_read_b128 v[240:243], v0 offset:9728\n"
      "ds_read_b128 v[200:203], v0 offset:8704\n"
      "v_mfma_f32_16x16x32_bf16 v[110:113], a[48:51], v[48:51], 0\n"
      "v_cvt_pk_bf16_f32 v65, v162, v163\n"
      "v_cvt_pk_bf16_f32 v29, v196, v197\n"
      "v_mfma_f32_16x16x32_bf16 v[110:113], a[52:55], v[216:219], v[110:113]\n"
      "v_cvt_pk_bf16_f32 v106, v60, v61\n"
      "v_cvt_pk_bf16_f32 v107, v62, v63\n"
      "v_mfma_f32_16x16x32_bf16 v[110:113], a[56:59], v[132:135], v[110:113]\n"
      "v_cvt_pk_bf16_f32 v64, v160, v161\n"
      "v_mfma_f32_16x16x32_bf16 v[110:113], a[60:63], v[224:227], v[110:113]\n"
      "ds_write_b64 v252, v[28:29] offset:2816\n"
      "v_cvt_pk_bf16_f32 v220, v208, v209\n"
      "v_mfma_f32_16x16x32_bf16 v[180:183], a[16:19], v[48:51], 0\n"
      "v_mfma_f32_16x16x32_bf16 v[180:183], a[20:23], v[216:219], v[180:183]\n"
      "v_mfma_f32_16x16x32_bf16 v[180:183], a[24:27], v[132:135], v[180:183]\n"
      "v_mfma_f32_16x16x32_bf16 v[180:183], a[28:31], v[224:227], v[180:183]\n"
      "ds_read_b128 v[32:35], v10 offset:0\n"
      "v_cvt_pk_bf16_f32 v24, v82, v83\n"
      "v_mfma_f32_16x16x32_bf16 v[160:163], v[64:67], v[88:91], 0\n"
      "v_exp_f32 v152, v152\n"
      "v_mfma_f32_16x16x32_bf16 v[160:163], v[68:71], v[92:95], v[160:163]\n"
      "v_exp_f32 v154, v154\n"
      "v_mfma_f32_16x16x32_bf16 v[96:99], a[0:3], v[48:51], 0\n"
      "v_cvt_pk_bf16_f32 v63, v20, v21\n"
      "v_mov_b64 v[144:145], 0\n"
      "v_mfma_f32_16x16x32_bf16 v[96:99], a[4:7], v[216:219], v[96:99]\n"
      "v_exp_f32 v153, v153\n"
      "v_mfma_f32_16x16x32_bf16 v[96:99], a[8:11], v[132:135], v[96:99]\n"
      "buffer_load_dwordx4 v1, s[40:43], s73 offen offset:2048 lds\n"
      "v_mfma_f32_16x16x32_bf16 v[96:99], a[12:15], v[224:227], v[96:99]\n"
      "ds_write_b64 v253, v[164:165] offset:2176\n"
      "s_waitcnt lgkmcnt(6)\n"
      "v_mfma_f32_16x16x32_bf16 v[166:169], a[48:51], v[192:195], 0\n"
      "s_waitcnt lgkmcnt(3)\n"
      "v_mfma_f32_16x16x32_bf16 v[166:169], a[52:55], v[200:203], v[166:169]\n"
      "v_cvt_pk_bf16_f32 v122, v76, v77\n"
      "v_mfma_f32_16x16x32_bf16 v[166:169], a[56:59], v[184:187], v[166:169]\n"
      "s_nop 0\n"
      "v_cvt_pk_bf16_f32 v77, v98, v99\n"
      "v_cvt_pk_bf16_f32 v223, v178, v179\n"
      "v_mfma_f32_16x16x32_bf16 v[166:169], a[60:63], v[240:243], v[166:169]\n"
      "s_add_u32 m0, s53, 16384\n"
      "ds_write_b64 v253, v[238:239] offset:6144\n"
      "v_cvt_pk_bf16_f32 v79, v42, v43\n"
      "v_mfma_f32_16x16x32_bf16 v[28:31], a[0:3], v[192:195], 0\n"
      "ds_read_b128 v[236:239], v9 offset:2048\n"
      "v_cvt_pk_bf16_f32 v121, v158, v159\n"
      "v_mfma_f32_16x16x32_bf16 v[28:31], a[4:7], v[200:203], v[28:31]\n"
      "v_mfma_f32_16x16x32_bf16 v[28:31], a[8:11], v[184:187], v[28:31]\n"
      "buffer_load_dwordx4 v1, s[40:43], s76 offen offset:1024 lds\n"
      "v_mfma_f32_16x16x32_bf16 v[28:31], a[12:15], v[240:243], v[28:31]\n"
      "ds_write_b64 v252, v[78:79] offset:256\n"
      "v_mfma_f32_16x16x32_bf16 v[208:211], a[32:35], v[192:195], 0\n"
      "v_cvt_pk_bf16_f32 v76, v96, v97\n"
      "v_mfma_f32_16x16x32_bf16 v[208:211], a[36:39], v[200:203], v[208:211]\n"
      "v_cvt_pk_bf16_f32 v82, v110, v111\n"
      "v_cvt_pk_bf16_f32 v80, v44, v45\n"
      "v_mfma_f32_16x16x32_bf16 v[208:211], a[40:43], v[184:187], v[208:211]\n"
      "v_mfma_f32_16x16x32_bf16 v[208:211], a[44:47], v[240:243], v[208:211]\n"
      "ds_write_b128 v251, v[24:27] offset:2304\n"
      "v_cvt_pk_bf16_f32 v20, v28, v29\n"
      "v_exp_f32 v160, v160\n"
      "v_mfma_f32_16x16x32_bf16 v[156:159], v[192:195], a[96:99], 0\n"
      "v_exp_f32 v163, v163\n"
      "v_mfma_f32_16x16x32_bf16 v[156:159], v[200:203], a[100:103], v[156:159]\n"
      "v_mfma_f32_16x16x32_bf16 v[156:159], v[184:187], a[104:107], v[156:159]\n"
      "v_exp_f32 v161, v161\n"
      "v_mfma_f32_16x16x32_bf16 v[156:159], v[240:243], a[108:111], v[156:159]\n"
      "v_mfma_f32_16x16x32_bf16 v[176:179], a[16:19], v[192:195], 0\n"
      "v_cvt_pk_bf16_f32 v25, v210, v211\n"
      "v_mfma_f32_16x16x32_bf16 v[176:179], a[20:23], v[200:203], v[176:179]\n"
      "v_cvt_pk_bf16_f32 v26, v166, v167\n"
      "v_cvt_pk_bf16_f32 v105, v114, v115\n"
      "v_mfma_f32_16x16x32_bf16 v[176:179], a[24:27], v[184:187], v[176:179]\n"
      "v_mfma_f32_16x16x32_bf16 v[176:179], a[28:31], v[240:243], v[176:179]\n"
      "v_cvt_pk_bf16_f32 v79, v182, v183\n"
      "v_cvt_pk_bf16_f32 v21, v30, v31\n"
      "v_mfma_f32_16x16x32_bf16 v[44:47], v[192:195], a[112:115], 0\n"
      "v_cvt_pk_bf16_f32 v62, v18, v19\n"
      "v_mfma_f32_16x16x32_bf16 v[44:47], v[200:203], a[116:119], v[44:47]\n"
      "s_waitcnt vmcnt(9)\n"
      "ds_read_b128 v[40:43], v0 offset:4096\n"
      "ds_read_b128 v[96:99], v0 offset:5632\n"
      "v_mfma_f32_16x16x32_bf16 v[44:47], v[184:187], a[120:123], v[44:47]\n"
      "v_mfma_f32_16x16x32_bf16 v[44:47], v[240:243], a[124:127], v[44:47]\n"
      "ds_write_b64 v253, v[254:255] offset:2304\n"
      "s_waitcnt lgkmcnt(5)\n"
      "v_smfmac_f32_16x16x64_bf16 v[144:147], v[32:35], v[232:239], v5\n"
      "ds_read_b128 v[108:111], v0 offset:4608\n"
      "v_cvt_pk_bf16_f32 v229, v22, v23\n"
      "v_mfma_f32_16x16x32_bf16 v[196:199], v[192:195], a[80:83], 0\n"
      "ds_read_b128 v[164:167], v0 offset:5120\n"
      "v_cvt_pk_bf16_f32 v24, v208, v209\n"
      "v_mfma_f32_16x16x32_bf16 v[196:199], v[200:203], a[84:87], v[196:199]\n"
      "buffer_load_dwordx4 v1, s[40:43], s76 offen offset:3072 lds\n"
      "v_mfma_f32_16x16x32_bf16 v[196:199], v[184:187], a[88:91], v[196:199]\n"
      "v_cvt_pk_bf16_f32 v22, v176, v177\n"
      "v_mfma_f32_16x16x32_bf16 v[196:199], v[240:243], a[92:95], v[196:199]\n"
      "v_cvt_pk_bf16_f32 v237, v46, v47\n"
      "v_mfma_f32_16x16x32_bf16 v[208:211], v[192:195], a[64:67], 0\n"
      "v_exp_f32 v162, v162\n"
      "v_mfma_f32_16x16x32_bf16 v[208:211], v[200:203], a[68:71], v[208:211]\n"
      "v_cvt_pk_bf16_f32 v176, v160, v161\n"
      "v_cvt_pk_bf16_f32 v23, v178, v179\n"
      "v_mfma_f32_16x16x32_bf16 v[208:211], v[184:187], a[72:75], v[208:211]\n"
      "v_mov_b64 v[192:193], 0\n"
      "v_cvt_pk_bf16_f32 v60, v156, v157\n"
      "v_mfma_f32_16x16x32_bf16 v[208:211], v[240:243], a[76:79], v[208:211]\n"
      "ds_write_b64 v253, v[204:205] offset:6400\n"
      "v_mov_b64 v[28:29], 0\n"
      "v_mfma_f32_16x16x32_bf16 v[232:235], v[48:51], a[80:83], 0\n"
      "v_mfma_f32_16x16x32_bf16 v[232:235], v[216:219], a[84:87], v[232:235]\n"
      "v_cvt_pk_bf16_f32 v61, v158, v159\n"
      "v_cvt_pk_bf16_f32 v177, v162, v163\n"
      "v_mfma_f32_16x16x32_bf16 v[232:235], v[132:135], a[88:91], v[232:235]\n"
      "v_mov_b64 v[202:203], 0\n"
      "v_mfma_f32_16x16x32_bf16 v[232:235], v[224:227], a[92:95], v[232:235]\n"
      "v_cvt_pk_bf16_f32 v115, v58, v59\n"
      "v_add_f32 v144, v144, v145\n"
      "v_smfmac_f32_16x16x64_bf16 v[202:205], v[32:35], v[136:143], v5\n"
      "s_waitcnt lgkmcnt(5)\n"
      "v_mfma_f32_16x16x32_bf16 v[160:163], a[32:35], v[40:43], 0\n"
      "v_cvt_pk_bf16_f32 v179, v154, v155\n"
      "s_waitcnt lgkmcnt(2)\n"
      "v_mfma_f32_16x16x32_bf16 v[160:163], a[36:39], v[108:111], v[160:163]\n"
      "s_waitcnt lgkmcnt(1)\n"
      "v_mfma_f32_16x16x32_bf16 v[160:163], a[40:43], v[164:167], v[160:163]\n"
      "v_cvt_pk_bf16_f32 v27, v168, v169\n"
      "v_mfma_f32_16x16x32_bf16 v[160:163], a[44:47], v[96:99], v[160:163]\n"
      "v_mov_b32 v145, 0\n"
      "v_cvt_pk_bf16_f32 v187, v234, v235\n"
      "v_mfma_f32_16x16x32_bf16 v[252:255], v[40:43], a[112:115], 0\n"
      "v_mfma_f32_16x16x32_bf16 v[252:255], v[108:111], a[116:119], v[252:255]\n"
      "v_add_u32 v10, 64, v10\n"
      "v_mfma_f32_16x16x32_bf16 v[252:255], v[164:167], a[120:123], v[252:255]\n"
      "v_cvt_pk_bf16_f32 v78, v180, v181\n"
      "v_cvt_pk_bf16_f32 v83, v112, v113\n"
      "v_mfma_f32_16x16x32_bf16 v[252:255], v[96:99], a[124:127], v[252:255]\n"
      "v_exp_f32_dpp v145, v144 quad_perm:[0,1,2,3] row_mask:0xf bank_mask:0x1\n"
      "v_mfma_f32_16x16x32_bf16 v[154:157], v[76:79], v[88:91], 0\n"
      "v_cvt_pk_bf16_f32 v239, v86, v87\n"
      "v_mfma_f32_16x16x32_bf16 v[154:157], v[80:83], v[92:95], v[154:157]\n"
      "v_cvt_pk_bf16_f32 v114, v56, v57\n"
      "v_smfmac_f32_16x16x64_bf16 v[192:195], v[32:35], v[64:71], v5\n"
      "buffer_load_dwordx4 v1, s[40:43], s75 offen offset:0 lds\n"
      "v_mfma_f32_16x16x32_bf16 v[168:171], a[48:51], v[40:43], 0\n"
      "v_cvt_pk_bf16_f32 v234, v252, v253\n"
      "v_cvt_pk_bf16_f32 v112, v208, v209\n"
      "v_mfma_f32_16x16x32_bf16 v[168:171], a[52:55], v[108:111], v[168:171]\n"
      "s_nop 0\n"
      "v_exp_f32 v157, v157\n"
      "v_mfma_f32_16x16x32_bf16 v[168:171], a[56:59], v[164:167], v[168:171]\n"
      "v_cvt_pk_bf16_f32 v185, v198, v199\n"
      "v_cvt_pk_bf16_f32 v184, v196, v197\n"
      "v_mfma_f32_16x16x32_bf16 v[168:171], a[60:63], v[96:99], v[168:171]\n"
      "v_mov_b64 v[146:147], 0\n"
      "v_mfma_f32_16x16x32_bf16 v[56:59], v[20:23], v[88:91], 0\n"
      "v_cvt_pk_bf16_f32 v236, v44, v45\n"
      "v_mfma_f32_16x16x32_bf16 v[56:59], v[24:27], v[92:95], v[56:59]\n"
      "ds_write_b128 v251, v[220:223] offset:256\n"
      "v_cvt_pk_bf16_f32 v208, v160, v161\n"
      "v_cvt_pk_bf16_f32 v209, v162, v163\n"
      "v_mfma_f32_16x16x32_bf16 v[160:163], v[40:43], a[80:83], 0\n"
      "v_cvt_pk_bf16_f32 v113, v210, v211\n"
      "v_cvt_pk_bf16_f32 v238, v84, v85\n"
      "v_mfma_f32_16x16x32_bf16 v[160:163], v[108:111], a[84:87], v[160:163]\n"
      "v_cvt_pk_bf16_f32 v210, v168, v169\n"
      "v_cvt_pk_bf16_f32 v235, v254, v255\n"
      "v_mfma_f32_16x16x32_bf16 v[160:163], v[164:167], a[88:91], v[160:163]\n"
      "v_mfma_f32_16x16x32_bf16 v[160:163], v[96:99], a[92:95], v[160:163]\n"
      "v_exp_f32 v56, v56\n"
      "v_mfma_f32_16x16x32_bf16 v[194:197], v[40:43], a[64:67], 0\n"
      "v_exp_f32 v58, v58\n"
      "v_mfma_f32_16x16x32_bf16 v[194:197], v[108:111], a[68:71], v[194:197]\n"
      "v_exp_f32 v59, v59\n"
      "v_mfma_f32_16x16x32_bf16 v[194:197], v[164:167], a[72:75], v[194:197]\n"
      "v_mfma_f32_16x16x32_bf16 v[194:197], v[96:99], a[76:79], v[194:197]\n"
      "v_exp_f32 v154, v154\n"
      "v_mfma_f32_16x16x32_bf16 v[84:87], a[0:3], v[40:43], 0\n"
      "v_cvt_pk_bf16_f32 v186, v232, v233\n"
      "v_add_f32 v192, v192, v193\n"
      "v_mfma_f32_16x16x32_bf16 v[84:87], a[4:7], v[108:111], v[84:87]\n"
      "v_add_f32 v202, v202, v203\n"
      "v_mfma_f32_16x16x32_bf16 v[84:87], a[8:11], v[164:167], v[84:87]\n"
      "buffer_load_dwordx4 v1, s[40:43], s75 offen offset:2048 lds\n"
      "v_mfma_f32_16x16x32_bf16 v[84:87], a[12:15], v[96:99], v[84:87]\n"
      "v_cvt_pk_bf16_f32 v178, v152, v153\n"
      "v_smfmac_f32_16x16x64_bf16 v[28:31], v[32:35], v[76:83], v5\n"
      "s_waitcnt vmcnt(8)\n"
      "ds_read_b128 v[44:47], v0 offset:0\n"
      "v_mfma_f32_16x16x32_bf16 v[180:183], v[40:43], a[96:99], 0\n"
      "L_h1_%=:\n"
      "s_cbranch_execz L_hr_%=\n"
      "ds_read_b128 v[248:251], v0 offset:512\n"
      "ds_read_b128 v[252:255], v0 offset:1024\n"
      "v_mfma_f32_16x16x32_bf16 v[180:183], v[108:111], a[100:103], v[180:183]\n"
      "v_exp_f32 v57, v57\n"
      "v_mfma_f32_16x16x32_bf16 v[180:183], v[164:167], a[104:107], v[180:183]\n"
      "ds_read_b128 v[240:243], v0 offset:1536\n"
      "v_cvt_pk_bf16_f32 v211, v170, v171\n"
      "v_mfma_f32_16x16x32_bf16 v[180:183], v[96:99], a[108:111], v[180:183]\n"
      "v_exp_f32 v156, v156\n"
      "v_smfmac_f32_16x16x64_bf16 v[146:149], v[32:35], v[20:27], v5\n"
      "v_add_f32 v28, v28, v29\n"
      "v_mfma_f32_16x16x32_bf16 v[168:171], a[16:19], v[40:43], 0\n"
      "v_mfma_f32_16x16x32_bf16 v[168:171], a[20:23], v[108:111], v[168:171]\n"
      "v_exp_f32 v202, v202\n"
      "v_mfma_f32_16x16x32_bf16 v[168:171], a[24:27], v[164:167], v[168:171]\n"
      "v_exp_f32 v28, v28\n"
      "v_mfma_f32_16x16x32_bf16 v[168:171], a[28:31], v[96:99], v[168:171]\n"
      "v_cvt_pk_bf16_f32 v99, v156, v157\n"
      "s_waitcnt lgkmcnt(3)\n"
      "v_mfma_f32_16x16x32_bf16 v[108:111], v[44:47], a[112:115], 0\n"
      "s_waitcnt lgkmcnt(2)\n"
      "v_mfma_f32_16x16x32_bf16 v[108:111], v[248:251], a[116:119], v[108:111]\n"
      "v_cvt_pk_bf16_f32 v97, v58, v59\n"
      "s_waitcnt lgkmcnt(1)\n"
      "v_mfma_f32_16x16x32_bf16 v[108:111], v[252:255], a[120:123], v[108:111]\n"
      "s_waitcnt lgkmcnt(0)\n"
      "v_mfma_f32_16x16x32_bf16 v[108:111], v[240:243], a[124:127], v[108:111]\n"
      "v_cvt_pk_bf16_f32 v204, v84, v85\n"
      "v_cvt_pk_bf16_f32 v205, v86, v87\n"
      "v_mfma_f32_16x16x32_bf16 v[40:43], a[32:35], v[44:47], 0\n"
      "v_mfma_f32_16x16x32_bf16 v[40:43], a[36:39], v[248:251], v[40:43]\n"
      "v_exp_f32 v192, v192\n"
      "v_mfma_f32_16x16x32_bf16 v[40:43], a[40:43], v[252:255], v[40:43]\n"
      "v_mfma_f32_16x16x32_bf16 v[40:43], a[44:47], v[240:243], v[40:43]\n"
      "v_cvt_pk_bf16_f32 v206, v168, v169\n"
      "v_cvt_pk_bf16_f32 v207, v170, v171\n"
      "v_mfma_f32_16x16x32_bf16 v[164:167], a[48:51], v[44:47], 0\n"
      "s_add_u32 m0, s53, 12288\n"
      "v_exp_f32 v155, v155\n"
      "v_mfma_f32_16x16x32_bf16 v[164:167], a[52:55], v[248:251], v[164:167]\n"
      "s_nop 1\n"
      "v_cvt_pk_bf16_f32 v169, v42, v43\n"
      "v_mfma_f32_16x16x32_bf16 v[164:167], a[56:59], v[252:255], v[164:167]\n"
      "v_cvt_pk_bf16_f32 v98, v154, v155\n"
      "v_cvt_pk_bf16_f32 v232, v108, v109\n"
      "v_mfma_f32_16x16x32_bf16 v[164:167], a[60:63], v[240:243], v[164:167]\n"
      "v_cvt_pk_bf16_f32 v96, v56, v57\n"
      "v_cvt_pk_bf16_f32 v233, v110, v111\n"
      "v_mfma_f32_16x16x32_bf16 v[156:159], a[16:19], v[44:47], 0\n"
      "buffer_load_dwordx4 v1, s[40:43], s77 offen offset:0 lds\n"
      "v_mfma_f32_16x16x32_bf16 v[156:159], a[20:23], v[248:251], v[156:159]\n"
      "v_mov_b32_dpp v144, v145 quad_perm:[1,1,3,3] row_mask:0xf bank_mask:0xf\n"
      "v_cvt_pk_bf16_f32 v111, v196, v197\n"
      "v_mfma_f32_16x16x32_bf16 v[156:159], a[24:27], v[252:255], v[156:159]\n"
      "v_cvt_pk_bf16_f32 v59, v182, v183\n"
      "v_mfma_f32_16x16x32_bf16 v[156:159], a[28:31], v[240:243], v[156:159]\n"
      "v_cvt_pk_bf16_f32 v168, v40, v41\n"
      "v_cvt_pk_bf16_f32 v110, v194, v195\n"
      "v_mfma_f32_16x16x32_bf16 v[196:199], a[0:3], v[44:47], 0\n"
      "v_cvt_pk_bf16_f32 v58, v180, v181\n"
      "v_mfma_f32_16x16x32_bf16 v[196:199], a[4:7], v[248:251], v[196:199]\n"
      "v_mfma_f32_16x16x32_bf16 v[196:199], a[8:11], v[252:255], v[196:199]\n"
      "v_cvt_pk_bf16_f32 v183, v162, v163\n"
      "v_cvt_pk_bf16_f32 v182, v160, v161\n"
      "v_mfma_f32_16x16x32_bf16 v[196:199], a[12:15], v[240:243], v[196:199]\n"
      "v_cvt_pk_bf16_f32 v171, v166, v167\n"
      "v_cvt_pk_bf16_f32 v166, v156, v157\n"
      "v_mfma_f32_16x16x32_bf16 v[160:163], v[44:47], a[64:67], 0\n"
      "v_cvt_pk_bf16_f32 v167, v158, v159\n"
      "v_cvt_pk_bf16_f32 v170, v164, v165\n"
      "v_mfma_f32_16x16x32_bf16 v[160:163], v[248:251], a[68:71], v[160:163]\n"
      "v_mov_b64 v[156:157], 0\n"
      "v_cvt_pk_bf16_f32 v144, v145, v144\n"
      "v_mfma_f32_16x16x32_bf16 v[160:163], v[252:255], a[72:75], v[160:163]\n"
      "v_cndmask_b32_dpp v193, v202, v192, vcc quad_perm:[0,0,2,2] row_mask:0xf bank_mask:0xf\n"
      "v_cvt_pk_bf16_f32 v164, v196, v197\n"
      "v_mfma_f32_16x16x32_bf16 v[160:163], v[240:243], a[76:79], v[160:163]\n"
      "v_mov_b64 v[84:85], 0\n"
      "v_cvt_pk_bf16_f32 v165, v198, v199\n"
      "v_smfmac_f32_16x16x64_bf16 v[156:159], v[32:35], v[204:211], v5\n"
      "ds_read2_b32 v[222:223], v16 offset0:128 offset1:130\n"
      "ds_read2_b32 v[220:221], v16 offset0:0 offset1:2\n"
      "v_smfmac_f32_16x16x64_bf16 v[84:87], v[32:35], v[164:171], v5\n"
      "s_nop 1\n"
      "v_cvt_pk_bf16_f32 v108, v160, v161\n"
      "v_mfma_f32_16x16x32_bf16 v[196:199], v[44:47], a[80:83], 0\n"
      "v_add_f32 v146, v146, v147\n"
      "v_cvt_pk_bf16_f32 v109, v162, v163\n"
      "v_mfma_f32_16x16x32_bf16 v[196:199], v[248:251], a[84:87], v[196:199]\n"
      "v_add_f32 v156, v156, v157\n"
      "v_add_f32 v84, v84, v85\n"
      "v_mfma_f32_16x16x32_bf16 v[196:199], v[252:255], a[88:91], v[196:199]\n"
      "v_exp_f32 v146, v146\n"
      "v_mfma_f32_16x16x32_bf16 v[196:199], v[240:243], a[92:95], v[196:199]\n"
      "buffer_load_dwordx4 v1, s[40:43], s77 offen offset:2048 lds\n"
      "v_mfma_f32_16x16x32_bf16 v[158:161], v[44:47], a[96:99], 0\n"
      "v_exp_f32 v84, v84\n"
      "v_mfma_f32_16x16x32_bf16 v[158:161], v[248:251], a[100:103], v[158:161]\n"
      "v_exp_f32 v156, v156\n"
      "v_mfma_f32_16x16x32_bf16 v[158:161], v[252:255], a[104:107], v[158:161]\n"
      "s_nop 0\n"
      "v_cndmask_b32_dpp v157, v28, v156, vcc quad_perm:[0,0,2,2] row_mask:0xf bank_mask:0xf\n"
      "v_cndmask_b32_dpp v85, v146, v84, vcc quad_perm:[0,0,2,2] row_mask:0xf bank_mask:0xf\n"
      "v_mfma_f32_16x16x32_bf16 v[158:161], v[240:243], a[108:111], v[158:161]\n"
      "s_not_b64 vcc, vcc\n"
      "v_cndmask_b32_dpp v203, v192, v202, vcc quad_perm:[1,1,3,3] row_mask:0xf bank_mask:0xf\n"
      "v_mov_b64 v[248:249], 0\n"
      "v_cndmask_b32_dpp v147, v84, v146, vcc quad_perm:[1,1,3,3] row_mask:0xf bank_mask:0xf\n"
      "v_cvt_pk_bf16_f32 v193, v193, v203\n"
      "v_mfma_f32_16x16x32_bf16 v[148:151], v[164:167], v[88:91], 0\n"
      "v_cndmask_b32_dpp v29, v156, v28, vcc quad_perm:[1,1,3,3] row_mask:0xf bank_mask:0xf\n"
      "v_cvt_pk_bf16_f32 v180, v196, v197\n"
      "v_mfma_f32_16x16x32_bf16 v[148:151], v[168:171], v[92:95], v[148:151]\n"
      "s_waitcnt lgkmcnt(0)\n"
      "v_mov_b32_dpp v44, v220 row_ror:8 row_mask:0xf bank_mask:0xf\n"
      "v_add_f32 v192, v192, v202\n"
      "v_mov_b64 v[250:251], 0\n"
      "v_mfma_f32_16x16x32_bf16 v[240:243], v[164:167], v[100:103], 0\n"
      "v_add_f32 v84, v84, v156\n"
      "v_cvt_pk_bf16_f32 v157, v157, v29\n"
      "v_mfma_f32_16x16x32_bf16 v[240:243], v[168:171], v[104:107], v[240:243]\n"
      "v_add_f32 v192, v192, v145\n"
      "v_mov_b32_dpp v45, v221 row_ror:8 row_mask:0xf bank_mask:0xf\n"
      "v_mov_b32_dpp v47, v223 row_ror:8 row_mask:0xf bank_mask:0xf\n"
      "v_mfma_f32_16x16x32_bf16 v[152:155], v[204:207], v[88:91], 0\n"
      "v_cvt_pk_bf16_f32 v56, v158, v159\n"
      "v_add_f32 v146, v146, v28\n"
      "v_mfma_f32_16x16x32_bf16 v[152:155], v[208:211], v[92:95], v[152:155]\n"
      "v_cvt_pk_bf16_f32 v85, v85, v147\n"
      "v_exp_f32 v242, v242\n"
      "v_add_f32 v84, v84, v146\n"
      "v_mov_b32_dpp v46, v222 row_ror:8 row_mask:0xf bank_mask:0xf\n"
      "v_smfmac_f32_16x16x64_bf16 v[248:251], v[220:223], v[88:95], v5\n"
      "ds_bpermute_b32 v43, v6, v157 offset:16\n"
      "v_smfmac_f32_16x16x64_bf16 v[248:251], v[44:47], v[100:107], v5\n"
      "ds_bpermute_b32 v255, v7, v193 offset:4\n"
      "v_exp_f32 v240, v240\n"
      "v_mfma_f32_16x16x32_bf16 v[32:35], v[76:79], v[100:103], 0\n"
      "v_exp_f32 v243, v243\n"
      "v_mfma_f32_16x16x32_bf16 v[32:35], v[80:83], v[104:107], v[32:35]\n"
      "v_exp_f32 v148, v148\n"
      "v_mfma_f32_16x16x32_bf16 v[200:203], v[204:207], v[100:103], 0\n"
      "ds_bpermute_b32 v41, v6, v157\n"
      "v_mfma_f32_16x16x32_bf16 v[200:203], v[208:211], v[104:107], v[200:203]\n"
      "ds_bpermute_b32 v254, v7, v193\n"
      "v_add_f32 v90, v84, v192\n"
      "v_mfma_f32_16x16x32_bf16 v[156:159], v[136:139], v[100:103], 0\n"
      "s_nop 0\n"
      "v_exp_f32 v35, v35\n"
      "v_mfma_f32_16x16x32_bf16 v[156:159], v[140:143], v[104:107], v[156:159]\n"
      "v_exp_f32 v34, v34\n"
      "v_mfma_f32_16x16x32_bf16 v[192:195], v[20:23], v[100:103], 0\n"
      "ds_bpermute_b32 v40, v6, v85\n"
      "v_mfma_f32_16x16x32_bf16 v[192:195], v[24:27], v[104:107], v[192:195]\n"
      "v_add_f32 v250, v250, v251\n"
      "v_exp_f32 v201, v201\n"
      "v_exp_f32 v149, v149\n"
      "v_cvt_pk_bf16_f32 v57, v160, v161\n"
      "v_mfma_f32_16x16x32_bf16 v[92:95], a[128:131], v[48:51], 0\n"
      "ds_bpermute_b32 v42, v6, v85 offset:16\n"
      "v_mfma_f32_16x16x32_bf16 v[92:95], a[132:135], v[216:219], v[92:95]\n"
      "v_exp_f32 v158, v158\n"
      "v_mfma_f32_16x16x32_bf16 v[92:95], a[136:139], v[132:135], v[92:95]\n"
      "v_exp_f32 v150, v150\n"
      "v_mfma_f32_16x16x32_bf16 v[92:95], a[140:143], v[224:227], v[92:95]\n"
      "v_exp_f32 v192, v192\n"
      "v_exp_f32 v155, v155\n"
      "v_exp_f32 v153, v153\n"
      "v_add_f32 v248, v248, v249\n"
      "v_exp_f32 v248, v248\n"
      "v_exp_f32 v250, v250\n"
      "v_mfma_f32_16x16x32_bf16 v[86:89], v[64:67], v[100:103], 0\n"
      "v_exp_f32 v152, v152\n"
      "v_mfma_f32_16x16x32_bf16 v[86:89], v[68:71], v[104:107], v[86:89]\n"
      "v_exp_f32 v195, v195\n"
      "v_exp_f32 v154, v154\n"
      "v_mfma_f32_16x16x32_bf16 v[100:103], a[144:147], v[48:51], 0\n"
      "v_exp_f32 v157, v157\n"
      "v_mfma_f32_16x16x32_bf16 v[100:103], a[148:151], v[216:219], v[100:103]\n"
      "v_exp_f32 v241, v241\n"
      "v_mfma_f32_16x16x32_bf16 v[100:103], a[152:155], v[132:135], v[100:103]\n"
      "v_exp_f32 v203, v203\n"
      "v_mfma_f32_16x16x32_bf16 v[100:103], a[156:159], v[224:227], v[100:103]\n"
      "v_cvt_pk_bf16_f32 v181, v198, v199\n"
      "v_exp_f32 v89, v89\n"
      "v_exp_f32 v200, v200\n"
      "v_exp_f32 v194, v194\n"
      "v_exp_f32 v193, v193\n"
      "v_cvt_pk_bf16_f32 v29, v94, v95\n"
      "v_cvt_pk_bf16_f32 v196, v240, v241\n"
      "v_exp_f32 v202, v202\n"
      "v_exp_f32 v33, v33\n"
      "v_cvt_pk_bf16_f32 v31, v102, v103\n"
      "v_cvt_pk_bf16_f32 v197, v242, v243\n"
      "v_exp_f32 v159, v159\n"
      "v_cvt_pk_bf16_f32 v199, v202, v203\n"
      "v_cvt_pk_bf16_f32 v30, v100, v101\n"
      "v_exp_f32 v86, v86\n"
      "v_add_f32 v100, v248, v250\n"
      "v_cvt_pk_bf16_f32 v95, v154, v155\n"
      "v_exp_f32 v151, v151\n"
      "v_cvt_pk_bf16_f32 v198, v200, v201\n"
      "v_cvt_pk_bf16_f32 v28, v92, v93\n"
      "v_cvt_pk_bf16_f32 v162, v248, v250\n"
      "v_cvt_pk_bf16_f32 v200, v192, v193\n"
      "v_cvt_pk_bf16_f32 v94, v152, v153\n"
      "v_cvt_pk_bf16_f32 v203, v34, v35\n"
      "v_cvt_pk_bf16_f32 v163, v248, v250\n"
      "v_mfma_f32_16x16x32_bf16 v[240:243], a[176:179], v[48:51], 0\n"
      "v_cvt_pk_bf16_f32 v201, v194, v195\n"
      "v_add_f32 v101, v248, v250\n"
      "v_mfma_f32_16x16x32_bf16 v[240:243], a[180:183], v[216:219], v[240:243]\n"
      "v_permlane16_swap_b32 v162, v163\n"
      "v_mfma_f32_16x16x32_bf16 v[240:243], a[184:187], v[132:135], v[240:243]\n"
      "v_exp_f32 v88, v88\n"
      "v_mfma_f32_16x16x32_bf16 v[240:243], a[188:191], v[224:227], v[240:243]\n"
      "ds_read_b64 v[18:19], v11 offset:0\n"
      "v_permlane16_swap_b32 v100, v101\n"
      "v_mov_b64 v[248:249], v[100:101]\n"
      "v_mfma_f32_16x16x32_bf16 v[152:155], a[160:163], v[48:51], 0\n"
      "v_exp_f32 v32, v32\n"
      "v_mfma_f32_16x16x32_bf16 v[152:155], a[164:167], v[216:219], v[152:155]\n"
      "v_cvt_pk_bf16_f32 v202, v32, v33\n"
      "v_cvt_pk_bf16_f32 v193, v88, v89\n"
      "v_mfma_f32_16x16x32_bf16 v[152:155], a[168:171], v[132:135], v[152:155]\n"
      "v_exp_f32 v156, v156\n"
      "v_mfma_f32_16x16x32_bf16 v[152:155], a[172:175], v[224:227], v[152:155]\n"
      "v_cvt_pk_bf16_f32 v92, v148, v149\n"
      "v_cvt_pk_bf16_f32 v93, v150, v151\n"
      "v_cvt_pk_bf16_f32 v194, v156, v157\n"
      "v_smfmac_f32_16x16x64_bf16 v[248:251], v[12:15], v[196:203], v5\n"
      "ds_bpermute_b32 v253, v7, v144 offset:8\n"
      "v_smfmac_f32_16x16x64_bf16 v[100:103], v[12:15], v[92:99], v5\n"
      "ds_bpermute_b32 v252, v7, v144 offset:248\n"
      "v_cvt_pk_bf16_f32 v195, v158, v159\n"
      "v_cvt_pk_bf16_f32 v35, v242, v243\n"
      "v_exp_f32 v87, v87\n"
      "v_cvt_pk_bf16_f32 v32, v152, v153\n"
      "v_cvt_pk_bf16_f32 v33, v154, v155\n"
      "v_smfmac_f32_16x16x32_bf16 v[100:103], v[12:13], v[176:179], v5\n"
      "v_cvt_pk_bf16_f32 v192, v86, v87\n"
      "v_cvt_pk_bf16_f32 v34, v240, v241\n"
      "s_waitcnt lgkmcnt(2)\n"
      "v_mfma_f32_16x16x4_4b_bf16 v[146:161], v[162:163], v[18:19], 0 cbsz:2 abid:0\n"
      "s_nop 3\n"
      "v_add_f32 v100, v100, v101\n"
      "v_rcp_f32 v101, v100\n"
      "v_smfmac_f32_16x16x32_bf16 v[248:251], v[12:13], v[192:195], v5\n"
      "ds_read_b64 v[88:89], v11 offset:4096\n"
      "ds_bpermute_b32 v102, v4, v101\n"
      "s_nop 0\n"
      "v_mfma_f32_16x16x32_bf16 v[150:153], v[92:95], v[180:183], v[150:153]\n"
      "v_mfma_f32_16x16x32_bf16 v[150:153], v[96:99], v[184:187], v[150:153]\n"
      "v_add_f32_dpp v90, v90, v90 row_ror:8 row_mask:0xf bank_mask:0xf\n"
      "v_mfma_f32_16x16x32_bf16 v[150:153], v[176:179], v[120:123], v[150:153]\n"
      "s_nop 0\n"
      "v_add_f32_dpp v90, v90, v90 row_ror:4 row_mask:0xf bank_mask:0xf\n"
      "v_mfma_f32_16x16x32_bf16 v[146:149], v[92:95], v[108:111], v[146:149]\n"
      "buffer_load_dwordx4 v1, s[40:43], s78 offen offset:1024 lds\n"
      "v_mfma_f32_16x16x32_bf16 v[146:149], v[96:99], v[112:115], v[146:149]\n"
      "v_add_f32_dpp v90, v90, v90 row_ror:2 row_mask:0xf bank_mask:0xf\n"
      "v_mfma_f32_16x16x32_bf16 v[146:149], v[176:179], v[52:55], v[146:149]\n"
      "s_nop 0\n"
      "v_add_f32_dpp v90, v90, v90 row_ror:1 row_mask:0xf bank_mask:0xf\n"
      "v_mfma_f32_16x16x32_bf16 v[158:161], v[92:95], v[232:235], v[158:161]\n"
      "v_rcp_f32 v91, v90\n"
      "v_mfma_f32_16x16x32_bf16 v[158:161], v[96:99], v[236:239], v[158:161]\n"
      "v_mfma_f32_16x16x32_bf16 v[158:161], v[176:179], v[228:231], v[158:161]\n"
      "s_waitcnt lgkmcnt(0)\n"
      "s_nop 0\n"
      "v_mul_f32_dpp v149, v102, v149 row_newbcast:12 row_mask:0xf bank_mask:0xf\n"
      "v_mfma_f32_16x16x32_bf16 v[154:157], v[92:95], v[56:59], v[154:157]\n"
      "v_mul_f32_dpp v153, v102, v153 row_newbcast:12 row_mask:0xf bank_mask:0xf\n"
      "v_mul_f32_dpp v150, v102, v150 row_newbcast:0 row_mask:0xf bank_mask:0xf\n"
      "v_mfma_f32_16x16x32_bf16 v[154:157], v[96:99], v[60:63], v[154:157]\n"
      "v_mul_f32_dpp v151, v102, v151 row_newbcast:4 row_mask:0xf bank_mask:0xf\n"
      "v_mul_f32_dpp v152, v102, v152 row_newbcast:8 row_mask:0xf bank_mask:0xf\n"
      "v_mfma_f32_16x16x32_bf16 v[154:157], v[176:179], v[116:119], v[154:157]\n"
      "v_add_u32 v11, 0x200, v11\n"
      "v_mul_f32_dpp v146, v102, v146 row_newbcast:0 row_mask:0xf bank_mask:0xf\n"
      "v_mfma_f32_16x16x32_bf16 v[150:153], v[212:215], a[208:211], v[150:153]\n"
      "v_mul_f32_dpp v148, v102, v148 row_newbcast:8 row_mask:0xf bank_mask:0xf\n"
      "v_mul_f32_dpp v147, v102, v147 row_newbcast:4 row_mask:0xf bank_mask:0xf\n"
      "v_mfma_f32_16x16x32_bf16 v[150:153], v[124:127], a[212:215], v[150:153]\n"
      "v_mfma_f32_16x16x32_bf16 v[150:153], v[188:191], a[216:219], v[150:153]\n"
      "v_maximum3_f32 v8, v8, v90, v91\n"
      "v_mul_f32_dpp v161, v102, v161 row_newbcast:12 row_mask:0xf bank_mask:0xf\n"
      "v_mfma_f32_16x16x32_bf16 v[150:153], v[244:247], a[220:223], v[150:153]\n"
      "v_mul_f32_dpp v155, v102, v155 row_newbcast:4 row_mask:0xf bank_mask:0xf\n"
      "v_mul_f32_dpp v156, v102, v156 row_newbcast:8 row_mask:0xf bank_mask:0xf\n"
      "v_mfma_f32_16x16x32_bf16 v[146:149], v[212:215], a[192:195], v[146:149]\n"
      "v_maximum3_f32 v8, v8, v100, v101\n"
      "v_mul_f32_dpp v157, v102, v157 row_newbcast:12 row_mask:0xf bank_mask:0xf\n"
      "v_mfma_f32_16x16x32_bf16 v[146:149], v[124:127], a[196:199], v[146:149]\n"
      "v_mul_f32_dpp v154, v102, v154 row_newbcast:0 row_mask:0xf bank_mask:0xf\n"
      "v_mfma_f32_16x16x32_bf16 v[146:149], v[188:191], a[200:203], v[146:149]\n"
      "v_mul_f32_dpp v159, v102, v159 row_newbcast:4 row_mask:0xf bank_mask:0xf\n"
      "v_mul_f32_dpp v160, v102, v160 row_newbcast:8 row_mask:0xf bank_mask:0xf\n"
      "v_mfma_f32_16x16x32_bf16 v[146:149], v[244:247], a[204:207], v[146:149]\n"
      "v_mul_f32_dpp v158, v102, v158 row_newbcast:0 row_mask:0xf bank_mask:0xf\n"
      "v_add_f32 v248, v248, v249\n"
      "v_mfma_f32_16x16x32_bf16 v[154:157], v[212:215], a[224:227], v[154:157]\n"
      "v_rcp_f32 v249, v248\n"
      "v_mfma_f32_16x16x32_bf16 v[154:157], v[124:127], a[228:231], v[154:157]\n"
      "v_mfma_f32_16x16x32_bf16 v[154:157], v[188:191], a[232:235], v[154:157]\n"
      "ds_bpermute_b32 v250, v4, v249 offset:128\n"
      "v_mfma_f32_16x16x32_bf16 v[154:157], v[244:247], a[236:239], v[154:157]\n"
      "v_maximum3_f32 v8, v8, v248, v249\n"
      "v_cvt_pk_bf16_f32 v146, v146, v150\n"
      "v_mfma_f32_16x16x4_4b_bf16 v[92:107], v[162:163], v[18:19], 0 cbsz:2 abid:2\n"
      "v_cvt_pk_bf16_f32 v150, v147, v151\n"
      "v_pk_max_i16 v150, v150, 0\n"
      "buffer_load_dwordx4 v1, s[40:43], s78 offen offset:3072 lds\n"
      "v_cvt_pk_bf16_f32 v148, v148, v152\n"
      "v_mfma_f32_16x16x32_bf16 v[158:161], v[212:215], a[240:243], v[158:161]\n"
      "v_mfma_f32_16x16x32_bf16 v[158:161], v[124:127], a[244:247], v[158:161]\n"
      "v_mfma_f32_16x16x32_bf16 v[158:161], v[188:191], a[248:251], v[158:161]\n"
      "v_mfma_f32_16x16x32_bf16 v[158:161], v[244:247], a[252:255], v[158:161]\n"
      "v_cvt_pk_bf16_f32 v152, v149, v153\n"
      "v_pk_max_i16 v146, v146, 0\n"
      "v_mfma_f32_16x16x32_bf16 v[92:95], v[196:199], v[108:111], v[92:95]\n"
      "v_mfma_f32_16x16x32_bf16 v[92:95], v[200:203], v[112:115], v[92:95]\n"
      "v_and_b32 v213, 0xffff0000, v88\n"
      "v_add_u32 v16, 64, v16\n"
      "v_mfma_f32_16x16x32_bf16 v[92:95], v[192:195], v[52:55], v[92:95]\n"
      "s_nop 0\n"
      "v_cvt_pk_bf16_f32 v153, v157, v161\n"
      "v_pk_max_i16 v152, v152, 0\n"
      "v_mfma_f32_16x16x32_bf16 v[100:103], v[196:199], v[56:59], v[100:103]\n"
      "v_cvt_pk_bf16_f32 v149, v156, v160\n"
      "v_pk_max_i16 v149, v149, 0\n"
      "v_mfma_f32_16x16x32_bf16 v[100:103], v[200:203], v[60:63], v[100:103]\n"
      "v_cvt_pk_bf16_f32 v147, v154, v158\n"
      "v_mfma_f32_16x16x32_bf16 v[100:103], v[192:195], v[116:119], v[100:103]\n"
      "ds_read_b128 v[240:243], v0 offset:9216\n"
      "ds_read_b128 v[160:163], v0 offset:8192\n"
      "v_mfma_f32_16x16x32_bf16 v[104:107], v[196:199], v[232:235], v[104:107]\n"
      "s_add_u32 m0, s53, 8192\n"
      "ds_read_b128 v[124:127], v0 offset:9728\n"
      "v_cvt_pk_bf16_f32 v151, v155, v159\n"
      "v_mfma_f32_16x16x32_bf16 v[104:107], v[200:203], v[236:239], v[104:107]\n"
      "s_waitcnt lgkmcnt(3)\n"
      "v_mul_f32_dpp v95, v250, v95 row_newbcast:12 row_mask:0xf bank_mask:0xf\n"
      "v_mul_f32_dpp v92, v250, v92 row_newbcast:0 row_mask:0xf bank_mask:0xf\n"
      "v_mfma_f32_16x16x32_bf16 v[104:107], v[192:195], v[228:231], v[104:107]\n"
      "ds_read_b128 v[84:87], v0 offset:8704\n"
      "v_pk_max_i16 v148, v148, 0\n"
      "v_mfma_f32_16x16x32_bf16 v[96:99], v[196:199], v[180:183], v[96:99]\n"
      "v_mfma_f32_16x16x32_bf16 v[96:99], v[200:203], v[184:187], v[96:99]\n"
      "v_pk_max_i16 v151, v151, 0\n"
      "v_pk_max_i16 v153, v153, 0\n"
      "v_mfma_f32_16x16x32_bf16 v[96:99], v[192:195], v[120:123], v[96:99]\n"
      "v_and_b32 v215, 0xffff0000, v89\n"
      "v_lshlrev_b32 v212, 16, v88\n"
      "v_mfma_f32_16x16x4_4b_bf16 v[188:203], v[252:253], v[18:19], 0 cbsz:2 abid:0\n"
      "buffer_store_dwordx2 v[152:153], v2, s[44:47], s73 offen offset:768 nt sc1\n"
      "v_mul_f32_dpp v103, v250, v103 row_newbcast:12 row_mask:0xf bank_mask:0xf\n"
      "v_mul_f32_dpp v93, v250, v93 row_newbcast:4 row_mask:0xf bank_mask:0xf\n"
      "v_mul_f32_dpp v101, v250, v101 row_newbcast:4 row_mask:0xf bank_mask:0xf\n"
      "v_mul_f32_dpp v100, v250, v100 row_newbcast:0 row_mask:0xf bank_mask:0xf\n"
      "v_mfma_f32_16x16x32_bf16 v[176:179], v[20:23], v[28:31], 0\n"
      "v_mul_f32_dpp v96, v250, v96 row_newbcast:0 row_mask:0xf bank_mask:0xf\n"
      "v_mul_f32_dpp v106, v250, v106 row_newbcast:8 row_mask:0xf bank_mask:0xf\n"
      "v_mfma_f32_16x16x32_bf16 v[176:179], v[24:27], v[32:35], v[176:179]\n"
      "v_mul_f32_dpp v98, v250, v98 row_newbcast:8 row_mask:0xf bank_mask:0xf\n"
      "v_mul_f32_dpp v99, v250, v99 row_newbcast:12 row_mask:0xf bank_mask:0xf\n"
      "v_smfmac_f32_16x16x64_bf16 v[196:199], v[40:43], v[56:63], v5\n"
      "v_smfmac_f32_16x16x64_bf16 v[192:195], v[40:43], v[180:187], v5\n"
      "s_waitcnt lgkmcnt(0)\n"
      "buffer_load_dwordx4 v1, s[40:43], s91 offen offset:3072 lds\n"
      "v_smfmac_f32_16x16x64_bf16 v[188:191], v[40:43], v[108:115], v5\n"
      "v_mul_f32_dpp v94, v250, v94 row_newbcast:8 row_mask:0xf bank_mask:0xf\n"
      "v_smfmac_f32_16x16x64_bf16 v[200:203], v[40:43], v[232:239], v5\n"
      "v_mul_f32_dpp v107, v250, v107 row_newbcast:12 row_mask:0xf bank_mask:0xf\n"
      "v_mfma_f32_16x16x32_bf16 v[244:247], v[204:207], v[28:31], 0\n"
      "v_mul_f32_dpp v102, v250, v102 row_newbcast:8 row_mask:0xf bank_mask:0xf\n"
      "v_mul_f32_dpp v97, v250, v97 row_newbcast:4 row_mask:0xf bank_mask:0xf\n"
      "v_mfma_f32_16x16x32_bf16 v[244:247], v[208:211], v[32:35], v[244:247]\n"
      "v_lshlrev_b32 v214, 16, v89\n"
      "v_mul_f32_dpp v104, v250, v104 row_newbcast:0 row_mask:0xf bank_mask:0xf\n"
      "v_smfmac_f32_16x16x32_bf16 v[200:203], v[254:255], v[228:231], v5\n"
      "v_mul_f32_dpp v105, v250, v105 row_newbcast:4 row_mask:0xf bank_mask:0xf\n"
      "v_smfmac_f32_16x16x32_bf16 v[196:199], v[254:255], v[116:119], v5\n"
      "v_pk_max_i16 v147, v147, 0\n"
      "v_smfmac_f32_16x16x32_bf16 v[188:191], v[254:255], v[52:55], v5\n"
      "buffer_load_dwordx4 v1, s[40:43], s91 offen offset:1024 lds\n"
      "v_smfmac_f32_16x16x32_bf16 v[192:195], v[254:255], v[120:123], v5\n"
      "buffer_store_dwordx2 v[146:147], v2, s[44:47], s73 offen offset:0 nt sc1\n"
      "v_mfma_f32_16x16x32_bf16 v[152:155], v[64:67], v[28:31], 0\n"
      "v_exp_f32 v179, v179\n"
      "v_mfma_f32_16x16x32_bf16 v[152:155], v[68:71], v[32:35], v[152:155]\n"
      "v_exp_f32 v246, v246\n"
      "v_mfma_f32_16x16x32_bf16 v[104:107], v[172:175], a[240:243], v[104:107]\n"
      "v_exp_f32 v244, v244\n"
      "v_mfma_f32_16x16x32_bf16 v[104:107], v[72:75], a[244:247], v[104:107]\n"
      "v_add_f32 v188, v188, v189\n"
      "v_add_f32 v196, v196, v197\n"
      "v_mfma_f32_16x16x32_bf16 v[104:107], v[128:131], a[248:251], v[104:107]\n"
      "v_add_f32 v192, v192, v193\n"
      "v_fma_f32 v192, v192, v91, v213\n"
      "v_mfma_f32_16x16x32_bf16 v[104:107], v[36:39], a[252:255], v[104:107]\n"
      "v_exp_f32 v177, v177\n"
      "v_mfma_f32_16x16x32_bf16 v[252:255], v[136:139], v[28:31], 0\n"
      "v_fma_f32 v188, v188, v91, v212\n"
      "v_fma_f32 v196, v196, v91, v214\n"
      "v_mfma_f32_16x16x32_bf16 v[252:255], v[140:143], v[32:35], v[252:255]\n"
      "v_exp_f32 v152, v152\n"
      "v_mfma_f32_16x16x32_bf16 v[96:99], v[172:175], a[208:211], v[96:99]\n"
      "v_add_f32 v200, v200, v201\n"
      "v_fma_f32 v200, v200, v91, v215\n"
      "v_mfma_f32_16x16x32_bf16 v[96:99], v[72:75], a[212:215], v[96:99]\n"
      "v_cvt_pk_bf16_f32 v189, v196, v200\n"
      "v_cvt_pk_bf16_f32 v188, v188, v192\n"
      "v_mfma_f32_16x16x32_bf16 v[96:99], v[128:131], a[216:219], v[96:99]\n"
      "v_exp_f32 v255, v255\n"
      "v_mfma_f32_16x16x32_bf16 v[96:99], v[36:39], a[220:223], v[96:99]\n"
      "v_exp_f32 v155, v155\n"
      "v_mfma_f32_16x16x32_bf16 v[88:91], a[144:147], v[160:163], 0\n"
      "v_exp_f32 v247, v247\n"
      "v_mfma_f32_16x16x32_bf16 v[88:91], a[148:151], v[84:87], v[88:91]\n"
      "v_exp_f32 v245, v245\n"
      "v_mfma_f32_16x16x32_bf16 v[88:91], a[152:155], v[240:243], v[88:91]\n"
      "v_exp_f32 v153, v153\n"
      "v_mfma_f32_16x16x32_bf16 v[88:91], a[156:159], v[124:127], v[88:91]\n"
      "v_exp_f32 v176, v176\n"
      "v_mfma_f32_16x16x32_bf16 v[156:159], v[164:167], v[28:31], 0\n"
      "v_exp_f32 v178, v178\n"
      "v_mfma_f32_16x16x32_bf16 v[156:159], v[168:171], v[32:35], v[156:159]\n"
      "v_cvt_pk_bf16_f32 v196, v176, v177\n"
      "v_mfma_f32_16x16x32_bf16 v[40:43], v[76:79], v[28:31], 0\n"
      "v_cvt_pk_bf16_f32 v197, v178, v179\n"
      "v_cvt_pk_bf16_f32 v194, v244, v245\n"
      "v_mfma_f32_16x16x32_bf16 v[40:43], v[80:83], v[32:35], v[40:43]\n"
      "buffer_store_dwordx2 v[148:149], v2, s[44:47], s73 offen offset:512 nt sc1\n"
      "v_mfma_f32_16x16x32_bf16 v[92:95], v[172:175], a[192:195], v[92:95]\n"
      "v_exp_f32 v252, v252\n"
      "v_mfma_f32_16x16x32_bf16 v[92:95], v[72:75], a[196:199], v[92:95]\n"
      "v_exp_f32 v253, v253\n"
      "v_mfma_f32_16x16x32_bf16 v[92:95], v[128:131], a[200:203], v[92:95]\n"
      "v_exp_f32 v159, v159\n"
      "v_mfma_f32_16x16x32_bf16 v[92:95], v[36:39], a[204:207], v[92:95]\n"
      "v_exp_f32 v43, v43\n"
      "v_mfma_f32_16x16x32_bf16 v[212:215], a[160:163], v[160:163], 0\n"
      "buffer_load_dwordx4 v1, s[40:43], s90 offen offset:2048 lds\n"
      "v_mfma_f32_16x16x32_bf16 v[212:215], a[164:167], v[84:87], v[212:215]\n"
      "v_exp_f32 v42, v42\n"
      "v_mfma_f32_16x16x32_bf16 v[212:215], a[168:171], v[240:243], v[212:215]\n"
      "v_exp_f32 v157, v157\n"
      "v_mfma_f32_16x16x32_bf16 v[212:215], a[172:175], v[124:127], v[212:215]\n"
      "v_exp_f32 v40, v40\n"
      "v_mfma_f32_16x16x32_bf16 v[100:103], v[172:175], a[224:227], v[100:103]\n"
      "v_exp_f32 v254, v254\n"
      "v_mfma_f32_16x16x32_bf16 v[100:103], v[72:75], a[228:231], v[100:103]\n"
      "v_exp_f32 v158, v158\n"
      "v_mfma_f32_16x16x32_bf16 v[100:103], v[128:131], a[232:235], v[100:103]\n"
      "v_exp_f32 v156, v156\n"
      "v_mfma_f32_16x16x32_bf16 v[100:103], v[36:39], a[236:239], v[100:103]\n"
      "v_exp_f32 v41, v41\n"
      "v_mfma_f32_16x16x32_bf16 v[172:175], a[128:131], v[160:163], 0\n"
      "v_cvt_pk_bf16_f32 v192, v156, v157\n"
      "v_mov_b64 v[128:129], 0\n"
      "v_mfma_f32_16x16x32_bf16 v[172:175], a[132:135], v[84:87], v[172:175]\n"
      "v_cvt_pk_bf16_f32 v176, v152, v153\n"
      "v_cvt_pk_bf16_f32 v178, v252, v253\n"
      "v_mfma_f32_16x16x32_bf16 v[172:175], a[136:139], v[240:243], v[172:175]\n"
      "v_mov_b64 v[130:131], 0\n"
      "v_cvt_pk_bf16_f32 v198, v40, v41\n"
      "v_mfma_f32_16x16x32_bf16 v[172:175], a[140:143], v[124:127], v[172:175]\n"
      "v_exp_f32 v154, v154\n"
      "v_mfma_f32_16x16x32_bf16 v[72:75], a[176:179], v[160:163], 0\n"
      "v_cvt_pk_bf16_f32 v195, v246, v247\n"
      "v_cvt_pk_bf16_f32 v252, v212, v213\n"
      "v_mfma_f32_16x16x32_bf16 v[72:75], a[180:183], v[84:87], v[72:75]\n"
      "v_cvt_pk_bf16_f32 v179, v254, v255\n"
      "v_cvt_pk_bf16_f32 v250, v88, v89\n"
      "v_mfma_f32_16x16x32_bf16 v[72:75], a[184:187], v[240:243], v[72:75]\n"
      "v_cvt_pk_bf16_f32 v249, v174, v175\n"
      "v_cvt_pk_bf16_f32 v251, v90, v91\n"
      "v_mfma_f32_16x16x32_bf16 v[72:75], a[188:191], v[124:127], v[72:75]\n"
      "v_cvt_pk_bf16_f32 v248, v172, v173\n"
      "v_pk_max_i16 v189, v189, 0\n"
      "v_cvt_pk_bf16_f32 v92, v92, v96\n"
      "v_cvt_pk_bf16_f32 v94, v94, v98\n"
      "v_cvt_pk_bf16_f32 v96, v93, v97\n"
      "v_cvt_pk_bf16_f32 v253, v214, v215\n"
      "v_mfma_f32_16x16x32_bf16 v[172:175], v[136:139], v[248:251], 0\n"
      "s_nop 0\n"
      "v_cvt_pk_bf16_f32 v255, v74, v75\n"
      "v_cvt_pk_bf16_f32 v254, v72, v73\n"
      "v_smfmac_f32_16x16x64_bf16 v[128:131], v[220:223], v[28:35], v5\n"
      "buffer_load_dwordx4 v1, s[40:43], s90 offen offset:0 lds\n"
      "v_smfmac_f32_16x16x64_bf16 v[128:131], v[44:47], v[248:255], v5\n"
      "buffer_store_dwordx2 v[150:151], v2, s[44:47], s73 offen offset:256 nt sc1\n"
      "v_pk_max_i16 v188, v188, 0\n"
      "v_cvt_pk_bf16_f32 v199, v42, v43\n"
      "v_mfma_f32_16x16x32_bf16 v[200:203], v[76:79], v[248:251], 0\n"
      "v_pk_max_i16 v92, v92, 0\n"
      "v_cvt_pk_bf16_f32 v177, v154, v155\n"
      "v_mfma_f32_16x16x32_bf16 v[200:203], v[80:83], v[252:255], v[200:203]\n"
      "s_nop 0\n"
      "v_add_f32 v130, v130, v131\n"
      "v_exp_f32 v130, v130\n"
      "v_mfma_f32_16x16x32_bf16 v[172:175], v[140:143], v[252:255], v[172:175]\n"
      "v_add_f32 v128, v128, v129\n"
      "v_exp_f32 v128, v128\n"
      "v_mfma_f32_16x16x32_bf16 v[150:153], v[204:207], v[248:251], 0\n"
      "v_cvt_pk_bf16_f32 v193, v158, v159\n"
      "v_add_f32 v213, v128, v130\n"
      "v_mfma_f32_16x16x32_bf16 v[150:153], v[208:211], v[252:255], v[150:153]\n"
      "v_add_f32 v212, v128, v130\n"
      "v_cvt_pk_bf16_f32 v155, v128, v130\n"
      "v_mfma_f32_16x16x32_bf16 v[244:247], v[64:67], v[248:251], 0\n"
      "v_permlane16_swap_b32 v212, v213\n"
      "v_mfma_f32_16x16x32_bf16 v[244:247], v[68:71], v[252:255], v[244:247]\n"
      "v_cvt_pk_bf16_f32 v154, v128, v130\n"
      "v_mov_b64 v[148:149], v[212:213]\n"
      "v_smfmac_f32_16x16x64_bf16 v[212:215], v[12:15], v[192:199], v5\n"
      "v_cvt_pk_bf16_f32 v98, v95, v99\n"
      "v_mfma_f32_16x16x32_bf16 v[128:131], v[164:167], v[248:251], 0\n"
      "s_add_u32 m0, s53, 4096\n"
      "v_exp_f32 v203, v203\n"
      "v_mfma_f32_16x16x32_bf16 v[128:131], v[168:171], v[252:255], v[128:131]\n"
      "v_permlane16_swap_b32 v154, v155\n"
      "v_smfmac_f32_16x16x32_bf16 v[212:215], v[12:13], v[176:179], v5\n"
      "v_pk_max_i16 v94, v94, 0\n"
      "v_mfma_f32_16x16x4_4b_bf16 v[28:43], v[154:155], v[18:19], 0 cbsz:2 abid:0\n"
      "v_exp_f32 v151, v151\n"
      "v_cvt_pk_bf16_f32 v97, v101, v105\n"
      "v_cvt_pk_bf16_f32 v95, v102, v106\n"
      "v_cvt_pk_bf16_f32 v93, v100, v104\n"
      "v_exp_f32 v130, v130\n"
      "v_cvt_pk_bf16_f32 v99, v103, v107\n"
      "v_mfma_f32_16x16x32_bf16 v[144:147], v[20:23], v[248:251], 0\n"
      "ds_read_b128 v[100:103], v0 offset:4096\n"
      "v_add_f32 v212, v212, v213\n"
      "v_mfma_f32_16x16x32_bf16 v[144:147], v[24:27], v[252:255], v[144:147]\n"
      "v_rcp_f32 v213, v212\n"
      "v_mfma_f32_16x16x32_bf16 v[28:31], v[192:195], v[108:111], v[28:31]\n"
      "ds_bpermute_b32 v214, v4, v213\n"
      "v_mfma_f32_16x16x32_bf16 v[28:31], v[196:199], v[112:115], v[28:31]\n"
      "v_exp_f32 v201, v201\n"
      "v_mfma_f32_16x16x32_bf16 v[28:31], v[176:179], v[52:55], v[28:31]\n"
      "buffer_store_dwordx2 v[188:189], v3, s[44:47], s79 offen offset:0 nt sc1\n"
      "ds_read_b128 v[72:75], v0 offset:5632\n"
      "v_mfma_f32_16x16x32_bf16 v[32:35], v[192:195], v[180:183], v[32:35]\n"
      "ds_read_b128 v[156:159], v0 offset:4608\n"
      "ds_read_b128 v[88:91], v0 offset:5120\n"
      "v_mfma_f32_16x16x32_bf16 v[32:35], v[196:199], v[184:187], v[32:35]\n"
      "v_exp_f32 v202, v202\n"
      "v_mfma_f32_16x16x32_bf16 v[32:35], v[176:179], v[120:123], v[32:35]\n"
      "v_exp_f32 v147, v147\n"
      "v_mfma_f32_16x16x32_bf16 v[40:43], v[192:195], v[232:235], v[40:43]\n"
      "v_exp_f32 v131, v131\n"
      "v_mfma_f32_16x16x32_bf16 v[40:43], v[196:199], v[236:239], v[40:43]\n"
      "v_exp_f32 v129, v129\n"
      "v_mfma_f32_16x16x32_bf16 v[40:43], v[176:179], v[228:231], v[40:43]\n"
      "v_exp_f32 v150, v150\n"
      "v_mfma_f32_16x16x32_bf16 v[36:39], v[192:195], v[56:59], v[36:39]\n"
      "v_exp_f32 v128, v128\n"
      "v_mfma_f32_16x16x32_bf16 v[36:39], v[196:199], v[60:63], v[36:39]\n"
      "v_cvt_pk_bf16_f32 v248, v128, v129\n"
      "v_cvt_pk_bf16_f32 v249, v130, v131\n"
      "v_mfma_f32_16x16x32_bf16 v[36:39], v[176:179], v[116:119], v[36:39]\n"
      "s_waitcnt lgkmcnt(3)\n"
      "v_mul_f32_dpp v34, v214, v34 row_newbcast:8 row_mask:0xf bank_mask:0xf\n"
      "v_mul_f32_dpp v28, v214, v28 row_newbcast:0 row_mask:0xf bank_mask:0xf\n"
      "v_mfma_f32_16x16x32_bf16 v[176:179], a[160:163], v[100:103], 0\n"
      "v_mul_f32_dpp v32, v214, v32 row_newbcast:0 row_mask:0xf bank_mask:0xf\n"
      "s_waitcnt lgkmcnt(1)\n"
      "v_mfma_f32_16x16x32_bf16 v[176:179], a[164:167], v[156:159], v[176:179]\n"
      "s_waitcnt lgkmcnt(0)\n"
      "buffer_load_dwordx4 v1, s[40:43], s92 offen offset:2048 lds\n"
      "v_mfma_f32_16x16x32_bf16 v[176:179], a[168:171], v[88:91], v[176:179]\n"
      "v_exp_f32 v246, v246\n"
      "v_mfma_f32_16x16x32_bf16 v[176:179], a[172:175], v[72:75], v[176:179]\n"
      "v_cvt_pk_bf16_f32 v250, v150, v151\n"
      "v_mul_f32_dpp v38, v214, v38 row_newbcast:8 row_mask:0xf bank_mask:0xf\n"
      "v_mfma_f32_16x16x32_bf16 v[128:131], a[176:179], v[100:103], 0\n"
      "v_mul_f32_dpp v42, v214, v42 row_newbcast:8 row_mask:0xf bank_mask:0xf\n"
      "v_mul_f32_dpp v29, v214, v29 row_newbcast:4 row_mask:0xf bank_mask:0xf\n"
      "v_mfma_f32_16x16x32_bf16 v[128:131], a[180:183], v[156:159], v[128:131]\n"
      "v_mul_f32_dpp v40, v214, v40 row_newbcast:0 row_mask:0xf bank_mask:0xf\n"
      "v_mul_f32_dpp v35, v214, v35 row_newbcast:12 row_mask:0xf bank_mask:0xf\n"
      "v_mfma_f32_16x16x32_bf16 v[128:131], a[184:187], v[88:91], v[128:131]\n"
      "L_h2_%=:\n"
      "s_cbranch_execz L_hr_%=\n"
      "v_exp_f32 v172, v172\n"
      "v_mfma_f32_16x16x32_bf16 v[128:131], a[188:191], v[72:75], v[128:131]\n"
      "v_pk_max_i16 v95, v95, 0\n"
      "v_mul_f32_dpp v36, v214, v36 row_newbcast:0 row_mask:0xf bank_mask:0xf\n"
      "v_mfma_f32_16x16x32_bf16 v[196:199], a[144:147], v[100:103], 0\n"
      "v_mul_f32_dpp v33, v214, v33 row_newbcast:4 row_mask:0xf bank_mask:0xf\n"
      "v_maximum3_f32 v8, v8, v212, v213\n"
      "v_mfma_f32_16x16x32_bf16 v[196:199], a[148:151], v[156:159], v[196:199]\n"
      "v_pk_max_i16 v98, v98, 0\n"
      "v_mul_f32_dpp v39, v214, v39 row_newbcast:12 row_mask:0xf bank_mask:0xf\n"
      "v_mfma_f32_16x16x32_bf16 v[196:199], a[152:155], v[88:91], v[196:199]\n"
      "v_mul_f32_dpp v43, v214, v43 row_newbcast:12 row_mask:0xf bank_mask:0xf\n"
      "v_mul_f32_dpp v37, v214, v37 row_newbcast:4 row_mask:0xf bank_mask:0xf\n"
      "v_mfma_f32_16x16x32_bf16 v[196:199], a[156:159], v[72:75], v[196:199]\n"
      "v_mul_f32_dpp v31, v214, v31 row_newbcast:12 row_mask:0xf bank_mask:0xf\n"
      "v_mul_f32_dpp v30, v214, v30 row_newbcast:8 row_mask:0xf bank_mask:0xf\n"
      "v_mfma_f32_16x16x32_bf16 v[32:35], v[48:51], a[208:211], v[32:35]\n"
      "v_exp_f32 v247, v247\n"
      "v_mfma_f32_16x16x32_bf16 v[32:35], v[216:219], a[212:215], v[32:35]\n"
      "v_exp_f32 v174, v174\n"
      "v_mfma_f32_16x16x32_bf16 v[32:35], v[132:135], a[216:219], v[32:35]\n"
      "v_exp_f32 v200, v200\n"
      "v_mfma_f32_16x16x32_bf16 v[32:35], v[224:227], a[220:223], v[32:35]\n"
      "v_exp_f32 v152, v152\n"
      "v_mfma_f32_16x16x32_bf16 v[28:31], v[48:51], a[192:195], v[28:31]\n"
      "buffer_load_dwordx4 v1, s[40:43], s93 offen offset:1024 lds\n"
      "v_mfma_f32_16x16x32_bf16 v[28:31], v[216:219], a[196:199], v[28:31]\n"
      "v_exp_f32 v146, v146\n"
      "v_mfma_f32_16x16x32_bf16 v[28:31], v[132:135], a[200:203], v[28:31]\n"
      "v_mul_f32_dpp v41, v214, v41 row_newbcast:4 row_mask:0xf bank_mask:0xf\n"
      "v_cvt_pk_bf16_f32 v215, v198, v199\n"
      "v_mfma_f32_16x16x32_bf16 v[28:31], v[224:227], a[204:207], v[28:31]\n"
      "buffer_store_dwordx2 v[94:95], v2, s[44:47], s75 offen offset:512 nt sc1\n"
      "v_cvt_pk_bf16_f32 v214, v196, v197\n"
      "v_mfma_f32_16x16x32_bf16 v[40:43], v[48:51], a[240:243], v[40:43]\n"
      "v_exp_f32 v244, v244\n"
      "v_mfma_f32_16x16x32_bf16 v[40:43], v[216:219], a[244:247], v[40:43]\n"
      "v_exp_f32 v173, v173\n"
      "v_mfma_f32_16x16x32_bf16 v[40:43], v[132:135], a[248:251], v[40:43]\n"
      "v_cvt_pk_bf16_f32 v254, v200, v201\n"
      "v_cvt_pk_bf16_f32 v30, v30, v34\n"
      "v_mfma_f32_16x16x32_bf16 v[40:43], v[224:227], a[252:255], v[40:43]\n"
      "v_exp_f32 v144, v144\n"
      "v_mfma_f32_16x16x32_bf16 v[36:39], v[48:51], a[224:227], v[36:39]\n"
      "v_exp_f32 v175, v175\n"
      "v_mfma_f32_16x16x32_bf16 v[36:39], v[216:219], a[228:231], v[36:39]\n"
      "v_exp_f32 v145, v145\n"
      "v_mfma_f32_16x16x32_bf16 v[36:39], v[132:135], a[232:235], v[36:39]\n"
      "v_exp_f32 v153, v153\n"
      "v_mfma_f32_16x16x32_bf16 v[36:39], v[224:227], a[236:239], v[36:39]\n"
      "v_cvt_pk_bf16_f32 v251, v152, v153\n"
      "v_pk_max_i16 v99, v99, 0\n"
      "v_mfma_f32_16x16x32_bf16 v[224:227], a[128:131], v[100:103], 0\n"
      "v_cvt_pk_bf16_f32 v51, v174, v175\n"
      "v_cvt_pk_bf16_f32 v255, v202, v203\n"
      "v_mfma_f32_16x16x32_bf16 v[224:227], a[132:135], v[156:159], v[224:227]\n"
      "ds_read_b128 v[104:107], v0 offset:512\n"
      "v_cvt_pk_bf16_f32 v253, v146, v147\n"
      "v_mfma_f32_16x16x32_bf16 v[224:227], a[136:139], v[88:91], v[224:227]\n"
      "v_pk_max_i16 v93, v93, 0\n"
      "v_cvt_pk_bf16_f32 v252, v144, v145\n"
      "v_mfma_f32_16x16x32_bf16 v[224:227], a[140:143], v[72:75], v[224:227]\n"
      "v_exp_f32 v245, v245\n"
      "v_smfmac_f32_16x16x64_bf16 v[148:151], v[12:15], v[248:255], v5\n"
      "buffer_store_dwordx2 v[92:93], v2, s[44:47], s75 offen offset:0 nt sc1\n"
      "v_cvt_pk_bf16_f32 v50, v172, v173\n"
      "v_mfma_f32_16x16x4_4b_bf16 v[188:203], v[154:155], v[18:19], 0 cbsz:2 abid:2\n"
      "v_cvt_pk_bf16_f32 v49, v246, v247\n"
      "v_cvt_pk_bf16_f32 v48, v244, v245\n"
      "v_cvt_pk_bf16_f32 v34, v31, v35\n"
      "v_cvt_pk_bf16_f32 v35, v39, v43\n"
      "v_cvt_pk_bf16_f32 v28, v28, v32\n"
      "v_cvt_pk_bf16_f32 v212, v224, v225\n"
      "v_smfmac_f32_16x16x32_bf16 v[148:151], v[12:13], v[48:51], v5\n"
      "ds_read_b128 v[132:135], v0 offset:1536\n"
      "v_cvt_pk_bf16_f32 v217, v178, v179\n"
      "v_cvt_pk_bf16_f32 v219, v130, v131\n"
      "v_cvt_pk_bf16_f32 v213, v226, v227\n"
      "v_mfma_f32_16x16x32_bf16 v[188:191], v[248:251], v[108:111], v[188:191]\n"
      "v_pk_max_i16 v34, v34, 0\n"
      "v_mov_b64 v[152:153], 0\n"
      "v_mfma_f32_16x16x32_bf16 v[188:191], v[252:255], v[112:115], v[188:191]\n"
      "v_cvt_pk_bf16_f32 v216, v176, v177\n"
      "v_add_f32 v148, v148, v149\n"
      "v_mfma_f32_16x16x32_bf16 v[188:191], v[48:51], v[52:55], v[188:191]\n"
      "v_mov_b64 v[154:155], 0\n"
      "v_cvt_pk_bf16_f32 v218, v128, v129\n"
      "v_mfma_f32_16x16x32_bf16 v[224:227], v[76:79], v[212:215], 0\n"
      "buffer_load_dwordx4 v1, s[40:43], s93 offen offset:3072 lds\n"
      "v_mfma_f32_16x16x32_bf16 v[224:227], v[80:83], v[216:219], v[224:227]\n"
      "v_rcp_f32 v149, v148\n"
      "v_mfma_f32_16x16x32_bf16 v[176:179], v[20:23], v[212:215], 0\n"
      "ds_read_b128 v[172:175], v0 offset:1024\n"
      "v_cvt_pk_bf16_f32 v31, v38, v42\n"
      "v_mfma_f32_16x16x32_bf16 v[176:179], v[24:27], v[216:219], v[176:179]\n"
      "v_cvt_pk_bf16_f32 v32, v29, v33\n"
      "v_cvt_pk_bf16_f32 v33, v37, v41\n"
      "v_smfmac_f32_16x16x64_bf16 v[152:155], v[220:223], v[212:219], v5\n"
      "ds_read_b128 v[144:147], v0 offset:0\n"
      "v_cvt_pk_bf16_f32 v29, v36, v40\n"
      "v_mfma_f32_16x16x32_bf16 v[192:195], v[248:251], v[180:183], v[192:195]\n"
      "ds_bpermute_b32 v150, v4, v149 offset:128\n"
      "v_mfma_f32_16x16x32_bf16 v[192:195], v[252:255], v[184:187], v[192:195]\n"
      "v_exp_f32 v179, v179\n"
      "v_mfma_f32_16x16x32_bf16 v[192:195], v[48:51], v[120:123], v[192:195]\n"
      "v_exp_f32 v225, v225\n"
      "v_mfma_f32_16x16x32_bf16 v[92:95], v[204:207], v[212:215], 0\n"
      "v_exp_f32 v224, v224\n"
      "v_mfma_f32_16x16x32_bf16 v[92:95], v[208:211], v[216:219], v[92:95]\n"
      "v_exp_f32 v226, v226\n"
      "v_mfma_f32_16x16x32_bf16 v[128:131], v[64:67], v[212:215], 0\n"
      "v_exp_f32 v176, v176\n"
      "v_mfma_f32_16x16x32_bf16 v[128:131], v[68:71], v[216:219], v[128:131]\n"
      "buffer_load_dwordx4 v1, s[40:43], s92 offen offset:0 lds\n"
      "v_mfma_f32_16x16x32_bf16 v[196:199], v[248:251], v[56:59], v[196:199]\n"
      "v_exp_f32 v177, v177\n"
      "v_mfma_f32_16x16x32_bf16 v[196:199], v[252:255], v[60:63], v[196:199]\n"
      "v_exp_f32 v178, v178\n"
      "v_mfma_f32_16x16x32_bf16 v[196:199], v[48:51], v[116:119], v[196:199]\n"
      "v_exp_f32 v227, v227\n"
      "v_mfma_f32_16x16x32_bf16 v[200:203], v[248:251], v[232:235], v[200:203]\n"
      "v_cvt_pk_bf16_f32 v221, v178, v179\n"
      "v_cvt_pk_bf16_f32 v220, v176, v177\n"
      "v_mfma_f32_16x16x32_bf16 v[200:203], v[252:255], v[236:239], v[200:203]\n"
      "v_exp_f32 v93, v93\n"
      "v_mfma_f32_16x16x32_bf16 v[200:203], v[48:51], v[228:231], v[200:203]\n"
      "s_waitcnt lgkmcnt(0)\n"
      "v_mul_f32_dpp v196, v150, v196 row_newbcast:0 row_mask:0xf bank_mask:0xf\n"
      "v_mul_f32_dpp v191, v150, v191 row_newbcast:12 row_mask:0xf bank_mask:0xf\n"
      "v_mfma_f32_16x16x32_bf16 v[36:39], v[164:167], v[212:215], 0\n"
      "v_mul_f32_dpp v192, v150, v192 row_newbcast:0 row_mask:0xf bank_mask:0xf\n"
      "v_mul_f32_dpp v188, v150, v188 row_newbcast:0 row_mask:0xf bank_mask:0xf\n"
      "v_mfma_f32_16x16x32_bf16 v[36:39], v[168:171], v[216:219], v[36:39]\n"
      "s_nop 0\n"
      "v_mul_f32_dpp v201, v150, v201 row_newbcast:4 row_mask:0xf bank_mask:0xf\n"
      "v_mul_f32_dpp v194, v150, v194 row_newbcast:8 row_mask:0xf bank_mask:0xf\n"
      "v_mfma_f32_16x16x32_bf16 v[176:179], v[136:139], v[212:215], 0\n"
      "v_mul_f32_dpp v200, v150, v200 row_newbcast:0 row_mask:0xf bank_mask:0xf\n"
      "v_mfma_f32_16x16x32_bf16 v[176:179], v[140:143], v[216:219], v[176:179]\n"
      "v_exp_f32 v92, v92\n"
      "v_mfma_f32_16x16x32_bf16 v[216:219], a[160:163], v[144:147], 0\n"
      "v_cvt_pk_bf16_f32 v222, v224, v225\n"
      "v_mul_f32_dpp v197, v150, v197 row_newbcast:4 row_mask:0xf bank_mask:0xf\n"
      "v_mfma_f32_16x16x32_bf16 v[216:219], a[164:167], v[104:107], v[216:219]\n"
      "v_exp_f32 v37, v37\n"
      "v_mfma_f32_16x16x32_bf16 v[216:219], a[168:171], v[172:175], v[216:219]\n"
      "v_exp_f32 v95, v95\n"
      "v_mfma_f32_16x16x32_bf16 v[216:219], a[172:175], v[132:135], v[216:219]\n"
      "v_exp_f32 v129, v129\n"
      "v_mfma_f32_16x16x32_bf16 v[252:255], a[144:147], v[144:147], 0\n"
      "v_exp_f32 v176, v176\n"
      "v_mfma_f32_16x16x32_bf16 v[252:255], a[148:151], v[104:107], v[252:255]\n"
      "v_exp_f32 v94, v94\n"
      "v_mfma_f32_16x16x32_bf16 v[252:255], a[152:155], v[172:175], v[252:255]\n"
      "v_exp_f32 v38, v38\n"
      "v_mfma_f32_16x16x32_bf16 v[252:255], a[156:159], v[132:135], v[252:255]\n"
      "v_cvt_pk_bf16_f32 v223, v226, v227\n"
      "v_cvt_pk_bf16_f32 v251, v218, v219\n"
      "v_mfma_f32_16x16x32_bf16 v[48:51], a[176:179], v[144:147], 0\n"
      "v_exp_f32 v39, v39\n"
      "v_mfma_f32_16x16x32_bf16 v[48:51], a[180:183], v[104:107], v[48:51]\n"
      "v_cvt_pk_bf16_f32 v250, v216, v217\n"
      "v_mul_f32_dpp v198, v150, v198 row_newbcast:8 row_mask:0xf bank_mask:0xf\n"
      "v_mfma_f32_16x16x32_bf16 v[48:51], a[184:187], v[172:175], v[48:51]\n"
      "v_cvt_pk_bf16_f32 v248, v252, v253\n"
      "v_mul_f32_dpp v190, v150, v190 row_newbcast:8 row_mask:0xf bank_mask:0xf\n"
      "v_mfma_f32_16x16x32_bf16 v[48:51], a[188:191], v[132:135], v[48:51]\n"
      "v_mul_f32_dpp v189, v150, v189 row_newbcast:4 row_mask:0xf bank_mask:0xf\n"
      "v_cvt_pk_bf16_f32 v249, v254, v255\n"
      "v_mfma_f32_16x16x32_bf16 v[224:227], a[128:131], v[144:147], 0\n"
      "v_exp_f32 v131, v131\n"
      "v_mfma_f32_16x16x32_bf16 v[224:227], a[132:135], v[104:107], v[224:227]\n"
      "s_add_u32 m0, s53, 0\n"
      "v_exp_f32 v36, v36\n"
      "v_mfma_f32_16x16x32_bf16 v[224:227], a[136:139], v[172:175], v[224:227]\n"
      "v_cvt_pk_bf16_f32 v252, v48, v49\n"
      "v_cvt_pk_bf16_f32 v253, v50, v51\n"
      "v_mfma_f32_16x16x32_bf16 v[224:227], a[140:143], v[132:135], v[224:227]\n"
      "v_exp_f32 v177, v177\n"
      "v_mfma_f32_16x16x32_bf16 v[188:191], v[160:163], a[192:195], v[188:191]\n"
      "v_mul_f32_dpp v199, v150, v199 row_newbcast:12 row_mask:0xf bank_mask:0xf\n"
      "v_mul_f32_dpp v203, v150, v203 row_newbcast:12 row_mask:0xf bank_mask:0xf\n"
      "v_mfma_f32_16x16x32_bf16 v[188:191], v[84:87], a[196:199], v[188:191]\n"
      "v_cvt_pk_bf16_f32 v217, v38, v39\n"
      "v_cvt_pk_bf16_f32 v218, v92, v93\n"
      "v_mfma_f32_16x16x32_bf16 v[188:191], v[240:243], a[200:203], v[188:191]\n"
      "v_cvt_pk_bf16_f32 v247, v226, v227\n"
      "v_cvt_pk_bf16_f32 v246, v224, v225\n"
      "v_mfma_f32_16x16x32_bf16 v[188:191], v[124:127], a[204:207], v[188:191]\n"
      "v_mul_f32_dpp v195, v150, v195 row_newbcast:12 row_mask:0xf bank_mask:0xf\n"
      "v_mul_f32_dpp v193, v150, v193 row_newbcast:4 row_mask:0xf bank_mask:0xf\n"
      "v_smfmac_f32_16x16x64_bf16 v[152:155], v[44:47], v[246:253], v5\n"
      "buffer_load_dwordx4 v1, s[40:43], s95 offen offset:3072 lds\n"
      "v_mfma_f32_16x16x32_bf16 v[196:199], v[160:163], a[224:227], v[196:199]\n"
      "v_mul_f32_dpp v202, v150, v202 row_newbcast:8 row_mask:0xf bank_mask:0xf\n"
      "v_cvt_pk_bf16_f32 v219, v94, v95\n"
      "v_mfma_f32_16x16x32_bf16 v[196:199], v[84:87], a[228:231], v[196:199]\n"
      "v_exp_f32 v128, v128\n"
      "v_mfma_f32_16x16x32_bf16 v[196:199], v[240:243], a[232:235], v[196:199]\n"
      "v_cvt_pk_bf16_f32 v224, v128, v129\n"
      "v_add_f32 v152, v152, v153\n"
      "v_mfma_f32_16x16x32_bf16 v[196:199], v[124:127], a[236:239], v[196:199]\n"
      "v_add_f32 v154, v154, v155\n"
      "v_cvt_pk_bf16_f32 v216, v36, v37\n"
      "v_mfma_f32_16x16x32_bf16 v[192:195], v[160:163], a[208:211], v[192:195]\n"
      "v_exp_f32 v152, v152\n"
      "v_mfma_f32_16x16x32_bf16 v[192:195], v[84:87], a[212:215], v[192:195]\n"
      "v_exp_f32 v179, v179\n"
      "v_mfma_f32_16x16x32_bf16 v[192:195], v[240:243], a[216:219], v[192:195]\n"
      "v_exp_f32 v178, v178\n"
      "v_mfma_f32_16x16x32_bf16 v[192:195], v[124:127], a[220:223], v[192:195]\n"
      "v_exp_f32 v154, v154\n"
      "v_mfma_f32_16x16x32_bf16 v[200:203], v[160:163], a[240:243], v[200:203]\n"
      "v_cvt_pk_bf16_f32 v94, v152, v154\n"
      "v_add_f32 v160, v152, v154\n"
      "v_mfma_f32_16x16x32_bf16 v[200:203], v[84:87], a[244:247], v[200:203]\n"
      "v_add_f32 v161, v152, v154\n"
      "v_cvt_pk_bf16_f32 v95, v152, v154\n"
      "v_mfma_f32_16x16x32_bf16 v[200:203], v[240:243], a[248:251], v[200:203]\n"
      "s_nop 0\n"
      "v_permlane16_swap_b32 v94, v95\n"
      "v_mfma_f32_16x16x32_bf16 v[200:203], v[124:127], a[252:255], v[200:203]\n"
      "v_permlane16_swap_b32 v160, v161\n"
      "v_mov_b64 v[128:129], v[160:161]\n"
      "v_pk_max_i16 v32, v32, 0\n"
      "v_smfmac_f32_16x16x64_bf16 v[160:163], v[12:15], v[216:223], v5\n"
      "buffer_store_dwordx2 v[98:99], v2, s[44:47], s75 offen offset:768 nt sc1\n"
      "v_cvt_pk_bf16_f32 v188, v188, v192\n"
      "v_mfma_f32_16x16x4_4b_bf16 v[36:51], v[94:95], v[18:19], 0 cbsz:2 abid:0\n"
      "v_cvt_pk_bf16_f32 v227, v178, v179\n"
      "v_exp_f32 v130, v130\n"
      "v_cvt_pk_bf16_f32 v226, v176, v177\n"
      "v_cvt_pk_bf16_f32 v225, v130, v131\n"
      "v_pk_max_i16 v29, v29, 0\n"
      "s_nop 0\n"
      "v_smfmac_f32_16x16x32_bf16 v[160:163], v[12:13], v[224:227], v5\n"
      "buffer_load_dwordx4 v1, s[40:43], s94 offen offset:2048 lds\n"
      "v_pk_max_i16 v28, v28, 0\n"
      "s_nop 0\n"
      "v_mfma_f32_16x16x32_bf16 v[40:43], v[216:219], v[180:183], v[40:43]\n"
      "v_pk_max_i16 v97, v97, 0\n"
      "v_cvt_pk_bf16_f32 v192, v189, v193\n"
      "v_mfma_f32_16x16x32_bf16 v[40:43], v[220:223], v[184:187], v[40:43]\n"
      "s_not_b64 vcc, vcc\n"
      "v_pk_max_i16 v96, v96, 0\n"
      "v_add_f32 v160, v160, v161\n"
      "v_mfma_f32_16x16x32_bf16 v[40:43], v[224:227], v[120:123], v[40:43]\n"
      "v_rcp_f32 v161, v160\n"
      "v_mfma_f32_16x16x32_bf16 v[36:39], v[216:219], v[108:111], v[36:39]\n"
      "ds_bpermute_b32 v162, v4, v161\n"
      "v_mfma_f32_16x16x32_bf16 v[36:39], v[220:223], v[112:115], v[36:39]\n"
      "v_maximum3_f32 v8, v8, v148, v149\n"
      "v_mfma_f32_16x16x32_bf16 v[36:39], v[224:227], v[52:55], v[36:39]\n"
      "v_pk_max_i16 v188, v188, 0\n"
      "v_mfma_f32_16x16x32_bf16 v[48:51], v[216:219], v[232:235], v[48:51]\n"
      "v_mfma_f32_16x16x32_bf16 v[48:51], v[220:223], v[236:239], v[48:51]\n"
      "v_cvt_pk_bf16_f32 v190, v190, v194\n"
      "v_cvt_pk_bf16_f32 v194, v191, v195\n"
      "v_mfma_f32_16x16x32_bf16 v[48:51], v[224:227], v[228:231], v[48:51]\n"
      "buffer_store_dwordx2 v[96:97], v2, s[44:47], s75 offen offset:256 nt sc1\n"
      "v_cvt_pk_bf16_f32 v195, v199, v203\n"
      "v_mfma_f32_16x16x32_bf16 v[44:47], v[216:219], v[56:59], v[44:47]\n"
      "buffer_load_dwordx4 v1, s[40:43], s95 offen offset:1024 lds\n"
      "v_mfma_f32_16x16x32_bf16 v[44:47], v[220:223], v[60:63], v[44:47]\n"
      "v_cvt_pk_bf16_f32 v189, v196, v200\n"
      "v_maximum3_f32 v8, v8, v160, v161\n"
      "v_mfma_f32_16x16x32_bf16 v[44:47], v[224:227], v[116:119], v[44:47]\n"
      "v_cvt_pk_bf16_f32 v193, v197, v201\n"
      "v_cvt_pk_bf16_f32 v191, v198, v202\n"
      "v_mfma_f32_16x16x4_4b_bf16 v[212:227], v[94:95], v[18:19], 0 cbsz:2 abid:2\n"
      "s_waitcnt lgkmcnt(0)\n"
      "v_mul_f32_dpp v38, v162, v38 row_newbcast:8 row_mask:0xf bank_mask:0xf\n"
      "v_mul_f32_dpp v39, v162, v39 row_newbcast:12 row_mask:0xf bank_mask:0xf\n"
      "v_mul_f32_dpp v37, v162, v37 row_newbcast:4 row_mask:0xf bank_mask:0xf\n"
      "v_mul_f32_dpp v36, v162, v36 row_newbcast:0 row_mask:0xf bank_mask:0xf\n"
      "v_pk_max_i16 v192, v192, 0\n"
      "v_mul_f32_dpp v46, v162, v46 row_newbcast:8 row_mask:0xf bank_mask:0xf\n"
      "v_mul_f32_dpp v47, v162, v47 row_newbcast:12 row_mask:0xf bank_mask:0xf\n"
      "v_mul_f32_dpp v41, v162, v41 row_newbcast:4 row_mask:0xf bank_mask:0xf\n"
      "v_mfma_f32_16x16x32_bf16 v[36:39], v[100:103], a[192:195], v[36:39]\n"
      "v_mul_f32_dpp v45, v162, v45 row_newbcast:4 row_mask:0xf bank_mask:0xf\n"
      "v_mul_f32_dpp v50, v162, v50 row_newbcast:8 row_mask:0xf bank_mask:0xf\n"
      "v_mfma_f32_16x16x32_bf16 v[36:39], v[156:159], a[196:199], v[36:39]\n"
      "v_mul_f32_dpp v49, v162, v49 row_newbcast:4 row_mask:0xf bank_mask:0xf\n"
      "v_mul_f32_dpp v44, v162, v44 row_newbcast:0 row_mask:0xf bank_mask:0xf\n"
      "v_mfma_f32_16x16x32_bf16 v[36:39], v[88:91], a[200:203], v[36:39]\n"
      "v_mul_f32_dpp v51, v162, v51 row_newbcast:12 row_mask:0xf bank_mask:0xf\n"
      "v_mul_f32_dpp v48, v162, v48 row_newbcast:0 row_mask:0xf bank_mask:0xf\n"
      "v_mfma_f32_16x16x32_bf16 v[36:39], v[72:75], a[204:207], v[36:39]\n"
      "v_pk_max_i16 v193, v193, 0\n"
      "v_pk_max_i16 v190, v190, 0\n"
      "v_mfma_f32_16x16x32_bf16 v[48:51], v[100:103], a[240:243], v[48:51]\n"
      "v_mfma_f32_16x16x32_bf16 v[48:51], v[156:159], a[244:247], v[48:51]\n"
      "v_mul_f32_dpp v43, v162, v43 row_newbcast:12 row_mask:0xf bank_mask:0xf\n"
      "v_pk_max_i16 v35, v35, 0\n"
      "v_mfma_f32_16x16x32_bf16 v[48:51], v[88:91], a[248:251], v[48:51]\n"
      "v_pk_max_i16 v30, v30, 0\n"
      "v_pk_max_i16 v195, v195, 0\n"
      "v_mfma_f32_16x16x32_bf16 v[48:51], v[72:75], a[252:255], v[48:51]\n"
      "v_pk_max_i16 v189, v189, 0\n"
      "v_add_u32 v9, 64, v9\n"
      "v_mfma_f32_16x16x32_bf16 v[44:47], v[100:103], a[224:227], v[44:47]\n"
      "v_mul_f32_dpp v42, v162, v42 row_newbcast:8 row_mask:0xf bank_mask:0xf\n"
      "v_mul_f32_dpp v40, v162, v40 row_newbcast:0 row_mask:0xf bank_mask:0xf\n"
      "v_mfma_f32_16x16x32_bf16 v[44:47], v[156:159], a[228:231], v[44:47]\n"
      "v_pk_max_i16 v31, v31, 0\n"
      "v_mfma_f32_16x16x32_bf16 v[44:47], v[88:91], a[232:235], v[44:47]\n"
      "s_add_u32 s44, s44, 13107200\n s_addc_u32 s45, s45, 0\n"
      "v_mfma_f32_16x16x32_bf16 v[44:47], v[72:75], a[236:239], v[44:47]\n"
      "v_pk_max_i16 v33, v33, 0\n"
      "v_pk_max_i16 v191, v191, 0\n"
      "v_mfma_f32_16x16x32_bf16 v[40:43], v[100:103], a[208:211], v[40:43]\n"
      "v_pk_max_i16 v194, v194, 0\n"
      "v_mfma_f32_16x16x32_bf16 v[40:43], v[156:159], a[212:215], v[40:43]\n"
      "buffer_load_dwordx4 v1, s[40:43], s94 offen offset:0 lds\n"
      "v_mfma_f32_16x16x32_bf16 v[40:43], v[88:91], a[216:219], v[40:43]\n"
      "s_add_u32 s40, s40, 13107200\n s_addc_u32 s41, s41, 0\n"
      "v_mfma_f32_16x16x32_bf16 v[40:43], v[72:75], a[220:223], v[40:43]\n"
      "s_nop 0\n"
      "L_task_%=:\n"
      "v_mfma_f32_16x16x32_bf16 v[240:243], v[136:139], v[246:249], 0\n"
      "s_waitcnt vmcnt(29)\n"
      "ds_read_b128 v[92:95], v0 offset:20480\n"
      "ds_read_b128 v[96:99], v0 offset:21504\n"
      "v_mfma_f32_16x16x32_bf16 v[240:243], v[140:143], v[250:253], v[240:243]\n"
      "buffer_store_dwordx2 v[28:29], v2, s[44:47], s100 offen offset:0 nt sc1\n"
      "ds_read_b128 v[88:91], v0 offset:20992\n"
      "v_mfma_f32_16x16x32_bf16 v[156:159], v[76:79], v[246:249], 0\n"
      "s_waitcnt vmcnt(14)\n"
      "ds_read_b128 v[160:163], v0 offset:8192\n"
      "ds_read_b128 v[200:203], v0 offset:9728\n"
      "v_mfma_f32_16x16x32_bf16 v[156:159], v[80:83], v[250:253], v[156:159]\n"
      "ds_read_b128 v[84:87], v0 offset:9216\n"
      "ds_read_b128 v[148:151], v0 offset:8704\n"
      "v_mfma_f32_16x16x32_bf16 v[100:103], v[64:67], v[246:249], 0\n"
      "ds_read_b128 v[124:127], v0 offset:16384\n"
      "ds_read_b128 v[72:75], v0 offset:17408\n"
      "v_mfma_f32_16x16x32_bf16 v[100:103], v[68:71], v[250:253], v[100:103]\n"
      "s_nop 1\n"
      "v_exp_f32 v158, v158\n"
      "v_mfma_f32_16x16x32_bf16 v[64:67], v[164:167], v[246:249], 0\n"
      "v_cvt_pk_bf16_f32 v36, v36, v40\n"
      "v_mfma_f32_16x16x32_bf16 v[64:67], v[168:171], v[250:253], v[64:67]\n"
      "v_exp_f32 v157, v157\n"
      "v_mfma_f32_16x16x32_bf16 v[168:171], v[204:207], v[246:249], 0\n"
      "v_exp_f32 v103, v103\n"
      "v_mfma_f32_16x16x32_bf16 v[168:171], v[208:211], v[250:253], v[168:171]\n"
      "v_exp_f32 v102, v102\n"
      "v_mfma_f32_16x16x32_bf16 v[152:155], v[20:23], v[246:249], 0\n"
      "v_exp_f32 v242, v242\n"
      "v_mfma_f32_16x16x32_bf16 v[152:155], v[24:27], v[250:253], v[152:155]\n"
      "s_waitcnt lgkmcnt(5)\n"
      "v_mfma_f32_16x16x32_bf16 v[80:83], a[32:35], v[160:163], 0\n"
      "s_waitcnt lgkmcnt(2)\n"
      "v_mfma_f32_16x16x32_bf16 v[80:83], a[36:39], v[148:151], v[80:83]\n"
      "v_mfma_f32_16x16x32_bf16 v[80:83], a[40:43], v[84:87], v[80:83]\n"
      "v_cvt_pk_bf16_f32 v40, v37, v41\n"
      "v_mfma_f32_16x16x32_bf16 v[80:83], a[44:47], v[200:203], v[80:83]\n"
      "v_exp_f32 v159, v159\n"
      "v_mfma_f32_16x16x32_bf16 v[140:143], a[48:51], v[160:163], 0\n"
      "v_exp_f32 v153, v153\n"
      "v_mfma_f32_16x16x32_bf16 v[140:143], a[52:55], v[148:151], v[140:143]\n"
      "v_cvt_pk_bf16_f32 v211, v158, v159\n"
      "v_mfma_f32_16x16x32_bf16 v[140:143], a[56:59], v[84:87], v[140:143]\n"
      "v_exp_f32 v101, v101\n"
      "v_mfma_f32_16x16x32_bf16 v[140:143], a[60:63], v[200:203], v[140:143]\n"
      "ds_read_b128 v[176:179], v0 offset:22016\n"
      "v_mfma_f32_16x16x32_bf16 v[164:167], v[160:163], a[80:83], 0\n"
      "v_exp_f32 v243, v243\n"
      "v_mfma_f32_16x16x32_bf16 v[164:167], v[148:151], a[84:87], v[164:167]\n"
      "v_exp_f32 v66, v66\n"
      "v_mfma_f32_16x16x32_bf16 v[164:167], v[84:87], a[88:91], v[164:167]\n"
      "v_mfma_f32_16x16x32_bf16 v[164:167], v[200:203], a[92:95], v[164:167]\n"
      "buffer_store_dwordx2 v[190:191], v2, s[44:47], s101 offen offset:512 nt sc1\n"
      "v_mfma_f32_16x16x32_bf16 v[76:79], v[160:163], a[64:67], 0\n"
      "ds_read_b128 v[196:199], v0 offset:17920\n"
      "v_mfma_f32_16x16x32_bf16 v[76:79], v[148:151], a[68:71], v[76:79]\n"
      "v_mfma_f32_16x16x32_bf16 v[76:79], v[84:87], a[72:75], v[76:79]\n"
      "v_mfma_f32_16x16x32_bf16 v[76:79], v[200:203], a[76:79], v[76:79]\n"
      "s_add_u32 m0, s53, 20480\n"
      "v_exp_f32 v64, v64\n"
      "v_mfma_f32_16x16x32_bf16 v[136:139], v[160:163], a[112:115], 0\n"
      "v_exp_f32 v156, v156\n"
      "v_mfma_f32_16x16x32_bf16 v[136:139], v[148:151], a[116:119], v[136:139]\n"
      "v_cvt_pk_bf16_f32 v210, v156, v157\n"
      "v_mfma_f32_16x16x32_bf16 v[136:139], v[84:87], a[120:123], v[136:139]\n"
      "v_cvt_pk_bf16_f32 v38, v38, v42\n"
      "v_mfma_f32_16x16x32_bf16 v[136:139], v[200:203], a[124:127], v[136:139]\n"
      "v_exp_f32 v154, v154\n"
      "v_mfma_f32_16x16x32_bf16 v[68:71], a[0:3], v[160:163], 0\n"
      "v_exp_f32 v241, v241\n"
      "v_mfma_f32_16x16x32_bf16 v[68:71], a[4:7], v[148:151], v[68:71]\n"
      "v_exp_f32 v152, v152\n"
      "v_mfma_f32_16x16x32_bf16 v[68:71], a[8:11], v[84:87], v[68:71]\n"
      "v_exp_f32 v240, v240\n"
      "v_mfma_f32_16x16x32_bf16 v[68:71], a[12:15], v[200:203], v[68:71]\n"
      "v_cvt_pk_bf16_f32 v41, v45, v49\n"
      "v_pk_max_i16 v41, v41, 0\n"
      "v_mfma_f32_16x16x32_bf16 v[252:255], a[16:19], v[160:163], 0\n"
      "s_waitcnt lgkmcnt(1)\n"
      "buffer_load_dwordx4 v1, s[40:43], s74 offen offset:1024 lds\n"
      "v_mfma_f32_16x16x32_bf16 v[252:255], a[20:23], v[148:151], v[252:255]\n"
      "v_exp_f32 v155, v155\n"
      "v_mfma_f32_16x16x32_bf16 v[252:255], a[24:27], v[84:87], v[252:255]\n"
      "v_exp_f32 v65, v65\n"
      "v_mfma_f32_16x16x32_bf16 v[252:255], a[28:31], v[200:203], v[252:255]\n"
      "v_cvt_pk_bf16_f32 v209, v154, v155\n"
      "v_cvt_pk_bf16_f32 v208, v152, v153\n"
      "v_mfma_f32_16x16x32_bf16 v[152:155], v[160:163], a[96:99], 0\n"
      "v_cvt_pk_bf16_f32 v42, v39, v43\n"
      "v_cvt_pk_bf16_f32 v19, v102, v103\n"
      "v_mfma_f32_16x16x32_bf16 v[152:155], v[148:151], a[100:103], v[152:155]\n"
      "v_exp_f32 v100, v100\n"
      "v_mfma_f32_16x16x32_bf16 v[152:155], v[84:87], a[104:107], v[152:155]\n"
      "ds_read2_b32 v[160:161], v16 offset0:0 offset1:2\n"
      "v_mfma_f32_16x16x32_bf16 v[152:155], v[200:203], a[108:111], v[152:155]\n"
      "buffer_store_dwordx2 v[32:33], v2, s[44:47], s100 offen offset:256 nt sc1\n"
      "v_cvt_pk_bf16_f32 v25, v82, v83\n"
      "v_mfma_f32_16x16x32_bf16 v[148:151], a[176:179], v[92:95], 0\n"
      "v_exp_f32 v168, v168\n"
      "v_mfma_f32_16x16x32_bf16 v[148:151], a[180:183], v[88:91], v[148:151]\n"
      "v_cvt_pk_bf16_f32 v21, v242, v243\n"
      "v_mfma_f32_16x16x32_bf16 v[148:151], a[184:187], v[96:99], v[148:151]\n"
      "v_exp_f32 v170, v170\n"
      "v_mfma_f32_16x16x32_bf16 v[148:151], a[188:191], v[176:179], v[148:151]\n"
      "v_exp_f32 v67, v67\n"
      "v_mfma_f32_16x16x32_bf16 v[200:203], a[160:163], v[92:95], 0\n"
      "v_mfma_f32_16x16x32_bf16 v[200:203], a[164:167], v[88:91], v[200:203]\n"
      "v_exp_f32 v171, v171\n"
      "v_mfma_f32_16x16x32_bf16 v[200:203], a[168:171], v[96:99], v[200:203]\n"
      "v_mfma_f32_16x16x32_bf16 v[200:203], a[172:175], v[176:179], v[200:203]\n"
      "v_cvt_pk_bf16_f32 v205, v66, v67\n"
      "v_cvt_pk_bf16_f32 v204, v64, v65\n"
      "v_mfma_f32_16x16x32_bf16 v[64:67], a[32:35], v[92:95], 0\n"
      "v_mfma_f32_16x16x32_bf16 v[64:67], a[36:39], v[88:91], v[64:67]\n"
      "v_cvt_pk_bf16_f32 v18, v100, v101\n"
      "v_cvt_pk_bf16_f32 v207, v170, v171\n"
      "v_mfma_f32_16x16x32_bf16 v[64:67], a[40:43], v[96:99], v[64:67]\n"
      "v_mfma_f32_16x16x32_bf16 v[64:67], a[44:47], v[176:179], v[64:67]\n"
      "v_exp_f32 v169, v169\n"
      "v_mfma_f32_16x16x32_bf16 v[248:251], a[0:3], v[92:95], 0\n"
      "v_mfma_f32_16x16x32_bf16 v[248:251], a[4:7], v[88:91], v[248:251]\n"
      "v_cvt_pk_bf16_f32 v206, v168, v169\n"
      "v_mfma_f32_16x16x32_bf16 v[248:251], a[8:11], v[96:99], v[248:251]\n"
      "v_cvt_pk_bf16_f32 v244, v200, v201\n"
      "v_mfma_f32_16x16x32_bf16 v[248:251], a[12:15], v[176:179], v[248:251]\n"
      "ds_read_b128 v[100:103], v0 offset:16896\n"
      "v_cvt_pk_bf16_f32 v20, v240, v241\n"
      "v_smfmac_f32_16x16x64_bf16 v[128:131], v[12:15], v[204:211], v5\n"
      "v_mfma_f32_16x16x32_bf16 v[224:227], v[204:207], v[232:235], v[224:227]\n"
      "v_mfma_f32_16x16x32_bf16 v[224:227], v[208:211], v[236:239], v[224:227]\n"
      "v_cvt_pk_bf16_f32 v43, v47, v51\n"
      "v_cvt_pk_bf16_f32 v245, v202, v203\n"
      "v_mfma_f32_16x16x32_bf16 v[224:227], v[18:21], v[228:231], v[224:227]\n"
      "buffer_load_dwordx4 v1, s[40:43], s73 offen offset:2048 lds\n"
      "v_smfmac_f32_16x16x32_bf16 v[128:131], v[12:13], v[18:21], v5\n"
      "buffer_store_dwordx2 v[192:193], v2, s[44:47], s101 offen offset:256 nt sc1\n"
      "v_mfma_f32_16x16x32_bf16 v[232:235], a[16:19], v[92:95], 0\n"
      "v_mfma_f32_16x16x32_bf16 v[232:235], a[20:23], v[88:91], v[232:235]\n"
      "ds_read_b128 v[168:171], v9 offset:2048\n"
      "v_mfma_f32_16x16x32_bf16 v[232:235], a[24:27], v[96:99], v[232:235]\n"
      "ds_read2_b32 v[162:163], v16 offset0:128 offset1:130\n"
      "v_mfma_f32_16x16x32_bf16 v[232:235], a[28:31], v[176:179], v[232:235]\n"
      "v_cvt_pk_bf16_f32 v37, v44, v48\n"
      "v_cvt_pk_bf16_f32 v24, v80, v81\n"
      "v_mfma_f32_16x16x32_bf16 v[220:223], v[204:207], v[56:59], v[220:223]\n"
      "v_mfma_f32_16x16x32_bf16 v[220:223], v[208:211], v[60:63], v[220:223]\n"
      "s_waitcnt vmcnt(12)\n"
      "ds_read_b128 v[80:83], v0 offset:4096\n"
      "v_mfma_f32_16x16x32_bf16 v[220:223], v[18:21], v[116:119], v[220:223]\n"
      "buffer_load_dwordx4 v1, s[40:43], s74 offen offset:3072 lds\n"
      "v_mfma_f32_16x16x32_bf16 v[212:215], v[204:207], v[108:111], v[212:215]\n"
      "v_mfma_f32_16x16x32_bf16 v[212:215], v[208:211], v[112:115], v[212:215]\n"
      "ds_read_b128 v[116:119], v0 offset:5632\n"
      "v_add_f32 v128, v128, v129\n"
      "v_mfma_f32_16x16x32_bf16 v[212:215], v[18:21], v[52:55], v[212:215]\n"
      "v_cvt_pk_bf16_f32 v22, v252, v253\n"
      "v_cvt_pk_bf16_f32 v236, v136, v137\n"
      "v_mfma_f32_16x16x32_bf16 v[200:203], a[16:19], v[124:127], 0\n"
      "v_cvt_pk_bf16_f32 v136, v248, v249\n"
      "s_waitcnt lgkmcnt(4)\n"
      "v_mfma_f32_16x16x32_bf16 v[200:203], a[20:23], v[100:103], v[200:203]\n"
      "ds_read_b128 v[108:111], v0 offset:4608\n"
      "v_mfma_f32_16x16x32_bf16 v[200:203], a[24:27], v[72:75], v[200:203]\n"
      "v_rcp_f32 v129, v128\n"
      "v_mfma_f32_16x16x32_bf16 v[200:203], a[28:31], v[196:199], v[200:203]\n"
      "ds_bpermute_b32 v130, v4, v129 offset:128\n"
      "v_mfma_f32_16x16x32_bf16 v[216:219], v[204:207], v[180:183], v[216:219]\n"
      "v_mfma_f32_16x16x32_bf16 v[216:219], v[208:211], v[184:187], v[216:219]\n"
      "v_cvt_pk_bf16_f32 v184, v164, v165\n"
      "v_cvt_pk_bf16_f32 v27, v142, v143\n"
      "v_mfma_f32_16x16x32_bf16 v[216:219], v[18:21], v[120:123], v[216:219]\n"
      "v_mfma_f32_16x16x32_bf16 v[56:59], a[128:131], v[124:127], 0\n"
      "v_cvt_pk_bf16_f32 v237, v138, v139\n"
      "v_pk_max_i16 v38, v38, 0\n"
      "v_mfma_f32_16x16x32_bf16 v[56:59], a[132:135], v[100:103], v[56:59]\n"
      "ds_read_b128 v[52:55], v10 offset:0\n"
      "v_cvt_pk_bf16_f32 v185, v166, v167\n"
      "v_mfma_f32_16x16x32_bf16 v[56:59], a[136:139], v[72:75], v[56:59]\n"
      "v_cvt_pk_bf16_f32 v21, v70, v71\n"
      "v_mfma_f32_16x16x32_bf16 v[56:59], a[140:143], v[196:199], v[56:59]\n"
      "buffer_store_dwordx2 v[30:31], v2, s[44:47], s100 offen offset:512 nt sc1\n"
      "ds_read_b128 v[180:183], v0 offset:5120\n"
      "v_mfma_f32_16x16x32_bf16 v[208:211], a[48:51], v[92:95], 0\n"
      "v_mfma_f32_16x16x32_bf16 v[208:211], a[52:55], v[88:91], v[208:211]\n"
      "s_waitcnt lgkmcnt(2)\n"
      "v_mul_f32_dpp v219, v130, v219 row_newbcast:12 row_mask:0xf bank_mask:0xf\n"
      "v_mul_f32_dpp v216, v130, v216 row_newbcast:0 row_mask:0xf bank_mask:0xf\n"
      "v_mfma_f32_16x16x32_bf16 v[208:211], a[56:59], v[96:99], v[208:211]\n"
      "v_cvt_pk_bf16_f32 v20, v68, v69\n"
      "v_mul_f32_dpp v220, v130, v220 row_newbcast:0 row_mask:0xf bank_mask:0xf\n"
      "v_mfma_f32_16x16x32_bf16 v[208:211], a[60:63], v[176:179], v[208:211]\n"
      "v_cvt_pk_bf16_f32 v246, v148, v149\n"
      "v_mfma_f32_16x16x32_bf16 v[112:115], a[128:131], v[92:95], 0\n"
      "v_cvt_pk_bf16_f32 v60, v152, v153\n"
      "v_mfma_f32_16x16x32_bf16 v[112:115], a[132:135], v[88:91], v[112:115]\n"
      "v_mfma_f32_16x16x32_bf16 v[112:115], a[136:139], v[96:99], v[112:115]\n"
      "v_mfma_f32_16x16x32_bf16 v[112:115], a[140:143], v[176:179], v[112:115]\n"
      "v_pk_max_i16 v36, v36, 0\n"
      "v_cvt_pk_bf16_f32 v247, v150, v151\n"
      "v_mfma_f32_16x16x32_bf16 v[148:151], a[16:19], v[80:83], 0\n"
      "v_mfma_f32_16x16x32_bf16 v[148:151], a[20:23], v[108:111], v[148:151]\n"
      "s_waitcnt lgkmcnt(0)\n"
      "v_mfma_f32_16x16x32_bf16 v[148:151], a[24:27], v[180:183], v[148:151]\n"
      "ds_read_b128 v[164:167], v9 offset:0\n"
      "v_mul_f32_dpp v217, v130, v217 row_newbcast:4 row_mask:0xf bank_mask:0xf\n"
      "v_mfma_f32_16x16x32_bf16 v[148:151], a[28:31], v[116:119], v[148:151]\n"
      "v_mul_f32_dpp v214, v130, v214 row_newbcast:8 row_mask:0xf bank_mask:0xf\n"
      "v_mfma_f32_16x16x32_bf16 v[68:71], a[144:147], v[92:95], 0\n"
      "buffer_load_dwordx4 v1, s[40:43], s73 offen offset:0 lds\n"
      "v_mfma_f32_16x16x32_bf16 v[68:71], a[148:151], v[88:91], v[68:71]\n"
      "v_cvt_pk_bf16_f32 v142, v208, v209\n"
      "v_cvt_pk_bf16_f32 v26, v140, v141\n"
      "v_mfma_f32_16x16x32_bf16 v[68:71], a[152:155], v[96:99], v[68:71]\n"
      "v_mfma_f32_16x16x32_bf16 v[68:71], a[156:159], v[176:179], v[68:71]\n"
      "v_cvt_pk_bf16_f32 v39, v46, v50\n"
      "v_mfma_f32_16x16x32_bf16 v[48:51], a[32:35], v[80:83], 0\n"
      "v_mfma_f32_16x16x32_bf16 v[48:51], a[36:39], v[108:111], v[48:51]\n"
      "v_mul_f32_dpp v225, v130, v225 row_newbcast:4 row_mask:0xf bank_mask:0xf\n"
      "v_cvt_pk_bf16_f32 v139, v234, v235\n"
      "v_mfma_f32_16x16x32_bf16 v[48:51], a[40:43], v[180:183], v[48:51]\n"
      "s_add_u32 m0, s53, 16384\n"
      "v_cvt_pk_bf16_f32 v240, v112, v113\n"
      "v_mov_b64 v[152:153], 0\n"
      "v_mfma_f32_16x16x32_bf16 v[48:51], a[44:47], v[116:119], v[48:51]\n"
      "buffer_store_dwordx2 v[194:195], v2, s[44:47], s101 offen offset:768 nt sc1\n"
      "v_cvt_pk_bf16_f32 v23, v254, v255\n"
      "v_mfma_f32_16x16x32_bf16 v[120:123], a[32:35], v[124:127], 0\n"
      "v_cvt_pk_bf16_f32 v61, v154, v155\n"
      "v_cvt_pk_bf16_f32 v241, v114, v115\n"
      "v_mfma_f32_16x16x32_bf16 v[120:123], a[36:39], v[100:103], v[120:123]\n"
      "v_mfma_f32_16x16x32_bf16 v[120:123], a[40:43], v[72:75], v[120:123]\n"
      "v_mfma_f32_16x16x32_bf16 v[120:123], a[44:47], v[196:199], v[120:123]\n"
      "v_mov_b64 v[230:231], 0\n"
      "v_cvt_pk_bf16_f32 v208, v48, v49\n"
      "v_mfma_f32_16x16x32_bf16 v[28:31], v[80:83], a[112:115], 0\n"
      "v_mfma_f32_16x16x32_bf16 v[28:31], v[108:111], a[116:119], v[28:31]\n"
      "v_mfma_f32_16x16x32_bf16 v[28:31], v[180:183], a[120:123], v[28:31]\n"
      "v_cvt_pk_bf16_f32 v207, v150, v151\n"
      "v_mul_f32_dpp v212, v130, v212 row_newbcast:0 row_mask:0xf bank_mask:0xf\n"
      "v_mfma_f32_16x16x32_bf16 v[28:31], v[116:119], a[124:127], v[28:31]\n"
      "v_mul_f32_dpp v218, v130, v218 row_newbcast:8 row_mask:0xf bank_mask:0xf\n"
      "v_cvt_pk_bf16_f32 v143, v210, v211\n"
      "v_mfma_f32_16x16x32_bf16 v[44:47], a[0:3], v[80:83], 0\n"
      "L_h3_%=:\n"
      "s_cbranch_execz L_hr_%=\n"
      "v_mul_f32_dpp v221, v130, v221 row_newbcast:4 row_mask:0xf bank_mask:0xf\n"
      "v_mul_f32_dpp v224, v130, v224 row_newbcast:0 row_mask:0xf bank_mask:0xf\n"
      "v_mfma_f32_16x16x32_bf16 v[44:47], a[4:7], v[108:111], v[44:47]\n"
      "buffer_load_dwordx4 v1, s[40:43], s76 offen offset:3072 lds\n"
      "v_mfma_f32_16x16x32_bf16 v[44:47], a[8:11], v[180:183], v[44:47]\n"
      "v_mul_f32_dpp v215, v130, v215 row_newbcast:12 row_mask:0xf bank_mask:0xf\n"
      "v_cvt_pk_bf16_f32 v141, v66, v67\n"
      "v_mfma_f32_16x16x32_bf16 v[44:47], a[12:15], v[116:119], v[44:47]\n"
      "v_mfma_f32_16x16x32_bf16 v[154:157], a[48:51], v[80:83], 0\n"
      "v_pk_max_i16 v40, v40, 0\n"
      "v_cvt_pk_bf16_f32 v242, v68, v69\n"
      "v_mfma_f32_16x16x32_bf16 v[154:157], a[52:55], v[108:111], v[154:157]\n"
      "v_cvt_pk_bf16_f32 v138, v232, v233\n"
      "v_mfma_f32_16x16x32_bf16 v[154:157], a[56:59], v[180:183], v[154:157]\n"
      "v_cvt_pk_bf16_f32 v243, v70, v71\n"
      "v_mfma_f32_16x16x32_bf16 v[154:157], a[60:63], v[116:119], v[154:157]\n"
      "buffer_store_dwordx2 v[34:35], v2, s[44:47], s100 offen offset:768 nt sc1\n"
      "v_mul_f32_dpp v226, v130, v226 row_newbcast:8 row_mask:0xf bank_mask:0xf\n"
      "v_mfma_f32_16x16x32_bf16 v[112:115], a[0:3], v[124:127], 0\n"
      "v_mul_f32_dpp v213, v130, v213 row_newbcast:4 row_mask:0xf bank_mask:0xf\n"
      "v_mul_f32_dpp v223, v130, v223 row_newbcast:12 row_mask:0xf bank_mask:0xf\n"
      "v_mfma_f32_16x16x32_bf16 v[112:115], a[4:7], v[100:103], v[112:115]\n"
      "v_cvt_pk_bf16_f32 v66, v200, v201\n"
      "v_cvt_pk_bf16_f32 v235, v30, v31\n"
      "v_mfma_f32_16x16x32_bf16 v[112:115], a[8:11], v[72:75], v[112:115]\n"
      "v_mfma_f32_16x16x32_bf16 v[112:115], a[12:15], v[196:199], v[112:115]\n"
      "v_cvt_pk_bf16_f32 v67, v202, v203\n"
      "v_cvt_pk_bf16_f32 v68, v120, v121\n"
      "v_mfma_f32_16x16x32_bf16 v[252:255], v[80:83], a[96:99], 0\n"
      "v_cvt_pk_bf16_f32 v211, v156, v157\n"
      "v_mfma_f32_16x16x32_bf16 v[252:255], v[108:111], a[100:103], v[252:255]\n"
      "v_mul_f32_dpp v227, v130, v227 row_newbcast:12 row_mask:0xf bank_mask:0xf\n"
      "v_cvt_pk_bf16_f32 v140, v64, v65\n"
      "v_mfma_f32_16x16x32_bf16 v[252:255], v[180:183], a[104:107], v[252:255]\n"
      "v_cvt_pk_bf16_f32 v137, v250, v251\n"
      "v_cvt_pk_bf16_f32 v205, v46, v47\n"
      "v_mfma_f32_16x16x32_bf16 v[252:255], v[116:119], a[108:111], v[252:255]\n"
      "v_cvt_pk_bf16_f32 v64, v112, v113\n"
      "v_mfma_f32_16x16x32_bf16 v[248:251], a[48:51], v[124:127], 0\n"
      "ds_read_b128 v[192:195], v0 offset:12288\n"
      "v_cvt_pk_bf16_f32 v69, v122, v123\n"
      "v_mfma_f32_16x16x32_bf16 v[248:251], a[52:55], v[100:103], v[248:251]\n"
      "v_pk_max_i16 v42, v42, 0\n"
      "v_mfma_f32_16x16x32_bf16 v[248:251], a[56:59], v[72:75], v[248:251]\n"
      "v_mfma_f32_16x16x32_bf16 v[248:251], a[60:63], v[196:199], v[248:251]\n"
      "v_cvt_pk_bf16_f32 v234, v28, v29\n"
      "v_mul_f32_dpp v222, v130, v222 row_newbcast:8 row_mask:0xf bank_mask:0xf\n"
      "v_mfma_f32_16x16x32_bf16 v[32:35], v[136:139], v[240:243], 0\n"
      "v_mfma_f32_16x16x32_bf16 v[32:35], v[140:143], v[244:247], v[32:35]\n"
      "v_cvt_pk_bf16_f32 v210, v154, v155\n"
      "v_cvt_pk_bf16_f32 v206, v148, v149\n"
      "v_mfma_f32_16x16x32_bf16 v[216:219], v[144:147], a[208:211], v[216:219]\n"
      "v_cvt_pk_bf16_f32 v29, v58, v59\n"
      "v_mfma_f32_16x16x32_bf16 v[216:219], v[104:107], a[212:215], v[216:219]\n"
      "v_mfma_f32_16x16x32_bf16 v[216:219], v[172:175], a[216:219], v[216:219]\n"
      "v_cvt_pk_bf16_f32 v65, v114, v115\n"
      "v_mfma_f32_16x16x32_bf16 v[216:219], v[132:135], a[220:223], v[216:219]\n"
      "v_exp_f32 v33, v33\n"
      "v_mfma_f32_16x16x32_bf16 v[212:215], v[144:147], a[192:195], v[212:215]\n"
      "v_mov_b64 v[120:121], 0\n"
      "v_mfma_f32_16x16x32_bf16 v[212:215], v[104:107], a[196:199], v[212:215]\n"
      "v_cvt_pk_bf16_f32 v70, v248, v249\n"
      "v_cvt_pk_bf16_f32 v209, v50, v51\n"
      "v_mfma_f32_16x16x32_bf16 v[212:215], v[172:175], a[200:203], v[212:215]\n"
      "v_exp_f32 v34, v34\n"
      "v_mfma_f32_16x16x32_bf16 v[212:215], v[132:135], a[204:207], v[212:215]\n"
      "buffer_load_dwordx4 v1, s[40:43], s75 offen offset:0 lds\n"
      "v_mfma_f32_16x16x32_bf16 v[220:223], v[144:147], a[224:227], v[220:223]\n"
      "v_mfma_f32_16x16x32_bf16 v[220:223], v[104:107], a[228:231], v[220:223]\n"
      "ds_read_b128 v[48:51], v0 offset:13824\n"
      "v_cvt_pk_bf16_f32 v28, v56, v57\n"
      "v_mfma_f32_16x16x32_bf16 v[220:223], v[172:175], a[232:235], v[220:223]\n"
      "v_cvt_pk_bf16_f32 v204, v44, v45\n"
      "v_mov_b64 v[248:249], 0\n"
      "v_mfma_f32_16x16x32_bf16 v[220:223], v[132:135], a[236:239], v[220:223]\n"
      "v_mfma_f32_16x16x32_bf16 v[224:227], v[144:147], a[240:243], v[224:227]\n"
      "s_waitcnt vmcnt(13)\n"
      "ds_read_b128 v[44:47], v0 offset:0\n"
      "ds_read_b128 v[148:151], v0 offset:1024\n"
      "v_mfma_f32_16x16x32_bf16 v[224:227], v[104:107], a[244:247], v[224:227]\n"
      "ds_read_b128 v[200:203], v0 offset:12800\n"
      "v_cvt_pk_bf16_f32 v214, v214, v218\n"
      "v_mfma_f32_16x16x32_bf16 v[224:227], v[172:175], a[248:251], v[224:227]\n"
      "v_cvt_pk_bf16_f32 v113, v78, v79\n"
      "v_cvt_pk_bf16_f32 v212, v212, v216\n"
      "v_mfma_f32_16x16x32_bf16 v[224:227], v[132:135], a[252:255], v[224:227]\n"
      "v_cvt_pk_bf16_f32 v71, v250, v251\n"
      "s_waitcnt lgkmcnt(5)\n"
      "v_smfmac_f32_16x16x64_bf16 v[230:233], v[52:55], v[164:171], v5\n"
      "ds_read_b128 v[164:167], v0 offset:1536\n"
      "v_cvt_pk_bf16_f32 v218, v215, v219\n"
      "v_smfmac_f32_16x16x64_bf16 v[248:251], v[52:55], v[20:27], v5\n"
      "buffer_store_dwordx2 v[188:189], v2, s[44:47], s101 offen offset:0 nt sc1\n"
      "v_smfmac_f32_16x16x64_bf16 v[120:123], v[52:55], v[204:211], v5\n"
      "ds_read_b128 v[104:107], v0 offset:13312\n"
      "v_cvt_pk_bf16_f32 v59, v254, v255\n"
      "v_mfma_f32_16x16x32_bf16 v[132:135], v[80:83], a[64:67], 0\n"
      "v_mfma_f32_16x16x32_bf16 v[132:135], v[108:111], a[68:71], v[132:135]\n"
      "ds_read_b128 v[168:171], v0 offset:512\n"
      "v_add_f32 v230, v230, v231\n"
      "v_mfma_f32_16x16x32_bf16 v[132:135], v[180:183], a[72:75], v[132:135]\n"
      "v_mfma_f32_16x16x32_bf16 v[132:135], v[116:119], a[76:79], v[132:135]\n"
      "v_cvt_pk_bf16_f32 v216, v213, v217\n"
      "v_cvt_pk_bf16_f32 v215, v222, v226\n"
      "v_mfma_f32_16x16x32_bf16 v[154:157], v[80:83], a[80:83], 0\n"
      "v_pk_max_i16 v39, v39, 0\n"
      "v_mov_b64 v[144:145], 0\n"
      "v_mfma_f32_16x16x32_bf16 v[154:157], v[108:111], a[84:87], v[154:157]\n"
      "v_cvt_pk_bf16_f32 v217, v221, v225\n"
      "v_mfma_f32_16x16x32_bf16 v[154:157], v[180:183], a[88:91], v[154:157]\n"
      "v_cvt_pk_bf16_f32 v111, v134, v135\n"
      "v_cvt_pk_bf16_f32 v219, v223, v227\n"
      "v_mfma_f32_16x16x32_bf16 v[154:157], v[116:119], a[92:95], v[154:157]\n"
      "v_pk_max_i16 v37, v37, 0\n"
      "v_cvt_pk_bf16_f32 v112, v76, v77\n"
      "v_mfma_f32_16x16x32_bf16 v[80:83], v[64:67], v[240:243], 0\n"
      "v_maximum3_f32 v8, v8, v128, v129\n"
      "v_pk_max_i16 v43, v43, 0\n"
      "v_mfma_f32_16x16x32_bf16 v[80:83], v[68:71], v[244:247], v[80:83]\n"
      "v_pk_max_i16 v219, v219, 0\n"
      "s_waitcnt lgkmcnt(5)\n"
      "v_mfma_f32_16x16x32_bf16 v[172:175], v[44:47], a[80:83], 0\n"
      "s_waitcnt lgkmcnt(0)\n"
      "v_mfma_f32_16x16x32_bf16 v[172:175], v[168:171], a[84:87], v[172:175]\n"
      "v_cvt_pk_bf16_f32 v213, v220, v224\n"
      "v_mfma_f32_16x16x32_bf16 v[172:175], v[148:151], a[88:91], v[172:175]\n"
      "v_mfma_f32_16x16x32_bf16 v[172:175], v[164:167], a[92:95], v[172:175]\n"
      "buffer_store_dwordx2 v[42:43], v2, s[44:47], s34 offen offset:768 nt sc1\n"
      "v_add_f32 v248, v248, v249\n"
      "v_mfma_f32_16x16x32_bf16 v[76:79], v[44:47], a[112:115], 0\n"
      "v_exp_f32 v80, v80\n"
      "v_mfma_f32_16x16x32_bf16 v[76:79], v[168:171], a[116:119], v[76:79]\n"
      "v_cvt_pk_bf16_f32 v182, v154, v155\n"
      "v_pk_max_i16 v212, v212, 0\n"
      "v_mfma_f32_16x16x32_bf16 v[76:79], v[148:151], a[120:123], v[76:79]\n"
      "v_exp_f32 v82, v82\n"
      "v_mfma_f32_16x16x32_bf16 v[76:79], v[164:167], a[124:127], v[76:79]\n"
      "v_cvt_pk_bf16_f32 v180, v172, v173\n"
      "v_mov_b64 v[146:147], 0\n"
      "v_mfma_f32_16x16x32_bf16 v[224:227], v[44:47], a[96:99], 0\n"
      "v_mfma_f32_16x16x32_bf16 v[224:227], v[168:171], a[100:103], v[224:227]\n"
      "buffer_load_dwordx4 v1, s[40:43], s75 offen offset:2048 lds\n"
      "v_mfma_f32_16x16x32_bf16 v[224:227], v[148:151], a[104:107], v[224:227]\n"
      "v_cvt_pk_bf16_f32 v110, v132, v133\n"
      "v_mfma_f32_16x16x32_bf16 v[224:227], v[164:167], a[108:111], v[224:227]\n"
      "v_mov_b64 v[116:117], 0\n"
      "v_cvt_pk_bf16_f32 v232, v76, v77\n"
      "v_smfmac_f32_16x16x64_bf16 v[152:155], v[52:55], v[64:71], v5\n"
      "v_cvt_pk_bf16_f32 v233, v78, v79\n"
      "v_smfmac_f32_16x16x64_bf16 v[116:119], v[52:55], v[136:143], v5\n"
      "v_mov_b32 v231, 0\n"
      "v_mfma_f32_16x16x32_bf16 v[132:135], v[44:47], a[64:67], 0\n"
      "v_exp_f32 v248, v248\n"
      "v_mfma_f32_16x16x32_bf16 v[132:135], v[168:171], a[68:71], v[132:135]\n"
      "v_exp_f32 v81, v81\n"
      "v_mfma_f32_16x16x32_bf16 v[132:135], v[148:151], a[72:75], v[132:135]\n"
      "v_exp_f32_dpp v231, v230 quad_perm:[0,1,2,3] row_mask:0xf bank_mask:0x1\n"
      "v_mfma_f32_16x16x32_bf16 v[132:135], v[164:167], a[76:79], v[132:135]\n"
      "v_exp_f32 v32, v32\n"
      "v_mfma_f32_16x16x32_bf16 v[76:79], a[32:35], v[44:47], 0\n"
      "v_cvt_pk_bf16_f32 v58, v252, v253\n"
      "v_mfma_f32_16x16x32_bf16 v[76:79], a[36:39], v[168:171], v[76:79]\n"
      "v_cvt_pk_bf16_f32 v183, v156, v157\n"
      "v_mfma_f32_16x16x32_bf16 v[76:79], a[40:43], v[148:151], v[76:79]\n"
      "v_mfma_f32_16x16x32_bf16 v[76:79], a[44:47], v[164:167], v[76:79]\n"
      "buffer_store_dwordx2 v[40:41], v2, s[44:47], s34 offen offset:256 nt sc1\n"
      "v_mov_b32_dpp v230, v231 quad_perm:[1,1,3,3] row_mask:0xf bank_mask:0xf\n"
      "v_mfma_f32_16x16x32_bf16 v[252:255], a[16:19], v[44:47], 0\n"
      "v_cvt_pk_bf16_f32 v128, v80, v81\n"
      "v_add_f32 v152, v152, v153\n"
      "v_mfma_f32_16x16x32_bf16 v[252:255], a[20:23], v[168:171], v[252:255]\n"
      "v_add_f32 v116, v116, v117\n"
      "v_add_f32 v120, v120, v121\n"
      "v_mfma_f32_16x16x32_bf16 v[252:255], a[24:27], v[148:151], v[252:255]\n"
      "v_cvt_pk_bf16_f32 v109, v134, v135\n"
      "v_pk_max_i16 v217, v217, 0\n"
      "v_mfma_f32_16x16x32_bf16 v[252:255], a[28:31], v[164:167], v[252:255]\n"
      "v_exp_f32 v35, v35\n"
      "v_mfma_f32_16x16x32_bf16 v[220:223], v[204:207], v[240:243], 0\n"
      "v_cvt_pk_bf16_f32 v57, v226, v227\n"
      "v_cvt_pk_bf16_f32 v230, v231, v230\n"
      "v_mfma_f32_16x16x32_bf16 v[220:223], v[208:211], v[244:247], v[220:223]\n"
      "v_exp_f32 v83, v83\n"
      "v_mfma_f32_16x16x32_bf16 v[154:157], a[48:51], v[44:47], 0\n"
      "v_exp_f32 v116, v116\n"
      "v_mfma_f32_16x16x32_bf16 v[154:157], a[52:55], v[168:171], v[154:157]\n"
      "v_cvt_pk_bf16_f32 v181, v174, v175\n"
      "v_pk_max_i16 v218, v218, 0\n"
      "v_mfma_f32_16x16x32_bf16 v[154:157], a[56:59], v[148:151], v[154:157]\n"
      "v_cvt_pk_bf16_f32 v108, v132, v133\n"
      "v_cvt_pk_bf16_f32 v130, v32, v33\n"
      "v_mfma_f32_16x16x32_bf16 v[154:157], a[60:63], v[164:167], v[154:157]\n"
      "v_exp_f32 v222, v222\n"
      "v_mfma_f32_16x16x32_bf16 v[132:135], a[0:3], v[44:47], 0\n"
      "v_mov_b64 v[172:173], 0\n"
      "v_pk_max_i16 v214, v214, 0\n"
      "v_mfma_f32_16x16x32_bf16 v[132:135], a[4:7], v[168:171], v[132:135]\n"
      "v_cvt_pk_bf16_f32 v56, v224, v225\n"
      "v_mov_b64 v[224:225], 0\n"
      "v_mfma_f32_16x16x32_bf16 v[132:135], a[8:11], v[148:151], v[132:135]\n"
      "v_exp_f32 v223, v223\n"
      "v_mfma_f32_16x16x32_bf16 v[132:135], a[12:15], v[164:167], v[132:135]\n"
      "v_exp_f32 v152, v152\n"
      "v_mfma_f32_16x16x32_bf16 v[40:43], a[48:51], v[192:195], 0\n"
      "v_cvt_pk_bf16_f32 v169, v78, v79\n"
      "v_cndmask_b32_dpp v153, v116, v152, vcc quad_perm:[0,0,2,2] row_mask:0xf bank_mask:0xf\n"
      "v_mfma_f32_16x16x32_bf16 v[40:43], a[52:55], v[200:203], v[40:43]\n"
      "v_cvt_pk_bf16_f32 v171, v156, v157\n"
      "v_cvt_pk_bf16_f32 v166, v252, v253\n"
      "v_mfma_f32_16x16x32_bf16 v[40:43], a[56:59], v[104:107], v[40:43]\n"
      "v_cvt_pk_bf16_f32 v131, v34, v35\n"
      "v_cvt_pk_bf16_f32 v170, v154, v155\n"
      "v_mfma_f32_16x16x32_bf16 v[40:43], a[60:63], v[48:51], v[40:43]\n"
      "v_cvt_pk_bf16_f32 v129, v82, v83\n"
      "v_cvt_pk_bf16_f32 v165, v134, v135\n"
      "v_mfma_f32_16x16x32_bf16 v[148:151], a[0:3], v[192:195], 0\n"
      "v_mfma_f32_16x16x32_bf16 v[148:151], a[4:7], v[200:203], v[148:151]\n"
      "v_mov_b32_dpp v44, v160 row_ror:8 row_mask:0xf bank_mask:0xf\n"
      "v_mfma_f32_16x16x32_bf16 v[148:151], a[8:11], v[104:107], v[148:151]\n"
      "v_exp_f32 v221, v221\n"
      "v_mfma_f32_16x16x32_bf16 v[148:151], a[12:15], v[48:51], v[148:151]\n"
      "buffer_store_dwordx2 v[36:37], v2, s[44:47], s34 offen offset:0 nt sc1\n"
      "v_pk_max_i16 v213, v213, 0\n"
      "v_mfma_f32_16x16x32_bf16 v[32:35], a[16:19], v[192:195], 0\n"
      "v_cvt_pk_bf16_f32 v82, v40, v41\n"
      "v_cvt_pk_bf16_f32 v167, v254, v255\n"
      "v_mfma_f32_16x16x32_bf16 v[32:35], a[20:23], v[200:203], v[32:35]\n"
      "v_cvt_pk_bf16_f32 v168, v76, v77\n"
      "v_cvt_pk_bf16_f32 v164, v132, v133\n"
      "v_mfma_f32_16x16x32_bf16 v[32:35], a[24:27], v[104:107], v[32:35]\n"
      "buffer_load_dwordx4 v1, s[40:43], s76 offen offset:1024 lds\n"
      "v_mfma_f32_16x16x32_bf16 v[32:35], a[28:31], v[48:51], v[32:35]\n"
      "v_cvt_pk_bf16_f32 v76, v148, v149\n"
      "v_pk_max_i16 v216, v216, 0\n"
      "v_mfma_f32_16x16x32_bf16 v[132:135], a[32:35], v[192:195], 0\n"
      "v_exp_f32 v120, v120\n"
      "v_mfma_f32_16x16x32_bf16 v[132:135], a[36:39], v[200:203], v[132:135]\n"
      "v_mov_b32_dpp v45, v161 row_ror:8 row_mask:0xf bank_mask:0xf\n"
      "v_cvt_pk_bf16_f32 v251, v222, v223\n"
      "v_mfma_f32_16x16x32_bf16 v[132:135], a[40:43], v[104:107], v[132:135]\n"
      "v_exp_f32 v220, v220\n"
      "v_mfma_f32_16x16x32_bf16 v[132:135], a[44:47], v[48:51], v[132:135]\n"
      "v_cvt_pk_bf16_f32 v77, v150, v151\n"
      "s_add_u32 m0, s53, 12288\n"
      "v_smfmac_f32_16x16x64_bf16 v[224:227], v[52:55], v[164:171], v5\n"
      "v_cvt_pk_bf16_f32 v78, v32, v33\n"
      "v_mfma_f32_16x16x32_bf16 v[252:255], v[164:167], v[240:243], 0\n"
      "v_cvt_pk_bf16_f32 v83, v42, v43\n"
      "v_pk_max_i16 v215, v215, 0\n"
      "v_mfma_f32_16x16x32_bf16 v[252:255], v[168:171], v[244:247], v[252:255]\n"
      "v_cvt_pk_bf16_f32 v79, v34, v35\n"
      "v_cvt_pk_bf16_f32 v81, v134, v135\n"
      "v_mfma_f32_16x16x32_bf16 v[40:43], v[20:23], v[240:243], 0\n"
      "v_add_f32 v224, v224, v225\n"
      "v_cvt_pk_bf16_f32 v80, v132, v133\n"
      "v_mfma_f32_16x16x32_bf16 v[40:43], v[24:27], v[244:247], v[40:43]\n"
      "v_exp_f32 v224, v224\n"
      "v_smfmac_f32_16x16x64_bf16 v[172:175], v[52:55], v[76:83], v5\n"
      "buffer_load_dwordx4 v1, s[40:43], s77 offen offset:0 lds\n"
      "v_mfma_f32_16x16x32_bf16 v[52:55], a[176:179], v[124:127], 0\n"
      "v_exp_f32 v253, v253\n"
      "v_mfma_f32_16x16x32_bf16 v[52:55], a[180:183], v[100:103], v[52:55]\n"
      "v_cvt_pk_bf16_f32 v250, v220, v221\n"
      "v_mov_b32_dpp v47, v163 row_ror:8 row_mask:0xf bank_mask:0xf\n"
      "v_mfma_f32_16x16x32_bf16 v[52:55], a[184:187], v[72:75], v[52:55]\n"
      "v_exp_f32 v252, v252\n"
      "v_mfma_f32_16x16x32_bf16 v[52:55], a[188:191], v[196:199], v[52:55]\n"
      "v_exp_f32 v41, v41\n"
      "v_mfma_f32_16x16x32_bf16 v[220:223], a[160:163], v[124:127], 0\n"
      "v_exp_f32 v254, v254\n"
      "v_mfma_f32_16x16x32_bf16 v[220:223], a[164:167], v[100:103], v[220:223]\n"
      "v_add_f32 v172, v172, v173\n"
      "v_cndmask_b32_dpp v225, v248, v224, vcc quad_perm:[0,0,2,2] row_mask:0xf bank_mask:0xf\n"
      "v_mfma_f32_16x16x32_bf16 v[220:223], a[168:171], v[72:75], v[220:223]\n"
      "v_exp_f32 v43, v43\n"
      "v_mfma_f32_16x16x32_bf16 v[220:223], a[172:175], v[196:199], v[220:223]\n"
      "v_cvt_pk_bf16_f32 v35, v54, v55\n"
      "v_mfma_f32_16x16x32_bf16 v[132:135], a[144:147], v[124:127], 0\n"
      "v_exp_f32 v172, v172\n"
      "v_mfma_f32_16x16x32_bf16 v[132:135], a[148:151], v[100:103], v[132:135]\n"
      "v_cvt_pk_bf16_f32 v34, v52, v53\n"
      "v_cndmask_b32_dpp v121, v172, v120, vcc quad_perm:[0,0,2,2] row_mask:0xf bank_mask:0xf\n"
      "v_mfma_f32_16x16x32_bf16 v[132:135], a[152:155], v[72:75], v[132:135]\n"
      "s_not_b64 vcc, vcc\n"
      "v_cndmask_b32_dpp v249, v224, v248, vcc quad_perm:[1,1,3,3] row_mask:0xf bank_mask:0xf\n"
      "v_add_f32 v224, v224, v120\n"
      "v_mfma_f32_16x16x32_bf16 v[132:135], a[156:159], v[196:199], v[132:135]\n"
      "v_add_f32 v248, v248, v172\n"
      "v_cndmask_b32_dpp v117, v152, v116, vcc quad_perm:[1,1,3,3] row_mask:0xf bank_mask:0xf\n"
      "v_mfma_f32_16x16x32_bf16 v[52:55], v[76:79], v[240:243], 0\n"
      "v_cvt_pk_bf16_f32 v153, v153, v117\n"
      "v_cvt_pk_bf16_f32 v32, v220, v221\n"
      "v_mfma_f32_16x16x32_bf16 v[52:55], v[80:83], v[244:247], v[52:55]\n"
      "buffer_store_dwordx2 v[38:39], v2, s[44:47], s34 offen offset:512 nt sc1\n"
      "v_add_f32 v152, v152, v116\n"
      "v_mfma_f32_16x16x32_bf16 v[116:119], v[192:195], a[96:99], 0\n"
      "v_add_f32 v224, v224, v248\n"
      "v_cvt_pk_bf16_f32 v225, v225, v249\n"
      "v_mfma_f32_16x16x32_bf16 v[116:119], v[200:203], a[100:103], v[116:119]\n"
      "v_add_f32 v152, v152, v231\n"
      "v_cvt_pk_bf16_f32 v30, v132, v133\n"
      "v_mfma_f32_16x16x32_bf16 v[116:119], v[104:107], a[104:107], v[116:119]\n"
      "v_cvt_pk_bf16_f32 v33, v222, v223\n"
      "v_cvt_pk_bf16_f32 v31, v134, v135\n"
      "v_mfma_f32_16x16x32_bf16 v[116:119], v[48:51], a[108:111], v[116:119]\n"
      "ds_bpermute_b32 v174, v6, v225 offset:16\n"
      "v_mfma_f32_16x16x32_bf16 v[220:223], v[20:23], v[28:31], 0\n"
      "v_exp_f32 v54, v54\n"
      "v_mfma_f32_16x16x32_bf16 v[220:223], v[24:27], v[32:35], v[220:223]\n"
      "v_cvt_pk_bf16_f32 v248, v252, v253\n"
      "v_cndmask_b32_dpp v173, v120, v172, vcc quad_perm:[1,1,3,3] row_mask:0xf bank_mask:0xf\n"
      "v_mfma_f32_16x16x32_bf16 v[36:39], v[76:79], v[28:31], 0\n"
      "v_cvt_pk_bf16_f32 v121, v121, v173\n"
      "v_add_f32 v18, v224, v152\n"
      "v_mfma_f32_16x16x32_bf16 v[36:39], v[80:83], v[32:35], v[36:39]\n"
      "ds_bpermute_b32 v173, v6, v121\n"
      "v_mfma_f32_16x16x32_bf16 v[132:135], v[192:195], a[80:83], 0\n"
      "v_exp_f32 v40, v40\n"
      "v_mfma_f32_16x16x32_bf16 v[132:135], v[200:203], a[84:87], v[132:135]\n"
      "v_exp_f32 v52, v52\n"
      "v_mfma_f32_16x16x32_bf16 v[132:135], v[104:107], a[88:91], v[132:135]\n"
      "v_add_f32_dpp v18, v18, v18 row_ror:8 row_mask:0xf bank_mask:0xf\n"
      "v_mov_b32_dpp v46, v162 row_ror:8 row_mask:0xf bank_mask:0xf\n"
      "v_mfma_f32_16x16x32_bf16 v[132:135], v[48:51], a[92:95], v[132:135]\n"
      "v_exp_f32 v42, v42\n"
      "v_smfmac_f32_16x16x64_bf16 v[144:147], v[160:163], v[240:247], v5\n"
      "ds_bpermute_b32 v158, v7, v230 offset:248\n"
      "v_smfmac_f32_16x16x64_bf16 v[144:147], v[44:47], v[28:35], v5\n"
      "ds_bpermute_b32 v175, v6, v121 offset:16\n"
      "v_mfma_f32_16x16x32_bf16 v[120:123], v[204:207], v[28:31], 0\n"
      "v_exp_f32 v39, v39\n"
      "v_mfma_f32_16x16x32_bf16 v[120:123], v[208:211], v[32:35], v[120:123]\n"
      "v_exp_f32 v223, v223\n"
      "v_mfma_f32_16x16x32_bf16 v[148:151], v[164:167], v[28:31], 0\n"
      "v_exp_f32 v255, v255\n"
      "v_mfma_f32_16x16x32_bf16 v[148:151], v[168:171], v[32:35], v[148:151]\n"
      "v_cvt_pk_bf16_f32 v187, v134, v135\n"
      "v_cvt_pk_bf16_f32 v186, v132, v133\n"
      "v_mfma_f32_16x16x32_bf16 v[244:247], v[64:67], v[28:31], 0\n"
      "buffer_load_dwordx4 v1, s[40:43], s78 offen offset:1024 lds\n"
      "v_mfma_f32_16x16x32_bf16 v[244:247], v[68:71], v[32:35], v[244:247]\n"
      "buffer_store_dwordx2 v[218:219], v2, s[44:47], s35 offen offset:768 nt sc1\n"
      "v_cvt_pk_bf16_f32 v249, v254, v255\n"
      "v_mfma_f32_16x16x32_bf16 v[132:135], v[92:95], a[64:67], 0\n"
      "v_exp_f32 v151, v151\n"
      "v_mfma_f32_16x16x32_bf16 v[132:135], v[88:91], a[68:71], v[132:135]\n"
      "v_exp_f32 v149, v149\n"
      "v_mfma_f32_16x16x32_bf16 v[132:135], v[96:99], a[72:75], v[132:135]\n"
      "v_exp_f32 v122, v122\n"
      "v_mfma_f32_16x16x32_bf16 v[132:135], v[176:179], a[76:79], v[132:135]\n"
      "v_exp_f32 v53, v53\n"
      "v_mfma_f32_16x16x32_bf16 v[240:243], v[136:139], v[28:31], 0\n"
      "v_exp_f32 v245, v245\n"
      "v_mfma_f32_16x16x32_bf16 v[240:243], v[140:143], v[32:35], v[240:243]\n"
      "v_cvt_pk_bf16_f32 v253, v42, v43\n"
      "v_cvt_pk_bf16_f32 v62, v116, v117\n"
      "v_mfma_f32_16x16x32_bf16 v[32:35], v[192:195], a[112:115], 0\n"
      "v_exp_f32 v55, v55\n"
      "v_mfma_f32_16x16x32_bf16 v[32:35], v[200:203], a[116:119], v[32:35]\n"
      "ds_bpermute_b32 v159, v7, v230 offset:8\n"
      "v_mfma_f32_16x16x32_bf16 v[32:35], v[104:107], a[120:123], v[32:35]\n"
      "v_cvt_pk_bf16_f32 v254, v52, v53\n"
      "v_cvt_pk_bf16_f32 v63, v118, v119\n"
      "v_mfma_f32_16x16x32_bf16 v[32:35], v[48:51], a[124:127], v[32:35]\n"
      "v_exp_f32 v120, v120\n"
      "v_mfma_f32_16x16x32_bf16 v[28:31], v[192:195], a[64:67], 0\n"
      "v_exp_f32 v222, v222\n"
      "v_mfma_f32_16x16x32_bf16 v[28:31], v[200:203], a[68:71], v[28:31]\n"
      "v_exp_f32 v150, v150\n"
      "v_mfma_f32_16x16x32_bf16 v[28:31], v[104:107], a[72:75], v[28:31]\n"
      "v_exp_f32 v246, v246\n"
      "v_mfma_f32_16x16x32_bf16 v[28:31], v[48:51], a[76:79], v[28:31]\n"
      "ds_bpermute_b32 v172, v6, v225\n"
      "v_mfma_f32_16x16x32_bf16 v[116:119], v[124:127], a[64:67], 0\n"
      "v_exp_f32 v38, v38\n"
      "v_mfma_f32_16x16x32_bf16 v[116:119], v[100:103], a[68:71], v[116:119]\n"
      "v_exp_f32 v123, v123\n"
      "v_mfma_f32_16x16x32_bf16 v[116:119], v[72:75], a[72:75], v[116:119]\n"
      "v_exp_f32 v148, v148\n"
      "v_mfma_f32_16x16x32_bf16 v[116:119], v[196:199], a[76:79], v[116:119]\n"
      "v_exp_f32 v121, v121\n"
      "v_mfma_f32_16x16x32_bf16 v[224:227], v[124:127], a[112:115], 0\n"
      "v_cvt_pk_bf16_f32 v115, v30, v31\n"
      "v_cvt_pk_bf16_f32 v114, v28, v29\n"
      "v_mfma_f32_16x16x32_bf16 v[224:227], v[100:103], a[116:119], v[224:227]\n"
      "v_cvt_pk_bf16_f32 v29, v150, v151\n"
      "v_cvt_pk_bf16_f32 v255, v54, v55\n"
      "v_mfma_f32_16x16x32_bf16 v[224:227], v[72:75], a[120:123], v[224:227]\n"
      "v_exp_f32 v221, v221\n"
      "v_mfma_f32_16x16x32_bf16 v[224:227], v[196:199], a[124:127], v[224:227]\n"
      "buffer_store_dwordx2 v[216:217], v2, s[44:47], s35 offen offset:256 nt sc1\n"
      "v_add_f32 v146, v146, v147\n"
      "v_mfma_f32_16x16x32_bf16 v[154:157], v[124:127], a[80:83], 0\n"
      "v_cvt_pk_bf16_f32 v28, v148, v149\n"
      "v_cvt_pk_bf16_f32 v30, v120, v121\n"
      "v_mfma_f32_16x16x32_bf16 v[154:157], v[100:103], a[84:87], v[154:157]\n"
      "v_exp_f32 v241, v241\n"
      "v_mfma_f32_16x16x32_bf16 v[154:157], v[72:75], a[88:91], v[154:157]\n"
      "v_exp_f32 v242, v242\n"
      "v_mfma_f32_16x16x32_bf16 v[154:157], v[196:199], a[92:95], v[154:157]\n"
      "v_cvt_pk_bf16_f32 v229, v226, v227\n"
      "v_cvt_pk_bf16_f32 v252, v40, v41\n"
      "v_mfma_f32_16x16x32_bf16 v[40:43], v[92:95], a[112:115], 0\n"
      "v_exp_f32 v247, v247\n"
      "v_mfma_f32_16x16x32_bf16 v[40:43], v[88:91], a[116:119], v[40:43]\n"
      "v_exp_f32 v36, v36\n"
      "v_mfma_f32_16x16x32_bf16 v[40:43], v[96:99], a[120:123], v[40:43]\n"
      "v_exp_f32 v244, v244\n"
      "v_mfma_f32_16x16x32_bf16 v[40:43], v[176:179], a[124:127], v[40:43]\n"
      "v_exp_f32 v37, v37\n"
      "v_mfma_f32_16x16x32_bf16 v[148:151], v[124:127], a[96:99], 0\n"
      "v_cvt_pk_bf16_f32 v31, v122, v123\n"
      "v_cvt_pk_bf16_f32 v52, v116, v117\n"
      "v_mfma_f32_16x16x32_bf16 v[148:151], v[100:103], a[100:103], v[148:151]\n"
      "v_cvt_pk_bf16_f32 v239, v34, v35\n"
      "v_cvt_pk_bf16_f32 v228, v224, v225\n"
      "v_mfma_f32_16x16x32_bf16 v[148:151], v[72:75], a[104:107], v[148:151]\n"
      "v_exp_f32 v243, v243\n"
      "v_mfma_f32_16x16x32_bf16 v[148:151], v[196:199], a[108:111], v[148:151]\n"
      "v_cvt_pk_bf16_f32 v55, v134, v135\n"
      "v_cvt_pk_bf16_f32 v54, v132, v133\n"
      "v_mfma_f32_16x16x32_bf16 v[216:219], a[176:179], v[192:195], 0\n"
      "v_cvt_pk_bf16_f32 v53, v118, v119\n"
      "v_cvt_pk_bf16_f32 v230, v40, v41\n"
      "v_mfma_f32_16x16x32_bf16 v[216:219], a[180:183], v[200:203], v[216:219]\n"
      "v_cvt_pk_bf16_f32 v121, v156, v157\n"
      "v_cvt_pk_bf16_f32 v120, v154, v155\n"
      "v_mfma_f32_16x16x32_bf16 v[216:219], a[184:187], v[104:107], v[216:219]\n"
      "v_exp_f32 v220, v220\n"
      "v_mfma_f32_16x16x32_bf16 v[216:219], a[188:191], v[48:51], v[216:219]\n"
      "v_exp_f32 v240, v240\n"
      "v_mfma_f32_16x16x32_bf16 v[224:227], v[92:95], a[96:99], 0\n"
      "v_cvt_pk_bf16_f32 v134, v240, v241\n"
      "v_mfma_f32_16x16x32_bf16 v[224:227], v[88:91], a[100:103], v[224:227]\n"
      "v_cvt_pk_bf16_f32 v135, v242, v243\n"
      "v_cvt_pk_bf16_f32 v231, v42, v43\n"
      "v_mfma_f32_16x16x32_bf16 v[224:227], v[96:99], a[104:107], v[224:227]\n"
      "v_cvt_pk_bf16_f32 v132, v244, v245\n"
      "v_cvt_pk_bf16_f32 v42, v216, v217\n"
      "v_mfma_f32_16x16x32_bf16 v[224:227], v[176:179], a[108:111], v[224:227]\n"
      "buffer_store_dwordx2 v[212:213], v2, s[44:47], s35 offen offset:0 nt sc1\n"
      "v_cvt_pk_bf16_f32 v133, v246, v247\n"
      "v_mfma_f32_16x16x32_bf16 v[240:243], a[144:147], v[192:195], 0\n"
      "v_cvt_pk_bf16_f32 v238, v32, v33\n"
      "v_add_f32 v144, v144, v145\n"
      "v_mfma_f32_16x16x32_bf16 v[240:243], a[148:151], v[200:203], v[240:243]\n"
      "v_exp_f32 v144, v144\n"
      "v_mfma_f32_16x16x32_bf16 v[240:243], a[152:155], v[104:107], v[240:243]\n"
      "v_exp_f32 v146, v146\n"
      "v_mfma_f32_16x16x32_bf16 v[240:243], a[156:159], v[48:51], v[240:243]\n"
      "v_cvt_pk_bf16_f32 v35, v38, v39\n"
      "v_cvt_pk_bf16_f32 v34, v36, v37\n"
      "v_mfma_f32_16x16x32_bf16 v[244:247], v[92:95], a[80:83], 0\n"
      "v_cvt_pk_bf16_f32 v43, v218, v219\n"
      "v_cvt_pk_bf16_f32 v117, v150, v151\n"
      "v_mfma_f32_16x16x32_bf16 v[244:247], v[88:91], a[84:87], v[244:247]\n"
      "ds_read_b64 v[156:157], v11 offset:0\n"
      "v_cvt_pk_bf16_f32 v116, v148, v149\n"
      "v_mfma_f32_16x16x32_bf16 v[244:247], v[96:99], a[88:91], v[244:247]\n"
      "v_cvt_pk_bf16_f32 v149, v144, v146\n"
      "v_cvt_pk_bf16_f32 v119, v226, v227\n"
      "v_mfma_f32_16x16x32_bf16 v[244:247], v[176:179], a[92:95], v[244:247]\n"
      "v_cvt_pk_bf16_f32 v39, v242, v243\n"
      "v_cvt_pk_bf16_f32 v118, v224, v225\n"
      "v_mfma_f32_16x16x32_bf16 v[224:227], a[128:131], v[192:195], 0\n"
      "v_cvt_pk_bf16_f32 v148, v144, v146\n"
      "v_cvt_pk_bf16_f32 v38, v240, v241\n"
      "v_mfma_f32_16x16x32_bf16 v[224:227], a[132:135], v[200:203], v[224:227]\n"
      "ds_bpermute_b32 v145, v7, v153 offset:4\n"
      "v_mfma_f32_16x16x32_bf16 v[224:227], a[136:139], v[104:107], v[224:227]\n"
      "v_permlane16_swap_b32 v148, v149\n"
      "v_mfma_f32_16x16x32_bf16 v[224:227], a[140:143], v[48:51], v[224:227]\n"
      "v_cvt_pk_bf16_f32 v32, v220, v221\n"
      "v_cvt_pk_bf16_f32 v33, v222, v223\n"
      "v_mfma_f32_16x16x32_bf16 v[220:223], a[160:163], v[192:195], 0\n"
      "v_cvt_pk_bf16_f32 v122, v244, v245\n"
      "v_cvt_pk_bf16_f32 v123, v246, v247\n"
      "v_mfma_f32_16x16x32_bf16 v[220:223], a[164:167], v[200:203], v[220:223]\n"
      "v_add_f32 v245, v144, v146\n"
      "v_add_f32 v244, v144, v146\n"
      "v_mfma_f32_16x16x32_bf16 v[220:223], a[168:171], v[104:107], v[220:223]\n"
      "s_nop 0\n"
      "v_permlane16_swap_b32 v244, v245\n"
      "v_mfma_f32_16x16x32_bf16 v[220:223], a[172:175], v[48:51], v[220:223]\n"
      "v_cvt_pk_bf16_f32 v36, v224, v225\n"
      "v_mov_b64 v[188:189], v[244:245]\n"
      "v_smfmac_f32_16x16x64_bf16 v[244:247], v[12:15], v[248:255], v5\n"
      "buffer_store_dwordx2 v[214:215], v2, s[44:47], s35 offen offset:512 nt sc1\n"
      "v_cvt_pk_bf16_f32 v37, v226, v227\n"
      "v_smfmac_f32_16x16x64_bf16 v[188:191], v[12:15], v[28:35], v5\n"
      "s_nop 1\n"
      "v_cvt_pk_bf16_f32 v40, v220, v221\n"
      "v_smfmac_f32_16x16x32_bf16 v[244:247], v[12:13], v[128:131], v5\n"
      "ds_bpermute_b32 v144, v7, v153\n"
      "v_cvt_pk_bf16_f32 v41, v222, v223\n"
      "s_waitcnt lgkmcnt(2)\n"
      "v_mfma_f32_16x16x4_4b_bf16 v[212:227], v[148:149], v[156:157], 0 cbsz:2 abid:0\n"
      "s_nop 3\n"
      "v_add_f32 v244, v244, v245\n"
      "v_rcp_f32 v245, v244\n"
      "v_smfmac_f32_16x16x32_bf16 v[188:191], v[12:13], v[132:135], v5\n"
      "ds_bpermute_b32 v246, v4, v245\n"
      "v_add_u32 v16, 64, v16\n"
      "s_nop 0\n"
      "v_mfma_f32_16x16x32_bf16 v[216:219], v[248:251], v[180:183], v[216:219]\n"
      "v_add_f32_dpp v18, v18, v18 row_ror:4 row_mask:0xf bank_mask:0xf\n"
      "v_mfma_f32_16x16x32_bf16 v[216:219], v[252:255], v[184:187], v[216:219]\n"
      "buffer_load_dwordx4 v1, s[40:43], s78 offen offset:3072 lds\n"
      "v_mfma_f32_16x16x32_bf16 v[216:219], v[128:131], v[120:123], v[216:219]\n"
      "v_mfma_f32_16x16x32_bf16 v[212:215], v[248:251], v[108:111], v[212:215]\n"
      "v_mfma_f32_16x16x32_bf16 v[212:215], v[252:255], v[112:115], v[212:215]\n"
      "v_add_f32 v188, v188, v189\n"
      "v_add_f32_dpp v18, v18, v18 row_ror:2 row_mask:0xf bank_mask:0xf\n"
      "v_mfma_f32_16x16x32_bf16 v[212:215], v[128:131], v[52:55], v[212:215]\n"
      "v_rcp_f32 v189, v188\n"
      "v_mfma_f32_16x16x32_bf16 v[224:227], v[248:251], v[232:235], v[224:227]\n"
      "v_mfma_f32_16x16x32_bf16 v[224:227], v[252:255], v[236:239], v[224:227]\n"
      "ds_bpermute_b32 v190, v4, v189 offset:128\n"
      "v_mfma_f32_16x16x32_bf16 v[224:227], v[128:131], v[228:231], v[224:227]\n"
      "s_waitcnt lgkmcnt(1)\n"
      "v_mul_f32_dpp v217, v246, v217 row_newbcast:4 row_mask:0xf bank_mask:0xf\n"
      "v_add_f32_dpp v18, v18, v18 row_ror:1 row_mask:0xf bank_mask:0xf\n"
      "v_mfma_f32_16x16x32_bf16 v[220:223], v[248:251], v[56:59], v[220:223]\n"
      "v_mul_f32_dpp v219, v246, v219 row_newbcast:12 row_mask:0xf bank_mask:0xf\n"
      "v_mul_f32_dpp v212, v246, v212 row_newbcast:0 row_mask:0xf bank_mask:0xf\n"
      "v_mfma_f32_16x16x32_bf16 v[220:223], v[252:255], v[60:63], v[220:223]\n"
      "v_mul_f32_dpp v213, v246, v213 row_newbcast:4 row_mask:0xf bank_mask:0xf\n"
      "v_mul_f32_dpp v218, v246, v218 row_newbcast:8 row_mask:0xf bank_mask:0xf\n"
      "v_mfma_f32_16x16x32_bf16 v[220:223], v[128:131], v[116:119], v[220:223]\n"
      "v_mul_f32_dpp v216, v246, v216 row_newbcast:0 row_mask:0xf bank_mask:0xf\n"
      "v_mfma_f32_16x16x32_bf16 v[84:87], v[64:67], v[36:39], 0\n"
      "buffer_load_dwordx4 v1, s[40:43], s77 offen offset:2048 lds\n"
      "v_mfma_f32_16x16x32_bf16 v[84:87], v[68:71], v[40:43], v[84:87]\n"
      "v_mul_f32_dpp v226, v246, v226 row_newbcast:8 row_mask:0xf bank_mask:0xf\n"
      "v_mfma_f32_16x16x32_bf16 v[216:219], v[92:95], a[208:211], v[216:219]\n"
      "v_rcp_f32 v19, v18\n"
      "v_mfma_f32_16x16x32_bf16 v[216:219], v[88:91], a[212:215], v[216:219]\n"
      "v_mul_f32_dpp v214, v246, v214 row_newbcast:8 row_mask:0xf bank_mask:0xf\n"
      "v_mfma_f32_16x16x32_bf16 v[216:219], v[96:99], a[216:219], v[216:219]\n"
      "v_mul_f32_dpp v215, v246, v215 row_newbcast:12 row_mask:0xf bank_mask:0xf\n"
      "v_mul_f32_dpp v223, v246, v223 row_newbcast:12 row_mask:0xf bank_mask:0xf\n"
      "v_mfma_f32_16x16x32_bf16 v[216:219], v[176:179], a[220:223], v[216:219]\n"
      "v_maximum3_f32 v8, v8, v18, v19\n"
      "v_mul_f32_dpp v221, v246, v221 row_newbcast:4 row_mask:0xf bank_mask:0xf\n"
      "v_mfma_f32_16x16x32_bf16 v[212:215], v[92:95], a[192:195], v[212:215]\n"
      "v_mul_f32_dpp v220, v246, v220 row_newbcast:0 row_mask:0xf bank_mask:0xf\n"
      "v_mul_f32_dpp v222, v246, v222 row_newbcast:8 row_mask:0xf bank_mask:0xf\n"
      "v_mfma_f32_16x16x32_bf16 v[212:215], v[88:91], a[196:199], v[212:215]\n"
      "ds_read_b128 v[128:131], v0 offset:8192\n"
      "v_mfma_f32_16x16x32_bf16 v[212:215], v[96:99], a[200:203], v[212:215]\n"
      "v_add_u32 v9, 64, v9\n"
      "v_maximum3_f32 v8, v8, v244, v245\n"
      "v_mfma_f32_16x16x32_bf16 v[212:215], v[176:179], a[204:207], v[212:215]\n"
      "v_mfma_f32_16x16x32_bf16 v[220:223], v[92:95], a[224:227], v[220:223]\n"
      "v_exp_f32 v87, v87\n"
      "v_mfma_f32_16x16x32_bf16 v[220:223], v[88:91], a[228:231], v[220:223]\n"
      "v_mul_f32_dpp v225, v246, v225 row_newbcast:4 row_mask:0xf bank_mask:0xf\n"
      "v_mul_f32_dpp v224, v246, v224 row_newbcast:0 row_mask:0xf bank_mask:0xf\n"
      "v_mfma_f32_16x16x32_bf16 v[220:223], v[96:99], a[232:235], v[220:223]\n"
      "v_mul_f32_dpp v227, v246, v227 row_newbcast:12 row_mask:0xf bank_mask:0xf\n"
      "v_mfma_f32_16x16x32_bf16 v[220:223], v[176:179], a[236:239], v[220:223]\n"
      "L_h4_%=:\n"
      "s_cbranch_execz L_hr_%=\n"
      "ds_read_b128 v[152:155], v0 offset:8704\n"
      "v_mfma_f32_16x16x32_bf16 v[224:227], v[92:95], a[240:243], v[224:227]\n"
      "v_exp_f32 v84, v84\n"
      "v_mfma_f32_16x16x32_bf16 v[224:227], v[88:91], a[244:247], v[224:227]\n"
      "v_cvt_pk_bf16_f32 v212, v212, v216\n"
      "v_pk_max_i16 v212, v212, 0\n"
      "v_mfma_f32_16x16x32_bf16 v[224:227], v[96:99], a[248:251], v[224:227]\n"
      "v_exp_f32 v85, v85\n"
      "v_mfma_f32_16x16x32_bf16 v[224:227], v[176:179], a[252:255], v[224:227]\n"
      "v_cvt_pk_bf16_f32 v214, v214, v218\n"
      "v_pk_max_i16 v214, v214, 0\n"
      "v_mfma_f32_16x16x4_4b_bf16 v[240:255], v[148:149], v[156:157], 0 cbsz:2 abid:2\n"
      "ds_read_b128 v[148:151], v0 offset:9216\n"
      "v_cvt_pk_bf16_f32 v216, v213, v217\n"
      "v_cvt_pk_bf16_f32 v218, v215, v219\n"
      "s_nop 1\n"
      "v_cvt_pk_bf16_f32 v215, v222, v226\n"
      "v_pk_max_i16 v215, v215, 0\n"
      "v_mfma_f32_16x16x32_bf16 v[176:179], v[136:139], v[36:39], 0\n"
      "v_cvt_pk_bf16_f32 v213, v220, v224\n"
      "v_cvt_pk_bf16_f32 v217, v221, v225\n"
      "v_mfma_f32_16x16x32_bf16 v[176:179], v[140:143], v[40:43], v[176:179]\n"
      "buffer_store_dwordx2 v[214:215], v2, s[44:47], s73 offen offset:512 nt sc1\n"
      "v_cvt_pk_bf16_f32 v219, v223, v227\n"
      "v_mfma_f32_16x16x32_bf16 v[248:251], v[28:31], v[56:59], v[248:251]\n"
      "ds_read_b128 v[224:227], v0 offset:9728\n"
      "v_pk_max_i16 v217, v217, 0\n"
      "v_mfma_f32_16x16x32_bf16 v[248:251], v[32:35], v[60:63], v[248:251]\n"
      "v_exp_f32 v86, v86\n"
      "v_mfma_f32_16x16x32_bf16 v[248:251], v[132:135], v[116:119], v[248:251]\n"
      "v_exp_f32 v178, v178\n"
      "v_mfma_f32_16x16x32_bf16 v[240:243], v[28:31], v[108:111], v[240:243]\n"
      "v_exp_f32 v177, v177\n"
      "v_mfma_f32_16x16x32_bf16 v[240:243], v[32:35], v[112:115], v[240:243]\n"
      "v_cvt_pk_bf16_f32 v221, v86, v87\n"
      "v_cvt_pk_bf16_f32 v220, v84, v85\n"
      "v_mfma_f32_16x16x32_bf16 v[240:243], v[132:135], v[52:55], v[240:243]\n"
      "v_exp_f32 v179, v179\n"
      "v_mfma_f32_16x16x32_bf16 v[244:247], v[28:31], v[180:183], v[244:247]\n"
      "s_add_u32 m0, s53, 8192\n"
      "v_exp_f32 v176, v176\n"
      "v_mfma_f32_16x16x32_bf16 v[244:247], v[32:35], v[184:187], v[244:247]\n"
      "v_pk_max_i16 v213, v213, 0\n"
      "v_pk_max_i16 v219, v219, 0\n"
      "v_mfma_f32_16x16x32_bf16 v[244:247], v[132:135], v[120:123], v[244:247]\n"
      "s_waitcnt lgkmcnt(4)\n"
      "v_mul_f32_dpp v248, v190, v248 row_newbcast:0 row_mask:0xf bank_mask:0xf\n"
      "v_cvt_pk_bf16_f32 v222, v176, v177\n"
      "v_mfma_f32_16x16x32_bf16 v[252:255], v[28:31], v[232:235], v[252:255]\n"
      "v_mul_f32_dpp v250, v190, v250 row_newbcast:8 row_mask:0xf bank_mask:0xf\n"
      "v_mfma_f32_16x16x32_bf16 v[252:255], v[32:35], v[236:239], v[252:255]\n"
      "v_mul_f32_dpp v240, v190, v240 row_newbcast:0 row_mask:0xf bank_mask:0xf\n"
      "v_mul_f32_dpp v243, v190, v243 row_newbcast:12 row_mask:0xf bank_mask:0xf\n"
      "v_mfma_f32_16x16x32_bf16 v[252:255], v[132:135], v[228:231], v[252:255]\n"
      "s_waitcnt lgkmcnt(0)\n"
      "buffer_load_dwordx4 v1, s[40:43], s91 offen offset:3072 lds\n"
      "v_mfma_f32_16x16x4_4b_bf16 v[84:99], v[158:159], v[156:157], 0 cbsz:2 abid:0\n"
      "buffer_store_dwordx2 v[212:213], v2, s[44:47], s73 offen offset:0 nt sc1\n"
      "v_mul_f32_dpp v244, v190, v244 row_newbcast:0 row_mask:0xf bank_mask:0xf\n"
      "v_mul_f32_dpp v245, v190, v245 row_newbcast:4 row_mask:0xf bank_mask:0xf\n"
      "v_pk_max_i16 v218, v218, 0\n"
      "v_mfma_f32_16x16x32_bf16 v[28:31], v[164:167], v[36:39], 0\n"
      "v_mul_f32_dpp v253, v190, v253 row_newbcast:4 row_mask:0xf bank_mask:0xf\n"
      "v_mul_f32_dpp v252, v190, v252 row_newbcast:0 row_mask:0xf bank_mask:0xf\n"
      "v_mfma_f32_16x16x32_bf16 v[28:31], v[168:171], v[40:43], v[28:31]\n"
      "v_mul_f32_dpp v249, v190, v249 row_newbcast:4 row_mask:0xf bank_mask:0xf\n"
      "s_nop 0\n"
      "v_smfmac_f32_16x16x64_bf16 v[88:91], v[172:175], v[180:187], v5\n"
      "ds_read_b64 v[132:133], v11 offset:4096\n"
      "v_mul_f32_dpp v246, v190, v246 row_newbcast:8 row_mask:0xf bank_mask:0xf\n"
      "v_smfmac_f32_16x16x64_bf16 v[84:87], v[172:175], v[108:115], v5\n"
      "buffer_load_dwordx4 v1, s[40:43], s90 offen offset:0 lds\n"
      "v_smfmac_f32_16x16x64_bf16 v[92:95], v[172:175], v[56:63], v5\n"
      "v_smfmac_f32_16x16x64_bf16 v[96:99], v[172:175], v[232:239], v5\n"
      "v_mul_f32_dpp v247, v190, v247 row_newbcast:12 row_mask:0xf bank_mask:0xf\n"
      "v_mfma_f32_16x16x32_bf16 v[32:35], v[76:79], v[36:39], 0\n"
      "v_exp_f32 v30, v30\n"
      "v_mfma_f32_16x16x32_bf16 v[32:35], v[80:83], v[40:43], v[32:35]\n"
      "v_exp_f32 v29, v29\n"
      "v_smfmac_f32_16x16x32_bf16 v[92:95], v[144:145], v[116:119], v5\n"
      "v_smfmac_f32_16x16x32_bf16 v[88:91], v[144:145], v[120:123], v5\n"
      "v_maximum3_f32 v8, v8, v188, v189\n"
      "v_smfmac_f32_16x16x32_bf16 v[96:99], v[144:145], v[228:231], v5\n"
      "v_mul_f32_dpp v254, v190, v254 row_newbcast:8 row_mask:0xf bank_mask:0xf\n"
      "v_smfmac_f32_16x16x32_bf16 v[84:87], v[144:145], v[52:55], v5\n"
      "v_mul_f32_dpp v242, v190, v242 row_newbcast:8 row_mask:0xf bank_mask:0xf\n"
      "v_cvt_pk_bf16_f32 v223, v178, v179\n"
      "v_mul_f32_dpp v251, v190, v251 row_newbcast:12 row_mask:0xf bank_mask:0xf\n"
      "v_mfma_f32_16x16x32_bf16 v[212:215], v[20:23], v[36:39], 0\n"
      "v_mul_f32_dpp v241, v190, v241 row_newbcast:4 row_mask:0xf bank_mask:0xf\n"
      "v_mul_f32_dpp v255, v190, v255 row_newbcast:12 row_mask:0xf bank_mask:0xf\n"
      "v_mfma_f32_16x16x32_bf16 v[212:215], v[24:27], v[40:43], v[212:215]\n"
      "v_exp_f32 v31, v31\n"
      "v_mfma_f32_16x16x32_bf16 v[240:243], v[124:127], a[192:195], v[240:243]\n"
      "s_waitcnt lgkmcnt(0)\n"
      "v_and_b32 v191, 0xffff0000, v133\n"
      "v_add_f32 v96, v96, v97\n"
      "v_mfma_f32_16x16x32_bf16 v[240:243], v[100:103], a[196:199], v[240:243]\n"
      "v_fma_f32 v96, v96, v19, v191\n"
      "v_lshlrev_b32 v190, 16, v133\n"
      "v_mfma_f32_16x16x32_bf16 v[240:243], v[72:75], a[200:203], v[240:243]\n"
      "v_exp_f32 v214, v214\n"
      "v_mfma_f32_16x16x32_bf16 v[240:243], v[196:199], a[204:207], v[240:243]\n"
      "v_exp_f32 v213, v213\n"
      "v_mfma_f32_16x16x32_bf16 v[244:247], v[124:127], a[208:211], v[244:247]\n"
      "v_exp_f32 v28, v28\n"
      "v_mfma_f32_16x16x32_bf16 v[244:247], v[100:103], a[212:215], v[244:247]\n"
      "v_exp_f32 v32, v32\n"
      "v_mfma_f32_16x16x32_bf16 v[244:247], v[72:75], a[216:219], v[244:247]\n"
      "v_exp_f32 v212, v212\n"
      "v_mfma_f32_16x16x32_bf16 v[244:247], v[196:199], a[220:223], v[244:247]\n"
      "buffer_store_dwordx2 v[218:219], v2, s[44:47], s73 offen offset:768 nt sc1\n"
      "v_lshlrev_b32 v188, 16, v132\n"
      "v_mfma_f32_16x16x32_bf16 v[252:255], v[124:127], a[240:243], v[252:255]\n"
      "v_add_f32 v84, v84, v85\n"
      "v_fma_f32 v84, v84, v19, v188\n"
      "v_mfma_f32_16x16x32_bf16 v[252:255], v[100:103], a[244:247], v[252:255]\n"
      "v_and_b32 v189, 0xffff0000, v132\n"
      "v_add_f32 v92, v92, v93\n"
      "v_mfma_f32_16x16x32_bf16 v[252:255], v[72:75], a[248:251], v[252:255]\n"
      "v_fma_f32 v92, v92, v19, v190\n"
      "v_cvt_pk_bf16_f32 v85, v92, v96\n"
      "v_mfma_f32_16x16x32_bf16 v[252:255], v[196:199], a[252:255], v[252:255]\n"
      "v_exp_f32 v34, v34\n"
      "v_mfma_f32_16x16x32_bf16 v[172:175], v[204:207], v[36:39], 0\n"
      "buffer_load_dwordx4 v1, s[40:43], s91 offen offset:1024 lds\n"
      "v_mfma_f32_16x16x32_bf16 v[172:175], v[208:211], v[40:43], v[172:175]\n"
      "v_exp_f32 v215, v215\n"
      "v_mfma_f32_16x16x32_bf16 v[132:135], a[144:147], v[128:131], 0\n"
      "v_exp_f32 v33, v33\n"
      "v_mfma_f32_16x16x32_bf16 v[132:135], a[148:151], v[152:155], v[132:135]\n"
      "v_exp_f32 v35, v35\n"
      "v_mfma_f32_16x16x32_bf16 v[132:135], a[152:155], v[148:151], v[132:135]\n"
      "v_add_f32 v88, v88, v89\n"
      "v_fma_f32 v88, v88, v19, v189\n"
      "v_mfma_f32_16x16x32_bf16 v[132:135], a[156:159], v[224:227], v[132:135]\n"
      "v_exp_f32 v174, v174\n"
      "v_mfma_f32_16x16x32_bf16 v[144:147], a[128:131], v[128:131], 0\n"
      "v_pk_max_i16 v216, v216, 0\n"
      "v_mfma_f32_16x16x32_bf16 v[144:147], a[132:135], v[152:155], v[144:147]\n"
      "v_exp_f32 v172, v172\n"
      "v_mfma_f32_16x16x32_bf16 v[144:147], a[136:139], v[148:151], v[144:147]\n"
      "v_cvt_pk_bf16_f32 v99, v34, v35\n"
      "v_cvt_pk_bf16_f32 v98, v32, v33\n"
      "v_mfma_f32_16x16x32_bf16 v[144:147], a[140:143], v[224:227], v[144:147]\n"
      "v_cvt_pk_bf16_f32 v93, v30, v31\n"
      "v_cvt_pk_bf16_f32 v92, v28, v29\n"
      "v_mfma_f32_16x16x32_bf16 v[188:191], a[176:179], v[128:131], 0\n"
      "v_pk_max_i16 v85, v85, 0\n"
      "v_cvt_pk_bf16_f32 v84, v84, v88\n"
      "v_mfma_f32_16x16x32_bf16 v[188:191], a[180:183], v[152:155], v[188:191]\n"
      "v_cvt_pk_bf16_f32 v96, v212, v213\n"
      "v_pk_max_i16 v84, v84, 0\n"
      "v_mfma_f32_16x16x32_bf16 v[188:191], a[184:187], v[148:151], v[188:191]\n"
      "v_cvt_pk_bf16_f32 v31, v134, v135\n"
      "v_cvt_pk_bf16_f32 v28, v144, v145\n"
      "v_mfma_f32_16x16x32_bf16 v[188:191], a[188:191], v[224:227], v[188:191]\n"
      "v_cvt_pk_bf16_f32 v30, v132, v133\n"
      "v_cvt_pk_bf16_f32 v240, v240, v244\n"
      "v_mfma_f32_16x16x32_bf16 v[132:135], a[160:163], v[128:131], 0\n"
      "v_cvt_pk_bf16_f32 v244, v241, v245\n"
      "v_cvt_pk_bf16_f32 v29, v146, v147\n"
      "v_mfma_f32_16x16x32_bf16 v[132:135], a[164:167], v[152:155], v[132:135]\n"
      "v_exp_f32 v175, v175\n"
      "v_mfma_f32_16x16x32_bf16 v[132:135], a[168:171], v[148:151], v[132:135]\n"
      "v_exp_f32 v173, v173\n"
      "v_mfma_f32_16x16x32_bf16 v[132:135], a[172:175], v[224:227], v[132:135]\n"
      "buffer_store_dwordx2 v[216:217], v2, s[44:47], s73 offen offset:256 nt sc1\n"
      "v_mov_b64 v[176:177], 0\n"
      "v_mfma_f32_16x16x32_bf16 v[248:251], v[124:127], a[224:227], v[248:251]\n"
      "v_cvt_pk_bf16_f32 v95, v174, v175\n"
      "v_cvt_pk_bf16_f32 v34, v188, v189\n"
      "v_mfma_f32_16x16x32_bf16 v[248:251], v[100:103], a[228:231], v[248:251]\n"
      "v_cvt_pk_bf16_f32 v35, v190, v191\n"
      "v_cvt_pk_bf16_f32 v242, v242, v246\n"
      "v_mfma_f32_16x16x32_bf16 v[248:251], v[72:75], a[232:235], v[248:251]\n"
      "v_cvt_pk_bf16_f32 v33, v134, v135\n"
      "v_mov_b64 v[174:175], 0\n"
      "v_mfma_f32_16x16x32_bf16 v[248:251], v[196:199], a[236:239], v[248:251]\n"
      "v_cvt_pk_bf16_f32 v32, v132, v133\n"
      "v_cvt_pk_bf16_f32 v246, v243, v247\n"
      "v_smfmac_f32_16x16x64_bf16 v[174:177], v[160:163], v[36:43], v5\n"
      "buffer_load_dwordx4 v1, s[40:43], s90 offen offset:2048 lds\n"
      "v_smfmac_f32_16x16x64_bf16 v[174:177], v[44:47], v[28:35], v5\n"
      "ds_read_b128 v[72:75], v0 offset:4608\n"
      "v_pk_max_i16 v242, v242, 0\n"
      "v_mfma_f32_16x16x32_bf16 v[88:91], v[64:67], v[28:31], 0\n"
      "v_cvt_pk_bf16_f32 v245, v249, v253\n"
      "v_cvt_pk_bf16_f32 v94, v172, v173\n"
      "v_mfma_f32_16x16x32_bf16 v[88:91], v[68:71], v[32:35], v[88:91]\n"
      "v_cvt_pk_bf16_f32 v247, v251, v255\n"
      "v_pk_max_i16 v247, v247, 0\n"
      "v_mfma_f32_16x16x32_bf16 v[144:147], v[20:23], v[28:31], 0\n"
      "v_add_f32 v174, v174, v175\n"
      "v_add_f32 v176, v176, v177\n"
      "v_mfma_f32_16x16x32_bf16 v[144:147], v[24:27], v[32:35], v[144:147]\n"
      "v_exp_f32 v176, v176\n"
      "v_mfma_f32_16x16x32_bf16 v[196:199], v[76:79], v[28:31], 0\n"
      "v_exp_f32 v174, v174\n"
      "v_mfma_f32_16x16x32_bf16 v[196:199], v[80:83], v[32:35], v[196:199]\n"
      "v_add_f32 v213, v174, v176\n"
      "v_add_f32 v212, v174, v176\n"
      "v_mfma_f32_16x16x32_bf16 v[132:135], v[204:207], v[28:31], 0\n"
      "s_nop 0\n"
      "v_permlane16_swap_b32 v212, v213\n"
      "v_mfma_f32_16x16x32_bf16 v[132:135], v[208:211], v[32:35], v[132:135]\n"
      "v_cvt_pk_bf16_f32 v217, v174, v176\n"
      "v_cvt_pk_bf16_f32 v216, v174, v176\n"
      "v_mfma_f32_16x16x32_bf16 v[172:175], v[136:139], v[28:31], 0\n"
      "v_exp_f32 v197, v197\n"
      "v_mfma_f32_16x16x32_bf16 v[172:175], v[140:143], v[32:35], v[172:175]\n"
      "v_exp_f32 v144, v144\n"
      "v_mfma_f32_16x16x32_bf16 v[188:191], v[164:167], v[28:31], 0\n"
      "v_mov_b64 v[176:177], v[212:213]\n"
      "v_cvt_pk_bf16_f32 v97, v214, v215\n"
      "v_mfma_f32_16x16x32_bf16 v[188:191], v[168:171], v[32:35], v[188:191]\n"
      "v_permlane16_swap_b32 v216, v217\n"
      "v_smfmac_f32_16x16x64_bf16 v[212:215], v[12:15], v[92:99], v5\n"
      "buffer_store_dwordx2 v[84:85], v3, s[44:47], s79 offen offset:0 nt sc1\n"
      "v_pk_max_i16 v240, v240, 0\n"
      "v_mfma_f32_16x16x4_4b_bf16 v[28:43], v[216:217], v[156:157], 0 cbsz:2 abid:0\n"
      "ds_read_b128 v[124:127], v0 offset:5632\n"
      "v_cvt_pk_bf16_f32 v243, v250, v254\n"
      "v_exp_f32 v88, v88\n"
      "v_cvt_pk_bf16_f32 v241, v248, v252\n"
      "v_pk_max_i16 v241, v241, 0\n"
      "v_smfmac_f32_16x16x32_bf16 v[212:215], v[12:13], v[220:223], v5\n"
      "s_add_u32 m0, s53, 4096\n"
      "ds_read_b128 v[100:103], v0 offset:4096\n"
      "v_pk_max_i16 v243, v243, 0\n"
      "v_exp_f32 v145, v145\n"
      "v_mfma_f32_16x16x32_bf16 v[32:35], v[92:95], v[180:183], v[32:35]\n"
      "v_exp_f32 v132, v132\n"
      "v_mfma_f32_16x16x32_bf16 v[32:35], v[96:99], v[184:187], v[32:35]\n"
      "ds_read_b128 v[84:87], v0 offset:5120\n"
      "v_add_f32 v212, v212, v213\n"
      "v_mfma_f32_16x16x32_bf16 v[32:35], v[220:223], v[120:123], v[32:35]\n"
      "v_rcp_f32 v213, v212\n"
      "v_mfma_f32_16x16x32_bf16 v[28:31], v[92:95], v[108:111], v[28:31]\n"
      "ds_bpermute_b32 v214, v4, v213\n"
      "v_mfma_f32_16x16x32_bf16 v[28:31], v[96:99], v[112:115], v[28:31]\n"
      "v_exp_f32 v147, v147\n"
      "v_mfma_f32_16x16x32_bf16 v[28:31], v[220:223], v[52:55], v[28:31]\n"
      "v_exp_f32 v146, v146\n"
      "v_mfma_f32_16x16x32_bf16 v[36:39], v[92:95], v[56:59], v[36:39]\n"
      "v_exp_f32 v91, v91\n"
      "v_mfma_f32_16x16x32_bf16 v[36:39], v[96:99], v[60:63], v[36:39]\n"
      "v_exp_f32 v172, v172\n"
      "v_mfma_f32_16x16x32_bf16 v[36:39], v[220:223], v[116:119], v[36:39]\n"
      "v_exp_f32 v196, v196\n"
      "v_mfma_f32_16x16x32_bf16 v[40:43], v[92:95], v[232:235], v[40:43]\n"
      "v_exp_f32 v173, v173\n"
      "v_mfma_f32_16x16x32_bf16 v[40:43], v[96:99], v[236:239], v[40:43]\n"
      "v_exp_f32 v198, v198\n"
      "v_mfma_f32_16x16x32_bf16 v[40:43], v[220:223], v[228:231], v[40:43]\n"
      "s_waitcnt lgkmcnt(1)\n"
      "buffer_load_dwordx4 v1, s[40:43], s92 offen offset:0 lds\n"
      "v_mfma_f32_16x16x32_bf16 v[94:97], a[176:179], v[100:103], 0\n"
      "v_exp_f32 v134, v134\n"
      "v_mfma_f32_16x16x32_bf16 v[94:97], a[180:183], v[72:75], v[94:97]\n"
      "s_waitcnt lgkmcnt(0)\n"
      "v_mul_f32_dpp v28, v214, v28 row_newbcast:0 row_mask:0xf bank_mask:0xf\n"
      "v_mul_f32_dpp v29, v214, v29 row_newbcast:4 row_mask:0xf bank_mask:0xf\n"
      "v_mfma_f32_16x16x32_bf16 v[94:97], a[184:187], v[84:87], v[94:97]\n"
      "v_cvt_pk_bf16_f32 v253, v146, v147\n"
      "v_mul_f32_dpp v34, v214, v34 row_newbcast:8 row_mask:0xf bank_mask:0xf\n"
      "v_mfma_f32_16x16x32_bf16 v[94:97], a[188:191], v[124:127], v[94:97]\n"
      "v_exp_f32 v189, v189\n"
      "v_mfma_f32_16x16x32_bf16 v[218:221], a[128:131], v[100:103], 0\n"
      "v_exp_f32 v135, v135\n"
      "v_mfma_f32_16x16x32_bf16 v[218:221], a[132:135], v[72:75], v[218:221]\n"
      "v_mul_f32_dpp v32, v214, v32 row_newbcast:0 row_mask:0xf bank_mask:0xf\n"
      "v_mul_f32_dpp v33, v214, v33 row_newbcast:4 row_mask:0xf bank_mask:0xf\n"
      "v_mfma_f32_16x16x32_bf16 v[218:221], a[136:139], v[84:87], v[218:221]\n"
      "v_mul_f32_dpp v30, v214, v30 row_newbcast:8 row_mask:0xf bank_mask:0xf\n"
      "v_mul_f32_dpp v35, v214, v35 row_newbcast:12 row_mask:0xf bank_mask:0xf\n"
      "v_mfma_f32_16x16x32_bf16 v[218:221], a[140:143], v[124:127], v[218:221]\n"
      "v_exp_f32 v89, v89\n"
      "v_mfma_f32_16x16x32_bf16 v[32:35], v[192:195], a[208:211], v[32:35]\n"
      "v_mul_f32_dpp v38, v214, v38 row_newbcast:8 row_mask:0xf bank_mask:0xf\n"
      "v_mul_f32_dpp v42, v214, v42 row_newbcast:8 row_mask:0xf bank_mask:0xf\n"
      "v_mfma_f32_16x16x32_bf16 v[32:35], v[200:203], a[212:215], v[32:35]\n"
      "v_mul_f32_dpp v41, v214, v41 row_newbcast:4 row_mask:0xf bank_mask:0xf\n"
      "v_cvt_pk_bf16_f32 v252, v144, v145\n"
      "v_mfma_f32_16x16x32_bf16 v[32:35], v[104:107], a[216:219], v[32:35]\n"
      "v_mul_f32_dpp v36, v214, v36 row_newbcast:0 row_mask:0xf bank_mask:0xf\n"
      "v_mul_f32_dpp v31, v214, v31 row_newbcast:12 row_mask:0xf bank_mask:0xf\n"
      "v_mfma_f32_16x16x32_bf16 v[32:35], v[48:51], a[220:223], v[32:35]\n"
      "v_exp_f32 v188, v188\n"
      "v_mfma_f32_16x16x32_bf16 v[28:31], v[192:195], a[192:195], v[28:31]\n"
      "v_mul_f32_dpp v40, v214, v40 row_newbcast:0 row_mask:0xf bank_mask:0xf\n"
      "v_mfma_f32_16x16x32_bf16 v[28:31], v[200:203], a[196:199], v[28:31]\n"
      "v_exp_f32 v174, v174\n"
      "v_mfma_f32_16x16x32_bf16 v[28:31], v[104:107], a[200:203], v[28:31]\n"
      "v_mul_f32_dpp v37, v214, v37 row_newbcast:4 row_mask:0xf bank_mask:0xf\n"
      "v_mul_f32_dpp v39, v214, v39 row_newbcast:12 row_mask:0xf bank_mask:0xf\n"
      "v_mfma_f32_16x16x32_bf16 v[28:31], v[48:51], a[204:207], v[28:31]\n"
      "v_maximum3_f32 v8, v8, v212, v213\n"
      "v_cvt_pk_bf16_f32 v254, v196, v197\n"
      "v_mfma_f32_16x16x32_bf16 v[36:39], v[192:195], a[224:227], v[36:39]\n"
      "v_exp_f32 v191, v191\n"
      "v_mfma_f32_16x16x32_bf16 v[36:39], v[200:203], a[228:231], v[36:39]\n"
      "buffer_load_dwordx4 v1, s[40:43], s93 offen offset:3072 lds\n"
      "v_mfma_f32_16x16x32_bf16 v[36:39], v[104:107], a[232:235], v[36:39]\n"
      "v_mul_f32_dpp v43, v214, v43 row_newbcast:12 row_mask:0xf bank_mask:0xf\n"
      "v_cvt_pk_bf16_f32 v28, v28, v32\n"
      "v_mfma_f32_16x16x32_bf16 v[36:39], v[48:51], a[236:239], v[36:39]\n"
      "ds_read_b128 v[144:147], v0 offset:0\n"
      "v_cvt_pk_bf16_f32 v214, v172, v173\n"
      "v_mfma_f32_16x16x32_bf16 v[40:43], v[192:195], a[240:243], v[40:43]\n"
      "v_exp_f32 v199, v199\n"
      "v_mfma_f32_16x16x32_bf16 v[40:43], v[200:203], a[244:247], v[40:43]\n"
      "v_exp_f32 v133, v133\n"
      "v_mfma_f32_16x16x32_bf16 v[40:43], v[104:107], a[248:251], v[40:43]\n"
      "v_cvt_pk_bf16_f32 v248, v188, v189\n"
      "v_mfma_f32_16x16x32_bf16 v[40:43], v[48:51], a[252:255], v[40:43]\n"
      "v_exp_f32 v190, v190\n"
      "v_mfma_f32_16x16x32_bf16 v[104:107], a[160:163], v[100:103], 0\n"
      "v_exp_f32 v90, v90\n"
      "v_mfma_f32_16x16x32_bf16 v[104:107], a[164:167], v[72:75], v[104:107]\n"
      "v_cvt_pk_bf16_f32 v213, v90, v91\n"
      "v_mov_b64 v[50:51], 0\n"
      "v_mfma_f32_16x16x32_bf16 v[104:107], a[168:171], v[84:87], v[104:107]\n"
      "v_cvt_pk_bf16_f32 v249, v190, v191\n"
      "v_pk_max_i16 v246, v246, 0\n"
      "v_mfma_f32_16x16x32_bf16 v[104:107], a[172:175], v[124:127], v[104:107]\n"
      "v_cvt_pk_bf16_f32 v212, v88, v89\n"
      "v_cvt_pk_bf16_f32 v250, v132, v133\n"
      "v_mfma_f32_16x16x32_bf16 v[88:91], a[144:147], v[100:103], 0\n"
      "v_cvt_pk_bf16_f32 v99, v96, v97\n"
      "v_cvt_pk_bf16_f32 v255, v198, v199\n"
      "v_mfma_f32_16x16x32_bf16 v[88:91], a[148:151], v[72:75], v[88:91]\n"
      "v_cvt_pk_bf16_f32 v251, v134, v135\n"
      "v_cvt_pk_bf16_f32 v98, v94, v95\n"
      "v_mfma_f32_16x16x32_bf16 v[88:91], a[152:155], v[84:87], v[88:91]\n"
      "v_cvt_pk_bf16_f32 v32, v29, v33\n"
      "v_cvt_pk_bf16_f32 v33, v37, v41\n"
      "v_mfma_f32_16x16x32_bf16 v[88:91], a[156:159], v[124:127], v[88:91]\n"
      "v_exp_f32 v175, v175\n"
      "v_smfmac_f32_16x16x64_bf16 v[176:179], v[12:15], v[248:255], v5\n"
      "buffer_store_dwordx2 v[246:247], v2, s[44:47], s75 offen offset:768 nt sc1\n"
      "v_cvt_pk_bf16_f32 v215, v174, v175\n"
      "v_mfma_f32_16x16x4_4b_bf16 v[188:203], v[216:217], v[156:157], 0 cbsz:2 abid:2\n"
      "v_cvt_pk_bf16_f32 v30, v30, v34\n"
      "v_pk_max_i16 v245, v245, 0\n"
      "v_cvt_pk_bf16_f32 v93, v220, v221\n"
      "v_cvt_pk_bf16_f32 v29, v36, v40\n"
      "v_cvt_pk_bf16_f32 v96, v104, v105\n"
      "v_cvt_pk_bf16_f32 v97, v106, v107\n"
      "v_smfmac_f32_16x16x32_bf16 v[176:179], v[12:13], v[212:215], v5\n"
      "ds_read_b128 v[172:175], v0 offset:1024\n"
      "v_cvt_pk_bf16_f32 v34, v31, v35\n"
      "v_cvt_pk_bf16_f32 v95, v90, v91\n"
      "v_cvt_pk_bf16_f32 v94, v88, v89\n"
      "v_mfma_f32_16x16x32_bf16 v[200:203], v[248:251], v[232:235], v[200:203]\n"
      "v_cvt_pk_bf16_f32 v92, v218, v219\n"
      "v_cvt_pk_bf16_f32 v35, v39, v43\n"
      "v_mfma_f32_16x16x32_bf16 v[200:203], v[252:255], v[236:239], v[200:203]\n"
      "buffer_load_dwordx4 v1, s[40:43], s93 offen offset:1024 lds\n"
      "v_mfma_f32_16x16x32_bf16 v[200:203], v[212:215], v[228:231], v[200:203]\n"
      "ds_read_b128 v[104:107], v0 offset:512\n"
      "v_add_f32 v176, v176, v177\n"
      "v_mfma_f32_16x16x32_bf16 v[88:91], v[76:79], v[92:95], 0\n"
      "v_cvt_pk_bf16_f32 v31, v38, v42\n"
      "v_mov_b64 v[48:49], 0\n"
      "v_mfma_f32_16x16x32_bf16 v[88:91], v[80:83], v[96:99], v[88:91]\n"
      "ds_read_b128 v[132:135], v0 offset:1536\n"
      "v_pk_max_i16 v35, v35, 0\n"
      "v_mfma_f32_16x16x32_bf16 v[40:43], v[20:23], v[92:95], 0\n"
      "v_rcp_f32 v177, v176\n"
      "v_mfma_f32_16x16x32_bf16 v[40:43], v[24:27], v[96:99], v[40:43]\n"
      "ds_bpermute_b32 v178, v4, v177 offset:128\n"
      "v_smfmac_f32_16x16x64_bf16 v[48:51], v[160:163], v[92:99], v5\n"
      "buffer_store_dwordx2 v[240:241], v2, s[44:47], s75 offen offset:0 nt sc1\n"
      "v_pk_max_i16 v29, v29, 0\n"
      "v_mfma_f32_16x16x32_bf16 v[36:39], v[204:207], v[92:95], 0\n"
      "v_exp_f32 v90, v90\n"
      "v_mfma_f32_16x16x32_bf16 v[36:39], v[208:211], v[96:99], v[36:39]\n"
      "v_exp_f32 v88, v88\n"
      "v_mfma_f32_16x16x32_bf16 v[188:191], v[248:251], v[108:111], v[188:191]\n"
      "v_exp_f32 v42, v42\n"
      "v_mfma_f32_16x16x32_bf16 v[188:191], v[252:255], v[112:115], v[188:191]\n"
      "v_exp_f32 v41, v41\n"
      "v_mfma_f32_16x16x32_bf16 v[188:191], v[212:215], v[52:55], v[188:191]\n"
      "buffer_load_dwordx4 v1, s[40:43], s92 offen offset:2048 lds\n"
      "v_mfma_f32_16x16x32_bf16 v[196:199], v[248:251], v[56:59], v[196:199]\n"
      "v_exp_f32 v91, v91\n"
      "v_mfma_f32_16x16x32_bf16 v[196:199], v[252:255], v[60:63], v[196:199]\n"
      "v_exp_f32 v39, v39\n"
      "v_mfma_f32_16x16x32_bf16 v[196:199], v[212:215], v[116:119], v[196:199]\n"
      "v_exp_f32 v89, v89\n"
      "v_mfma_f32_16x16x32_bf16 v[192:195], v[248:251], v[180:183], v[192:195]\n"
      "v_exp_f32 v36, v36\n"
      "v_mfma_f32_16x16x32_bf16 v[192:195], v[252:255], v[184:187], v[192:195]\n"
      "s_waitcnt lgkmcnt(0)\n"
      "v_mul_f32_dpp v200, v178, v200 row_newbcast:0 row_mask:0xf bank_mask:0xf\n"
      "v_mul_f32_dpp v188, v178, v188 row_newbcast:0 row_mask:0xf bank_mask:0xf\n"
      "v_mfma_f32_16x16x32_bf16 v[192:195], v[212:215], v[120:123], v[192:195]\n"
      "v_mul_f32_dpp v201, v178, v201 row_newbcast:4 row_mask:0xf bank_mask:0xf\n"
      "v_cvt_pk_bf16_f32 v218, v88, v89\n"
      "v_mfma_f32_16x16x32_bf16 v[220:223], v[64:67], v[92:95], 0\n"
      "v_exp_f32 v40, v40\n"
      "v_mfma_f32_16x16x32_bf16 v[220:223], v[68:71], v[96:99], v[220:223]\n"
      "v_exp_f32 v43, v43\n"
      "v_mfma_f32_16x16x32_bf16 v[160:163], v[136:139], v[92:95], 0\n"
      "v_cvt_pk_bf16_f32 v217, v42, v43\n"
      "v_mul_f32_dpp v192, v178, v192 row_newbcast:0 row_mask:0xf bank_mask:0xf\n"
      "v_mfma_f32_16x16x32_bf16 v[160:163], v[140:143], v[96:99], v[160:163]\n"
      "v_cvt_pk_bf16_f32 v216, v40, v41\n"
      "v_mul_f32_dpp v203, v178, v203 row_newbcast:12 row_mask:0xf bank_mask:0xf\n"
      "v_mfma_f32_16x16x32_bf16 v[40:43], v[164:167], v[92:95], 0\n"
      "v_exp_f32 v37, v37\n"
      "v_mfma_f32_16x16x32_bf16 v[40:43], v[168:171], v[96:99], v[40:43]\n"
      "v_exp_f32 v221, v221\n"
      "v_mfma_f32_16x16x32_bf16 v[92:95], a[144:147], v[144:147], 0\n"
      "s_nop 0\n"
      "v_exp_f32 v161, v161\n"
      "v_mfma_f32_16x16x32_bf16 v[92:95], a[148:151], v[104:107], v[92:95]\n"
      "v_cvt_pk_bf16_f32 v219, v90, v91\n"
      "v_mul_f32_dpp v189, v178, v189 row_newbcast:4 row_mask:0xf bank_mask:0xf\n"
      "v_mfma_f32_16x16x32_bf16 v[92:95], a[152:155], v[172:175], v[92:95]\n"
      "v_exp_f32 v38, v38\n"
      "v_mfma_f32_16x16x32_bf16 v[92:95], a[156:159], v[132:135], v[92:95]\n"
      "buffer_store_dwordx2 v[242:243], v2, s[44:47], s75 offen offset:512 nt sc1\n"
      "v_mul_f32_dpp v191, v178, v191 row_newbcast:12 row_mask:0xf bank_mask:0xf\n"
      "v_mfma_f32_16x16x32_bf16 v[88:91], a[128:131], v[144:147], 0\n"
      "v_exp_f32 v220, v220\n"
      "v_mfma_f32_16x16x32_bf16 v[88:91], a[132:135], v[104:107], v[88:91]\n"
      "v_exp_f32 v43, v43\n"
      "v_mfma_f32_16x16x32_bf16 v[88:91], a[136:139], v[172:175], v[88:91]\n"
      "s_nop 0\n"
      "v_cvt_pk_bf16_f32 v249, v94, v95\n"
      "v_mul_f32_dpp v197, v178, v197 row_newbcast:4 row_mask:0xf bank_mask:0xf\n"
      "v_mfma_f32_16x16x32_bf16 v[88:91], a[140:143], v[132:135], v[88:91]\n"
      "v_exp_f32 v223, v223\n"
      "v_mfma_f32_16x16x32_bf16 v[212:215], a[176:179], v[144:147], 0\n"
      "v_mul_f32_dpp v198, v178, v198 row_newbcast:8 row_mask:0xf bank_mask:0xf\n"
      "v_mul_f32_dpp v190, v178, v190 row_newbcast:8 row_mask:0xf bank_mask:0xf\n"
      "v_mfma_f32_16x16x32_bf16 v[212:215], a[180:183], v[104:107], v[212:215]\n"
      "v_exp_f32 v163, v163\n"
      "v_mfma_f32_16x16x32_bf16 v[212:215], a[184:187], v[172:175], v[212:215]\n"
      "v_mul_f32_dpp v195, v178, v195 row_newbcast:12 row_mask:0xf bank_mask:0xf\n"
      "v_cvt_pk_bf16_f32 v247, v90, v91\n"
      "v_mfma_f32_16x16x32_bf16 v[212:215], a[188:191], v[132:135], v[212:215]\n"
      "s_not_b64 vcc, vcc\n"
      "v_mul_f32_dpp v194, v178, v194 row_newbcast:8 row_mask:0xf bank_mask:0xf\n"
      "v_cvt_pk_bf16_f32 v248, v92, v93\n"
      "v_mfma_f32_16x16x32_bf16 v[92:95], a[160:163], v[144:147], 0\n"
      "v_exp_f32 v160, v160\n"
      "v_mfma_f32_16x16x32_bf16 v[92:95], a[164:167], v[104:107], v[92:95]\n"
      "v_cvt_pk_bf16_f32 v246, v88, v89\n"
      "v_mul_f32_dpp v202, v178, v202 row_newbcast:8 row_mask:0xf bank_mask:0xf\n"
      "v_mfma_f32_16x16x32_bf16 v[92:95], a[168:171], v[172:175], v[92:95]\n"
      "v_cvt_pk_bf16_f32 v252, v212, v213\n"
      "v_cvt_pk_bf16_f32 v253, v214, v215\n"
      "v_mfma_f32_16x16x32_bf16 v[92:95], a[172:175], v[132:135], v[92:95]\n"
      "s_add_u32 m0, s53, 0\n"
      "v_exp_f32 v162, v162\n"
      "v_mfma_f32_16x16x32_bf16 v[188:191], v[128:131], a[192:195], v[188:191]\n"
      "buffer_load_dwordx4 v1, s[40:43], s94 offen offset:2048 lds\n"
      "v_mfma_f32_16x16x32_bf16 v[188:191], v[152:155], a[196:199], v[188:191]\n"
      "v_exp_f32 v42, v42\n"
      "v_mfma_f32_16x16x32_bf16 v[188:191], v[148:151], a[200:203], v[188:191]\n"
      "s_nop 0\n"
      "v_cvt_pk_bf16_f32 v250, v92, v93\n"
      "v_cvt_pk_bf16_f32 v251, v94, v95\n"
      "v_mfma_f32_16x16x32_bf16 v[188:191], v[224:227], a[204:207], v[188:191]\n"
      "v_cvt_pk_bf16_f32 v213, v42, v43\n"
      "v_mul_f32_dpp v199, v178, v199 row_newbcast:12 row_mask:0xf bank_mask:0xf\n"
      "v_smfmac_f32_16x16x64_bf16 v[48:51], v[44:47], v[246:253], v5\n"
      "v_cvt_pk_bf16_f32 v215, v38, v39\n"
      "v_mfma_f32_16x16x32_bf16 v[200:203], v[128:131], a[240:243], v[200:203]\n"
      "v_mul_f32_dpp v193, v178, v193 row_newbcast:4 row_mask:0xf bank_mask:0xf\n"
      "v_mul_f32_dpp v196, v178, v196 row_newbcast:0 row_mask:0xf bank_mask:0xf\n"
      "v_mfma_f32_16x16x32_bf16 v[200:203], v[152:155], a[244:247], v[200:203]\n"
      "v_exp_f32 v40, v40\n"
      "v_mfma_f32_16x16x32_bf16 v[200:203], v[148:151], a[248:251], v[200:203]\n"
      "v_exp_f32 v41, v41\n"
      "v_mfma_f32_16x16x32_bf16 v[200:203], v[224:227], a[252:255], v[200:203]\n"
      "v_add_f32 v50, v50, v51\n"
      "v_add_f32 v48, v48, v49\n"
      "v_mfma_f32_16x16x32_bf16 v[192:195], v[128:131], a[208:211], v[192:195]\n"
      "v_exp_f32 v48, v48\n"
      "v_mfma_f32_16x16x32_bf16 v[192:195], v[152:155], a[212:215], v[192:195]\n"
      "v_exp_f32 v50, v50\n"
      "v_mfma_f32_16x16x32_bf16 v[192:195], v[148:151], a[216:219], v[192:195]\n"
      "v_cvt_pk_bf16_f32 v242, v48, v50\n"
      "v_cvt_pk_bf16_f32 v243, v48, v50\n"
      "v_mfma_f32_16x16x32_bf16 v[192:195], v[224:227], a[220:223], v[192:195]\n"
      "v_add_f32 v88, v48, v50\n"
      "v_add_f32 v89, v48, v50\n"
      "v_mfma_f32_16x16x32_bf16 v[196:199], v[128:131], a[224:227], v[196:199]\n"
      "s_nop 0\n"
      "v_permlane16_swap_b32 v88, v89\n"
      "v_mfma_f32_16x16x32_bf16 v[196:199], v[152:155], a[228:231], v[196:199]\n"
      "v_permlane16_swap_b32 v242, v243\n"
      "v_mfma_f32_16x16x32_bf16 v[196:199], v[148:151], a[232:235], v[196:199]\n"
      "v_cvt_pk_bf16_f32 v212, v40, v41\n"
      "v_cvt_pk_bf16_f32 v214, v36, v37\n"
      "v_mfma_f32_16x16x32_bf16 v[196:199], v[224:227], a[236:239], v[196:199]\n"
      "v_cvt_pk_bf16_f32 v224, v220, v221\n"
      "v_mov_b64 v[128:129], v[88:89]\n"
      "v_smfmac_f32_16x16x64_bf16 v[88:91], v[12:15], v[212:219], v5\n"
      "v_cvt_pk_bf16_f32 v226, v160, v161\n"
      "v_mfma_f32_16x16x4_4b_bf16 v[36:51], v[242:243], v[156:157], 0 cbsz:2 abid:0\n"
      "v_exp_f32 v222, v222\n"
      "v_cvt_pk_bf16_f32 v227, v162, v163\n"
      "v_cvt_pk_bf16_f32 v225, v222, v223\n"
      "v_cvt_pk_bf16_f32 v188, v188, v192\n"
      "v_cvt_pk_bf16_f32 v190, v190, v194\n"
      "v_smfmac_f32_16x16x32_bf16 v[88:91], v[12:13], v[224:227], v5\n"
      "s_nop 3\n"
      "v_mfma_f32_16x16x32_bf16 v[40:43], v[212:215], v[180:183], v[40:43]\n"
      "buffer_load_dwordx4 v1, s[40:43], s95 offen offset:1024 lds\n"
      "v_mfma_f32_16x16x32_bf16 v[40:43], v[216:219], v[184:187], v[40:43]\n"
      "v_pk_max_i16 v244, v244, 0\n"
      "v_add_f32 v88, v88, v89\n"
      "v_mfma_f32_16x16x32_bf16 v[40:43], v[224:227], v[120:123], v[40:43]\n"
      "v_rcp_f32 v89, v88\n"
      "v_mfma_f32_16x16x32_bf16 v[48:51], v[212:215], v[232:235], v[48:51]\n"
      "ds_bpermute_b32 v90, v4, v89\n"
      "v_mfma_f32_16x16x32_bf16 v[48:51], v[216:219], v[236:239], v[48:51]\n"
      "v_cvt_pk_bf16_f32 v192, v189, v193\n"
      "v_cvt_pk_bf16_f32 v194, v191, v195\n"
      "v_mfma_f32_16x16x32_bf16 v[48:51], v[224:227], v[228:231], v[48:51]\n"
      "v_pk_max_i16 v192, v192, 0\n"
      "v_cvt_pk_bf16_f32 v189, v196, v200\n"
      "v_mfma_f32_16x16x32_bf16 v[36:39], v[212:215], v[108:111], v[36:39]\n"
      "v_pk_max_i16 v194, v194, 0\n"
      "v_cvt_pk_bf16_f32 v195, v199, v203\n"
      "v_mfma_f32_16x16x32_bf16 v[36:39], v[216:219], v[112:115], v[36:39]\n"
      "v_cvt_pk_bf16_f32 v193, v197, v201\n"
      "v_pk_max_i16 v31, v31, 0\n"
      "v_mfma_f32_16x16x32_bf16 v[36:39], v[224:227], v[52:55], v[36:39]\n"
      "v_mfma_f32_16x16x32_bf16 v[44:47], v[212:215], v[56:59], v[44:47]\n"
      "buffer_load_dwordx4 v1, s[40:43], s94 offen offset:0 lds\n"
      "v_mfma_f32_16x16x32_bf16 v[44:47], v[216:219], v[60:63], v[44:47]\n"
      "v_pk_max_i16 v189, v189, 0\n"
      "v_mfma_f32_16x16x32_bf16 v[44:47], v[224:227], v[116:119], v[44:47]\n"
      "buffer_store_dwordx2 v[244:245], v2, s[44:47], s75 offen offset:256 nt sc1\n"
      "v_cvt_pk_bf16_f32 v191, v198, v202\n"
      "v_mfma_f32_16x16x4_4b_bf16 v[212:227], v[242:243], v[156:157], 0 cbsz:2 abid:2\n"
      "s_waitcnt lgkmcnt(0)\n"
      "v_mul_f32_dpp v51, v90, v51 row_newbcast:12 row_mask:0xf bank_mask:0xf\n"
      "v_mul_f32_dpp v48, v90, v48 row_newbcast:0 row_mask:0xf bank_mask:0xf\n"
      "v_mul_f32_dpp v43, v90, v43 row_newbcast:12 row_mask:0xf bank_mask:0xf\n"
      "v_mul_f32_dpp v50, v90, v50 row_newbcast:8 row_mask:0xf bank_mask:0xf\n"
      "v_mul_f32_dpp v49, v90, v49 row_newbcast:4 row_mask:0xf bank_mask:0xf\n"
      "v_mul_f32_dpp v45, v90, v45 row_newbcast:4 row_mask:0xf bank_mask:0xf\n"
      "v_mul_f32_dpp v46, v90, v46 row_newbcast:8 row_mask:0xf bank_mask:0xf\n"
      "v_pk_max_i16 v191, v191, 0\n"
      "v_mfma_f32_16x16x32_bf16 v[48:51], v[100:103], a[240:243], v[48:51]\n"
      "v_pk_max_i16 v34, v34, 0\n"
      "v_mul_f32_dpp v41, v90, v41 row_newbcast:4 row_mask:0xf bank_mask:0xf\n"
      "v_mfma_f32_16x16x32_bf16 v[48:51], v[72:75], a[244:247], v[48:51]\n"
      "v_mul_f32_dpp v37, v90, v37 row_newbcast:4 row_mask:0xf bank_mask:0xf\n"
      "v_mul_f32_dpp v39, v90, v39 row_newbcast:12 row_mask:0xf bank_mask:0xf\n"
      "v_mfma_f32_16x16x32_bf16 v[48:51], v[84:87], a[248:251], v[48:51]\n"
      "v_mul_f32_dpp v40, v90, v40 row_newbcast:0 row_mask:0xf bank_mask:0xf\n"
      "v_mul_f32_dpp v42, v90, v42 row_newbcast:8 row_mask:0xf bank_mask:0xf\n"
      "v_mfma_f32_16x16x32_bf16 v[48:51], v[124:127], a[252:255], v[48:51]\n"
      "v_pk_max_i16 v33, v33, 0\n"
      "v_mfma_f32_16x16x32_bf16 v[40:43], v[100:103], a[208:211], v[40:43]\n"
      "v_maximum3_f32 v8, v8, v176, v177\n"
      "v_pk_max_i16 v30, v30, 0\n"
      "v_mfma_f32_16x16x32_bf16 v[40:43], v[72:75], a[212:215], v[40:43]\n"
      "v_mul_f32_dpp v44, v90, v44 row_newbcast:0 row_mask:0xf bank_mask:0xf\n"
      "v_pk_max_i16 v28, v28, 0\n"
      "v_mfma_f32_16x16x32_bf16 v[40:43], v[84:87], a[216:219], v[40:43]\n"
      "v_mul_f32_dpp v47, v90, v47 row_newbcast:12 row_mask:0xf bank_mask:0xf\n"
      "v_maximum3_f32 v8, v8, v88, v89\n"
      "v_mfma_f32_16x16x32_bf16 v[40:43], v[124:127], a[220:223], v[40:43]\n"
      "v_pk_max_i16 v190, v190, 0\n"
      "v_add_u32 v10, 64, v10\n"
      "v_mfma_f32_16x16x32_bf16 v[44:47], v[100:103], a[224:227], v[44:47]\n"
      "v_mul_f32_dpp v36, v90, v36 row_newbcast:0 row_mask:0xf bank_mask:0xf\n"
      "v_mul_f32_dpp v38, v90, v38 row_newbcast:8 row_mask:0xf bank_mask:0xf\n"
      "v_mfma_f32_16x16x32_bf16 v[44:47], v[72:75], a[228:231], v[44:47]\n"
      "L_h5_%=:\n"
      "s_cbranch_execz L_hr_%=\n"
      "v_add_u32 v11, 0x200, v11\n"
      "v_mfma_f32_16x16x32_bf16 v[44:47], v[84:87], a[232:235], v[44:47]\n"
      "s_add_u32 s44, s44, 13107200\n s_addc_u32 s45, s45, 0\n"
      "v_pk_max_i16 v193, v193, 0\n"
      "v_mfma_f32_16x16x32_bf16 v[44:47], v[124:127], a[236:239], v[44:47]\n"
      "v_pk_max_i16 v195, v195, 0\n"
      "v_pk_max_i16 v32, v32, 0\n"
      "v_mfma_f32_16x16x32_bf16 v[36:39], v[100:103], a[192:195], v[36:39]\n"
      "v_pk_max_i16 v188, v188, 0\n"
      "v_mfma_f32_16x16x32_bf16 v[36:39], v[72:75], a[196:199], v[36:39]\n"
      "buffer_load_dwordx4 v1, s[40:43], s95 offen offset:3072 lds\n"
      "v_mfma_f32_16x16x32_bf16 v[36:39], v[84:87], a[200:203], v[36:39]\n"
      "s_add_u32 s40, s40, 13107200\n s_addc_u32 s41, s41, 0\n"
      "s_add_u32 s38, s38, s38\n"
      "v_mfma_f32_16x16x32_bf16 v[36:39], v[124:127], a[204:207], v[36:39]\n"
      "s_cbranch_scc0 L_task_%=\n"
      "s_cmp_lg_u32 s38, 0\n"
      "s_cbranch_scc1 L_e5_%=\n"
      "L_ln_%=:\n"
      "v_mfma_f32_16x16x32_bf16 v[88:91], v[204:207], v[246:249], 0\n"
      "s_waitcnt vmcnt(40)\n"
      "ds_read_b128 v[92:95], v0 offset:20480\n"
      "ds_read_b128 v[84:87], v0 offset:21504\n"
      "v_mfma_f32_16x16x32_bf16 v[88:91], v[208:211], v[250:253], v[88:91]\n"
      "s_waitcnt vmcnt(30)\n"
      "ds_read_b128 v[72:75], v0 offset:16384\n"
      "ds_read_b128 v[204:207], v0 offset:17920\n"
      "v_mfma_f32_16x16x32_bf16 v[124:127], v[164:167], v[246:249], 0\n"
      "ds_read_b128 v[148:151], v0 offset:16896\n"
      "ds_read_b128 v[164:167], v0 offset:17408\n"
      "v_mfma_f32_16x16x32_bf16 v[124:127], v[168:171], v[250:253], v[124:127]\n"
      "buffer_store_dwordx2 v[28:29], v2, s[44:47], s100 offen offset:0 nt sc1\n"
      "v_mfma_f32_16x16x32_bf16 v[208:211], v[76:79], v[246:249], 0\n"
      "v_mfma_f32_16x16x32_bf16 v[208:211], v[80:83], v[250:253], v[208:211]\n"
      "ds_read_b128 v[76:79], v0 offset:20992\n"
      "ds_read_b128 v[80:83], v0 offset:22016\n"
      "v_mfma_f32_16x16x32_bf16 v[156:159], v[64:67], v[246:249], 0\n"
      "v_cvt_pk_bf16_f32 v38, v38, v42\n"
      "v_cvt_pk_bf16_f32 v36, v36, v40\n"
      "v_mfma_f32_16x16x32_bf16 v[156:159], v[68:71], v[250:253], v[156:159]\n"
      "v_exp_f32 v89, v89\n"
      "v_mfma_f32_16x16x32_bf16 v[240:243], v[20:23], v[246:249], 0\n"
      "v_exp_f32 v124, v124\n"
      "v_mfma_f32_16x16x32_bf16 v[240:243], v[24:27], v[250:253], v[240:243]\n"
      "v_exp_f32 v91, v91\n"
      "v_mfma_f32_16x16x32_bf16 v[176:179], v[136:139], v[246:249], 0\n"
      "v_mfma_f32_16x16x32_bf16 v[176:179], v[140:143], v[250:253], v[176:179]\n"
      "buffer_store_dwordx2 v[190:191], v2, s[44:47], s101 offen offset:512 nt sc1\n"
      "s_waitcnt lgkmcnt(5)\n"
      "v_mfma_f32_16x16x32_bf16 v[140:143], a[32:35], v[72:75], 0\n"
      "s_waitcnt lgkmcnt(3)\n"
      "v_mfma_f32_16x16x32_bf16 v[140:143], a[36:39], v[148:151], v[140:143]\n"
      "s_waitcnt lgkmcnt(2)\n"
      "v_mfma_f32_16x16x32_bf16 v[140:143], a[40:43], v[164:167], v[140:143]\n"
      "v_cvt_pk_bf16_f32 v42, v39, v43\n"
      "v_mfma_f32_16x16x32_bf16 v[140:143], a[44:47], v[204:207], v[140:143]\n"
      "v_exp_f32 v240, v240\n"
      "v_mfma_f32_16x16x32_bf16 v[168:171], a[144:147], v[72:75], 0\n"
      "v_mfma_f32_16x16x32_bf16 v[168:171], a[148:151], v[148:151], v[168:171]\n"
      "v_exp_f32 v211, v211\n"
      "v_mfma_f32_16x16x32_bf16 v[168:171], a[152:155], v[164:167], v[168:171]\n"
      "v_exp_f32 v179, v179\n"
      "v_mfma_f32_16x16x32_bf16 v[168:171], a[156:159], v[204:207], v[168:171]\n"
      "v_exp_f32 v156, v156\n"
      "v_mfma_f32_16x16x32_bf16 v[160:163], a[32:35], v[92:95], 0\n"
      "v_pk_max_i16 v36, v36, 0\n"
      "s_waitcnt lgkmcnt(1)\n"
      "v_mfma_f32_16x16x32_bf16 v[160:163], a[36:39], v[76:79], v[160:163]\n"
      "v_mfma_f32_16x16x32_bf16 v[160:163], a[40:43], v[84:87], v[160:163]\n"
      "s_waitcnt lgkmcnt(0)\n"
      "v_mfma_f32_16x16x32_bf16 v[160:163], a[44:47], v[80:83], v[160:163]\n"
      "v_exp_f32 v208, v208\n"
      "v_mfma_f32_16x16x32_bf16 v[20:23], a[176:179], v[92:95], 0\n"
      "v_exp_f32 v127, v127\n"
      "v_mfma_f32_16x16x32_bf16 v[20:23], a[180:183], v[76:79], v[20:23]\n"
      "v_exp_f32 v157, v157\n"
      "v_mfma_f32_16x16x32_bf16 v[20:23], a[184:187], v[84:87], v[20:23]\n"
      "v_exp_f32 v243, v243\n"
      "v_mfma_f32_16x16x32_bf16 v[20:23], a[188:191], v[80:83], v[20:23]\n"
      "v_cvt_pk_bf16_f32 v100, v156, v157\n"
      "v_pk_max_i16 v42, v42, 0\n"
      "v_mfma_f32_16x16x32_bf16 v[196:199], a[48:51], v[72:75], 0\n"
      "v_exp_f32 v159, v159\n"
      "v_mfma_f32_16x16x32_bf16 v[196:199], a[52:55], v[148:151], v[196:199]\n"
      "v_exp_f32 v210, v210\n"
      "v_mfma_f32_16x16x32_bf16 v[196:199], a[56:59], v[164:167], v[196:199]\n"
      "v_exp_f32 v158, v158\n"
      "v_mfma_f32_16x16x32_bf16 v[196:199], a[60:63], v[204:207], v[196:199]\n"
      "v_exp_f32 v126, v126\n"
      "v_mfma_f32_16x16x32_bf16 v[26:29], a[0:3], v[92:95], 0\n"
      "v_mfma_f32_16x16x32_bf16 v[26:29], a[4:7], v[76:79], v[26:29]\n"
      "v_exp_f32 v241, v241\n"
      "v_mfma_f32_16x16x32_bf16 v[26:29], a[8:11], v[84:87], v[26:29]\n"
      "v_cvt_pk_bf16_f32 v101, v158, v159\n"
      "v_cvt_pk_bf16_f32 v40, v37, v41\n"
      "v_mfma_f32_16x16x32_bf16 v[26:29], a[12:15], v[80:83], v[26:29]\n"
      "buffer_store_dwordx2 v[192:193], v2, s[44:47], s101 offen offset:256 nt sc1\n"
      "v_cvt_pk_bf16_f32 v39, v46, v50\n"
      "v_mfma_f32_16x16x32_bf16 v[244:247], a[16:19], v[92:95], 0\n"
      "v_cvt_pk_bf16_f32 v252, v240, v241\n"
      "v_pk_max_i16 v39, v39, 0\n"
      "v_mfma_f32_16x16x32_bf16 v[244:247], a[20:23], v[76:79], v[244:247]\n"
      "v_exp_f32 v209, v209\n"
      "v_mfma_f32_16x16x32_bf16 v[244:247], a[24:27], v[84:87], v[244:247]\n"
      "v_exp_f32 v178, v178\n"
      "v_mfma_f32_16x16x32_bf16 v[244:247], a[28:31], v[80:83], v[244:247]\n"
      "v_exp_f32 v177, v177\n"
      "v_mfma_f32_16x16x32_bf16 v[152:155], a[128:131], v[92:95], 0\n"
      "v_mfma_f32_16x16x32_bf16 v[152:155], a[132:135], v[76:79], v[152:155]\n"
      "v_exp_f32 v176, v176\n"
      "v_mfma_f32_16x16x32_bf16 v[152:155], a[136:139], v[84:87], v[152:155]\n"
      "v_exp_f32 v88, v88\n"
      "v_mfma_f32_16x16x32_bf16 v[152:155], a[140:143], v[80:83], v[152:155]\n"
      "v_exp_f32 v90, v90\n"
      "v_mfma_f32_16x16x32_bf16 v[156:159], a[48:51], v[92:95], 0\n"
      "v_cvt_pk_bf16_f32 v251, v90, v91\n"
      "v_cvt_pk_bf16_f32 v254, v208, v209\n"
      "v_mfma_f32_16x16x32_bf16 v[156:159], a[52:55], v[76:79], v[156:159]\n"
      "v_mfma_f32_16x16x32_bf16 v[156:159], a[56:59], v[84:87], v[156:159]\n"
      "v_cvt_pk_bf16_f32 v68, v160, v161\n"
      "v_mfma_f32_16x16x32_bf16 v[156:159], a[60:63], v[80:83], v[156:159]\n"
      "v_cvt_pk_bf16_f32 v103, v178, v179\n"
      "v_cvt_pk_bf16_f32 v255, v210, v211\n"
      "v_mfma_f32_16x16x32_bf16 v[136:139], a[176:179], v[72:75], 0\n"
      "v_exp_f32 v242, v242\n"
      "v_mfma_f32_16x16x32_bf16 v[136:139], a[180:183], v[148:151], v[136:139]\n"
      "v_cvt_pk_bf16_f32 v241, v142, v143\n"
      "v_cvt_pk_bf16_f32 v250, v88, v89\n"
      "v_mfma_f32_16x16x32_bf16 v[136:139], a[184:187], v[164:167], v[136:139]\n"
      "v_cvt_pk_bf16_f32 v240, v140, v141\n"
      "v_cvt_pk_bf16_f32 v102, v176, v177\n"
      "v_mfma_f32_16x16x32_bf16 v[136:139], a[188:191], v[204:207], v[136:139]\n"
      "v_exp_f32 v125, v125\n"
      "v_mfma_f32_16x16x32_bf16 v[88:91], a[128:131], v[72:75], 0\n"
      "v_mfma_f32_16x16x32_bf16 v[88:91], a[132:135], v[148:151], v[88:91]\n"
      "v_cvt_pk_bf16_f32 v249, v126, v127\n"
      "v_mfma_f32_16x16x32_bf16 v[88:91], a[136:139], v[164:167], v[88:91]\n"
      "v_mfma_f32_16x16x32_bf16 v[88:91], a[140:143], v[204:207], v[88:91]\n"
      "buffer_store_dwordx2 v[30:31], v2, s[44:47], s100 offen offset:512 nt sc1\n"
      "v_cvt_pk_bf16_f32 v253, v242, v243\n"
      "v_mfma_f32_16x16x32_bf16 v[200:203], a[160:163], v[92:95], 0\n"
      "s_waitcnt vmcnt(17)\n"
      "ds_read_b128 v[190:193], v0 offset:8192\n"
      "v_mfma_f32_16x16x32_bf16 v[200:203], a[164:167], v[76:79], v[200:203]\n"
      "v_cvt_pk_bf16_f32 v69, v162, v163\n"
      "v_mfma_f32_16x16x32_bf16 v[200:203], a[168:171], v[84:87], v[200:203]\n"
      "v_cvt_pk_bf16_f32 v248, v124, v125\n"
      "v_mfma_f32_16x16x32_bf16 v[200:203], a[172:175], v[80:83], v[200:203]\n"
      "ds_read_b128 v[124:127], v9 offset:2048\n"
      "v_pk_max_i16 v40, v40, 0\n"
      "v_smfmac_f32_16x16x64_bf16 v[128:131], v[12:15], v[248:255], v5\n"
      "ds_read_b128 v[140:143], v0 offset:9216\n"
      "v_cvt_pk_bf16_f32 v243, v198, v199\n"
      "v_mfma_f32_16x16x32_bf16 v[220:223], v[248:251], v[56:59], v[220:223]\n"
      "ds_read_b128 v[208:211], v0 offset:9728\n"
      "v_mfma_f32_16x16x32_bf16 v[220:223], v[252:255], v[60:63], v[220:223]\n"
      "v_mfma_f32_16x16x32_bf16 v[220:223], v[100:103], v[116:119], v[220:223]\n"
      "v_cvt_pk_bf16_f32 v31, v22, v23\n"
      "v_smfmac_f32_16x16x32_bf16 v[128:131], v[12:13], v[100:103], v5\n"
      "buffer_store_dwordx2 v[194:195], v2, s[44:47], s101 offen offset:768 nt sc1\n"
      "v_mfma_f32_16x16x32_bf16 v[216:219], v[248:251], v[180:183], v[216:219]\n"
      "v_cvt_pk_bf16_f32 v65, v28, v29\n"
      "v_cvt_pk_bf16_f32 v43, v47, v51\n"
      "v_mfma_f32_16x16x32_bf16 v[216:219], v[252:255], v[184:187], v[216:219]\n"
      "v_cvt_pk_bf16_f32 v41, v45, v49\n"
      "v_mfma_f32_16x16x32_bf16 v[216:219], v[100:103], v[120:123], v[216:219]\n"
      "ds_read_b128 v[176:179], v0 offset:8704\n"
      "v_add_f32 v128, v128, v129\n"
      "v_mfma_f32_16x16x32_bf16 v[224:227], v[248:251], v[232:235], v[224:227]\n"
      "v_mfma_f32_16x16x32_bf16 v[224:227], v[252:255], v[236:239], v[224:227]\n"
      "v_rcp_f32 v129, v128\n"
      "v_mfma_f32_16x16x32_bf16 v[224:227], v[100:103], v[228:231], v[224:227]\n"
      "ds_bpermute_b32 v130, v4, v129 offset:128\n"
      "v_mfma_f32_16x16x32_bf16 v[232:235], a[144:147], v[92:95], 0\n"
      "v_mfma_f32_16x16x32_bf16 v[232:235], a[148:151], v[76:79], v[232:235]\n"
      "v_mfma_f32_16x16x32_bf16 v[232:235], a[152:155], v[84:87], v[232:235]\n"
      "ds_read_b128 v[116:119], v10 offset:0\n"
      "v_mfma_f32_16x16x32_bf16 v[232:235], a[156:159], v[80:83], v[232:235]\n"
      "ds_read_b128 v[120:123], v9 offset:0\n"
      "v_mfma_f32_16x16x32_bf16 v[212:215], v[248:251], v[108:111], v[212:215]\n"
      "v_cvt_pk_bf16_f32 v37, v44, v48\n"
      "v_mov_b64 v[44:45], 0\n"
      "v_mfma_f32_16x16x32_bf16 v[212:215], v[252:255], v[112:115], v[212:215]\n"
      "v_mfma_f32_16x16x32_bf16 v[212:215], v[100:103], v[52:55], v[212:215]\n"
      "v_cvt_pk_bf16_f32 v24, v152, v153\n"
      "v_mfma_f32_16x16x32_bf16 v[100:103], a[16:19], v[72:75], 0\n"
      "v_mfma_f32_16x16x32_bf16 v[100:103], a[20:23], v[148:151], v[100:103]\n"
      "v_cvt_pk_bf16_f32 v64, v26, v27\n"
      "v_cvt_pk_bf16_f32 v28, v200, v201\n"
      "v_mfma_f32_16x16x32_bf16 v[100:103], a[24:27], v[164:167], v[100:103]\n"
      "s_waitcnt lgkmcnt(2)\n"
      "v_mul_f32_dpp v216, v130, v216 row_newbcast:0 row_mask:0xf bank_mask:0xf\n"
      "v_mul_f32_dpp v217, v130, v217 row_newbcast:4 row_mask:0xf bank_mask:0xf\n"
      "v_mfma_f32_16x16x32_bf16 v[100:103], a[28:31], v[204:207], v[100:103]\n"
      "v_cvt_pk_bf16_f32 v67, v246, v247\n"
      "v_mul_f32_dpp v214, v130, v214 row_newbcast:8 row_mask:0xf bank_mask:0xf\n"
      "v_mfma_f32_16x16x32_bf16 v[252:255], a[160:163], v[72:75], 0\n"
      "v_mul_f32_dpp v212, v130, v212 row_newbcast:0 row_mask:0xf bank_mask:0xf\n"
      "v_cvt_pk_bf16_f32 v30, v20, v21\n"
      "v_mfma_f32_16x16x32_bf16 v[252:255], a[164:167], v[148:151], v[252:255]\n"
      "v_mul_f32_dpp v225, v130, v225 row_newbcast:4 row_mask:0xf bank_mask:0xf\n"
      "v_mfma_f32_16x16x32_bf16 v[252:255], a[168:171], v[164:167], v[252:255]\n"
      "v_pk_max_i16 v37, v37, 0\n"
      "v_mul_f32_dpp v223, v130, v223 row_newbcast:12 row_mask:0xf bank_mask:0xf\n"
      "v_mfma_f32_16x16x32_bf16 v[252:255], a[172:175], v[204:207], v[252:255]\n"
      "buffer_store_dwordx2 v[36:37], v2, s[44:47], s34 offen offset:0 nt sc1\n"
      "v_cvt_pk_bf16_f32 v238, v100, v101\n"
      "v_mfma_f32_16x16x32_bf16 v[48:51], a[0:3], v[72:75], 0\n"
      "v_cvt_pk_bf16_f32 v29, v202, v203\n"
      "v_mfma_f32_16x16x32_bf16 v[48:51], a[4:7], v[148:151], v[48:51]\n"
      "v_mul_f32_dpp v219, v130, v219 row_newbcast:12 row_mask:0xf bank_mask:0xf\n"
      "v_mfma_f32_16x16x32_bf16 v[48:51], a[8:11], v[164:167], v[48:51]\n"
      "v_mul_f32_dpp v213, v130, v213 row_newbcast:4 row_mask:0xf bank_mask:0xf\n"
      "v_mul_f32_dpp v227, v130, v227 row_newbcast:12 row_mask:0xf bank_mask:0xf\n"
      "v_mfma_f32_16x16x32_bf16 v[48:51], a[12:15], v[204:207], v[48:51]\n"
      "v_cvt_pk_bf16_f32 v58, v136, v137\n"
      "v_cvt_pk_bf16_f32 v27, v234, v235\n"
      "v_mfma_f32_16x16x32_bf16 v[228:231], v[190:193], a[96:99], 0\n"
      "v_mul_f32_dpp v224, v130, v224 row_newbcast:0 row_mask:0xf bank_mask:0xf\n"
      "v_mfma_f32_16x16x32_bf16 v[228:231], v[176:179], a[100:103], v[228:231]\n"
      "v_mov_b64 v[246:247], 0\n"
      "v_pk_max_i16 v38, v38, 0\n"
      "v_mfma_f32_16x16x32_bf16 v[228:231], v[140:143], a[104:107], v[228:231]\n"
      "v_pk_max_i16 v41, v41, 0\n"
      "v_mfma_f32_16x16x32_bf16 v[228:231], v[208:211], a[108:111], v[228:231]\n"
      "v_mul_f32_dpp v220, v130, v220 row_newbcast:0 row_mask:0xf bank_mask:0xf\n"
      "v_mfma_f32_16x16x32_bf16 v[184:187], v[190:193], a[80:83], 0\n"
      "v_mul_f32_dpp v222, v130, v222 row_newbcast:8 row_mask:0xf bank_mask:0xf\n"
      "v_mfma_f32_16x16x32_bf16 v[184:187], v[176:179], a[84:87], v[184:187]\n"
      "v_mfma_f32_16x16x32_bf16 v[184:187], v[140:143], a[88:91], v[184:187]\n"
      "v_mov_b64 v[152:153], 0\n"
      "v_mfma_f32_16x16x32_bf16 v[184:187], v[208:211], a[92:95], v[184:187]\n"
      "v_cvt_pk_bf16_f32 v71, v158, v159\n"
      "v_mfma_f32_16x16x32_bf16 v[20:23], v[190:193], a[112:115], 0\n"
      "v_cvt_pk_bf16_f32 v57, v254, v255\n"
      "v_mul_f32_dpp v218, v130, v218 row_newbcast:8 row_mask:0xf bank_mask:0xf\n"
      "v_mfma_f32_16x16x32_bf16 v[20:23], v[176:179], a[116:119], v[20:23]\n"
      "v_mul_f32_dpp v215, v130, v215 row_newbcast:12 row_mask:0xf bank_mask:0xf\n"
      "v_cvt_pk_bf16_f32 v136, v228, v229\n"
      "v_mfma_f32_16x16x32_bf16 v[20:23], v[140:143], a[120:123], v[20:23]\n"
      "v_cvt_pk_bf16_f32 v236, v48, v49\n"
      "v_mfma_f32_16x16x32_bf16 v[20:23], v[208:211], a[124:127], v[20:23]\n"
      "v_cvt_pk_bf16_f32 v55, v170, v171\n"
      "v_mul_f32_dpp v226, v130, v226 row_newbcast:8 row_mask:0xf bank_mask:0xf\n"
      "v_mfma_f32_16x16x32_bf16 v[212:215], v[144:147], a[192:195], v[212:215]\n"
      "v_cvt_pk_bf16_f32 v66, v244, v245\n"
      "v_cvt_pk_bf16_f32 v237, v50, v51\n"
      "v_mfma_f32_16x16x32_bf16 v[212:215], v[104:107], a[196:199], v[212:215]\n"
      "v_mfma_f32_16x16x32_bf16 v[212:215], v[172:175], a[200:203], v[212:215]\n"
      "v_cvt_pk_bf16_f32 v239, v102, v103\n"
      "v_mfma_f32_16x16x32_bf16 v[212:215], v[132:135], a[204:207], v[212:215]\n"
      "v_cvt_pk_bf16_f32 v53, v90, v91\n"
      "v_cvt_pk_bf16_f32 v242, v196, v197\n"
      "v_mfma_f32_16x16x32_bf16 v[248:251], a[32:35], v[190:193], 0\n"
      "v_cvt_pk_bf16_f32 v26, v232, v233\n"
      "v_mfma_f32_16x16x32_bf16 v[248:251], a[36:39], v[176:179], v[248:251]\n"
      "v_mfma_f32_16x16x32_bf16 v[248:251], a[40:43], v[140:143], v[248:251]\n"
      "v_mfma_f32_16x16x32_bf16 v[248:251], a[44:47], v[208:211], v[248:251]\n"
      "buffer_store_dwordx2 v[40:41], v2, s[44:47], s34 offen offset:256 nt sc1\n"
      "v_maximum3_f32 v8, v8, v128, v129\n"
      "v_mfma_f32_16x16x32_bf16 v[224:227], v[144:147], a[240:243], v[224:227]\n"
      "v_mfma_f32_16x16x32_bf16 v[224:227], v[104:107], a[244:247], v[224:227]\n"
      "v_mfma_f32_16x16x32_bf16 v[224:227], v[172:175], a[248:251], v[224:227]\n"
      "v_mfma_f32_16x16x32_bf16 v[224:227], v[132:135], a[252:255], v[224:227]\n"
      "ds_read_b128 v[232:235], v0 offset:12288\n"
      "v_cvt_pk_bf16_f32 v70, v156, v157\n"
      "v_mfma_f32_16x16x32_bf16 v[216:219], v[144:147], a[208:211], v[216:219]\n"
      "s_waitcnt vmcnt(13)\n"
      "ds_read_b128 v[156:159], v0 offset:4096\n"
      "ds_read_b128 v[48:51], v0 offset:5632\n"
      "v_mfma_f32_16x16x32_bf16 v[216:219], v[104:107], a[212:215], v[216:219]\n"
      "v_cvt_pk_bf16_f32 v108, v248, v249\n"
      "v_mul_f32_dpp v221, v130, v221 row_newbcast:4 row_mask:0xf bank_mask:0xf\n"
      "v_mfma_f32_16x16x32_bf16 v[216:219], v[172:175], a[216:219], v[216:219]\n"
      "v_mfma_f32_16x16x32_bf16 v[216:219], v[132:135], a[220:223], v[216:219]\n"
      "v_cvt_pk_bf16_f32 v25, v154, v155\n"
      "v_mfma_f32_16x16x32_bf16 v[100:103], a[0:3], v[190:193], 0\n"
      "ds_read2_b32 v[112:113], v16 offset0:0 offset1:2\n"
      "v_mfma_f32_16x16x32_bf16 v[100:103], a[4:7], v[176:179], v[100:103]\n"
      "v_mfma_f32_16x16x32_bf16 v[100:103], a[8:11], v[140:143], v[100:103]\n"
      "ds_read_b128 v[194:197], v0 offset:5120\n"
      "v_mfma_f32_16x16x32_bf16 v[100:103], a[12:15], v[208:211], v[100:103]\n"
      "s_waitcnt lgkmcnt(6)\n"
      "v_smfmac_f32_16x16x64_bf16 v[152:155], v[116:119], v[236:243], v5\n"
      "buffer_store_dwordx2 v[38:39], v2, s[44:47], s34 offen offset:512 nt sc1\n"
      "v_mfma_f32_16x16x32_bf16 v[128:131], v[236:239], v[24:27], 0\n"
      "v_mfma_f32_16x16x32_bf16 v[128:131], v[240:243], v[28:31], v[128:131]\n"
      "ds_read_b128 v[180:183], v0 offset:13824\n"
      "v_cvt_pk_bf16_f32 v52, v88, v89\n"
      "v_mfma_f32_16x16x32_bf16 v[88:91], v[64:67], v[24:27], 0\n"
      "v_cvt_pk_bf16_f32 v214, v214, v218\n"
      "v_mfma_f32_16x16x32_bf16 v[88:91], v[68:71], v[28:31], v[88:91]\n"
      "s_waitcnt lgkmcnt(6)\n"
      "v_smfmac_f32_16x16x64_bf16 v[246:249], v[116:119], v[120:127], v5\n"
      "ds_read_b128 v[120:123], v0 offset:4608\n"
      "v_mfma_f32_16x16x32_bf16 v[200:203], a[48:51], v[190:193], 0\n"
      "v_cvt_pk_bf16_f32 v229, v22, v23\n"
      "v_cvt_pk_bf16_f32 v109, v250, v251\n"
      "v_mfma_f32_16x16x32_bf16 v[200:203], a[52:55], v[176:179], v[200:203]\n"
      "v_cvt_pk_bf16_f32 v59, v138, v139\n"
      "v_cvt_pk_bf16_f32 v212, v212, v216\n"
      "v_mfma_f32_16x16x32_bf16 v[200:203], a[56:59], v[140:143], v[200:203]\n"
      "v_cvt_pk_bf16_f32 v216, v213, v217\n"
      "v_add_f32 v152, v152, v153\n"
      "v_mfma_f32_16x16x32_bf16 v[200:203], a[60:63], v[208:211], v[200:203]\n"
      "v_cvt_pk_bf16_f32 v218, v215, v219\n"
      "v_cvt_pk_bf16_f32 v41, v186, v187\n"
      "v_mfma_f32_16x16x32_bf16 v[220:223], v[144:147], a[224:227], v[220:223]\n"
      "v_mfma_f32_16x16x32_bf16 v[220:223], v[104:107], a[228:231], v[220:223]\n"
      "v_cvt_pk_bf16_f32 v40, v184, v185\n"
      "v_mfma_f32_16x16x32_bf16 v[220:223], v[172:175], a[232:235], v[220:223]\n"
      "v_exp_f32 v129, v129\n"
      "v_mfma_f32_16x16x32_bf16 v[220:223], v[132:135], a[236:239], v[220:223]\n"
      "buffer_store_dwordx2 v[188:189], v2, s[44:47], s101 offen offset:0 nt sc1\n"
      "s_waitcnt lgkmcnt(5)\n"
      "v_mfma_f32_16x16x32_bf16 v[36:39], a[0:3], v[156:159], 0\n"
      "v_cvt_pk_bf16_f32 v111, v202, v203\n"
      "s_waitcnt lgkmcnt(0)\n"
      "v_mfma_f32_16x16x32_bf16 v[36:39], a[4:7], v[120:123], v[36:39]\n"
      "v_mfma_f32_16x16x32_bf16 v[36:39], a[8:11], v[194:197], v[36:39]\n"
      "v_exp_f32 v89, v89\n"
      "v_mfma_f32_16x16x32_bf16 v[36:39], a[12:15], v[48:51], v[36:39]\n"
      "v_cvt_pk_bf16_f32 v105, v102, v103\n"
      "v_mfma_f32_16x16x32_bf16 v[124:127], v[156:159], a[80:83], 0\n"
      "v_pk_max_i16 v216, v216, 0\n"
      "v_cvt_pk_bf16_f32 v215, v222, v226\n"
      "v_mfma_f32_16x16x32_bf16 v[124:127], v[120:123], a[84:87], v[124:127]\n"
      "v_mfma_f32_16x16x32_bf16 v[124:127], v[194:197], a[88:91], v[124:127]\n"
      "v_cvt_pk_bf16_f32 v219, v223, v227\n"
      "v_mfma_f32_16x16x32_bf16 v[124:127], v[48:51], a[92:95], v[124:127]\n"
      "v_pk_max_i16 v219, v219, 0\n"
      "v_cvt_pk_bf16_f32 v54, v168, v169\n"
      "v_mfma_f32_16x16x32_bf16 v[172:175], v[190:193], a[64:67], 0\n"
      "v_cvt_pk_bf16_f32 v110, v200, v201\n"
      "v_mfma_f32_16x16x32_bf16 v[172:175], v[176:179], a[68:71], v[172:175]\n"
      "v_mfma_f32_16x16x32_bf16 v[172:175], v[140:143], a[72:75], v[172:175]\n"
      "v_mfma_f32_16x16x32_bf16 v[172:175], v[208:211], a[76:79], v[172:175]\n"
      "v_exp_f32 v130, v130\n"
      "v_mfma_f32_16x16x32_bf16 v[200:203], a[48:51], v[156:159], 0\n"
      "v_mfma_f32_16x16x32_bf16 v[200:203], a[52:55], v[120:123], v[200:203]\n"
      "v_cvt_pk_bf16_f32 v213, v220, v224\n"
      "v_cvt_pk_bf16_f32 v185, v38, v39\n"
      "v_mfma_f32_16x16x32_bf16 v[200:203], a[56:59], v[194:197], v[200:203]\n"
      "s_nop 1\n"
      "v_cvt_pk_bf16_f32 v144, v172, v173\n"
      "v_mfma_f32_16x16x32_bf16 v[200:203], a[60:63], v[48:51], v[200:203]\n"
      "v_exp_f32 v88, v88\n"
      "v_mfma_f32_16x16x32_bf16 v[168:171], a[16:19], v[190:193], 0\n"
      "ds_read_b128 v[248:251], v0 offset:13312\n"
      "v_pk_max_i16 v213, v213, 0\n"
      "v_mfma_f32_16x16x32_bf16 v[168:171], a[20:23], v[176:179], v[168:171]\n"
      "v_exp_f32 v91, v91\n"
      "v_mfma_f32_16x16x32_bf16 v[168:171], a[24:27], v[140:143], v[168:171]\n"
      "v_cvt_pk_bf16_f32 v56, v252, v253\n"
      "v_cvt_pk_bf16_f32 v39, v126, v127\n"
      "v_mfma_f32_16x16x32_bf16 v[168:171], a[28:31], v[208:211], v[168:171]\n"
      "ds_read_b128 v[252:255], v0 offset:12800\n"
      "v_mfma_f32_16x16x32_bf16 v[176:179], a[16:19], v[156:159], 0\n"
      "v_cvt_pk_bf16_f32 v191, v202, v203\n"
      "v_mfma_f32_16x16x32_bf16 v[176:179], a[20:23], v[120:123], v[176:179]\n"
      "v_mfma_f32_16x16x32_bf16 v[176:179], a[24:27], v[194:197], v[176:179]\n"
      "v_cvt_pk_bf16_f32 v38, v124, v125\n"
      "v_cvt_pk_bf16_f32 v217, v221, v225\n"
      "v_mfma_f32_16x16x32_bf16 v[176:179], a[28:31], v[48:51], v[176:179]\n"
      "buffer_store_dwordx2 v[34:35], v2, s[44:47], s100 offen offset:768 nt sc1\n"
      "v_mfma_f32_16x16x32_bf16 v[208:211], a[32:35], v[156:159], 0\n"
      "v_cvt_pk_bf16_f32 v190, v200, v201\n"
      "v_cvt_pk_bf16_f32 v106, v168, v169\n"
      "v_mfma_f32_16x16x32_bf16 v[208:211], a[36:39], v[120:123], v[208:211]\n"
      "v_mfma_f32_16x16x32_bf16 v[208:211], a[40:43], v[194:197], v[208:211]\n"
      "v_mfma_f32_16x16x32_bf16 v[208:211], a[44:47], v[48:51], v[208:211]\n"
      "s_nop 0\n"
      "v_cvt_pk_bf16_f32 v187, v178, v179\n"
      "v_mov_b64 v[138:139], 0\n"
      "v_mfma_f32_16x16x32_bf16 v[140:143], a[0:3], v[232:235], 0\n"
      "v_cvt_pk_bf16_f32 v126, v88, v89\n"
      "s_waitcnt lgkmcnt(0)\n"
      "v_mfma_f32_16x16x32_bf16 v[140:143], a[4:7], v[252:255], v[140:143]\n"
      "v_cvt_pk_bf16_f32 v186, v176, v177\n"
      "v_mfma_f32_16x16x32_bf16 v[140:143], a[8:11], v[248:251], v[140:143]\n"
      "v_exp_f32 v90, v90\n"
      "v_mfma_f32_16x16x32_bf16 v[140:143], a[12:15], v[180:183], v[140:143]\n"
      "v_pk_max_i16 v43, v43, 0\n"
      "v_pk_max_i16 v212, v212, 0\n"
      "v_mfma_f32_16x16x32_bf16 v[132:135], v[232:235], a[80:83], 0\n"
      "v_mfma_f32_16x16x32_bf16 v[132:135], v[252:255], a[84:87], v[132:135]\n"
      "v_exp_f32 v128, v128\n"
      "v_mfma_f32_16x16x32_bf16 v[132:135], v[248:251], a[88:91], v[132:135]\n"
      "v_mfma_f32_16x16x32_bf16 v[132:135], v[180:183], a[92:95], v[132:135]\n"
      "v_cvt_pk_bf16_f32 v124, v128, v129\n"
      "v_smfmac_f32_16x16x64_bf16 v[44:47], v[116:119], v[64:71], v5\n"
      "buffer_store_dwordx2 v[42:43], v2, s[44:47], s34 offen offset:768 nt sc1\n"
      "v_mfma_f32_16x16x32_bf16 v[200:203], a[16:19], v[232:235], 0\n"
      "v_exp_f32 v152, v152\n"
      "v_mfma_f32_16x16x32_bf16 v[200:203], a[20:23], v[252:255], v[200:203]\n"
      "v_cvt_pk_bf16_f32 v104, v100, v101\n"
      "v_add_f32 v246, v246, v247\n"
      "v_mfma_f32_16x16x32_bf16 v[200:203], a[24:27], v[248:251], v[200:203]\n"
      "v_cvt_pk_bf16_f32 v43, v134, v135\n"
      "v_add_f32 v44, v44, v45\n"
      "v_mfma_f32_16x16x32_bf16 v[200:203], a[28:31], v[180:183], v[200:203]\n"
      "v_pk_max_i16 v217, v217, 0\n"
      "v_cvt_pk_bf16_f32 v107, v170, v171\n"
      "v_mfma_f32_16x16x32_bf16 v[224:227], a[48:51], v[232:235], 0\n"
      "v_exp_f32 v131, v131\n"
      "v_mfma_f32_16x16x32_bf16 v[224:227], a[52:55], v[252:255], v[224:227]\n"
      "v_mov_b32 v247, 0\n"
      "v_mfma_f32_16x16x32_bf16 v[224:227], a[56:59], v[248:251], v[224:227]\n"
      "v_cvt_pk_bf16_f32 v189, v210, v211\n"
      "v_cvt_pk_bf16_f32 v188, v208, v209\n"
      "v_mfma_f32_16x16x32_bf16 v[224:227], a[60:63], v[180:183], v[224:227]\n"
      "v_cvt_pk_bf16_f32 v184, v36, v37\n"
      "v_cvt_pk_bf16_f32 v170, v200, v201\n"
      "v_mfma_f32_16x16x32_bf16 v[208:211], v[156:159], a[96:99], 0\n"
      "s_waitcnt vmcnt(11)\n"
      "ds_read_b128 v[34:37], v0 offset:0\n"
      "v_cvt_pk_bf16_f32 v127, v90, v91\n"
      "v_mfma_f32_16x16x32_bf16 v[208:211], v[120:123], a[100:103], v[208:211]\n"
      "ds_read_b128 v[176:179], v0 offset:1536\n"
      "v_cvt_pk_bf16_f32 v125, v130, v131\n"
      "v_mfma_f32_16x16x32_bf16 v[208:211], v[194:197], a[104:107], v[208:211]\n"
      "ds_read_b128 v[220:223], v0 offset:1024\n"
      "v_cvt_pk_bf16_f32 v228, v20, v21\n"
      "v_mfma_f32_16x16x32_bf16 v[208:211], v[48:51], a[108:111], v[208:211]\n"
      "ds_read2_b32 v[114:115], v16 offset0:128 offset1:130\n"
      "v_cvt_pk_bf16_f32 v171, v202, v203\n"
      "v_mfma_f32_16x16x32_bf16 v[88:91], v[156:159], a[64:67], 0\n"
      "v_cvt_pk_bf16_f32 v42, v132, v133\n"
      "v_mfma_f32_16x16x32_bf16 v[88:91], v[120:123], a[68:71], v[88:91]\n"
      "v_exp_f32_dpp v247, v246 quad_perm:[0,1,2,3] row_mask:0xf bank_mask:0x1\n"
      "v_mfma_f32_16x16x32_bf16 v[88:91], v[194:197], a[72:75], v[88:91]\n"
      "v_cvt_pk_bf16_f32 v137, v230, v231\n"
      "v_mov_b64 v[20:21], 0\n"
      "v_mfma_f32_16x16x32_bf16 v[88:91], v[48:51], a[76:79], v[88:91]\n"
      "buffer_store_dwordx2 v[212:213], v2, s[44:47], s35 offen offset:0 nt sc1\n"
      "ds_read_b128 v[128:131], v0 offset:512\n"
      "v_mfma_f32_16x16x32_bf16 v[100:103], v[156:159], a[112:115], 0\n"
      "v_mfma_f32_16x16x32_bf16 v[100:103], v[120:123], a[116:119], v[100:103]\n"
      "v_cvt_pk_bf16_f32 v168, v140, v141\n"
      "v_cvt_pk_bf16_f32 v145, v174, v175\n"
      "v_mfma_f32_16x16x32_bf16 v[100:103], v[194:197], a[120:123], v[100:103]\n"
      "v_cvt_pk_bf16_f32 v174, v224, v225\n"
      "v_mfma_f32_16x16x32_bf16 v[100:103], v[48:51], a[124:127], v[100:103]\n"
      "v_pk_max_i16 v218, v218, 0\n"
      "v_smfmac_f32_16x16x64_bf16 v[138:141], v[116:119], v[184:191], v5\n"
      "v_mfma_f32_16x16x32_bf16 v[120:123], v[104:107], v[24:27], 0\n"
      "v_pk_max_i16 v215, v215, 0\n"
      "v_cvt_pk_bf16_f32 v135, v210, v211\n"
      "v_mfma_f32_16x16x32_bf16 v[120:123], v[108:111], v[28:31], v[120:123]\n"
      "s_waitcnt lgkmcnt(1)\n"
      "v_mov_b32_dpp v194, v114 row_ror:8 row_mask:0xf bank_mask:0xf\n"
      "v_mov_b32_dpp v246, v247 quad_perm:[1,1,3,3] row_mask:0xf bank_mask:0xf\n"
      "v_smfmac_f32_16x16x64_bf16 v[20:23], v[116:119], v[104:111], v5\n"
      "v_add_f32 v138, v138, v139\n"
      "v_mfma_f32_16x16x32_bf16 v[48:51], a[32:35], v[34:37], 0\n"
      "s_waitcnt lgkmcnt(0)\n"
      "v_mfma_f32_16x16x32_bf16 v[48:51], a[36:39], v[128:131], v[48:51]\n"
      "v_mfma_f32_16x16x32_bf16 v[48:51], a[40:43], v[220:223], v[48:51]\n"
      "v_cvt_pk_bf16_f32 v246, v247, v246\n"
      "v_cvt_pk_bf16_f32 v175, v226, v227\n"
      "v_mfma_f32_16x16x32_bf16 v[48:51], a[44:47], v[176:179], v[48:51]\n"
      "v_exp_f32 v44, v44\n"
      "v_mfma_f32_16x16x32_bf16 v[200:203], a[48:51], v[34:37], 0\n"
      "v_exp_f32 v121, v121\n"
      "v_mfma_f32_16x16x32_bf16 v[200:203], a[52:55], v[128:131], v[200:203]\n"
      "v_cvt_pk_bf16_f32 v169, v142, v143\n"
      "v_cndmask_b32_dpp v153, v44, v152, vcc quad_perm:[0,0,2,2] row_mask:0xf bank_mask:0xf\n"
      "v_mfma_f32_16x16x32_bf16 v[200:203], a[56:59], v[220:223], v[200:203]\n"
      "v_exp_f32 v120, v120\n"
      "v_mfma_f32_16x16x32_bf16 v[200:203], a[60:63], v[176:179], v[200:203]\n"
      "v_cvt_pk_bf16_f32 v227, v102, v103\n"
      "v_pk_max_i16 v214, v214, 0\n"
      "v_mfma_f32_16x16x32_bf16 v[156:159], a[16:19], v[34:37], 0\n"
      "v_cvt_pk_bf16_f32 v134, v208, v209\n"
      "v_cvt_pk_bf16_f32 v226, v100, v101\n"
      "v_mfma_f32_16x16x32_bf16 v[156:159], a[20:23], v[128:131], v[156:159]\n"
      "v_mfma_f32_16x16x32_bf16 v[156:159], a[24:27], v[220:223], v[156:159]\n"
      "v_cvt_pk_bf16_f32 v143, v90, v91\n"
      "v_cvt_pk_bf16_f32 v142, v88, v89\n"
      "v_mfma_f32_16x16x32_bf16 v[156:159], a[28:31], v[176:179], v[156:159]\n"
      "buffer_store_dwordx2 v[214:215], v2, s[44:47], s35 offen offset:512 nt sc1\n"
      "v_mov_b32_dpp v192, v112 row_ror:8 row_mask:0xf bank_mask:0xf\n"
      "v_mfma_f32_16x16x32_bf16 v[88:91], a[0:3], v[34:37], 0\n"
      "v_add_f32 v20, v20, v21\n"
      "v_mfma_f32_16x16x32_bf16 v[88:91], a[4:7], v[128:131], v[88:91]\n"
      "v_cvt_pk_bf16_f32 v214, v200, v201\n"
      "v_cvt_pk_bf16_f32 v213, v50, v51\n"
      "v_mfma_f32_16x16x32_bf16 v[88:91], a[8:11], v[220:223], v[88:91]\n"
      "v_mfma_f32_16x16x32_bf16 v[88:91], a[12:15], v[176:179], v[88:91]\n"
      "v_cvt_pk_bf16_f32 v215, v202, v203\n"
      "v_cvt_pk_bf16_f32 v211, v158, v159\n"
      "v_mfma_f32_16x16x32_bf16 v[100:103], v[34:37], a[112:115], 0\n"
      "v_cvt_pk_bf16_f32 v212, v48, v49\n"
      "v_mfma_f32_16x16x32_bf16 v[100:103], v[128:131], a[116:119], v[100:103]\n"
      "v_mov_b32_dpp v195, v115 row_ror:8 row_mask:0xf bank_mask:0xf\n"
      "v_mfma_f32_16x16x32_bf16 v[100:103], v[220:223], a[120:123], v[100:103]\n"
      "ds_bpermute_b32 v158, v7, v246 offset:248\n"
      "v_mfma_f32_16x16x32_bf16 v[100:103], v[176:179], a[124:127], v[100:103]\n"
      "v_cvt_pk_bf16_f32 v208, v88, v89\n"
      "v_cvt_pk_bf16_f32 v209, v90, v91\n"
      "v_mfma_f32_16x16x32_bf16 v[200:203], a[32:35], v[232:235], 0\n"
      "v_exp_f32 v122, v122\n"
      "v_mfma_f32_16x16x32_bf16 v[200:203], a[36:39], v[252:255], v[200:203]\n"
      "v_cvt_pk_bf16_f32 v210, v156, v157\n"
      "v_mfma_f32_16x16x32_bf16 v[200:203], a[40:43], v[248:251], v[200:203]\n"
      "s_nop 0\n"
      "v_cvt_pk_bf16_f32 v225, v102, v103\n"
      "v_mfma_f32_16x16x32_bf16 v[200:203], a[44:47], v[180:183], v[200:203]\n"
      "v_cvt_pk_bf16_f32 v224, v100, v101\n"
      "v_mov_b64 v[88:89], 0\n"
      "v_mfma_f32_16x16x32_bf16 v[100:103], v[34:37], a[96:99], 0\n"
      "v_exp_f32 v123, v123\n"
      "v_mfma_f32_16x16x32_bf16 v[100:103], v[128:131], a[100:103], v[100:103]\n"
      "ds_bpermute_b32 v159, v7, v246 offset:8\n"
      "v_mfma_f32_16x16x32_bf16 v[100:103], v[220:223], a[104:107], v[100:103]\n"
      "v_mfma_f32_16x16x32_bf16 v[100:103], v[176:179], a[108:111], v[100:103]\n"
      "buffer_store_dwordx2 v[32:33], v2, s[44:47], s100 offen offset:256 nt sc1\n"
      "v_cvt_pk_bf16_f32 v173, v202, v203\n"
      "v_smfmac_f32_16x16x64_bf16 v[88:91], v[116:119], v[208:215], v5\n"
      "v_cvt_pk_bf16_f32 v172, v200, v201\n"
      "v_mfma_f32_16x16x32_bf16 v[48:51], v[232:235], a[112:115], 0\n"
      "v_exp_f32 v138, v138\n"
      "v_mfma_f32_16x16x32_bf16 v[48:51], v[252:255], a[116:119], v[48:51]\n"
      "v_mov_b32_dpp v193, v113 row_ror:8 row_mask:0xf bank_mask:0xf\n"
      "v_mov_b64 v[200:201], 0\n"
      "v_mfma_f32_16x16x32_bf16 v[48:51], v[248:251], a[120:123], v[48:51]\n"
      "v_cvt_pk_bf16_f32 v133, v102, v103\n"
      "v_cvt_pk_bf16_f32 v132, v100, v101\n"
      "v_mfma_f32_16x16x32_bf16 v[48:51], v[180:183], a[124:127], v[48:51]\n"
      "v_exp_f32 v20, v20\n"
      "v_smfmac_f32_16x16x64_bf16 v[200:203], v[116:119], v[168:175], v5\n"
      "v_add_f32 v88, v88, v89\n"
      "v_mfma_f32_16x16x32_bf16 v[100:103], v[34:37], a[80:83], 0\n"
      "v_mov_b64 v[116:117], 0\n"
      "v_mov_b64 v[118:119], 0\n"
      "v_mfma_f32_16x16x32_bf16 v[100:103], v[128:131], a[84:87], v[100:103]\n"
      "s_nop 0\n"
      "v_cvt_pk_bf16_f32 v230, v48, v49\n"
      "v_cvt_pk_bf16_f32 v231, v50, v51\n"
      "v_mfma_f32_16x16x32_bf16 v[100:103], v[220:223], a[88:91], v[100:103]\n"
      "v_exp_f32 v88, v88\n"
      "v_mfma_f32_16x16x32_bf16 v[100:103], v[176:179], a[92:95], v[100:103]\n"
      "v_add_f32 v200, v200, v201\n"
      "v_cndmask_b32_dpp v89, v20, v88, vcc quad_perm:[0,0,2,2] row_mask:0xf bank_mask:0xf\n"
      "v_mfma_f32_16x16x32_bf16 v[196:199], v[34:37], a[64:67], 0\n"
      "v_exp_f32 v200, v200\n"
      "v_mfma_f32_16x16x32_bf16 v[196:199], v[128:131], a[68:71], v[196:199]\n"
      "v_cvt_pk_bf16_f32 v49, v122, v123\n"
      "v_cndmask_b32_dpp v139, v200, v138, vcc quad_perm:[0,0,2,2] row_mask:0xf bank_mask:0xf\n"
      "v_mfma_f32_16x16x32_bf16 v[196:199], v[220:223], a[72:75], v[196:199]\n"
      "L_h6_%=:\n"
      "s_cbranch_execz L_hr_%=\n"
      "v_cvt_pk_bf16_f32 v37, v102, v103\n"
      "v_cvt_pk_bf16_f32 v48, v120, v121\n"
      "v_mfma_f32_16x16x32_bf16 v[196:199], v[176:179], a[76:79], v[196:199]\n"
      "buffer_store_dwordx2 v[216:217], v2, s[44:47], s35 offen offset:256 nt sc1\n"
      "v_cvt_pk_bf16_f32 v36, v100, v101\n"
      "v_mfma_f32_16x16x32_bf16 v[120:123], v[184:187], v[24:27], 0\n"
      "s_not_b64 vcc, vcc\n"
      "v_cndmask_b32_dpp v45, v152, v44, vcc quad_perm:[1,1,3,3] row_mask:0xf bank_mask:0xf\n"
      "v_cndmask_b32_dpp v201, v138, v200, vcc quad_perm:[1,1,3,3] row_mask:0xf bank_mask:0xf\n"
      "v_mfma_f32_16x16x32_bf16 v[120:123], v[188:191], v[28:31], v[120:123]\n"
      "v_cvt_pk_bf16_f32 v153, v153, v45\n"
      "v_cvt_pk_bf16_f32 v140, v196, v197\n"
      "v_mfma_f32_16x16x32_bf16 v[32:35], v[208:211], v[52:55], 0\n"
      "v_cndmask_b32_dpp v21, v88, v20, vcc quad_perm:[1,1,3,3] row_mask:0xf bank_mask:0xf\n"
      "v_add_f32 v88, v88, v138\n"
      "v_mfma_f32_16x16x32_bf16 v[32:35], v[212:215], v[56:59], v[32:35]\n"
      "v_add_f32 v152, v152, v44\n"
      "v_cvt_pk_bf16_f32 v139, v139, v201\n"
      "v_mfma_f32_16x16x32_bf16 v[100:103], v[168:171], v[24:27], 0\n"
      "v_cvt_pk_bf16_f32 v89, v89, v21\n"
      "v_add_f32 v20, v20, v200\n"
      "v_mfma_f32_16x16x32_bf16 v[100:103], v[172:175], v[28:31], v[100:103]\n"
      "v_add_f32 v152, v152, v247\n"
      "v_cvt_pk_bf16_f32 v141, v198, v199\n"
      "v_smfmac_f32_16x16x64_bf16 v[116:119], v[112:115], v[24:31], v5\n"
      "ds_bpermute_b32 v161, v6, v139\n"
      "v_smfmac_f32_16x16x64_bf16 v[116:119], v[192:195], v[52:59], v5\n"
      "ds_bpermute_b32 v19, v7, v153 offset:4\n"
      "v_mfma_f32_16x16x32_bf16 v[200:203], v[208:211], v[24:27], 0\n"
      "v_exp_f32 v122, v122\n"
      "v_mfma_f32_16x16x32_bf16 v[200:203], v[212:215], v[28:31], v[200:203]\n"
      "v_exp_f32 v102, v102\n"
      "v_mfma_f32_16x16x32_bf16 v[176:179], v[232:235], a[64:67], 0\n"
      "v_exp_f32 v101, v101\n"
      "v_mfma_f32_16x16x32_bf16 v[176:179], v[252:255], a[68:71], v[176:179]\n"
      "ds_bpermute_b32 v162, v6, v89 offset:16\n"
      "v_mfma_f32_16x16x32_bf16 v[176:179], v[248:251], a[72:75], v[176:179]\n"
      "v_exp_f32 v120, v120\n"
      "v_mfma_f32_16x16x32_bf16 v[176:179], v[180:183], a[76:79], v[176:179]\n"
      "v_exp_f32 v35, v35\n"
      "v_mfma_f32_16x16x32_bf16 v[244:247], v[168:171], v[52:55], 0\n"
      "v_exp_f32 v200, v200\n"
      "v_mfma_f32_16x16x32_bf16 v[244:247], v[172:175], v[56:59], v[244:247]\n"
      "v_exp_f32 v203, v203\n"
      "v_mfma_f32_16x16x32_bf16 v[24:27], v[236:239], v[52:55], 0\n"
      "v_exp_f32 v100, v100\n"
      "v_mfma_f32_16x16x32_bf16 v[24:27], v[240:243], v[56:59], v[24:27]\n"
      "v_exp_f32 v202, v202\n"
      "v_add_f32 v88, v88, v20\n"
      "v_mfma_f32_16x16x32_bf16 v[28:31], v[104:107], v[52:55], 0\n"
      "v_exp_f32 v121, v121\n"
      "v_mfma_f32_16x16x32_bf16 v[28:31], v[108:111], v[56:59], v[28:31]\n"
      "v_exp_f32 v244, v244\n"
      "v_mfma_f32_16x16x32_bf16 v[196:199], a[128:131], v[232:235], 0\n"
      "s_nop 0\n"
      "v_exp_f32 v24, v24\n"
      "v_mfma_f32_16x16x32_bf16 v[196:199], a[132:135], v[252:255], v[196:199]\n"
      "v_exp_f32 v27, v27\n"
      "v_mfma_f32_16x16x32_bf16 v[196:199], a[136:139], v[248:251], v[196:199]\n"
      "s_nop 0\n"
      "v_exp_f32 v30, v30\n"
      "v_mfma_f32_16x16x32_bf16 v[196:199], a[140:143], v[180:183], v[196:199]\n"
      "v_exp_f32 v201, v201\n"
      "v_mfma_f32_16x16x32_bf16 v[128:131], v[72:75], a[96:99], 0\n"
      "v_cvt_pk_bf16_f32 v147, v178, v179\n"
      "v_cvt_pk_bf16_f32 v50, v100, v101\n"
      "v_mfma_f32_16x16x32_bf16 v[128:131], v[148:151], a[100:103], v[128:131]\n"
      "v_cvt_pk_bf16_f32 v44, v200, v201\n"
      "v_cvt_pk_bf16_f32 v46, v120, v121\n"
      "v_mfma_f32_16x16x32_bf16 v[128:131], v[164:167], a[104:107], v[128:131]\n"
      "ds_bpermute_b32 v163, v6, v139 offset:16\n"
      "v_mfma_f32_16x16x32_bf16 v[128:131], v[204:207], a[108:111], v[128:131]\n"
      "buffer_store_dwordx2 v[218:219], v2, s[44:47], s35 offen offset:768 nt sc1\n"
      "v_exp_f32 v25, v25\n"
      "v_mfma_f32_16x16x32_bf16 v[20:23], v[184:187], v[52:55], 0\n"
      "v_exp_f32 v28, v28\n"
      "v_mfma_f32_16x16x32_bf16 v[20:23], v[188:191], v[56:59], v[20:23]\n"
      "v_exp_f32 v123, v123\n"
      "v_mfma_f32_16x16x32_bf16 v[220:223], v[64:67], v[52:55], 0\n"
      "v_add_f32 v118, v118, v119\n"
      "v_add_f32 v116, v116, v117\n"
      "v_mfma_f32_16x16x32_bf16 v[220:223], v[68:71], v[56:59], v[220:223]\n"
      "v_cvt_pk_bf16_f32 v146, v176, v177\n"
      "v_cvt_pk_bf16_f32 v45, v202, v203\n"
      "v_cvt_pk_bf16_f32 v47, v122, v123\n"
      "v_mfma_f32_16x16x32_bf16 v[52:55], v[92:95], a[96:99], 0\n"
      "v_exp_f32 v21, v21\n"
      "v_mfma_f32_16x16x32_bf16 v[52:55], v[76:79], a[100:103], v[52:55]\n"
      "ds_bpermute_b32 v160, v6, v89\n"
      "v_mfma_f32_16x16x32_bf16 v[52:55], v[84:87], a[104:107], v[52:55]\n"
      "v_exp_f32 v116, v116\n"
      "v_mfma_f32_16x16x32_bf16 v[52:55], v[80:83], a[108:111], v[52:55]\n"
      "v_exp_f32 v31, v31\n"
      "v_mfma_f32_16x16x32_bf16 v[154:157], a[176:179], v[232:235], 0\n"
      "v_exp_f32 v247, v247\n"
      "v_mfma_f32_16x16x32_bf16 v[154:157], a[180:183], v[252:255], v[154:157]\n"
      "v_exp_f32 v246, v246\n"
      "v_mfma_f32_16x16x32_bf16 v[154:157], a[184:187], v[248:251], v[154:157]\n"
      "v_exp_f32 v245, v245\n"
      "v_mfma_f32_16x16x32_bf16 v[154:157], a[188:191], v[180:183], v[154:157]\n"
      "v_exp_f32 v222, v222\n"
      "v_mfma_f32_16x16x32_bf16 v[176:179], v[92:95], a[80:83], 0\n"
      "v_exp_f32 v220, v220\n"
      "v_mfma_f32_16x16x32_bf16 v[176:179], v[76:79], a[84:87], v[176:179]\n"
      "v_cvt_pk_bf16_f32 v122, v244, v245\n"
      "v_cvt_pk_bf16_f32 v216, v196, v197\n"
      "v_mfma_f32_16x16x32_bf16 v[176:179], v[84:87], a[88:91], v[176:179]\n"
      "v_exp_f32 v103, v103\n"
      "v_mfma_f32_16x16x32_bf16 v[176:179], v[80:83], a[92:95], v[176:179]\n"
      "v_cvt_pk_bf16_f32 v51, v102, v103\n"
      "v_add_f32 v200, v88, v152\n"
      "v_mfma_f32_16x16x32_bf16 v[100:103], v[232:235], a[96:99], 0\n"
      "v_exp_f32 v118, v118\n"
      "v_mfma_f32_16x16x32_bf16 v[100:103], v[252:255], a[100:103], v[100:103]\n"
      "v_exp_f32 v26, v26\n"
      "v_mfma_f32_16x16x32_bf16 v[100:103], v[248:251], a[104:107], v[100:103]\n"
      "v_cvt_pk_bf16_f32 v123, v246, v247\n"
      "v_cvt_pk_bf16_f32 v246, v176, v177\n"
      "v_mfma_f32_16x16x32_bf16 v[100:103], v[180:183], a[108:111], v[100:103]\n"
      "v_cvt_pk_bf16_f32 v247, v178, v179\n"
      "v_cvt_pk_bf16_f32 v217, v198, v199\n"
      "v_mfma_f32_16x16x32_bf16 v[176:179], v[72:75], a[64:67], 0\n"
      "ds_bpermute_b32 v18, v7, v153\n"
      "v_mfma_f32_16x16x32_bf16 v[176:179], v[148:151], a[68:71], v[176:179]\n"
      "v_exp_f32 v34, v34\n"
      "v_mfma_f32_16x16x32_bf16 v[176:179], v[164:167], a[72:75], v[176:179]\n"
      "v_exp_f32 v22, v22\n"
      "v_mfma_f32_16x16x32_bf16 v[176:179], v[204:207], a[76:79], v[176:179]\n"
      "v_cvt_pk_bf16_f32 v138, v100, v101\n"
      "v_cvt_pk_bf16_f32 v60, v128, v129\n"
      "v_mfma_f32_16x16x32_bf16 v[196:199], v[72:75], a[80:83], 0\n"
      "v_cvt_pk_bf16_f32 v63, v54, v55\n"
      "v_cvt_pk_bf16_f32 v62, v52, v53\n"
      "v_mfma_f32_16x16x32_bf16 v[196:199], v[148:151], a[84:87], v[196:199]\n"
      "v_add_f32 v101, v116, v118\n"
      "v_cvt_pk_bf16_f32 v139, v102, v103\n"
      "v_mfma_f32_16x16x32_bf16 v[196:199], v[164:167], a[88:91], v[196:199]\n"
      "v_exp_f32 v32, v32\n"
      "v_mfma_f32_16x16x32_bf16 v[196:199], v[204:207], a[92:95], v[196:199]\n"
      "v_cvt_pk_bf16_f32 v61, v130, v131\n"
      "v_exp_f32 v223, v223\n"
      "v_mfma_f32_16x16x32_bf16 v[52:55], v[92:95], a[64:67], 0\n"
      "v_cvt_pk_bf16_f32 v89, v178, v179\n"
      "v_add_f32 v100, v116, v118\n"
      "v_mfma_f32_16x16x32_bf16 v[52:55], v[76:79], a[68:71], v[52:55]\n"
      "v_cvt_pk_bf16_f32 v203, v116, v118\n"
      "v_cvt_pk_bf16_f32 v88, v176, v177\n"
      "v_mfma_f32_16x16x32_bf16 v[52:55], v[84:87], a[72:75], v[52:55]\n"
      "v_cvt_pk_bf16_f32 v202, v116, v118\n"
      "v_cvt_pk_bf16_f32 v128, v24, v25\n"
      "v_mfma_f32_16x16x32_bf16 v[52:55], v[80:83], a[76:79], v[52:55]\n"
      "v_exp_f32 v23, v23\n"
      "v_mfma_f32_16x16x32_bf16 v[116:119], a[160:163], v[232:235], 0\n"
      "v_exp_f32 v221, v221\n"
      "v_mfma_f32_16x16x32_bf16 v[116:119], a[164:167], v[252:255], v[116:119]\n"
      "v_cvt_pk_bf16_f32 v131, v222, v223\n"
      "v_cvt_pk_bf16_f32 v130, v220, v221\n"
      "v_mfma_f32_16x16x32_bf16 v[116:119], a[168:171], v[248:251], v[116:119]\n"
      "v_cvt_pk_bf16_f32 v244, v196, v197\n"
      "v_cvt_pk_bf16_f32 v223, v156, v157\n"
      "v_mfma_f32_16x16x32_bf16 v[116:119], a[172:175], v[180:183], v[116:119]\n"
      "v_cvt_pk_bf16_f32 v91, v54, v55\n"
      "v_cvt_pk_bf16_f32 v222, v154, v155\n"
      "v_mfma_f32_16x16x32_bf16 v[176:179], a[144:147], v[232:235], 0\n"
      "v_cvt_pk_bf16_f32 v121, v30, v31\n"
      "v_cvt_pk_bf16_f32 v90, v52, v53\n"
      "v_mfma_f32_16x16x32_bf16 v[176:179], a[148:151], v[252:255], v[176:179]\n"
      "v_exp_f32 v33, v33\n"
      "v_mfma_f32_16x16x32_bf16 v[176:179], a[152:155], v[248:251], v[176:179]\n"
      "v_cvt_pk_bf16_f32 v245, v198, v199\n"
      "v_cvt_pk_bf16_f32 v220, v116, v117\n"
      "v_mfma_f32_16x16x32_bf16 v[176:179], a[156:159], v[180:183], v[176:179]\n"
      "ds_read_b64 v[156:157], v11 offset:0\n"
      "v_cvt_pk_bf16_f32 v221, v118, v119\n"
      "v_mfma_f32_16x16x32_bf16 v[52:55], v[72:75], a[112:115], 0\n"
      "v_exp_f32 v20, v20\n"
      "v_mfma_f32_16x16x32_bf16 v[52:55], v[148:151], a[116:119], v[52:55]\n"
      "v_permlane16_swap_b32 v100, v101\n"
      "v_mfma_f32_16x16x32_bf16 v[52:55], v[164:167], a[120:123], v[52:55]\n"
      "v_cvt_pk_bf16_f32 v118, v20, v21\n"
      "v_cvt_pk_bf16_f32 v119, v22, v23\n"
      "v_mfma_f32_16x16x32_bf16 v[52:55], v[204:207], a[124:127], v[52:55]\n"
      "v_permlane16_swap_b32 v202, v203\n"
      "v_mfma_f32_16x16x32_bf16 v[20:23], v[92:95], a[112:115], 0\n"
      "v_cvt_pk_bf16_f32 v116, v32, v33\n"
      "v_cvt_pk_bf16_f32 v117, v34, v35\n"
      "v_mfma_f32_16x16x32_bf16 v[20:23], v[76:79], a[116:119], v[20:23]\n"
      "v_exp_f32 v29, v29\n"
      "v_mfma_f32_16x16x32_bf16 v[20:23], v[84:87], a[120:123], v[20:23]\n"
      "v_cvt_pk_bf16_f32 v219, v178, v179\n"
      "v_cvt_pk_bf16_f32 v218, v176, v177\n"
      "v_mfma_f32_16x16x32_bf16 v[20:23], v[80:83], a[124:127], v[20:23]\n"
      "v_cvt_pk_bf16_f32 v196, v52, v53\n"
      "v_cvt_pk_bf16_f32 v120, v28, v29\n"
      "v_mov_b64 v[152:153], v[100:101]\n"
      "v_smfmac_f32_16x16x64_bf16 v[100:103], v[12:15], v[44:51], v5\n"
      "v_cvt_pk_bf16_f32 v129, v26, v27\n"
      "v_smfmac_f32_16x16x64_bf16 v[152:155], v[12:15], v[116:123], v5\n"
      "s_nop 1\n"
      "v_cvt_pk_bf16_f32 v199, v22, v23\n"
      "v_cvt_pk_bf16_f32 v198, v20, v21\n"
      "v_smfmac_f32_16x16x32_bf16 v[100:103], v[12:13], v[124:127], v5\n"
      "v_cvt_pk_bf16_f32 v197, v54, v55\n"
      "s_waitcnt lgkmcnt(0)\n"
      "v_mfma_f32_16x16x4_4b_bf16 v[20:35], v[202:203], v[156:157], 0 cbsz:2 abid:0\n"
      "s_nop 4\n"
      "v_add_f32 v100, v100, v101\n"
      "v_add_f32_dpp v200, v200, v200 row_ror:8 row_mask:0xf bank_mask:0xf\n"
      "v_smfmac_f32_16x16x32_bf16 v[152:155], v[12:13], v[128:131], v5\n"
      "s_nop 1\n"
      "v_mfma_f32_16x16x32_bf16 v[20:23], v[44:47], v[140:143], v[20:23]\n"
      "v_rcp_f32 v101, v100\n"
      "v_mfma_f32_16x16x32_bf16 v[20:23], v[48:51], v[144:147], v[20:23]\n"
      "ds_bpermute_b32 v102, v4, v101\n"
      "v_mfma_f32_16x16x32_bf16 v[20:23], v[124:127], v[88:91], v[20:23]\n"
      "v_mfma_f32_16x16x32_bf16 v[28:31], v[44:47], v[132:135], v[28:31]\n"
      "v_mfma_f32_16x16x32_bf16 v[28:31], v[48:51], v[136:139], v[28:31]\n"
      "v_mfma_f32_16x16x32_bf16 v[28:31], v[124:127], v[60:63], v[28:31]\n"
      "v_mfma_f32_16x16x32_bf16 v[24:27], v[44:47], v[36:39], v[24:27]\n"
      "v_add_f32_dpp v200, v200, v200 row_ror:4 row_mask:0xf bank_mask:0xf\n"
      "v_add_f32 v152, v152, v153\n"
      "v_mfma_f32_16x16x32_bf16 v[24:27], v[48:51], v[40:43], v[24:27]\n"
      "v_mfma_f32_16x16x32_bf16 v[24:27], v[124:127], v[244:247], v[24:27]\n"
      "v_rcp_f32 v153, v152\n"
      "v_mfma_f32_16x16x32_bf16 v[32:35], v[44:47], v[224:227], v[32:35]\n"
      "v_mfma_f32_16x16x32_bf16 v[32:35], v[48:51], v[228:231], v[32:35]\n"
      "s_waitcnt lgkmcnt(0)\n"
      "v_mul_f32_dpp v23, v102, v23 row_newbcast:12 row_mask:0xf bank_mask:0xf\n"
      "v_mfma_f32_16x16x32_bf16 v[32:35], v[124:127], v[196:199], v[32:35]\n"
      "s_nop 1\n"
      "v_mul_f32_dpp v24, v102, v24 row_newbcast:0 row_mask:0xf bank_mask:0xf\n"
      "v_mfma_f32_16x16x32_bf16 v[176:179], v[64:67], v[216:219], 0\n"
      "v_mul_f32_dpp v27, v102, v27 row_newbcast:12 row_mask:0xf bank_mask:0xf\n"
      "v_mfma_f32_16x16x32_bf16 v[176:179], v[68:71], v[220:223], v[176:179]\n"
      "v_mul_f32_dpp v21, v102, v21 row_newbcast:4 row_mask:0xf bank_mask:0xf\n"
      "v_mfma_f32_16x16x32_bf16 v[124:127], v[236:239], v[216:219], 0\n"
      "v_mfma_f32_16x16x32_bf16 v[124:127], v[240:243], v[220:223], v[124:127]\n"
      "v_mul_f32_dpp v30, v102, v30 row_newbcast:8 row_mask:0xf bank_mask:0xf\n"
      "v_mul_f32_dpp v25, v102, v25 row_newbcast:4 row_mask:0xf bank_mask:0xf\n"
      "v_mfma_f32_16x16x32_bf16 v[96:99], v[104:107], v[216:219], 0\n"
      "v_mul_f32_dpp v28, v102, v28 row_newbcast:0 row_mask:0xf bank_mask:0xf\n"
      "v_mul_f32_dpp v26, v102, v26 row_newbcast:8 row_mask:0xf bank_mask:0xf\n"
      "v_mfma_f32_16x16x32_bf16 v[96:99], v[108:111], v[220:223], v[96:99]\n"
      "v_mul_f32_dpp v32, v102, v32 row_newbcast:0 row_mask:0xf bank_mask:0xf\n"
      "v_mul_f32_dpp v22, v102, v22 row_newbcast:8 row_mask:0xf bank_mask:0xf\n"
      "v_mfma_f32_16x16x32_bf16 v[24:27], v[92:95], a[208:211], v[24:27]\n"
      "v_exp_f32 v126, v126\n"
      "v_mfma_f32_16x16x32_bf16 v[24:27], v[76:79], a[212:215], v[24:27]\n"
      "v_mul_f32_dpp v35, v102, v35 row_newbcast:12 row_mask:0xf bank_mask:0xf\n"
      "v_mul_f32_dpp v31, v102, v31 row_newbcast:12 row_mask:0xf bank_mask:0xf\n"
      "v_mfma_f32_16x16x32_bf16 v[24:27], v[84:87], a[216:219], v[24:27]\n"
      "v_mul_f32_dpp v29, v102, v29 row_newbcast:4 row_mask:0xf bank_mask:0xf\n"
      "v_mfma_f32_16x16x32_bf16 v[24:27], v[80:83], a[220:223], v[24:27]\n"
      "v_add_f32_dpp v200, v200, v200 row_ror:2 row_mask:0xf bank_mask:0xf\n"
      "v_mul_f32_dpp v33, v102, v33 row_newbcast:4 row_mask:0xf bank_mask:0xf\n"
      "v_mfma_f32_16x16x32_bf16 v[28:31], v[92:95], a[224:227], v[28:31]\n"
      "v_exp_f32 v179, v179\n"
      "v_mfma_f32_16x16x32_bf16 v[28:31], v[76:79], a[228:231], v[28:31]\n"
      "v_mul_f32_dpp v20, v102, v20 row_newbcast:0 row_mask:0xf bank_mask:0xf\n"
      "v_mul_f32_dpp v34, v102, v34 row_newbcast:8 row_mask:0xf bank_mask:0xf\n"
      "v_mfma_f32_16x16x32_bf16 v[28:31], v[84:87], a[232:235], v[28:31]\n"
      "v_exp_f32 v125, v125\n"
      "v_mfma_f32_16x16x32_bf16 v[28:31], v[80:83], a[236:239], v[28:31]\n"
      "v_add_f32_dpp v200, v200, v200 row_ror:1 row_mask:0xf bank_mask:0xf\n"
      "v_mfma_f32_16x16x32_bf16 v[20:23], v[92:95], a[192:195], v[20:23]\n"
      "v_rcp_f32 v201, v200\n"
      "v_mfma_f32_16x16x32_bf16 v[20:23], v[76:79], a[196:199], v[20:23]\n"
      "v_exp_f32 v124, v124\n"
      "v_mfma_f32_16x16x32_bf16 v[20:23], v[84:87], a[200:203], v[20:23]\n"
      "v_exp_f32 v127, v127\n"
      "v_mfma_f32_16x16x32_bf16 v[20:23], v[80:83], a[204:207], v[20:23]\n"
      "v_exp_f32 v97, v97\n"
      "v_mfma_f32_16x16x32_bf16 v[32:35], v[92:95], a[240:243], v[32:35]\n"
      "v_exp_f32 v178, v178\n"
      "v_mfma_f32_16x16x32_bf16 v[32:35], v[76:79], a[244:247], v[32:35]\n"
      "v_maximum3_f32 v8, v8, v200, v201\n"
      "v_mfma_f32_16x16x32_bf16 v[32:35], v[84:87], a[248:251], v[32:35]\n"
      "v_exp_f32 v99, v99\n"
      "v_mfma_f32_16x16x32_bf16 v[32:35], v[80:83], a[252:255], v[32:35]\n"
      "v_exp_f32 v176, v176\n"
      "v_mfma_f32_16x16x4_4b_bf16 v[44:59], v[202:203], v[156:157], 0 cbsz:2 abid:2\n"
      "ds_bpermute_b32 v154, v4, v153 offset:128\n"
      "v_cvt_pk_bf16_f32 v95, v178, v179\n"
      "v_maximum3_f32 v8, v8, v100, v101\n"
      "v_exp_f32 v177, v177\n"
      "v_cvt_pk_bf16_f32 v22, v22, v26\n"
      "v_mfma_f32_16x16x32_bf16 v[100:103], v[184:187], v[216:219], 0\n"
      "v_exp_f32 v98, v98\n"
      "v_mfma_f32_16x16x32_bf16 v[100:103], v[188:191], v[220:223], v[100:103]\n"
      "v_cvt_pk_bf16_f32 v20, v20, v24\n"
      "v_cvt_pk_bf16_f32 v24, v21, v25\n"
      "v_mfma_f32_16x16x32_bf16 v[48:51], v[116:119], v[36:39], v[48:51]\n"
      "v_cvt_pk_bf16_f32 v21, v28, v32\n"
      "v_cvt_pk_bf16_f32 v93, v126, v127\n"
      "v_mfma_f32_16x16x32_bf16 v[48:51], v[120:123], v[40:43], v[48:51]\n"
      "v_cvt_pk_bf16_f32 v26, v23, v27\n"
      "v_cvt_pk_bf16_f32 v27, v31, v35\n"
      "v_mfma_f32_16x16x32_bf16 v[48:51], v[128:131], v[244:247], v[48:51]\n"
      "v_pk_max_i16 v27, v27, 0\n"
      "v_cvt_pk_bf16_f32 v25, v29, v33\n"
      "v_mfma_f32_16x16x32_bf16 v[56:59], v[116:119], v[224:227], v[56:59]\n"
      "v_maximum3_f32 v8, v8, v152, v153\n"
      "v_mfma_f32_16x16x32_bf16 v[56:59], v[120:123], v[228:231], v[56:59]\n"
      "v_exp_f32 v102, v102\n"
      "v_mfma_f32_16x16x32_bf16 v[56:59], v[128:131], v[196:199], v[56:59]\n"
      "v_exp_f32 v100, v100\n"
      "v_mfma_f32_16x16x32_bf16 v[44:47], v[116:119], v[140:143], v[44:47]\n"
      "v_cvt_pk_bf16_f32 v94, v176, v177\n"
      "v_mfma_f32_16x16x32_bf16 v[44:47], v[120:123], v[144:147], v[44:47]\n"
      "s_waitcnt lgkmcnt(0)\n"
      "v_mul_f32_dpp v48, v154, v48 row_newbcast:0 row_mask:0xf bank_mask:0xf\n"
      "v_cvt_pk_bf16_f32 v92, v124, v125\n"
      "v_mfma_f32_16x16x32_bf16 v[44:47], v[128:131], v[88:91], v[44:47]\n"
      "ds_read_b128 v[80:83], v0 offset:8704\n"
      "v_mul_f32_dpp v59, v154, v59 row_newbcast:12 row_mask:0xf bank_mask:0xf\n"
      "v_mfma_f32_16x16x32_bf16 v[52:55], v[116:119], v[132:135], v[52:55]\n"
      "v_mfma_f32_16x16x32_bf16 v[52:55], v[120:123], v[136:139], v[52:55]\n"
      "v_cvt_pk_bf16_f32 v23, v30, v34\n"
      "v_pk_max_i16 v23, v23, 0\n"
      "v_mfma_f32_16x16x32_bf16 v[52:55], v[128:131], v[60:63], v[52:55]\n"
      "ds_read_b64 v[202:203], v11 offset:4096\n"
      "v_mul_f32_dpp v46, v154, v46 row_newbcast:8 row_mask:0xf bank_mask:0xf\n"
      "v_mfma_f32_16x16x32_bf16 v[28:31], v[168:171], v[216:219], 0\n"
      "v_mul_f32_dpp v49, v154, v49 row_newbcast:4 row_mask:0xf bank_mask:0xf\n"
      "v_mfma_f32_16x16x32_bf16 v[28:31], v[172:175], v[220:223], v[28:31]\n"
      "v_mul_f32_dpp v50, v154, v50 row_newbcast:8 row_mask:0xf bank_mask:0xf\n"
      "v_mul_f32_dpp v57, v154, v57 row_newbcast:4 row_mask:0xf bank_mask:0xf\n"
      "v_mfma_f32_16x16x4_4b_bf16 v[116:131], v[158:159], v[156:157], 0 cbsz:2 abid:0\n"
      "v_mul_f32_dpp v53, v154, v53 row_newbcast:4 row_mask:0xf bank_mask:0xf\n"
      "v_pk_max_i16 v22, v22, 0\n"
      "v_exp_f32 v96, v96\n"
      "v_mul_f32_dpp v45, v154, v45 row_newbcast:4 row_mask:0xf bank_mask:0xf\n"
      "v_mul_f32_dpp v55, v154, v55 row_newbcast:12 row_mask:0xf bank_mask:0xf\n"
      "v_mul_f32_dpp v44, v154, v44 row_newbcast:0 row_mask:0xf bank_mask:0xf\n"
      "v_pk_max_i16 v21, v21, 0\n"
      "v_mfma_f32_16x16x32_bf16 v[32:35], v[208:211], v[216:219], 0\n"
      "v_mul_f32_dpp v51, v154, v51 row_newbcast:12 row_mask:0xf bank_mask:0xf\n"
      "v_mul_f32_dpp v47, v154, v47 row_newbcast:12 row_mask:0xf bank_mask:0xf\n"
      "v_mfma_f32_16x16x32_bf16 v[32:35], v[212:215], v[220:223], v[32:35]\n"
      "v_mul_f32_dpp v54, v154, v54 row_newbcast:8 row_mask:0xf bank_mask:0xf\n"
      "v_mul_f32_dpp v58, v154, v58 row_newbcast:8 row_mask:0xf bank_mask:0xf\n"
      "v_smfmac_f32_16x16x64_bf16 v[116:119], v[160:163], v[140:147], v5\n"
      "v_mul_f32_dpp v52, v154, v52 row_newbcast:0 row_mask:0xf bank_mask:0xf\n"
      "v_mfma_f32_16x16x32_bf16 v[44:47], v[72:75], a[192:195], v[44:47]\n"
      "v_pk_max_i16 v24, v24, 0\n"
      "v_pk_max_i16 v25, v25, 0\n"
      "v_mfma_f32_16x16x32_bf16 v[44:47], v[148:151], a[196:199], v[44:47]\n"
      "v_exp_f32 v103, v103\n"
      "v_mfma_f32_16x16x32_bf16 v[44:47], v[164:167], a[200:203], v[44:47]\n"
      "v_mul_f32_dpp v56, v154, v56 row_newbcast:0 row_mask:0xf bank_mask:0xf\n"
      "v_pk_max_i16 v26, v26, 0\n"
      "v_mfma_f32_16x16x32_bf16 v[44:47], v[204:207], a[204:207], v[44:47]\n"
      "v_exp_f32 v29, v29\n"
      "v_mfma_f32_16x16x32_bf16 v[48:51], v[72:75], a[208:211], v[48:51]\n"
      "v_mov_b64 v[178:179], 0\n"
      "v_mfma_f32_16x16x32_bf16 v[48:51], v[148:151], a[212:215], v[48:51]\n"
      "v_exp_f32 v34, v34\n"
      "v_mfma_f32_16x16x32_bf16 v[48:51], v[164:167], a[216:219], v[48:51]\n"
      "v_mov_b64 v[176:177], 0\n"
      "v_mfma_f32_16x16x32_bf16 v[48:51], v[204:207], a[220:223], v[48:51]\n"
      "v_exp_f32 v28, v28\n"
      "v_smfmac_f32_16x16x64_bf16 v[176:179], v[112:115], v[216:223], v5\n"
      "buffer_store_dwordx2 v[26:27], v2, s[44:47], s73 offen offset:768 nt sc1\n"
      "ds_read_b128 v[84:87], v0 offset:9216\n"
      "v_smfmac_f32_16x16x64_bf16 v[120:123], v[160:163], v[36:43], v5\n"
      "buffer_store_dwordx2 v[24:25], v2, s[44:47], s73 offen offset:256 nt sc1\n"
      "ds_read_b128 v[76:79], v0 offset:9728\n"
      "v_smfmac_f32_16x16x64_bf16 v[128:131], v[160:163], v[224:231], v5\n"
      "ds_read_b128 v[152:155], v0 offset:8192\n"
      "v_smfmac_f32_16x16x64_bf16 v[124:127], v[160:163], v[132:139], v5\n"
      "v_cvt_pk_bf16_f32 v44, v44, v48\n"
      "v_mfma_f32_16x16x32_bf16 v[52:55], v[72:75], a[224:227], v[52:55]\n"
      "v_cvt_pk_bf16_f32 v48, v45, v49\n"
      "v_cvt_pk_bf16_f32 v219, v102, v103\n"
      "v_mfma_f32_16x16x32_bf16 v[52:55], v[148:151], a[228:231], v[52:55]\n"
      "v_exp_f32 v33, v33\n"
      "v_mfma_f32_16x16x32_bf16 v[52:55], v[164:167], a[232:235], v[52:55]\n"
      "v_cvt_pk_bf16_f32 v46, v46, v50\n"
      "v_cvt_pk_bf16_f32 v50, v47, v51\n"
      "v_mfma_f32_16x16x32_bf16 v[52:55], v[204:207], a[236:239], v[52:55]\n"
      "v_pk_max_i16 v20, v20, 0\n"
      "v_mfma_f32_16x16x32_bf16 v[56:59], v[72:75], a[240:243], v[56:59]\n"
      "v_exp_f32 v101, v101\n"
      "v_mfma_f32_16x16x32_bf16 v[56:59], v[148:151], a[244:247], v[56:59]\n"
      "v_exp_f32 v35, v35\n"
      "v_mfma_f32_16x16x32_bf16 v[56:59], v[164:167], a[248:251], v[56:59]\n"
      "v_exp_f32 v32, v32\n"
      "v_mfma_f32_16x16x32_bf16 v[56:59], v[204:207], a[252:255], v[56:59]\n"
      "v_pk_max_i16 v46, v46, 0\n"
      "v_pk_max_i16 v48, v48, 0\n"
      "v_smfmac_f32_16x16x32_bf16 v[128:131], v[18:19], v[196:199], v5\n"
      "buffer_store_dwordx2 v[22:23], v2, s[44:47], s73 offen offset:512 nt sc1\n"
      "s_waitcnt lgkmcnt(0)\n"
      "v_mfma_f32_16x16x32_bf16 v[164:167], a[144:147], v[152:155], 0\n"
      "v_mfma_f32_16x16x32_bf16 v[164:167], a[148:151], v[80:83], v[164:167]\n"
      "v_cvt_pk_bf16_f32 v218, v100, v101\n"
      "v_cvt_pk_bf16_f32 v47, v54, v58\n"
      "v_mfma_f32_16x16x32_bf16 v[164:167], a[152:155], v[84:87], v[164:167]\n"
      "v_cvt_pk_bf16_f32 v49, v53, v57\n"
      "v_pk_max_i16 v49, v49, 0\n"
      "v_mfma_f32_16x16x32_bf16 v[164:167], a[156:159], v[76:79], v[164:167]\n"
      "v_pk_max_i16 v44, v44, 0\n"
      "v_cvt_pk_bf16_f32 v51, v55, v59\n"
      "v_mfma_f32_16x16x32_bf16 v[204:207], a[128:131], v[152:155], 0\n"
      "v_exp_f32 v31, v31\n"
      "v_mfma_f32_16x16x32_bf16 v[204:207], a[132:135], v[80:83], v[204:207]\n"
      "v_pk_max_i16 v51, v51, 0\n"
      "v_and_b32 v75, 0xffff0000, v203\n"
      "v_mfma_f32_16x16x32_bf16 v[204:207], a[136:139], v[84:87], v[204:207]\n"
      "v_cvt_pk_bf16_f32 v222, v28, v29\n"
      "v_pk_max_i16 v50, v50, 0\n"
      "v_mfma_f32_16x16x32_bf16 v[204:207], a[140:143], v[76:79], v[204:207]\n"
      "buffer_store_dwordx2 v[50:51], v2, s[44:47], s75 offen offset:768 nt sc1\n"
      "v_cvt_pk_bf16_f32 v221, v98, v99\n"
      "v_mfma_f32_16x16x32_bf16 v[148:151], a[160:163], v[152:155], 0\n"
      "v_lshlrev_b32 v74, 16, v203\n"
      "v_and_b32 v73, 0xffff0000, v202\n"
      "v_mfma_f32_16x16x32_bf16 v[148:151], a[164:167], v[80:83], v[148:151]\n"
      "v_exp_f32 v30, v30\n"
      "v_mfma_f32_16x16x32_bf16 v[148:151], a[168:171], v[84:87], v[148:151]\n"
      "v_cvt_pk_bf16_f32 v220, v96, v97\n"
      "v_cvt_pk_bf16_f32 v45, v52, v56\n"
      "v_mfma_f32_16x16x32_bf16 v[148:151], a[172:175], v[76:79], v[148:151]\n"
      "v_lshlrev_b32 v72, 16, v202\n"
      "v_cvt_pk_bf16_f32 v217, v34, v35\n"
      "v_mfma_f32_16x16x32_bf16 v[96:99], a[176:179], v[152:155], 0\n"
      "v_add_f32 v128, v128, v129\n"
      "v_cvt_pk_bf16_f32 v223, v30, v31\n"
      "v_mfma_f32_16x16x32_bf16 v[96:99], a[180:183], v[80:83], v[96:99]\n"
      "v_cvt_pk_bf16_f32 v24, v204, v205\n"
      "v_cvt_pk_bf16_f32 v26, v164, v165\n"
      "v_mfma_f32_16x16x32_bf16 v[96:99], a[184:187], v[84:87], v[96:99]\n"
      "v_cvt_pk_bf16_f32 v25, v206, v207\n"
      "v_cvt_pk_bf16_f32 v27, v166, v167\n"
      "v_mfma_f32_16x16x32_bf16 v[96:99], a[188:191], v[76:79], v[96:99]\n"
      "v_cvt_pk_bf16_f32 v28, v148, v149\n"
      "v_fma_f32 v128, v128, v201, v75\n"
      "v_smfmac_f32_16x16x32_bf16 v[116:119], v[18:19], v[88:91], v5\n"
      "ds_read_b128 v[164:167], v0 offset:4608\n"
      "v_cvt_pk_bf16_f32 v216, v32, v33\n"
      "v_smfmac_f32_16x16x32_bf16 v[124:127], v[18:19], v[60:63], v5\n"
      "ds_read_b128 v[204:207], v0 offset:5120\n"
      "v_pk_max_i16 v47, v47, 0\n"
      "v_smfmac_f32_16x16x32_bf16 v[120:123], v[18:19], v[244:247], v5\n"
      "v_cvt_pk_bf16_f32 v29, v150, v151\n"
      "v_cvt_pk_bf16_f32 v31, v98, v99\n"
      "v_cvt_pk_bf16_f32 v30, v96, v97\n"
      "v_mfma_f32_16x16x32_bf16 v[52:55], v[64:67], v[24:27], 0\n"
      "s_nop 0\n"
      "v_add_f32 v124, v124, v125\n"
      "v_add_f32 v116, v116, v117\n"
      "v_mfma_f32_16x16x32_bf16 v[52:55], v[68:71], v[28:31], v[52:55]\n"
      "v_add_f32 v120, v120, v121\n"
      "v_fma_f32 v124, v124, v201, v74\n"
      "v_smfmac_f32_16x16x64_bf16 v[176:179], v[192:195], v[24:31], v5\n"
      "buffer_store_dwordx2 v[20:21], v2, s[44:47], s73 offen offset:0 nt sc1\n"
      "v_fma_f32 v120, v120, v201, v73\n"
      "v_mfma_f32_16x16x32_bf16 v[96:99], v[236:239], v[24:27], 0\n"
      "ds_read_b128 v[148:151], v0 offset:5632\n"
      "ds_read_b128 v[158:161], v0 offset:4096\n"
      "v_mfma_f32_16x16x32_bf16 v[96:99], v[240:243], v[28:31], v[96:99]\n"
      "buffer_store_dwordx2 v[48:49], v2, s[44:47], s75 offen offset:256 nt sc1\n"
      "v_fma_f32 v116, v116, v201, v72\n"
      "v_mfma_f32_16x16x32_bf16 v[56:59], v[208:211], v[24:27], 0\n"
      "v_add_f32 v178, v178, v179\n"
      "v_cvt_pk_bf16_f32 v117, v124, v128\n"
      "v_mfma_f32_16x16x32_bf16 v[56:59], v[212:215], v[28:31], v[56:59]\n"
      "v_exp_f32 v178, v178\n"
      "v_mfma_f32_16x16x32_bf16 v[124:127], v[168:171], v[24:27], 0\n"
      "v_cvt_pk_bf16_f32 v116, v116, v120\n"
      "v_add_f32 v176, v176, v177\n"
      "v_mfma_f32_16x16x32_bf16 v[124:127], v[172:175], v[28:31], v[124:127]\n"
      "v_exp_f32 v176, v176\n"
      "v_mfma_f32_16x16x32_bf16 v[48:51], v[184:187], v[24:27], 0\n"
      "v_cvt_pk_bf16_f32 v19, v176, v178\n"
      "v_cvt_pk_bf16_f32 v18, v176, v178\n"
      "v_mfma_f32_16x16x32_bf16 v[48:51], v[188:191], v[28:31], v[48:51]\n"
      "v_add_f32 v101, v176, v178\n"
      "v_pk_max_i16 v116, v116, 0\n"
      "v_mfma_f32_16x16x32_bf16 v[118:121], v[104:107], v[24:27], 0\n"
      "v_permlane16_swap_b32 v18, v19\n"
      "v_mfma_f32_16x16x32_bf16 v[118:121], v[108:111], v[28:31], v[118:121]\n"
      "v_exp_f32 v59, v59\n"
      "v_mfma_f32_16x16x4_4b_bf16 v[20:35], v[18:19], v[156:157], 0 cbsz:2 abid:0\n"
      "v_add_f32 v100, v176, v178\n"
      "v_pk_max_i16 v117, v117, 0\n"
      "v_exp_f32 v54, v54\n"
      "v_permlane16_swap_b32 v100, v101\n"
      "v_mov_b64 v[200:201], v[100:101]\n"
      "s_nop 0\n"
      "v_smfmac_f32_16x16x64_bf16 v[100:103], v[12:15], v[216:223], v5\n"
      "s_nop 2\n"
      "v_mfma_f32_16x16x32_bf16 v[28:31], v[216:219], v[132:135], v[28:31]\n"
      "v_exp_f32 v121, v121\n"
      "v_mfma_f32_16x16x32_bf16 v[28:31], v[220:223], v[136:139], v[28:31]\n"
      "v_exp_f32 v49, v49\n"
      "v_mfma_f32_16x16x32_bf16 v[28:31], v[92:95], v[60:63], v[28:31]\n"
      "v_exp_f32 v97, v97\n"
      "v_smfmac_f32_16x16x32_bf16 v[100:103], v[12:13], v[92:95], v5\n"
      "v_pk_max_i16 v45, v45, 0\n"
      "v_exp_f32 v118, v118\n"
      "v_mfma_f32_16x16x32_bf16 v[20:23], v[216:219], v[140:143], v[20:23]\n"
      "v_exp_f32 v120, v120\n"
      "v_mfma_f32_16x16x32_bf16 v[20:23], v[220:223], v[144:147], v[20:23]\n"
      "v_exp_f32 v50, v50\n"
      "v_mfma_f32_16x16x32_bf16 v[20:23], v[92:95], v[88:91], v[20:23]\n"
      "buffer_store_dwordx2 v[116:117], v3, s[44:47], s79 offen offset:0 nt sc1\n"
      "v_add_f32 v100, v100, v101\n"
      "v_mfma_f32_16x16x32_bf16 v[32:35], v[216:219], v[224:227], v[32:35]\n"
      "v_rcp_f32 v101, v100\n"
      "v_mfma_f32_16x16x32_bf16 v[32:35], v[220:223], v[228:231], v[32:35]\n"
      "ds_bpermute_b32 v102, v4, v101\n"
      "v_mfma_f32_16x16x32_bf16 v[32:35], v[92:95], v[196:199], v[32:35]\n"
      "s_waitcnt lgkmcnt(1)\n"
      "v_mfma_f32_16x16x32_bf16 v[128:131], a[160:163], v[158:161], 0\n"
      "v_exp_f32 v127, v127\n"
      "v_mfma_f32_16x16x32_bf16 v[128:131], a[164:167], v[164:167], v[128:131]\n"
      "v_exp_f32 v119, v119\n"
      "v_mfma_f32_16x16x32_bf16 v[128:131], a[168:171], v[204:207], v[128:131]\n"
      "v_exp_f32 v57, v57\n"
      "v_mfma_f32_16x16x32_bf16 v[128:131], a[172:175], v[148:151], v[128:131]\n"
      "v_exp_f32 v58, v58\n"
      "v_mfma_f32_16x16x32_bf16 v[24:27], v[216:219], v[36:39], v[24:27]\n"
      "v_exp_f32 v55, v55\n"
      "v_mfma_f32_16x16x32_bf16 v[24:27], v[220:223], v[40:43], v[24:27]\n"
      "v_exp_f32 v99, v99\n"
      "v_mfma_f32_16x16x32_bf16 v[24:27], v[92:95], v[244:247], v[24:27]\n"
      "v_exp_f32 v124, v124\n"
      "v_mfma_f32_16x16x32_bf16 v[220:223], a[176:179], v[158:161], 0\n"
      "v_exp_f32 v56, v56\n"
      "v_mfma_f32_16x16x32_bf16 v[220:223], a[180:183], v[164:167], v[220:223]\n"
      "s_waitcnt lgkmcnt(0)\n"
      "v_mul_f32_dpp v33, v102, v33 row_newbcast:4 row_mask:0xf bank_mask:0xf\n"
      "v_mul_f32_dpp v35, v102, v35 row_newbcast:12 row_mask:0xf bank_mask:0xf\n"
      "v_mfma_f32_16x16x32_bf16 v[220:223], a[184:187], v[204:207], v[220:223]\n"
      "v_mul_f32_dpp v34, v102, v34 row_newbcast:8 row_mask:0xf bank_mask:0xf\n"
      "v_mul_f32_dpp v32, v102, v32 row_newbcast:0 row_mask:0xf bank_mask:0xf\n"
      "v_mfma_f32_16x16x32_bf16 v[220:223], a[188:191], v[148:151], v[220:223]\n"
      "v_mul_f32_dpp v24, v102, v24 row_newbcast:0 row_mask:0xf bank_mask:0xf\n"
      "v_mul_f32_dpp v26, v102, v26 row_newbcast:8 row_mask:0xf bank_mask:0xf\n"
      "v_mfma_f32_16x16x32_bf16 v[92:95], a[128:131], v[158:161], 0\n"
      "v_mul_f32_dpp v30, v102, v30 row_newbcast:8 row_mask:0xf bank_mask:0xf\n"
      "v_mul_f32_dpp v29, v102, v29 row_newbcast:4 row_mask:0xf bank_mask:0xf\n"
      "v_mfma_f32_16x16x32_bf16 v[92:95], a[132:135], v[164:167], v[92:95]\n"
      "v_mul_f32_dpp v28, v102, v28 row_newbcast:0 row_mask:0xf bank_mask:0xf\n"
      "v_mul_f32_dpp v31, v102, v31 row_newbcast:12 row_mask:0xf bank_mask:0xf\n"
      "v_mfma_f32_16x16x32_bf16 v[92:95], a[136:139], v[204:207], v[92:95]\n"
      "v_exp_f32 v51, v51\n"
      "v_mfma_f32_16x16x32_bf16 v[92:95], a[140:143], v[148:151], v[92:95]\n"
      "buffer_store_dwordx2 v[46:47], v2, s[44:47], s75 offen offset:512 nt sc1\n"
      "v_cvt_pk_bf16_f32 v219, v50, v51\n"
      "v_mfma_f32_16x16x32_bf16 v[28:31], v[232:235], a[224:227], v[28:31]\n"
      "v_mul_f32_dpp v25, v102, v25 row_newbcast:4 row_mask:0xf bank_mask:0xf\n"
      "v_mul_f32_dpp v27, v102, v27 row_newbcast:12 row_mask:0xf bank_mask:0xf\n"
      "v_mfma_f32_16x16x32_bf16 v[28:31], v[252:255], a[228:231], v[28:31]\n"
      "v_exp_f32 v48, v48\n"
      "v_mfma_f32_16x16x32_bf16 v[28:31], v[248:251], a[232:235], v[28:31]\n"
      "v_mul_f32_dpp v20, v102, v20 row_newbcast:0 row_mask:0xf bank_mask:0xf\n"
      "v_cvt_pk_bf16_f32 v217, v58, v59\n"
      "v_mfma_f32_16x16x32_bf16 v[28:31], v[180:183], a[236:239], v[28:31]\n"
      "buffer_store_dwordx2 v[44:45], v2, s[44:47], s75 offen offset:0 nt sc1\n"
      "v_cvt_pk_bf16_f32 v218, v48, v49\n"
      "v_mfma_f32_16x16x32_bf16 v[32:35], v[232:235], a[240:243], v[32:35]\n"
      "v_cvt_pk_bf16_f32 v216, v56, v57\n"
      "v_mul_f32_dpp v23, v102, v23 row_newbcast:12 row_mask:0xf bank_mask:0xf\n"
      "v_mfma_f32_16x16x32_bf16 v[32:35], v[252:255], a[244:247], v[32:35]\n"
      "v_cvt_pk_bf16_f32 v122, v220, v221\n"
      "v_mfma_f32_16x16x32_bf16 v[32:35], v[248:251], a[248:251], v[32:35]\n"
      "s_add_u32 s44, s44, 13107200\n s_addc_u32 s45, s45, 0\n"
      "v_mov_b64 v[178:179], 0\n"
      "v_mfma_f32_16x16x32_bf16 v[32:35], v[180:183], a[252:255], v[32:35]\n"
      "v_mul_f32_dpp v21, v102, v21 row_newbcast:4 row_mask:0xf bank_mask:0xf\n"
      "v_maximum3_f32 v8, v8, v100, v101\n"
      "v_mfma_f32_16x16x32_bf16 v[48:51], a[144:147], v[158:161], 0\n"
      "v_mul_f32_dpp v22, v102, v22 row_newbcast:8 row_mask:0xf bank_mask:0xf\n"
      "v_cvt_pk_bf16_f32 v123, v222, v223\n"
      "v_mfma_f32_16x16x32_bf16 v[48:51], a[148:151], v[164:167], v[48:51]\n"
      "v_exp_f32 v96, v96\n"
      "v_mfma_f32_16x16x32_bf16 v[48:51], a[152:155], v[204:207], v[48:51]\n"
      "L_h7_%=:\n"
      "s_cbranch_execz L_hr_%=\n"
      "v_exp_f32 v53, v53\n"
      "v_mfma_f32_16x16x32_bf16 v[48:51], a[156:159], v[148:151], v[48:51]\n"
      "v_exp_f32 v125, v125\n"
      "v_mfma_f32_16x16x32_bf16 v[24:27], v[232:235], a[208:211], v[24:27]\n"
      "v_exp_f32 v98, v98\n"
      "v_mfma_f32_16x16x32_bf16 v[24:27], v[252:255], a[212:215], v[24:27]\n"
      "v_exp_f32 v126, v126\n"
      "v_mfma_f32_16x16x32_bf16 v[24:27], v[248:251], a[216:219], v[24:27]\n"
      "v_exp_f32 v52, v52\n"
      "v_mfma_f32_16x16x32_bf16 v[24:27], v[180:183], a[220:223], v[24:27]\n"
      "v_cvt_pk_bf16_f32 v221, v120, v121\n"
      "v_cvt_pk_bf16_f32 v117, v94, v95\n"
      "v_mfma_f32_16x16x32_bf16 v[20:23], v[232:235], a[192:195], v[20:23]\n"
      "v_cvt_pk_bf16_f32 v75, v54, v55\n"
      "v_cvt_pk_bf16_f32 v223, v126, v127\n"
      "v_mfma_f32_16x16x32_bf16 v[20:23], v[252:255], a[196:199], v[20:23]\n"
      "v_cvt_pk_bf16_f32 v222, v124, v125\n"
      "v_cvt_pk_bf16_f32 v73, v98, v99\n"
      "v_mfma_f32_16x16x32_bf16 v[20:23], v[248:251], a[200:203], v[20:23]\n"
      "v_cvt_pk_bf16_f32 v220, v118, v119\n"
      "v_cvt_pk_bf16_f32 v120, v128, v129\n"
      "v_mfma_f32_16x16x32_bf16 v[20:23], v[180:183], a[204:207], v[20:23]\n"
      "v_cvt_pk_bf16_f32 v74, v52, v53\n"
      "v_cvt_pk_bf16_f32 v119, v50, v51\n"
      "v_smfmac_f32_16x16x64_bf16 v[200:203], v[12:15], v[216:223], v5\n"
      "ds_read_b128 v[180:183], v0 offset:1024\n"
      "v_cvt_pk_bf16_f32 v118, v48, v49\n"
      "v_mfma_f32_16x16x4_4b_bf16 v[44:59], v[18:19], v[156:157], 0 cbsz:2 abid:2\n"
      "ds_read_b128 v[232:235], v0 offset:0\n"
      "v_cvt_pk_bf16_f32 v116, v92, v93\n"
      "v_cvt_pk_bf16_f32 v72, v96, v97\n"
      "v_cvt_pk_bf16_f32 v20, v20, v24\n"
      "v_cvt_pk_bf16_f32 v22, v22, v26\n"
      "v_cvt_pk_bf16_f32 v26, v23, v27\n"
      "v_smfmac_f32_16x16x32_bf16 v[200:203], v[12:13], v[72:75], v5\n"
      "ds_read_b128 v[124:127], v0 offset:1536\n"
      "v_cvt_pk_bf16_f32 v121, v130, v131\n"
      "v_pk_max_i16 v22, v22, 0\n"
      "v_cvt_pk_bf16_f32 v24, v21, v25\n"
      "v_mfma_f32_16x16x32_bf16 v[96:99], v[104:107], v[116:119], 0\n"
      "v_cvt_pk_bf16_f32 v21, v28, v32\n"
      "v_pk_max_i16 v20, v20, 0\n"
      "v_mfma_f32_16x16x32_bf16 v[96:99], v[108:111], v[120:123], v[96:99]\n"
      "v_pk_max_i16 v26, v26, 0\n"
      "v_cvt_pk_bf16_f32 v27, v31, v35\n"
      "v_mfma_f32_16x16x32_bf16 v[100:103], v[236:239], v[116:119], 0\n"
      "v_cvt_pk_bf16_f32 v25, v29, v33\n"
      "v_mov_b64 v[176:177], 0\n"
      "v_mfma_f32_16x16x32_bf16 v[100:103], v[240:243], v[120:123], v[100:103]\n"
      "v_pk_max_i16 v27, v27, 0\n"
      "v_cvt_pk_bf16_f32 v23, v30, v34\n"
      "v_mfma_f32_16x16x32_bf16 v[248:251], v[184:187], v[116:119], 0\n"
      "ds_read_b128 v[128:131], v0 offset:512\n"
      "v_add_f32 v200, v200, v201\n"
      "v_mfma_f32_16x16x32_bf16 v[248:251], v[188:191], v[120:123], v[248:251]\n"
      "v_exp_f32 v97, v97\n"
      "v_mfma_f32_16x16x32_bf16 v[52:55], v[216:219], v[132:135], v[52:55]\n"
      "v_exp_f32 v100, v100\n"
      "v_mfma_f32_16x16x32_bf16 v[52:55], v[220:223], v[136:139], v[52:55]\n"
      "v_rcp_f32 v201, v200\n"
      "v_mfma_f32_16x16x32_bf16 v[52:55], v[72:75], v[60:63], v[52:55]\n"
      "v_exp_f32 v98, v98\n"
      "v_mfma_f32_16x16x32_bf16 v[48:51], v[216:219], v[36:39], v[48:51]\n"
      "ds_bpermute_b32 v202, v4, v201 offset:128\n"
      "v_mfma_f32_16x16x32_bf16 v[48:51], v[220:223], v[40:43], v[48:51]\n"
      "v_exp_f32 v101, v101\n"
      "v_mfma_f32_16x16x32_bf16 v[48:51], v[72:75], v[244:247], v[48:51]\n"
      "v_exp_f32 v102, v102\n"
      "v_mfma_f32_16x16x32_bf16 v[28:31], v[168:171], v[116:119], 0\n"
      "v_pk_max_i16 v23, v23, 0\n"
      "v_pk_max_i16 v25, v25, 0\n"
      "v_mfma_f32_16x16x32_bf16 v[28:31], v[172:175], v[120:123], v[28:31]\n"
      "v_exp_f32 v248, v248\n"
      "v_mfma_f32_16x16x32_bf16 v[56:59], v[216:219], v[224:227], v[56:59]\n"
      "v_exp_f32 v251, v251\n"
      "v_mfma_f32_16x16x32_bf16 v[56:59], v[220:223], v[228:231], v[56:59]\n"
      "v_exp_f32 v99, v99\n"
      "v_mfma_f32_16x16x32_bf16 v[56:59], v[72:75], v[196:199], v[56:59]\n"
      "v_pk_max_i16 v24, v24, 0\n"
      "v_pk_max_i16 v21, v21, 0\n"
      "v_mfma_f32_16x16x32_bf16 v[44:47], v[216:219], v[140:143], v[44:47]\n"
      "v_exp_f32 v249, v249\n"
      "v_mfma_f32_16x16x32_bf16 v[44:47], v[220:223], v[144:147], v[44:47]\n"
      "v_exp_f32 v31, v31\n"
      "v_mfma_f32_16x16x32_bf16 v[44:47], v[72:75], v[88:91], v[44:47]\n"
      "buffer_store_dwordx2 v[20:21], v2, s[44:47], s100 offen offset:0 nt sc1\n"
      "v_cvt_pk_bf16_f32 v218, v248, v249\n"
      "v_mfma_f32_16x16x32_bf16 v[32:35], v[208:211], v[116:119], 0\n"
      "s_waitcnt lgkmcnt(0)\n"
      "v_mul_f32_dpp v51, v202, v51 row_newbcast:12 row_mask:0xf bank_mask:0xf\n"
      "v_mul_f32_dpp v53, v202, v53 row_newbcast:4 row_mask:0xf bank_mask:0xf\n"
      "v_mfma_f32_16x16x32_bf16 v[32:35], v[212:215], v[120:123], v[32:35]\n"
      "v_exp_f32 v250, v250\n"
      "v_mfma_f32_16x16x32_bf16 v[18:21], a[176:179], v[232:235], 0\n"
      "v_mul_f32_dpp v48, v202, v48 row_newbcast:0 row_mask:0xf bank_mask:0xf\n"
      "v_cvt_pk_bf16_f32 v219, v250, v251\n"
      "v_mfma_f32_16x16x32_bf16 v[18:21], a[180:183], v[128:131], v[18:21]\n"
      "v_mul_f32_dpp v49, v202, v49 row_newbcast:4 row_mask:0xf bank_mask:0xf\n"
      "v_mul_f32_dpp v50, v202, v50 row_newbcast:8 row_mask:0xf bank_mask:0xf\n"
      "v_mfma_f32_16x16x32_bf16 v[18:21], a[184:187], v[180:183], v[18:21]\n"
      "v_exp_f32 v28, v28\n"
      "v_mfma_f32_16x16x32_bf16 v[18:21], a[188:191], v[124:127], v[18:21]\n"
      "v_exp_f32 v33, v33\n"
      "v_smfmac_f32_16x16x64_bf16 v[176:179], v[112:115], v[116:123], v5\n"
      "buffer_store_dwordx2 v[22:23], v2, s[44:47], s100 offen offset:512 nt sc1\n"
      "v_mul_f32_dpp v52, v202, v52 row_newbcast:0 row_mask:0xf bank_mask:0xf\n"
      "v_mfma_f32_16x16x32_bf16 v[112:115], a[144:147], v[232:235], 0\n"
      "v_exp_f32 v32, v32\n"
      "v_mfma_f32_16x16x32_bf16 v[112:115], a[148:151], v[128:131], v[112:115]\n"
      "v_exp_f32 v29, v29\n"
      "v_mfma_f32_16x16x32_bf16 v[112:115], a[152:155], v[180:183], v[112:115]\n"
      "v_cvt_pk_bf16_f32 v255, v20, v21\n"
      "v_mul_f32_dpp v57, v202, v57 row_newbcast:4 row_mask:0xf bank_mask:0xf\n"
      "v_mfma_f32_16x16x32_bf16 v[112:115], a[156:159], v[124:127], v[112:115]\n"
      "v_exp_f32 v96, v96\n"
      "v_mfma_f32_16x16x32_bf16 v[220:223], a[128:131], v[232:235], 0\n"
      "v_exp_f32 v103, v103\n"
      "v_mfma_f32_16x16x32_bf16 v[220:223], a[132:135], v[128:131], v[220:223]\n"
      "v_mul_f32_dpp v47, v202, v47 row_newbcast:12 row_mask:0xf bank_mask:0xf\n"
      "v_cvt_pk_bf16_f32 v254, v18, v19\n"
      "v_mfma_f32_16x16x32_bf16 v[220:223], a[136:139], v[180:183], v[220:223]\n"
      "v_exp_f32 v35, v35\n"
      "v_mfma_f32_16x16x32_bf16 v[220:223], a[140:143], v[124:127], v[220:223]\n"
      "v_mul_f32_dpp v54, v202, v54 row_newbcast:8 row_mask:0xf bank_mask:0xf\n"
      "v_mul_f32_dpp v46, v202, v46 row_newbcast:8 row_mask:0xf bank_mask:0xf\n"
      "v_mfma_f32_16x16x32_bf16 v[20:23], v[64:67], v[116:119], 0\n"
      "v_mul_f32_dpp v59, v202, v59 row_newbcast:12 row_mask:0xf bank_mask:0xf\n"
      "v_mul_f32_dpp v44, v202, v44 row_newbcast:0 row_mask:0xf bank_mask:0xf\n"
      "v_mfma_f32_16x16x32_bf16 v[20:23], v[68:71], v[120:123], v[20:23]\n"
      "v_cvt_pk_bf16_f32 v251, v114, v115\n"
      "v_cvt_pk_bf16_f32 v250, v112, v113\n"
      "v_mfma_f32_16x16x32_bf16 v[120:123], a[160:163], v[232:235], 0\n"
      "v_mul_f32_dpp v45, v202, v45 row_newbcast:4 row_mask:0xf bank_mask:0xf\n"
      "v_cvt_pk_bf16_f32 v117, v102, v103\n"
      "v_mfma_f32_16x16x32_bf16 v[120:123], a[164:167], v[128:131], v[120:123]\n"
      "v_cvt_pk_bf16_f32 v249, v222, v223\n"
      "v_cvt_pk_bf16_f32 v116, v100, v101\n"
      "v_mfma_f32_16x16x32_bf16 v[120:123], a[168:171], v[180:183], v[120:123]\n"
      "v_exp_f32 v30, v30\n"
      "v_mfma_f32_16x16x32_bf16 v[120:123], a[172:175], v[124:127], v[120:123]\n"
      "v_exp_f32 v22, v22\n"
      "v_mfma_f32_16x16x32_bf16 v[48:51], v[152:155], a[208:211], v[48:51]\n"
      "v_mul_f32_dpp v55, v202, v55 row_newbcast:12 row_mask:0xf bank_mask:0xf\n"
      "v_cvt_pk_bf16_f32 v248, v220, v221\n"
      "v_mfma_f32_16x16x32_bf16 v[48:51], v[80:83], a[212:215], v[48:51]\n"
      "v_exp_f32 v34, v34\n"
      "v_mfma_f32_16x16x32_bf16 v[48:51], v[84:87], a[216:219], v[48:51]\n"
      "s_nop 0\n"
      "v_cvt_pk_bf16_f32 v252, v120, v121\n"
      "v_cvt_pk_bf16_f32 v253, v122, v123\n"
      "v_mfma_f32_16x16x32_bf16 v[48:51], v[76:79], a[220:223], v[48:51]\n"
      "v_cvt_pk_bf16_f32 v220, v96, v97\n"
      "v_mul_f32_dpp v56, v202, v56 row_newbcast:0 row_mask:0xf bank_mask:0xf\n"
      "v_mfma_f32_16x16x32_bf16 v[120:123], v[184:187], v[248:251], 0\n"
      "v_mul_f32_dpp v58, v202, v58 row_newbcast:8 row_mask:0xf bank_mask:0xf\n"
      "v_cvt_pk_bf16_f32 v221, v98, v99\n"
      "v_mfma_f32_16x16x32_bf16 v[120:123], v[188:191], v[252:255], v[120:123]\n"
      "v_cvt_pk_bf16_f32 v223, v30, v31\n"
      "v_cvt_pk_bf16_f32 v222, v28, v29\n"
      "v_smfmac_f32_16x16x64_bf16 v[176:179], v[192:195], v[248:255], v5\n"
      "buffer_store_dwordx2 v[24:25], v2, s[44:47], s100 offen offset:256 nt sc1\n"
      "v_cvt_pk_bf16_f32 v216, v32, v33\n"
      "v_mfma_f32_16x16x32_bf16 v[192:195], v[236:239], v[248:251], 0\n"
      "v_exp_f32 v20, v20\n"
      "v_mfma_f32_16x16x32_bf16 v[192:195], v[240:243], v[252:255], v[192:195]\n"
      "v_exp_f32 v23, v23\n"
      "v_mfma_f32_16x16x32_bf16 v[52:55], v[152:155], a[224:227], v[52:55]\n"
      "s_nop 0\n"
      "v_add_f32 v176, v176, v177\n"
      "v_add_f32 v178, v178, v179\n"
      "v_mfma_f32_16x16x32_bf16 v[52:55], v[80:83], a[228:231], v[52:55]\n"
      "v_exp_f32 v178, v178\n"
      "v_mfma_f32_16x16x32_bf16 v[52:55], v[84:87], a[232:235], v[52:55]\n"
      "v_exp_f32 v123, v123\n"
      "v_mfma_f32_16x16x32_bf16 v[52:55], v[76:79], a[236:239], v[52:55]\n"
      "v_exp_f32 v176, v176\n"
      "v_mfma_f32_16x16x32_bf16 v[236:239], v[64:67], v[248:251], 0\n"
      "v_cvt_pk_bf16_f32 v189, v176, v178\n"
      "v_add_f32 v240, v176, v178\n"
      "v_mfma_f32_16x16x32_bf16 v[236:239], v[68:71], v[252:255], v[236:239]\n"
      "v_cvt_pk_bf16_f32 v119, v22, v23\n"
      "v_cvt_pk_bf16_f32 v188, v176, v178\n"
      "v_mfma_f32_16x16x32_bf16 v[44:47], v[152:155], a[192:195], v[44:47]\n"
      "v_exp_f32 v121, v121\n"
      "v_mfma_f32_16x16x32_bf16 v[44:47], v[80:83], a[196:199], v[44:47]\n"
      "v_permlane16_swap_b32 v188, v189\n"
      "v_mfma_f32_16x16x32_bf16 v[44:47], v[84:87], a[200:203], v[44:47]\n"
      "v_cvt_pk_bf16_f32 v217, v34, v35\n"
      "v_add_f32 v241, v176, v178\n"
      "v_mfma_f32_16x16x32_bf16 v[44:47], v[76:79], a[204:207], v[44:47]\n"
      "v_exp_f32 v236, v236\n"
      "v_mfma_f32_16x16x32_bf16 v[92:95], v[168:171], v[248:251], 0\n"
      "v_permlane16_swap_b32 v240, v241\n"
      "v_mfma_f32_16x16x32_bf16 v[92:95], v[172:175], v[252:255], v[92:95]\n"
      "v_exp_f32 v21, v21\n"
      "v_mfma_f32_16x16x32_bf16 v[56:59], v[152:155], a[240:243], v[56:59]\n"
      "v_exp_f32 v120, v120\n"
      "v_mfma_f32_16x16x32_bf16 v[56:59], v[80:83], a[244:247], v[56:59]\n"
      "v_exp_f32 v194, v194\n"
      "v_mfma_f32_16x16x32_bf16 v[56:59], v[84:87], a[248:251], v[56:59]\n"
      "v_exp_f32 v237, v237\n"
      "v_mfma_f32_16x16x32_bf16 v[56:59], v[76:79], a[252:255], v[56:59]\n"
      "v_cvt_pk_bf16_f32 v170, v120, v121\n"
      "v_mov_b64 v[76:77], v[240:241]\n"
      "v_smfmac_f32_16x16x64_bf16 v[240:243], v[12:15], v[216:223], v5\n"
      "buffer_store_dwordx2 v[26:27], v2, s[44:47], s100 offen offset:768 nt sc1\n"
      "v_cvt_pk_bf16_f32 v118, v20, v21\n"
      "v_mfma_f32_16x16x32_bf16 v[84:87], v[208:211], v[248:251], 0\n"
      "v_exp_f32 v239, v239\n"
      "v_mfma_f32_16x16x32_bf16 v[84:87], v[212:215], v[252:255], v[84:87]\n"
      "v_cvt_pk_bf16_f32 v44, v44, v48\n"
      "v_cvt_pk_bf16_f32 v48, v45, v49\n"
      "v_cvt_pk_bf16_f32 v49, v53, v57\n"
      "v_cvt_pk_bf16_f32 v46, v46, v50\n"
      "v_smfmac_f32_16x16x32_bf16 v[240:243], v[12:13], v[116:119], v5\n"
      "v_pk_max_i16 v49, v49, 0\n"
      "v_mfma_f32_16x16x4_4b_bf16 v[20:35], v[188:189], v[156:157], 0 cbsz:2 abid:0\n"
      "v_cvt_pk_bf16_f32 v50, v47, v51\n"
      "v_cvt_pk_bf16_f32 v47, v54, v58\n"
      "v_pk_max_i16 v47, v47, 0\n"
      "v_cvt_pk_bf16_f32 v51, v55, v59\n"
      "v_cvt_pk_bf16_f32 v45, v52, v56\n"
      "v_exp_f32 v122, v122\n"
      "v_pk_max_i16 v44, v44, 0\n"
      "v_mfma_f32_16x16x32_bf16 v[212:215], v[104:107], v[248:251], 0\n"
      "v_pk_max_i16 v45, v45, 0\n"
      "v_pk_max_i16 v46, v46, 0\n"
      "v_mfma_f32_16x16x32_bf16 v[212:215], v[108:111], v[252:255], v[212:215]\n"
      "buffer_store_dwordx2 v[44:45], v2, s[44:47], s101 offen offset:0 nt sc1\n"
      "v_add_f32 v240, v240, v241\n"
      "v_mfma_f32_16x16x32_bf16 v[32:35], v[216:219], v[224:227], v[32:35]\n"
      "v_rcp_f32 v241, v240\n"
      "v_mfma_f32_16x16x32_bf16 v[32:35], v[220:223], v[228:231], v[32:35]\n"
      "ds_bpermute_b32 v242, v4, v241\n"
      "v_mfma_f32_16x16x32_bf16 v[32:35], v[116:119], v[196:199], v[32:35]\n"
      "v_exp_f32 v84, v84\n"
      "v_mfma_f32_16x16x32_bf16 v[20:23], v[216:219], v[140:143], v[20:23]\n"
      "v_exp_f32 v214, v214\n"
      "v_mfma_f32_16x16x32_bf16 v[20:23], v[220:223], v[144:147], v[20:23]\n"
      "v_exp_f32 v87, v87\n"
      "v_mfma_f32_16x16x32_bf16 v[20:23], v[116:119], v[88:91], v[20:23]\n"
      "v_exp_f32 v213, v213\n"
      "v_mfma_f32_16x16x32_bf16 v[24:27], v[216:219], v[36:39], v[24:27]\n"
      "v_exp_f32 v85, v85\n"
      "v_mfma_f32_16x16x32_bf16 v[24:27], v[220:223], v[40:43], v[24:27]\n"
      "v_exp_f32 v95, v95\n"
      "v_mfma_f32_16x16x32_bf16 v[24:27], v[116:119], v[244:247], v[24:27]\n"
      "buffer_store_dwordx2 v[46:47], v2, s[44:47], s101 offen offset:512 nt sc1\n"
      "v_pk_max_i16 v48, v48, 0\n"
      "v_mfma_f32_16x16x32_bf16 v[28:31], v[216:219], v[132:135], v[28:31]\n"
      "v_exp_f32 v86, v86\n"
      "v_mfma_f32_16x16x32_bf16 v[28:31], v[220:223], v[136:139], v[28:31]\n"
      "v_exp_f32 v215, v215\n"
      "v_mfma_f32_16x16x32_bf16 v[28:31], v[116:119], v[60:63], v[28:31]\n"
      "s_waitcnt lgkmcnt(0)\n"
      "v_mul_f32_dpp v21, v242, v21 row_newbcast:4 row_mask:0xf bank_mask:0xf\n"
      "v_mul_f32_dpp v24, v242, v24 row_newbcast:0 row_mask:0xf bank_mask:0xf\n"
      "v_mfma_f32_16x16x4_4b_bf16 v[104:119], v[188:189], v[156:157], 0 cbsz:2 abid:2\n"
      "v_mul_f32_dpp v26, v242, v26 row_newbcast:8 row_mask:0xf bank_mask:0xf\n"
      "v_cvt_pk_bf16_f32 v173, v214, v215\n"
      "v_mul_f32_dpp v27, v242, v27 row_newbcast:12 row_mask:0xf bank_mask:0xf\n"
      "v_mul_f32_dpp v32, v242, v32 row_newbcast:0 row_mask:0xf bank_mask:0xf\n"
      "v_mul_f32_dpp v23, v242, v23 row_newbcast:12 row_mask:0xf bank_mask:0xf\n"
      "v_mul_f32_dpp v31, v242, v31 row_newbcast:12 row_mask:0xf bank_mask:0xf\n"
      "v_mul_f32_dpp v25, v242, v25 row_newbcast:4 row_mask:0xf bank_mask:0xf\n"
      "v_mul_f32_dpp v35, v242, v35 row_newbcast:12 row_mask:0xf bank_mask:0xf\n"
      "v_cvt_pk_bf16_f32 v169, v86, v87\n"
      "v_mfma_f32_16x16x32_bf16 v[24:27], v[158:161], a[208:211], v[24:27]\n"
      "v_mul_f32_dpp v34, v242, v34 row_newbcast:8 row_mask:0xf bank_mask:0xf\n"
      "v_mul_f32_dpp v20, v242, v20 row_newbcast:0 row_mask:0xf bank_mask:0xf\n"
      "v_mfma_f32_16x16x32_bf16 v[24:27], v[164:167], a[212:215], v[24:27]\n"
      "v_exp_f32 v94, v94\n"
      "v_mfma_f32_16x16x32_bf16 v[24:27], v[204:207], a[216:219], v[24:27]\n"
      "v_exp_f32 v92, v92\n"
      "v_mfma_f32_16x16x32_bf16 v[24:27], v[148:151], a[220:223], v[24:27]\n"
      "buffer_store_dwordx2 v[48:49], v2, s[44:47], s101 offen offset:256 nt sc1\n"
      "v_mul_f32_dpp v22, v242, v22 row_newbcast:8 row_mask:0xf bank_mask:0xf\n"
      "v_mul_f32_dpp v29, v242, v29 row_newbcast:4 row_mask:0xf bank_mask:0xf\n"
      "v_cvt_pk_bf16_f32 v175, v94, v95\n"
      "v_mfma_f32_16x16x32_bf16 v[20:23], v[158:161], a[192:195], v[20:23]\n"
      "v_exp_f32 v212, v212\n"
      "v_mfma_f32_16x16x32_bf16 v[20:23], v[164:167], a[196:199], v[20:23]\n"
      "v_cvt_pk_bf16_f32 v172, v212, v213\n"
      "v_mul_f32_dpp v33, v242, v33 row_newbcast:4 row_mask:0xf bank_mask:0xf\n"
      "v_mfma_f32_16x16x32_bf16 v[20:23], v[204:207], a[200:203], v[20:23]\n"
      "v_exp_f32 v93, v93\n"
      "v_mfma_f32_16x16x32_bf16 v[20:23], v[148:151], a[204:207], v[20:23]\n"
      "v_cvt_pk_bf16_f32 v171, v122, v123\n"
      "v_cvt_pk_bf16_f32 v174, v92, v93\n"
      "v_pk_max_i16 v51, v51, 0\n"
      "v_mfma_f32_16x16x32_bf16 v[32:35], v[158:161], a[240:243], v[32:35]\n"
      "v_exp_f32 v192, v192\n"
      "v_mfma_f32_16x16x32_bf16 v[32:35], v[164:167], a[244:247], v[32:35]\n"
      "v_cvt_pk_bf16_f32 v168, v84, v85\n"
      "v_mul_f32_dpp v28, v242, v28 row_newbcast:0 row_mask:0xf bank_mask:0xf\n"
      "v_mfma_f32_16x16x32_bf16 v[32:35], v[204:207], a[248:251], v[32:35]\n"
      "v_exp_f32 v238, v238\n"
      "v_mfma_f32_16x16x32_bf16 v[32:35], v[148:151], a[252:255], v[32:35]\n"
      "v_mul_f32_dpp v30, v242, v30 row_newbcast:8 row_mask:0xf bank_mask:0xf\n"
      "v_pk_max_i16 v50, v50, 0\n"
      "v_smfmac_f32_16x16x64_bf16 v[76:79], v[12:15], v[168:175], v5\n"
      "buffer_store_dwordx2 v[50:51], v2, s[44:47], s101 offen offset:768 nt sc1\n"
      "v_cvt_pk_bf16_f32 v94, v236, v237\n"
      "v_mfma_f32_16x16x32_bf16 v[28:31], v[158:161], a[224:227], v[28:31]\n"
      "v_exp_f32 v193, v193\n"
      "v_mfma_f32_16x16x32_bf16 v[28:31], v[164:167], a[228:231], v[28:31]\n"
      "v_cvt_pk_bf16_f32 v92, v192, v193\n"
      "v_cvt_pk_bf16_f32 v95, v238, v239\n"
      "v_mfma_f32_16x16x32_bf16 v[28:31], v[204:207], a[232:235], v[28:31]\n"
      "v_exp_f32 v195, v195\n"
      "v_mfma_f32_16x16x32_bf16 v[28:31], v[148:151], a[236:239], v[28:31]\n"
      "v_cvt_pk_bf16_f32 v93, v194, v195\n"
      "v_cvt_pk_bf16_f32 v22, v22, v26\n"
      "v_cvt_pk_bf16_f32 v26, v23, v27\n"
      "v_pk_max_i16 v26, v26, 0\n"
      "v_smfmac_f32_16x16x32_bf16 v[76:79], v[12:13], v[92:95], v5\n"
      "s_nop 2\n"
      "v_cvt_pk_bf16_f32 v27, v31, v35\n"
      "v_pk_max_i16 v27, v27, 0\n"
      "v_mfma_f32_16x16x32_bf16 v[104:107], v[168:171], v[140:143], v[104:107]\n"
      "s_nop 1\n"
      "v_add_f32 v76, v76, v77\n"
      "v_mfma_f32_16x16x32_bf16 v[104:107], v[172:175], v[144:147], v[104:107]\n"
      "v_rcp_f32 v77, v76\n"
      "v_mfma_f32_16x16x32_bf16 v[104:107], v[92:95], v[88:91], v[104:107]\n"
      "buffer_store_dwordx2 v[26:27], v2, s[44:47], s34 offen offset:768 nt sc1\n"
      "ds_bpermute_b32 v78, v4, v77 offset:128\n"
      "v_mfma_f32_16x16x32_bf16 v[108:111], v[168:171], v[36:39], v[108:111]\n"
      "v_cvt_pk_bf16_f32 v20, v20, v24\n"
      "v_cvt_pk_bf16_f32 v24, v21, v25\n"
      "v_mfma_f32_16x16x32_bf16 v[108:111], v[172:175], v[40:43], v[108:111]\n"
      "v_mfma_f32_16x16x32_bf16 v[108:111], v[92:95], v[244:247], v[108:111]\n"
      "v_cvt_pk_bf16_f32 v25, v29, v33\n"
      "v_pk_max_i16 v20, v20, 0\n"
      "v_mfma_f32_16x16x32_bf16 v[116:119], v[168:171], v[224:227], v[116:119]\n"
      "v_pk_max_i16 v25, v25, 0\n"
      "v_pk_max_i16 v24, v24, 0\n"
      "v_mfma_f32_16x16x32_bf16 v[116:119], v[172:175], v[228:231], v[116:119]\n"
      "v_mfma_f32_16x16x32_bf16 v[116:119], v[92:95], v[196:199], v[116:119]\n"
      "buffer_store_dwordx2 v[24:25], v2, s[44:47], s34 offen offset:256 nt sc1\n"
      "v_pk_max_i16 v22, v22, 0\n"
      "v_mfma_f32_16x16x32_bf16 v[112:115], v[168:171], v[132:135], v[112:115]\n"
      "v_cvt_pk_bf16_f32 v23, v30, v34\n"
      "v_cvt_pk_bf16_f32 v21, v28, v32\n"
      "v_mfma_f32_16x16x32_bf16 v[112:115], v[172:175], v[136:139], v[112:115]\n"
      "v_mfma_f32_16x16x32_bf16 v[112:115], v[92:95], v[60:63], v[112:115]\n"
      "s_waitcnt lgkmcnt(0)\n"
      "v_mul_f32_dpp v110, v78, v110 row_newbcast:8 row_mask:0xf bank_mask:0xf\n"
      "v_mul_f32_dpp v109, v78, v109 row_newbcast:4 row_mask:0xf bank_mask:0xf\n"
      "v_mul_f32_dpp v111, v78, v111 row_newbcast:12 row_mask:0xf bank_mask:0xf\n"
      "v_mul_f32_dpp v108, v78, v108 row_newbcast:0 row_mask:0xf bank_mask:0xf\n"
      "v_mul_f32_dpp v106, v78, v106 row_newbcast:8 row_mask:0xf bank_mask:0xf\n"
      "v_pk_max_i16 v23, v23, 0\n"
      "v_mfma_f32_16x16x32_bf16 v[108:111], v[232:235], a[208:211], v[108:111]\n"
      "v_mul_f32_dpp v104, v78, v104 row_newbcast:0 row_mask:0xf bank_mask:0xf\n"
      "v_mfma_f32_16x16x32_bf16 v[108:111], v[128:131], a[212:215], v[108:111]\n"
      "v_mul_f32_dpp v105, v78, v105 row_newbcast:4 row_mask:0xf bank_mask:0xf\n"
      "v_pk_max_i16 v21, v21, 0\n"
      "v_mfma_f32_16x16x32_bf16 v[108:111], v[180:183], a[216:219], v[108:111]\n"
      "v_mul_f32_dpp v118, v78, v118 row_newbcast:8 row_mask:0xf bank_mask:0xf\n"
      "v_mul_f32_dpp v107, v78, v107 row_newbcast:12 row_mask:0xf bank_mask:0xf\n"
      "v_mfma_f32_16x16x32_bf16 v[108:111], v[124:127], a[220:223], v[108:111]\n"
      "buffer_store_dwordx2 v[22:23], v2, s[44:47], s34 offen offset:512 nt sc1\n"
      "v_mul_f32_dpp v116, v78, v116 row_newbcast:0 row_mask:0xf bank_mask:0xf\n"
      "v_mfma_f32_16x16x32_bf16 v[104:107], v[232:235], a[192:195], v[104:107]\n"
      "v_mul_f32_dpp v114, v78, v114 row_newbcast:8 row_mask:0xf bank_mask:0xf\n"
      "v_mul_f32_dpp v113, v78, v113 row_newbcast:4 row_mask:0xf bank_mask:0xf\n"
      "v_mfma_f32_16x16x32_bf16 v[104:107], v[128:131], a[196:199], v[104:107]\n"
      "v_mul_f32_dpp v119, v78, v119 row_newbcast:12 row_mask:0xf bank_mask:0xf\n"
      "v_mul_f32_dpp v117, v78, v117 row_newbcast:4 row_mask:0xf bank_mask:0xf\n"
      "v_mfma_f32_16x16x32_bf16 v[104:107], v[180:183], a[200:203], v[104:107]\n"
      "v_mfma_f32_16x16x32_bf16 v[104:107], v[124:127], a[204:207], v[104:107]\n"
      "buffer_store_dwordx2 v[20:21], v2, s[44:47], s34 offen offset:0 nt sc1\n"
      "v_mul_f32_dpp v115, v78, v115 row_newbcast:12 row_mask:0xf bank_mask:0xf\n"
      "v_mfma_f32_16x16x32_bf16 v[116:119], v[232:235], a[240:243], v[116:119]\n"
      "v_mul_f32_dpp v112, v78, v112 row_newbcast:0 row_mask:0xf bank_mask:0xf\n"
      "v_mfma_f32_16x16x32_bf16 v[116:119], v[128:131], a[244:247], v[116:119]\n"
      "v_mfma_f32_16x16x32_bf16 v[116:119], v[180:183], a[248:251], v[116:119]\n"
      "v_mfma_f32_16x16x32_bf16 v[116:119], v[124:127], a[252:255], v[116:119]\n"
      "s_nop 0\n"
      "v_cvt_pk_bf16_f32 v106, v106, v110\n"
      "v_mfma_f32_16x16x32_bf16 v[112:115], v[232:235], a[224:227], v[112:115]\n"
      "v_pk_max_i16 v106, v106, 0\n"
      "v_mfma_f32_16x16x32_bf16 v[112:115], v[128:131], a[228:231], v[112:115]\n"
      "v_cvt_pk_bf16_f32 v110, v107, v111\n"
      "v_mfma_f32_16x16x32_bf16 v[112:115], v[180:183], a[232:235], v[112:115]\n"
      "v_mfma_f32_16x16x32_bf16 v[112:115], v[124:127], a[236:239], v[112:115]\n"
      "s_nop 7\n"
      "v_cvt_pk_bf16_f32 v107, v114, v118\n"
      "v_pk_max_i16 v107, v107, 0\n"
      "buffer_store_dwordx2 v[106:107], v2, s[44:47], s35 offen offset:512 nt sc1\n"
      "v_cvt_pk_bf16_f32 v104, v104, v108\n"
      "v_cvt_pk_bf16_f32 v108, v105, v109\n"
      "v_cvt_pk_bf16_f32 v109, v113, v117\n"
      "v_pk_max_i16 v108, v108, 0\n"
      "v_cvt_pk_bf16_f32 v111, v115, v119\n"
      "v_pk_max_i16 v111, v111, 0\n"
      "v_pk_max_i16 v109, v109, 0\n"
      "v_maximum3_f32 v8, v8, v200, v201\n"
      "buffer_store_dwordx2 v[108:109], v2, s[44:47], s35 offen offset:256 nt sc1\n"
      "v_maximum3_f32 v8, v8, v240, v241\n"
      "v_pk_max_i16 v110, v110, 0\n"
      "s_not_b64 vcc, vcc\n"
      "v_cvt_pk_bf16_f32 v105, v112, v116\n"
      "v_pk_max_i16 v105, v105, 0\n"
      "buffer_store_dwordx2 v[110:111], v2, s[44:47], s35 offen offset:768 nt sc1\n"
      "v_pk_max_i16 v104, v104, 0\n"
      "v_maximum3_f32 v8, v8, v76, v77\n"
      "buffer_store_dwordx2 v[104:105], v2, s[44:47], s35 offen offset:0 nt sc1\n"
      "L_end_%=:\n"
      "s_waitcnt vmcnt(0) lgkmcnt(0)\n"
      "v_cmp_nge_f32 vcc, 0x53800000, v8\n"
      "s_nop 4\n"
      "s_cbranch_vccnz L_ai_end\n"
      "s_endpgm\n"
      "L_ai_end:\n"
      "s_mov_b32 s40, s4\n"
      "s_and_b32 s41, s5, 0xffff\n"
      "s_mov_b32 s42, 104857600\n"
      "s_mov_b32 s44, s14\n"
      "s_and_b32 s45, s15, 0xffff\n"
      "s_mov_b32 s46, 104857600\n"
      "s_mov_b32 s48, s18\n"
      "s_add_u32 s49, s18, 13107200\n"
      "s_mov_b32 s50, 8\n"
      "s_mov_b32 vcc_lo, 0x55555555\n"
      "s_mov_b32 vcc_hi, 0x55555555\n"
      "v_subrev_u32 v9, 448, v9\n"
      "v_subrev_u32 v10, 448, v10\n"
      "v_subrev_u32 v11, 3584, v11\n"
      "s_add_u32 s52, s48, 0\n"
      "s_add_u32 m0, s53, 0\n"
      "s_nop 0\n"
      "buffer_load_dwordx4 v1, s[40:43], s52 offen offset:0 lds\n"
      "buffer_load_dwordx4 v1, s[40:43], s52 offen offset:2048 lds\n"
      "s_add_u32 m0, s53, 896\n"
      "s_nop 0\n"
      "buffer_load_dwordx4 v1, s[40:43], s52 offen offset:128 lds\n"
      "buffer_load_dwordx4 v1, s[40:43], s52 offen offset:2176 lds\n"
      "s_add_u32 s52, s48, 4096\n"
      "s_add_u32 m0, s53, 4096\n"
      "s_nop 0\n"
      "buffer_load_dwordx4 v1, s[40:43], s52 offen offset:0 lds\n"
      "buffer_load_dwordx4 v1, s[40:43], s52 offen offset:2048 lds\n"
      "s_add_u32 m0, s53, 4992\n"
      "s_nop 0\n"
      "buffer_load_dwordx4 v1, s[40:43], s52 offen offset:128 lds\n"
      "buffer_load_dwordx4 v1, s[40:43], s52 offen offset:2176 lds\n"
      "s_add_u32 s52, s48, 8192\n"
      "s_add_u32 m0, s53, 8192\n"
      "s_nop 0\n"
      "buffer_load_dwordx4 v1, s[40:43], s52 offen offset:0 lds\n"
      "buffer_load_dwordx4 v1, s[40:43], s52 offen offset:2048 lds\n"
      "s_add_u32 m0, s53, 9088\n"
      "s_nop 0\n"
      "buffer_load_dwordx4 v1, s[40:43], s52 offen offset:128 lds\n"
      "buffer_load_dwordx4 v1, s[40:43], s52 offen offset:2176 lds\n"
      "s_add_u32 s52, s48, 12288\n"
      "s_add_u32 m0, s53, 12288\n"
      "s_nop 0\n"
      "buffer_load_dwordx4 v1, s[40:43], s52 offen offset:0 lds\n"
      "buffer_load_dwordx4 v1, s[40:43], s52 offen offset:2048 lds\n"
      "s_add_u32 m0, s53, 13184\n"
      "s_nop 0\n"
      "buffer_load_dwordx4 v1, s[40:43], s52 offen offset:128 lds\n"
      "buffer_load_dwordx4 v1, s[40:43], s52 offen offset:2176 lds\n"
      "s_add_u32 s52, s48, 16384\n"
      "s_add_u32 m0, s53, 16384\n"
      "s_nop 0\n"
      "buffer_load_dwordx4 v1, s[40:43], s52 offen offset:0 lds\n"
      "buffer_load_dwordx4 v1, s[40:43], s52 offen offset:2048 lds\n"
      "s_add_u32 m0, s53, 17280\n"
      "s_nop 0\n"
      "buffer_load_dwordx4 v1, s[40:43], s52 offen offset:128 lds\n"
      "buffer_load_dwordx4 v1, s[40:43], s52 offen offset:2176 lds\n"
      "s_add_u32 s52, s48, 20480\n"
      "s_add_u32 m0, s53, 20480\n"
      "s_nop 0\n"
      "buffer_load_dwordx4 v1, s[40:43], s52 offen offset:0 lds\n"
      "buffer_load_dwordx4 v1, s[40:43], s52 offen offset:2048 lds\n"
      "s_add_u32 m0, s53, 21376\n"
      "s_nop 0\n"
      "buffer_load_dwordx4 v1, s[40:43], s52 offen offset:128 lds\n"
      "buffer_load_dwordx4 v1, s[40:43], s52 offen offset:2176 lds\n"
      "s_waitcnt vmcnt(0)\n"
      "L_safe_%=:\n"
      "s_waitcnt vmcnt(45)\n"
      "ds_read_b128 v[148:151], v0 offset:12288\n"
      "ds_read_b128 v[136:139], v0 offset:12800\n"
      "ds_read_b128 v[140:143], v0 offset:13312\n"
      "ds_read_b128 v[144:147], v0 offset:13824\n"
      "s_waitcnt vmcnt(41)\n"
      "ds_read_b128 v[252:255], v0 offset:16384\n"
      "ds_read_b128 v[48:51], v0 offset:16896\n"
      "ds_read_b128 v[124:127], v0 offset:17408\n"
      "ds_read_b128 v[116:119], v0 offset:17920\n"
      "s_waitcnt lgkmcnt(7)\n"
      "v_mfma_f32_16x16x32_bf16 v[216:219], a[0:3], v[148:151], 0\n"
      "s_waitcnt lgkmcnt(6)\n"
      "v_mfma_f32_16x16x32_bf16 v[216:219], a[4:7], v[136:139], v[216:219]\n"
      "s_waitcnt lgkmcnt(5)\n"
      "v_mfma_f32_16x16x32_bf16 v[216:219], a[8:11], v[140:143], v[216:219]\n"
      "s_waitcnt lgkmcnt(4)\n"
      "v_mfma_f32_16x16x32_bf16 v[216:219], a[12:15], v[144:147], v[216:219]\n"
      "v_mfma_f32_16x16x32_bf16 v[96:99], a[16:19], v[148:151], 0\n"
      "v_mfma_f32_16x16x32_bf16 v[96:99], a[20:23], v[136:139], v[96:99]\n"
      "v_mfma_f32_16x16x32_bf16 v[96:99], a[24:27], v[140:143], v[96:99]\n"
      "v_mfma_f32_16x16x32_bf16 v[96:99], a[28:31], v[144:147], v[96:99]\n"
      "v_mfma_f32_16x16x32_bf16 v[24:27], a[32:35], v[148:151], 0\n"
      "v_mfma_f32_16x16x32_bf16 v[24:27], a[36:39], v[136:139], v[24:27]\n"
      "v_mfma_f32_16x16x32_bf16 v[24:27], a[40:43], v[140:143], v[24:27]\n"
      "v_mfma_f32_16x16x32_bf16 v[24:27], a[44:47], v[144:147], v[24:27]\n"
      "v_cvt_pk_bf16_f32 v92, v216, v217\n"
      "v_cvt_pk_bf16_f32 v93, v218, v219\n"
      "v_mfma_f32_16x16x32_bf16 v[216:219], a[48:51], v[148:151], 0\n"
      "v_mfma_f32_16x16x32_bf16 v[216:219], a[52:55], v[136:139], v[216:219]\n"
      "v_cvt_pk_bf16_f32 v94, v96, v97\n"
      "v_cvt_pk_bf16_f32 v95, v98, v99\n"
      "v_mfma_f32_16x16x32_bf16 v[216:219], a[56:59], v[140:143], v[216:219]\n"
      "v_mfma_f32_16x16x32_bf16 v[216:219], a[60:63], v[144:147], v[216:219]\n"
      "v_cvt_pk_bf16_f32 v96, v24, v25\n"
      "v_cvt_pk_bf16_f32 v97, v26, v27\n"
      "v_mfma_f32_16x16x32_bf16 v[248:251], v[148:151], a[64:67], 0\n"
      "v_mfma_f32_16x16x32_bf16 v[248:251], v[136:139], a[68:71], v[248:251]\n"
      "v_mfma_f32_16x16x32_bf16 v[248:251], v[140:143], a[72:75], v[248:251]\n"
      "v_mfma_f32_16x16x32_bf16 v[248:251], v[144:147], a[76:79], v[248:251]\n"
      "v_mfma_f32_16x16x32_bf16 v[24:27], v[148:151], a[80:83], 0\n"
      "v_mfma_f32_16x16x32_bf16 v[24:27], v[136:139], a[84:87], v[24:27]\n"
      "v_cvt_pk_bf16_f32 v98, v216, v217\n"
      "v_cvt_pk_bf16_f32 v99, v218, v219\n"
      "v_mfma_f32_16x16x32_bf16 v[24:27], v[140:143], a[88:91], v[24:27]\n"
      "v_mfma_f32_16x16x32_bf16 v[24:27], v[144:147], a[92:95], v[24:27]\n"
      "v_mfma_f32_16x16x32_bf16 v[216:219], v[148:151], a[96:99], 0\n"
      "v_mfma_f32_16x16x32_bf16 v[216:219], v[136:139], a[100:103], v[216:219]\n"
      "v_cvt_pk_bf16_f32 v58, v248, v249\n"
      "v_cvt_pk_bf16_f32 v59, v250, v251\n"
      "v_mfma_f32_16x16x32_bf16 v[216:219], v[140:143], a[104:107], v[216:219]\n"
      "v_mfma_f32_16x16x32_bf16 v[216:219], v[144:147], a[108:111], v[216:219]\n"
      "v_mfma_f32_16x16x32_bf16 v[248:251], v[148:151], a[112:115], 0\n"
      "v_mfma_f32_16x16x32_bf16 v[248:251], v[136:139], a[116:119], v[248:251]\n"
      "v_cvt_pk_bf16_f32 v134, v24, v25\n"
      "v_cvt_pk_bf16_f32 v135, v26, v27\n"
      "v_mfma_f32_16x16x32_bf16 v[248:251], v[140:143], a[120:123], v[248:251]\n"
      "v_mfma_f32_16x16x32_bf16 v[248:251], v[144:147], a[124:127], v[248:251]\n"
      "s_waitcnt vmcnt(37)\n"
      "ds_read_b128 v[24:27], v0 offset:20480\n"
      "ds_read_b128 v[152:155], v0 offset:20992\n"
      "ds_read_b128 v[20:23], v0 offset:21504\n"
      "ds_read_b128 v[156:159], v0 offset:22016\n"
      "v_cvt_pk_bf16_f32 v106, v216, v217\n"
      "v_cvt_pk_bf16_f32 v107, v218, v219\n"
      "s_waitcnt lgkmcnt(7)\n"
      "v_mfma_f32_16x16x32_bf16 v[220:223], a[0:3], v[252:255], 0\n"
      "s_waitcnt lgkmcnt(6)\n"
      "v_mfma_f32_16x16x32_bf16 v[220:223], a[4:7], v[48:51], v[220:223]\n"
      "v_cvt_pk_bf16_f32 v198, v248, v249\n"
      "v_cvt_pk_bf16_f32 v199, v250, v251\n"
      "s_waitcnt lgkmcnt(5)\n"
      "v_mfma_f32_16x16x32_bf16 v[220:223], a[8:11], v[124:127], v[220:223]\n"
      "s_waitcnt lgkmcnt(4)\n"
      "v_mfma_f32_16x16x32_bf16 v[220:223], a[12:15], v[116:119], v[220:223]\n"
      "v_mfma_f32_16x16x32_bf16 v[52:55], a[16:19], v[252:255], 0\n"
      "v_mfma_f32_16x16x32_bf16 v[52:55], a[20:23], v[48:51], v[52:55]\n"
      "v_mfma_f32_16x16x32_bf16 v[52:55], a[24:27], v[124:127], v[52:55]\n"
      "v_mfma_f32_16x16x32_bf16 v[52:55], a[28:31], v[116:119], v[52:55]\n"
      "v_mfma_f32_16x16x32_bf16 v[244:247], a[32:35], v[252:255], 0\n"
      "v_mfma_f32_16x16x32_bf16 v[244:247], a[36:39], v[48:51], v[244:247]\n"
      "v_mfma_f32_16x16x32_bf16 v[244:247], a[40:43], v[124:127], v[244:247]\n"
      "v_mfma_f32_16x16x32_bf16 v[244:247], a[44:47], v[116:119], v[244:247]\n"
      "v_cvt_pk_bf16_f32 v216, v220, v221\n"
      "v_cvt_pk_bf16_f32 v217, v222, v223\n"
      "v_mfma_f32_16x16x32_bf16 v[248:251], a[48:51], v[252:255], 0\n"
      "v_mfma_f32_16x16x32_bf16 v[248:251], a[52:55], v[48:51], v[248:251]\n"
      "v_cvt_pk_bf16_f32 v218, v52, v53\n"
      "v_cvt_pk_bf16_f32 v219, v54, v55\n"
      "v_mfma_f32_16x16x32_bf16 v[248:251], a[56:59], v[124:127], v[248:251]\n"
      "v_mfma_f32_16x16x32_bf16 v[248:251], a[60:63], v[116:119], v[248:251]\n"
      "v_cvt_pk_bf16_f32 v220, v244, v245\n"
      "v_cvt_pk_bf16_f32 v221, v246, v247\n"
      "v_mfma_f32_16x16x32_bf16 v[244:247], v[252:255], a[64:67], 0\n"
      "v_mfma_f32_16x16x32_bf16 v[244:247], v[48:51], a[68:71], v[244:247]\n"
      "v_mfma_f32_16x16x32_bf16 v[244:247], v[124:127], a[72:75], v[244:247]\n"
      "v_mfma_f32_16x16x32_bf16 v[244:247], v[116:119], a[76:79], v[244:247]\n"
      "v_mfma_f32_16x16x32_bf16 v[52:55], v[252:255], a[80:83], 0\n"
      "v_mfma_f32_16x16x32_bf16 v[52:55], v[48:51], a[84:87], v[52:55]\n"
      "v_cvt_pk_bf16_f32 v222, v248, v249\n"
      "v_cvt_pk_bf16_f32 v223, v250, v251\n"
      "v_mfma_f32_16x16x32_bf16 v[52:55], v[124:127], a[88:91], v[52:55]\n"
      "v_mfma_f32_16x16x32_bf16 v[52:55], v[116:119], a[92:95], v[52:55]\n"
      "v_mfma_f32_16x16x32_bf16 v[248:251], v[252:255], a[96:99], 0\n"
      "v_mfma_f32_16x16x32_bf16 v[248:251], v[48:51], a[100:103], v[248:251]\n"
      "v_cvt_pk_bf16_f32 v120, v244, v245\n"
      "v_cvt_pk_bf16_f32 v121, v246, v247\n"
      "v_mfma_f32_16x16x32_bf16 v[248:251], v[124:127], a[104:107], v[248:251]\n"
      "v_mfma_f32_16x16x32_bf16 v[248:251], v[116:119], a[108:111], v[248:251]\n"
      "v_mfma_f32_16x16x32_bf16 v[244:247], v[252:255], a[112:115], 0\n"
      "v_mfma_f32_16x16x32_bf16 v[244:247], v[48:51], a[116:119], v[244:247]\n"
      "v_cvt_pk_bf16_f32 v68, v52, v53\n"
      "v_cvt_pk_bf16_f32 v69, v54, v55\n"
      "v_mfma_f32_16x16x32_bf16 v[244:247], v[124:127], a[120:123], v[244:247]\n"
      "v_mfma_f32_16x16x32_bf16 v[244:247], v[116:119], a[124:127], v[244:247]\n"
      "s_waitcnt vmcnt(25)\n"
      "ds_read_b128 v[52:55], v0 offset:8192\n"
      "ds_read_b128 v[128:131], v0 offset:8704\n"
      "ds_read_b128 v[168:171], v0 offset:9216\n"
      "ds_read_b128 v[164:167], v0 offset:9728\n"
      "v_cvt_pk_bf16_f32 v36, v248, v249\n"
      "v_cvt_pk_bf16_f32 v37, v250, v251\n"
      "s_waitcnt lgkmcnt(7)\n"
      "v_mfma_f32_16x16x32_bf16 v[248:251], a[0:3], v[24:27], 0\n"
      "s_add_u32 m0, s53, 12288\n"
      "s_add_u32 s54, s49, 12288\n"
      "buffer_load_dwordx4 v1, s[40:43], s54 offen offset:0 lds\n"
      "v_cvt_pk_bf16_f32 v240, v244, v245\n"
      "v_cvt_pk_bf16_f32 v241, v246, v247\n"
      "s_waitcnt lgkmcnt(6)\n"
      "v_mfma_f32_16x16x32_bf16 v[248:251], a[4:7], v[152:155], v[248:251]\n"
      "s_waitcnt lgkmcnt(5)\n"
      "v_mfma_f32_16x16x32_bf16 v[248:251], a[8:11], v[20:23], v[248:251]\n"
      "buffer_load_dwordx4 v1, s[40:43], s54 offen offset:2048 lds\n"
      "s_add_u32 m0, s53, 13184\n"
      "s_waitcnt lgkmcnt(4)\n"
      "v_mfma_f32_16x16x32_bf16 v[248:251], a[12:15], v[156:159], v[248:251]\n"
      "v_mfma_f32_16x16x32_bf16 v[40:43], a[16:19], v[24:27], 0\n"
      "buffer_load_dwordx4 v1, s[40:43], s54 offen offset:128 lds\n"
      "v_mfma_f32_16x16x32_bf16 v[40:43], a[20:23], v[152:155], v[40:43]\n"
      "v_mfma_f32_16x16x32_bf16 v[40:43], a[24:27], v[20:23], v[40:43]\n"
      "buffer_load_dwordx4 v1, s[40:43], s54 offen offset:2176 lds\n"
      "v_mfma_f32_16x16x32_bf16 v[40:43], a[28:31], v[156:159], v[40:43]\n"
      "v_mfma_f32_16x16x32_bf16 v[100:103], a[32:35], v[24:27], 0\n"
      "v_mfma_f32_16x16x32_bf16 v[100:103], a[36:39], v[152:155], v[100:103]\n"
      "v_cvt_pk_bf16_f32 v244, v248, v249\n"
      "v_cvt_pk_bf16_f32 v245, v250, v251\n"
      "v_mfma_f32_16x16x32_bf16 v[100:103], a[40:43], v[20:23], v[100:103]\n"
      "v_mfma_f32_16x16x32_bf16 v[100:103], a[44:47], v[156:159], v[100:103]\n"
      "v_mfma_f32_16x16x32_bf16 v[44:47], a[48:51], v[24:27], 0\n"
      "v_mfma_f32_16x16x32_bf16 v[44:47], a[52:55], v[152:155], v[44:47]\n"
      "v_cvt_pk_bf16_f32 v246, v40, v41\n"
      "v_cvt_pk_bf16_f32 v247, v42, v43\n"
      "v_mfma_f32_16x16x32_bf16 v[44:47], a[56:59], v[20:23], v[44:47]\n"
      "v_mfma_f32_16x16x32_bf16 v[44:47], a[60:63], v[156:159], v[44:47]\n"
      "v_mfma_f32_16x16x32_bf16 v[40:43], v[24:27], a[64:67], 0\n"
      "v_mfma_f32_16x16x32_bf16 v[40:43], v[152:155], a[68:71], v[40:43]\n"
      "v_cvt_pk_bf16_f32 v248, v100, v101\n"
      "v_cvt_pk_bf16_f32 v249, v102, v103\n"
      "v_mfma_f32_16x16x32_bf16 v[40:43], v[20:23], a[72:75], v[40:43]\n"
      "v_mfma_f32_16x16x32_bf16 v[40:43], v[156:159], a[76:79], v[40:43]\n"
      "v_mfma_f32_16x16x32_bf16 v[100:103], v[24:27], a[80:83], 0\n"
      "v_mfma_f32_16x16x32_bf16 v[100:103], v[152:155], a[84:87], v[100:103]\n"
      "v_cvt_pk_bf16_f32 v250, v44, v45\n"
      "v_cvt_pk_bf16_f32 v251, v46, v47\n"
      "v_mfma_f32_16x16x32_bf16 v[100:103], v[20:23], a[88:91], v[100:103]\n"
      "v_mfma_f32_16x16x32_bf16 v[100:103], v[156:159], a[92:95], v[100:103]\n"
      "v_mfma_f32_16x16x32_bf16 v[44:47], v[24:27], a[96:99], 0\n"
      "v_mfma_f32_16x16x32_bf16 v[44:47], v[152:155], a[100:103], v[44:47]\n"
      "v_cvt_pk_bf16_f32 v122, v40, v41\n"
      "v_cvt_pk_bf16_f32 v123, v42, v43\n"
      "v_mfma_f32_16x16x32_bf16 v[44:47], v[20:23], a[104:107], v[44:47]\n"
      "v_mfma_f32_16x16x32_bf16 v[44:47], v[156:159], a[108:111], v[44:47]\n"
      "v_mfma_f32_16x16x32_bf16 v[40:43], v[24:27], a[112:115], 0\n"
      "v_mfma_f32_16x16x32_bf16 v[40:43], v[152:155], a[116:119], v[40:43]\n"
      "v_cvt_pk_bf16_f32 v70, v100, v101\n"
      "v_cvt_pk_bf16_f32 v71, v102, v103\n"
      "v_mfma_f32_16x16x32_bf16 v[40:43], v[20:23], a[120:123], v[40:43]\n"
      "v_mfma_f32_16x16x32_bf16 v[40:43], v[156:159], a[124:127], v[40:43]\n"
      "s_waitcnt vmcnt(21)\n"
      "ds_read_b128 v[100:103], v0 offset:4096\n"
      "ds_read_b128 v[108:111], v0 offset:4608\n"
      "ds_read_b128 v[176:179], v0 offset:5120\n"
      "ds_read_b128 v[172:175], v0 offset:5632\n"
      "v_cvt_pk_bf16_f32 v38, v44, v45\n"
      "v_cvt_pk_bf16_f32 v39, v46, v47\n"
      "s_waitcnt lgkmcnt(7)\n"
      "v_mfma_f32_16x16x32_bf16 v[44:47], a[0:3], v[52:55], 0\n"
      "s_add_u32 m0, s53, 16384\n"
      "s_add_u32 s54, s49, 16384\n"
      "buffer_load_dwordx4 v1, s[40:43], s54 offen offset:0 lds\n"
      "v_cvt_pk_bf16_f32 v242, v40, v41\n"
      "v_cvt_pk_bf16_f32 v243, v42, v43\n"
      "s_waitcnt lgkmcnt(6)\n"
      "v_mfma_f32_16x16x32_bf16 v[44:47], a[4:7], v[128:131], v[44:47]\n"
      "s_waitcnt lgkmcnt(5)\n"
      "v_mfma_f32_16x16x32_bf16 v[44:47], a[8:11], v[168:171], v[44:47]\n"
      "buffer_load_dwordx4 v1, s[40:43], s54 offen offset:2048 lds\n"
      "s_add_u32 m0, s53, 17280\n"
      "s_waitcnt lgkmcnt(4)\n"
      "v_mfma_f32_16x16x32_bf16 v[44:47], a[12:15], v[164:167], v[44:47]\n"
      "v_mfma_f32_16x16x32_bf16 v[112:115], a[16:19], v[52:55], 0\n"
      "buffer_load_dwordx4 v1, s[40:43], s54 offen offset:128 lds\n"
      "v_mfma_f32_16x16x32_bf16 v[112:115], a[20:23], v[128:131], v[112:115]\n"
      "v_mfma_f32_16x16x32_bf16 v[112:115], a[24:27], v[168:171], v[112:115]\n"
      "buffer_load_dwordx4 v1, s[40:43], s54 offen offset:2176 lds\n"
      "v_mfma_f32_16x16x32_bf16 v[112:115], a[28:31], v[164:167], v[112:115]\n"
      "v_mfma_f32_16x16x32_bf16 v[192:195], a[32:35], v[52:55], 0\n"
      "v_mfma_f32_16x16x32_bf16 v[192:195], a[36:39], v[128:131], v[192:195]\n"
      "v_cvt_pk_bf16_f32 v40, v44, v45\n"
      "v_cvt_pk_bf16_f32 v41, v46, v47\n"
      "v_mfma_f32_16x16x32_bf16 v[192:195], a[40:43], v[168:171], v[192:195]\n"
      "v_mfma_f32_16x16x32_bf16 v[192:195], a[44:47], v[164:167], v[192:195]\n"
      "v_mfma_f32_16x16x32_bf16 v[224:227], a[48:51], v[52:55], 0\n"
      "v_mfma_f32_16x16x32_bf16 v[224:227], a[52:55], v[128:131], v[224:227]\n"
      "v_cvt_pk_bf16_f32 v42, v112, v113\n"
      "v_cvt_pk_bf16_f32 v43, v114, v115\n"
      "v_mfma_f32_16x16x32_bf16 v[224:227], a[56:59], v[168:171], v[224:227]\n"
      "v_mfma_f32_16x16x32_bf16 v[224:227], a[60:63], v[164:167], v[224:227]\n"
      "v_mfma_f32_16x16x32_bf16 v[32:35], v[52:55], a[64:67], 0\n"
      "v_mfma_f32_16x16x32_bf16 v[32:35], v[128:131], a[68:71], v[32:35]\n"
      "v_cvt_pk_bf16_f32 v44, v192, v193\n"
      "v_cvt_pk_bf16_f32 v45, v194, v195\n"
      "v_mfma_f32_16x16x32_bf16 v[32:35], v[168:171], a[72:75], v[32:35]\n"
      "v_mfma_f32_16x16x32_bf16 v[32:35], v[164:167], a[76:79], v[32:35]\n"
      "v_mfma_f32_16x16x32_bf16 v[160:163], v[52:55], a[80:83], 0\n"
      "v_mfma_f32_16x16x32_bf16 v[160:163], v[128:131], a[84:87], v[160:163]\n"
      "v_cvt_pk_bf16_f32 v46, v224, v225\n"
      "v_cvt_pk_bf16_f32 v47, v226, v227\n"
      "v_mfma_f32_16x16x32_bf16 v[160:163], v[168:171], a[88:91], v[160:163]\n"
      "v_mfma_f32_16x16x32_bf16 v[160:163], v[164:167], a[92:95], v[160:163]\n"
      "v_mfma_f32_16x16x32_bf16 v[112:115], v[52:55], a[96:99], 0\n"
      "v_mfma_f32_16x16x32_bf16 v[112:115], v[128:131], a[100:103], v[112:115]\n"
      "v_cvt_pk_bf16_f32 v56, v32, v33\n"
      "v_cvt_pk_bf16_f32 v57, v34, v35\n"
      "v_mfma_f32_16x16x32_bf16 v[112:115], v[168:171], a[104:107], v[112:115]\n"
      "v_mfma_f32_16x16x32_bf16 v[112:115], v[164:167], a[108:111], v[112:115]\n"
      "v_mfma_f32_16x16x32_bf16 v[192:195], v[52:55], a[112:115], 0\n"
      "v_mfma_f32_16x16x32_bf16 v[192:195], v[128:131], a[116:119], v[192:195]\n"
      "v_cvt_pk_bf16_f32 v132, v160, v161\n"
      "v_cvt_pk_bf16_f32 v133, v162, v163\n"
      "v_mfma_f32_16x16x32_bf16 v[192:195], v[168:171], a[120:123], v[192:195]\n"
      "v_mfma_f32_16x16x32_bf16 v[192:195], v[164:167], a[124:127], v[192:195]\n"
      "s_waitcnt vmcnt(17)\n"
      "ds_read_b128 v[224:227], v0 offset:0\n"
      "ds_read_b128 v[32:35], v0 offset:512\n"
      "ds_read_b128 v[160:163], v0 offset:1024\n"
      "ds_read_b128 v[28:31], v0 offset:1536\n"
      "v_cvt_pk_bf16_f32 v104, v112, v113\n"
      "v_cvt_pk_bf16_f32 v105, v114, v115\n"
      "s_waitcnt lgkmcnt(7)\n"
      "v_mfma_f32_16x16x32_bf16 v[168:171], a[0:3], v[100:103], 0\n"
      "s_add_u32 m0, s53, 20480\n"
      "s_add_u32 s54, s49, 20480\n"
      "buffer_load_dwordx4 v1, s[40:43], s54 offen offset:0 lds\n"
      "v_cvt_pk_bf16_f32 v196, v192, v193\n"
      "v_cvt_pk_bf16_f32 v197, v194, v195\n"
      "s_waitcnt lgkmcnt(6)\n"
      "v_mfma_f32_16x16x32_bf16 v[168:171], a[4:7], v[108:111], v[168:171]\n"
      "s_waitcnt lgkmcnt(5)\n"
      "v_mfma_f32_16x16x32_bf16 v[168:171], a[8:11], v[176:179], v[168:171]\n"
      "buffer_load_dwordx4 v1, s[40:43], s54 offen offset:2048 lds\n"
      "s_add_u32 m0, s53, 21376\n"
      "s_waitcnt lgkmcnt(4)\n"
      "v_mfma_f32_16x16x32_bf16 v[168:171], a[12:15], v[172:175], v[168:171]\n"
      "v_mfma_f32_16x16x32_bf16 v[112:115], a[16:19], v[100:103], 0\n"
      "buffer_load_dwordx4 v1, s[40:43], s54 offen offset:128 lds\n"
      "v_mfma_f32_16x16x32_bf16 v[112:115], a[20:23], v[108:111], v[112:115]\n"
      "v_mfma_f32_16x16x32_bf16 v[112:115], a[24:27], v[176:179], v[112:115]\n"
      "buffer_load_dwordx4 v1, s[40:43], s54 offen offset:2176 lds\n"
      "v_mfma_f32_16x16x32_bf16 v[112:115], a[28:31], v[172:175], v[112:115]\n"
      "v_mfma_f32_16x16x32_bf16 v[192:195], a[32:35], v[100:103], 0\n"
      "v_mfma_f32_16x16x32_bf16 v[192:195], a[36:39], v[108:111], v[192:195]\n"
      "v_cvt_pk_bf16_f32 v164, v168, v169\n"
      "v_cvt_pk_bf16_f32 v165, v170, v171\n"
      "v_mfma_f32_16x16x32_bf16 v[192:195], a[40:43], v[176:179], v[192:195]\n"
      "v_mfma_f32_16x16x32_bf16 v[192:195], a[44:47], v[172:175], v[192:195]\n"
      "v_mfma_f32_16x16x32_bf16 v[52:55], a[48:51], v[100:103], 0\n"
      "v_mfma_f32_16x16x32_bf16 v[52:55], a[52:55], v[108:111], v[52:55]\n"
      "v_cvt_pk_bf16_f32 v166, v112, v113\n"
      "v_cvt_pk_bf16_f32 v167, v114, v115\n"
      "v_mfma_f32_16x16x32_bf16 v[52:55], a[56:59], v[176:179], v[52:55]\n"
      "v_mfma_f32_16x16x32_bf16 v[52:55], a[60:63], v[172:175], v[52:55]\n"
      "v_mfma_f32_16x16x32_bf16 v[112:115], v[100:103], a[64:67], 0\n"
      "v_mfma_f32_16x16x32_bf16 v[112:115], v[108:111], a[68:71], v[112:115]\n"
      "v_cvt_pk_bf16_f32 v168, v192, v193\n"
      "v_cvt_pk_bf16_f32 v169, v194, v195\n"
      "v_mfma_f32_16x16x32_bf16 v[112:115], v[176:179], a[72:75], v[112:115]\n"
      "v_mfma_f32_16x16x32_bf16 v[112:115], v[172:175], a[76:79], v[112:115]\n"
      "v_mfma_f32_16x16x32_bf16 v[192:195], v[100:103], a[80:83], 0\n"
      "v_mfma_f32_16x16x32_bf16 v[192:195], v[108:111], a[84:87], v[192:195]\n"
      "v_cvt_pk_bf16_f32 v170, v52, v53\n"
      "v_cvt_pk_bf16_f32 v171, v54, v55\n"
      "v_mfma_f32_16x16x32_bf16 v[192:195], v[176:179], a[88:91], v[192:195]\n"
      "v_mfma_f32_16x16x32_bf16 v[192:195], v[172:175], a[92:95], v[192:195]\n"
      "v_mfma_f32_16x16x32_bf16 v[88:91], v[100:103], a[96:99], 0\n"
      "v_mfma_f32_16x16x32_bf16 v[88:91], v[108:111], a[100:103], v[88:91]\n"
      "v_cvt_pk_bf16_f32 v54, v112, v113\n"
      "v_cvt_pk_bf16_f32 v55, v114, v115\n"
      "v_mfma_f32_16x16x32_bf16 v[88:91], v[176:179], a[104:107], v[88:91]\n"
      "v_mfma_f32_16x16x32_bf16 v[88:91], v[172:175], a[108:111], v[88:91]\n"
      "v_mfma_f32_16x16x32_bf16 v[112:115], v[100:103], a[112:115], 0\n"
      "v_mfma_f32_16x16x32_bf16 v[112:115], v[108:111], a[116:119], v[112:115]\n"
      "v_cvt_pk_bf16_f32 v130, v192, v193\n"
      "v_cvt_pk_bf16_f32 v131, v194, v195\n"
      "v_mfma_f32_16x16x32_bf16 v[112:115], v[176:179], a[120:123], v[112:115]\n"
      "v_mfma_f32_16x16x32_bf16 v[112:115], v[172:175], a[124:127], v[112:115]\n"
      "ds_read_b128 v[172:175], v9 offset:0\n"
      "ds_read_b128 v[176:179], v9 offset:2048\n"
      "v_cvt_pk_bf16_f32 v102, v88, v89\n"
      "v_cvt_pk_bf16_f32 v103, v90, v91\n"
      "s_waitcnt lgkmcnt(5)\n"
      "v_mfma_f32_16x16x32_bf16 v[88:91], a[0:3], v[224:227], 0\n"
      "s_waitcnt lgkmcnt(4)\n"
      "v_mfma_f32_16x16x32_bf16 v[88:91], a[4:7], v[32:35], v[88:91]\n"
      "s_waitcnt lgkmcnt(3)\n"
      "v_mfma_f32_16x16x32_bf16 v[88:91], a[8:11], v[160:163], v[88:91]\n"
      "s_waitcnt lgkmcnt(2)\n"
      "v_mfma_f32_16x16x32_bf16 v[88:91], a[12:15], v[28:31], v[88:91]\n"
      "v_cvt_pk_bf16_f32 v194, v112, v113\n"
      "v_cvt_pk_bf16_f32 v195, v114, v115\n"
      "v_mfma_f32_16x16x32_bf16 v[112:115], a[16:19], v[224:227], 0\n"
      "v_mfma_f32_16x16x32_bf16 v[112:115], a[20:23], v[32:35], v[112:115]\n"
      "v_mfma_f32_16x16x32_bf16 v[112:115], a[24:27], v[160:163], v[112:115]\n"
      "v_mfma_f32_16x16x32_bf16 v[112:115], a[28:31], v[28:31], v[112:115]\n"
      "v_mfma_f32_16x16x32_bf16 v[190:193], a[32:35], v[224:227], 0\n"
      "v_mfma_f32_16x16x32_bf16 v[190:193], a[36:39], v[32:35], v[190:193]\n"
      "v_cvt_pk_bf16_f32 v108, v88, v89\n"
      "v_cvt_pk_bf16_f32 v109, v90, v91\n"
      "v_mfma_f32_16x16x32_bf16 v[190:193], a[40:43], v[160:163], v[190:193]\n"
      "v_mfma_f32_16x16x32_bf16 v[190:193], a[44:47], v[28:31], v[190:193]\n"
      "v_mfma_f32_16x16x32_bf16 v[88:91], a[48:51], v[224:227], 0\n"
      "v_mfma_f32_16x16x32_bf16 v[88:91], a[52:55], v[32:35], v[88:91]\n"
      "v_cvt_pk_bf16_f32 v110, v112, v113\n"
      "v_cvt_pk_bf16_f32 v111, v114, v115\n"
      "v_mfma_f32_16x16x32_bf16 v[88:91], a[56:59], v[160:163], v[88:91]\n"
      "v_mfma_f32_16x16x32_bf16 v[88:91], a[60:63], v[28:31], v[88:91]\n"
      "v_add_u32 v9, 64, v9\n"
      "v_mfma_f32_16x16x32_bf16 v[228:231], v[224:227], a[64:67], 0\n"
      "v_cvt_pk_bf16_f32 v112, v190, v191\n"
      "v_cvt_pk_bf16_f32 v113, v192, v193\n"
      "v_mfma_f32_16x16x32_bf16 v[228:231], v[32:35], a[68:71], v[228:231]\n"
      "v_mfma_f32_16x16x32_bf16 v[228:231], v[160:163], a[72:75], v[228:231]\n"
      "v_mfma_f32_16x16x32_bf16 v[228:231], v[28:31], a[76:79], v[228:231]\n"
      "v_mfma_f32_16x16x32_bf16 v[190:193], v[224:227], a[80:83], 0\n"
      "v_cvt_pk_bf16_f32 v114, v88, v89\n"
      "v_cvt_pk_bf16_f32 v115, v90, v91\n"
      "v_mfma_f32_16x16x32_bf16 v[190:193], v[32:35], a[84:87], v[190:193]\n"
      "v_mfma_f32_16x16x32_bf16 v[190:193], v[160:163], a[88:91], v[190:193]\n"
      "v_mfma_f32_16x16x32_bf16 v[190:193], v[28:31], a[92:95], v[190:193]\n"
      "v_mfma_f32_16x16x32_bf16 v[88:91], v[224:227], a[96:99], 0\n"
      "v_mfma_f32_16x16x32_bf16 v[88:91], v[32:35], a[100:103], v[88:91]\n"
      "v_cvt_pk_bf16_f32 v52, v228, v229\n"
      "v_cvt_pk_bf16_f32 v53, v230, v231\n"
      "v_mfma_f32_16x16x32_bf16 v[88:91], v[160:163], a[104:107], v[88:91]\n"
      "v_mfma_f32_16x16x32_bf16 v[88:91], v[28:31], a[108:111], v[88:91]\n"
      "v_mfma_f32_16x16x32_bf16 v[228:231], v[224:227], a[112:115], 0\n"
      "v_mfma_f32_16x16x32_bf16 v[228:231], v[32:35], a[116:119], v[228:231]\n"
      "v_cvt_pk_bf16_f32 v128, v190, v191\n"
      "v_cvt_pk_bf16_f32 v129, v192, v193\n"
      "v_mfma_f32_16x16x32_bf16 v[228:231], v[160:163], a[120:123], v[228:231]\n"
      "v_mfma_f32_16x16x32_bf16 v[228:231], v[28:31], a[124:127], v[228:231]\n"
      "ds_read_b64 v[18:19], v11 offset:0\n"
      "v_mfma_f32_16x16x32_bf16 v[28:31], a[128:131], v[24:27], 0\n"
      "v_cvt_pk_bf16_f32 v100, v88, v89\n"
      "v_cvt_pk_bf16_f32 v101, v90, v91\n"
      "v_mfma_f32_16x16x32_bf16 v[28:31], a[132:135], v[152:155], v[28:31]\n"
      "v_mfma_f32_16x16x32_bf16 v[28:31], a[136:139], v[20:23], v[28:31]\n"
      "v_mfma_f32_16x16x32_bf16 v[28:31], a[140:143], v[156:159], v[28:31]\n"
      "v_mfma_f32_16x16x32_bf16 v[88:91], a[144:147], v[24:27], 0\n"
      "v_cvt_pk_bf16_f32 v192, v228, v229\n"
      "v_cvt_pk_bf16_f32 v193, v230, v231\n"
      "v_mfma_f32_16x16x32_bf16 v[88:91], a[148:151], v[152:155], v[88:91]\n"
      "v_mfma_f32_16x16x32_bf16 v[88:91], a[152:155], v[20:23], v[88:91]\n"
      "v_mfma_f32_16x16x32_bf16 v[88:91], a[156:159], v[156:159], v[88:91]\n"
      "v_mfma_f32_16x16x32_bf16 v[228:231], a[160:163], v[24:27], 0\n"
      "v_mfma_f32_16x16x32_bf16 v[228:231], a[164:167], v[152:155], v[228:231]\n"
      "v_cvt_pk_bf16_f32 v160, v28, v29\n"
      "v_cvt_pk_bf16_f32 v161, v30, v31\n"
      "v_mfma_f32_16x16x32_bf16 v[228:231], a[168:171], v[20:23], v[228:231]\n"
      "v_mfma_f32_16x16x32_bf16 v[228:231], a[172:175], v[156:159], v[228:231]\n"
      "v_mfma_f32_16x16x32_bf16 v[32:35], a[176:179], v[24:27], 0\n"
      "v_mfma_f32_16x16x32_bf16 v[32:35], a[180:183], v[152:155], v[32:35]\n"
      "v_cvt_pk_bf16_f32 v162, v88, v89\n"
      "v_cvt_pk_bf16_f32 v163, v90, v91\n"
      "v_mfma_f32_16x16x32_bf16 v[32:35], a[184:187], v[20:23], v[32:35]\n"
      "v_mfma_f32_16x16x32_bf16 v[32:35], a[188:191], v[156:159], v[32:35]\n"
      "s_waitcnt lgkmcnt(2)\n"
      "v_mfma_f32_16x16x32_bf16 v[28:31], v[172:175], v[160:163], 0\n"
      "v_cvt_pk_bf16_f32 v88, v228, v229\n"
      "v_cvt_pk_bf16_f32 v89, v230, v231\n"
      "s_nop 3\n"
      "v_cvt_pk_bf16_f32 v90, v32, v33\n"
      "v_cvt_pk_bf16_f32 v91, v34, v35\n"
      "s_waitcnt lgkmcnt(1)\n"
      "s_nop 0\n"
      "v_mfma_f32_16x16x32_bf16 v[28:31], v[176:179], v[88:91], v[28:31]\n"
      "v_mfma_f32_16x16x32_bf16 v[228:231], v[108:111], v[160:163], 0\n"
      "v_mfma_f32_16x16x32_bf16 v[228:231], v[112:115], v[88:91], v[228:231]\n"
      "v_mfma_f32_16x16x32_bf16 v[32:35], v[164:167], v[160:163], 0\n"
      "v_mfma_f32_16x16x32_bf16 v[32:35], v[168:171], v[88:91], v[32:35]\n"
      "v_mfma_f32_16x16x32_bf16 v[224:227], v[40:43], v[160:163], 0\n"
      "v_mfma_f32_16x16x32_bf16 v[224:227], v[44:47], v[88:91], v[224:227]\n"
      "v_mfma_f32_16x16x32_bf16 v[200:203], v[92:95], v[160:163], 0\n"
      "v_mfma_f32_16x16x32_bf16 v[200:203], v[96:99], v[88:91], v[200:203]\n"
      "v_max_f32 v190, v28, v29\n"
      "v_max3_f32 v190, v190, v30, v31\n"
      "v_max3_f32 v190, v190, v228, v229\n"
      "v_max3_f32 v190, v190, v230, v231\n"
      "v_max3_f32 v190, v190, v32, v33\n"
      "v_mfma_f32_16x16x32_bf16 v[180:183], v[216:219], v[160:163], 0\n"
      "v_max3_f32 v190, v190, v34, v35\n"
      "v_max3_f32 v190, v190, v224, v225\n"
      "v_max3_f32 v190, v190, v226, v227\n"
      "v_max3_f32 v190, v190, v200, v201\n"
      "v_max3_f32 v190, v190, v202, v203\n"
      "v_mfma_f32_16x16x32_bf16 v[180:183], v[220:223], v[88:91], v[180:183]\n"
      "v_mfma_f32_16x16x32_bf16 v[208:211], v[244:247], v[160:163], 0\n"
      "v_mfma_f32_16x16x32_bf16 v[208:211], v[248:251], v[88:91], v[208:211]\n"
      "v_mfma_f32_16x16x32_bf16 v[184:187], a[128:131], v[252:255], 0\n"
      "v_mfma_f32_16x16x32_bf16 v[184:187], a[132:135], v[48:51], v[184:187]\n"
      "v_mfma_f32_16x16x32_bf16 v[184:187], a[136:139], v[124:127], v[184:187]\n"
      "v_mfma_f32_16x16x32_bf16 v[184:187], a[140:143], v[116:119], v[184:187]\n"
      "v_mfma_f32_16x16x32_bf16 v[88:91], a[144:147], v[252:255], 0\n"
      "v_mfma_f32_16x16x32_bf16 v[88:91], a[148:151], v[48:51], v[88:91]\n"
      "v_max3_f32 v190, v190, v180, v181\n"
      "v_max3_f32 v190, v190, v182, v183\n"
      "v_max3_f32 v190, v190, v208, v209\n"
      "v_max3_f32 v190, v190, v210, v211\n"
      "v_mov_b32 v162, v190\n"
      "v_mfma_f32_16x16x32_bf16 v[88:91], a[152:155], v[124:127], v[88:91]\n"
      "v_cvt_pk_bf16_f32 v160, v184, v185\n"
      "v_permlane16_swap_b32 v190, v162\n"
      "v_max_f32 v190, v190, v162\n"
      "v_mov_b32 v162, v190\n"
      "v_cvt_pk_bf16_f32 v161, v186, v187\n"
      "v_mfma_f32_16x16x32_bf16 v[88:91], a[156:159], v[116:119], v[88:91]\n"
      "v_permlane32_swap_b32 v190, v162\n"
      "v_max_f32 v190, v190, v162\n"
      "v_sub_f32 v28, v28, v190\n"
      "v_sub_f32 v29, v29, v190\n"
      "v_sub_f32 v30, v30, v190\n"
      "v_sub_f32 v31, v31, v190\n"
      "v_exp_f32 v28, v28\n"
      "v_mfma_f32_16x16x32_bf16 v[184:187], a[160:163], v[252:255], 0\n"
      "v_exp_f32 v29, v29\n"
      "v_exp_f32 v30, v30\n"
      "v_exp_f32 v31, v31\n"
      "v_cvt_pk_bf16_f32 v162, v28, v29\n"
      "v_cvt_pk_bf16_f32 v163, v30, v31\n"
      "v_mfma_f32_16x16x32_bf16 v[184:187], a[164:167], v[48:51], v[184:187]\n"
      "v_sub_f32 v228, v228, v190\n"
      "v_sub_f32 v229, v229, v190\n"
      "v_sub_f32 v230, v230, v190\n"
      "v_sub_f32 v231, v231, v190\n"
      "v_sub_f32 v32, v32, v190\n"
      "v_sub_f32 v33, v33, v190\n"
      "v_sub_f32 v34, v34, v190\n"
      "v_sub_f32 v35, v35, v190\n"
      "v_mfma_f32_16x16x32_bf16 v[184:187], a[168:171], v[124:127], v[184:187]\n"
      "v_sub_f32 v224, v224, v190\n"
      "v_sub_f32 v225, v225, v190\n"
      "v_sub_f32 v226, v226, v190\n"
      "v_sub_f32 v227, v227, v190\n"
      "v_sub_f32 v200, v200, v190\n"
      "v_sub_f32 v201, v201, v190\n"
      "v_sub_f32 v202, v202, v190\n"
      "v_sub_f32 v203, v203, v190\n"
      "v_mfma_f32_16x16x32_bf16 v[184:187], a[172:175], v[116:119], v[184:187]\n"
      "v_add_f32 v188, v28, v29\n"
      "v_add_f32 v189, v30, v31\n"
      "v_exp_f32 v228, v228\n"
      "v_exp_f32 v229, v229\n"
      "v_exp_f32 v230, v230\n"
      "v_cvt_pk_bf16_f32 v28, v228, v229\n"
      "v_mfma_f32_16x16x32_bf16 v[84:87], a[176:179], v[252:255], 0\n"
      "v_exp_f32 v231, v231\n"
      "v_exp_f32 v32, v32\n"
      "v_cvt_pk_bf16_f32 v29, v230, v231\n"
      "v_exp_f32 v33, v33\n"
      "v_mfma_f32_16x16x32_bf16 v[84:87], a[180:183], v[48:51], v[84:87]\n"
      "v_exp_f32 v34, v34\n"
      "v_exp_f32 v35, v35\n"
      "v_cvt_pk_bf16_f32 v30, v32, v33\n"
      "v_cvt_pk_bf16_f32 v31, v34, v35\n"
      "v_exp_f32 v224, v224\n"
      "v_sub_f32 v180, v180, v190\n"
      "v_mfma_f32_16x16x32_bf16 v[84:87], a[184:187], v[124:127], v[84:87]\n"
      "v_exp_f32 v225, v225\n"
      "v_exp_f32 v226, v226\n"
      "v_exp_f32 v227, v227\n"
      "v_cvt_pk_bf16_f32 v32, v224, v225\n"
      "v_cvt_pk_bf16_f32 v33, v226, v227\n"
      "v_mfma_f32_16x16x32_bf16 v[84:87], a[188:191], v[116:119], v[84:87]\n"
      "v_exp_f32 v200, v200\n"
      "v_exp_f32 v201, v201\n"
      "v_exp_f32 v202, v202\n"
      "v_exp_f32 v203, v203\n"
      "s_waitcnt lgkmcnt(0)\n"
      "v_mfma_f32_16x16x4_4b_bf16 v[224:239], v[162:163], v[18:19], 0 cbsz:2 abid:0\n"
      "v_cvt_pk_bf16_f32 v34, v200, v201\n"
      "v_cvt_pk_bf16_f32 v35, v202, v203\n"
      "v_sub_f32 v181, v181, v190\n"
      "v_sub_f32 v182, v182, v190\n"
      "v_sub_f32 v183, v183, v190\n"
      "v_sub_f32 v208, v208, v190\n"
      "v_sub_f32 v209, v209, v190\n"
      "v_sub_f32 v210, v210, v190\n"
      "v_sub_f32 v211, v211, v190\n"
      "v_exp_f32 v180, v180\n"
      "v_exp_f32 v181, v181\n"
      "v_exp_f32 v182, v182\n"
      "v_exp_f32 v183, v183\n"
      "v_cvt_pk_bf16_f32 v200, v180, v181\n"
      "v_cvt_pk_bf16_f32 v201, v182, v183\n"
      "v_exp_f32 v208, v208\n"
      "v_exp_f32 v209, v209\n"
      "v_smfmac_f32_16x16x64_bf16 v[188:191], v[12:15], v[28:35], v5\n"
      "v_exp_f32 v210, v210\n"
      "v_exp_f32 v211, v211\n"
      "v_mfma_f32_16x16x32_bf16 v[224:227], v[28:31], v[52:55], v[224:227]\n"
      "v_cvt_pk_bf16_f32 v202, v208, v209\n"
      "v_cvt_pk_bf16_f32 v203, v210, v211\n"
      "v_cvt_pk_bf16_f32 v162, v88, v89\n"
      "v_cvt_pk_bf16_f32 v163, v90, v91\n"
      "v_mfma_f32_16x16x32_bf16 v[224:227], v[32:35], v[56:59], v[224:227]\n"
      "v_cvt_pk_bf16_f32 v88, v184, v185\n"
      "v_cvt_pk_bf16_f32 v89, v186, v187\n"
      "v_cvt_pk_bf16_f32 v90, v84, v85\n"
      "v_mfma_f32_16x16x32_bf16 v[224:227], v[200:203], v[120:123], v[224:227]\n"
      "v_cvt_pk_bf16_f32 v91, v86, v87\n"
      "v_smfmac_f32_16x16x32_bf16 v[188:191], v[12:13], v[200:203], v5\n"
      "v_mfma_f32_16x16x32_bf16 v[228:231], v[28:31], v[128:131], v[228:231]\n"
      "v_mfma_f32_16x16x32_bf16 v[228:231], v[32:35], v[132:135], v[228:231]\n"
      "v_mfma_f32_16x16x32_bf16 v[228:231], v[200:203], v[68:71], v[228:231]\n"
      "v_mfma_f32_16x16x32_bf16 v[232:235], v[28:31], v[100:103], v[232:235]\n"
      "v_mfma_f32_16x16x32_bf16 v[232:235], v[32:35], v[104:107], v[232:235]\n"
      "v_mfma_f32_16x16x32_bf16 v[232:235], v[200:203], v[36:39], v[232:235]\n"
      "v_mfma_f32_16x16x32_bf16 v[236:239], v[28:31], v[192:195], v[236:239]\n"
      "v_mfma_f32_16x16x32_bf16 v[236:239], v[32:35], v[196:199], v[236:239]\n"
      "v_add_f32 v188, v188, v189\n"
      "v_rcp_f32 v189, v188\n"
      "v_mfma_f32_16x16x32_bf16 v[236:239], v[200:203], v[240:243], v[236:239]\n"
      "ds_bpermute_b32 v190, v4, v189\n"
      "s_waitcnt lgkmcnt(0)\n"
      "v_mul_f32_dpp v224, v190, v224 row_newbcast:0 row_mask:0xf bank_mask:0xf\n"
      "v_mul_f32_dpp v225, v190, v225 row_newbcast:4 row_mask:0xf bank_mask:0xf\n"
      "v_mul_f32_dpp v226, v190, v226 row_newbcast:8 row_mask:0xf bank_mask:0xf\n"
      "v_mul_f32_dpp v227, v190, v227 row_newbcast:12 row_mask:0xf bank_mask:0xf\n"
      "v_mul_f32_dpp v228, v190, v228 row_newbcast:0 row_mask:0xf bank_mask:0xf\n"
      "v_mul_f32_dpp v229, v190, v229 row_newbcast:4 row_mask:0xf bank_mask:0xf\n"
      "v_mfma_f32_16x16x32_bf16 v[224:227], v[24:27], a[192:195], v[224:227]\n"
      "v_mul_f32_dpp v230, v190, v230 row_newbcast:8 row_mask:0xf bank_mask:0xf\n"
      "v_mul_f32_dpp v231, v190, v231 row_newbcast:12 row_mask:0xf bank_mask:0xf\n"
      "v_mfma_f32_16x16x32_bf16 v[224:227], v[152:155], a[196:199], v[224:227]\n"
      "v_mul_f32_dpp v232, v190, v232 row_newbcast:0 row_mask:0xf bank_mask:0xf\n"
      "v_mul_f32_dpp v233, v190, v233 row_newbcast:4 row_mask:0xf bank_mask:0xf\n"
      "v_mul_f32_dpp v234, v190, v234 row_newbcast:8 row_mask:0xf bank_mask:0xf\n"
      "v_mfma_f32_16x16x32_bf16 v[224:227], v[20:23], a[200:203], v[224:227]\n"
      "v_mul_f32_dpp v235, v190, v235 row_newbcast:12 row_mask:0xf bank_mask:0xf\n"
      "v_mul_f32_dpp v236, v190, v236 row_newbcast:0 row_mask:0xf bank_mask:0xf\n"
      "v_mul_f32_dpp v237, v190, v237 row_newbcast:4 row_mask:0xf bank_mask:0xf\n"
      "v_mfma_f32_16x16x32_bf16 v[224:227], v[156:159], a[204:207], v[224:227]\n"
      "v_mul_f32_dpp v238, v190, v238 row_newbcast:8 row_mask:0xf bank_mask:0xf\n"
      "v_mul_f32_dpp v239, v190, v239 row_newbcast:12 row_mask:0xf bank_mask:0xf\n"
      "v_mfma_f32_16x16x32_bf16 v[228:231], v[24:27], a[208:211], v[228:231]\n"
      "v_mfma_f32_16x16x32_bf16 v[228:231], v[152:155], a[212:215], v[228:231]\n"
      "v_mfma_f32_16x16x32_bf16 v[228:231], v[20:23], a[216:219], v[228:231]\n"
      "v_mfma_f32_16x16x32_bf16 v[228:231], v[156:159], a[220:223], v[228:231]\n"
      "v_mfma_f32_16x16x32_bf16 v[232:235], v[24:27], a[224:227], v[232:235]\n"
      "v_mfma_f32_16x16x32_bf16 v[232:235], v[152:155], a[228:231], v[232:235]\n"
      "v_mfma_f32_16x16x32_bf16 v[232:235], v[20:23], a[232:235], v[232:235]\n"
      "v_mfma_f32_16x16x32_bf16 v[232:235], v[156:159], a[236:239], v[232:235]\n"
      "v_mfma_f32_16x16x32_bf16 v[236:239], v[24:27], a[240:243], v[236:239]\n"
      "v_mfma_f32_16x16x32_bf16 v[236:239], v[152:155], a[244:247], v[236:239]\n"
      "v_mfma_f32_16x16x32_bf16 v[236:239], v[20:23], a[248:251], v[236:239]\n"
      "v_mfma_f32_16x16x32_bf16 v[236:239], v[156:159], a[252:255], v[236:239]\n"
      "v_cvt_pk_bf16_f32 v224, v224, v228\n"
      "v_cvt_pk_bf16_f32 v228, v225, v229\n"
      "v_pk_max_i16 v224, v224, 0\n"
      "v_pk_max_i16 v228, v228, 0\n"
      "v_mfma_f32_16x16x32_bf16 v[20:23], v[172:175], v[160:163], 0\n"
      "v_cvt_pk_bf16_f32 v226, v226, v230\n"
      "v_cvt_pk_bf16_f32 v230, v227, v231\n"
      "v_pk_max_i16 v226, v226, 0\n"
      "v_cvt_pk_bf16_f32 v225, v232, v236\n"
      "v_mfma_f32_16x16x32_bf16 v[20:23], v[176:179], v[88:91], v[20:23]\n"
      "v_cvt_pk_bf16_f32 v229, v233, v237\n"
      "v_pk_max_i16 v225, v225, 0\n"
      "v_pk_max_i16 v229, v229, 0\n"
      "v_cvt_pk_bf16_f32 v227, v234, v238\n"
      "v_mfma_f32_16x16x32_bf16 v[24:27], v[108:111], v[160:163], 0\n"
      "v_cvt_pk_bf16_f32 v231, v235, v239\n"
      "v_pk_max_i16 v227, v227, 0\n"
      "v_pk_max_i16 v230, v230, 0\n"
      "v_max_f32 v186, v20, v21\n"
      "v_mfma_f32_16x16x32_bf16 v[24:27], v[112:115], v[88:91], v[24:27]\n"
      "v_max3_f32 v186, v186, v22, v23\n"
      "v_pk_max_i16 v231, v231, 0\n"
      "v_mfma_f32_16x16x32_bf16 v[28:31], v[164:167], v[160:163], 0\n"
      "v_mfma_f32_16x16x32_bf16 v[28:31], v[168:171], v[88:91], v[28:31]\n"
      "v_mfma_f32_16x16x32_bf16 v[236:239], v[40:43], v[160:163], 0\n"
      "v_mfma_f32_16x16x32_bf16 v[236:239], v[44:47], v[88:91], v[236:239]\n"
      "v_mfma_f32_16x16x32_bf16 v[232:235], v[92:95], v[160:163], 0\n"
      "v_mfma_f32_16x16x32_bf16 v[232:235], v[96:99], v[88:91], v[232:235]\n"
      "v_max3_f32 v186, v186, v24, v25\n"
      "v_max3_f32 v186, v186, v26, v27\n"
      "v_mfma_f32_16x16x32_bf16 v[156:159], v[216:219], v[160:163], 0\n"
      "v_mfma_f32_16x16x32_bf16 v[156:159], v[220:223], v[88:91], v[156:159]\n"
      "v_max3_f32 v186, v186, v28, v29\n"
      "v_max3_f32 v186, v186, v30, v31\n"
      "v_max3_f32 v186, v186, v236, v237\n"
      "v_max3_f32 v186, v186, v238, v239\n"
      "v_max3_f32 v186, v186, v232, v233\n"
      "v_max3_f32 v186, v186, v234, v235\n"
      "v_mfma_f32_16x16x32_bf16 v[152:155], v[244:247], v[160:163], 0\n"
      "v_mfma_f32_16x16x32_bf16 v[152:155], v[248:251], v[88:91], v[152:155]\n"
      "v_max3_f32 v186, v186, v156, v157\n"
      "v_max3_f32 v186, v186, v158, v159\n"
      "v_mfma_f32_16x16x32_bf16 v[160:163], a[128:131], v[148:151], 0\n"
      "v_mfma_f32_16x16x32_bf16 v[160:163], a[132:135], v[136:139], v[160:163]\n"
      "s_add_u32 s56, s48, 20480\n"
      "buffer_store_dwordx2 v[224:225], v2, s[44:47], s56 offen offset:0 nt sc1\n"
      "buffer_store_dwordx2 v[228:229], v2, s[44:47], s56 offen offset:256 nt sc1\n"
      "v_mfma_f32_16x16x32_bf16 v[160:163], a[136:139], v[140:143], v[160:163]\n"
      "v_max3_f32 v186, v186, v152, v153\n"
      "v_max3_f32 v186, v186, v154, v155\n"
      "v_mov_b32 v88, v186\n"
      "buffer_store_dwordx2 v[226:227], v2, s[44:47], s56 offen offset:512 nt sc1\n"
      "v_mfma_f32_16x16x32_bf16 v[160:163], a[140:143], v[144:147], v[160:163]\n"
      "v_permlane16_swap_b32 v186, v88\n"
      "v_max_f32 v186, v186, v88\n"
      "v_mfma_f32_16x16x32_bf16 v[32:35], a[144:147], v[148:151], 0\n"
      "v_mov_b32 v88, v186\n"
      "buffer_store_dwordx2 v[230:231], v2, s[44:47], s56 offen offset:768 nt sc1\n"
      "v_mfma_f32_16x16x32_bf16 v[32:35], a[148:151], v[136:139], v[32:35]\n"
      "v_permlane32_swap_b32 v186, v88\n"
      "v_max_f32 v186, v186, v88\n"
      "v_sub_f32 v20, v20, v186\n"
      "v_sub_f32 v21, v21, v186\n"
      "v_sub_f32 v22, v22, v186\n"
      "v_sub_f32 v23, v23, v186\n"
      "v_sub_f32 v24, v24, v186\n"
      "v_mfma_f32_16x16x32_bf16 v[32:35], a[152:155], v[140:143], v[32:35]\n"
      "v_exp_f32 v20, v20\n"
      "v_exp_f32 v21, v21\n"
      "v_exp_f32 v22, v22\n"
      "v_cvt_pk_bf16_f32 v190, v20, v21\n"
      "v_mfma_f32_16x16x32_bf16 v[32:35], a[156:159], v[144:147], v[32:35]\n"
      "v_exp_f32 v23, v23\n"
      "v_sub_f32 v25, v25, v186\n"
      "v_cvt_pk_bf16_f32 v191, v22, v23\n"
      "v_sub_f32 v26, v26, v186\n"
      "v_sub_f32 v27, v27, v186\n"
      "v_sub_f32 v28, v28, v186\n"
      "v_mfma_f32_16x16x32_bf16 v[88:91], a[160:163], v[148:151], 0\n"
      "v_sub_f32 v29, v29, v186\n"
      "v_sub_f32 v30, v30, v186\n"
      "v_sub_f32 v31, v31, v186\n"
      "v_sub_f32 v236, v236, v186\n"
      "v_sub_f32 v237, v237, v186\n"
      "v_sub_f32 v238, v238, v186\n"
      "v_sub_f32 v239, v239, v186\n"
      "v_mfma_f32_16x16x32_bf16 v[88:91], a[164:167], v[136:139], v[88:91]\n"
      "v_sub_f32 v232, v232, v186\n"
      "v_sub_f32 v233, v233, v186\n"
      "v_sub_f32 v234, v234, v186\n"
      "v_sub_f32 v235, v235, v186\n"
      "v_add_f32 v184, v20, v21\n"
      "v_add_f32 v185, v22, v23\n"
      "v_mfma_f32_16x16x32_bf16 v[88:91], a[168:171], v[140:143], v[88:91]\n"
      "v_exp_f32 v24, v24\n"
      "v_exp_f32 v25, v25\n"
      "v_exp_f32 v26, v26\n"
      "v_exp_f32 v27, v27\n"
      "v_mfma_f32_16x16x32_bf16 v[88:91], a[172:175], v[144:147], v[88:91]\n"
      "v_cvt_pk_bf16_f32 v20, v24, v25\n"
      "v_cvt_pk_bf16_f32 v21, v26, v27\n"
      "v_exp_f32 v28, v28\n"
      "v_exp_f32 v29, v29\n"
      "v_mfma_f32_16x16x32_bf16 v[208:211], a[176:179], v[148:151], 0\n"
      "v_exp_f32 v30, v30\n"
      "v_exp_f32 v31, v31\n"
      "v_cvt_pk_bf16_f32 v22, v28, v29\n"
      "v_cvt_pk_bf16_f32 v23, v30, v31\n"
      "v_exp_f32 v236, v236\n"
      "v_mfma_f32_16x16x32_bf16 v[208:211], a[180:183], v[136:139], v[208:211]\n"
      "v_exp_f32 v237, v237\n"
      "v_exp_f32 v238, v238\n"
      "v_exp_f32 v239, v239\n"
      "v_cvt_pk_bf16_f32 v24, v236, v237\n"
      "v_mfma_f32_16x16x32_bf16 v[208:211], a[184:187], v[140:143], v[208:211]\n"
      "v_cvt_pk_bf16_f32 v25, v238, v239\n"
      "v_exp_f32 v232, v232\n"
      "v_exp_f32 v233, v233\n"
      "v_exp_f32 v234, v234\n"
      "v_mfma_f32_16x16x32_bf16 v[208:211], a[188:191], v[144:147], v[208:211]\n"
      "v_exp_f32 v235, v235\n"
      "v_cvt_pk_bf16_f32 v26, v232, v233\n"
      "v_cvt_pk_bf16_f32 v27, v234, v235\n"
      "v_sub_f32 v156, v156, v186\n"
      "v_sub_f32 v157, v157, v186\n"
      "v_sub_f32 v158, v158, v186\n"
      "v_mfma_f32_16x16x4_4b_bf16 v[224:239], v[190:191], v[18:19], 0 cbsz:2 abid:0\n"
      "v_sub_f32 v159, v159, v186\n"
      "v_sub_f32 v152, v152, v186\n"
      "v_sub_f32 v153, v153, v186\n"
      "v_sub_f32 v154, v154, v186\n"
      "v_sub_f32 v155, v155, v186\n"
      "v_exp_f32 v156, v156\n"
      "v_exp_f32 v157, v157\n"
      "v_exp_f32 v158, v158\n"
      "v_exp_f32 v159, v159\n"
      "v_cvt_pk_bf16_f32 v28, v156, v157\n"
      "v_cvt_pk_bf16_f32 v29, v158, v159\n"
      "v_exp_f32 v152, v152\n"
      "v_exp_f32 v153, v153\n"
      "v_cvt_pk_bf16_f32 v156, v160, v161\n"
      "v_smfmac_f32_16x16x64_bf16 v[184:187], v[12:15], v[20:27], v5\n"
      "v_exp_f32 v154, v154\n"
      "v_exp_f32 v155, v155\n"
      "v_mfma_f32_16x16x32_bf16 v[224:227], v[20:23], v[52:55], v[224:227]\n"
      "v_cvt_pk_bf16_f32 v30, v152, v153\n"
      "v_cvt_pk_bf16_f32 v31, v154, v155\n"
      "v_cvt_pk_bf16_f32 v157, v162, v163\n"
      "v_mfma_f32_16x16x32_bf16 v[224:227], v[24:27], v[56:59], v[224:227]\n"
      "v_cvt_pk_bf16_f32 v158, v32, v33\n"
      "v_cvt_pk_bf16_f32 v159, v34, v35\n"
      "v_cvt_pk_bf16_f32 v32, v88, v89\n"
      "v_mfma_f32_16x16x32_bf16 v[224:227], v[28:31], v[120:123], v[224:227]\n"
      "v_cvt_pk_bf16_f32 v33, v90, v91\n"
      "v_cvt_pk_bf16_f32 v34, v208, v209\n"
      "v_cvt_pk_bf16_f32 v35, v210, v211\n"
      "v_smfmac_f32_16x16x32_bf16 v[184:187], v[12:13], v[28:31], v5\n"
      "v_mfma_f32_16x16x32_bf16 v[228:231], v[20:23], v[128:131], v[228:231]\n"
      "v_mfma_f32_16x16x32_bf16 v[228:231], v[24:27], v[132:135], v[228:231]\n"
      "v_mfma_f32_16x16x32_bf16 v[228:231], v[28:31], v[68:71], v[228:231]\n"
      "v_mfma_f32_16x16x32_bf16 v[232:235], v[20:23], v[100:103], v[232:235]\n"
      "v_mfma_f32_16x16x32_bf16 v[232:235], v[24:27], v[104:107], v[232:235]\n"
      "v_mfma_f32_16x16x32_bf16 v[232:235], v[28:31], v[36:39], v[232:235]\n"
      "v_mfma_f32_16x16x32_bf16 v[236:239], v[20:23], v[192:195], v[236:239]\n"
      "v_mfma_f32_16x16x32_bf16 v[236:239], v[24:27], v[196:199], v[236:239]\n"
      "v_add_f32 v184, v184, v185\n"
      "v_rcp_f32 v185, v184\n"
      "v_mfma_f32_16x16x32_bf16 v[236:239], v[28:31], v[240:243], v[236:239]\n"
      "ds_bpermute_b32 v186, v4, v185\n"
      "s_waitcnt lgkmcnt(0)\n"
      "v_mul_f32_dpp v224, v186, v224 row_newbcast:0 row_mask:0xf bank_mask:0xf\n"
      "v_mul_f32_dpp v225, v186, v225 row_newbcast:4 row_mask:0xf bank_mask:0xf\n"
      "v_mul_f32_dpp v226, v186, v226 row_newbcast:8 row_mask:0xf bank_mask:0xf\n"
      "v_mul_f32_dpp v227, v186, v227 row_newbcast:12 row_mask:0xf bank_mask:0xf\n"
      "v_mul_f32_dpp v228, v186, v228 row_newbcast:0 row_mask:0xf bank_mask:0xf\n"
      "v_mul_f32_dpp v229, v186, v229 row_newbcast:4 row_mask:0xf bank_mask:0xf\n"
      "v_mfma_f32_16x16x32_bf16 v[224:227], v[252:255], a[192:195], v[224:227]\n"
      "v_mul_f32_dpp v230, v186, v230 row_newbcast:8 row_mask:0xf bank_mask:0xf\n"
      "v_mul_f32_dpp v231, v186, v231 row_newbcast:12 row_mask:0xf bank_mask:0xf\n"
      "v_mfma_f32_16x16x32_bf16 v[224:227], v[48:51], a[196:199], v[224:227]\n"
      "v_mul_f32_dpp v232, v186, v232 row_newbcast:0 row_mask:0xf bank_mask:0xf\n"
      "v_mul_f32_dpp v233, v186, v233 row_newbcast:4 row_mask:0xf bank_mask:0xf\n"
      "v_mul_f32_dpp v234, v186, v234 row_newbcast:8 row_mask:0xf bank_mask:0xf\n"
      "v_mfma_f32_16x16x32_bf16 v[224:227], v[124:127], a[200:203], v[224:227]\n"
      "v_mul_f32_dpp v235, v186, v235 row_newbcast:12 row_mask:0xf bank_mask:0xf\n"
      "v_mul_f32_dpp v236, v186, v236 row_newbcast:0 row_mask:0xf bank_mask:0xf\n"
      "v_mul_f32_dpp v237, v186, v237 row_newbcast:4 row_mask:0xf bank_mask:0xf\n"
      "v_mul_f32_dpp v238, v186, v238 row_newbcast:8 row_mask:0xf bank_mask:0xf\n"
      "v_mfma_f32_16x16x32_bf16 v[224:227], v[116:119], a[204:207], v[224:227]\n"
      "v_mul_f32_dpp v239, v186, v239 row_newbcast:12 row_mask:0xf bank_mask:0xf\n"
      "v_mfma_f32_16x16x32_bf16 v[228:231], v[252:255], a[208:211], v[228:231]\n"
      "v_mfma_f32_16x16x32_bf16 v[228:231], v[48:51], a[212:215], v[228:231]\n"
      "v_mfma_f32_16x16x32_bf16 v[228:231], v[124:127], a[216:219], v[228:231]\n"
      "v_mfma_f32_16x16x32_bf16 v[228:231], v[116:119], a[220:223], v[228:231]\n"
      "v_mfma_f32_16x16x32_bf16 v[232:235], v[252:255], a[224:227], v[232:235]\n"
      "v_mfma_f32_16x16x32_bf16 v[232:235], v[48:51], a[228:231], v[232:235]\n"
      "v_mfma_f32_16x16x32_bf16 v[232:235], v[124:127], a[232:235], v[232:235]\n"
      "v_mfma_f32_16x16x32_bf16 v[232:235], v[116:119], a[236:239], v[232:235]\n"
      "v_mfma_f32_16x16x32_bf16 v[236:239], v[252:255], a[240:243], v[236:239]\n"
      "v_mfma_f32_16x16x32_bf16 v[236:239], v[48:51], a[244:247], v[236:239]\n"
      "v_mfma_f32_16x16x32_bf16 v[236:239], v[124:127], a[248:251], v[236:239]\n"
      "v_mfma_f32_16x16x32_bf16 v[236:239], v[116:119], a[252:255], v[236:239]\n"
      "v_cvt_pk_bf16_f32 v224, v224, v228\n"
      "v_cvt_pk_bf16_f32 v228, v225, v229\n"
      "v_pk_max_i16 v224, v224, 0\n"
      "v_pk_max_i16 v228, v228, 0\n"
      "v_cvt_pk_bf16_f32 v226, v226, v230\n"
      "v_mfma_f32_16x16x32_bf16 v[20:23], v[172:175], v[156:159], 0\n"
      "v_cvt_pk_bf16_f32 v230, v227, v231\n"
      "v_pk_max_i16 v226, v226, 0\n"
      "v_cvt_pk_bf16_f32 v225, v232, v236\n"
      "v_cvt_pk_bf16_f32 v229, v233, v237\n"
      "v_mfma_f32_16x16x32_bf16 v[20:23], v[176:179], v[32:35], v[20:23]\n"
      "v_pk_max_i16 v225, v225, 0\n"
      "v_pk_max_i16 v229, v229, 0\n"
      "v_cvt_pk_bf16_f32 v227, v234, v238\n"
      "v_cvt_pk_bf16_f32 v231, v235, v239\n"
      "v_pk_max_i16 v227, v227, 0\n"
      "v_mfma_f32_16x16x32_bf16 v[88:91], v[108:111], v[156:159], 0\n"
      "v_pk_max_i16 v230, v230, 0\n"
      "v_pk_max_i16 v231, v231, 0\n"
      "v_max_f32 v190, v20, v21\n"
      "v_max3_f32 v190, v190, v22, v23\n"
      "v_mfma_f32_16x16x32_bf16 v[88:91], v[112:115], v[32:35], v[88:91]\n"
      "v_mfma_f32_16x16x32_bf16 v[24:27], v[164:167], v[156:159], 0\n"
      "v_mfma_f32_16x16x32_bf16 v[24:27], v[168:171], v[32:35], v[24:27]\n"
      "v_mfma_f32_16x16x32_bf16 v[236:239], v[40:43], v[156:159], 0\n"
      "v_mfma_f32_16x16x32_bf16 v[236:239], v[44:47], v[32:35], v[236:239]\n"
      "v_mfma_f32_16x16x32_bf16 v[232:235], v[92:95], v[156:159], 0\n"
      "v_mfma_f32_16x16x32_bf16 v[232:235], v[96:99], v[32:35], v[232:235]\n"
      "v_mfma_f32_16x16x32_bf16 v[160:163], v[216:219], v[156:159], 0\n"
      "v_mfma_f32_16x16x32_bf16 v[160:163], v[220:223], v[32:35], v[160:163]\n"
      "v_max3_f32 v190, v190, v88, v89\n"
      "v_max3_f32 v190, v190, v90, v91\n"
      "v_max3_f32 v190, v190, v24, v25\n"
      "v_max3_f32 v190, v190, v26, v27\n"
      "v_max3_f32 v190, v190, v236, v237\n"
      "v_max3_f32 v190, v190, v238, v239\n"
      "v_max3_f32 v190, v190, v232, v233\n"
      "v_mfma_f32_16x16x32_bf16 v[152:155], v[244:247], v[156:159], 0\n"
      "v_max3_f32 v190, v190, v234, v235\n"
      "v_max3_f32 v190, v190, v160, v161\n"
      "v_max3_f32 v190, v190, v162, v163\n"
      "v_mfma_f32_16x16x32_bf16 v[152:155], v[248:251], v[32:35], v[152:155]\n"
      "ds_read_b128 v[116:119], v0 offset:8192\n"
      "ds_read_b128 v[124:127], v0 offset:8704\n"
      "ds_read_b128 v[48:51], v0 offset:9216\n"
      "ds_read_b128 v[252:255], v0 offset:9728\n"
      "s_add_u32 s56, s48, 16384\n"
      "buffer_store_dwordx2 v[224:225], v2, s[44:47], s56 offen offset:0 nt sc1\n"
      "buffer_store_dwordx2 v[228:229], v2, s[44:47], s56 offen offset:256 nt sc1\n"
      "buffer_store_dwordx2 v[226:227], v2, s[44:47], s56 offen offset:512 nt sc1\n"
      "v_max3_f32 v190, v190, v152, v153\n"
      "v_max3_f32 v190, v190, v154, v155\n"
      "v_mov_b32 v32, v190\n"
      "buffer_store_dwordx2 v[230:231], v2, s[44:47], s56 offen offset:768 nt sc1\n"
      "s_nop 0\n"
      "v_permlane16_swap_b32 v190, v32\n"
      "v_max_f32 v190, v190, v32\n"
      "v_mov_b32 v32, v190\n"
      "s_nop 1\n"
      "v_permlane32_swap_b32 v190, v32\n"
      "v_max_f32 v190, v190, v32\n"
      "v_sub_f32 v20, v20, v190\n"
      "v_sub_f32 v21, v21, v190\n"
      "v_sub_f32 v22, v22, v190\n"
      "s_waitcnt lgkmcnt(3)\n"
      "v_mfma_f32_16x16x32_bf16 v[32:35], a[128:131], v[116:119], 0\n"
      "v_sub_f32 v23, v23, v190\n"
      "v_sub_f32 v88, v88, v190\n"
      "s_waitcnt lgkmcnt(2)\n"
      "v_mfma_f32_16x16x32_bf16 v[32:35], a[132:135], v[124:127], v[32:35]\n"
      "v_exp_f32 v20, v20\n"
      "s_waitcnt lgkmcnt(1)\n"
      "v_mfma_f32_16x16x32_bf16 v[32:35], a[136:139], v[48:51], v[32:35]\n"
      "v_exp_f32 v21, v21\n"
      "s_waitcnt lgkmcnt(0)\n"
      "v_mfma_f32_16x16x32_bf16 v[32:35], a[140:143], v[252:255], v[32:35]\n"
      "v_exp_f32 v22, v22\n"
      "v_mfma_f32_16x16x32_bf16 v[28:31], a[144:147], v[116:119], 0\n"
      "v_exp_f32 v23, v23\n"
      "v_mfma_f32_16x16x32_bf16 v[28:31], a[148:151], v[124:127], v[28:31]\n"
      "v_cvt_pk_bf16_f32 v156, v20, v21\n"
      "v_cvt_pk_bf16_f32 v157, v22, v23\n"
      "v_mfma_f32_16x16x32_bf16 v[28:31], a[152:155], v[48:51], v[28:31]\n"
      "v_sub_f32 v89, v89, v190\n"
      "v_sub_f32 v90, v90, v190\n"
      "v_sub_f32 v91, v91, v190\n"
      "v_sub_f32 v24, v24, v190\n"
      "v_mfma_f32_16x16x32_bf16 v[28:31], a[156:159], v[252:255], v[28:31]\n"
      "v_sub_f32 v25, v25, v190\n"
      "v_sub_f32 v26, v26, v190\n"
      "v_sub_f32 v27, v27, v190\n"
      "v_sub_f32 v236, v236, v190\n"
      "v_sub_f32 v237, v237, v190\n"
      "v_sub_f32 v238, v238, v190\n"
      "v_mfma_f32_16x16x32_bf16 v[184:187], a[160:163], v[116:119], 0\n"
      "v_sub_f32 v239, v239, v190\n"
      "v_sub_f32 v232, v232, v190\n"
      "v_sub_f32 v233, v233, v190\n"
      "v_sub_f32 v234, v234, v190\n"
      "v_sub_f32 v235, v235, v190\n"
      "v_add_f32 v86, v20, v21\n"
      "v_add_f32 v87, v22, v23\n"
      "v_mfma_f32_16x16x32_bf16 v[184:187], a[164:167], v[124:127], v[184:187]\n"
      "v_exp_f32 v88, v88\n"
      "v_exp_f32 v89, v89\n"
      "v_exp_f32 v90, v90\n"
      "v_mfma_f32_16x16x32_bf16 v[184:187], a[168:171], v[48:51], v[184:187]\n"
      "v_exp_f32 v91, v91\n"
      "v_cvt_pk_bf16_f32 v20, v88, v89\n"
      "v_cvt_pk_bf16_f32 v21, v90, v91\n"
      "v_exp_f32 v24, v24\n"
      "v_mfma_f32_16x16x32_bf16 v[184:187], a[172:175], v[252:255], v[184:187]\n"
      "v_exp_f32 v25, v25\n"
      "v_exp_f32 v26, v26\n"
      "v_exp_f32 v27, v27\n"
      "v_cvt_pk_bf16_f32 v22, v24, v25\n"
      "v_mfma_f32_16x16x32_bf16 v[208:211], a[176:179], v[116:119], 0\n"
      "v_cvt_pk_bf16_f32 v23, v26, v27\n"
      "v_exp_f32 v236, v236\n"
      "v_exp_f32 v237, v237\n"
      "v_exp_f32 v238, v238\n"
      "v_mfma_f32_16x16x32_bf16 v[208:211], a[180:183], v[124:127], v[208:211]\n"
      "v_exp_f32 v239, v239\n"
      "v_cvt_pk_bf16_f32 v24, v236, v237\n"
      "v_cvt_pk_bf16_f32 v25, v238, v239\n"
      "v_exp_f32 v232, v232\n"
      "v_mfma_f32_16x16x32_bf16 v[208:211], a[184:187], v[48:51], v[208:211]\n"
      "v_exp_f32 v233, v233\n"
      "v_exp_f32 v234, v234\n"
      "v_exp_f32 v235, v235\n"
      "v_cvt_pk_bf16_f32 v26, v232, v233\n"
      "v_mfma_f32_16x16x32_bf16 v[208:211], a[188:191], v[252:255], v[208:211]\n"
      "v_cvt_pk_bf16_f32 v27, v234, v235\n"
      "v_sub_f32 v160, v160, v190\n"
      "v_sub_f32 v161, v161, v190\n"
      "v_sub_f32 v162, v162, v190\n"
      "v_sub_f32 v163, v163, v190\n"
      "v_sub_f32 v152, v152, v190\n"
      "v_mfma_f32_16x16x4_4b_bf16 v[224:239], v[156:157], v[18:19], 0 cbsz:2 abid:0\n"
      "s_add_u32 m0, s53, 8192\n"
      "s_add_u32 s54, s49, 8192\n"
      "buffer_load_dwordx4 v1, s[40:43], s54 offen offset:0 lds\n"
      "v_sub_f32 v153, v153, v190\n"
      "v_sub_f32 v154, v154, v190\n"
      "v_sub_f32 v155, v155, v190\n"
      "v_exp_f32 v160, v160\n"
      "v_exp_f32 v161, v161\n"
      "v_exp_f32 v162, v162\n"
      "v_exp_f32 v163, v163\n"
      "v_cvt_pk_bf16_f32 v188, v160, v161\n"
      "v_cvt_pk_bf16_f32 v189, v162, v163\n"
      "v_exp_f32 v152, v152\n"
      "v_exp_f32 v153, v153\n"
      "v_exp_f32 v154, v154\n"
      "v_smfmac_f32_16x16x64_bf16 v[86:89], v[12:15], v[20:27], v5\n"
      "v_exp_f32 v155, v155\n"
      "v_cvt_pk_bf16_f32 v190, v152, v153\n"
      "v_mfma_f32_16x16x32_bf16 v[224:227], v[20:23], v[52:55], v[224:227]\n"
      "buffer_load_dwordx4 v1, s[40:43], s54 offen offset:2048 lds\n"
      "s_add_u32 m0, s53, 9088\n"
      "v_cvt_pk_bf16_f32 v191, v154, v155\n"
      "v_cvt_pk_bf16_f32 v156, v32, v33\n"
      "v_cvt_pk_bf16_f32 v157, v34, v35\n"
      "v_mfma_f32_16x16x32_bf16 v[224:227], v[24:27], v[56:59], v[224:227]\n"
      "v_cvt_pk_bf16_f32 v158, v28, v29\n"
      "v_cvt_pk_bf16_f32 v159, v30, v31\n"
      "v_cvt_pk_bf16_f32 v32, v184, v185\n"
      "v_mfma_f32_16x16x32_bf16 v[224:227], v[188:191], v[120:123], v[224:227]\n"
      "buffer_load_dwordx4 v1, s[40:43], s54 offen offset:128 lds\n"
      "v_cvt_pk_bf16_f32 v33, v186, v187\n"
      "v_cvt_pk_bf16_f32 v34, v208, v209\n"
      "v_cvt_pk_bf16_f32 v35, v210, v211\n"
      "v_smfmac_f32_16x16x32_bf16 v[86:89], v[12:13], v[188:191], v5\n"
      "v_mfma_f32_16x16x32_bf16 v[228:231], v[20:23], v[128:131], v[228:231]\n"
      "buffer_load_dwordx4 v1, s[40:43], s54 offen offset:2176 lds\n"
      "v_mfma_f32_16x16x32_bf16 v[228:231], v[24:27], v[132:135], v[228:231]\n"
      "v_mfma_f32_16x16x32_bf16 v[228:231], v[188:191], v[68:71], v[228:231]\n"
      "v_mfma_f32_16x16x32_bf16 v[232:235], v[20:23], v[100:103], v[232:235]\n"
      "v_mfma_f32_16x16x32_bf16 v[232:235], v[24:27], v[104:107], v[232:235]\n"
      "v_mfma_f32_16x16x32_bf16 v[232:235], v[188:191], v[36:39], v[232:235]\n"
      "v_mfma_f32_16x16x32_bf16 v[236:239], v[20:23], v[192:195], v[236:239]\n"
      "v_add_f32 v86, v86, v87\n"
      "v_rcp_f32 v87, v86\n"
      "v_mfma_f32_16x16x32_bf16 v[236:239], v[24:27], v[196:199], v[236:239]\n"
      "ds_bpermute_b32 v88, v4, v87\n"
      "v_mfma_f32_16x16x32_bf16 v[236:239], v[188:191], v[240:243], v[236:239]\n"
      "s_waitcnt lgkmcnt(0)\n"
      "v_mul_f32_dpp v224, v88, v224 row_newbcast:0 row_mask:0xf bank_mask:0xf\n"
      "v_mul_f32_dpp v225, v88, v225 row_newbcast:4 row_mask:0xf bank_mask:0xf\n"
      "v_mul_f32_dpp v226, v88, v226 row_newbcast:8 row_mask:0xf bank_mask:0xf\n"
      "v_mul_f32_dpp v227, v88, v227 row_newbcast:12 row_mask:0xf bank_mask:0xf\n"
      "v_mul_f32_dpp v228, v88, v228 row_newbcast:0 row_mask:0xf bank_mask:0xf\n"
      "v_mul_f32_dpp v229, v88, v229 row_newbcast:4 row_mask:0xf bank_mask:0xf\n"
      "v_mfma_f32_16x16x32_bf16 v[224:227], v[148:151], a[192:195], v[224:227]\n"
      "v_mul_f32_dpp v230, v88, v230 row_newbcast:8 row_mask:0xf bank_mask:0xf\n"
      "v_mul_f32_dpp v231, v88, v231 row_newbcast:12 row_mask:0xf bank_mask:0xf\n"
      "v_mfma_f32_16x16x32_bf16 v[224:227], v[136:139], a[196:199], v[224:227]\n"
      "v_mul_f32_dpp v232, v88, v232 row_newbcast:0 row_mask:0xf bank_mask:0xf\n"
      "v_mul_f32_dpp v233, v88, v233 row_newbcast:4 row_mask:0xf bank_mask:0xf\n"
      "v_mul_f32_dpp v234, v88, v234 row_newbcast:8 row_mask:0xf bank_mask:0xf\n"
      "v_mfma_f32_16x16x32_bf16 v[224:227], v[140:143], a[200:203], v[224:227]\n"
      "v_mul_f32_dpp v235, v88, v235 row_newbcast:12 row_mask:0xf bank_mask:0xf\n"
      "v_mul_f32_dpp v236, v88, v236 row_newbcast:0 row_mask:0xf bank_mask:0xf\n"
      "v_mul_f32_dpp v237, v88, v237 row_newbcast:4 row_mask:0xf bank_mask:0xf\n"
      "v_mul_f32_dpp v238, v88, v238 row_newbcast:8 row_mask:0xf bank_mask:0xf\n"
      "v_mfma_f32_16x16x32_bf16 v[224:227], v[144:147], a[204:207], v[224:227]\n"
      "v_mul_f32_dpp v239, v88, v239 row_newbcast:12 row_mask:0xf bank_mask:0xf\n"
      "v_mfma_f32_16x16x32_bf16 v[228:231], v[148:151], a[208:211], v[228:231]\n"
      "v_mfma_f32_16x16x32_bf16 v[228:231], v[136:139], a[212:215], v[228:231]\n"
      "v_mfma_f32_16x16x32_bf16 v[228:231], v[140:143], a[216:219], v[228:231]\n"
      "v_mfma_f32_16x16x32_bf16 v[228:231], v[144:147], a[220:223], v[228:231]\n"
      "v_mfma_f32_16x16x32_bf16 v[232:235], v[148:151], a[224:227], v[232:235]\n"
      "v_mfma_f32_16x16x32_bf16 v[232:235], v[136:139], a[228:231], v[232:235]\n"
      "v_mfma_f32_16x16x32_bf16 v[232:235], v[140:143], a[232:235], v[232:235]\n"
      "v_mfma_f32_16x16x32_bf16 v[232:235], v[144:147], a[236:239], v[232:235]\n"
      "v_mfma_f32_16x16x32_bf16 v[236:239], v[148:151], a[240:243], v[236:239]\n"
      "v_mfma_f32_16x16x32_bf16 v[236:239], v[136:139], a[244:247], v[236:239]\n"
      "v_mfma_f32_16x16x32_bf16 v[236:239], v[140:143], a[248:251], v[236:239]\n"
      "v_mfma_f32_16x16x32_bf16 v[236:239], v[144:147], a[252:255], v[236:239]\n"
      "v_cvt_pk_bf16_f32 v224, v224, v228\n"
      "v_cvt_pk_bf16_f32 v228, v225, v229\n"
      "v_pk_max_i16 v224, v224, 0\n"
      "v_pk_max_i16 v228, v228, 0\n"
      "v_cvt_pk_bf16_f32 v226, v226, v230\n"
      "v_mfma_f32_16x16x32_bf16 v[140:143], v[172:175], v[156:159], 0\n"
      "v_cvt_pk_bf16_f32 v230, v227, v231\n"
      "v_pk_max_i16 v226, v226, 0\n"
      "v_cvt_pk_bf16_f32 v225, v232, v236\n"
      "v_cvt_pk_bf16_f32 v229, v233, v237\n"
      "v_mfma_f32_16x16x32_bf16 v[140:143], v[176:179], v[32:35], v[140:143]\n"
      "v_pk_max_i16 v225, v225, 0\n"
      "v_pk_max_i16 v229, v229, 0\n"
      "v_cvt_pk_bf16_f32 v227, v234, v238\n"
      "v_cvt_pk_bf16_f32 v231, v235, v239\n"
      "v_pk_max_i16 v227, v227, 0\n"
      "v_mfma_f32_16x16x32_bf16 v[88:91], v[108:111], v[156:159], 0\n"
      "v_pk_max_i16 v230, v230, 0\n"
      "v_pk_max_i16 v231, v231, 0\n"
      "v_max_f32 v188, v140, v141\n"
      "v_max3_f32 v188, v188, v142, v143\n"
      "v_mfma_f32_16x16x32_bf16 v[88:91], v[112:115], v[32:35], v[88:91]\n"
      "v_mfma_f32_16x16x32_bf16 v[144:147], v[164:167], v[156:159], 0\n"
      "v_mfma_f32_16x16x32_bf16 v[144:147], v[168:171], v[32:35], v[144:147]\n"
      "v_mfma_f32_16x16x32_bf16 v[236:239], v[40:43], v[156:159], 0\n"
      "v_mfma_f32_16x16x32_bf16 v[236:239], v[44:47], v[32:35], v[236:239]\n"
      "v_mfma_f32_16x16x32_bf16 v[232:235], v[92:95], v[156:159], 0\n"
      "v_mfma_f32_16x16x32_bf16 v[232:235], v[96:99], v[32:35], v[232:235]\n"
      "v_mfma_f32_16x16x32_bf16 v[160:163], v[216:219], v[156:159], 0\n"
      "v_mfma_f32_16x16x32_bf16 v[160:163], v[220:223], v[32:35], v[160:163]\n"
      "v_max3_f32 v188, v188, v88, v89\n"
      "v_max3_f32 v188, v188, v90, v91\n"
      "v_max3_f32 v188, v188, v144, v145\n"
      "v_max3_f32 v188, v188, v146, v147\n"
      "v_max3_f32 v188, v188, v236, v237\n"
      "v_max3_f32 v188, v188, v238, v239\n"
      "v_max3_f32 v188, v188, v232, v233\n"
      "v_mfma_f32_16x16x32_bf16 v[152:155], v[244:247], v[156:159], 0\n"
      "v_max3_f32 v188, v188, v234, v235\n"
      "v_max3_f32 v188, v188, v160, v161\n"
      "v_max3_f32 v188, v188, v162, v163\n"
      "v_mfma_f32_16x16x32_bf16 v[152:155], v[248:251], v[32:35], v[152:155]\n"
      "ds_read_b128 v[136:139], v0 offset:4096\n"
      "ds_read_b128 v[148:151], v0 offset:4608\n"
      "ds_read_b128 v[24:27], v0 offset:5120\n"
      "ds_read_b128 v[20:23], v0 offset:5632\n"
      "s_add_u32 s56, s48, 12288\n"
      "buffer_store_dwordx2 v[224:225], v2, s[44:47], s56 offen offset:0 nt sc1\n"
      "buffer_store_dwordx2 v[228:229], v2, s[44:47], s56 offen offset:256 nt sc1\n"
      "buffer_store_dwordx2 v[226:227], v2, s[44:47], s56 offen offset:512 nt sc1\n"
      "v_max3_f32 v188, v188, v152, v153\n"
      "v_max3_f32 v188, v188, v154, v155\n"
      "v_mov_b32 v32, v188\n"
      "buffer_store_dwordx2 v[230:231], v2, s[44:47], s56 offen offset:768 nt sc1\n"
      "s_nop 0\n"
      "v_permlane16_swap_b32 v188, v32\n"
      "v_max_f32 v188, v188, v32\n"
      "v_mov_b32 v32, v188\n"
      "s_nop 1\n"
      "v_permlane32_swap_b32 v188, v32\n"
      "v_max_f32 v188, v188, v32\n"
      "v_sub_f32 v140, v140, v188\n"
      "v_sub_f32 v141, v141, v188\n"
      "v_sub_f32 v142, v142, v188\n"
      "s_waitcnt lgkmcnt(3)\n"
      "v_mfma_f32_16x16x32_bf16 v[32:35], a[128:131], v[136:139], 0\n"
      "v_sub_f32 v143, v143, v188\n"
      "v_sub_f32 v88, v88, v188\n"
      "s_waitcnt lgkmcnt(2)\n"
      "v_mfma_f32_16x16x32_bf16 v[32:35], a[132:135], v[148:151], v[32:35]\n"
      "v_exp_f32 v140, v140\n"
      "s_waitcnt lgkmcnt(1)\n"
      "v_mfma_f32_16x16x32_bf16 v[32:35], a[136:139], v[24:27], v[32:35]\n"
      "v_exp_f32 v141, v141\n"
      "s_waitcnt lgkmcnt(0)\n"
      "v_mfma_f32_16x16x32_bf16 v[32:35], a[140:143], v[20:23], v[32:35]\n"
      "v_exp_f32 v142, v142\n"
      "v_mfma_f32_16x16x32_bf16 v[28:31], a[144:147], v[136:139], 0\n"
      "v_exp_f32 v143, v143\n"
      "v_mfma_f32_16x16x32_bf16 v[28:31], a[148:151], v[148:151], v[28:31]\n"
      "v_cvt_pk_bf16_f32 v156, v140, v141\n"
      "v_cvt_pk_bf16_f32 v157, v142, v143\n"
      "v_mfma_f32_16x16x32_bf16 v[28:31], a[152:155], v[24:27], v[28:31]\n"
      "v_sub_f32 v89, v89, v188\n"
      "v_sub_f32 v90, v90, v188\n"
      "v_sub_f32 v91, v91, v188\n"
      "v_sub_f32 v144, v144, v188\n"
      "v_mfma_f32_16x16x32_bf16 v[28:31], a[156:159], v[20:23], v[28:31]\n"
      "v_sub_f32 v145, v145, v188\n"
      "v_sub_f32 v146, v146, v188\n"
      "v_sub_f32 v147, v147, v188\n"
      "v_sub_f32 v236, v236, v188\n"
      "v_sub_f32 v237, v237, v188\n"
      "v_sub_f32 v238, v238, v188\n"
      "v_mfma_f32_16x16x32_bf16 v[184:187], a[160:163], v[136:139], 0\n"
      "v_sub_f32 v239, v239, v188\n"
      "v_sub_f32 v232, v232, v188\n"
      "v_sub_f32 v233, v233, v188\n"
      "v_sub_f32 v234, v234, v188\n"
      "v_sub_f32 v235, v235, v188\n"
      "v_add_f32 v86, v140, v141\n"
      "v_add_f32 v87, v142, v143\n"
      "v_mfma_f32_16x16x32_bf16 v[184:187], a[164:167], v[148:151], v[184:187]\n"
      "v_exp_f32 v88, v88\n"
      "v_exp_f32 v89, v89\n"
      "v_exp_f32 v90, v90\n"
      "v_mfma_f32_16x16x32_bf16 v[184:187], a[168:171], v[24:27], v[184:187]\n"
      "v_exp_f32 v91, v91\n"
      "v_cvt_pk_bf16_f32 v140, v88, v89\n"
      "v_cvt_pk_bf16_f32 v141, v90, v91\n"
      "v_exp_f32 v144, v144\n"
      "v_mfma_f32_16x16x32_bf16 v[184:187], a[172:175], v[20:23], v[184:187]\n"
      "v_exp_f32 v145, v145\n"
      "v_exp_f32 v146, v146\n"
      "v_exp_f32 v147, v147\n"
      "v_cvt_pk_bf16_f32 v142, v144, v145\n"
      "v_mfma_f32_16x16x32_bf16 v[208:211], a[176:179], v[136:139], 0\n"
      "v_cvt_pk_bf16_f32 v143, v146, v147\n"
      "v_exp_f32 v236, v236\n"
      "v_exp_f32 v237, v237\n"
      "v_exp_f32 v238, v238\n"
      "v_mfma_f32_16x16x32_bf16 v[208:211], a[180:183], v[148:151], v[208:211]\n"
      "v_exp_f32 v239, v239\n"
      "v_cvt_pk_bf16_f32 v144, v236, v237\n"
      "v_cvt_pk_bf16_f32 v145, v238, v239\n"
      "v_exp_f32 v232, v232\n"
      "v_mfma_f32_16x16x32_bf16 v[208:211], a[184:187], v[24:27], v[208:211]\n"
      "v_exp_f32 v233, v233\n"
      "v_exp_f32 v234, v234\n"
      "v_exp_f32 v235, v235\n"
      "v_cvt_pk_bf16_f32 v146, v232, v233\n"
      "v_mfma_f32_16x16x32_bf16 v[208:211], a[188:191], v[20:23], v[208:211]\n"
      "v_cvt_pk_bf16_f32 v147, v234, v235\n"
      "v_sub_f32 v160, v160, v188\n"
      "v_sub_f32 v161, v161, v188\n"
      "v_sub_f32 v162, v162, v188\n"
      "v_sub_f32 v163, v163, v188\n"
      "v_sub_f32 v152, v152, v188\n"
      "v_mfma_f32_16x16x4_4b_bf16 v[224:239], v[156:157], v[18:19], 0 cbsz:2 abid:0\n"
      "s_add_u32 m0, s53, 4096\n"
      "s_add_u32 s54, s49, 4096\n"
      "buffer_load_dwordx4 v1, s[40:43], s54 offen offset:0 lds\n"
      "v_sub_f32 v153, v153, v188\n"
      "v_sub_f32 v154, v154, v188\n"
      "v_sub_f32 v155, v155, v188\n"
      "v_exp_f32 v160, v160\n"
      "v_exp_f32 v161, v161\n"
      "v_exp_f32 v162, v162\n"
      "v_exp_f32 v163, v163\n"
      "v_cvt_pk_bf16_f32 v188, v160, v161\n"
      "v_cvt_pk_bf16_f32 v189, v162, v163\n"
      "v_exp_f32 v152, v152\n"
      "v_exp_f32 v153, v153\n"
      "v_exp_f32 v154, v154\n"
      "v_smfmac_f32_16x16x64_bf16 v[86:89], v[12:15], v[140:147], v5\n"
      "v_exp_f32 v155, v155\n"
      "v_cvt_pk_bf16_f32 v190, v152, v153\n"
      "v_mfma_f32_16x16x32_bf16 v[224:227], v[140:143], v[52:55], v[224:227]\n"
      "buffer_load_dwordx4 v1, s[40:43], s54 offen offset:2048 lds\n"
      "s_add_u32 m0, s53, 4992\n"
      "v_cvt_pk_bf16_f32 v191, v154, v155\n"
      "v_cvt_pk_bf16_f32 v156, v32, v33\n"
      "v_cvt_pk_bf16_f32 v157, v34, v35\n"
      "v_mfma_f32_16x16x32_bf16 v[224:227], v[144:147], v[56:59], v[224:227]\n"
      "v_cvt_pk_bf16_f32 v158, v28, v29\n"
      "v_cvt_pk_bf16_f32 v159, v30, v31\n"
      "v_cvt_pk_bf16_f32 v32, v184, v185\n"
      "v_cvt_pk_bf16_f32 v33, v186, v187\n"
      "v_mfma_f32_16x16x32_bf16 v[224:227], v[188:191], v[120:123], v[224:227]\n"
      "buffer_load_dwordx4 v1, s[40:43], s54 offen offset:128 lds\n"
      "v_cvt_pk_bf16_f32 v34, v208, v209\n"
      "v_cvt_pk_bf16_f32 v35, v210, v211\n"
      "v_smfmac_f32_16x16x32_bf16 v[86:89], v[12:13], v[188:191], v5\n"
      "v_mfma_f32_16x16x32_bf16 v[228:231], v[140:143], v[128:131], v[228:231]\n"
      "buffer_load_dwordx4 v1, s[40:43], s54 offen offset:2176 lds\n"
      "v_mfma_f32_16x16x32_bf16 v[228:231], v[144:147], v[132:135], v[228:231]\n"
      "v_mfma_f32_16x16x32_bf16 v[228:231], v[188:191], v[68:71], v[228:231]\n"
      "v_mfma_f32_16x16x32_bf16 v[232:235], v[140:143], v[100:103], v[232:235]\n"
      "v_mfma_f32_16x16x32_bf16 v[232:235], v[144:147], v[104:107], v[232:235]\n"
      "v_mfma_f32_16x16x32_bf16 v[232:235], v[188:191], v[36:39], v[232:235]\n"
      "v_mfma_f32_16x16x32_bf16 v[236:239], v[140:143], v[192:195], v[236:239]\n"
      "v_add_f32 v86, v86, v87\n"
      "v_rcp_f32 v87, v86\n"
      "v_mfma_f32_16x16x32_bf16 v[236:239], v[144:147], v[196:199], v[236:239]\n"
      "ds_bpermute_b32 v88, v4, v87\n"
      "v_mfma_f32_16x16x32_bf16 v[236:239], v[188:191], v[240:243], v[236:239]\n"
      "s_waitcnt lgkmcnt(0)\n"
      "v_mul_f32_dpp v224, v88, v224 row_newbcast:0 row_mask:0xf bank_mask:0xf\n"
      "v_mul_f32_dpp v225, v88, v225 row_newbcast:4 row_mask:0xf bank_mask:0xf\n"
      "v_mul_f32_dpp v226, v88, v226 row_newbcast:8 row_mask:0xf bank_mask:0xf\n"
      "v_mul_f32_dpp v227, v88, v227 row_newbcast:12 row_mask:0xf bank_mask:0xf\n"
      "v_mul_f32_dpp v228, v88, v228 row_newbcast:0 row_mask:0xf bank_mask:0xf\n"
      "v_mul_f32_dpp v229, v88, v229 row_newbcast:4 row_mask:0xf bank_mask:0xf\n"
      "v_mfma_f32_16x16x32_bf16 v[224:227], v[116:119], a[192:195], v[224:227]\n"
      "v_mul_f32_dpp v230, v88, v230 row_newbcast:8 row_mask:0xf bank_mask:0xf\n"
      "v_mul_f32_dpp v231, v88, v231 row_newbcast:12 row_mask:0xf bank_mask:0xf\n"
      "v_mfma_f32_16x16x32_bf16 v[224:227], v[124:127], a[196:199], v[224:227]\n"
      "v_mul_f32_dpp v232, v88, v232 row_newbcast:0 row_mask:0xf bank_mask:0xf\n"
      "v_mul_f32_dpp v233, v88, v233 row_newbcast:4 row_mask:0xf bank_mask:0xf\n"
      "v_mul_f32_dpp v234, v88, v234 row_newbcast:8 row_mask:0xf bank_mask:0xf\n"
      "v_mul_f32_dpp v235, v88, v235 row_newbcast:12 row_mask:0xf bank_mask:0xf\n"
      "v_mfma_f32_16x16x32_bf16 v[224:227], v[48:51], a[200:203], v[224:227]\n"
      "v_mul_f32_dpp v236, v88, v236 row_newbcast:0 row_mask:0xf bank_mask:0xf\n"
      "v_mul_f32_dpp v237, v88, v237 row_newbcast:4 row_mask:0xf bank_mask:0xf\n"
      "v_mul_f32_dpp v238, v88, v238 row_newbcast:8 row_mask:0xf bank_mask:0xf\n"
      "v_mfma_f32_16x16x32_bf16 v[224:227], v[252:255], a[204:207], v[224:227]\n"
      "v_mul_f32_dpp v239, v88, v239 row_newbcast:12 row_mask:0xf bank_mask:0xf\n"
      "v_mfma_f32_16x16x32_bf16 v[228:231], v[116:119], a[208:211], v[228:231]\n"
      "v_mfma_f32_16x16x32_bf16 v[228:231], v[124:127], a[212:215], v[228:231]\n"
      "v_mfma_f32_16x16x32_bf16 v[228:231], v[48:51], a[216:219], v[228:231]\n"
      "v_mfma_f32_16x16x32_bf16 v[228:231], v[252:255], a[220:223], v[228:231]\n"
      "v_mfma_f32_16x16x32_bf16 v[232:235], v[116:119], a[224:227], v[232:235]\n"
      "v_mfma_f32_16x16x32_bf16 v[232:235], v[124:127], a[228:231], v[232:235]\n"
      "v_mfma_f32_16x16x32_bf16 v[232:235], v[48:51], a[232:235], v[232:235]\n"
      "v_mfma_f32_16x16x32_bf16 v[232:235], v[252:255], a[236:239], v[232:235]\n"
      "v_mfma_f32_16x16x32_bf16 v[236:239], v[116:119], a[240:243], v[236:239]\n"
      "v_mfma_f32_16x16x32_bf16 v[236:239], v[124:127], a[244:247], v[236:239]\n"
      "v_mfma_f32_16x16x32_bf16 v[236:239], v[48:51], a[248:251], v[236:239]\n"
      "v_mfma_f32_16x16x32_bf16 v[236:239], v[252:255], a[252:255], v[236:239]\n"
      "v_cvt_pk_bf16_f32 v224, v224, v228\n"
      "v_cvt_pk_bf16_f32 v228, v225, v229\n"
      "v_pk_max_i16 v224, v224, 0\n"
      "v_pk_max_i16 v228, v228, 0\n"
      "v_cvt_pk_bf16_f32 v226, v226, v230\n"
      "v_mfma_f32_16x16x32_bf16 v[140:143], v[172:175], v[156:159], 0\n"
      "v_cvt_pk_bf16_f32 v230, v227, v231\n"
      "v_pk_max_i16 v226, v226, 0\n"
      "v_cvt_pk_bf16_f32 v225, v232, v236\n"
      "v_cvt_pk_bf16_f32 v229, v233, v237\n"
      "v_mfma_f32_16x16x32_bf16 v[140:143], v[176:179], v[32:35], v[140:143]\n"
      "v_pk_max_i16 v225, v225, 0\n"
      "v_pk_max_i16 v229, v229, 0\n"
      "v_cvt_pk_bf16_f32 v227, v234, v238\n"
      "v_cvt_pk_bf16_f32 v231, v235, v239\n"
      "v_pk_max_i16 v227, v227, 0\n"
      "v_mfma_f32_16x16x32_bf16 v[88:91], v[108:111], v[156:159], 0\n"
      "v_pk_max_i16 v230, v230, 0\n"
      "v_pk_max_i16 v231, v231, 0\n"
      "v_max_f32 v188, v140, v141\n"
      "v_max3_f32 v188, v188, v142, v143\n"
      "v_mfma_f32_16x16x32_bf16 v[88:91], v[112:115], v[32:35], v[88:91]\n"
      "v_mfma_f32_16x16x32_bf16 v[144:147], v[164:167], v[156:159], 0\n"
      "v_mfma_f32_16x16x32_bf16 v[144:147], v[168:171], v[32:35], v[144:147]\n"
      "v_mfma_f32_16x16x32_bf16 v[236:239], v[40:43], v[156:159], 0\n"
      "v_mfma_f32_16x16x32_bf16 v[236:239], v[44:47], v[32:35], v[236:239]\n"
      "v_mfma_f32_16x16x32_bf16 v[232:235], v[92:95], v[156:159], 0\n"
      "v_mfma_f32_16x16x32_bf16 v[232:235], v[96:99], v[32:35], v[232:235]\n"
      "v_mfma_f32_16x16x32_bf16 v[160:163], v[216:219], v[156:159], 0\n"
      "v_mfma_f32_16x16x32_bf16 v[160:163], v[220:223], v[32:35], v[160:163]\n"
      "v_max3_f32 v188, v188, v88, v89\n"
      "v_max3_f32 v188, v188, v90, v91\n"
      "v_max3_f32 v188, v188, v144, v145\n"
      "v_max3_f32 v188, v188, v146, v147\n"
      "v_max3_f32 v188, v188, v236, v237\n"
      "v_max3_f32 v188, v188, v238, v239\n"
      "v_max3_f32 v188, v188, v232, v233\n"
      "v_mfma_f32_16x16x32_bf16 v[152:155], v[244:247], v[156:159], 0\n"
      "v_max3_f32 v188, v188, v234, v235\n"
      "v_max3_f32 v188, v188, v160, v161\n"
      "v_max3_f32 v188, v188, v162, v163\n"
      "v_mfma_f32_16x16x32_bf16 v[152:155], v[248:251], v[32:35], v[152:155]\n"
      "ds_read_b128 v[124:127], v0 offset:0\n"
      "ds_read_b128 v[48:51], v0 offset:512\n"
      "ds_read_b128 v[116:119], v0 offset:1024\n"
      "ds_read_b128 v[252:255], v0 offset:1536\n"
      "s_add_u32 s56, s48, 8192\n"
      "buffer_store_dwordx2 v[224:225], v2, s[44:47], s56 offen offset:0 nt sc1\n"
      "buffer_store_dwordx2 v[228:229], v2, s[44:47], s56 offen offset:256 nt sc1\n"
      "buffer_store_dwordx2 v[226:227], v2, s[44:47], s56 offen offset:512 nt sc1\n"
      "v_max3_f32 v188, v188, v152, v153\n"
      "v_max3_f32 v188, v188, v154, v155\n"
      "v_mov_b32 v32, v188\n"
      "buffer_store_dwordx2 v[230:231], v2, s[44:47], s56 offen offset:768 nt sc1\n"
      "s_nop 0\n"
      "v_permlane16_swap_b32 v188, v32\n"
      "v_max_f32 v188, v188, v32\n"
      "v_mov_b32 v32, v188\n"
      "s_nop 1\n"
      "v_permlane32_swap_b32 v188, v32\n"
      "v_max_f32 v188, v188, v32\n"
      "v_sub_f32 v140, v140, v188\n"
      "v_sub_f32 v141, v141, v188\n"
      "v_sub_f32 v142, v142, v188\n"
      "s_waitcnt lgkmcnt(3)\n"
      "v_mfma_f32_16x16x32_bf16 v[32:35], a[128:131], v[124:127], 0\n"
      "v_sub_f32 v143, v143, v188\n"
      "v_sub_f32 v88, v88, v188\n"
      "s_waitcnt lgkmcnt(2)\n"
      "v_mfma_f32_16x16x32_bf16 v[32:35], a[132:135], v[48:51], v[32:35]\n"
      "v_exp_f32 v140, v140\n"
      "s_waitcnt lgkmcnt(1)\n"
      "v_mfma_f32_16x16x32_bf16 v[32:35], a[136:139], v[116:119], v[32:35]\n"
      "v_exp_f32 v141, v141\n"
      "s_waitcnt lgkmcnt(0)\n"
      "v_mfma_f32_16x16x32_bf16 v[32:35], a[140:143], v[252:255], v[32:35]\n"
      "v_exp_f32 v142, v142\n"
      "v_mfma_f32_16x16x32_bf16 v[28:31], a[144:147], v[124:127], 0\n"
      "v_exp_f32 v143, v143\n"
      "v_mfma_f32_16x16x32_bf16 v[28:31], a[148:151], v[48:51], v[28:31]\n"
      "v_cvt_pk_bf16_f32 v190, v140, v141\n"
      "v_cvt_pk_bf16_f32 v191, v142, v143\n"
      "v_mfma_f32_16x16x32_bf16 v[28:31], a[152:155], v[116:119], v[28:31]\n"
      "v_sub_f32 v89, v89, v188\n"
      "v_sub_f32 v90, v90, v188\n"
      "v_sub_f32 v91, v91, v188\n"
      "v_sub_f32 v144, v144, v188\n"
      "v_mfma_f32_16x16x32_bf16 v[28:31], a[156:159], v[252:255], v[28:31]\n"
      "v_sub_f32 v145, v145, v188\n"
      "v_sub_f32 v146, v146, v188\n"
      "v_sub_f32 v147, v147, v188\n"
      "v_sub_f32 v236, v236, v188\n"
      "v_sub_f32 v237, v237, v188\n"
      "v_sub_f32 v238, v238, v188\n"
      "v_mfma_f32_16x16x32_bf16 v[184:187], a[160:163], v[124:127], 0\n"
      "v_sub_f32 v239, v239, v188\n"
      "v_sub_f32 v232, v232, v188\n"
      "v_sub_f32 v233, v233, v188\n"
      "v_sub_f32 v234, v234, v188\n"
      "v_sub_f32 v235, v235, v188\n"
      "v_add_f32 v86, v140, v141\n"
      "v_add_f32 v87, v142, v143\n"
      "v_mfma_f32_16x16x32_bf16 v[184:187], a[164:167], v[48:51], v[184:187]\n"
      "v_exp_f32 v88, v88\n"
      "v_exp_f32 v89, v89\n"
      "v_exp_f32 v90, v90\n"
      "v_mfma_f32_16x16x32_bf16 v[184:187], a[168:171], v[116:119], v[184:187]\n"
      "v_exp_f32 v91, v91\n"
      "v_cvt_pk_bf16_f32 v140, v88, v89\n"
      "v_cvt_pk_bf16_f32 v141, v90, v91\n"
      "v_exp_f32 v144, v144\n"
      "v_mfma_f32_16x16x32_bf16 v[184:187], a[172:175], v[252:255], v[184:187]\n"
      "v_exp_f32 v145, v145\n"
      "v_exp_f32 v146, v146\n"
      "v_exp_f32 v147, v147\n"
      "v_cvt_pk_bf16_f32 v142, v144, v145\n"
      "v_mfma_f32_16x16x32_bf16 v[208:211], a[176:179], v[124:127], 0\n"
      "v_cvt_pk_bf16_f32 v143, v146, v147\n"
      "v_exp_f32 v236, v236\n"
      "v_exp_f32 v237, v237\n"
      "v_exp_f32 v238, v238\n"
      "v_mfma_f32_16x16x32_bf16 v[208:211], a[180:183], v[48:51], v[208:211]\n"
      "v_exp_f32 v239, v239\n"
      "v_cvt_pk_bf16_f32 v144, v236, v237\n"
      "v_cvt_pk_bf16_f32 v145, v238, v239\n"
      "v_exp_f32 v232, v232\n"
      "v_mfma_f32_16x16x32_bf16 v[208:211], a[184:187], v[116:119], v[208:211]\n"
      "v_exp_f32 v233, v233\n"
      "v_exp_f32 v234, v234\n"
      "v_exp_f32 v235, v235\n"
      "v_cvt_pk_bf16_f32 v146, v232, v233\n"
      "v_mfma_f32_16x16x32_bf16 v[208:211], a[188:191], v[252:255], v[208:211]\n"
      "ds_read_b128 v[156:159], v10 offset:0\n"
      "v_cvt_pk_bf16_f32 v147, v234, v235\n"
      "v_sub_f32 v160, v160, v188\n"
      "v_sub_f32 v161, v161, v188\n"
      "v_sub_f32 v162, v162, v188\n"
      "v_sub_f32 v163, v163, v188\n"
      "v_sub_f32 v152, v152, v188\n"
      "v_mfma_f32_16x16x4_4b_bf16 v[224:239], v[190:191], v[18:19], 0 cbsz:2 abid:0\n"
      "s_add_u32 m0, s53, 0\n"
      "s_add_u32 s54, s49, 0\n"
      "buffer_load_dwordx4 v1, s[40:43], s54 offen offset:0 lds\n"
      "v_sub_f32 v153, v153, v188\n"
      "v_sub_f32 v154, v154, v188\n"
      "v_sub_f32 v155, v155, v188\n"
      "v_exp_f32 v160, v160\n"
      "v_exp_f32 v161, v161\n"
      "v_exp_f32 v162, v162\n"
      "v_exp_f32 v163, v163\n"
      "v_cvt_pk_bf16_f32 v188, v160, v161\n"
      "v_cvt_pk_bf16_f32 v189, v162, v163\n"
      "v_exp_f32 v152, v152\n"
      "v_exp_f32 v153, v153\n"
      "v_exp_f32 v154, v154\n"
      "v_smfmac_f32_16x16x64_bf16 v[86:89], v[12:15], v[140:147], v5\n"
      "v_exp_f32 v155, v155\n"
      "v_cvt_pk_bf16_f32 v190, v152, v153\n"
      "v_mfma_f32_16x16x32_bf16 v[224:227], v[140:143], v[52:55], v[224:227]\n"
      "buffer_load_dwordx4 v1, s[40:43], s54 offen offset:2048 lds\n"
      "s_add_u32 m0, s53, 896\n"
      "v_cvt_pk_bf16_f32 v191, v154, v155\n"
      "v_cvt_pk_bf16_f32 v152, v32, v33\n"
      "v_cvt_pk_bf16_f32 v153, v34, v35\n"
      "v_cvt_pk_bf16_f32 v154, v28, v29\n"
      "v_mfma_f32_16x16x32_bf16 v[224:227], v[144:147], v[56:59], v[224:227]\n"
      "v_cvt_pk_bf16_f32 v155, v30, v31\n"
      "v_cvt_pk_bf16_f32 v32, v184, v185\n"
      "v_cvt_pk_bf16_f32 v33, v186, v187\n"
      "v_cvt_pk_bf16_f32 v34, v208, v209\n"
      "v_mfma_f32_16x16x32_bf16 v[224:227], v[188:191], v[120:123], v[224:227]\n"
      "buffer_load_dwordx4 v1, s[40:43], s54 offen offset:128 lds\n"
      "v_cvt_pk_bf16_f32 v35, v210, v211\n"
      "v_smfmac_f32_16x16x32_bf16 v[86:89], v[12:13], v[188:191], v5\n"
      "v_mfma_f32_16x16x32_bf16 v[228:231], v[140:143], v[128:131], v[228:231]\n"
      "buffer_load_dwordx4 v1, s[40:43], s54 offen offset:2176 lds\n"
      "v_mfma_f32_16x16x32_bf16 v[228:231], v[144:147], v[132:135], v[228:231]\n"
      "v_mfma_f32_16x16x32_bf16 v[228:231], v[188:191], v[68:71], v[228:231]\n"
      "v_add_u32 v10, 64, v10\n"
      "v_mfma_f32_16x16x32_bf16 v[232:235], v[140:143], v[100:103], v[232:235]\n"
      "v_mfma_f32_16x16x32_bf16 v[232:235], v[144:147], v[104:107], v[232:235]\n"
      "v_mfma_f32_16x16x32_bf16 v[232:235], v[188:191], v[36:39], v[232:235]\n"
      "v_add_f32 v86, v86, v87\n"
      "v_rcp_f32 v87, v86\n"
      "v_mfma_f32_16x16x32_bf16 v[236:239], v[140:143], v[192:195], v[236:239]\n"
      "ds_bpermute_b32 v88, v4, v87\n"
      "v_mfma_f32_16x16x32_bf16 v[236:239], v[144:147], v[196:199], v[236:239]\n"
      "v_mfma_f32_16x16x32_bf16 v[236:239], v[188:191], v[240:243], v[236:239]\n"
      "s_waitcnt lgkmcnt(0)\n"
      "v_mul_f32_dpp v224, v88, v224 row_newbcast:0 row_mask:0xf bank_mask:0xf\n"
      "v_mul_f32_dpp v225, v88, v225 row_newbcast:4 row_mask:0xf bank_mask:0xf\n"
      "v_mul_f32_dpp v226, v88, v226 row_newbcast:8 row_mask:0xf bank_mask:0xf\n"
      "v_mul_f32_dpp v227, v88, v227 row_newbcast:12 row_mask:0xf bank_mask:0xf\n"
      "v_mul_f32_dpp v228, v88, v228 row_newbcast:0 row_mask:0xf bank_mask:0xf\n"
      "v_mul_f32_dpp v229, v88, v229 row_newbcast:4 row_mask:0xf bank_mask:0xf\n"
      "v_mfma_f32_16x16x32_bf16 v[224:227], v[136:139], a[192:195], v[224:227]\n"
      "v_mul_f32_dpp v230, v88, v230 row_newbcast:8 row_mask:0xf bank_mask:0xf\n"
      "v_mul_f32_dpp v231, v88, v231 row_newbcast:12 row_mask:0xf bank_mask:0xf\n"
      "v_mul_f32_dpp v232, v88, v232 row_newbcast:0 row_mask:0xf bank_mask:0xf\n"
      "v_mfma_f32_16x16x32_bf16 v[224:227], v[148:151], a[196:199], v[224:227]\n"
      "v_mul_f32_dpp v233, v88, v233 row_newbcast:4 row_mask:0xf bank_mask:0xf\n"
      "v_mul_f32_dpp v234, v88, v234 row_newbcast:8 row_mask:0xf bank_mask:0xf\n"
      "v_mul_f32_dpp v235, v88, v235 row_newbcast:12 row_mask:0xf bank_mask:0xf\n"
      "v_mul_f32_dpp v236, v88, v236 row_newbcast:0 row_mask:0xf bank_mask:0xf\n"
      "v_mfma_f32_16x16x32_bf16 v[224:227], v[24:27], a[200:203], v[224:227]\n"
      "v_mul_f32_dpp v237, v88, v237 row_newbcast:4 row_mask:0xf bank_mask:0xf\n"
      "v_mul_f32_dpp v238, v88, v238 row_newbcast:8 row_mask:0xf bank_mask:0xf\n"
      "v_mul_f32_dpp v239, v88, v239 row_newbcast:12 row_mask:0xf bank_mask:0xf\n"
      "v_mfma_f32_16x16x32_bf16 v[224:227], v[20:23], a[204:207], v[224:227]\n"
      "v_mfma_f32_16x16x32_bf16 v[228:231], v[136:139], a[208:211], v[228:231]\n"
      "v_mfma_f32_16x16x32_bf16 v[228:231], v[148:151], a[212:215], v[228:231]\n"
      "v_mfma_f32_16x16x32_bf16 v[228:231], v[24:27], a[216:219], v[228:231]\n"
      "v_mfma_f32_16x16x32_bf16 v[228:231], v[20:23], a[220:223], v[228:231]\n"
      "v_mfma_f32_16x16x32_bf16 v[232:235], v[136:139], a[224:227], v[232:235]\n"
      "v_mfma_f32_16x16x32_bf16 v[232:235], v[148:151], a[228:231], v[232:235]\n"
      "v_mfma_f32_16x16x32_bf16 v[232:235], v[24:27], a[232:235], v[232:235]\n"
      "v_mfma_f32_16x16x32_bf16 v[232:235], v[20:23], a[236:239], v[232:235]\n"
      "v_mfma_f32_16x16x32_bf16 v[236:239], v[136:139], a[240:243], v[236:239]\n"
      "v_mfma_f32_16x16x32_bf16 v[236:239], v[148:151], a[244:247], v[236:239]\n"
      "v_mfma_f32_16x16x32_bf16 v[236:239], v[24:27], a[248:251], v[236:239]\n"
      "v_mfma_f32_16x16x32_bf16 v[236:239], v[20:23], a[252:255], v[236:239]\n"
      "v_cvt_pk_bf16_f32 v224, v224, v228\n"
      "v_cvt_pk_bf16_f32 v228, v225, v229\n"
      "v_pk_max_i16 v224, v224, 0\n"
      "v_pk_max_i16 v228, v228, 0\n"
      "v_cvt_pk_bf16_f32 v226, v226, v230\n"
      "v_cvt_pk_bf16_f32 v230, v227, v231\n"
      "v_pk_max_i16 v226, v226, 0\n"
      "v_mfma_f32_16x16x32_bf16 v[148:151], v[172:175], v[152:155], 0\n"
      "v_cvt_pk_bf16_f32 v225, v232, v236\n"
      "v_cvt_pk_bf16_f32 v229, v233, v237\n"
      "v_pk_max_i16 v225, v225, 0\n"
      "v_pk_max_i16 v229, v229, 0\n"
      "v_cvt_pk_bf16_f32 v227, v234, v238\n"
      "v_cvt_pk_bf16_f32 v231, v235, v239\n"
      "v_pk_max_i16 v227, v227, 0\n"
      "v_mfma_f32_16x16x32_bf16 v[148:151], v[176:179], v[32:35], v[148:151]\n"
      "v_pk_max_i16 v230, v230, 0\n"
      "v_pk_max_i16 v231, v231, 0\n"
      "v_mfma_f32_16x16x32_bf16 v[136:139], v[108:111], v[152:155], 0\n"
      "v_mfma_f32_16x16x32_bf16 v[136:139], v[112:115], v[32:35], v[136:139]\n"
      "v_mfma_f32_16x16x32_bf16 v[236:239], v[164:167], v[152:155], 0\n"
      "v_mfma_f32_16x16x32_bf16 v[236:239], v[168:171], v[32:35], v[236:239]\n"
      "v_mfma_f32_16x16x32_bf16 v[160:163], v[40:43], v[152:155], 0\n"
      "v_mfma_f32_16x16x32_bf16 v[160:163], v[44:47], v[32:35], v[160:163]\n"
      "v_max_f32 v208, v148, v149\n"
      "v_max3_f32 v208, v208, v150, v151\n"
      "v_mfma_f32_16x16x32_bf16 v[28:31], v[92:95], v[152:155], 0\n"
      "v_mfma_f32_16x16x32_bf16 v[28:31], v[96:99], v[32:35], v[28:31]\n"
      "v_max3_f32 v208, v208, v136, v137\n"
      "v_max3_f32 v208, v208, v138, v139\n"
      "v_max3_f32 v208, v208, v236, v237\n"
      "v_max3_f32 v208, v208, v238, v239\n"
      "v_max3_f32 v208, v208, v160, v161\n"
      "v_max3_f32 v208, v208, v162, v163\n"
      "v_mfma_f32_16x16x32_bf16 v[24:27], v[216:219], v[152:155], 0\n"
      "v_mfma_f32_16x16x32_bf16 v[24:27], v[220:223], v[32:35], v[24:27]\n"
      "v_max3_f32 v208, v208, v28, v29\n"
      "v_max3_f32 v208, v208, v30, v31\n"
      "v_mfma_f32_16x16x32_bf16 v[20:23], v[244:247], v[152:155], 0\n"
      "v_mfma_f32_16x16x32_bf16 v[20:23], v[248:251], v[32:35], v[20:23]\n"
      "ds_read_b64 v[152:153], v11 offset:4096\n"
      "s_nop 2\n"
      "v_max3_f32 v208, v208, v24, v25\n"
      "v_max3_f32 v208, v208, v26, v27\n"
      "s_nop 1\n"
      "v_max3_f32 v208, v208, v20, v21\n"
      "v_max3_f32 v208, v208, v22, v23\n"
      "v_mov_b32 v32, v208\n"
      "s_nop 1\n"
      "v_permlane16_swap_b32 v208, v32\n"
      "v_max_f32 v208, v208, v32\n"
      "v_mov_b32 v32, v208\n"
      "s_nop 1\n"
      "v_permlane32_swap_b32 v208, v32\n"
      "v_max_f32 v208, v208, v32\n"
      "v_sub_f32 v148, v148, v208\n"
      "v_sub_f32 v149, v149, v208\n"
      "v_sub_f32 v150, v150, v208\n"
      "v_sub_f32 v151, v151, v208\n"
      "v_exp_f32 v148, v148\n"
      "v_exp_f32 v149, v149\n"
      "v_exp_f32 v150, v150\n"
      "v_exp_f32 v151, v151\n"
      "v_cvt_pk_bf16_f32 v234, v148, v149\n"
      "v_cvt_pk_bf16_f32 v235, v150, v151\n"
      "v_sub_f32 v136, v136, v208\n"
      "v_sub_f32 v137, v137, v208\n"
      "v_sub_f32 v138, v138, v208\n"
      "v_sub_f32 v139, v139, v208\n"
      "v_sub_f32 v236, v236, v208\n"
      "v_sub_f32 v237, v237, v208\n"
      "v_sub_f32 v238, v238, v208\n"
      "v_sub_f32 v239, v239, v208\n"
      "v_sub_f32 v160, v160, v208\n"
      "v_sub_f32 v161, v161, v208\n"
      "v_sub_f32 v162, v162, v208\n"
      "v_sub_f32 v163, v163, v208\n"
      "v_sub_f32 v28, v28, v208\n"
      "v_sub_f32 v29, v29, v208\n"
      "v_sub_f32 v30, v30, v208\n"
      "v_sub_f32 v31, v31, v208\n"
      "v_add_f32 v32, v148, v149\n"
      "v_add_f32 v33, v150, v151\n"
      "v_exp_f32 v136, v136\n"
      "v_exp_f32 v137, v137\n"
      "v_exp_f32 v138, v138\n"
      "v_exp_f32 v139, v139\n"
      "v_cvt_pk_bf16_f32 v232, v136, v137\n"
      "v_cvt_pk_bf16_f32 v233, v138, v139\n"
      "v_exp_f32 v236, v236\n"
      "v_mfma_f32_16x16x4_4b_bf16 v[136:151], v[234:235], v[18:19], 0 cbsz:2 abid:0\n"
      "v_exp_f32 v237, v237\n"
      "v_exp_f32 v238, v238\n"
      "v_exp_f32 v239, v239\n"
      "v_cvt_pk_bf16_f32 v234, v236, v237\n"
      "v_cvt_pk_bf16_f32 v235, v238, v239\n"
      "v_exp_f32 v160, v160\n"
      "v_exp_f32 v161, v161\n"
      "v_exp_f32 v162, v162\n"
      "v_exp_f32 v163, v163\n"
      "v_cvt_pk_bf16_f32 v236, v160, v161\n"
      "v_cvt_pk_bf16_f32 v237, v162, v163\n"
      "v_exp_f32 v28, v28\n"
      "v_exp_f32 v29, v29\n"
      "v_exp_f32 v30, v30\n"
      "v_exp_f32 v31, v31\n"
      "v_cvt_pk_bf16_f32 v238, v28, v29\n"
      "v_cvt_pk_bf16_f32 v239, v30, v31\n"
      "v_sub_f32 v24, v24, v208\n"
      "v_sub_f32 v25, v25, v208\n"
      "v_sub_f32 v26, v26, v208\n"
      "v_sub_f32 v27, v27, v208\n"
      "v_sub_f32 v20, v20, v208\n"
      "v_sub_f32 v21, v21, v208\n"
      "v_sub_f32 v22, v22, v208\n"
      "v_sub_f32 v23, v23, v208\n"
      "v_exp_f32 v24, v24\n"
      "v_exp_f32 v25, v25\n"
      "v_exp_f32 v26, v26\n"
      "v_exp_f32 v27, v27\n"
      "v_cvt_pk_bf16_f32 v208, v24, v25\n"
      "v_cvt_pk_bf16_f32 v209, v26, v27\n"
      "v_exp_f32 v20, v20\n"
      "v_exp_f32 v21, v21\n"
      "v_exp_f32 v22, v22\n"
      "v_exp_f32 v23, v23\n"
      "v_cvt_pk_bf16_f32 v210, v20, v21\n"
      "v_cvt_pk_bf16_f32 v211, v22, v23\n"
      "v_smfmac_f32_16x16x64_bf16 v[32:35], v[12:15], v[232:239], v5\n"
      "s_add_u32 s56, s48, 4096\n"
      "buffer_store_dwordx2 v[224:225], v2, s[44:47], s56 offen offset:0 nt sc1\n"
      "v_mfma_f32_16x16x32_bf16 v[136:139], v[232:235], v[52:55], v[136:139]\n"
      "buffer_store_dwordx2 v[228:229], v2, s[44:47], s56 offen offset:256 nt sc1\n"
      "v_mfma_f32_16x16x32_bf16 v[136:139], v[236:239], v[56:59], v[136:139]\n"
      "buffer_store_dwordx2 v[226:227], v2, s[44:47], s56 offen offset:512 nt sc1\n"
      "v_mfma_f32_16x16x32_bf16 v[136:139], v[208:211], v[120:123], v[136:139]\n"
      "buffer_store_dwordx2 v[230:231], v2, s[44:47], s56 offen offset:768 nt sc1\n"
      "v_smfmac_f32_16x16x32_bf16 v[32:35], v[12:13], v[208:211], v5\n"
      "s_waitcnt lgkmcnt(0)\n"
      "v_lshlrev_b32 v228, 16, v152\n"
      "v_and_b32 v229, 0xffff0000, v152\n"
      "v_mfma_f32_16x16x32_bf16 v[140:143], v[232:235], v[128:131], v[140:143]\n"
      "v_mov_b64 v[224:225], 0\n"
      "v_lshlrev_b32 v230, 16, v153\n"
      "v_and_b32 v231, 0xffff0000, v153\n"
      "v_mfma_f32_16x16x32_bf16 v[140:143], v[236:239], v[132:135], v[140:143]\n"
      "v_mov_b64 v[20:21], 0\n"
      "v_add_f32 v32, v32, v33\n"
      "v_rcp_f32 v33, v32\n"
      "v_mfma_f32_16x16x32_bf16 v[140:143], v[208:211], v[68:71], v[140:143]\n"
      "ds_bpermute_b32 v34, v4, v33\n"
      "v_mov_b64 v[28:29], 0\n"
      "v_mfma_f32_16x16x32_bf16 v[144:147], v[232:235], v[100:103], v[144:147]\n"
      "v_mov_b64 v[160:161], 0\n"
      "v_mfma_f32_16x16x32_bf16 v[144:147], v[236:239], v[104:107], v[144:147]\n"
      "v_mov_b64 v[24:25], 0\n"
      "v_mfma_f32_16x16x32_bf16 v[144:147], v[208:211], v[36:39], v[144:147]\n"
      "v_mov_b64 v[32:33], 0\n"
      "v_mfma_f32_16x16x32_bf16 v[148:151], v[232:235], v[192:195], v[148:151]\n"
      "v_mov_b64 v[232:233], 0\n"
      "v_mfma_f32_16x16x32_bf16 v[148:151], v[236:239], v[196:199], v[148:151]\n"
      "v_mfma_f32_16x16x32_bf16 v[148:151], v[208:211], v[240:243], v[148:151]\n"
      "s_waitcnt lgkmcnt(0)\n"
      "v_mul_f32_dpp v136, v34, v136 row_newbcast:0 row_mask:0xf bank_mask:0xf\n"
      "v_mul_f32_dpp v137, v34, v137 row_newbcast:4 row_mask:0xf bank_mask:0xf\n"
      "v_mul_f32_dpp v138, v34, v138 row_newbcast:8 row_mask:0xf bank_mask:0xf\n"
      "v_mul_f32_dpp v139, v34, v139 row_newbcast:12 row_mask:0xf bank_mask:0xf\n"
      "v_mul_f32_dpp v140, v34, v140 row_newbcast:0 row_mask:0xf bank_mask:0xf\n"
      "v_mul_f32_dpp v141, v34, v141 row_newbcast:4 row_mask:0xf bank_mask:0xf\n"
      "v_mfma_f32_16x16x32_bf16 v[136:139], v[124:127], a[192:195], v[136:139]\n"
      "v_mul_f32_dpp v142, v34, v142 row_newbcast:8 row_mask:0xf bank_mask:0xf\n"
      "v_mul_f32_dpp v143, v34, v143 row_newbcast:12 row_mask:0xf bank_mask:0xf\n"
      "v_mul_f32_dpp v144, v34, v144 row_newbcast:0 row_mask:0xf bank_mask:0xf\n"
      "v_mfma_f32_16x16x32_bf16 v[136:139], v[48:51], a[196:199], v[136:139]\n"
      "v_mul_f32_dpp v145, v34, v145 row_newbcast:4 row_mask:0xf bank_mask:0xf\n"
      "v_mul_f32_dpp v146, v34, v146 row_newbcast:8 row_mask:0xf bank_mask:0xf\n"
      "v_mul_f32_dpp v147, v34, v147 row_newbcast:12 row_mask:0xf bank_mask:0xf\n"
      "v_mul_f32_dpp v148, v34, v148 row_newbcast:0 row_mask:0xf bank_mask:0xf\n"
      "v_mfma_f32_16x16x32_bf16 v[136:139], v[116:119], a[200:203], v[136:139]\n"
      "v_mul_f32_dpp v149, v34, v149 row_newbcast:4 row_mask:0xf bank_mask:0xf\n"
      "v_mul_f32_dpp v150, v34, v150 row_newbcast:8 row_mask:0xf bank_mask:0xf\n"
      "v_mul_f32_dpp v151, v34, v151 row_newbcast:12 row_mask:0xf bank_mask:0xf\n"
      "v_add_u32 v11, 0x200, v11\n"
      "v_mfma_f32_16x16x32_bf16 v[136:139], v[252:255], a[204:207], v[136:139]\n"
      "v_mfma_f32_16x16x32_bf16 v[140:143], v[124:127], a[208:211], v[140:143]\n"
      "v_mfma_f32_16x16x32_bf16 v[140:143], v[48:51], a[212:215], v[140:143]\n"
      "v_mfma_f32_16x16x32_bf16 v[140:143], v[116:119], a[216:219], v[140:143]\n"
      "v_mfma_f32_16x16x32_bf16 v[140:143], v[252:255], a[220:223], v[140:143]\n"
      "s_add_u32 s56, s48, 0\n"
      "v_mfma_f32_16x16x32_bf16 v[144:147], v[124:127], a[224:227], v[144:147]\n"
      "v_mfma_f32_16x16x32_bf16 v[144:147], v[48:51], a[228:231], v[144:147]\n"
      "v_mfma_f32_16x16x32_bf16 v[144:147], v[116:119], a[232:235], v[144:147]\n"
      "v_mfma_f32_16x16x32_bf16 v[144:147], v[252:255], a[236:239], v[144:147]\n"
      "v_mfma_f32_16x16x32_bf16 v[148:151], v[124:127], a[240:243], v[148:151]\n"
      "v_mfma_f32_16x16x32_bf16 v[148:151], v[48:51], a[244:247], v[148:151]\n"
      "v_mfma_f32_16x16x32_bf16 v[148:151], v[116:119], a[248:251], v[148:151]\n"
      "v_cvt_pk_bf16_f32 v136, v136, v140\n"
      "v_cvt_pk_bf16_f32 v140, v137, v141\n"
      "v_pk_max_i16 v136, v136, 0\n"
      "v_pk_max_i16 v140, v140, 0\n"
      "v_cvt_pk_bf16_f32 v138, v138, v142\n"
      "v_cvt_pk_bf16_f32 v142, v139, v143\n"
      "v_pk_max_i16 v138, v138, 0\n"
      "v_mfma_f32_16x16x32_bf16 v[148:151], v[252:255], a[252:255], v[148:151]\n"
      "v_pk_max_i16 v142, v142, 0\n"
      "v_smfmac_f32_16x16x64_bf16 v[224:227], v[156:159], v[108:115], v5\n"
      "v_smfmac_f32_16x16x64_bf16 v[20:23], v[156:159], v[164:171], v5\n"
      "v_smfmac_f32_16x16x64_bf16 v[28:31], v[156:159], v[40:47], v5\n"
      "v_smfmac_f32_16x16x64_bf16 v[160:163], v[156:159], v[92:99], v5\n"
      "v_smfmac_f32_16x16x64_bf16 v[24:27], v[156:159], v[216:223], v5\n"
      "v_smfmac_f32_16x16x64_bf16 v[32:35], v[156:159], v[244:251], v5\n"
      "v_smfmac_f32_16x16x64_bf16 v[232:235], v[156:159], v[172:179], v5\n"
      "v_cvt_pk_bf16_f32 v137, v144, v148\n"
      "v_cvt_pk_bf16_f32 v141, v145, v149\n"
      "v_add_f32 v224, v224, v225\n"
      "v_add_f32 v20, v20, v21\n"
      "v_add_f32 v28, v28, v29\n"
      "v_add_f32 v160, v160, v161\n"
      "v_add_f32 v24, v24, v25\n"
      "v_add_f32 v32, v32, v33\n"
      "v_add_f32 v232, v232, v233\n"
      "v_max3_f32 v148, v224, v20, v28\n"
      "v_max3_f32 v148, v148, v160, v24\n"
      "v_max3_f32 v148, v148, v32, v232\n"
      "v_mov_b32 v233, 0\n"
      "v_pk_max_i16 v137, v137, 0\n"
      "v_max_f32_dpp v148, v148, v148 row_ror:8 row_mask:0xf bank_mask:0xf\n"
      "v_pk_max_i16 v141, v141, 0\n"
      "v_cvt_pk_bf16_f32 v139, v146, v150\n"
      "v_max_f32_dpp v148, v148, v148 row_ror:4 row_mask:0xf bank_mask:0xf\n"
      "v_cvt_pk_bf16_f32 v143, v147, v151\n"
      "v_pk_max_i16 v139, v139, 0\n"
      "v_max_f32_dpp v148, v148, v148 row_ror:2 row_mask:0xf bank_mask:0xf\n"
      "v_pk_max_i16 v143, v143, 0\n"
      "buffer_store_dwordx2 v[136:137], v2, s[44:47], s56 offen offset:0 nt sc1\n"
      "v_max_f32_dpp v148, v148, v148 row_ror:1 row_mask:0xf bank_mask:0xf\n"
      "v_sub_f32 v232, v232, v148\n"
      "v_sub_f32 v224, v224, v148\n"
      "v_sub_f32 v20, v20, v148\n"
      "v_exp_f32_dpp v233, v232 quad_perm:[0,1,2,3] row_mask:0xf bank_mask:0x1\n"
      "v_sub_f32 v28, v28, v148\n"
      "v_sub_f32 v160, v160, v148\n"
      "v_mov_b32_dpp v232, v233 quad_perm:[1,1,3,3] row_mask:0xf bank_mask:0xf\n"
      "v_cvt_pk_bf16_f32 v232, v233, v232\n"
      "ds_bpermute_b32 v136, v7, v232 offset:248\n"
      "ds_bpermute_b32 v137, v7, v232 offset:8\n"
      "v_sub_f32 v24, v24, v148\n"
      "v_sub_f32 v32, v32, v148\n"
      "v_exp_f32 v224, v224\n"
      "v_exp_f32 v20, v20\n"
      "v_exp_f32 v28, v28\n"
      "v_exp_f32 v160, v160\n"
      "v_exp_f32 v24, v24\n"
      "v_exp_f32 v32, v32\n"
      "v_cndmask_b32_dpp v225, v28, v224, vcc quad_perm:[0,0,2,2] row_mask:0xf bank_mask:0xf\n"
      "v_cndmask_b32_dpp v21, v160, v20, vcc quad_perm:[0,0,2,2] row_mask:0xf bank_mask:0xf\n"
      "v_cndmask_b32_dpp v25, v32, v24, vcc quad_perm:[0,0,2,2] row_mask:0xf bank_mask:0xf\n"
      "s_not_b64 vcc, vcc\n"
      "v_cndmask_b32_dpp v29, v224, v28, vcc quad_perm:[1,1,3,3] row_mask:0xf bank_mask:0xf\n"
      "v_cndmask_b32_dpp v161, v20, v160, vcc quad_perm:[1,1,3,3] row_mask:0xf bank_mask:0xf\n"
      "v_cvt_pk_bf16_f32 v225, v225, v29\n"
      "v_cvt_pk_bf16_f32 v21, v21, v161\n"
      "ds_bpermute_b32 v148, v6, v225\n"
      "ds_bpermute_b32 v149, v6, v21\n"
      "ds_bpermute_b32 v150, v6, v225 offset:16\n"
      "ds_bpermute_b32 v151, v6, v21 offset:16\n"
      "v_cndmask_b32_dpp v33, v24, v32, vcc quad_perm:[1,1,3,3] row_mask:0xf bank_mask:0xf\n"
      "v_cvt_pk_bf16_f32 v25, v25, v33\n"
      "ds_bpermute_b32 v146, v7, v25\n"
      "ds_bpermute_b32 v147, v7, v25 offset:4\n"
      "buffer_store_dwordx2 v[140:141], v2, s[44:47], s56 offen offset:256 nt sc1\n"
      "buffer_store_dwordx2 v[138:139], v2, s[44:47], s56 offen offset:512 nt sc1\n"
      "s_waitcnt lgkmcnt(6)\n"
      "v_mfma_f32_16x16x4_4b_bf16 v[164:179], v[136:137], v[18:19], 0 cbsz:2 abid:0\n"
      "buffer_store_dwordx2 v[142:143], v2, s[44:47], s56 offen offset:768 nt sc1\n"
      "s_not_b64 vcc, vcc\n"
      "v_add_f32 v224, v224, v20\n"
      "v_add_f32 v28, v28, v160\n"
      "v_add_f32 v24, v24, v32\n"
      "v_add_f32 v224, v224, v28\n"
      "v_add_f32 v24, v24, v233\n"
      "v_add_f32 v142, v224, v24\n"
      "s_add_u32 s56, s48, 24576\n"
      "s_nop 0\n"
      "v_add_f32_dpp v142, v142, v142 row_ror:8 row_mask:0xf bank_mask:0xf\n"
      "s_nop 1\n"
      "v_add_f32_dpp v142, v142, v142 row_ror:4 row_mask:0xf bank_mask:0xf\n"
      "s_nop 1\n"
      "v_add_f32_dpp v142, v142, v142 row_ror:2 row_mask:0xf bank_mask:0xf\n"
      "s_waitcnt lgkmcnt(2)\n"
      "v_smfmac_f32_16x16x64_bf16 v[164:167], v[148:151], v[52:59], v5\n"
      "v_smfmac_f32_16x16x64_bf16 v[168:171], v[148:151], v[128:135], v5\n"
      "v_add_f32_dpp v142, v142, v142 row_ror:1 row_mask:0xf bank_mask:0xf\n"
      "v_smfmac_f32_16x16x64_bf16 v[172:175], v[148:151], v[100:107], v5\n"
      "v_smfmac_f32_16x16x64_bf16 v[176:179], v[148:151], v[192:199], v5\n"
      "v_rcp_f32 v143, v142\n"
      "s_waitcnt lgkmcnt(0)\n"
      "v_smfmac_f32_16x16x32_bf16 v[164:167], v[146:147], v[120:123], v5\n"
      "v_smfmac_f32_16x16x32_bf16 v[168:171], v[146:147], v[68:71], v5\n"
      "s_nop 0\n"
      "v_smfmac_f32_16x16x32_bf16 v[172:175], v[146:147], v[36:39], v5\n"
      "v_smfmac_f32_16x16x32_bf16 v[176:179], v[146:147], v[240:243], v5\n"
      "s_nop 3\n"
      "v_add_f32 v164, v164, v165\n"
      "v_fma_f32 v164, v164, v143, v228\n"
      "v_add_f32 v168, v168, v169\n"
      "v_fma_f32 v168, v168, v143, v229\n"
      "v_cvt_pk_bf16_f32 v164, v164, v168\n"
      "v_pk_max_i16 v164, v164, 0\n"
      "v_add_f32 v172, v172, v173\n"
      "v_fma_f32 v172, v172, v143, v230\n"
      "v_add_f32 v176, v176, v177\n"
      "v_fma_f32 v176, v176, v143, v231\n"
      "v_cvt_pk_bf16_f32 v165, v172, v176\n"
      "v_pk_max_i16 v165, v165, 0\n"
      "buffer_store_dwordx2 v[164:165], v3, s[44:47], s56 offen offset:0 nt sc1\n"
      "s_mov_b32 s48, s49\n"
      "s_add_u32 s49, s49, 13107200\n"
      "s_sub_u32 s50, s50, 1\n"
      "s_cmp_eq_u32 s50, 1\n"
      "s_cselect_b32 s49, 0x80000000, s49\n"
      "s_cmp_lg_u32 s50, 0\n"
      "s_cbranch_scc1 L_safe_%=\n"
      "s_waitcnt vmcnt(0) lgkmcnt(0)\n"
      "L_done_%=:\n"
      "s_endpgm\n"
      ".p2align 12\n"
      :
      : [ka] "s"(__builtin_amdgcn_kernarg_segment_ptr()), [g] "s"(blockIdx.x), [wave] "s"(wave), [ldsb] "s"(ldsb)
      : "v0", "v1", "v2", "v3", "v4", "v5", "v6", "v7", "v8", "v9", "v10", "v11", "v12", "v13", "v14", "v15", "v16", "v17", "v18", "v19", "v20", "v21", "v22", "v23", "v24", "v25", "v26", "v27", "v28", "v29", "v30", "v31", "v32", "v33", "v34", "v35", "v36", "v37", "v38", "v39", "v40", "v41", "v42", "v43", "v44", "v45", "v46", "v47", "v48", "v49", "v50", "v51", "v52", "v53", "v54", "v55", "v56", "v57", "v58", "v59", "v60", "v61", "v62", "v63", "v64", "v65", "v66", "v67", "v68", "v69", "v70", "v71", "v72", "v73", "v74", "v75", "v76", "v77", "v78", "v79", "v80", "v81", "v82", "v83", "v84", "v85", "v86", "v87", "v88", "v89", "v90", "v91", "v92", "v93", "v94", "v95", "v96", "v97", "v98", "v99", "v100", "v101", "v102", "v103", "v104", "v105", "v106", "v107", "v108", "v109", "v110", "v111", "v112", "v113", "v114", "v115", "v116", "v117", "v118", "v119", "v120", "v121", "v122", "v123", "v124", "v125", "v126", "v127", "v128", "v129", "v130", "v131", "v132", "v133", "v134", "v135", "v136", "v137", "v138", "v139", "v140", "v141", "v142", "v143", "v144", "v145", "v146", "v147", "v148", "v149", "v150", "v151", "v152", "v153", "v154", "v155", "v156", "v157", "v158", "v159", "v160", "v161", "v162", "v163", "v164", "v165", "v166", "v167", "v168", "v169", "v170", "v171", "v172", "v173", "v174", "v175", "v176", "v177", "v178", "v179", "v180", "v181", "v182", "v183", "v184", "v185", "v186", "v187", "v188", "v189", "v190", "v191", "v192", "v193", "v194", "v195", "v196", "v197", "v198", "v199", "v200", "v201", "v202", "v203", "v204", "v205", "v206", "v207", "v208", "v209", "v210", "v211", "v212", "v213", "v214", "v215", "v216", "v217", "v218", "v219", "v220", "v221", "v222", "v223", "v224", "v225", "v226", "v227", "v228", "v229", "v230", "v231", "v232", "v233", "v234", "v235", "v236", "v237", "v238", "v239", "v240", "v241", "v242", "v243", "v244", "v245", "v246", "v247", "v248", "v249", "v250", "v251", "v252", "v253", "v254", "v255", "a0", "a1", "a2", "a3", "a4", "a5", "a6", "a7", "a8", "a9", "a10", "a11", "a12", "a13", "a14", "a15", "a16", "a17", "a18", "a19", "a20", "a21", "a22", "a23", "a24", "a25", "a26", "a27", "a28", "a29", "a30", "a31", "a32", "a33", "a34", "a35", "a36", "a37", "a38", "a39", "a40", "a41", "a42", "a43", "a44", "a45", "a46", "a47", "a48", "a49", "a50", "a51", "a52", "a53", "a54", "a55", "a56", "a57", "a58", "a59", "a60", "a61", "a62", "a63", "a64", "a65", "a66", "a67", "a68", "a69", "a70", "a71", "a72", "a73", "a74", "a75", "a76", "a77", "a78", "a79", "a80", "a81", "a82", "a83", "a84", "a85", "a86", "a87", "a88", "a89", "a90", "a91", "a92", "a93", "a94", "a95", "a96", "a97", "a98", "a99", "a100", "a101", "a102", "a103", "a104", "a105", "a106", "a107", "a108", "a109", "a110", "a111", "a112", "a113", "a114", "a115", "a116", "a117", "a118", "a119", "a120", "a121", "a122", "a123", "a124", "a125", "a126", "a127", "a128", "a129", "a130", "a131", "a132", "a133", "a134", "a135", "a136", "a137", "a138", "a139", "a140", "a141", "a142", "a143", "a144", "a145", "a146", "a147", "a148", "a149", "a150", "a151", "a152", "a153", "a154", "a155", "a156", "a157", "a158", "a159", "a160", "a161", "a162", "a163", "a164", "a165", "a166", "a167", "a168", "a169", "a170", "a171", "a172", "a173", "a174", "a175", "a176", "a177", "a178", "a179", "a180", "a181", "a182", "a183", "a184", "a185", "a186", "a187", "a188", "a189", "a190", "a191", "a192", "a193", "a194", "a195", "a196", "a197", "a198", "a199", "a200", "a201", "a202", "a203", "a204", "a205", "a206", "a207", "a208", "a209", "a210", "a211", "a212", "a213", "a214", "a215", "a216", "a217", "a218", "a219", "a220", "a221", "a222", "a223", "a224", "a225", "a226", "a227", "a228", "a229", "a230", "a231", "a232", "a233", "a234", "a235", "a236", "a237", "a238", "a239", "a240", "a241", "a242", "a243", "a244", "a245", "a246", "a247", "a248", "a249", "a250", "a251", "a252", "a253", "a254", "a255", "s4", "s5", "s6", "s7", "s8", "s9", "s10", "s11", "s12", "s13", "s14", "s15", "s16", "s17", "s18", "s19", "s20", "s21", "s22", "s23", "s24", "s25", "s26", "s27", "s28", "s29", "s30", "s31", "s32", "s33", "s38", "s39", "s40", "s41", "s42", "s43", "s44", "s45", "s46", "s47", "s48", "s49", "s50", "s51", "s52", "s53", "s54", "s55", "s56", "s57", "s73", "s74", "s75", "s76", "s77", "s78", "s79", "s90", "s91", "s92", "s93", "s94", "s95", "s100", "s101", "s34", "s35", "vcc", "scc", "m0", "memory");
}

extern "C" void kernel_launch(void* const* d_in, const int* in_sizes, int n_in,
                              void* d_out, int out_size, void* d_ws, size_t ws_size,
                              hipStream_t stream) {
  const uint16_t* e = (const uint16_t*)d_in[0];
  const uint16_t* wq = (const uint16_t*)d_in[1];
  const uint16_t* wk = (const uint16_t*)d_in[2];
  const uint16_t* wv = (const uint16_t*)d_in[3];
  const uint16_t* wres = (const uint16_t*)d_in[4];
  uint16_t* out = (uint16_t*)d_out;
  autoint_kernel<<<256, 256, 0, stream>>>(e, wq, wk, wv, wres, out, (uint8_t*)d_ws);
}
```

```cpp
#include <hip/hip_runtime.h>
#include <stdint.h>

__global__ __launch_bounds__(256, 1)
void autoint_kernel(const uint16_t* __restrict__ e, const uint16_t* __restrict__ wq,
                    const uint16_t* __restrict__ wk, const uint16_t* __restrict__ wv,
                    const uint16_t* __restrict__ wres, uint16_t* out, uint8_t* ws) {
  __shared__ __attribute__((aligned(16))) uint8_t lds[163840];
  const unsigned wave = __builtin_amdgcn_readfirstlane(threadIdx.x >> 6);
  const unsigned ldsb = __builtin_amdgcn_readfirstlane((unsigned)(size_t)lds);
  asm volatile(
      "s_load_dwordx8 s[4:11], %[ka], 0x0\n"
      "s_load_dwordx4 s[12:15], %[ka], 0x20\n"
      "s_load_dwordx2 s[16:17], %[ka], 0x30\n"
      "s_lshl_b32 s22, %[wave], 3\n"
      "s_add_u32 s22, s22, 8\n"
      "s_load_dwordx2 s[20:21], %[ka], s22\n"
      "v_mbcnt_lo_u32_b32 v240, -1, 0\n"
      "v_mbcnt_hi_u32_b32 v240, -1, v240\n"
      "s_getpc_b64 s[50:51]\n"
      "L_ai_pc:\n"
      "s_bfe_u32 s54, %[g], 0x20003\n"
      "s_lshl2_add_u32 s54, s54, %[wave]\n"
      "s_lshl_b32 s54, s54, 13\n"
      "v_lshlrev_b32 v250, 7, v240\n"
      "v_add_u32 v250, s54, v250\n"
      "v_min_u32 v250, L_ai_end-L_ai_pc, v250\n"
      "global_load_dword v17, v250, s[50:51]\n"
      "s_bfe_u32 s19, %[g], 0x10003\n"
      "s_and_b32 s18, %[g], 7\n"
      "s_lshr_b32 s54, %[g], 4\n"
      "s_lshl4_add_u32 s18, s18, s54\n"
      "s_lshl_b32 s18, s18, 2\n"
      "s_add_u32 s18, s18, %[wave]\n"
      "s_mul_i32 s18, s18, 25600\n"
      "s_mov_b32 s46, 32768\n"
      "s_mov_b32 s47, 0x20000\n"
      "v_lshrrev_b32 v234, 3, v240\n"
      "v_and_b32 v235, 7, v240\n"
      "v_lshlrev_b32 v233, 4, v235\n"
      "v_lshl_add_u32 v246, v234, 9, v233\n"
      "v_lshl_add_u32 v236, v234, 10, v233\n"
      "s_lshl_b32 s52, %[wave], 12\n"
      "s_lshl_b32 s55, s19, 14\n"
      "s_add_u32 s52, s52, s55\n"
      "s_lshl_b32 s57, %[wave], 13\n"
      "s_lshl_b32 s55, s19, 7\n"
      "s_add_u32 s57, s57, s55\n"
      "v_lshlrev_b32 v230, 7, v235\n"
      "s_lshr_b32 s23, %[g], 3\n"
      "s_lshl_b32 s23, s23, 10\n"
      "s_sub_u32 s49, s18, 13108224\n"
      "s_ashr_i32 s50, s49, 31\n"
      "s_mov_b32 s42, 104884224\n"
      "s_mov_b32 s43, 0x20000\n"
      "v_and_b32 v241, 15, v240\n"
      "v_lshrrev_b32 v242, 4, v240\n"
      "v_lshrrev_b32 v243, 2, v241\n"
      "v_and_b32 v244, 3, v240\n"
      "s_mul_i32 s53, %[wave], 24576\n"
      "s_add_u32 s53, s53, %[ldsb]\n"
      "s_add_u32 s53, s53, 65536\n"
      "v_and_b32 v248, 2, v234\n"
      "v_xor_b32 v248, v248, v244\n"
      "v_lshlrev_b32 v248, 4, v248\n"
      "v_lshrrev_b32 v249, 5, v240\n"
      "v_lshl_add_u32 v249, v249, 6, v248\n"
      "v_bfe_u32 v248, v240, 2, 3\n"
      "v_lshl_add_u32 v1, v248, 8, v249\n"
      "v_and_b32 v248, 1, v242\n"
      "v_mul_u32_u24 v247, 13106176, v248\n"
      "v_add_u32 v247, v247, v1\n"
      "s_mov_b32 s73, 13128704\n"
      "s_mov_b32 s74, 13127808\n"
      "s_mov_b32 s75, 13124608\n"
      "s_mov_b32 s76, 13123712\n"
      "s_mov_b32 s77, 13120512\n"
      "s_mov_b32 s78, 13119616\n"
      "s_mov_b32 s79, 13132800\n"
      "s_mov_b32 s90, 13116416\n"
      "s_mov_b32 s91, 13115520\n"
      "s_mov_b32 s92, 13112320\n"
      "s_mov_b32 s93, 13111424\n"
      "s_mov_b32 s94, 13108224\n"
      "s_mov_b32 s95, 13107328\n"
      "s_mov_b32 s100, 13312\n"
      "s_mov_b32 s101, 9216\n"
      "s_mov_b32 s34, 5120\n"
      "s_mov_b32 s35, 1024\n"
      "s_mov_b32 s51, 0x3e38aa3b\n"
      "s_lshl_b32 s54, %[wave], 10\n"
      "s_add_u32 s54, s54, %[ldsb]\n"
      "v_lshlrev_b32 v255, 8, v242\n"
      "s_waitcnt lgkmcnt(0)\n"
      "s_mov_b32 s44, s20\n"
      "s_and_b32 s45, s21, 0xffff\n"
      "buffer_load_dword v17, v230, s[44:47], s23 offen\n"
      "s_mov_b32 s44, s8\n s_and_b32 s45, s9, 0xffff\n"
      "buffer_load_dwordx4 v[64:67], v246, s[44:47], s52 offen offset:0\n"
      "s_lshr_b32 s33, %[g], 3\n"
      "s_lshl2_add_u32 s33, s33, %[wave]\n"
      "s_bitcmp1_b32 s33, 6\n"
      "s_cselect_b64 s[24:25], s[14:15], s[4:5]\n"
      "s_and_b32 s33, s33, 63\n"
      "s_lshl_b32 s33, s33, 21\n"
      "s_min_u32 s33, s33, 104857596\n"
      "s_load_dword s32, s[24:25], s33\n"
      "s_add_u32 s40, s4, s49\n"
      "s_addc_u32 s41, s5, s50\n"
      "s_and_b32 s41, s41, 0xffff\n"
      "buffer_load_dwordx4 v[68:71], v246, s[44:47], s52 offen offset:128\n"
      "buffer_load_dwordx4 v[72:75], v246, s[44:47], s52 offen offset:256\n"
      "buffer_load_dwordx4 v[76:79], v246, s[44:47], s52 offen offset:384\n"
      "s_mov_b32 s44, s10\n s_and_b32 s45, s11, 0xffff\n"
      "buffer_load_dwordx4 v[80:83], v246, s[44:47], s52 offen offset:0\n"
      "buffer_load_dwordx4 v[84:87], v246, s[44:47], s52 offen offset:128\n"
      "buffer_load_dwordx4 v[88:91], v246, s[44:47], s52 offen offset:256\n"
      "buffer_load_dwordx4 v[92:95], v246, s[44:47], s52 offen offset:384\n"
      "s_add_u32 m0, s53, 0\n s_mov_b32 s30, 13132800\n"
      "s_nop 0\n"
      "buffer_load_dwordx4 v247, s[40:43], s30 offen offset:0 lds\n"
      "v_lshl_add_u32 v255, v235, 5, v255\n"
      "v_lshrrev_b32 v248, 2, v235\n"
      "v_lshl_add_u32 v255, v248, 4, v255\n"
      "v_and_b32 v248, 1, v234\n"
      "v_lshl_add_u32 v255, v248, 3, v255\n"
      "v_add_u32 v255, s54, v255\n"
      "v_xor_b32 v232, 16, v255\n"
      "v_mov_b32 v238, 0x05040100\n"
      "v_mov_b32 v239, 0x07060302\n"
      "v_xor_b32 v254, v248, v240\n"
      "s_mov_b32 s30, 39345152\n"
      "s_nop 0\n"
      "buffer_load_dwordx4 v247, s[40:43], s30 offen offset:2048 lds\n"
      "v_lshlrev_b32 v254, 4, v254\n"
      "v_add_u32 v254, %[ldsb], v254\n"
      "v_lshlrev_b32 v245, 3, v241\n"
      "v_lshrrev_b32 v248, 3, v241\n"
      "v_lshlrev_b32 v248, 11, v248\n"
      "v_lshl_add_u32 v248, v235, 6, v248\n"
      "v_lshrrev_b32 v249, 1, v241\n"
      "v_and_b32 v249, 2, v249\n"
      "v_xor_b32 v249, v249, v242\n"
      "v_lshl_add_u32 v248, v249, 4, v248\n"
      "s_mov_b32 s30, 13131904\n"
      "s_nop 0\n"
      "buffer_load_dwordx4 v247, s[40:43], s30 offen offset:1024 lds\n"
      "v_add_u32 v0, s53, v248\n"
      "s_lshl_b32 s54, s19, 7\n"
      "v_add_u32 v248, s54, v245\n"
      "v_lshl_add_u32 v2, v242, 10, v248\n"
      "v_lshl_add_u32 v3, v242, 8, v248\n"
      "v_and_b32 v4, 60, v240\n"
      "v_and_b32 v248, 1, v240\n"
      "v_mul_u32_u24 v5, 0xaaaa, v248\n"
      "v_add_u32 v5, 0x4444, v5\n"
      "v_lshlrev_b32 v7, 6, v243\n"
      "s_mov_b32 s30, 39344256\n"
      "s_nop 0\n"
      "buffer_load_dwordx4 v247, s[40:43], s30 offen offset:3072 lds\n"
      "v_lshl_add_u32 v7, v248, 3, v7\n"
      "v_and_b32 v249, 1, v242\n"
      "v_lshl_add_u32 v6, v249, 5, v7\n"
      "v_lshrrev_b32 v250, 1, v242\n"
      "v_lshl_add_u32 v6, v250, 2, v6\n"
      "v_lshl_add_u32 v7, v242, 4, v7\n"
      "v_mov_b32 v8, 1.0\n"
      "s_lshl_b32 s54, %[wave], 14\n"
      "s_add_u32 s54, s54, %[ldsb]\n"
      "v_lshlrev_b32 v16, 10, v242\n"
      "v_lshrrev_b32 v231, 1, v241\n"
      "v_lshl_add_u32 v16, v231, 4, v16\n"
      "v_lshl_add_u32 v16, v248, 2, v16\n"
      "v_add_u32 v16, s54, v16\n"
      "v_lshrrev_b32 v231, 3, v241\n"
      "v_lshl_add_u32 v16, v231, 20, v16\n"
      "v_lshlrev_b32 v9, 9, v242\n"
      "v_add_u32 v9, s54, v9\n"
      "v_lshl_add_u32 v251, v241, 4, v9\n"
      "v_lshl_add_u32 v9, v244, 4, v9\n"
      "s_add_u32 s54, s54, 4096\n"
      "v_lshlrev_b32 v10, 10, v242\n"
      "v_lshl_add_u32 v10, v243, 4, v10\n"
      "v_lshl_add_u32 v10, v248, 9, v10\n"
      "v_add_u32 v10, s54, v10\n"
      "v_lshlrev_b32 v252, 10, v250\n"
      "v_lshl_add_u32 v252, v241, 4, v252\n"
      "v_lshl_add_u32 v252, v249, 3, v252\n"
      "v_add_u32 v252, s54, v252\n"
      "s_add_u32 s54, s54, 4096\n"
      "v_lshlrev_b32 v11, 3, v240\n"
      "v_add_u32 v11, s54, v11\n"
      "v_lshl_add_u32 v253, v242, 9, v245\n"
      "v_add_u32 v253, s54, v253\n"
      "v_mov_b32 v12, 0x3f803f80\n"
      "v_mov_b32 v13, 0x3f803f80\n"
      "v_mov_b32 v14, 0x3f803f80\n"
      "v_mov_b32 v15, 0x3f803f80\n"
      "s_mov_b32 vcc_lo, 0x55555555\n"
      "s_mov_b32 vcc_hi, 0x55555555\n"
      "s_mov_b32 s26, 16384\n"
      "s_mov_b32 s27, 0x20000\n"
      "s_branch L_hs_%=\n"
      "L_hr_%=:\n"
      "s_setpc_b64 vcc\n"
      "L_e5_%=:\n"
      "s_getpc_b64 s[56:57]\n"
      "L_te_%=:\n"
      "s_lshl_b32 s55, %[wave], 12\n"
      "s_add_u32 s55, s55, L_ln_%=-L_te_%=\n"
      "v_mbcnt_lo_u32_b32 v17, -1, 0\n"
      "v_mbcnt_hi_u32_b32 v17, -1, v17\n"
      "v_lshlrev_b32 v17, 6, v17\n"
      "v_add_u32 v17, s55, v17\n"
      "v_min_u32 v17, L_ai_end-L_te_%=, v17\n"
      "global_load_dword v17, v17, s[56:57]\n"
      "s_branch L_task_%=\n"
      "L_hs_%=:\n"
      "s_mov_b32 s38, 0xc000000\n"
      "s_mov_b64 exec, 0\n"
      "s_cmp_eq_u32 %[wave], 0\n"
      "s_cbranch_scc0 L_hn1o0_%=\n"
      "s_call_b64 vcc, L_h0_%=\n"
      "L_hn1o0_%=:\n"
      "s_cmp_eq_u32 %[wave], 1\n"
      "s_cbranch_scc0 L_hn1o1_%=\n"
      "s_call_b64 vcc, L_h1_%=\n"
      "L_hn1o1_%=:\n"
      "s_cmp_eq_u32 %[wave], 2\n"
      "s_cbranch_scc0 L_hn1o2_%=\n"
      "s_call_b64 vcc, L_h2_%=\n"
      "L_hn1o2_%=:\n"
      "s_cmp_eq_u32 %[wave], 3\n"
      "s_cbranch_scc0 L_hn1o3_%=\n"
      "s_nop 0\n"
      "L_hn1o3_%=:\n"
      "s_mov_b64 exec, -1\n"
      "s_mov_b32 vcc_lo, 0x55555555\n"
      "s_mov_b32 vcc_hi, 0x55555555\n"
      "s_add_u32 m0, s53, 20480\n"
      "s_nop 0\n"
      "buffer_load_dwordx4 v1, s[40:43], s73 offen offset:0 nt lds\n"
      "s_mov_b32 s44, s12\n s_and_b32 s45, s13, 0xffff\n"
      "buffer_load_dwordx4 v1, s[40:43], s73 offen offset:2048 nt lds\n"
      "s_waitcnt vmcnt(12)\n"
      "v_perm_b32 v116, v68, v64, v238\n"
      "v_perm_b32 v222, v69, v65, v239\n"
      "v_perm_b32 v60, v69, v65, v238\n"
      "s_waitcnt vmcnt(10)\n"
      "v_perm_b32 v193, v79, v75, v238\n"
      "v_perm_b32 v225, v76, v72, v239\n"
      "buffer_load_dwordx4 v1, s[40:43], s74 offen offset:1024 nt lds\n"
      "buffer_load_dwordx4 v1, s[40:43], s74 offen offset:3072 nt lds\n"
      "s_mov_b64 exec, 0\n"
      "s_cmp_eq_u32 %[wave], 0\n"
      "s_cbranch_scc0 L_hn2o0_%=\n"
      "s_call_b64 vcc, L_h3_%=\n"
      "s_call_b64 vcc, L_h7_%=\n"
      "s_nop 0\n"
      "L_hn2o0_%=:\n"
      "s_cmp_eq_u32 %[wave], 1\n"
      "s_cbranch_scc0 L_hn2o1_%=\n"
      "s_call_b64 vcc, L_h4_%=\n"
      "s_nop 0\n"
      "s_nop 0\n"
      "L_hn2o1_%=:\n"
      "s_cmp_eq_u32 %[wave], 2\n"
      "s_cbranch_scc0 L_hn2o2_%=\n"
      "s_call_b64 vcc, L_h5_%=\n"
      "s_nop 0\n"
      "s_nop 0\n"
      "L_hn2o2_%=:\n"
      "s_cmp_eq_u32 %[wave], 3\n"
      "s_cbranch_scc0 L_hn2o3_%=\n"
      "s_call_b64 vcc, L_h6_%=\n"
      "s_nop 0\n"
      "s_nop 0\n"
      "L_hn2o3_%=:\n"
      "s_mov_b64 exec, -1\n"
      "s_mov_b32 vcc_lo, 0x55555555\n"
      "s_mov_b32 vcc_hi, 0x55555555\n"
      "v_perm_b32 v50, v70, v66, v239\n"
      "s_waitcnt vmcnt(10)\n"
      "v_perm_b32 v194, v84, v80, v238\n"
      "s_waitcnt vmcnt(8)\n"
      "v_perm_b32 v63, v93, v89, v239\n"
      "v_perm_b32 v195, v92, v88, v238\n"
      "v_perm_b32 v51, v78, v74, v239\n"
      "v_perm_b32 v221, v94, v90, v239\n"
      "v_perm_b32 v226, v85, v81, v238\n"
      "v_perm_b32 v98, v84, v80, v239\n"
      "v_perm_b32 v192, v71, v67, v238\n"
      "v_perm_b32 v61, v77, v73, v238\n"
      "buffer_load_dwordx4 v[228:231], v236, s[44:47], s57 offen offset:0\n"
      "v_perm_b32 v119, v94, v90, v238\n"
      "ds_write_b64 v232, v[50:51] offset:4096\n"
      "v_perm_b32 v118, v86, v82, v238\n"
      "v_perm_b32 v117, v76, v72, v238\n"
      "v_perm_b32 v101, v95, v91, v238\n"
      "ds_write_b64 v255, v[116:117] offset:0\n"
      "buffer_load_dwordx4 v[50:53], v236, s[44:47], s57 offen offset:256\n"
      "v_perm_b32 v227, v93, v89, v238\n"
      "v_perm_b32 v117, v95, v91, v239\n"
      "v_perm_b32 v62, v85, v81, v239\n"
      "v_perm_b32 v100, v87, v83, v238\n"
      "v_perm_b32 v220, v86, v82, v239\n"
      "v_perm_b32 v93, v79, v75, v239\n"
      "ds_write_b64 v232, v[100:101] offset:24576\n"
      "v_perm_b32 v116, v87, v83, v239\n"
      "ds_write_b64 v232, v[118:119] offset:16384\n"
      "v_perm_b32 v75, v78, v74, v238\n"
      "v_perm_b32 v223, v77, v73, v239\n"
      "ds_write_b64 v255, v[222:223] offset:12288\n"
      "ds_write_b64 v255, v[60:61] offset:8192\n"
      "v_perm_b32 v99, v92, v88, v239\n"
      "ds_write_b64 v255, v[98:99] offset:20480\n"
      "ds_write_b64 v232, v[220:221] offset:20480\n"
      "v_perm_b32 v74, v70, v66, v238\n"
      "ds_write_b64 v232, v[74:75] offset:0\n"
      "ds_write_b64 v255, v[226:227] offset:24576\n"
      "ds_write_b64 v255, v[194:195] offset:16384\n"
      "ds_write_b64 v255, v[62:63] offset:28672\n"
      "ds_write_b64 v232, v[116:117] offset:28672\n"
      "v_perm_b32 v224, v68, v64, v239\n"
      "ds_write_b64 v232, v[192:193] offset:8192\n"
      "ds_write_b64 v255, v[224:225] offset:4096\n"
      "v_perm_b32 v92, v71, v67, v239\n"
      "ds_write_b64 v232, v[92:93] offset:12288\n"
      "s_waitcnt lgkmcnt(0)\n"
      "s_barrier\n"
      "buffer_load_dwordx4 v[68:71], v236, s[44:47], s57 offen offset:512\n"
      "buffer_load_dwordx4 v[20:23], v236, s[44:47], s57 offen offset:768\n"
      "ds_read_b128 a[56:59], v254 offset:14336\n"
      "ds_read_b128 a[88:91], v254 offset:22528\n"
      "ds_read_b128 a[104:107], v254 offset:26624\n"
      "ds_read_b128 a[124:127], v254 offset:31744\n"
      "ds_read_b128 a[84:87], v254 offset:21504\n"
      "ds_read_b128 a[0:3], v254 offset:0\n"
      "ds_read_b128 a[48:51], v254 offset:12288\n"
      "ds_read_b128 a[108:111], v254 offset:27648\n"
      "ds_read_b128 a[76:79], v254 offset:19456\n"
      "s_mov_b32 s44, s6\n s_and_b32 s45, s7, 0xffff\n"
      "ds_read_b128 a[60:63], v254 offset:15360\n"
      "ds_read_b128 a[28:31], v254 offset:7168\n"
      "s_waitcnt vmcnt(8)\n"
      "ds_read_b128 v[72:75], v0 offset:0\n"
      "ds_read_b128 a[24:27], v254 offset:6144\n"
      "ds_read_b128 a[8:11], v254 offset:2048\n"
      "ds_read_b128 a[52:55], v254 offset:13312\n"
      "ds_read_b128 a[96:99], v254 offset:24576\n"
      "buffer_load_dwordx4 v[60:63], v246, s[44:47], s52 offen offset:0\n"
      "ds_read_b128 a[116:119], v254 offset:29696\n"
      "buffer_load_dwordx4 v[76:79], v246, s[44:47], s52 offen offset:128\n"
      "ds_read_b128 v[224:227], v0 offset:512\n"
      "ds_read_b128 a[12:15], v254 offset:3072\n"
      "ds_read_b128 a[4:7], v254 offset:1024\n"
      "ds_read_b128 a[72:75], v254 offset:18432\n"
      "ds_read_b128 a[68:71], v254 offset:17408\n"
      "ds_read_b128 v[220:223], v0 offset:1536\n"
      "ds_read_b128 a[40:43], v254 offset:10240\n"
      "s_mov_b32 s30, 65561600\n"
      "ds_read_b128 a[36:39], v254 offset:9216\n"
      "ds_read_b128 v[92:95], v0 offset:1024\n"
      "buffer_load_dwordx4 v[88:91], v246, s[44:47], s52 offen offset:256\n"
      "ds_read_b128 a[64:67], v254 offset:16384\n"
      "buffer_load_dwordx4 v[84:87], v246, s[44:47], s52 offen offset:384\n"
      "ds_read_b128 a[112:115], v254 offset:28672\n"
      "ds_read_b128 a[120:123], v254 offset:30720\n"
      "ds_read_b128 a[100:103], v254 offset:25600\n"
      "ds_read_b128 a[92:95], v254 offset:23552\n"
      "ds_read_b128 a[44:47], v254 offset:11264\n"
      "ds_read_b128 a[20:23], v254 offset:5120\n"
      "s_add_u32 m0, s53, 4096\n"
      "ds_read_b128 a[80:83], v254 offset:20480\n"
      "s_add_u32 s44, s14, s49\n s_addc_u32 s45, s15, s50\n s_and_b32 s45, s45, 0xffff\n s_mov_b32 s46, 13133824\n"
      "ds_read_b128 a[16:19], v254 offset:4096\n"
      "buffer_load_dwordx4 v247, s[40:43], s30 offen offset:0 lds\n"
      "s_waitcnt lgkmcnt(15)\n"
      "v_mfma_f32_16x16x32_bf16 v[116:119], v[72:75], a[96:99], 0\n"
      "s_waitcnt lgkmcnt(5)\n"
      "v_mfma_f32_16x16x32_bf16 v[116:119], v[224:227], a[100:103], v[116:119]\n"
      "v_mfma_f32_16x16x32_bf16 v[116:119], v[92:95], a[104:107], v[116:119]\n"
      "v_mfma_f32_16x16x32_bf16 v[116:119], v[220:223], a[108:111], v[116:119]\n"
      "ds_read_b128 a[32:35], v254 offset:8192\n"
      "v_mfma_f32_16x16x32_bf16 v[64:67], a[0:3], v[72:75], 0\n"
      "v_mfma_f32_16x16x32_bf16 v[64:67], a[4:7], v[224:227], v[64:67]\n"
      "v_mfma_f32_16x16x32_bf16 v[64:67], a[8:11], v[92:95], v[64:67]\n"
      "v_mfma_f32_16x16x32_bf16 v[64:67], a[12:15], v[220:223], v[64:67]\n"
      "s_mov_b32 s30, 91773952\n"
      "buffer_load_dwordx4 v247, s[40:43], s30 offen offset:2048 lds\n"
      "v_mfma_f32_16x16x32_bf16 v[98:101], a[48:51], v[72:75], 0\n"
      "v_mfma_f32_16x16x32_bf16 v[98:101], a[52:55], v[224:227], v[98:101]\n"
      "v_mfma_f32_16x16x32_bf16 v[98:101], a[56:59], v[92:95], v[98:101]\n"
      "v_mfma_f32_16x16x32_bf16 v[98:101], a[60:63], v[220:223], v[98:101]\n"
      "s_waitcnt lgkmcnt(2)\n"
      "v_mfma_f32_16x16x32_bf16 v[136:139], v[72:75], a[80:83], 0\n"
      "v_mfma_f32_16x16x32_bf16 v[136:139], v[224:227], a[84:87], v[136:139]\n"
      "v_mfma_f32_16x16x32_bf16 v[136:139], v[92:95], a[88:91], v[136:139]\n"
      "v_mfma_f32_16x16x32_bf16 v[136:139], v[220:223], a[92:95], v[136:139]\n"
      "v_mfma_f32_16x16x32_bf16 v[204:207], v[72:75], a[112:115], 0\n"
      "s_mov_b32 s30, 65560704\n"
      "v_mfma_f32_16x16x32_bf16 v[204:207], v[224:227], a[116:119], v[204:207]\n"
      "v_mfma_f32_16x16x32_bf16 v[204:207], v[92:95], a[120:123], v[204:207]\n"
      "v_mfma_f32_16x16x32_bf16 v[204:207], v[220:223], a[124:127], v[204:207]\n"
      "buffer_load_dwordx4 v247, s[40:43], s30 offen offset:1024 lds\n"
      "s_waitcnt lgkmcnt(1)\n"
      "v_mfma_f32_16x16x32_bf16 v[104:107], a[16:19], v[72:75], 0\n"
      "v_cvt_pk_bf16_f32 v234, v98, v99\n"
      "v_mfma_f32_16x16x32_bf16 v[104:107], a[20:23], v[224:227], v[104:107]\n"
      "v_mfma_f32_16x16x32_bf16 v[104:107], a[24:27], v[92:95], v[104:107]\n"
      "v_mfma_f32_16x16x32_bf16 v[104:107], a[28:31], v[220:223], v[104:107]\n"
      "s_nop 0\n"
      "v_cvt_pk_bf16_f32 v210, v204, v205\n"
      "s_nop 5\n"
      "v_cvt_pk_bf16_f32 v54, v104, v105\n"
      "s_waitcnt lgkmcnt(0)\n"
      "v_mfma_f32_16x16x32_bf16 v[96:99], a[32:35], v[72:75], 0\n"
      "v_mfma_f32_16x16x32_bf16 v[96:99], a[36:39], v[224:227], v[96:99]\n"
      "v_mfma_f32_16x16x32_bf16 v[96:99], a[40:43], v[92:95], v[96:99]\n"
      "v_mfma_f32_16x16x32_bf16 v[96:99], a[44:47], v[220:223], v[96:99]\n"
      "s_mov_b32 s30, 91773056\n"
      "buffer_load_dwordx4 v247, s[40:43], s30 offen offset:3072 lds\n"
      "s_waitcnt vmcnt(12)\n"
      "ds_read_b128 v[212:215], v0 offset:20480\n"
      "ds_read_b128 v[124:127], v0 offset:20992\n"
      "v_mfma_f32_16x16x32_bf16 v[28:31], v[72:75], a[64:67], 0\n"
      "ds_read_b128 v[188:191], v0 offset:21504\n"
      "ds_read_b128 v[244:247], v0 offset:22016\n"
      "v_mfma_f32_16x16x32_bf16 v[28:31], v[224:227], a[68:71], v[28:31]\n"
      "v_mfma_f32_16x16x32_bf16 v[28:31], v[92:95], a[72:75], v[28:31]\n"
      "v_mfma_f32_16x16x32_bf16 v[28:31], v[220:223], a[76:79], v[28:31]\n"
      "v_cvt_pk_bf16_f32 v233, v98, v99\n"
      "s_add_u32 m0, s53, 16384\n"
      "s_nop 0\n"
      "buffer_load_dwordx4 v1, s[40:43], s75 offen offset:0 nt lds\n"
      "buffer_load_dwordx4 v1, s[40:43], s75 offen offset:2048 nt lds\n"
      "s_waitcnt vmcnt(12)\n"
      "v_perm_b32 v44, v50, v228, v238\n"
      "v_perm_b32 v198, v53, v231, v239\n"
      "v_perm_b32 v104, v52, v230, v238\n"
      "v_perm_b32 v98, v50, v228, v239\n"
      "s_waitcnt lgkmcnt(3)\n"
      "v_mfma_f32_16x16x32_bf16 v[148:151], v[212:215], a[112:115], 0\n"
      "s_waitcnt lgkmcnt(2)\n"
      "v_mfma_f32_16x16x32_bf16 v[148:151], v[124:127], a[116:119], v[148:151]\n"
      "v_perm_b32 v204, v51, v229, v239\n"
      "s_waitcnt lgkmcnt(1)\n"
      "v_mfma_f32_16x16x32_bf16 v[148:151], v[188:191], a[120:123], v[148:151]\n"
      "s_waitcnt lgkmcnt(0)\n"
      "v_mfma_f32_16x16x32_bf16 v[148:151], v[244:247], a[124:127], v[148:151]\n"
      "v_perm_b32 v194, v51, v229, v238\n"
      "v_perm_b32 v50, v52, v230, v239\n"
      "v_mfma_f32_16x16x32_bf16 v[184:187], v[212:215], a[64:67], 0\n"
      "buffer_load_dwordx4 v1, s[40:43], s76 offen offset:1024 nt lds\n"
      "v_mfma_f32_16x16x32_bf16 v[184:187], v[124:127], a[68:71], v[184:187]\n"
      "v_mfma_f32_16x16x32_bf16 v[184:187], v[188:191], a[72:75], v[184:187]\n"
      "s_waitcnt vmcnt(11)\n"
      "v_perm_b32 v51, v22, v70, v239\n"
      "v_perm_b32 v99, v20, v68, v239\n"
      "v_mfma_f32_16x16x32_bf16 v[184:187], v[244:247], a[76:79], v[184:187]\n"
      "ds_write_b64 v232, v[50:51] offset:53248\n"
      "buffer_load_dwordx4 v1, s[40:43], s76 offen offset:3072 nt lds\n"
      "ds_write_b64 v255, v[98:99] offset:53248\n"
      "v_perm_b32 v195, v21, v69, v238\n"
      "s_waitcnt vmcnt(11)\n"
      "v_lshlrev_b32 v228, 16, v60\n"
      "v_lshlrev_b32 v166, 16, v61\n"
      "v_lshlrev_b32 v98, 16, v62\n"
      "v_lshlrev_b32 v236, 16, v63\n"
      "v_mul_f32 v166, s51, v166\n"
      "v_mul_f32 v98, s51, v98\n"
      "v_mfma_f32_16x16x32_bf16 v[48:51], a[16:19], v[212:215], 0\n"
      "v_perm_b32 v105, v22, v70, v238\n"
      "v_and_b32 v82, 0xffff0000, v63\n"
      "v_mfma_f32_16x16x32_bf16 v[48:51], a[20:23], v[124:127], v[48:51]\n"
      "v_perm_b32 v45, v20, v68, v238\n"
      "v_perm_b32 v199, v23, v71, v239\n"
      "v_mfma_f32_16x16x32_bf16 v[48:51], a[24:27], v[188:191], v[48:51]\n"
      "v_and_b32 v230, 0xffff0000, v60\n"
      "v_mul_f32 v230, s51, v230\n"
      "v_mfma_f32_16x16x32_bf16 v[48:51], a[28:31], v[244:247], v[48:51]\n"
      "ds_write_b64 v255, v[44:45] offset:49152\n"
      "ds_write_b64 v255, v[194:195] offset:57344\n"
      "ds_write_b64 v232, v[104:105] offset:49152\n"
      "v_mfma_f32_16x16x32_bf16 v[44:47], a[0:3], v[212:215], 0\n"
      "s_waitcnt vmcnt(9)\n"
      "v_lshlrev_b32 v20, 16, v89\n"
      "v_lshlrev_b32 v104, 16, v91\n"
      "v_mfma_f32_16x16x32_bf16 v[44:47], a[4:7], v[124:127], v[44:47]\n"
      "ds_write_b64 v232, v[198:199] offset:61440\n"
      "v_lshlrev_b32 v99, 16, v78\n"
      "v_lshlrev_b32 v167, 16, v77\n"
      "v_and_b32 v198, 0xffff0000, v90\n"
      "v_and_b32 v164, 0xffff0000, v89\n"
      "v_mul_f32 v198, s51, v198\n"
      "s_waitcnt vmcnt(8)\n"
      "v_lshlrev_b32 v35, 16, v86\n"
      "v_mul_f32 v35, s51, v35\n"
      "v_and_b32 v60, 0xffff0000, v62\n"
      "v_mul_f32 v99, s51, v99\n"
      "v_lshlrev_b32 v229, 16, v76\n"
      "v_mul_f32 v229, s51, v229\n"
      "v_lshlrev_b32 v105, 16, v87\n"
      "v_perm_b32 v68, v53, v231, v238\n"
      "v_and_b32 v199, 0xffff0000, v86\n"
      "v_perm_b32 v205, v21, v69, v239\n"
      "v_and_b32 v231, 0xffff0000, v76\n"
      "v_cvt_pk_bf16_f32 v53, v66, v67\n"
      "v_mul_f32 v167, s51, v167\n"
      "v_mul_f32 v199, s51, v199\n"
      "v_lshlrev_b32 v33, 16, v84\n"
      "v_cvt_pk_bf16_f32 v76, v166, v167\n"
      "v_mul_f32 v231, s51, v231\n"
      "v_and_b32 v62, 0xffff0000, v88\n"
      "v_lshlrev_b32 v34, 16, v90\n"
      "v_and_b32 v67, 0xffff0000, v77\n"
      "v_mul_f32 v34, s51, v34\n"
      "v_and_b32 v165, 0xffff0000, v85\n"
      "v_cvt_pk_bf16_f32 v239, v34, v35\n"
      "v_mul_f32 v228, s51, v228\n"
      "v_and_b32 v66, 0xffff0000, v61\n"
      "v_mul_f32 v20, s51, v20\n"
      "v_mul_f32 v67, s51, v67\n"
      "v_and_b32 v90, 0xffff0000, v91\n"
      "v_cvt_pk_bf16_f32 v89, v198, v199\n"
      "v_mul_f32 v90, s51, v90\n"
      "v_mul_f32 v105, s51, v105\n"
      "v_mul_f32 v104, s51, v104\n"
      "v_mul_f32 v82, s51, v82\n"
      "v_and_b32 v83, 0xffff0000, v79\n"
      "v_mul_f32 v62, s51, v62\n"
      "v_and_b32 v63, 0xffff0000, v84\n"
      "v_and_b32 v91, 0xffff0000, v87\n"
      "v_mul_f32 v91, s51, v91\n"
      "v_mul_f32 v66, s51, v66\n"
      "v_and_b32 v61, 0xffff0000, v78\n"
      "v_mul_f32 v236, s51, v236\n"
      "v_lshlrev_b32 v21, 16, v85\n"
      "v_lshlrev_b32 v237, 16, v79\n"
      "v_mul_f32 v237, s51, v237\n"
      "v_mul_f32 v61, s51, v61\n"
      "v_perm_b32 v69, v23, v71, v238\n"
      "v_mul_f32 v21, s51, v21\n"
      "v_mul_f32 v33, s51, v33\n"
      "v_cvt_pk_bf16_f32 v79, v90, v91\n"
      "v_cvt_pk_bf16_f32 v91, v104, v105\n"
      "v_lshlrev_b32 v32, 16, v88\n"
      "v_cvt_pk_bf16_f32 v238, v98, v99\n"
      "v_mul_f32 v83, s51, v83\n"
      "v_cvt_pk_bf16_f32 v78, v82, v83\n"
      "v_mul_f32 v32, s51, v32\n"
      "v_cvt_pk_bf16_f32 v195, v32, v33\n"
      "v_cvt_pk_bf16_f32 v77, v20, v21\n"
      "v_mul_f32 v63, s51, v63\n"
      "v_cvt_pk_bf16_f32 v90, v236, v237\n"
      "v_cvt_pk_bf16_f32 v194, v228, v229\n"
      "v_mul_f32 v165, s51, v165\n"
      "v_cvt_pk_bf16_f32 v237, v62, v63\n"
      "v_mul_f32 v164, s51, v164\n"
      "v_mul_f32 v60, s51, v60\n"
      "v_cvt_pk_bf16_f32 v88, v60, v61\n"
      "v_cvt_pk_bf16_f32 v61, v164, v165\n"
      "v_cvt_pk_bf16_f32 v60, v66, v67\n"
      "v_cvt_pk_bf16_f32 v236, v230, v231\n"
      "v_mfma_f32_16x16x32_bf16 v[228:231], v[212:215], a[96:99], 0\n"
      "s_add_u32 m0, s53, 12288\n"
      "ds_write_b64 v232, v[90:91] offset:40960\n"
      "ds_write_b64 v255, v[204:205] offset:61440\n"
      "ds_write_b64 v255, v[60:61] offset:45056\n"
      "ds_write_b64 v232, v[238:239] offset:32768\n"
      "ds_write_b64 v255, v[76:77] offset:40960\n"
      "ds_write_b64 v232, v[68:69] offset:57344\n"
      "ds_write_b64 v232, v[88:89] offset:36864\n"
      "ds_write_b64 v232, v[78:79] offset:45056\n"
      "ds_write_b64 v255, v[236:237] offset:36864\n"
      "ds_write_b64 v255, v[194:195] offset:32768\n"
      "buffer_load_dwordx4 v1, s[40:43], s77 offen offset:0 nt lds\n"
      "v_cvt_pk_bf16_f32 v235, v100, v101\n"
      "v_mfma_f32_16x16x32_bf16 v[236:239], a[48:51], v[212:215], 0\n"
      "v_mfma_f32_16x16x32_bf16 v[236:239], a[52:55], v[124:127], v[236:239]\n"
      "v_mfma_f32_16x16x32_bf16 v[236:239], a[56:59], v[188:191], v[236:239]\n"
      "buffer_load_dwordx4 v1, s[40:43], s77 offen offset:2048 nt lds\n"
      "v_mfma_f32_16x16x32_bf16 v[236:239], a[60:63], v[244:247], v[236:239]\n"
      "buffer_load_dwordx4 v1, s[40:43], s78 offen offset:1024 nt lds\n"
      "v_mfma_f32_16x16x32_bf16 v[76:79], v[212:215], a[80:83], 0\n"
      "v_mfma_f32_16x16x32_bf16 v[76:79], v[124:127], a[84:87], v[76:79]\n"
      "v_mfma_f32_16x16x32_bf16 v[76:79], v[188:191], a[88:91], v[76:79]\n"
      "v_mfma_f32_16x16x32_bf16 v[76:79], v[244:247], a[92:95], v[76:79]\n"
      "s_waitcnt lgkmcnt(0)\n"
      "s_barrier\n"
      "ds_read_b128 a[156:159], v254 offset:39936\n"
      "buffer_load_dwordx4 v1, s[40:43], s78 offen offset:3072 nt lds\n"
      "ds_read_b128 a[144:147], v254 offset:36864\n"
      "v_mfma_f32_16x16x32_bf16 v[164:167], a[32:35], v[212:215], 0\n"
      "ds_read_b128 a[148:151], v254 offset:37888\n"
      "v_mfma_f32_16x16x32_bf16 v[164:167], a[36:39], v[124:127], v[164:167]\n"
      "ds_read_b128 a[152:155], v254 offset:38912\n"
      "v_mfma_f32_16x16x32_bf16 v[164:167], a[40:43], v[188:191], v[164:167]\n"
      "v_mfma_f32_16x16x32_bf16 v[164:167], a[44:47], v[244:247], v[164:167]\n"
      "ds_read_b128 a[180:183], v254 offset:46080\n"
      "v_cvt_pk_bf16_f32 v99, v118, v119\n"
      "v_mfma_f32_16x16x32_bf16 v[228:231], v[124:127], a[100:103], v[228:231]\n"
      "ds_read_b128 a[164:167], v254 offset:41984\n"
      "v_mfma_f32_16x16x32_bf16 v[228:231], v[188:191], a[104:107], v[228:231]\n"
      "ds_read_b128 a[172:175], v254 offset:44032\n"
      "v_mfma_f32_16x16x32_bf16 v[228:231], v[244:247], a[108:111], v[228:231]\n"
      "ds_read_b128 a[176:179], v254 offset:45056\n"
      "v_cvt_pk_bf16_f32 v211, v206, v207\n"
      "v_mfma_f32_16x16x32_bf16 v[44:47], a[8:11], v[188:191], v[44:47]\n"
      "ds_read_b128 a[188:191], v254 offset:48128\n"
      "v_mfma_f32_16x16x32_bf16 v[44:47], a[12:15], v[244:247], v[44:47]\n"
      "ds_read_b128 a[160:163], v254 offset:40960\n"
      "s_waitcnt lgkmcnt(8)\n"
      "v_mfma_f32_16x16x32_bf16 v[88:91], a[144:147], v[72:75], 0\n"
      "ds_read_b128 a[184:187], v254 offset:47104\n"
      "s_waitcnt lgkmcnt(8)\n"
      "v_mfma_f32_16x16x32_bf16 v[88:91], a[148:151], v[224:227], v[88:91]\n"
      "ds_read_b128 a[192:195], v254 offset:49152\n"
      "s_waitcnt lgkmcnt(8)\n"
      "v_mfma_f32_16x16x32_bf16 v[88:91], a[152:155], v[92:95], v[88:91]\n"
      "ds_read_b128 a[204:207], v254 offset:52224\n"
      "s_add_u32 m0, s53, 8192\n"
      "v_mfma_f32_16x16x32_bf16 v[88:91], a[156:159], v[220:223], v[88:91]\n"
      "ds_read_b128 a[248:251], v254 offset:63488\n"
      "v_mfma_f32_16x16x32_bf16 v[68:71], a[144:147], v[212:215], 0\n"
      "ds_read_b128 a[168:171], v254 offset:43008\n"
      "v_cvt_pk_bf16_f32 v82, v28, v29\n"
      "v_mfma_f32_16x16x32_bf16 v[68:71], a[148:151], v[124:127], v[68:71]\n"
      "ds_read_b128 a[216:219], v254 offset:55296\n"
      "v_mfma_f32_16x16x32_bf16 v[68:71], a[152:155], v[188:191], v[68:71]\n"
      "ds_read_b128 a[240:243], v254 offset:61440\n"
      "v_mfma_f32_16x16x32_bf16 v[68:71], a[156:159], v[244:247], v[68:71]\n"
      "ds_read_b128 a[208:211], v254 offset:53248\n"
      "s_waitcnt lgkmcnt(10)\n"
      "v_mfma_f32_16x16x32_bf16 v[84:87], a[176:179], v[72:75], 0\n"
      "ds_read_b128 a[132:135], v254 offset:33792\n"
      "v_mfma_f32_16x16x32_bf16 v[84:87], a[180:183], v[224:227], v[84:87]\n"
      "s_waitcnt lgkmcnt(8)\n"
      "v_mfma_f32_16x16x32_bf16 v[84:87], a[184:187], v[92:95], v[84:87]\n"
      "L_h0_%=:\n"
      "s_cbranch_execz L_hr_%=\n"
      "v_mfma_f32_16x16x32_bf16 v[84:87], a[188:191], v[220:223], v[84:87]\n"
      "ds_read_b128 a[252:255], v254 offset:64512\n"
      "v_cvt_pk_bf16_f32 v52, v64, v65\n"
      "v_mfma_f32_16x16x32_bf16 v[64:67], a[176:179], v[212:215], 0\n"
      "buffer_load_dwordx4 v1, s[40:43], s90 offen offset:0 nt lds\n"
      "v_mfma_f32_16x16x32_bf16 v[64:67], a[180:183], v[124:127], v[64:67]\n"
      "v_cvt_pk_bf16_f32 v255, v90, v91\n"
      "v_mfma_f32_16x16x32_bf16 v[64:67], a[184:187], v[188:191], v[64:67]\n"
      "ds_read_b128 a[128:131], v254 offset:32768\n"
      "v_mfma_f32_16x16x32_bf16 v[64:67], a[188:191], v[244:247], v[64:67]\n"
      "ds_read_b128 a[244:247], v254 offset:62464\n"
      "v_cvt_pk_bf16_f32 v119, v230, v231\n"
      "v_mfma_f32_16x16x32_bf16 v[60:63], a[160:163], v[212:215], 0\n"
      "ds_read_b128 a[224:227], v254 offset:57344\n"
      "v_cvt_pk_bf16_f32 v230, v136, v137\n"
      "v_mfma_f32_16x16x32_bf16 v[60:63], a[164:167], v[124:127], v[60:63]\n"
      "ds_read_b128 a[140:143], v254 offset:35840\n"
      "s_waitcnt lgkmcnt(9)\n"
      "v_mfma_f32_16x16x32_bf16 v[60:63], a[168:171], v[188:191], v[60:63]\n"
      "v_mfma_f32_16x16x32_bf16 v[60:63], a[172:175], v[244:247], v[60:63]\n"
      "ds_read_b128 a[136:139], v254 offset:34816\n"
      "v_mfma_f32_16x16x32_bf16 v[204:207], a[160:163], v[72:75], 0\n"
      "ds_read_b128 a[228:231], v254 offset:58368\n"
      "v_mfma_f32_16x16x32_bf16 v[204:207], a[164:167], v[224:227], v[204:207]\n"
      "v_cvt_pk_bf16_f32 v123, v78, v79\n"
      "v_mfma_f32_16x16x32_bf16 v[204:207], a[168:171], v[92:95], v[204:207]\n"
      "buffer_load_dwordx4 v1, s[40:43], s90 offen offset:2048 nt lds\n"
      "v_mfma_f32_16x16x32_bf16 v[204:207], a[172:175], v[220:223], v[204:207]\n"
      "s_waitcnt lgkmcnt(9)\n"
      "v_mfma_f32_16x16x32_bf16 v[40:43], v[72:75], a[240:243], 0\n"
      "ds_read_b128 a[236:239], v254 offset:60416\n"
      "s_waitcnt lgkmcnt(5)\n"
      "v_mfma_f32_16x16x32_bf16 v[40:43], v[224:227], a[244:247], v[40:43]\n"
      "v_mfma_f32_16x16x32_bf16 v[40:43], v[92:95], a[248:251], v[40:43]\n"
      "ds_read_b128 a[200:203], v254 offset:51200\n"
      "v_cvt_pk_bf16_f32 v199, v88, v89\n"
      "v_mfma_f32_16x16x32_bf16 v[40:43], v[220:223], a[252:255], v[40:43]\n"
      "ds_read_b128 a[196:199], v254 offset:50176\n"
      "v_mfma_f32_16x16x32_bf16 v[88:91], a[128:131], v[72:75], 0\n"
      "v_mfma_f32_16x16x32_bf16 v[88:91], a[132:135], v[224:227], v[88:91]\n"
      "s_waitcnt lgkmcnt(4)\n"
      "v_mfma_f32_16x16x32_bf16 v[88:91], a[136:139], v[92:95], v[88:91]\n"
      "ds_read_b128 a[220:223], v254 offset:56320\n"
      "v_mfma_f32_16x16x32_bf16 v[88:91], a[140:143], v[220:223], v[88:91]\n"
      "buffer_load_dwordx4 v1, s[40:43], s91 offen offset:1024 nt lds\n"
      "v_mfma_f32_16x16x32_bf16 v[32:35], a[128:131], v[212:215], 0\n"
      "ds_read_b128 a[232:235], v254 offset:59392\n"
      "v_mfma_f32_16x16x32_bf16 v[32:35], a[132:135], v[124:127], v[32:35]\n"
      "v_mfma_f32_16x16x32_bf16 v[32:35], a[136:139], v[188:191], v[32:35]\n"
      "s_waitcnt vmcnt(11)\n"
      "ds_read_b128 v[20:23], v0 offset:4096\n"
      "v_mfma_f32_16x16x32_bf16 v[32:35], a[140:143], v[244:247], v[32:35]\n"
      "ds_read_b128 a[212:215], v254 offset:54272\n"
      "ds_read_b128 v[100:103], v0 offset:4608\n"
      "v_mfma_f32_16x16x32_bf16 v[24:27], v[72:75], a[192:195], 0\n"
      "v_cvt_pk_bf16_f32 v198, v88, v89\n"
      "s_waitcnt lgkmcnt(5)\n"
      "v_mfma_f32_16x16x32_bf16 v[24:27], v[224:227], a[196:199], v[24:27]\n"
      "ds_read_b128 v[216:219], v0 offset:5632\n"
      "ds_read_b128 v[112:115], v0 offset:5120\n"
      "v_mfma_f32_16x16x32_bf16 v[24:27], v[92:95], a[200:203], v[24:27]\n"
      "v_cvt_pk_bf16_f32 v231, v138, v139\n"
      "v_mfma_f32_16x16x32_bf16 v[24:27], v[220:223], a[204:207], v[24:27]\n"
      "v_cvt_pk_bf16_f32 v254, v90, v91\n"
      "v_mfma_f32_16x16x32_bf16 v[152:155], v[72:75], a[224:227], 0\n"
      "buffer_load_dwordx4 v1, s[40:43], s91 offen offset:3072 nt lds\n"
      "v_mfma_f32_16x16x32_bf16 v[152:155], v[224:227], a[228:231], v[152:155]\n"
      "s_waitcnt lgkmcnt(5)\n"
      "v_mfma_f32_16x16x32_bf16 v[152:155], v[92:95], a[232:235], v[152:155]\n"
      "v_mfma_f32_16x16x32_bf16 v[152:155], v[220:223], a[236:239], v[152:155]\n"
      "v_mfma_f32_16x16x32_bf16 v[160:163], v[72:75], a[208:211], 0\n"
      "s_waitcnt lgkmcnt(3)\n"
      "v_mfma_f32_16x16x32_bf16 v[160:163], v[224:227], a[212:215], v[160:163]\n"
      "v_cvt_pk_bf16_f32 v140, v164, v165\n"
      "v_mfma_f32_16x16x32_bf16 v[160:163], v[92:95], a[216:219], v[160:163]\n"
      "v_cvt_pk_bf16_f32 v83, v30, v31\n"
      "v_mfma_f32_16x16x32_bf16 v[160:163], v[220:223], a[220:223], v[160:163]\n"
      "v_mfma_f32_16x16x32_bf16 v[194:197], a[176:179], v[20:23], 0\n"
      "s_waitcnt lgkmcnt(2)\n"
      "v_mfma_f32_16x16x32_bf16 v[194:197], a[180:183], v[100:103], v[194:197]\n"
      "s_waitcnt lgkmcnt(0)\n"
      "v_mfma_f32_16x16x32_bf16 v[194:197], a[184:187], v[112:115], v[194:197]\n"
      "v_cvt_pk_bf16_f32 v19, v154, v42\n"
      "v_cvt_pk_bf16_f32 v57, v155, v43\n"
      "v_mfma_f32_16x16x32_bf16 v[194:197], a[188:191], v[216:219], v[194:197]\n"
      "s_barrier\n"
      "v_mfma_f32_16x16x32_bf16 v[78:81], v[20:23], a[192:195], 0\n"
      "v_mfma_f32_16x16x32_bf16 v[78:81], v[100:103], a[196:199], v[78:81]\n"
      "v_mfma_f32_16x16x32_bf16 v[78:81], v[112:115], a[200:203], v[78:81]\n"
      "v_mfma_f32_16x16x32_bf16 v[78:81], v[216:219], a[204:207], v[78:81]\n"
      "ds_write_b64 v253, v[210:211] offset:384\n"
      "v_mfma_f32_16x16x32_bf16 v[28:31], v[20:23], a[80:83], 0\n"
      "v_mfma_f32_16x16x32_bf16 v[28:31], v[100:103], a[84:87], v[28:31]\n"
      "v_cvt_pk_bf16_f32 v55, v106, v107\n"
      "v_mfma_f32_16x16x32_bf16 v[28:31], v[112:115], a[88:91], v[28:31]\n"
      "v_cvt_pk_bf16_f32 v232, v96, v97\n"
      "v_mfma_f32_16x16x32_bf16 v[28:31], v[216:219], a[92:95], v[28:31]\n"
      "ds_write_b64 v252, v[254:255] offset:512\n"
      "v_mfma_f32_16x16x32_bf16 v[104:107], a[160:163], v[20:23], 0\n"
      "v_mfma_f32_16x16x32_bf16 v[104:107], a[164:167], v[100:103], v[104:107]\n"
      "v_mfma_f32_16x16x32_bf16 v[104:107], a[168:171], v[112:115], v[104:107]\n"
      "v_mfma_f32_16x16x32_bf16 v[104:107], a[172:175], v[216:219], v[104:107]\n"
      "ds_write_b64 v253, v[230:231] offset:128\n"
      "v_mfma_f32_16x16x32_bf16 v[224:227], v[20:23], a[96:99], 0\n"
      "s_add_u32 m0, s53, 4096\n"
      "v_mfma_f32_16x16x32_bf16 v[224:227], v[100:103], a[100:103], v[224:227]\n"
      "v_mfma_f32_16x16x32_bf16 v[224:227], v[112:115], a[104:107], v[224:227]\n"
      "v_cvt_pk_bf16_f32 v92, v60, v61\n"
      "v_mfma_f32_16x16x32_bf16 v[224:227], v[216:219], a[108:111], v[224:227]\n"
      "ds_write_b128 v251, v[52:55] offset:0\n"
      "buffer_load_dwordx4 v1, s[40:43], s92 offen offset:0 nt lds\n"
      "v_cvt_pk_bf16_f32 v89, v34, v35\n"
      "v_mfma_f32_16x16x32_bf16 v[154:157], v[20:23], a[112:115], 0\n"
      "v_mfma_f32_16x16x32_bf16 v[154:157], v[100:103], a[116:119], v[154:157]\n"
      "v_mfma_f32_16x16x32_bf16 v[154:157], v[112:115], a[120:123], v[154:157]\n"
      "v_mfma_f32_16x16x32_bf16 v[154:157], v[216:219], a[124:127], v[154:157]\n"
      "s_nop 0\n"
      "v_cvt_pk_bf16_f32 v254, v224, v225\n"
      "v_cvt_pk_bf16_f32 v54, v184, v185\n"
      "v_mfma_f32_16x16x32_bf16 v[200:203], v[20:23], a[64:67], 0\n"
      "buffer_load_dwordx4 v1, s[40:43], s92 offen offset:2048 nt lds\n"
      "v_mfma_f32_16x16x32_bf16 v[200:203], v[100:103], a[68:71], v[200:203]\n"
      "v_cvt_pk_bf16_f32 v164, v24, v160\n"
      "v_mfma_f32_16x16x32_bf16 v[200:203], v[112:115], a[72:75], v[200:203]\n"
      "v_cvt_pk_bf16_f32 v56, v27, v163\n"
      "v_cvt_pk_bf16_f32 v230, v148, v149\n"
      "v_mfma_f32_16x16x32_bf16 v[200:203], v[216:219], a[76:79], v[200:203]\n"
      "v_cvt_pk_bf16_f32 v184, v204, v205\n"
      "v_cvt_pk_bf16_f32 v165, v152, v40\n"
      "v_mfma_f32_16x16x32_bf16 v[208:211], a[0:3], v[20:23], 0\n"
      "s_waitcnt vmcnt(10)\n"
      "ds_read_b128 v[172:175], v0 offset:16384\n"
      "ds_read_b128 v[128:131], v0 offset:17408\n"
      "v_mfma_f32_16x16x32_bf16 v[208:211], a[4:7], v[100:103], v[208:211]\n"
      "v_mfma_f32_16x16x32_bf16 v[208:211], a[8:11], v[112:115], v[208:211]\n"
      "buffer_load_dwordx4 v1, s[40:43], s93 offen offset:1024 nt lds\n"
      "v_mfma_f32_16x16x32_bf16 v[208:211], a[12:15], v[216:219], v[208:211]\n"
      "v_mfma_f32_16x16x32_bf16 v[180:183], v[20:23], a[240:243], 0\n"
      "v_cvt_pk_bf16_f32 v138, v48, v49\n"
      "v_mfma_f32_16x16x32_bf16 v[180:183], v[100:103], a[244:247], v[180:183]\n"
      "v_cvt_pk_bf16_f32 v231, v150, v151\n"
      "v_cvt_pk_bf16_f32 v27, v156, v157\n"
      "v_mfma_f32_16x16x32_bf16 v[180:183], v[112:115], a[248:251], v[180:183]\n"
      "v_mfma_f32_16x16x32_bf16 v[180:183], v[216:219], a[252:255], v[180:183]\n"
      "v_cvt_pk_bf16_f32 v18, v26, v162\n"
      "v_cvt_pk_bf16_f32 v136, v44, v45\n"
      "v_mfma_f32_16x16x32_bf16 v[176:179], a[16:19], v[20:23], 0\n"
      "ds_read_b128 v[36:39], v0 offset:17920\n"
      "ds_read_b128 v[72:75], v0 offset:16896\n"
      "v_mfma_f32_16x16x32_bf16 v[176:179], a[20:23], v[100:103], v[176:179]\n"
      "v_mfma_f32_16x16x32_bf16 v[176:179], a[24:27], v[112:115], v[176:179]\n"
      "v_cvt_pk_bf16_f32 v98, v116, v117\n"
      "v_mfma_f32_16x16x32_bf16 v[176:179], a[28:31], v[216:219], v[176:179]\n"
      "ds_write_b64 v253, v[164:165] offset:4096\n"
      "v_mfma_f32_16x16x32_bf16 v[58:61], a[128:131], v[20:23], 0\n"
      "v_mfma_f32_16x16x32_bf16 v[58:61], a[132:135], v[100:103], v[58:61]\n"
      "v_cvt_pk_bf16_f32 v26, v154, v155\n"
      "v_mfma_f32_16x16x32_bf16 v[58:61], a[136:139], v[112:115], v[58:61]\n"
      "v_mfma_f32_16x16x32_bf16 v[58:61], a[140:143], v[216:219], v[58:61]\n"
      "ds_write_b64 v253, v[82:83] offset:0\n"
      "v_cvt_pk_bf16_f32 v91, v70, v71\n"
      "v_mfma_f32_16x16x32_bf16 v[154:157], a[48:51], v[20:23], 0\n"
      "v_mfma_f32_16x16x32_bf16 v[154:157], a[52:55], v[100:103], v[154:157]\n"
      "v_mfma_f32_16x16x32_bf16 v[154:157], a[56:59], v[112:115], v[154:157]\n"
      "buffer_load_dwordx4 v1, s[40:43], s93 offen offset:3072 nt lds\n"
      "v_mfma_f32_16x16x32_bf16 v[154:157], a[60:63], v[216:219], v[154:157]\n"
      "ds_write_b64 v253, v[26:27] offset:2432\n"
      "v_mfma_f32_16x16x32_bf16 v[162:165], v[20:23], a[224:227], 0\n"
      "v_cvt_pk_bf16_f32 v185, v84, v85\n"
      "s_add_u32 m0, s53, 0\n"
      "v_mfma_f32_16x16x32_bf16 v[162:165], v[100:103], a[228:231], v[162:165]\n"
      "v_cvt_pk_bf16_f32 v88, v32, v33\n"
      "v_mfma_f32_16x16x32_bf16 v[162:165], v[112:115], a[232:235], v[162:165]\n"
      "v_mfma_f32_16x16x32_bf16 v[162:165], v[216:219], a[236:239], v[162:165]\n"
      "ds_write_b64 v252, v[198:199] offset:0\n"
      "v_cvt_pk_bf16_f32 v26, v154, v155\n"
      "v_mfma_f32_16x16x32_bf16 v[82:85], a[32:35], v[20:23], 0\n"
      "buffer_load_dwordx4 v1, s[40:43], s94 offen offset:0 nt lds\n"
      "v_mfma_f32_16x16x32_bf16 v[82:85], a[36:39], v[100:103], v[82:85]\n"
      "v_mfma_f32_16x16x32_bf16 v[82:85], a[40:43], v[112:115], v[82:85]\n"
      "buffer_load_dwordx4 v1, s[40:43], s94 offen offset:2048 nt lds\n"
      "v_mfma_f32_16x16x32_bf16 v[82:85], a[44:47], v[216:219], v[82:85]\n"
      "v_cvt_pk_bf16_f32 v221, v210, v211\n"
      "v_cvt_pk_bf16_f32 v160, v206, v207\n"
      "v_mfma_f32_16x16x32_bf16 v[240:243], v[20:23], a[208:211], 0\n"
      "v_cvt_pk_bf16_f32 v137, v46, v47\n"
      "v_cvt_pk_bf16_f32 v255, v226, v227\n"
      "v_mfma_f32_16x16x32_bf16 v[240:243], v[100:103], a[212:215], v[240:243]\n"
      "v_cvt_pk_bf16_f32 v71, v165, v183\n"
      "v_cvt_pk_bf16_f32 v27, v156, v157\n"
      "v_mfma_f32_16x16x32_bf16 v[240:243], v[112:115], a[216:219], v[240:243]\n"
      "v_mfma_f32_16x16x32_bf16 v[240:243], v[216:219], a[220:223], v[240:243]\n"
      "ds_write_b64 v253, v[98:99] offset:256\n"
      "s_waitcnt lgkmcnt(8)\n"
      "v_mfma_f32_16x16x32_bf16 v[154:157], a[16:19], v[172:175], 0\n"
      "s_waitcnt lgkmcnt(5)\n"
      "v_mfma_f32_16x16x32_bf16 v[154:157], a[20:23], v[72:75], v[154:157]\n"
      "v_cvt_pk_bf16_f32 v110, v200, v201\n"
      "v_mfma_f32_16x16x32_bf16 v[154:157], a[24:27], v[128:131], v[154:157]\n"
      "v_mfma_f32_16x16x32_bf16 v[154:157], a[28:31], v[36:39], v[154:157]\n"
      "v_mfma_f32_16x16x32_bf16 v[42:45], a[144:147], v[20:23], 0\n"
      "buffer_load_dwordx4 v1, s[40:43], s95 offen offset:1024 nt lds\n"
      "v_mfma_f32_16x16x32_bf16 v[42:45], a[148:151], v[100:103], v[42:45]\n"
      "v_cvt_pk_bf16_f32 v222, v176, v177\n"
      "v_mfma_f32_16x16x32_bf16 v[42:45], a[152:155], v[112:115], v[42:45]\n"
      "v_cvt_pk_bf16_f32 v142, v236, v237\n"
      "v_cvt_pk_bf16_f32 v111, v202, v203\n"
      "v_mfma_f32_16x16x32_bf16 v[42:45], a[156:159], v[216:219], v[42:45]\n"
      "v_cvt_pk_bf16_f32 v183, v194, v195\n"
      "v_cvt_pk_bf16_f32 v95, v66, v67\n"
      "v_mfma_f32_16x16x32_bf16 v[200:203], a[144:147], v[172:175], 0\n"
      "v_cvt_pk_bf16_f32 v205, v164, v182\n"
      "v_cvt_pk_bf16_f32 v113, v153, v41\n"
      "v_mfma_f32_16x16x32_bf16 v[200:203], a[148:151], v[72:75], v[200:203]\n"
      "v_mfma_f32_16x16x32_bf16 v[200:203], a[152:155], v[128:131], v[200:203]\n"
      "v_mfma_f32_16x16x32_bf16 v[200:203], a[156:159], v[36:39], v[200:203]\n"
      "v_cvt_pk_bf16_f32 v139, v50, v51\n"
      "v_cvt_pk_bf16_f32 v93, v62, v63\n"
      "v_mfma_f32_16x16x32_bf16 v[20:23], v[172:175], a[112:115], 0\n"
      "v_cvt_pk_bf16_f32 v143, v238, v239\n"
      "v_cvt_pk_bf16_f32 v164, v28, v29\n"
      "v_mfma_f32_16x16x32_bf16 v[20:23], v[72:75], a[116:119], v[20:23]\n"
      "v_mfma_f32_16x16x32_bf16 v[20:23], v[128:131], a[120:123], v[20:23]\n"
      "v_cvt_pk_bf16_f32 v112, v25, v161\n"
      "v_mfma_f32_16x16x32_bf16 v[20:23], v[36:39], a[124:127], v[20:23]\n"
      "ds_write_b64 v253, v[112:113] offset:4224\n"
      "v_cvt_pk_bf16_f32 v66, v154, v155\n"
      "v_mfma_f32_16x16x32_bf16 v[236:239], a[48:51], v[172:175], 0\n"
      "s_waitcnt vmcnt(11)\n"
      "ds_read_b128 v[48:51], v0 offset:12288\n"
      "ds_read_b128 v[224:227], v0 offset:13824\n"
      "v_mfma_f32_16x16x32_bf16 v[236:239], a[52:55], v[72:75], v[236:239]\n"
      "buffer_load_dwordx4 v1, s[40:43], s95 offen offset:3072 nt lds\n"
      "v_mfma_f32_16x16x32_bf16 v[236:239], a[56:59], v[128:131], v[236:239]\n"
      "v_cvt_pk_bf16_f32 v55, v186, v187\n"
      "v_cvt_pk_bf16_f32 v161, v86, v87\n"
      "v_mfma_f32_16x16x32_bf16 v[236:239], a[60:63], v[36:39], v[236:239]\n"
      "s_add_u32 m0, s53, 20480\n"
      "ds_write_b128 v251, v[232:235] offset:2048\n"
      "s_add_u32 s40, s40, 13107200\n s_addc_u32 s41, s41, 0\n"
      "ds_read_b128 v[216:219], v0 offset:12800\n"
      "v_mfma_f32_16x16x32_bf16 v[112:115], a[160:163], v[172:175], 0\n"
      "v_mfma_f32_16x16x32_bf16 v[112:115], a[164:167], v[72:75], v[112:115]\n"
      "v_mfma_f32_16x16x32_bf16 v[112:115], a[168:171], v[128:131], v[112:115]\n"
      "buffer_load_dwordx4 v1, s[40:43], s73 offen offset:0 lds\n"
      "v_mfma_f32_16x16x32_bf16 v[112:115], a[172:175], v[36:39], v[112:115]\n"
      "v_cvt_pk_bf16_f32 v70, v81, v243\n"
      "v_cvt_pk_bf16_f32 v148, v79, v241\n"
      "v_mfma_f32_16x16x32_bf16 v[232:235], v[172:175], a[96:99], 0\n"
      "v_mfma_f32_16x16x32_bf16 v[232:235], v[72:75], a[100:103], v[232:235]\n"
      "v_cvt_pk_bf16_f32 v149, v163, v181\n"
      "v_cvt_pk_bf16_f32 v90, v68, v69\n"
      "v_mfma_f32_16x16x32_bf16 v[232:235], v[128:131], a[104:107], v[232:235]\n"
      "v_cvt_pk_bf16_f32 v103, v202, v203\n"
      "v_mfma_f32_16x16x32_bf16 v[232:235], v[36:39], a[108:111], v[232:235]\n"
      "ds_write_b64 v253, v[70:71] offset:6528\n"
      "v_mfma_f32_16x16x32_bf16 v[96:99], a[32:35], v[172:175], 0\n"
      "v_cvt_pk_bf16_f32 v203, v44, v45\n"
      "v_cvt_pk_bf16_f32 v67, v156, v157\n"
      "v_mfma_f32_16x16x32_bf16 v[96:99], a[36:39], v[72:75], v[96:99]\n"
      "buffer_load_dwordx4 v1, s[40:43], s74 offen offset:1024 lds\n"
      "v_mfma_f32_16x16x32_bf16 v[96:99], a[40:43], v[128:131], v[96:99]\n"
      "v_mfma_f32_16x16x32_bf16 v[96:99], a[44:47], v[36:39], v[96:99]\n"
      "v_cvt_pk_bf16_f32 v71, v238, v239\n"
      "v_cvt_pk_bf16_f32 v25, v84, v85\n"
      "v_mfma_f32_16x16x32_bf16 v[156:159], v[172:175], a[64:67], 0\n"
      "ds_read_b128 v[132:135], v0 offset:13312\n"
      "v_cvt_pk_bf16_f32 v116, v232, v233\n"
      "v_mfma_f32_16x16x32_bf16 v[156:159], v[72:75], a[68:71], v[156:159]\n"
      "v_cvt_pk_bf16_f32 v28, v106, v107\n"
      "v_cvt_pk_bf16_f32 v94, v64, v65\n"
      "v_mfma_f32_16x16x32_bf16 v[156:159], v[128:131], a[72:75], v[156:159]\n"
      "v_mfma_f32_16x16x32_bf16 v[156:159], v[36:39], a[76:79], v[156:159]\n"
      "ds_write_b64 v253, v[18:19] offset:4352\n"
      "v_mfma_f32_16x16x32_bf16 v[62:65], a[128:131], v[172:175], 0\n"
      "v_cvt_pk_bf16_f32 v141, v166, v167\n"
      "v_cvt_pk_bf16_f32 v238, v78, v240\n"
      "v_mfma_f32_16x16x32_bf16 v[62:65], a[132:135], v[72:75], v[62:65]\n"
      "v_cvt_pk_bf16_f32 v78, v58, v59\n"
      "v_cvt_pk_bf16_f32 v165, v30, v31\n"
      "v_mfma_f32_16x16x32_bf16 v[62:65], a[136:139], v[128:131], v[62:65]\n"
      "v_mfma_f32_16x16x32_bf16 v[62:65], a[140:143], v[36:39], v[62:65]\n"
      "v_cvt_pk_bf16_f32 v53, v158, v159\n"
      "v_cvt_pk_bf16_f32 v118, v228, v229\n"
      "v_mfma_f32_16x16x32_bf16 v[152:155], v[136:139], v[88:91], 0\n"
      "v_mfma_f32_16x16x32_bf16 v[152:155], v[140:143], v[92:95], v[152:155]\n"
      "ds_write_b64 v253, v[110:111] offset:2048\n"
      "s_waitcnt lgkmcnt(7)\n"
      "v_mfma_f32_16x16x32_bf16 v[84:87], v[48:51], a[112:115], 0\n"
      "v_cvt_pk_bf16_f32 v182, v104, v105\n"
      "s_waitcnt lgkmcnt(4)\n"
      "v_mfma_f32_16x16x32_bf16 v[84:87], v[216:219], a[116:119], v[84:87]\n"
      "v_cvt_pk_bf16_f32 v101, v64, v65\n"
      "s_waitcnt lgkmcnt(2)\n"
      "v_mfma_f32_16x16x32_bf16 v[84:87], v[132:135], a[120:123], v[84:87]\n"
      "v_cvt_pk_bf16_f32 v100, v62, v63\n"
      "v_mfma_f32_16x16x32_bf16 v[84:87], v[224:227], a[124:127], v[84:87]\n"
      "ds_write_b64 v252, v[182:183] offset:2304\n"
      "v_cvt_pk_bf16_f32 v69, v98, v99\n"
      "v_mfma_f32_16x16x32_bf16 v[44:47], a[32:35], v[48:51], 0\n"
      "v_mfma_f32_16x16x32_bf16 v[44:47], a[36:39], v[216:219], v[44:47]\n"
      "v_cvt_pk_bf16_f32 v228, v20, v21\n"
      "v_mfma_f32_16x16x32_bf16 v[44:47], a[40:43], v[132:135], v[44:47]\n"
      "v_cvt_pk_bf16_f32 v52, v156, v157\n"
      "v_mfma_f32_16x16x32_bf16 v[44:47], a[44:47], v[224:227], v[44:47]\n"
      "ds_write_b64 v252, v[160:161] offset:2560\n"
      "v_mfma_f32_16x16x32_bf16 v[18:21], v[48:51], a[96:99], 0\n"
      "v_cvt_pk_bf16_f32 v117, v234, v235\n"
      "v_mfma_f32_16x16x32_bf16 v[18:21], v[216:219], a[100:103], v[18:21]\n"
      "v_cvt_pk_bf16_f32 v202, v60, v61\n"
      "v_cvt_pk_bf16_f32 v239, v162, v180\n"
      "v_mfma_f32_16x16x32_bf16 v[18:21], v[132:135], a[104:107], v[18:21]\n"
      "buffer_load_dwordx4 v1, s[40:43], s74 offen offset:3072 lds\n"
      "v_mfma_f32_16x16x32_bf16 v[18:21], v[224:227], a[108:111], v[18:21]\n"
      "ds_write_b64 v253, v[148:149] offset:6272\n"
      "v_cvt_pk_bf16_f32 v104, v112, v113\n"
      "v_mfma_f32_16x16x32_bf16 v[160:163], a[0:3], v[172:175], 0\n"
      "v_mfma_f32_16x16x32_bf16 v[160:163], a[4:7], v[72:75], v[160:163]\n"
      "v_mfma_f32_16x16x32_bf16 v[160:163], a[8:11], v[128:131], v[160:163]\n"
      "v_cvt_pk_bf16_f32 v102, v200, v201\n"
      "v_mfma_f32_16x16x32_bf16 v[160:163], a[12:15], v[36:39], v[160:163]\n"
      "ds_write_b64 v253, v[56:57] offset:4480\n"
      "v_mfma_f32_16x16x32_bf16 v[156:159], v[172:175], a[80:83], 0\n"
      "v_mfma_f32_16x16x32_bf16 v[156:159], v[72:75], a[84:87], v[156:159]\n"
      "v_cvt_pk_bf16_f32 v81, v46, v47\n"
      "v_mfma_f32_16x16x32_bf16 v[156:159], v[128:131], a[88:91], v[156:159]\n"
      "v_mfma_f32_16x16x32_bf16 v[156:159], v[36:39], a[92:95], v[156:159]\n"
      "ds_write_b64 v252, v[184:185] offset:2048\n"
      "ds_read_b128 v[232:235], v9 offset:0\n"
      "v_cvt_pk_bf16_f32 v70, v236, v237\n"
      "v_mfma_f32_16x16x32_bf16 v[60:63], a[176:179], v[172:175], 0\n"
      "v_exp_f32 v155, v155\n"
      "v_mfma_f32_16x16x32_bf16 v[60:63], a[180:183], v[72:75], v[60:63]\n"
      "v_mfma_f32_16x16x32_bf16 v[60:63], a[184:187], v[128:131], v[60:63]\n"
      "v_cvt_pk_bf16_f32 v68, v96, v97\n"
      "v_mfma_f32_16x16x32_bf16 v[60:63], a[188:191], v[36:39], v[60:63]\n"
      "ds_write_b64 v252, v[202:203] offset:768\n"
      "v_mfma_f32_16x16x32_bf16 v[56:59], v[48:51], a[64:67], 0\n"
      "s_waitcnt vmcnt(11)\n"
      "ds_read_b128 v[192:195], v0 offset:8192\n"
      "ds_read_b128 v[184:187], v0 offset:9216\n"
      "v_mfma_f32_16x16x32_bf16 v[56:59], v[216:219], a[68:71], v[56:59]\n"
      "v_cvt_pk_bf16_f32 v204, v80, v242\n"
      "v_cvt_pk_bf16_f32 v120, v156, v157\n"
      "v_mfma_f32_16x16x32_bf16 v[56:59], v[132:135], a[72:75], v[56:59]\n"
      "v_mfma_f32_16x16x32_bf16 v[56:59], v[224:227], a[76:79], v[56:59]\n"
      "ds_read_b128 v[240:243], v0 offset:9728\n"
      "ds_read_b128 v[200:203], v0 offset:8704\n"
      "v_mfma_f32_16x16x32_bf16 v[110:113], a[48:51], v[48:51], 0\n"
      "v_cvt_pk_bf16_f32 v65, v162, v163\n"
      "v_cvt_pk_bf16_f32 v29, v196, v197\n"
      "v_mfma_f32_16x16x32_bf16 v[110:113], a[52:55], v[216:219], v[110:113]\n"
      "v_cvt_pk_bf16_f32 v106, v60, v61\n"
      "v_cvt_pk_bf16_f32 v107, v62, v63\n"
      "v_mfma_f32_16x16x32_bf16 v[110:113], a[56:59], v[132:135], v[110:113]\n"
      "v_cvt_pk_bf16_f32 v64, v160, v161\n"
      "v_mfma_f32_16x16x32_bf16 v[110:113], a[60:63], v[224:227], v[110:113]\n"
      "ds_write_b64 v252, v[28:29] offset:2816\n"
      "v_cvt_pk_bf16_f32 v220, v208, v209\n"
      "v_mfma_f32_16x16x32_bf16 v[180:183], a[16:19], v[48:51], 0\n"
      "v_mfma_f32_16x16x32_bf16 v[180:183], a[20:23], v[216:219], v[180:183]\n"
      "v_mfma_f32_16x16x32_bf16 v[180:183], a[24:27], v[132:135], v[180:183]\n"
      "v_mfma_f32_16x16x32_bf16 v[180:183], a[28:31], v[224:227], v[180:183]\n"
      "ds_read_b128 v[32:35], v10 offset:0\n"
      "v_cvt_pk_bf16_f32 v24, v82, v83\n"
      "v_mfma_f32_16x16x32_bf16 v[160:163], v[64:67], v[88:91], 0\n"
      "v_exp_f32 v152, v152\n"
      "v_mfma_f32_16x16x32_bf16 v[160:163], v[68:71], v[92:95], v[160:163]\n"
      "v_exp_f32 v154, v154\n"
      "v_mfma_f32_16x16x32_bf16 v[96:99], a[0:3], v[48:51], 0\n"
      "v_cvt_pk_bf16_f32 v63, v20, v21\n"
      "v_mov_b64 v[144:145], 0\n"
      "v_mfma_f32_16x16x32_bf16 v[96:99], a[4:7], v[216:219], v[96:99]\n"
      "v_exp_f32 v153, v153\n"
      "v_mfma_f32_16x16x32_bf16 v[96:99], a[8:11], v[132:135], v[96:99]\n"
      "buffer_load_dwordx4 v1, s[40:43], s73 offen offset:2048 lds\n"
      "v_mfma_f32_16x16x32_bf16 v[96:99], a[12:15], v[224:227], v[96:99]\n"
      "ds_write_b64 v253, v[164:165] offset:2176\n"
      "s_waitcnt lgkmcnt(6)\n"
      "v_mfma_f32_16x16x32_bf16 v[166:169], a[48:51], v[192:195], 0\n"
      "s_waitcnt lgkmcnt(3)\n"
      "v_mfma_f32_16x16x32_bf16 v[166:169], a[52:55], v[200:203], v[166:169]\n"
      "v_cvt_pk_bf16_f32 v122, v76, v77\n"
      "v_mfma_f32_16x16x32_bf16 v[166:169], a[56:59], v[184:187], v[166:169]\n"
      "s_nop 0\n"
      "v_cvt_pk_bf16_f32 v77, v98, v99\n"
      "v_cvt_pk_bf16_f32 v223, v178, v179\n"
      "v_mfma_f32_16x16x32_bf16 v[166:169], a[60:63], v[240:243], v[166:169]\n"
      "s_add_u32 m0, s53, 16384\n"
      "ds_write_b64 v253, v[238:239] offset:6144\n"
      "v_cvt_pk_bf16_f32 v79, v42, v43\n"
      "v_mfma_f32_16x16x32_bf16 v[28:31], a[0:3], v[192:195], 0\n"
      "ds_read_b128 v[236:239], v9 offset:2048\n"
      "v_cvt_pk_bf16_f32 v121, v158, v159\n"
      "v_mfma_f32_16x16x32_bf16 v[28:31], a[4:7], v[200:203], v[28:31]\n"
      "v_mfma_f32_16x16x32_bf16 v[28:31], a[8:11], v[184:187], v[28:31]\n"
      "buffer_load_dwordx4 v1, s[40:43], s76 offen offset:1024 lds\n"
      "v_mfma_f32_16x16x32_bf16 v[28:31], a[12:15], v[240:243], v[28:31]\n"
      "ds_write_b64 v252, v[78:79] offset:256\n"
      "v_mfma_f32_16x16x32_bf16 v[208:211], a[32:35], v[192:195], 0\n"
      "v_cvt_pk_bf16_f32 v76, v96, v97\n"
      "v_mfma_f32_16x16x32_bf16 v[208:211], a[36:39], v[200:203], v[208:211]\n"
      "v_cvt_pk_bf16_f32 v82, v110, v111\n"
      "v_cvt_pk_bf16_f32 v80, v44, v45\n"
      "v_mfma_f32_16x16x32_bf16 v[208:211], a[40:43], v[184:187], v[208:211]\n"
      "v_mfma_f32_16x16x32_bf16 v[208:211], a[44:47], v[240:243], v[208:211]\n"
      "ds_write_b128 v251, v[24:27] offset:2304\n"
      "v_cvt_pk_bf16_f32 v20, v28, v29\n"
      "v_exp_f32 v160, v160\n"
      "v_mfma_f32_16x16x32_bf16 v[156:159], v[192:195], a[96:99], 0\n"
      "v_exp_f32 v163, v163\n"
      "v_mfma_f32_16x16x32_bf16 v[156:159], v[200:203], a[100:103], v[156:159]\n"
      "v_mfma_f32_16x16x32_bf16 v[156:159], v[184:187], a[104:107], v[156:159]\n"
      "v_exp_f32 v161, v161\n"
      "v_mfma_f32_16x16x32_bf16 v[156:159], v[240:243], a[108:111], v[156:159]\n"
      "v_mfma_f32_16x16x32_bf16 v[176:179], a[16:19], v[192:195], 0\n"
      "v_cvt_pk_bf16_f32 v25, v210, v211\n"
      "v_mfma_f32_16x16x32_bf16 v[176:179], a[20:23], v[200:203], v[176:179]\n"
      "v_cvt_pk_bf16_f32 v26, v166, v167\n"
      "v_cvt_pk_bf16_f32 v105, v114, v115\n"
      "v_mfma_f32_16x16x32_bf16 v[176:179], a[24:27], v[184:187], v[176:179]\n"
      "v_mfma_f32_16x16x32_bf16 v[176:179], a[28:31], v[240:243], v[176:179]\n"
      "v_cvt_pk_bf16_f32 v79, v182, v183\n"
      "v_cvt_pk_bf16_f32 v21, v30, v31\n"
      "v_mfma_f32_16x16x32_bf16 v[44:47], v[192:195], a[112:115], 0\n"
      "v_cvt_pk_bf16_f32 v62, v18, v19\n"
      "v_mfma_f32_16x16x32_bf16 v[44:47], v[200:203], a[116:119], v[44:47]\n"
      "s_waitcnt vmcnt(9)\n"
      "ds_read_b128 v[40:43], v0 offset:4096\n"
      "ds_read_b128 v[96:99], v0 offset:5632\n"
      "v_mfma_f32_16x16x32_bf16 v[44:47], v[184:187], a[120:123], v[44:47]\n"
      "v_mfma_f32_16x16x32_bf16 v[44:47], v[240:243], a[124:127], v[44:47]\n"
      "ds_write_b64 v253, v[254:255] offset:2304\n"
      "s_waitcnt lgkmcnt(5)\n"
      "v_smfmac_f32_16x16x64_bf16 v[144:147], v[32:35], v[232:239], v5\n"
      "ds_read_b128 v[108:111], v0 offset:4608\n"
      "v_cvt_pk_bf16_f32 v229, v22, v23\n"
      "v_mfma_f32_16x16x32_bf16 v[196:199], v[192:195], a[80:83], 0\n"
      "ds_read_b128 v[164:167], v0 offset:5120\n"
      "v_cvt_pk_bf16_f32 v24, v208, v209\n"
      "v_mfma_f32_16x16x32_bf16 v[196:199], v[200:203], a[84:87], v[196:199]\n"
      "buffer_load_dwordx4 v1, s[40:43], s76 offen offset:3072 lds\n"
      "v_mfma_f32_16x16x32_bf16 v[196:199], v[184:187], a[88:91], v[196:199]\n"
      "v_cvt_pk_bf16_f32 v22, v176, v177\n"
      "v_mfma_f32_16x16x32_bf16 v[196:199], v[240:243], a[92:95], v[196:199]\n"
      "v_cvt_pk_bf16_f32 v237, v46, v47\n"
      "v_mfma_f32_16x16x32_bf16 v[208:211], v[192:195], a[64:67], 0\n"
      "v_exp_f32 v162, v162\n"
      "v_mfma_f32_16x16x32_bf16 v[208:211], v[200:203], a[68:71], v[208:211]\n"
      "v_cvt_pk_bf16_f32 v176, v160, v161\n"
      "v_cvt_pk_bf16_f32 v23, v178, v179\n"
      "v_mfma_f32_16x16x32_bf16 v[208:211], v[184:187], a[72:75], v[208:211]\n"
      "v_mov_b64 v[192:193], 0\n"
      "v_cvt_pk_bf16_f32 v60, v156, v157\n"
      "v_mfma_f32_16x16x32_bf16 v[208:211], v[240:243], a[76:79], v[208:211]\n"
      "ds_write_b64 v253, v[204:205] offset:6400\n"
      "v_mov_b64 v[28:29], 0\n"
      "v_mfma_f32_16x16x32_bf16 v[232:235], v[48:51], a[80:83], 0\n"
      "v_mfma_f32_16x16x32_bf16 v[232:235], v[216:219], a[84:87], v[232:235]\n"
      "v_cvt_pk_bf16_f32 v61, v158, v159\n"
      "v_cvt_pk_bf16_f32 v177, v162, v163\n"
      "v_mfma_f32_16x16x32_bf16 v[232:235], v[132:135], a[88:91], v[232:235]\n"
      "v_mov_b64 v[202:203], 0\n"
      "v_mfma_f32_16x16x32_bf16 v[232:235], v[224:227], a[92:95], v[232:235]\n"
      "v_cvt_pk_bf16_f32 v115, v58, v59\n"
      "v_add_f32 v144, v144, v145\n"
      "v_smfmac_f32_16x16x64_bf16 v[202:205], v[32:35], v[136:143], v5\n"
      "s_waitcnt lgkmcnt(5)\n"
      "v_mfma_f32_16x16x32_bf16 v[160:163], a[32:35], v[40:43], 0\n"
      "v_cvt_pk_bf16_f32 v179, v154, v155\n"
      "s_waitcnt lgkmcnt(2)\n"
      "v_mfma_f32_16x16x32_bf16 v[160:163], a[36:39], v[108:111], v[160:163]\n"
      "s_waitcnt lgkmcnt(1)\n"
      "v_mfma_f32_16x16x32_bf16 v[160:163], a[40:43], v[164:167], v[160:163]\n"
      "v_cvt_pk_bf16_f32 v27, v168, v169\n"
      "v_mfma_f32_16x16x32_bf16 v[160:163], a[44:47], v[96:99], v[160:163]\n"
      "v_mov_b32 v145, 0\n"
      "v_cvt_pk_bf16_f32 v187, v234, v235\n"
      "v_mfma_f32_16x16x32_bf16 v[252:255], v[40:43], a[112:115], 0\n"
      "v_mfma_f32_16x16x32_bf16 v[252:255], v[108:111], a[116:119], v[252:255]\n"
      "v_add_u32 v10, 64, v10\n"
      "v_mfma_f32_16x16x32_bf16 v[252:255], v[164:167], a[120:123], v[252:255]\n"
      "v_cvt_pk_bf16_f32 v78, v180, v181\n"
      "v_cvt_pk_bf16_f32 v83, v112, v113\n"
      "v_mfma_f32_16x16x32_bf16 v[252:255], v[96:99], a[124:127], v[252:255]\n"
      "v_exp_f32_dpp v145, v144 quad_perm:[0,1,2,3] row_mask:0xf bank_mask:0x1\n"
      "v_mfma_f32_16x16x32_bf16 v[154:157], v[76:79], v[88:91], 0\n"
      "v_cvt_pk_bf16_f32 v239, v86, v87\n"
      "v_mfma_f32_16x16x32_bf16 v[154:157], v[80:83], v[92:95], v[154:157]\n"
      "v_cvt_pk_bf16_f32 v114, v56, v57\n"
      "v_smfmac_f32_16x16x64_bf16 v[192:195], v[32:35], v[64:71], v5\n"
      "buffer_load_dwordx4 v1, s[40:43], s75 offen offset:0 lds\n"
      "v_mfma_f32_16x16x32_bf16 v[168:171], a[48:51], v[40:43], 0\n"
      "v_cvt_pk_bf16_f32 v234, v252, v253\n"
      "v_cvt_pk_bf16_f32 v112, v208, v209\n"
      "v_mfma_f32_16x16x32_bf16 v[168:171], a[52:55], v[108:111], v[168:171]\n"
      "s_nop 0\n"
      "v_exp_f32 v157, v157\n"
      "v_mfma_f32_16x16x32_bf16 v[168:171], a[56:59], v[164:167], v[168:171]\n"
      "v_cvt_pk_bf16_f32 v185, v198, v199\n"
      "v_cvt_pk_bf16_f32 v184, v196, v197\n"
      "v_mfma_f32_16x16x32_bf16 v[168:171], a[60:63], v[96:99], v[168:171]\n"
      "v_mov_b64 v[146:147], 0\n"
      "v_mfma_f32_16x16x32_bf16 v[56:59], v[20:23], v[88:91], 0\n"
      "v_cvt_pk_bf16_f32 v236, v44, v45\n"
      "v_mfma_f32_16x16x32_bf16 v[56:59], v[24:27], v[92:95], v[56:59]\n"
      "ds_write_b128 v251, v[220:223] offset:256\n"
      "v_cvt_pk_bf16_f32 v208, v160, v161\n"
      "v_cvt_pk_bf16_f32 v209, v162, v163\n"
      "v_mfma_f32_16x16x32_bf16 v[160:163], v[40:43], a[80:83], 0\n"
      "v_cvt_pk_bf16_f32 v113, v210, v211\n"
      "v_cvt_pk_bf16_f32 v238, v84, v85\n"
      "v_mfma_f32_16x16x32_bf16 v[160:163], v[108:111], a[84:87], v[160:163]\n"
      "v_cvt_pk_bf16_f32 v210, v168, v169\n"
      "v_cvt_pk_bf16_f32 v235, v254, v255\n"
      "v_mfma_f32_16x16x32_bf16 v[160:163], v[164:167], a[88:91], v[160:163]\n"
      "v_mfma_f32_16x16x32_bf16 v[160:163], v[96:99], a[92:95], v[160:163]\n"
      "v_exp_f32 v56, v56\n"
      "v_mfma_f32_16x16x32_bf16 v[194:197], v[40:43], a[64:67], 0\n"
      "v_exp_f32 v58, v58\n"
      "v_mfma_f32_16x16x32_bf16 v[194:197], v[108:111], a[68:71], v[194:197]\n"
      "v_exp_f32 v59, v59\n"
      "v_mfma_f32_16x16x32_bf16 v[194:197], v[164:167], a[72:75], v[194:197]\n"
      "v_mfma_f32_16x16x32_bf16 v[194:197], v[96:99], a[76:79], v[194:197]\n"
      "v_exp_f32 v154, v154\n"
      "v_mfma_f32_16x16x32_bf16 v[84:87], a[0:3], v[40:43], 0\n"
      "v_cvt_pk_bf16_f32 v186, v232, v233\n"
      "v_add_f32 v192, v192, v193\n"
      "v_mfma_f32_16x16x32_bf16 v[84:87], a[4:7], v[108:111], v[84:87]\n"
      "v_add_f32 v202, v202, v203\n"
      "v_mfma_f32_16x16x32_bf16 v[84:87], a[8:11], v[164:167], v[84:87]\n"
      "buffer_load_dwordx4 v1, s[40:43], s75 offen offset:2048 lds\n"
      "v_mfma_f32_16x16x32_bf16 v[84:87], a[12:15], v[96:99], v[84:87]\n"
      "v_cvt_pk_bf16_f32 v178, v152, v153\n"
      "v_smfmac_f32_16x16x64_bf16 v[28:31], v[32:35], v[76:83], v5\n"
      "s_waitcnt vmcnt(8)\n"
      "ds_read_b128 v[44:47], v0 offset:0\n"
      "v_mfma_f32_16x16x32_bf16 v[180:183], v[40:43], a[96:99], 0\n"
      "L_h1_%=:\n"
      "s_cbranch_execz L_hr_%=\n"
      "ds_read_b128 v[248:251], v0 offset:512\n"
      "ds_read_b128 v[252:255], v0 offset:1024\n"
      "v_mfma_f32_16x16x32_bf16 v[180:183], v[108:111], a[100:103], v[180:183]\n"
      "v_exp_f32 v57, v57\n"
      "v_mfma_f32_16x16x32_bf16 v[180:183], v[164:167], a[104:107], v[180:183]\n"
      "ds_read_b128 v[240:243], v0 offset:1536\n"
      "v_cvt_pk_bf16_f32 v211, v170, v171\n"
      "v_mfma_f32_16x16x32_bf16 v[180:183], v[96:99], a[108:111], v[180:183]\n"
      "v_exp_f32 v156, v156\n"
      "v_smfmac_f32_16x16x64_bf16 v[146:149], v[32:35], v[20:27], v5\n"
      "v_add_f32 v28, v28, v29\n"
      "v_mfma_f32_16x16x32_bf16 v[168:171], a[16:19], v[40:43], 0\n"
      "v_mfma_f32_16x16x32_bf16 v[168:171], a[20:23], v[108:111], v[168:171]\n"
      "v_exp_f32 v202, v202\n"
      "v_mfma_f32_16x16x32_bf16 v[168:171], a[24:27], v[164:167], v[168:171]\n"
      "v_exp_f32 v28, v28\n"
      "v_mfma_f32_16x16x32_bf16 v[168:171], a[28:31], v[96:99], v[168:171]\n"
      "v_cvt_pk_bf16_f32 v99, v156, v157\n"
      "s_waitcnt lgkmcnt(3)\n"
      "v_mfma_f32_16x16x32_bf16 v[108:111], v[44:47], a[112:115], 0\n"
      "s_waitcnt lgkmcnt(2)\n"
      "v_mfma_f32_16x16x32_bf16 v[108:111], v[248:251], a[116:119], v[108:111]\n"
      "v_cvt_pk_bf16_f32 v97, v58, v59\n"
      "s_waitcnt lgkmcnt(1)\n"
      "v_mfma_f32_16x16x32_bf16 v[108:111], v[252:255], a[120:123], v[108:111]\n"
      "s_waitcnt lgkmcnt(0)\n"
      "v_mfma_f32_16x16x32_bf16 v[108:111], v[240:243], a[124:127], v[108:111]\n"
      "v_cvt_pk_bf16_f32 v204, v84, v85\n"
      "v_cvt_pk_bf16_f32 v205, v86, v87\n"
      "v_mfma_f32_16x16x32_bf16 v[40:43], a[32:35], v[44:47], 0\n"
      "v_mfma_f32_16x16x32_bf16 v[40:43], a[36:39], v[248:251], v[40:43]\n"
      "v_exp_f32 v192, v192\n"
      "v_mfma_f32_16x16x32_bf16 v[40:43], a[40:43], v[252:255], v[40:43]\n"
      "v_mfma_f32_16x16x32_bf16 v[40:43], a[44:47], v[240:243], v[40:43]\n"
      "v_cvt_pk_bf16_f32 v206, v168, v169\n"
      "v_cvt_pk_bf16_f32 v207, v170, v171\n"
      "v_mfma_f32_16x16x32_bf16 v[164:167], a[48:51], v[44:47], 0\n"
      "s_add_u32 m0, s53, 12288\n"
      "v_exp_f32 v155, v155\n"
      "v_mfma_f32_16x16x32_bf16 v[164:167], a[52:55], v[248:251], v[164:167]\n"
      "s_nop 1\n"
      "v_cvt_pk_bf16_f32 v169, v42, v43\n"
      "v_mfma_f32_16x16x32_bf16 v[164:167], a[56:59], v[252:255], v[164:167]\n"
      "v_cvt_pk_bf16_f32 v98, v154, v155\n"
      "v_cvt_pk_bf16_f32 v232, v108, v109\n"
      "v_mfma_f32_16x16x32_bf16 v[164:167], a[60:63], v[240:243], v[164:167]\n"
      "v_cvt_pk_bf16_f32 v96, v56, v57\n"
      "v_cvt_pk_bf16_f32 v233, v110, v111\n"
      "v_mfma_f32_16x16x32_bf16 v[156:159], a[16:19], v[44:47], 0\n"
      "buffer_load_dwordx4 v1, s[40:43], s77 offen offset:0 lds\n"
      "v_mfma_f32_16x16x32_bf16 v[156:159], a[20:23], v[248:251], v[156:159]\n"
      "v_mov_b32_dpp v144, v145 quad_perm:[1,1,3,3] row_mask:0xf bank_mask:0xf\n"
      "v_cvt_pk_bf16_f32 v111, v196, v197\n"
      "v_mfma_f32_16x16x32_bf16 v[156:159], a[24:27], v[252:255], v[156:159]\n"
      "v_cvt_pk_bf16_f32 v59, v182, v183\n"
      "v_mfma_f32_16x16x32_bf16 v[156:159], a[28:31], v[240:243], v[156:159]\n"
      "v_cvt_pk_bf16_f32 v168, v40, v41\n"
      "v_cvt_pk_bf16_f32 v110, v194, v195\n"
      "v_mfma_f32_16x16x32_bf16 v[196:199], a[0:3], v[44:47], 0\n"
      "v_cvt_pk_bf16_f32 v58, v180, v181\n"
      "v_mfma_f32_16x16x32_bf16 v[196:199], a[4:7], v[248:251], v[196:199]\n"
      "v_mfma_f32_16x16x32_bf16 v[196:199], a[8:11], v[252:255], v[196:199]\n"
      "v_cvt_pk_bf16_f32 v183, v162, v163\n"
      "v_cvt_pk_bf16_f32 v182, v160, v161\n"
      "v_mfma_f32_16x16x32_bf16 v[196:199], a[12:15], v[240:243], v[196:199]\n"
      "v_cvt_pk_bf16_f32 v171, v166, v167\n"
      "v_cvt_pk_bf16_f32 v166, v156, v157\n"
      "v_mfma_f32_16x16x32_bf16 v[160:163], v[44:47], a[64:67], 0\n"
      "v_cvt_pk_bf16_f32 v167, v158, v159\n"
      "v_cvt_pk_bf16_f32 v170, v164, v165\n"
      "v_mfma_f32_16x16x32_bf16 v[160:163], v[248:251], a[68:71], v[160:163]\n"
      "v_mov_b64 v[156:157], 0\n"
      "v_cvt_pk_bf16_f32 v144, v145, v144\n"
      "v_mfma_f32_16x16x32_bf16 v[160:163], v[252:255], a[72:75], v[160:163]\n"
      "v_cndmask_b32_dpp v193, v202, v192, vcc quad_perm:[0,0,2,2] row_mask:0xf bank_mask:0xf\n"
      "v_cvt_pk_bf16_f32 v164, v196, v197\n"
      "v_mfma_f32_16x16x32_bf16 v[160:163], v[240:243], a[76:79], v[160:163]\n"
      "v_mov_b64 v[84:85], 0\n"
      "v_cvt_pk_bf16_f32 v165, v198, v199\n"
      "v_smfmac_f32_16x16x64_bf16 v[156:159], v[32:35], v[204:211], v5\n"
      "ds_read2_b32 v[222:223], v16 offset0:128 offset1:130\n"
      "ds_read2_b32 v[220:221], v16 offset0:0 offset1:2\n"
      "v_smfmac_f32_16x16x64_bf16 v[84:87], v[32:35], v[164:171], v5\n"
      "s_nop 1\n"
      "v_cvt_pk_bf16_f32 v108, v160, v161\n"
      "v_mfma_f32_16x16x32_bf16 v[196:199], v[44:47], a[80:83], 0\n"
      "v_add_f32 v146, v146, v147\n"
      "v_cvt_pk_bf16_f32 v109, v162, v163\n"
      "v_mfma_f32_16x16x32_bf16 v[196:199], v[248:251], a[84:87], v[196:199]\n"
      "v_add_f32 v156, v156, v157\n"
      "v_add_f32 v84, v84, v85\n"
      "v_mfma_f32_16x16x32_bf16 v[196:199], v[252:255], a[88:91], v[196:199]\n"
      "v_exp_f32 v146, v146\n"
      "v_mfma_f32_16x16x32_bf16 v[196:199], v[240:243], a[92:95], v[196:199]\n"
      "buffer_load_dwordx4 v1, s[40:43], s77 offen offset:2048 lds\n"
      "v_mfma_f32_16x16x32_bf16 v[158:161], v[44:47], a[96:99], 0\n"
      "v_exp_f32 v84, v84\n"
      "v_mfma_f32_16x16x32_bf16 v[158:161], v[248:251], a[100:103], v[158:161]\n"
      "v_exp_f32 v156, v156\n"
      "v_mfma_f32_16x16x32_bf16 v[158:161], v[252:255], a[104:107], v[158:161]\n"
      "s_nop 0\n"
      "v_cndmask_b32_dpp v157, v28, v156, vcc quad_perm:[0,0,2,2] row_mask:0xf bank_mask:0xf\n"
      "v_cndmask_b32_dpp v85, v146, v84, vcc quad_perm:[0,0,2,2] row_mask:0xf bank_mask:0xf\n"
      "v_mfma_f32_16x16x32_bf16 v[158:161], v[240:243], a[108:111], v[158:161]\n"
      "s_not_b64 vcc, vcc\n"
      "v_cndmask_b32_dpp v203, v192, v202, vcc quad_perm:[1,1,3,3] row_mask:0xf bank_mask:0xf\n"
      "v_mov_b64 v[248:249], 0\n"
      "v_cndmask_b32_dpp v147, v84, v146, vcc quad_perm:[1,1,3,3] row_mask:0xf bank_mask:0xf\n"
      "v_cvt_pk_bf16_f32 v193, v193, v203\n"
      "v_mfma_f32_16x16x32_bf16 v[148:151], v[164:167], v[88:91], 0\n"
      "v_cndmask_b32_dpp v29, v156, v28, vcc quad_perm:[1,1,3,3] row_mask:0xf bank_mask:0xf\n"
      "v_cvt_pk_bf16_f32 v180, v196, v197\n"
      "v_mfma_f32_16x16x32_bf16 v[148:151], v[168:171], v[92:95], v[148:151]\n"
      "s_waitcnt lgkmcnt(0)\n"
      "v_mov_b32_dpp v44, v220 row_ror:8 row_mask:0xf bank_mask:0xf\n"
      "v_add_f32 v192, v192, v202\n"
      "v_mov_b64 v[250:251], 0\n"
      "v_mfma_f32_16x16x32_bf16 v[240:243], v[164:167], v[100:103], 0\n"
      "v_add_f32 v84, v84, v156\n"
      "v_cvt_pk_bf16_f32 v157, v157, v29\n"
      "v_mfma_f32_16x16x32_bf16 v[240:243], v[168:171], v[104:107], v[240:243]\n"
      "v_add_f32 v192, v192, v145\n"
      "v_mov_b32_dpp v45, v221 row_ror:8 row_mask:0xf bank_mask:0xf\n"
      "v_mov_b32_dpp v47, v223 row_ror:8 row_mask:0xf bank_mask:0xf\n"
      "v_mfma_f32_16x16x32_bf16 v[152:155], v[204:207], v[88:91], 0\n"
      "v_cvt_pk_bf16_f32 v56, v158, v159\n"
      "v_add_f32 v146, v146, v28\n"
      "v_mfma_f32_16x16x32_bf16 v[152:155], v[208:211], v[92:95], v[152:155]\n"
      "v_cvt_pk_bf16_f32 v85, v85, v147\n"
      "v_exp_f32 v242, v242\n"
      "v_add_f32 v84, v84, v146\n"
      "v_mov_b32_dpp v46, v222 row_ror:8 row_mask:0xf bank_mask:0xf\n"
      "v_smfmac_f32_16x16x64_bf16 v[248:251], v[220:223], v[88:95], v5\n"
      "ds_bpermute_b32 v43, v6, v157 offset:16\n"
      "v_smfmac_f32_16x16x64_bf16 v[248:251], v[44:47], v[100:107], v5\n"
      "ds_bpermute_b32 v255, v7, v193 offset:4\n"
      "v_exp_f32 v240, v240\n"
      "v_mfma_f32_16x16x32_bf16 v[32:35], v[76:79], v[100:103], 0\n"
      "v_exp_f32 v243, v243\n"
      "v_mfma_f32_16x16x32_bf16 v[32:35], v[80:83], v[104:107], v[32:35]\n"
      "v_exp_f32 v148, v148\n"
      "v_mfma_f32_16x16x32_bf16 v[200:203], v[204:207], v[100:103], 0\n"
      "ds_bpermute_b32 v41, v6, v157\n"
      "v_mfma_f32_16x16x32_bf16 v[200:203], v[208:211], v[104:107], v[200:203]\n"
      "ds_bpermute_b32 v254, v7, v193\n"
      "v_add_f32 v90, v84, v192\n"
      "v_mfma_f32_16x16x32_bf16 v[156:159], v[136:139], v[100:103], 0\n"
      "s_nop 0\n"
      "v_exp_f32 v35, v35\n"
      "v_mfma_f32_16x16x32_bf16 v[156:159], v[140:143], v[104:107], v[156:159]\n"
      "v_exp_f32 v34, v34\n"
      "v_mfma_f32_16x16x32_bf16 v[192:195], v[20:23], v[100:103], 0\n"
      "ds_bpermute_b32 v40, v6, v85\n"
      "v_mfma_f32_16x16x32_bf16 v[192:195], v[24:27], v[104:107], v[192:195]\n"
      "v_add_f32 v250, v250, v251\n"
      "v_exp_f32 v201, v201\n"
      "v_exp_f32 v149, v149\n"
      "v_cvt_pk_bf16_f32 v57, v160, v161\n"
      "v_mfma_f32_16x16x32_bf16 v[92:95], a[128:131], v[48:51], 0\n"
      "ds_bpermute_b32 v42, v6, v85 offset:16\n"
      "v_mfma_f32_16x16x32_bf16 v[92:95], a[132:135], v[216:219], v[92:95]\n"
      "v_exp_f32 v158, v158\n"
      "v_mfma_f32_16x16x32_bf16 v[92:95], a[136:139], v[132:135], v[92:95]\n"
      "v_exp_f32 v150, v150\n"
      "v_mfma_f32_16x16x32_bf16 v[92:95], a[140:143], v[224:227], v[92:95]\n"
      "v_exp_f32 v192, v192\n"
      "v_exp_f32 v155, v155\n"
      "v_exp_f32 v153, v153\n"
      "v_add_f32 v248, v248, v249\n"
      "v_exp_f32 v248, v248\n"
      "v_exp_f32 v250, v250\n"
      "v_mfma_f32_16x16x32_bf16 v[86:89], v[64:67], v[100:103], 0\n"
      "v_exp_f32 v152, v152\n"
      "v_mfma_f32_16x16x32_bf16 v[86:89], v[68:71], v[104:107], v[86:89]\n"
      "v_exp_f32 v195, v195\n"
      "v_exp_f32 v154, v154\n"
      "v_mfma_f32_16x16x32_bf16 v[100:103], a[144:147], v[48:51], 0\n"
      "v_exp_f32 v157, v157\n"
      "v_mfma_f32_16x16x32_bf16 v[100:103], a[148:151], v[216:219], v[100:103]\n"
      "v_exp_f32 v241, v241\n"
      "v_mfma_f32_16x16x32_bf16 v[100:103], a[152:155], v[132:135], v[100:103]\n"
      "v_exp_f32 v203, v203\n"
      "v_mfma_f32_16x16x32_bf16 v[100:103], a[156:159], v[224:227], v[100:103]\n"
      "v_cvt_pk_bf16_f32 v181, v198, v199\n"
      "v_exp_f32 v89, v89\n"
      "v_exp_f32 v200, v200\n"
      "v_exp_f32 v194, v194\n"
      "v_exp_f32 v193, v193\n"
      "v_cvt_pk_bf16_f32 v29, v94, v95\n"
      "v_cvt_pk_bf16_f32 v196, v240, v241\n"
      "v_exp_f32 v202, v202\n"
      "v_exp_f32 v33, v33\n"
      "v_cvt_pk_bf16_f32 v31, v102, v103\n"
      "v_cvt_pk_bf16_f32 v197, v242, v243\n"
      "v_exp_f32 v159, v159\n"
      "v_cvt_pk_bf16_f32 v199, v202, v203\n"
      "v_cvt_pk_bf16_f32 v30, v100, v101\n"
      "v_exp_f32 v86, v86\n"
      "v_add_f32 v100, v248, v250\n"
      "v_cvt_pk_bf16_f32 v95, v154, v155\n"
      "v_exp_f32 v151, v151\n"
      "v_cvt_pk_bf16_f32 v198, v200, v201\n"
      "v_cvt_pk_bf16_f32 v28, v92, v93\n"
      "v_cvt_pk_bf16_f32 v162, v248, v250\n"
      "v_cvt_pk_bf16_f32 v200, v192, v193\n"
      "v_cvt_pk_bf16_f32 v94, v152, v153\n"
      "v_cvt_pk_bf16_f32 v203, v34, v35\n"
      "v_cvt_pk_bf16_f32 v163, v248, v250\n"
      "v_mfma_f32_16x16x32_bf16 v[240:243], a[176:179], v[48:51], 0\n"
      "v_cvt_pk_bf16_f32 v201, v194, v195\n"
      "v_add_f32 v101, v248, v250\n"
      "v_mfma_f32_16x16x32_bf16 v[240:243], a[180:183], v[216:219], v[240:243]\n"
      "v_permlane16_swap_b32 v162, v163\n"
      "v_mfma_f32_16x16x32_bf16 v[240:243], a[184:187], v[132:135], v[240:243]\n"
      "v_exp_f32 v88, v88\n"
      "v_mfma_f32_16x16x32_bf16 v[240:243], a[188:191], v[224:227], v[240:243]\n"
      "ds_read_b64 v[18:19], v11 offset:0\n"
      "v_permlane16_swap_b32 v100, v101\n"
      "v_mov_b64 v[248:249], v[100:101]\n"
      "v_mfma_f32_16x16x32_bf16 v[152:155], a[160:163], v[48:51], 0\n"
      "v_exp_f32 v32, v32\n"
      "v_mfma_f32_16x16x32_bf16 v[152:155], a[164:167], v[216:219], v[152:155]\n"
      "v_cvt_pk_bf16_f32 v202, v32, v33\n"
      "v_cvt_pk_bf16_f32 v193, v88, v89\n"
      "v_mfma_f32_16x16x32_bf16 v[152:155], a[168:171], v[132:135], v[152:155]\n"
      "v_exp_f32 v156, v156\n"
      "v_mfma_f32_16x16x32_bf16 v[152:155], a[172:175], v[224:227], v[152:155]\n"
      "v_cvt_pk_bf16_f32 v92, v148, v149\n"
      "v_cvt_pk_bf16_f32 v93, v150, v151\n"
      "v_cvt_pk_bf16_f32 v194, v156, v157\n"
      "v_smfmac_f32_16x16x64_bf16 v[248:251], v[12:15], v[196:203], v5\n"
      "ds_bpermute_b32 v253, v7, v144 offset:8\n"
      "v_smfmac_f32_16x16x64_bf16 v[100:103], v[12:15], v[92:99], v5\n"
      "ds_bpermute_b32 v252, v7, v144 offset:248\n"
      "v_cvt_pk_bf16_f32 v195, v158, v159\n"
      "v_cvt_pk_bf16_f32 v35, v242, v243\n"
      "v_exp_f32 v87, v87\n"
      "v_cvt_pk_bf16_f32 v32, v152, v153\n"
      "v_cvt_pk_bf16_f32 v33, v154, v155\n"
      "v_smfmac_f32_16x16x32_bf16 v[100:103], v[12:13], v[176:179], v5\n"
      "v_cvt_pk_bf16_f32 v192, v86, v87\n"
      "v_cvt_pk_bf16_f32 v34, v240, v241\n"
      "s_waitcnt lgkmcnt(2)\n"
      "v_mfma_f32_16x16x4_4b_bf16 v[146:161], v[162:163], v[18:19], 0 cbsz:2 abid:0\n"
      "s_nop 3\n"
      "v_add_f32 v100, v100, v101\n"
      "v_rcp_f32 v101, v100\n"
      "v_smfmac_f32_16x16x32_bf16 v[248:251], v[12:13], v[192:195], v5\n"
      "ds_read_b64 v[88:89], v11 offset:4096\n"
      "ds_bpermute_b32 v102, v4, v101\n"
      "s_nop 0\n"
      "v_mfma_f32_16x16x32_bf16 v[150:153], v[92:95], v[180:183], v[150:153]\n"
      "v_mfma_f32_16x16x32_bf16 v[150:153], v[96:99], v[184:187], v[150:153]\n"
      "v_add_f32_dpp v90, v90, v90 row_ror:8 row_mask:0xf bank_mask:0xf\n"
      "v_mfma_f32_16x16x32_bf16 v[150:153], v[176:179], v[120:123], v[150:153]\n"
      "s_nop 0\n"
      "v_add_f32_dpp v90, v90, v90 row_ror:4 row_mask:0xf bank_mask:0xf\n"
      "v_mfma_f32_16x16x32_bf16 v[146:149], v[92:95], v[108:111], v[146:149]\n"
      "buffer_load_dwordx4 v1, s[40:43], s78 offen offset:1024 lds\n"
      "v_mfma_f32_16x16x32_bf16 v[146:149], v[96:99], v[112:115], v[146:149]\n"
      "v_add_f32_dpp v90, v90, v90 row_ror:2 row_mask:0xf bank_mask:0xf\n"
      "v_mfma_f32_16x16x32_bf16 v[146:149], v[176:179], v[52:55], v[146:149]\n"
      "s_nop 0\n"
      "v_add_f32_dpp v90, v90, v90 row_ror:1 row_mask:0xf bank_mask:0xf\n"
      "v_mfma_f32_16x16x32_bf16 v[158:161], v[92:95], v[232:235], v[158:161]\n"
      "v_rcp_f32 v91, v90\n"
      "v_mfma_f32_16x16x32_bf16 v[158:161], v[96:99], v[236:239], v[158:161]\n"
      "v_mfma_f32_16x16x32_bf16 v[158:161], v[176:179], v[228:231], v[158:161]\n"
      "s_waitcnt lgkmcnt(0)\n"
      "s_nop 0\n"
      "v_mul_f32_dpp v149, v102, v149 row_newbcast:12 row_mask:0xf bank_mask:0xf\n"
      "v_mfma_f32_16x16x32_bf16 v[154:157], v[92:95], v[56:59], v[154:157]\n"
      "v_mul_f32_dpp v153, v102, v153 row_newbcast:12 row_mask:0xf bank_mask:0xf\n"
      "v_mul_f32_dpp v150, v102, v150 row_newbcast:0 row_mask:0xf bank_mask:0xf\n"
      "v_mfma_f32_16x16x32_bf16 v[154:157], v[96:99], v[60:63], v[154:157]\n"
      "v_mul_f32_dpp v151, v102, v151 row_newbcast:4 row_mask:0xf bank_mask:0xf\n"
      "v_mul_f32_dpp v152, v102, v152 row_newbcast:8 row_mask:0xf bank_mask:0xf\n"
      "v_mfma_f32_16x16x32_bf16 v[154:157], v[176:179], v[116:119], v[154:157]\n"
      "v_add_u32 v11, 0x200, v11\n"
      "v_mul_f32_dpp v146, v102, v146 row_newbcast:0 row_mask:0xf bank_mask:0xf\n"
      "v_mfma_f32_16x16x32_bf16 v[150:153], v[212:215], a[208:211], v[150:153]\n"
      "v_mul_f32_dpp v148, v102, v148 row_newbcast:8 row_mask:0xf bank_mask:0xf\n"
      "v_mul_f32_dpp v147, v102, v147 row_newbcast:4 row_mask:0xf bank_mask:0xf\n"
      "v_mfma_f32_16x16x32_bf16 v[150:153], v[124:127], a[212:215], v[150:153]\n"
      "v_mfma_f32_16x16x32_bf16 v[150:153], v[188:191], a[216:219], v[150:153]\n"
      "v_maximum3_f32 v8, v8, v90, v91\n"
      "v_mul_f32_dpp v161, v102, v161 row_newbcast:12 row_mask:0xf bank_mask:0xf\n"
      "v_mfma_f32_16x16x32_bf16 v[150:153], v[244:247], a[220:223], v[150:153]\n"
      "v_mul_f32_dpp v155, v102, v155 row_newbcast:4 row_mask:0xf bank_mask:0xf\n"
      "v_mul_f32_dpp v156, v102, v156 row_newbcast:8 row_mask:0xf bank_mask:0xf\n"
      "v_mfma_f32_16x16x32_bf16 v[146:149], v[212:215], a[192:195], v[146:149]\n"
      "v_maximum3_f32 v8, v8, v100, v101\n"
      "v_mul_f32_dpp v157, v102, v157 row_newbcast:12 row_mask:0xf bank_mask:0xf\n"
      "v_mfma_f32_16x16x32_bf16 v[146:149], v[124:127], a[196:199], v[146:149]\n"
      "v_mul_f32_dpp v154, v102, v154 row_newbcast:0 row_mask:0xf bank_mask:0xf\n"
      "v_mfma_f32_16x16x32_bf16 v[146:149], v[188:191], a[200:203], v[146:149]\n"
      "v_mul_f32_dpp v159, v102, v159 row_newbcast:4 row_mask:0xf bank_mask:0xf\n"
      "v_mul_f32_dpp v160, v102, v160 row_newbcast:8 row_mask:0xf bank_mask:0xf\n"
      "v_mfma_f32_16x16x32_bf16 v[146:149], v[244:247], a[204:207], v[146:149]\n"
      "v_mul_f32_dpp v158, v102, v158 row_newbcast:0 row_mask:0xf bank_mask:0xf\n"
      "v_add_f32 v248, v248, v249\n"
      "v_mfma_f32_16x16x32_bf16 v[154:157], v[212:215], a[224:227], v[154:157]\n"
      "v_rcp_f32 v249, v248\n"
      "v_mfma_f32_16x16x32_bf16 v[154:157], v[124:127], a[228:231], v[154:157]\n"
      "v_mfma_f32_16x16x32_bf16 v[154:157], v[188:191], a[232:235], v[154:157]\n"
      "ds_bpermute_b32 v250, v4, v249 offset:128\n"
      "v_mfma_f32_16x16x32_bf16 v[154:157], v[244:247], a[236:239], v[154:157]\n"
      "v_maximum3_f32 v8, v8, v248, v249\n"
      "v_cvt_pk_bf16_f32 v146, v146, v150\n"
      "v_mfma_f32_16x16x4_4b_bf16 v[92:107], v[162:163], v[18:19], 0 cbsz:2 abid:2\n"
      "v_cvt_pk_bf16_f32 v150, v147, v151\n"
      "v_pk_max_i16 v150, v150, 0\n"
      "buffer_load_dwordx4 v1, s[40:43], s78 offen offset:3072 lds\n"
      "v_cvt_pk_bf16_f32 v148, v148, v152\n"
      "v_mfma_f32_16x16x32_bf16 v[158:161], v[212:215], a[240:243], v[158:161]\n"
      "v_mfma_f32_16x16x32_bf16 v[158:161], v[124:127], a[244:247], v[158:161]\n"
      "v_mfma_f32_16x16x32_bf16 v[158:161], v[188:191], a[248:251], v[158:161]\n"
      "v_mfma_f32_16x16x32_bf16 v[158:161], v[244:247], a[252:255], v[158:161]\n"
      "v_cvt_pk_bf16_f32 v152, v149, v153\n"
      "v_pk_max_i16 v146, v146, 0\n"
      "v_mfma_f32_16x16x32_bf16 v[92:95], v[196:199], v[108:111], v[92:95]\n"
      "v_mfma_f32_16x16x32_bf16 v[92:95], v[200:203], v[112:115], v[92:95]\n"
      "v_and_b32 v213, 0xffff0000, v88\n"
      "v_add_u32 v16, 64, v16\n"
      "v_mfma_f32_16x16x32_bf16 v[92:95], v[192:195], v[52:55], v[92:95]\n"
      "s_nop 0\n"
      "v_cvt_pk_bf16_f32 v153, v157, v161\n"
      "v_pk_max_i16 v152, v152, 0\n"
      "v_mfma_f32_16x16x32_bf16 v[100:103], v[196:199], v[56:59], v[100:103]\n"
      "v_cvt_pk_bf16_f32 v149, v156, v160\n"
      "v_pk_max_i16 v149, v149, 0\n"
      "v_mfma_f32_16x16x32_bf16 v[100:103], v[200:203], v[60:63], v[100:103]\n"
      "v_cvt_pk_bf16_f32 v147, v154, v158\n"
      "v_mfma_f32_16x16x32_bf16 v[100:103], v[192:195], v[116:119], v[100:103]\n"
      "ds_read_b128 v[240:243], v0 offset:9216\n"
      "ds_read_b128 v[160:163], v0 offset:8192\n"
      "v_mfma_f32_16x16x32_bf16 v[104:107], v[196:199], v[232:235], v[104:107]\n"
      "s_add_u32 m0, s53, 8192\n"
      "ds_read_b128 v[124:127], v0 offset:9728\n"
      "v_cvt_pk_bf16_f32 v151, v155, v159\n"
      "v_mfma_f32_16x16x32_bf16 v[104:107], v[200:203], v[236:239], v[104:107]\n"
      "s_waitcnt lgkmcnt(3)\n"
      "v_mul_f32_dpp v95, v250, v95 row_newbcast:12 row_mask:0xf bank_mask:0xf\n"
      "v_mul_f32_dpp v92, v250, v92 row_newbcast:0 row_mask:0xf bank_mask:0xf\n"
      "v_mfma_f32_16x16x32_bf16 v[104:107], v[192:195], v[228:231], v[104:107]\n"
      "ds_read_b128 v[84:87], v0 offset:8704\n"
      "v_pk_max_i16 v148, v148, 0\n"
      "v_mfma_f32_16x16x32_bf16 v[96:99], v[196:199], v[180:183], v[96:99]\n"
      "v_mfma_f32_16x16x32_bf16 v[96:99], v[200:203], v[184:187], v[96:99]\n"
      "v_pk_max_i16 v151, v151, 0\n"
      "v_pk_max_i16 v153, v153, 0\n"
      "v_mfma_f32_16x16x32_bf16 v[96:99], v[192:195], v[120:123], v[96:99]\n"
      "v_and_b32 v215, 0xffff0000, v89\n"
      "v_lshlrev_b32 v212, 16, v88\n"
      "v_mfma_f32_16x16x4_4b_bf16 v[188:203], v[252:253], v[18:19], 0 cbsz:2 abid:0\n"
      "buffer_store_dwordx2 v[152:153], v2, s[44:47], s73 offen offset:768 nt sc1\n"
      "v_mul_f32_dpp v103, v250, v103 row_newbcast:12 row_mask:0xf bank_mask:0xf\n"
      "v_mul_f32_dpp v93, v250, v93 row_newbcast:4 row_mask:0xf bank_mask:0xf\n"
      "v_mul_f32_dpp v101, v250, v101 row_newbcast:4 row_mask:0xf bank_mask:0xf\n"
      "v_mul_f32_dpp v100, v250, v100 row_newbcast:0 row_mask:0xf bank_mask:0xf\n"
      "v_mfma_f32_16x16x32_bf16 v[176:179], v[20:23], v[28:31], 0\n"
      "v_mul_f32_dpp v96, v250, v96 row_newbcast:0 row_mask:0xf bank_mask:0xf\n"
      "v_mul_f32_dpp v106, v250, v106 row_newbcast:8 row_mask:0xf bank_mask:0xf\n"
      "v_mfma_f32_16x16x32_bf16 v[176:179], v[24:27], v[32:35], v[176:179]\n"
      "v_mul_f32_dpp v98, v250, v98 row_newbcast:8 row_mask:0xf bank_mask:0xf\n"
      "v_mul_f32_dpp v99, v250, v99 row_newbcast:12 row_mask:0xf bank_mask:0xf\n"
      "v_smfmac_f32_16x16x64_bf16 v[196:199], v[40:43], v[56:63], v5\n"
      "v_smfmac_f32_16x16x64_bf16 v[192:195], v[40:43], v[180:187], v5\n"
      "s_waitcnt lgkmcnt(0)\n"
      "buffer_load_dwordx4 v1, s[40:43], s91 offen offset:3072 lds\n"
      "v_smfmac_f32_16x16x64_bf16 v[188:191], v[40:43], v[108:115], v5\n"
      "v_mul_f32_dpp v94, v250, v94 row_newbcast:8 row_mask:0xf bank_mask:0xf\n"
      "v_smfmac_f32_16x16x64_bf16 v[200:203], v[40:43], v[232:239], v5\n"
      "v_mul_f32_dpp v107, v250, v107 row_newbcast:12 row_mask:0xf bank_mask:0xf\n"
      "v_mfma_f32_16x16x32_bf16 v[244:247], v[204:207], v[28:31], 0\n"
      "v_mul_f32_dpp v102, v250, v102 row_newbcast:8 row_mask:0xf bank_mask:0xf\n"
      "v_mul_f32_dpp v97, v250, v97 row_newbcast:4 row_mask:0xf bank_mask:0xf\n"
      "v_mfma_f32_16x16x32_bf16 v[244:247], v[208:211], v[32:35], v[244:247]\n"
      "v_lshlrev_b32 v214, 16, v89\n"
      "v_mul_f32_dpp v104, v250, v104 row_newbcast:0 row_mask:0xf bank_mask:0xf\n"
      "v_smfmac_f32_16x16x32_bf16 v[200:203], v[254:255], v[228:231], v5\n"
      "v_mul_f32_dpp v105, v250, v105 row_newbcast:4 row_mask:0xf bank_mask:0xf\n"
      "v_smfmac_f32_16x16x32_bf16 v[196:199], v[254:255], v[116:119], v5\n"
      "v_pk_max_i16 v147, v147, 0\n"
      "v_smfmac_f32_16x16x32_bf16 v[188:191], v[254:255], v[52:55], v5\n"
      "buffer_load_dwordx4 v1, s[40:43], s91 offen offset:1024 lds\n"
      "v_smfmac_f32_16x16x32_bf16 v[192:195], v[254:255], v[120:123], v5\n"
      "buffer_store_dwordx2 v[146:147], v2, s[44:47], s73 offen offset:0 nt sc1\n"
      "v_mfma_f32_16x16x32_bf16 v[152:155], v[64:67], v[28:31], 0\n"
      "v_exp_f32 v179, v179\n"
      "v_mfma_f32_16x16x32_bf16 v[152:155], v[68:71], v[32:35], v[152:155]\n"
      "v_exp_f32 v246, v246\n"
      "v_mfma_f32_16x16x32_bf16 v[104:107], v[172:175], a[240:243], v[104:107]\n"
      "v_exp_f32 v244, v244\n"
      "v_mfma_f32_16x16x32_bf16 v[104:107], v[72:75], a[244:247], v[104:107]\n"
      "v_add_f32 v188, v188, v189\n"
      "v_add_f32 v196, v196, v197\n"
      "v_mfma_f32_16x16x32_bf16 v[104:107], v[128:131], a[248:251], v[104:107]\n"
      "v_add_f32 v192, v192, v193\n"
      "v_fma_f32 v192, v192, v91, v213\n"
      "v_mfma_f32_16x16x32_bf16 v[104:107], v[36:39], a[252:255], v[104:107]\n"
      "v_exp_f32 v177, v177\n"
      "v_mfma_f32_16x16x32_bf16 v[252:255], v[136:139], v[28:31], 0\n"
      "v_fma_f32 v188, v188, v91, v212\n"
      "v_fma_f32 v196, v196, v91, v214\n"
      "v_mfma_f32_16x16x32_bf16 v[252:255], v[140:143], v[32:35], v[252:255]\n"
      "v_exp_f32 v152, v152\n"
      "v_mfma_f32_16x16x32_bf16 v[96:99], v[172:175], a[208:211], v[96:99]\n"
      "v_add_f32 v200, v200, v201\n"
      "v_fma_f32 v200, v200, v91, v215\n"
      "v_mfma_f32_16x16x32_bf16 v[96:99], v[72:75], a[212:215], v[96:99]\n"
      "v_cvt_pk_bf16_f32 v189, v196, v200\n"
      "v_cvt_pk_bf16_f32 v188, v188, v192\n"
      "v_mfma_f32_16x16x32_bf16 v[96:99], v[128:131], a[216:219], v[96:99]\n"
      "v_exp_f32 v255, v255\n"
      "v_mfma_f32_16x16x32_bf16 v[96:99], v[36:39], a[220:223], v[96:99]\n"
      "v_exp_f32 v155, v155\n"
      "v_mfma_f32_16x16x32_bf16 v[88:91], a[144:147], v[160:163], 0\n"
      "v_exp_f32 v247, v247\n"
      "v_mfma_f32_16x16x32_bf16 v[88:91], a[148:151], v[84:87], v[88:91]\n"
      "v_exp_f32 v245, v245\n"
      "v_mfma_f32_16x16x32_bf16 v[88:91], a[152:155], v[240:243], v[88:91]\n"
      "v_exp_f32 v153, v153\n"
      "v_mfma_f32_16x16x32_bf16 v[88:91], a[156:159], v[124:127], v[88:91]\n"
      "v_exp_f32 v176, v176\n"
      "v_mfma_f32_16x16x32_bf16 v[156:159], v[164:167], v[28:31], 0\n"
      "v_exp_f32 v178, v178\n"
      "v_mfma_f32_16x16x32_bf16 v[156:159], v[168:171], v[32:35], v[156:159]\n"
      "v_cvt_pk_bf16_f32 v196, v176, v177\n"
      "v_mfma_f32_16x16x32_bf16 v[40:43], v[76:79], v[28:31], 0\n"
      "v_cvt_pk_bf16_f32 v197, v178, v179\n"
      "v_cvt_pk_bf16_f32 v194, v244, v245\n"
      "v_mfma_f32_16x16x32_bf16 v[40:43], v[80:83], v[32:35], v[40:43]\n"
      "buffer_store_dwordx2 v[148:149], v2, s[44:47], s73 offen offset:512 nt sc1\n"
      "v_mfma_f32_16x16x32_bf16 v[92:95], v[172:175], a[192:195], v[92:95]\n"
      "v_exp_f32 v252, v252\n"
      "v_mfma_f32_16x16x32_bf16 v[92:95], v[72:75], a[196:199], v[92:95]\n"
      "v_exp_f32 v253, v253\n"
      "v_mfma_f32_16x16x32_bf16 v[92:95], v[128:131], a[200:203], v[92:95]\n"
      "v_exp_f32 v159, v159\n"
      "v_mfma_f32_16x16x32_bf16 v[92:95], v[36:39], a[204:207], v[92:95]\n"
      "v_exp_f32 v43, v43\n"
      "v_mfma_f32_16x16x32_bf16 v[212:215], a[160:163], v[160:163], 0\n"
      "buffer_load_dwordx4 v1, s[40:43], s90 offen offset:2048 lds\n"
      "v_mfma_f32_16x16x32_bf16 v[212:215], a[164:167], v[84:87], v[212:215]\n"
      "v_exp_f32 v42, v42\n"
      "v_mfma_f32_16x16x32_bf16 v[212:215], a[168:171], v[240:243], v[212:215]\n"
      "v_exp_f32 v157, v157\n"
      "v_mfma_f32_16x16x32_bf16 v[212:215], a[172:175], v[124:127], v[212:215]\n"
      "v_exp_f32 v40, v40\n"
      "v_mfma_f32_16x16x32_bf16 v[100:103], v[172:175], a[224:227], v[100:103]\n"
      "v_exp_f32 v254, v254\n"
      "v_mfma_f32_16x16x32_bf16 v[100:103], v[72:75], a[228:231], v[100:103]\n"
      "v_exp_f32 v158, v158\n"
      "v_mfma_f32_16x16x32_bf16 v[100:103], v[128:131], a[232:235], v[100:103]\n"
      "v_exp_f32 v156, v156\n"
      "v_mfma_f32_16x16x32_bf16 v[100:103], v[36:39], a[236:239], v[100:103]\n"
      "v_exp_f32 v41, v41\n"
      "v_mfma_f32_16x16x32_bf16 v[172:175], a[128:131], v[160:163], 0\n"
      "v_cvt_pk_bf16_f32 v192, v156, v157\n"
      "v_mov_b64 v[128:129], 0\n"
      "v_mfma_f32_16x16x32_bf16 v[172:175], a[132:135], v[84:87], v[172:175]\n"
      "v_cvt_pk_bf16_f32 v176, v152, v153\n"
      "v_cvt_pk_bf16_f32 v178, v252, v253\n"
      "v_mfma_f32_16x16x32_bf16 v[172:175], a[136:139], v[240:243], v[172:175]\n"
      "v_mov_b64 v[130:131], 0\n"
      "v_cvt_pk_bf16_f32 v198, v40, v41\n"
      "v_mfma_f32_16x16x32_bf16 v[172:175], a[140:143], v[124:127], v[172:175]\n"
      "v_exp_f32 v154, v154\n"
      "v_mfma_f32_16x16x32_bf16 v[72:75], a[176:179], v[160:163], 0\n"
      "v_cvt_pk_bf16_f32 v195, v246, v247\n"
      "v_cvt_pk_bf16_f32 v252, v212, v213\n"
      "v_mfma_f32_16x16x32_bf16 v[72:75], a[180:183], v[84:87], v[72:75]\n"
      "v_cvt_pk_bf16_f32 v179, v254, v255\n"
      "v_cvt_pk_bf16_f32 v250, v88, v89\n"
      "v_mfma_f32_16x16x32_bf16 v[72:75], a[184:187], v[240:243], v[72:75]\n"
      "v_cvt_pk_bf16_f32 v249, v174, v175\n"
      "v_cvt_pk_bf16_f32 v251, v90, v91\n"
      "v_mfma_f32_16x16x32_bf16 v[72:75], a[188:191], v[124:127], v[72:75]\n"
      "v_cvt_pk_bf16_f32 v248, v172, v173\n"
      "v_pk_max_i16 v189, v189, 0\n"
      "v_cvt_pk_bf16_f32 v92, v92, v96\n"
      "v_cvt_pk_bf16_f32 v94, v94, v98\n"
      "v_cvt_pk_bf16_f32 v96, v93, v97\n"
      "v_cvt_pk_bf16_f32 v253, v214, v215\n"
      "v_mfma_f32_16x16x32_bf16 v[172:175], v[136:139], v[248:251], 0\n"
      "s_nop 0\n"
      "v_cvt_pk_bf16_f32 v255, v74, v75\n"
      "v_cvt_pk_bf16_f32 v254, v72, v73\n"
      "v_smfmac_f32_16x16x64_bf16 v[128:131], v[220:223], v[28:35], v5\n"
      "buffer_load_dwordx4 v1, s[40:43], s90 offen offset:0 lds\n"
      "v_smfmac_f32_16x16x64_bf16 v[128:131], v[44:47], v[248:255], v5\n"
      "buffer_store_dwordx2 v[150:151], v2, s[44:47], s73 offen offset:256 nt sc1\n"
      "v_pk_max_i16 v188, v188, 0\n"
      "v_cvt_pk_bf16_f32 v199, v42, v43\n"
      "v_mfma_f32_16x16x32_bf16 v[200:203], v[76:79], v[248:251], 0\n"
      "v_pk_max_i16 v92, v92, 0\n"
      "v_cvt_pk_bf16_f32 v177, v154, v155\n"
      "v_mfma_f32_16x16x32_bf16 v[200:203], v[80:83], v[252:255], v[200:203]\n"
      "s_nop 0\n"
      "v_add_f32 v130, v130, v131\n"
      "v_exp_f32 v130, v130\n"
      "v_mfma_f32_16x16x32_bf16 v[172:175], v[140:143], v[252:255], v[172:175]\n"
      "v_add_f32 v128, v128, v129\n"
      "v_exp_f32 v128, v128\n"
      "v_mfma_f32_16x16x32_bf16 v[150:153], v[204:207], v[248:251], 0\n"
      "v_cvt_pk_bf16_f32 v193, v158, v159\n"
      "v_add_f32 v213, v128, v130\n"
      "v_mfma_f32_16x16x32_bf16 v[150:153], v[208:211], v[252:255], v[150:153]\n"
      "v_add_f32 v212, v128, v130\n"
      "v_cvt_pk_bf16_f32 v155, v128, v130\n"
      "v_mfma_f32_16x16x32_bf16 v[244:247], v[64:67], v[248:251], 0\n"
      "v_permlane16_swap_b32 v212, v213\n"
      "v_mfma_f32_16x16x32_bf16 v[244:247], v[68:71], v[252:255], v[244:247]\n"
      "v_cvt_pk_bf16_f32 v154, v128, v130\n"
      "v_mov_b64 v[148:149], v[212:213]\n"
      "v_smfmac_f32_16x16x64_bf16 v[212:215], v[12:15], v[192:199], v5\n"
      "v_cvt_pk_bf16_f32 v98, v95, v99\n"
      "v_mfma_f32_16x16x32_bf16 v[128:131], v[164:167], v[248:251], 0\n"
      "s_add_u32 m0, s53, 4096\n"
      "v_exp_f32 v203, v203\n"
      "v_mfma_f32_16x16x32_bf16 v[128:131], v[168:171], v[252:255], v[128:131]\n"
      "v_permlane16_swap_b32 v154, v155\n"
      "v_smfmac_f32_16x16x32_bf16 v[212:215], v[12:13], v[176:179], v5\n"
      "v_pk_max_i16 v94, v94, 0\n"
      "v_mfma_f32_16x16x4_4b_bf16 v[28:43], v[154:155], v[18:19], 0 cbsz:2 abid:0\n"
      "v_exp_f32 v151, v151\n"
      "v_cvt_pk_bf16_f32 v97, v101, v105\n"
      "v_cvt_pk_bf16_f32 v95, v102, v106\n"
      "v_cvt_pk_bf16_f32 v93, v100, v104\n"
      "v_exp_f32 v130, v130\n"
      "v_cvt_pk_bf16_f32 v99, v103, v107\n"
      "v_mfma_f32_16x16x32_bf16 v[144:147], v[20:23], v[248:251], 0\n"
      "ds_read_b128 v[100:103], v0 offset:4096\n"
      "v_add_f32 v212, v212, v213\n"
      "v_mfma_f32_16x16x32_bf16 v[144:147], v[24:27], v[252:255], v[144:147]\n"
      "v_rcp_f32 v213, v212\n"
      "v_mfma_f32_16x16x32_bf16 v[28:31], v[192:195], v[108:111], v[28:31]\n"
      "ds_bpermute_b32 v214, v4, v213\n"
      "v_mfma_f32_16x16x32_bf16 v[28:31], v[196:199], v[112:115], v[28:31]\n"
      "v_exp_f32 v201, v201\n"
      "v_mfma_f32_16x16x32_bf16 v[28:31], v[176:179], v[52:55], v[28:31]\n"
      "buffer_store_dwordx2 v[188:189], v3, s[44:47], s79 offen offset:0 nt sc1\n"
      "ds_read_b128 v[72:75], v0 offset:5632\n"
      "v_mfma_f32_16x16x32_bf16 v[32:35], v[192:195], v[180:183], v[32:35]\n"
      "ds_read_b128 v[156:159], v0 offset:4608\n"
      "ds_read_b128 v[88:91], v0 offset:5120\n"
      "v_mfma_f32_16x16x32_bf16 v[32:35], v[196:199], v[184:187], v[32:35]\n"
      "v_exp_f32 v202, v202\n"
      "v_mfma_f32_16x16x32_bf16 v[32:35], v[176:179], v[120:123], v[32:35]\n"
      "v_exp_f32 v147, v147\n"
      "v_mfma_f32_16x16x32_bf16 v[40:43], v[192:195], v[232:235], v[40:43]\n"
      "v_exp_f32 v131, v131\n"
      "v_mfma_f32_16x16x32_bf16 v[40:43], v[196:199], v[236:239], v[40:43]\n"
      "v_exp_f32 v129, v129\n"
      "v_mfma_f32_16x16x32_bf16 v[40:43], v[176:179], v[228:231], v[40:43]\n"
      "v_exp_f32 v150, v150\n"
      "v_mfma_f32_16x16x32_bf16 v[36:39], v[192:195], v[56:59], v[36:39]\n"
      "v_exp_f32 v128, v128\n"
      "v_mfma_f32_16x16x32_bf16 v[36:39], v[196:199], v[60:63], v[36:39]\n"
      "v_cvt_pk_bf16_f32 v248, v128, v129\n"
      "v_cvt_pk_bf16_f32 v249, v130, v131\n"
      "v_mfma_f32_16x16x32_bf16 v[36:39], v[176:179], v[116:119], v[36:39]\n"
      "s_waitcnt lgkmcnt(3)\n"
      "v_mul_f32_dpp v34, v214, v34 row_newbcast:8 row_mask:0xf bank_mask:0xf\n"
      "v_mul_f32_dpp v28, v214, v28 row_newbcast:0 row_mask:0xf bank_mask:0xf\n"
      "v_mfma_f32_16x16x32_bf16 v[176:179], a[160:163], v[100:103], 0\n"
      "v_mul_f32_dpp v32, v214, v32 row_newbcast:0 row_mask:0xf bank_mask:0xf\n"
      "s_waitcnt lgkmcnt(1)\n"
      "v_mfma_f32_16x16x32_bf16 v[176:179], a[164:167], v[156:159], v[176:179]\n"
      "s_waitcnt lgkmcnt(0)\n"
      "buffer_load_dwordx4 v1, s[40:43], s92 offen offset:2048 lds\n"
      "v_mfma_f32_16x16x32_bf16 v[176:179], a[168:171], v[88:91], v[176:179]\n"
      "v_exp_f32 v246, v246\n"
      "v_mfma_f32_16x16x32_bf16 v[176:179], a[172:175], v[72:75], v[176:179]\n"
      "v_cvt_pk_bf16_f32 v250, v150, v151\n"
      "v_mul_f32_dpp v38, v214, v38 row_newbcast:8 row_mask:0xf bank_mask:0xf\n"
      "v_mfma_f32_16x16x32_bf16 v[128:131], a[176:179], v[100:103], 0\n"
      "v_mul_f32_dpp v42, v214, v42 row_newbcast:8 row_mask:0xf bank_mask:0xf\n"
      "v_mul_f32_dpp v29, v214, v29 row_newbcast:4 row_mask:0xf bank_mask:0xf\n"
      "v_mfma_f32_16x16x32_bf16 v[128:131], a[180:183], v[156:159], v[128:131]\n"
      "v_mul_f32_dpp v40, v214, v40 row_newbcast:0 row_mask:0xf bank_mask:0xf\n"
      "v_mul_f32_dpp v35, v214, v35 row_newbcast:12 row_mask:0xf bank_mask:0xf\n"
      "v_mfma_f32_16x16x32_bf16 v[128:131], a[184:187], v[88:91], v[128:131]\n"
      "L_h2_%=:\n"
      "s_cbranch_execz L_hr_%=\n"
      "v_exp_f32 v172, v172\n"
      "v_mfma_f32_16x16x32_bf16 v[128:131], a[188:191], v[72:75], v[128:131]\n"
      "v_pk_max_i16 v95, v95, 0\n"
      "v_mul_f32_dpp v36, v214, v36 row_newbcast:0 row_mask:0xf bank_mask:0xf\n"
      "v_mfma_f32_16x16x32_bf16 v[196:199], a[144:147], v[100:103], 0\n"
      "v_mul_f32_dpp v33, v214, v33 row_newbcast:4 row_mask:0xf bank_mask:0xf\n"
      "v_maximum3_f32 v8, v8, v212, v213\n"
      "v_mfma_f32_16x16x32_bf16 v[196:199], a[148:151], v[156:159], v[196:199]\n"
      "v_pk_max_i16 v98, v98, 0\n"
      "v_mul_f32_dpp v39, v214, v39 row_newbcast:12 row_mask:0xf bank_mask:0xf\n"
      "v_mfma_f32_16x16x32_bf16 v[196:199], a[152:155], v[88:91], v[196:199]\n"
      "v_mul_f32_dpp v43, v214, v43 row_newbcast:12 row_mask:0xf bank_mask:0xf\n"
      "v_mul_f32_dpp v37, v214, v37 row_newbcast:4 row_mask:0xf bank_mask:0xf\n"
      "v_mfma_f32_16x16x32_bf16 v[196:199], a[156:159], v[72:75], v[196:199]\n"
      "v_mul_f32_dpp v31, v214, v31 row_newbcast:12 row_mask:0xf bank_mask:0xf\n"
      "v_mul_f32_dpp v30, v214, v30 row_newbcast:8 row_mask:0xf bank_mask:0xf\n"
      "v_mfma_f32_16x16x32_bf16 v[32:35], v[48:51], a[208:211], v[32:35]\n"
      "v_exp_f32 v247, v247\n"
      "v_mfma_f32_16x16x32_bf16 v[32:35], v[216:219], a[212:215], v[32:35]\n"
      "v_exp_f32 v174, v174\n"
      "v_mfma_f32_16x16x32_bf16 v[32:35], v[132:135], a[216:219], v[32:35]\n"
      "v_exp_f32 v200, v200\n"
      "v_mfma_f32_16x16x32_bf16 v[32:35], v[224:227], a[220:223], v[32:35]\n"
      "v_exp_f32 v152, v152\n"
      "v_mfma_f32_16x16x32_bf16 v[28:31], v[48:51], a[192:195], v[28:31]\n"
      "buffer_load_dwordx4 v1, s[40:43], s93 offen offset:1024 lds\n"
      "v_mfma_f32_16x16x32_bf16 v[28:31], v[216:219], a[196:199], v[28:31]\n"
      "v_exp_f32 v146, v146\n"
      "v_mfma_f32_16x16x32_bf16 v[28:31], v[132:135], a[200:203], v[28:31]\n"
      "v_mul_f32_dpp v41, v214, v41 row_newbcast:4 row_mask:0xf bank_mask:0xf\n"
      "v_cvt_pk_bf16_f32 v215, v198, v199\n"
      "v_mfma_f32_16x16x32_bf16 v[28:31], v[224:227], a[204:207], v[28:31]\n"
      "buffer_store_dwordx2 v[94:95], v2, s[44:47], s75 offen offset:512 nt sc1\n"
      "v_cvt_pk_bf16_f32 v214, v196, v197\n"
      "v_mfma_f32_16x16x32_bf16 v[40:43], v[48:51], a[240:243], v[40:43]\n"
      "v_exp_f32 v244, v244\n"
      "v_mfma_f32_16x16x32_bf16 v[40:43], v[216:219], a[244:247], v[40:43]\n"
      "v_exp_f32 v173, v173\n"
      "v_mfma_f32_16x16x32_bf16 v[40:43], v[132:135], a[248:251], v[40:43]\n"
      "v_cvt_pk_bf16_f32 v254, v200, v201\n"
      "v_cvt_pk_bf16_f32 v30, v30, v34\n"
      "v_mfma_f32_16x16x32_bf16 v[40:43], v[224:227], a[252:255], v[40:43]\n"
      "v_exp_f32 v144, v144\n"
      "v_mfma_f32_16x16x32_bf16 v[36:39], v[48:51], a[224:227], v[36:39]\n"
      "v_exp_f32 v175, v175\n"
      "v_mfma_f32_16x16x32_bf16 v[36:39], v[216:219], a[228:231], v[36:39]\n"
      "v_exp_f32 v145, v145\n"
      "v_mfma_f32_16x16x32_bf16 v[36:39], v[132:135], a[232:235], v[36:39]\n"
      "v_exp_f32 v153, v153\n"
      "v_mfma_f32_16x16x32_bf16 v[36:39], v[224:227], a[236:239], v[36:39]\n"
      "v_cvt_pk_bf16_f32 v251, v152, v153\n"
      "v_pk_max_i16 v99, v99, 0\n"
      "v_mfma_f32_16x16x32_bf16 v[224:227], a[128:131], v[100:103], 0\n"
      "v_cvt_pk_bf16_f32 v51, v174, v175\n"
      "v_cvt_pk_bf16_f32 v255, v202, v203\n"
      "v_mfma_f32_16x16x32_bf16 v[224:227], a[132:135], v[156:159], v[224:227]\n"
      "ds_read_b128 v[104:107], v0 offset:512\n"
      "v_cvt_pk_bf16_f32 v253, v146, v147\n"
      "v_mfma_f32_16x16x32_bf16 v[224:227], a[136:139], v[88:91], v[224:227]\n"
      "v_pk_max_i16 v93, v93, 0\n"
      "v_cvt_pk_bf16_f32 v252, v144, v145\n"
      "v_mfma_f32_16x16x32_bf16 v[224:227], a[140:143], v[72:75], v[224:227]\n"
      "v_exp_f32 v245, v245\n"
      "v_smfmac_f32_16x16x64_bf16 v[148:151], v[12:15], v[248:255], v5\n"
      "buffer_store_dwordx2 v[92:93], v2, s[44:47], s75 offen offset:0 nt sc1\n"
      "v_cvt_pk_bf16_f32 v50, v172, v173\n"
      "v_mfma_f32_16x16x4_4b_bf16 v[188:203], v[154:155], v[18:19], 0 cbsz:2 abid:2\n"
      "v_cvt_pk_bf16_f32 v49, v246, v247\n"
      "v_cvt_pk_bf16_f32 v48, v244, v245\n"
      "v_cvt_pk_bf16_f32 v34, v31, v35\n"
      "v_cvt_pk_bf16_f32 v35, v39, v43\n"
      "v_cvt_pk_bf16_f32 v28, v28, v32\n"
      "v_cvt_pk_bf16_f32 v212, v224, v225\n"
      "v_smfmac_f32_16x16x32_bf16 v[148:151], v[12:13], v[48:51], v5\n"
      "ds_read_b128 v[132:135], v0 offset:1536\n"
      "v_cvt_pk_bf16_f32 v217, v178, v179\n"
      "v_cvt_pk_bf16_f32 v219, v130, v131\n"
      "v_cvt_pk_bf16_f32 v213, v226, v227\n"
      "v_mfma_f32_16x16x32_bf16 v[188:191], v[248:251], v[108:111], v[188:191]\n"
      "v_pk_max_i16 v34, v34, 0\n"
      "v_mov_b64 v[152:153], 0\n"
      "v_mfma_f32_16x16x32_bf16 v[188:191], v[252:255], v[112:115], v[188:191]\n"
      "v_cvt_pk_bf16_f32 v216, v176, v177\n"
      "v_add_f32 v148, v148, v149\n"
      "v_mfma_f32_16x16x32_bf16 v[188:191], v[48:51], v[52:55], v[188:191]\n"
      "v_mov_b64 v[154:155], 0\n"
      "v_cvt_pk_bf16_f32 v218, v128, v129\n"
      "v_mfma_f32_16x16x32_bf16 v[224:227], v[76:79], v[212:215], 0\n"
      "buffer_load_dwordx4 v1, s[40:43], s93 offen offset:3072 lds\n"
      "v_mfma_f32_16x16x32_bf16 v[224:227], v[80:83], v[216:219], v[224:227]\n"
      "v_rcp_f32 v149, v148\n"
      "v_mfma_f32_16x16x32_bf16 v[176:179], v[20:23], v[212:215], 0\n"
      "ds_read_b128 v[172:175], v0 offset:1024\n"
      "v_cvt_pk_bf16_f32 v31, v38, v42\n"
      "v_mfma_f32_16x16x32_bf16 v[176:179], v[24:27], v[216:219], v[176:179]\n"
      "v_cvt_pk_bf16_f32 v32, v29, v33\n"
      "v_cvt_pk_bf16_f32 v33, v37, v41\n"
      "v_smfmac_f32_16x16x64_bf16 v[152:155], v[220:223], v[212:219], v5\n"
      "ds_read_b128 v[144:147], v0 offset:0\n"
      "v_cvt_pk_bf16_f32 v29, v36, v40\n"
      "v_mfma_f32_16x16x32_bf16 v[192:195], v[248:251], v[180:183], v[192:195]\n"
      "ds_bpermute_b32 v150, v4, v149 offset:128\n"
      "v_mfma_f32_16x16x32_bf16 v[192:195], v[252:255], v[184:187], v[192:195]\n"
      "v_exp_f32 v179, v179\n"
      "v_mfma_f32_16x16x32_bf16 v[192:195], v[48:51], v[120:123], v[192:195]\n"
      "v_exp_f32 v225, v225\n"
      "v_mfma_f32_16x16x32_bf16 v[92:95], v[204:207], v[212:215], 0\n"
      "v_exp_f32 v224, v224\n"
      "v_mfma_f32_16x16x32_bf16 v[92:95], v[208:211], v[216:219], v[92:95]\n"
      "v_exp_f32 v226, v226\n"
      "v_mfma_f32_16x16x32_bf16 v[128:131], v[64:67], v[212:215], 0\n"
      "v_exp_f32 v176, v176\n"
      "v_mfma_f32_16x16x32_bf16 v[128:131], v[68:71], v[216:219], v[128:131]\n"
      "buffer_load_dwordx4 v1, s[40:43], s92 offen offset:0 lds\n"
      "v_mfma_f32_16x16x32_bf16 v[196:199], v[248:251], v[56:59], v[196:199]\n"
      "v_exp_f32 v177, v177\n"
      "v_mfma_f32_16x16x32_bf16 v[196:199], v[252:255], v[60:63], v[196:199]\n"
      "v_exp_f32 v178, v178\n"
      "v_mfma_f32_16x16x32_bf16 v[196:199], v[48:51], v[116:119], v[196:199]\n"
      "v_exp_f32 v227, v227\n"
      "v_mfma_f32_16x16x32_bf16 v[200:203], v[248:251], v[232:235], v[200:203]\n"
      "v_cvt_pk_bf16_f32 v221, v178, v179\n"
      "v_cvt_pk_bf16_f32 v220, v176, v177\n"
      "v_mfma_f32_16x16x32_bf16 v[200:203], v[252:255], v[236:239], v[200:203]\n"
      "v_exp_f32 v93, v93\n"
      "v_mfma_f32_16x16x32_bf16 v[200:203], v[48:51], v[228:231], v[200:203]\n"
      "s_waitcnt lgkmcnt(0)\n"
      "v_mul_f32_dpp v196, v150, v196 row_newbcast:0 row_mask:0xf bank_mask:0xf\n"
      "v_mul_f32_dpp v191, v150, v191 row_newbcast:12 row_mask:0xf bank_mask:0xf\n"
      "v_mfma_f32_16x16x32_bf16 v[36:39], v[164:167], v[212:215], 0\n"
      "v_mul_f32_dpp v192, v150, v192 row_newbcast:0 row_mask:0xf bank_mask:0xf\n"
      "v_mul_f32_dpp v188, v150, v188 row_newbcast:0 row_mask:0xf bank_mask:0xf\n"
      "v_mfma_f32_16x16x32_bf16 v[36:39], v[168:171], v[216:219], v[36:39]\n"
      "s_nop 0\n"
      "v_mul_f32_dpp v201, v150, v201 row_newbcast:4 row_mask:0xf bank_mask:0xf\n"
      "v_mul_f32_dpp v194, v150, v194 row_newbcast:8 row_mask:0xf bank_mask:0xf\n"
      "v_mfma_f32_16x16x32_bf16 v[176:179], v[136:139], v[212:215], 0\n"
      "v_mul_f32_dpp v200, v150, v200 row_newbcast:0 row_mask:0xf bank_mask:0xf\n"
      "v_mfma_f32_16x16x32_bf16 v[176:179], v[140:143], v[216:219], v[176:179]\n"
      "v_exp_f32 v92, v92\n"
      "v_mfma_f32_16x16x32_bf16 v[216:219], a[160:163], v[144:147], 0\n"
      "v_cvt_pk_bf16_f32 v222, v224, v225\n"
      "v_mul_f32_dpp v197, v150, v197 row_newbcast:4 row_mask:0xf bank_mask:0xf\n"
      "v_mfma_f32_16x16x32_bf16 v[216:219], a[164:167], v[104:107], v[216:219]\n"
      "v_exp_f32 v37, v37\n"
      "v_mfma_f32_16x16x32_bf16 v[216:219], a[168:171], v[172:175], v[216:219]\n"
      "v_exp_f32 v95, v95\n"
      "v_mfma_f32_16x16x32_bf16 v[216:219], a[172:175], v[132:135], v[216:219]\n"
      "v_exp_f32 v129, v129\n"
      "v_mfma_f32_16x16x32_bf16 v[252:255], a[144:147], v[144:147], 0\n"
      "v_exp_f32 v176, v176\n"
      "v_mfma_f32_16x16x32_bf16 v[252:255], a[148:151], v[104:107], v[252:255]\n"
      "v_exp_f32 v94, v94\n"
      "v_mfma_f32_16x16x32_bf16 v[252:255], a[152:155], v[172:175], v[252:255]\n"
      "v_exp_f32 v38, v38\n"
      "v_mfma_f32_16x16x32_bf16 v[252:255], a[156:159], v[132:135], v[252:255]\n"
      "v_cvt_pk_bf16_f32 v223, v226, v227\n"
      "v_cvt_pk_bf16_f32 v251, v218, v219\n"
      "v_mfma_f32_16x16x32_bf16 v[48:51], a[176:179], v[144:147], 0\n"
      "v_exp_f32 v39, v39\n"
      "v_mfma_f32_16x16x32_bf16 v[48:51], a[180:183], v[104:107], v[48:51]\n"
      "v_cvt_pk_bf16_f32 v250, v216, v217\n"
      "v_mul_f32_dpp v198, v150, v198 row_newbcast:8 row_mask:0xf bank_mask:0xf\n"
      "v_mfma_f32_16x16x32_bf16 v[48:51], a[184:187], v[172:175], v[48:51]\n"
      "v_cvt_pk_bf16_f32 v248, v252, v253\n"
      "v_mul_f32_dpp v190, v150, v190 row_newbcast:8 row_mask:0xf bank_mask:0xf\n"
      "v_mfma_f32_16x16x32_bf16 v[48:51], a[188:191], v[132:135], v[48:51]\n"
      "v_mul_f32_dpp v189, v150, v189 row_newbcast:4 row_mask:0xf bank_mask:0xf\n"
      "v_cvt_pk_bf16_f32 v249, v254, v255\n"
      "v_mfma_f32_16x16x32_bf16 v[224:227], a[128:131], v[144:147], 0\n"
      "v_exp_f32 v131, v131\n"
      "v_mfma_f32_16x16x32_bf16 v[224:227], a[132:135], v[104:107], v[224:227]\n"
      "s_add_u32 m0, s53, 0\n"
      "v_exp_f32 v36, v36\n"
      "v_mfma_f32_16x16x32_bf16 v[224:227], a[136:139], v[172:175], v[224:227]\n"
      "v_cvt_pk_bf16_f32 v252, v48, v49\n"
      "v_cvt_pk_bf16_f32 v253, v50, v51\n"
      "v_mfma_f32_16x16x32_bf16 v[224:227], a[140:143], v[132:135], v[224:227]\n"
      "v_exp_f32 v177, v177\n"
      "v_mfma_f32_16x16x32_bf16 v[188:191], v[160:163], a[192:195], v[188:191]\n"
      "v_mul_f32_dpp v199, v150, v199 row_newbcast:12 row_mask:0xf bank_mask:0xf\n"
      "v_mul_f32_dpp v203, v150, v203 row_newbcast:12 row_mask:0xf bank_mask:0xf\n"
      "v_mfma_f32_16x16x32_bf16 v[188:191], v[84:87], a[196:199], v[188:191]\n"
      "v_cvt_pk_bf16_f32 v217, v38, v39\n"
      "v_cvt_pk_bf16_f32 v218, v92, v93\n"
      "v_mfma_f32_16x16x32_bf16 v[188:191], v[240:243], a[200:203], v[188:191]\n"
      "v_cvt_pk_bf16_f32 v247, v226, v227\n"
      "v_cvt_pk_bf16_f32 v246, v224, v225\n"
      "v_mfma_f32_16x16x32_bf16 v[188:191], v[124:127], a[204:207], v[188:191]\n"
      "v_mul_f32_dpp v195, v150, v195 row_newbcast:12 row_mask:0xf bank_mask:0xf\n"
      "v_mul_f32_dpp v193, v150, v193 row_newbcast:4 row_mask:0xf bank_mask:0xf\n"
      "v_smfmac_f32_16x16x64_bf16 v[152:155], v[44:47], v[246:253], v5\n"
      "buffer_load_dwordx4 v1, s[40:43], s95 offen offset:3072 nt lds\n"
      "v_mfma_f32_16x16x32_bf16 v[196:199], v[160:163], a[224:227], v[196:199]\n"
      "v_mul_f32_dpp v202, v150, v202 row_newbcast:8 row_mask:0xf bank_mask:0xf\n"
      "v_cvt_pk_bf16_f32 v219, v94, v95\n"
      "v_mfma_f32_16x16x32_bf16 v[196:199], v[84:87], a[228:231], v[196:199]\n"
      "v_exp_f32 v128, v128\n"
      "v_mfma_f32_16x16x32_bf16 v[196:199], v[240:243], a[232:235], v[196:199]\n"
      "v_cvt_pk_bf16_f32 v224, v128, v129\n"
      "v_add_f32 v152, v152, v153\n"
      "v_mfma_f32_16x16x32_bf16 v[196:199], v[124:127], a[236:239], v[196:199]\n"
      "v_add_f32 v154, v154, v155\n"
      "v_cvt_pk_bf16_f32 v216, v36, v37\n"
      "v_mfma_f32_16x16x32_bf16 v[192:195], v[160:163], a[208:211], v[192:195]\n"
      "v_exp_f32 v152, v152\n"
      "v_mfma_f32_16x16x32_bf16 v[192:195], v[84:87], a[212:215], v[192:195]\n"
      "v_exp_f32 v179, v179\n"
      "v_mfma_f32_16x16x32_bf16 v[192:195], v[240:243], a[216:219], v[192:195]\n"
      "v_exp_f32 v178, v178\n"
      "v_mfma_f32_16x16x32_bf16 v[192:195], v[124:127], a[220:223], v[192:195]\n"
      "v_exp_f32 v154, v154\n"
      "v_mfma_f32_16x16x32_bf16 v[200:203], v[160:163], a[240:243], v[200:203]\n"
      "v_cvt_pk_bf16_f32 v94, v152, v154\n"
      "v_add_f32 v160, v152, v154\n"
      "v_mfma_f32_16x16x32_bf16 v[200:203], v[84:87], a[244:247], v[200:203]\n"
      "v_add_f32 v161, v152, v154\n"
      "v_cvt_pk_bf16_f32 v95, v152, v154\n"
      "v_mfma_f32_16x16x32_bf16 v[200:203], v[240:243], a[248:251], v[200:203]\n"
      "s_nop 0\n"
      "v_permlane16_swap_b32 v94, v95\n"
      "v_mfma_f32_16x16x32_bf16 v[200:203], v[124:127], a[252:255], v[200:203]\n"
      "v_permlane16_swap_b32 v160, v161\n"
      "v_mov_b64 v[128:129], v[160:161]\n"
      "v_pk_max_i16 v32, v32, 0\n"
      "v_smfmac_f32_16x16x64_bf16 v[160:163], v[12:15], v[216:223], v5\n"
      "buffer_store_dwordx2 v[98:99], v2, s[44:47], s75 offen offset:768 nt sc1\n"
      "v_cvt_pk_bf16_f32 v188, v188, v192\n"
      "v_mfma_f32_16x16x4_4b_bf16 v[36:51], v[94:95], v[18:19], 0 cbsz:2 abid:0\n"
      "v_cvt_pk_bf16_f32 v227, v178, v179\n"
      "v_exp_f32 v130, v130\n"
      "v_cvt_pk_bf16_f32 v226, v176, v177\n"
      "v_cvt_pk_bf16_f32 v225, v130, v131\n"
      "v_pk_max_i16 v29, v29, 0\n"
      "s_nop 0\n"
      "v_smfmac_f32_16x16x32_bf16 v[160:163], v[12:13], v[224:227], v5\n"
      "buffer_load_dwordx4 v1, s[40:43], s94 offen offset:2048 nt lds\n"
      "v_pk_max_i16 v28, v28, 0\n"
      "s_nop 0\n"
      "v_mfma_f32_16x16x32_bf16 v[40:43], v[216:219], v[180:183], v[40:43]\n"
      "v_pk_max_i16 v97, v97, 0\n"
      "v_cvt_pk_bf16_f32 v192, v189, v193\n"
      "v_mfma_f32_16x16x32_bf16 v[40:43], v[220:223], v[184:187], v[40:43]\n"
      "s_not_b64 vcc, vcc\n"
      "v_pk_max_i16 v96, v96, 0\n"
      "v_add_f32 v160, v160, v161\n"
      "v_mfma_f32_16x16x32_bf16 v[40:43], v[224:227], v[120:123], v[40:43]\n"
      "v_rcp_f32 v161, v160\n"
      "v_mfma_f32_16x16x32_bf16 v[36:39], v[216:219], v[108:111], v[36:39]\n"
      "ds_bpermute_b32 v162, v4, v161\n"
      "v_mfma_f32_16x16x32_bf16 v[36:39], v[220:223], v[112:115], v[36:39]\n"
      "v_maximum3_f32 v8, v8, v148, v149\n"
      "v_mfma_f32_16x16x32_bf16 v[36:39], v[224:227], v[52:55], v[36:39]\n"
      "v_pk_max_i16 v188, v188, 0\n"
      "v_mfma_f32_16x16x32_bf16 v[48:51], v[216:219], v[232:235], v[48:51]\n"
      "v_mfma_f32_16x16x32_bf16 v[48:51], v[220:223], v[236:239], v[48:51]\n"
      "v_cvt_pk_bf16_f32 v190, v190, v194\n"
      "v_cvt_pk_bf16_f32 v194, v191, v195\n"
      "v_mfma_f32_16x16x32_bf16 v[48:51], v[224:227], v[228:231], v[48:51]\n"
      "buffer_store_dwordx2 v[96:97], v2, s[44:47], s75 offen offset:256 nt sc1\n"
      "v_cvt_pk_bf16_f32 v195, v199, v203\n"
      "v_mfma_f32_16x16x32_bf16 v[44:47], v[216:219], v[56:59], v[44:47]\n"
      "buffer_load_dwordx4 v1, s[40:43], s95 offen offset:1024 nt lds\n"
      "v_mfma_f32_16x16x32_bf16 v[44:47], v[220:223], v[60:63], v[44:47]\n"
      "v_cvt_pk_bf16_f32 v189, v196, v200\n"
      "v_maximum3_f32 v8, v8, v160, v161\n"
      "v_mfma_f32_16x16x32_bf16 v[44:47], v[224:227], v[116:119], v[44:47]\n"
      "v_cvt_pk_bf16_f32 v193, v197, v201\n"
      "v_cvt_pk_bf16_f32 v191, v198, v202\n"
      "v_mfma_f32_16x16x4_4b_bf16 v[212:227], v[94:95], v[18:19], 0 cbsz:2 abid:2\n"
      "s_waitcnt lgkmcnt(0)\n"
      "v_mul_f32_dpp v38, v162, v38 row_newbcast:8 row_mask:0xf bank_mask:0xf\n"
      "v_mul_f32_dpp v39, v162, v39 row_newbcast:12 row_mask:0xf bank_mask:0xf\n"
      "v_mul_f32_dpp v37, v162, v37 row_newbcast:4 row_mask:0xf bank_mask:0xf\n"
      "v_mul_f32_dpp v36, v162, v36 row_newbcast:0 row_mask:0xf bank_mask:0xf\n"
      "v_pk_max_i16 v192, v192, 0\n"
      "v_mul_f32_dpp v46, v162, v46 row_newbcast:8 row_mask:0xf bank_mask:0xf\n"
      "v_mul_f32_dpp v47, v162, v47 row_newbcast:12 row_mask:0xf bank_mask:0xf\n"
      "v_mul_f32_dpp v41, v162, v41 row_newbcast:4 row_mask:0xf bank_mask:0xf\n"
      "v_mfma_f32_16x16x32_bf16 v[36:39], v[100:103], a[192:195], v[36:39]\n"
      "v_mul_f32_dpp v45, v162, v45 row_newbcast:4 row_mask:0xf bank_mask:0xf\n"
      "v_mul_f32_dpp v50, v162, v50 row_newbcast:8 row_mask:0xf bank_mask:0xf\n"
      "v_mfma_f32_16x16x32_bf16 v[36:39], v[156:159], a[196:199], v[36:39]\n"
      "v_mul_f32_dpp v49, v162, v49 row_newbcast:4 row_mask:0xf bank_mask:0xf\n"
      "v_mul_f32_dpp v44, v162, v44 row_newbcast:0 row_mask:0xf bank_mask:0xf\n"
      "v_mfma_f32_16x16x32_bf16 v[36:39], v[88:91], a[200:203], v[36:39]\n"
      "v_mul_f32_dpp v51, v162, v51 row_newbcast:12 row_mask:0xf bank_mask:0xf\n"
      "v_mul_f32_dpp v48, v162, v48 row_newbcast:0 row_mask:0xf bank_mask:0xf\n"
      "v_mfma_f32_16x16x32_bf16 v[36:39], v[72:75], a[204:207], v[36:39]\n"
      "v_pk_max_i16 v193, v193, 0\n"
      "v_pk_max_i16 v190, v190, 0\n"
      "v_mfma_f32_16x16x32_bf16 v[48:51], v[100:103], a[240:243], v[48:51]\n"
      "v_mfma_f32_16x16x32_bf16 v[48:51], v[156:159], a[244:247], v[48:51]\n"
      "v_mul_f32_dpp v43, v162, v43 row_newbcast:12 row_mask:0xf bank_mask:0xf\n"
      "v_pk_max_i16 v35, v35, 0\n"
      "v_mfma_f32_16x16x32_bf16 v[48:51], v[88:91], a[248:251], v[48:51]\n"
      "v_pk_max_i16 v30, v30, 0\n"
      "v_pk_max_i16 v195, v195, 0\n"
      "v_mfma_f32_16x16x32_bf16 v[48:51], v[72:75], a[252:255], v[48:51]\n"
      "v_pk_max_i16 v189, v189, 0\n"
      "v_add_u32 v9, 64, v9\n"
      "v_mfma_f32_16x16x32_bf16 v[44:47], v[100:103], a[224:227], v[44:47]\n"
      "v_mul_f32_dpp v42, v162, v42 row_newbcast:8 row_mask:0xf bank_mask:0xf\n"
      "v_mul_f32_dpp v40, v162, v40 row_newbcast:0 row_mask:0xf bank_mask:0xf\n"
      "v_mfma_f32_16x16x32_bf16 v[44:47], v[156:159], a[228:231], v[44:47]\n"
      "v_pk_max_i16 v31, v31, 0\n"
      "v_mfma_f32_16x16x32_bf16 v[44:47], v[88:91], a[232:235], v[44:47]\n"
      "s_add_u32 s44, s44, 13107200\n s_addc_u32 s45, s45, 0\n"
      "v_mfma_f32_16x16x32_bf16 v[44:47], v[72:75], a[236:239], v[44:47]\n"
      "v_pk_max_i16 v33, v33, 0\n"
      "v_pk_max_i16 v191, v191, 0\n"
      "v_mfma_f32_16x16x32_bf16 v[40:43], v[100:103], a[208:211], v[40:43]\n"
      "v_pk_max_i16 v194, v194, 0\n"
      "v_mfma_f32_16x16x32_bf16 v[40:43], v[156:159], a[212:215], v[40:43]\n"
      "buffer_load_dwordx4 v1, s[40:43], s94 offen offset:0 nt lds\n"
      "v_mfma_f32_16x16x32_bf16 v[40:43], v[88:91], a[216:219], v[40:43]\n"
      "s_add_u32 s40, s40, 13107200\n s_addc_u32 s41, s41, 0\n"
      "v_mfma_f32_16x16x32_bf16 v[40:43], v[72:75], a[220:223], v[40:43]\n"
      "s_nop 0\n"
      "L_task_%=:\n"
      "v_mfma_f32_16x16x32_bf16 v[240:243], v[136:139], v[246:249], 0\n"
      "s_waitcnt vmcnt(29)\n"
      "ds_read_b128 v[92:95], v0 offset:20480\n"
      "ds_read_b128 v[96:99], v0 offset:21504\n"
      "v_mfma_f32_16x16x32_bf16 v[240:243], v[140:143], v[250:253], v[240:243]\n"
      "buffer_store_dwordx2 v[28:29], v2, s[44:47], s100 offen offset:0 nt sc1\n"
      "ds_read_b128 v[88:91], v0 offset:20992\n"
      "v_mfma_f32_16x16x32_bf16 v[156:159], v[76:79], v[246:249], 0\n"
      "s_waitcnt vmcnt(14)\n"
      "ds_read_b128 v[160:163], v0 offset:8192\n"
      "ds_read_b128 v[200:203], v0 offset:9728\n"
      "v_mfma_f32_16x16x32_bf16 v[156:159], v[80:83], v[250:253], v[156:159]\n"
      "ds_read_b128 v[84:87], v0 offset:9216\n"
      "ds_read_b128 v[148:151], v0 offset:8704\n"
      "v_mfma_f32_16x16x32_bf16 v[100:103], v[64:67], v[246:249], 0\n"
      "ds_read_b128 v[124:127], v0 offset:16384\n"
      "ds_read_b128 v[72:75], v0 offset:17408\n"
      "v_mfma_f32_16x16x32_bf16 v[100:103], v[68:71], v[250:253], v[100:103]\n"
      "s_nop 1\n"
      "v_exp_f32 v158, v158\n"
      "v_mfma_f32_16x16x32_bf16 v[64:67], v[164:167], v[246:249], 0\n"
      "v_cvt_pk_bf16_f32 v36, v36, v40\n"
      "v_mfma_f32_16x16x32_bf16 v[64:67], v[168:171], v[250:253], v[64:67]\n"
      "v_exp_f32 v157, v157\n"
      "v_mfma_f32_16x16x32_bf16 v[168:171], v[204:207], v[246:249], 0\n"
      "v_exp_f32 v103, v103\n"
      "v_mfma_f32_16x16x32_bf16 v[168:171], v[208:211], v[250:253], v[168:171]\n"
      "v_exp_f32 v102, v102\n"
      "v_mfma_f32_16x16x32_bf16 v[152:155], v[20:23], v[246:249], 0\n"
      "v_exp_f32 v242, v242\n"
      "v_mfma_f32_16x16x32_bf16 v[152:155], v[24:27], v[250:253], v[152:155]\n"
      "s_waitcnt lgkmcnt(5)\n"
      "v_mfma_f32_16x16x32_bf16 v[80:83], a[32:35], v[160:163], 0\n"
      "s_waitcnt lgkmcnt(2)\n"
      "v_mfma_f32_16x16x32_bf16 v[80:83], a[36:39], v[148:151], v[80:83]\n"
      "v_mfma_f32_16x16x32_bf16 v[80:83], a[40:43], v[84:87], v[80:83]\n"
      "v_cvt_pk_bf16_f32 v40, v37, v41\n"
      "v_mfma_f32_16x16x32_bf16 v[80:83], a[44:47], v[200:203], v[80:83]\n"
      "v_exp_f32 v159, v159\n"
      "v_mfma_f32_16x16x32_bf16 v[140:143], a[48:51], v[160:163], 0\n"
      "v_exp_f32 v153, v153\n"
      "v_mfma_f32_16x16x32_bf16 v[140:143], a[52:55], v[148:151], v[140:143]\n"
      "v_cvt_pk_bf16_f32 v211, v158, v159\n"
      "v_mfma_f32_16x16x32_bf16 v[140:143], a[56:59], v[84:87], v[140:143]\n"
      "v_exp_f32 v101, v101\n"
      "v_mfma_f32_16x16x32_bf16 v[140:143], a[60:63], v[200:203], v[140:143]\n"
      "ds_read_b128 v[176:179], v0 offset:22016\n"
      "v_mfma_f32_16x16x32_bf16 v[164:167], v[160:163], a[80:83], 0\n"
      "v_exp_f32 v243, v243\n"
      "v_mfma_f32_16x16x32_bf16 v[164:167], v[148:151], a[84:87], v[164:167]\n"
      "v_exp_f32 v66, v66\n"
      "v_mfma_f32_16x16x32_bf16 v[164:167], v[84:87], a[88:91], v[164:167]\n"
      "v_mfma_f32_16x16x32_bf16 v[164:167], v[200:203], a[92:95], v[164:167]\n"
      "buffer_store_dwordx2 v[190:191], v2, s[44:47], s101 offen offset:512 nt sc1\n"
      "v_mfma_f32_16x16x32_bf16 v[76:79], v[160:163], a[64:67], 0\n"
      "ds_read_b128 v[196:199], v0 offset:17920\n"
      "v_mfma_f32_16x16x32_bf16 v[76:79], v[148:151], a[68:71], v[76:79]\n"
      "v_mfma_f32_16x16x32_bf16 v[76:79], v[84:87], a[72:75], v[76:79]\n"
      "v_mfma_f32_16x16x32_bf16 v[76:79], v[200:203], a[76:79], v[76:79]\n"
      "s_add_u32 m0, s53, 20480\n"
      "v_exp_f32 v64, v64\n"
      "v_mfma_f32_16x16x32_bf16 v[136:139], v[160:163], a[112:115], 0\n"
      "v_exp_f32 v156, v156\n"
      "v_mfma_f32_16x16x32_bf16 v[136:139], v[148:151], a[116:119], v[136:139]\n"
      "v_cvt_pk_bf16_f32 v210, v156, v157\n"
      "v_mfma_f32_16x16x32_bf16 v[136:139], v[84:87], a[120:123], v[136:139]\n"
      "v_cvt_pk_bf16_f32 v38, v38, v42\n"
      "v_mfma_f32_16x16x32_bf16 v[136:139], v[200:203], a[124:127], v[136:139]\n"
      "v_exp_f32 v154, v154\n"
      "v_mfma_f32_16x16x32_bf16 v[68:71], a[0:3], v[160:163], 0\n"
      "v_exp_f32 v241, v241\n"
      "v_mfma_f32_16x16x32_bf16 v[68:71], a[4:7], v[148:151], v[68:71]\n"
      "v_exp_f32 v152, v152\n"
      "v_mfma_f32_16x16x32_bf16 v[68:71], a[8:11], v[84:87], v[68:71]\n"
      "v_exp_f32 v240, v240\n"
      "v_mfma_f32_16x16x32_bf16 v[68:71], a[12:15], v[200:203], v[68:71]\n"
      "v_cvt_pk_bf16_f32 v41, v45, v49\n"
      "v_pk_max_i16 v41, v41, 0\n"
      "v_mfma_f32_16x16x32_bf16 v[252:255], a[16:19], v[160:163], 0\n"
      "s_waitcnt lgkmcnt(1)\n"
      "buffer_load_dwordx4 v1, s[40:43], s74 offen offset:1024 lds\n"
      "v_mfma_f32_16x16x32_bf16 v[252:255], a[20:23], v[148:151], v[252:255]\n"
      "v_exp_f32 v155, v155\n"
      "v_mfma_f32_16x16x32_bf16 v[252:255], a[24:27], v[84:87], v[252:255]\n"
      "v_exp_f32 v65, v65\n"
      "v_mfma_f32_16x16x32_bf16 v[252:255], a[28:31], v[200:203], v[252:255]\n"
      "v_cvt_pk_bf16_f32 v209, v154, v155\n"
      "v_cvt_pk_bf16_f32 v208, v152, v153\n"
      "v_mfma_f32_16x16x32_bf16 v[152:155], v[160:163], a[96:99], 0\n"
      "v_cvt_pk_bf16_f32 v42, v39, v43\n"
      "v_cvt_pk_bf16_f32 v19, v102, v103\n"
      "v_mfma_f32_16x16x32_bf16 v[152:155], v[148:151], a[100:103], v[152:155]\n"
      "v_exp_f32 v100, v100\n"
      "v_mfma_f32_16x16x32_bf16 v[152:155], v[84:87], a[104:107], v[152:155]\n"
      "ds_read2_b32 v[160:161], v16 offset0:0 offset1:2\n"
      "v_mfma_f32_16x16x32_bf16 v[152:155], v[200:203], a[108:111], v[152:155]\n"
      "buffer_store_dwordx2 v[32:33], v2, s[44:47], s100 offen offset:256 nt sc1\n"
      "v_cvt_pk_bf16_f32 v25, v82, v83\n"
      "v_mfma_f32_16x16x32_bf16 v[148:151], a[176:179], v[92:95], 0\n"
      "v_exp_f32 v168, v168\n"
      "v_mfma_f32_16x16x32_bf16 v[148:151], a[180:183], v[88:91], v[148:151]\n"
      "v_cvt_pk_bf16_f32 v21, v242, v243\n"
      "v_mfma_f32_16x16x32_bf16 v[148:151], a[184:187], v[96:99], v[148:151]\n"
      "v_exp_f32 v170, v170\n"
      "v_mfma_f32_16x16x32_bf16 v[148:151], a[188:191], v[176:179], v[148:151]\n"
      "v_exp_f32 v67, v67\n"
      "v_mfma_f32_16x16x32_bf16 v[200:203], a[160:163], v[92:95], 0\n"
      "v_mfma_f32_16x16x32_bf16 v[200:203], a[164:167], v[88:91], v[200:203]\n"
      "v_exp_f32 v171, v171\n"
      "v_mfma_f32_16x16x32_bf16 v[200:203], a[168:171], v[96:99], v[200:203]\n"
      "v_mfma_f32_16x16x32_bf16 v[200:203], a[172:175], v[176:179], v[200:203]\n"
      "v_cvt_pk_bf16_f32 v205, v66, v67\n"
      "v_cvt_pk_bf16_f32 v204, v64, v65\n"
      "v_mfma_f32_16x16x32_bf16 v[64:67], a[32:35], v[92:95], 0\n"
      "v_mfma_f32_16x16x32_bf16 v[64:67], a[36:39], v[88:91], v[64:67]\n"
      "v_cvt_pk_bf16_f32 v18, v100, v101\n"
      "v_cvt_pk_bf16_f32 v207, v170, v171\n"
      "v_mfma_f32_16x16x32_bf16 v[64:67], a[40:43], v[96:99], v[64:67]\n"
      "v_mfma_f32_16x16x32_bf16 v[64:67], a[44:47], v[176:179], v[64:67]\n"
      "v_exp_f32 v169, v169\n"
      "v_mfma_f32_16x16x32_bf16 v[248:251], a[0:3], v[92:95], 0\n"
      "v_mfma_f32_16x16x32_bf16 v[248:251], a[4:7], v[88:91], v[248:251]\n"
      "v_cvt_pk_bf16_f32 v206, v168, v169\n"
      "v_mfma_f32_16x16x32_bf16 v[248:251], a[8:11], v[96:99], v[248:251]\n"
      "v_cvt_pk_bf16_f32 v244, v200, v201\n"
      "v_mfma_f32_16x16x32_bf16 v[248:251], a[12:15], v[176:179], v[248:251]\n"
      "ds_read_b128 v[100:103], v0 offset:16896\n"
      "v_cvt_pk_bf16_f32 v20, v240, v241\n"
      "v_smfmac_f32_16x16x64_bf16 v[128:131], v[12:15], v[204:211], v5\n"
      "v_mfma_f32_16x16x32_bf16 v[224:227], v[204:207], v[232:235], v[224:227]\n"
      "v_mfma_f32_16x16x32_bf16 v[224:227], v[208:211], v[236:239], v[224:227]\n"
      "v_cvt_pk_bf16_f32 v43, v47, v51\n"
      "v_cvt_pk_bf16_f32 v245, v202, v203\n"
      "v_mfma_f32_16x16x32_bf16 v[224:227], v[18:21], v[228:231], v[224:227]\n"
      "buffer_load_dwordx4 v1, s[40:43], s73 offen offset:2048 lds\n"
      "v_smfmac_f32_16x16x32_bf16 v[128:131], v[12:13], v[18:21], v5\n"
      "buffer_store_dwordx2 v[192:193], v2, s[44:47], s101 offen offset:256 nt sc1\n"
      "v_mfma_f32_16x16x32_bf16 v[232:235], a[16:19], v[92:95], 0\n"
      "v_mfma_f32_16x16x32_bf16 v[232:235], a[20:23], v[88:91], v[232:235]\n"
      "ds_read_b128 v[168:171], v9 offset:2048\n"
      "v_mfma_f32_16x16x32_bf16 v[232:235], a[24:27], v[96:99], v[232:235]\n"
      "ds_read2_b32 v[162:163], v16 offset0:128 offset1:130\n"
      "v_mfma_f32_16x16x32_bf16 v[232:235], a[28:31], v[176:179], v[232:235]\n"
      "v_cvt_pk_bf16_f32 v37, v44, v48\n"
      "v_cvt_pk_bf16_f32 v24, v80, v81\n"
      "v_mfma_f32_16x16x32_bf16 v[220:223], v[204:207], v[56:59], v[220:223]\n"
      "v_mfma_f32_16x16x32_bf16 v[220:223], v[208:211], v[60:63], v[220:223]\n"
      "s_waitcnt vmcnt(12)\n"
      "ds_read_b128 v[80:83], v0 offset:4096\n"
      "v_mfma_f32_16x16x32_bf16 v[220:223], v[18:21], v[116:119], v[220:223]\n"
      "buffer_load_dwordx4 v1, s[40:43], s74 offen offset:3072 lds\n"
      "v_mfma_f32_16x16x32_bf16 v[212:215], v[204:207], v[108:111], v[212:215]\n"
      "v_mfma_f32_16x16x32_bf16 v[212:215], v[208:211], v[112:115], v[212:215]\n"
      "ds_read_b128 v[116:119], v0 offset:5632\n"
      "v_add_f32 v128, v128, v129\n"
      "v_mfma_f32_16x16x32_bf16 v[212:215], v[18:21], v[52:55], v[212:215]\n"
      "v_cvt_pk_bf16_f32 v22, v252, v253\n"
      "v_cvt_pk_bf16_f32 v236, v136, v137\n"
      "v_mfma_f32_16x16x32_bf16 v[200:203], a[16:19], v[124:127], 0\n"
      "v_cvt_pk_bf16_f32 v136, v248, v249\n"
      "s_waitcnt lgkmcnt(4)\n"
      "v_mfma_f32_16x16x32_bf16 v[200:203], a[20:23], v[100:103], v[200:203]\n"
      "ds_read_b128 v[108:111], v0 offset:4608\n"
      "v_mfma_f32_16x16x32_bf16 v[200:203], a[24:27], v[72:75], v[200:203]\n"
      "v_rcp_f32 v129, v128\n"
      "v_mfma_f32_16x16x32_bf16 v[200:203], a[28:31], v[196:199], v[200:203]\n"
      "ds_bpermute_b32 v130, v4, v129 offset:128\n"
      "v_mfma_f32_16x16x32_bf16 v[216:219], v[204:207], v[180:183], v[216:219]\n"
      "v_mfma_f32_16x16x32_bf16 v[216:219], v[208:211], v[184:187], v[216:219]\n"
      "v_cvt_pk_bf16_f32 v184, v164, v165\n"
      "v_cvt_pk_bf16_f32 v27, v142, v143\n"
      "v_mfma_f32_16x16x32_bf16 v[216:219], v[18:21], v[120:123], v[216:219]\n"
      "v_mfma_f32_16x16x32_bf16 v[56:59], a[128:131], v[124:127], 0\n"
      "v_cvt_pk_bf16_f32 v237, v138, v139\n"
      "v_pk_max_i16 v38, v38, 0\n"
      "v_mfma_f32_16x16x32_bf16 v[56:59], a[132:135], v[100:103], v[56:59]\n"
      "ds_read_b128 v[52:55], v10 offset:0\n"
      "v_cvt_pk_bf16_f32 v185, v166, v167\n"
      "v_mfma_f32_16x16x32_bf16 v[56:59], a[136:139], v[72:75], v[56:59]\n"
      "v_cvt_pk_bf16_f32 v21, v70, v71\n"
      "v_mfma_f32_16x16x32_bf16 v[56:59], a[140:143], v[196:199], v[56:59]\n"
      "buffer_store_dwordx2 v[30:31], v2, s[44:47], s100 offen offset:512 nt sc1\n"
      "ds_read_b128 v[180:183], v0 offset:5120\n"
      "v_mfma_f32_16x16x32_bf16 v[208:211], a[48:51], v[92:95], 0\n"
      "v_mfma_f32_16x16x32_bf16 v[208:211], a[52:55], v[88:91], v[208:211]\n"
      "s_waitcnt lgkmcnt(2)\n"
      "v_mul_f32_dpp v219, v130, v219 row_newbcast:12 row_mask:0xf bank_mask:0xf\n"
      "v_mul_f32_dpp v216, v130, v216 row_newbcast:0 row_mask:0xf bank_mask:0xf\n"
      "v_mfma_f32_16x16x32_bf16 v[208:211], a[56:59], v[96:99], v[208:211]\n"
      "v_cvt_pk_bf16_f32 v20, v68, v69\n"
      "v_mul_f32_dpp v220, v130, v220 row_newbcast:0 row_mask:0xf bank_mask:0xf\n"
      "v_mfma_f32_16x16x32_bf16 v[208:211], a[60:63], v[176:179], v[208:211]\n"
      "v_cvt_pk_bf16_f32 v246, v148, v149\n"
      "v_mfma_f32_16x16x32_bf16 v[112:115], a[128:131], v[92:95], 0\n"
      "v_cvt_pk_bf16_f32 v60, v152, v153\n"
      "v_mfma_f32_16x16x32_bf16 v[112:115], a[132:135], v[88:91], v[112:115]\n"
      "v_mfma_f32_16x16x32_bf16 v[112:115], a[136:139], v[96:99], v[112:115]\n"
      "v_mfma_f32_16x16x32_bf16 v[112:115], a[140:143], v[176:179], v[112:115]\n"
      "v_pk_max_i16 v36, v36, 0\n"
      "v_cvt_pk_bf16_f32 v247, v150, v151\n"
      "v_mfma_f32_16x16x32_bf16 v[148:151], a[16:19], v[80:83], 0\n"
      "v_mfma_f32_16x16x32_bf16 v[148:151], a[20:23], v[108:111], v[148:151]\n"
      "s_waitcnt lgkmcnt(0)\n"
      "v_mfma_f32_16x16x32_bf16 v[148:151], a[24:27], v[180:183], v[148:151]\n"
      "ds_read_b128 v[164:167], v9 offset:0\n"
      "v_mul_f32_dpp v217, v130, v217 row_newbcast:4 row_mask:0xf bank_mask:0xf\n"
      "v_mfma_f32_16x16x32_bf16 v[148:151], a[28:31], v[116:119], v[148:151]\n"
      "v_mul_f32_dpp v214, v130, v214 row_newbcast:8 row_mask:0xf bank_mask:0xf\n"
      "v_mfma_f32_16x16x32_bf16 v[68:71], a[144:147], v[92:95], 0\n"
      "buffer_load_dwordx4 v1, s[40:43], s73 offen offset:0 lds\n"
      "v_mfma_f32_16x16x32_bf16 v[68:71], a[148:151], v[88:91], v[68:71]\n"
      "v_cvt_pk_bf16_f32 v142, v208, v209\n"
      "v_cvt_pk_bf16_f32 v26, v140, v141\n"
      "v_mfma_f32_16x16x32_bf16 v[68:71], a[152:155], v[96:99], v[68:71]\n"
      "v_mfma_f32_16x16x32_bf16 v[68:71], a[156:159], v[176:179], v[68:71]\n"
      "v_cvt_pk_bf16_f32 v39, v46, v50\n"
      "v_mfma_f32_16x16x32_bf16 v[48:51], a[32:35], v[80:83], 0\n"
      "v_mfma_f32_16x16x32_bf16 v[48:51], a[36:39], v[108:111], v[48:51]\n"
      "v_mul_f32_dpp v225, v130, v225 row_newbcast:4 row_mask:0xf bank_mask:0xf\n"
      "v_cvt_pk_bf16_f32 v139, v234, v235\n"
      "v_mfma_f32_16x16x32_bf16 v[48:51], a[40:43], v[180:183], v[48:51]\n"
      "s_add_u32 m0, s53, 16384\n"
      "v_cvt_pk_bf16_f32 v240, v112, v113\n"
      "v_mov_b64 v[152:153], 0\n"
      "v_mfma_f32_16x16x32_bf16 v[48:51], a[44:47], v[116:119], v[48:51]\n"
      "buffer_store_dwordx2 v[194:195], v2, s[44:47], s101 offen offset:768 nt sc1\n"
      "v_cvt_pk_bf16_f32 v23, v254, v255\n"
      "v_mfma_f32_16x16x32_bf16 v[120:123], a[32:35], v[124:127], 0\n"
      "v_cvt_pk_bf16_f32 v61, v154, v155\n"
      "v_cvt_pk_bf16_f32 v241, v114, v115\n"
      "v_mfma_f32_16x16x32_bf16 v[120:123], a[36:39], v[100:103], v[120:123]\n"
      "v_mfma_f32_16x16x32_bf16 v[120:123], a[40:43], v[72:75], v[120:123]\n"
      "v_mfma_f32_16x16x32_bf16 v[120:123], a[44:47], v[196:199], v[120:123]\n"
      "v_mov_b64 v[230:231], 0\n"
      "v_cvt_pk_bf16_f32 v208, v48, v49\n"
      "v_mfma_f32_16x16x32_bf16 v[28:31], v[80:83], a[112:115], 0\n"
      "v_mfma_f32_16x16x32_bf16 v[28:31], v[108:111], a[116:119], v[28:31]\n"
      "v_mfma_f32_16x16x32_bf16 v[28:31], v[180:183], a[120:123], v[28:31]\n"
      "v_cvt_pk_bf16_f32 v207, v150, v151\n"
      "v_mul_f32_dpp v212, v130, v212 row_newbcast:0 row_mask:0xf bank_mask:0xf\n"
      "v_mfma_f32_16x16x32_bf16 v[28:31], v[116:119], a[124:127], v[28:31]\n"
      "v_mul_f32_dpp v218, v130, v218 row_newbcast:8 row_mask:0xf bank_mask:0xf\n"
      "v_cvt_pk_bf16_f32 v143, v210, v211\n"
      "v_mfma_f32_16x16x32_bf16 v[44:47], a[0:3], v[80:83], 0\n"
      "L_h3_%=:\n"
      "s_cbranch_execz L_hr_%=\n"
      "v_mul_f32_dpp v221, v130, v221 row_newbcast:4 row_mask:0xf bank_mask:0xf\n"
      "v_mul_f32_dpp v224, v130, v224 row_newbcast:0 row_mask:0xf bank_mask:0xf\n"
      "v_mfma_f32_16x16x32_bf16 v[44:47], a[4:7], v[108:111], v[44:47]\n"
      "buffer_load_dwordx4 v1, s[40:43], s76 offen offset:3072 lds\n"
      "v_mfma_f32_16x16x32_bf16 v[44:47], a[8:11], v[180:183], v[44:47]\n"
      "v_mul_f32_dpp v215, v130, v215 row_newbcast:12 row_mask:0xf bank_mask:0xf\n"
      "v_cvt_pk_bf16_f32 v141, v66, v67\n"
      "v_mfma_f32_16x16x32_bf16 v[44:47], a[12:15], v[116:119], v[44:47]\n"
      "v_mfma_f32_16x16x32_bf16 v[154:157], a[48:51], v[80:83], 0\n"
      "v_pk_max_i16 v40, v40, 0\n"
      "v_cvt_pk_bf16_f32 v242, v68, v69\n"
      "v_mfma_f32_16x16x32_bf16 v[154:157], a[52:55], v[108:111], v[154:157]\n"
      "v_cvt_pk_bf16_f32 v138, v232, v233\n"
      "v_mfma_f32_16x16x32_bf16 v[154:157], a[56:59], v[180:183], v[154:157]\n"
      "v_cvt_pk_bf16_f32 v243, v70, v71\n"
      "v_mfma_f32_16x16x32_bf16 v[154:157], a[60:63], v[116:119], v[154:157]\n"
      "buffer_store_dwordx2 v[34:35], v2, s[44:47], s100 offen offset:768 nt sc1\n"
      "v_mul_f32_dpp v226, v130, v226 row_newbcast:8 row_mask:0xf bank_mask:0xf\n"
      "v_mfma_f32_16x16x32_bf16 v[112:115], a[0:3], v[124:127], 0\n"
      "v_mul_f32_dpp v213, v130, v213 row_newbcast:4 row_mask:0xf bank_mask:0xf\n"
      "v_mul_f32_dpp v223, v130, v223 row_newbcast:12 row_mask:0xf bank_mask:0xf\n"
      "v_mfma_f32_16x16x32_bf16 v[112:115], a[4:7], v[100:103], v[112:115]\n"
      "v_cvt_pk_bf16_f32 v66, v200, v201\n"
      "v_cvt_pk_bf16_f32 v235, v30, v31\n"
      "v_mfma_f32_16x16x32_bf16 v[112:115], a[8:11], v[72:75], v[112:115]\n"
      "v_mfma_f32_16x16x32_bf16 v[112:115], a[12:15], v[196:199], v[112:115]\n"
      "v_cvt_pk_bf16_f32 v67, v202, v203\n"
      "v_cvt_pk_bf16_f32 v68, v120, v121\n"
      "v_mfma_f32_16x16x32_bf16 v[252:255], v[80:83], a[96:99], 0\n"
      "v_cvt_pk_bf16_f32 v211, v156, v157\n"
      "v_mfma_f32_16x16x32_bf16 v[252:255], v[108:111], a[100:103], v[252:255]\n"
      "v_mul_f32_dpp v227, v130, v227 row_newbcast:12 row_mask:0xf bank_mask:0xf\n"
      "v_cvt_pk_bf16_f32 v140, v64, v65\n"
      "v_mfma_f32_16x16x32_bf16 v[252:255], v[180:183], a[104:107], v[252:255]\n"
      "v_cvt_pk_bf16_f32 v137, v250, v251\n"
      "v_cvt_pk_bf16_f32 v205, v46, v47\n"
      "v_mfma_f32_16x16x32_bf16 v[252:255], v[116:119], a[108:111], v[252:255]\n"
      "v_cvt_pk_bf16_f32 v64, v112, v113\n"
      "v_mfma_f32_16x16x32_bf16 v[248:251], a[48:51], v[124:127], 0\n"
      "ds_read_b128 v[192:195], v0 offset:12288\n"
      "v_cvt_pk_bf16_f32 v69, v122, v123\n"
      "v_mfma_f32_16x16x32_bf16 v[248:251], a[52:55], v[100:103], v[248:251]\n"
      "v_pk_max_i16 v42, v42, 0\n"
      "v_mfma_f32_16x16x32_bf16 v[248:251], a[56:59], v[72:75], v[248:251]\n"
      "v_mfma_f32_16x16x32_bf16 v[248:251], a[60:63], v[196:199], v[248:251]\n"
      "v_cvt_pk_bf16_f32 v234, v28, v29\n"
      "v_mul_f32_dpp v222, v130, v222 row_newbcast:8 row_mask:0xf bank_mask:0xf\n"
      "v_mfma_f32_16x16x32_bf16 v[32:35], v[136:139], v[240:243], 0\n"
      "v_mfma_f32_16x16x32_bf16 v[32:35], v[140:143], v[244:247], v[32:35]\n"
      "v_cvt_pk_bf16_f32 v210, v154, v155\n"
      "v_cvt_pk_bf16_f32 v206, v148, v149\n"
      "v_mfma_f32_16x16x32_bf16 v[216:219], v[144:147], a[208:211], v[216:219]\n"
      "v_cvt_pk_bf16_f32 v29, v58, v59\n"
      "v_mfma_f32_16x16x32_bf16 v[216:219], v[104:107], a[212:215], v[216:219]\n"
      "v_mfma_f32_16x16x32_bf16 v[216:219], v[172:175], a[216:219], v[216:219]\n"
      "v_cvt_pk_bf16_f32 v65, v114, v115\n"
      "v_mfma_f32_16x16x32_bf16 v[216:219], v[132:135], a[220:223], v[216:219]\n"
      "v_exp_f32 v33, v33\n"
      "v_mfma_f32_16x16x32_bf16 v[212:215], v[144:147], a[192:195], v[212:215]\n"
      "v_mov_b64 v[120:121], 0\n"
      "v_mfma_f32_16x16x32_bf16 v[212:215], v[104:107], a[196:199], v[212:215]\n"
      "v_cvt_pk_bf16_f32 v70, v248, v249\n"
      "v_cvt_pk_bf16_f32 v209, v50, v51\n"
      "v_mfma_f32_16x16x32_bf16 v[212:215], v[172:175], a[200:203], v[212:215]\n"
      "v_exp_f32 v34, v34\n"
      "v_mfma_f32_16x16x32_bf16 v[212:215], v[132:135], a[204:207], v[212:215]\n"
      "buffer_load_dwordx4 v1, s[40:43], s75 offen offset:0 lds\n"
      "v_mfma_f32_16x16x32_bf16 v[220:223], v[144:147], a[224:227], v[220:223]\n"
      "v_mfma_f32_16x16x32_bf16 v[220:223], v[104:107], a[228:231], v[220:223]\n"
      "ds_read_b128 v[48:51], v0 offset:13824\n"
      "v_cvt_pk_bf16_f32 v28, v56, v57\n"
      "v_mfma_f32_16x16x32_bf16 v[220:223], v[172:175], a[232:235], v[220:223]\n"
      "v_cvt_pk_bf16_f32 v204, v44, v45\n"
      "v_mov_b64 v[248:249], 0\n"
      "v_mfma_f32_16x16x32_bf16 v[220:223], v[132:135], a[236:239], v[220:223]\n"
      "v_mfma_f32_16x16x32_bf16 v[224:227], v[144:147], a[240:243], v[224:227]\n"
      "s_waitcnt vmcnt(13)\n"
      "ds_read_b128 v[44:47], v0 offset:0\n"
      "ds_read_b128 v[148:151], v0 offset:1024\n"
      "v_mfma_f32_16x16x32_bf16 v[224:227], v[104:107], a[244:247], v[224:227]\n"
      "ds_read_b128 v[200:203], v0 offset:12800\n"
      "v_cvt_pk_bf16_f32 v214, v214, v218\n"
      "v_mfma_f32_16x16x32_bf16 v[224:227], v[172:175], a[248:251], v[224:227]\n"
      "v_cvt_pk_bf16_f32 v113, v78, v79\n"
      "v_cvt_pk_bf16_f32 v212, v212, v216\n"
      "v_mfma_f32_16x16x32_bf16 v[224:227], v[132:135], a[252:255], v[224:227]\n"
      "v_cvt_pk_bf16_f32 v71, v250, v251\n"
      "s_waitcnt lgkmcnt(5)\n"
      "v_smfmac_f32_16x16x64_bf16 v[230:233], v[52:55], v[164:171], v5\n"
      "ds_read_b128 v[164:167], v0 offset:1536\n"
      "v_cvt_pk_bf16_f32 v218, v215, v219\n"
      "v_smfmac_f32_16x16x64_bf16 v[248:251], v[52:55], v[20:27], v5\n"
      "buffer_store_dwordx2 v[188:189], v2, s[44:47], s101 offen offset:0 nt sc1\n"
      "v_smfmac_f32_16x16x64_bf16 v[120:123], v[52:55], v[204:211], v5\n"
      "ds_read_b128 v[104:107], v0 offset:13312\n"
      "v_cvt_pk_bf16_f32 v59, v254, v255\n"
      "v_mfma_f32_16x16x32_bf16 v[132:135], v[80:83], a[64:67], 0\n"
      "v_mfma_f32_16x16x32_bf16 v[132:135], v[108:111], a[68:71], v[132:135]\n"
      "ds_read_b128 v[168:171], v0 offset:512\n"
      "v_add_f32 v230, v230, v231\n"
      "v_mfma_f32_16x16x32_bf16 v[132:135], v[180:183], a[72:75], v[132:135]\n"
      "v_mfma_f32_16x16x32_bf16 v[132:135], v[116:119], a[76:79], v[132:135]\n"
      "v_cvt_pk_bf16_f32 v216, v213, v217\n"
      "v_cvt_pk_bf16_f32 v215, v222, v226\n"
      "v_mfma_f32_16x16x32_bf16 v[154:157], v[80:83], a[80:83], 0\n"
      "v_pk_max_i16 v39, v39, 0\n"
      "v_mov_b64 v[144:145], 0\n"
      "v_mfma_f32_16x16x32_bf16 v[154:157], v[108:111], a[84:87], v[154:157]\n"
      "v_cvt_pk_bf16_f32 v217, v221, v225\n"
      "v_mfma_f32_16x16x32_bf16 v[154:157], v[180:183], a[88:91], v[154:157]\n"
      "v_cvt_pk_bf16_f32 v111, v134, v135\n"
      "v_cvt_pk_bf16_f32 v219, v223, v227\n"
      "v_mfma_f32_16x16x32_bf16 v[154:157], v[116:119], a[92:95], v[154:157]\n"
      "v_pk_max_i16 v37, v37, 0\n"
      "v_cvt_pk_bf16_f32 v112, v76, v77\n"
      "v_mfma_f32_16x16x32_bf16 v[80:83], v[64:67], v[240:243], 0\n"
      "v_maximum3_f32 v8, v8, v128, v129\n"
      "v_pk_max_i16 v43, v43, 0\n"
      "v_mfma_f32_16x16x32_bf16 v[80:83], v[68:71], v[244:247], v[80:83]\n"
      "v_pk_max_i16 v219, v219, 0\n"
      "s_waitcnt lgkmcnt(5)\n"
      "v_mfma_f32_16x16x32_bf16 v[172:175], v[44:47], a[80:83], 0\n"
      "s_waitcnt lgkmcnt(0)\n"
      "v_mfma_f32_16x16x32_bf16 v[172:175], v[168:171], a[84:87], v[172:175]\n"
      "v_cvt_pk_bf16_f32 v213, v220, v224\n"
      "v_mfma_f32_16x16x32_bf16 v[172:175], v[148:151], a[88:91], v[172:175]\n"
      "v_mfma_f32_16x16x32_bf16 v[172:175], v[164:167], a[92:95], v[172:175]\n"
      "buffer_store_dwordx2 v[42:43], v2, s[44:47], s34 offen offset:768 nt sc1\n"
      "v_add_f32 v248, v248, v249\n"
      "v_mfma_f32_16x16x32_bf16 v[76:79], v[44:47], a[112:115], 0\n"
      "v_exp_f32 v80, v80\n"
      "v_mfma_f32_16x16x32_bf16 v[76:79], v[168:171], a[116:119], v[76:79]\n"
      "v_cvt_pk_bf16_f32 v182, v154, v155\n"
      "v_pk_max_i16 v212, v212, 0\n"
      "v_mfma_f32_16x16x32_bf16 v[76:79], v[148:151], a[120:123], v[76:79]\n"
      "v_exp_f32 v82, v82\n"
      "v_mfma_f32_16x16x32_bf16 v[76:79], v[164:167], a[124:127], v[76:79]\n"
      "v_cvt_pk_bf16_f32 v180, v172, v173\n"
      "v_mov_b64 v[146:147], 0\n"
      "v_mfma_f32_16x16x32_bf16 v[224:227], v[44:47], a[96:99], 0\n"
      "v_mfma_f32_16x16x32_bf16 v[224:227], v[168:171], a[100:103], v[224:227]\n"
      "buffer_load_dwordx4 v1, s[40:43], s75 offen offset:2048 lds\n"
      "v_mfma_f32_16x16x32_bf16 v[224:227], v[148:151], a[104:107], v[224:227]\n"
      "v_cvt_pk_bf16_f32 v110, v132, v133\n"
      "v_mfma_f32_16x16x32_bf16 v[224:227], v[164:167], a[108:111], v[224:227]\n"
      "v_mov_b64 v[116:117], 0\n"
      "v_cvt_pk_bf16_f32 v232, v76, v77\n"
      "v_smfmac_f32_16x16x64_bf16 v[152:155], v[52:55], v[64:71], v5\n"
      "v_cvt_pk_bf16_f32 v233, v78, v79\n"
      "v_smfmac_f32_16x16x64_bf16 v[116:119], v[52:55], v[136:143], v5\n"
      "v_mov_b32 v231, 0\n"
      "v_mfma_f32_16x16x32_bf16 v[132:135], v[44:47], a[64:67], 0\n"
      "v_exp_f32 v248, v248\n"
      "v_mfma_f32_16x16x32_bf16 v[132:135], v[168:171], a[68:71], v[132:135]\n"
      "v_exp_f32 v81, v81\n"
      "v_mfma_f32_16x16x32_bf16 v[132:135], v[148:151], a[72:75], v[132:135]\n"
      "v_exp_f32_dpp v231, v230 quad_perm:[0,1,2,3] row_mask:0xf bank_mask:0x1\n"
      "v_mfma_f32_16x16x32_bf16 v[132:135], v[164:167], a[76:79], v[132:135]\n"
      "v_exp_f32 v32, v32\n"
      "v_mfma_f32_16x16x32_bf16 v[76:79], a[32:35], v[44:47], 0\n"
      "v_cvt_pk_bf16_f32 v58, v252, v253\n"
      "v_mfma_f32_16x16x32_bf16 v[76:79], a[36:39], v[168:171], v[76:79]\n"
      "v_cvt_pk_bf16_f32 v183, v156, v157\n"
      "v_mfma_f32_16x16x32_bf16 v[76:79], a[40:43], v[148:151], v[76:79]\n"
      "v_mfma_f32_16x16x32_bf16 v[76:79], a[44:47], v[164:167], v[76:79]\n"
      "buffer_store_dwordx2 v[40:41], v2, s[44:47], s34 offen offset:256 nt sc1\n"
      "v_mov_b32_dpp v230, v231 quad_perm:[1,1,3,3] row_mask:0xf bank_mask:0xf\n"
      "v_mfma_f32_16x16x32_bf16 v[252:255], a[16:19], v[44:47], 0\n"
      "v_cvt_pk_bf16_f32 v128, v80, v81\n"
      "v_add_f32 v152, v152, v153\n"
      "v_mfma_f32_16x16x32_bf16 v[252:255], a[20:23], v[168:171], v[252:255]\n"
      "v_add_f32 v116, v116, v117\n"
      "v_add_f32 v120, v120, v121\n"
      "v_mfma_f32_16x16x32_bf16 v[252:255], a[24:27], v[148:151], v[252:255]\n"
      "v_cvt_pk_bf16_f32 v109, v134, v135\n"
      "v_pk_max_i16 v217, v217, 0\n"
      "v_mfma_f32_16x16x32_bf16 v[252:255], a[28:31], v[164:167], v[252:255]\n"
      "v_exp_f32 v35, v35\n"
      "v_mfma_f32_16x16x32_bf16 v[220:223], v[204:207], v[240:243], 0\n"
      "v_cvt_pk_bf16_f32 v57, v226, v227\n"
      "v_cvt_pk_bf16_f32 v230, v231, v230\n"
      "v_mfma_f32_16x16x32_bf16 v[220:223], v[208:211], v[244:247], v[220:223]\n"
      "v_exp_f32 v83, v83\n"
      "v_mfma_f32_16x16x32_bf16 v[154:157], a[48:51], v[44:47], 0\n"
      "v_exp_f32 v116, v116\n"
      "v_mfma_f32_16x16x32_bf16 v[154:157], a[52:55], v[168:171], v[154:157]\n"
      "v_cvt_pk_bf16_f32 v181, v174, v175\n"
      "v_pk_max_i16 v218, v218, 0\n"
      "v_mfma_f32_16x16x32_bf16 v[154:157], a[56:59], v[148:151], v[154:157]\n"
      "v_cvt_pk_bf16_f32 v108, v132, v133\n"
      "v_cvt_pk_bf16_f32 v130, v32, v33\n"
      "v_mfma_f32_16x16x32_bf16 v[154:157], a[60:63], v[164:167], v[154:157]\n"
      "v_exp_f32 v222, v222\n"
      "v_mfma_f32_16x16x32_bf16 v[132:135], a[0:3], v[44:47], 0\n"
      "v_mov_b64 v[172:173], 0\n"
      "v_pk_max_i16 v214, v214, 0\n"
      "v_mfma_f32_16x16x32_bf16 v[132:135], a[4:7], v[168:171], v[132:135]\n"
      "v_cvt_pk_bf16_f32 v56, v224, v225\n"
      "v_mov_b64 v[224:225], 0\n"
      "v_mfma_f32_16x16x32_bf16 v[132:135], a[8:11], v[148:151], v[132:135]\n"
      "v_exp_f32 v223, v223\n"
      "v_mfma_f32_16x16x32_bf16 v[132:135], a[12:15], v[164:167], v[132:135]\n"
      "v_exp_f32 v152, v152\n"
      "v_mfma_f32_16x16x32_bf16 v[40:43], a[48:51], v[192:195], 0\n"
      "v_cvt_pk_bf16_f32 v169, v78, v79\n"
      "v_cndmask_b32_dpp v153, v116, v152, vcc quad_perm:[0,0,2,2] row_mask:0xf bank_mask:0xf\n"
      "v_mfma_f32_16x16x32_bf16 v[40:43], a[52:55], v[200:203], v[40:43]\n"
      "v_cvt_pk_bf16_f32 v171, v156, v157\n"
      "v_cvt_pk_bf16_f32 v166, v252, v253\n"
      "v_mfma_f32_16x16x32_bf16 v[40:43], a[56:59], v[104:107], v[40:43]\n"
      "v_cvt_pk_bf16_f32 v131, v34, v35\n"
      "v_cvt_pk_bf16_f32 v170, v154, v155\n"
      "v_mfma_f32_16x16x32_bf16 v[40:43], a[60:63], v[48:51], v[40:43]\n"
      "v_cvt_pk_bf16_f32 v129, v82, v83\n"
      "v_cvt_pk_bf16_f32 v165, v134, v135\n"
      "v_mfma_f32_16x16x32_bf16 v[148:151], a[0:3], v[192:195], 0\n"
      "v_mfma_f32_16x16x32_bf16 v[148:151], a[4:7], v[200:203], v[148:151]\n"
      "v_mov_b32_dpp v44, v160 row_ror:8 row_mask:0xf bank_mask:0xf\n"
      "v_mfma_f32_16x16x32_bf16 v[148:151], a[8:11], v[104:107], v[148:151]\n"
      "v_exp_f32 v221, v221\n"
      "v_mfma_f32_16x16x32_bf16 v[148:151], a[12:15], v[48:51], v[148:151]\n"
      "buffer_store_dwordx2 v[36:37], v2, s[44:47], s34 offen offset:0 nt sc1\n"
      "v_pk_max_i16 v213, v213, 0\n"
      "v_mfma_f32_16x16x32_bf16 v[32:35], a[16:19], v[192:195], 0\n"
      "v_cvt_pk_bf16_f32 v82, v40, v41\n"
      "v_cvt_pk_bf16_f32 v167, v254, v255\n"
      "v_mfma_f32_16x16x32_bf16 v[32:35], a[20:23], v[200:203], v[32:35]\n"
      "v_cvt_pk_bf16_f32 v168, v76, v77\n"
      "v_cvt_pk_bf16_f32 v164, v132, v133\n"
      "v_mfma_f32_16x16x32_bf16 v[32:35], a[24:27], v[104:107], v[32:35]\n"
      "buffer_load_dwordx4 v1, s[40:43], s76 offen offset:1024 lds\n"
      "v_mfma_f32_16x16x32_bf16 v[32:35], a[28:31], v[48:51], v[32:35]\n"
      "v_cvt_pk_bf16_f32 v76, v148, v149\n"
      "v_pk_max_i16 v216, v216, 0\n"
      "v_mfma_f32_16x16x32_bf16 v[132:135], a[32:35], v[192:195], 0\n"
      "v_exp_f32 v120, v120\n"
      "v_mfma_f32_16x16x32_bf16 v[132:135], a[36:39], v[200:203], v[132:135]\n"
      "v_mov_b32_dpp v45, v161 row_ror:8 row_mask:0xf bank_mask:0xf\n"
      "v_cvt_pk_bf16_f32 v251, v222, v223\n"
      "v_mfma_f32_16x16x32_bf16 v[132:135], a[40:43], v[104:107], v[132:135]\n"
      "v_exp_f32 v220, v220\n"
      "v_mfma_f32_16x16x32_bf16 v[132:135], a[44:47], v[48:51], v[132:135]\n"
      "v_cvt_pk_bf16_f32 v77, v150, v151\n"
      "s_add_u32 m0, s53, 12288\n"
      "v_smfmac_f32_16x16x64_bf16 v[224:227], v[52:55], v[164:171], v5\n"
      "v_cvt_pk_bf16_f32 v78, v32, v33\n"
      "v_mfma_f32_16x16x32_bf16 v[252:255], v[164:167], v[240:243], 0\n"
      "v_cvt_pk_bf16_f32 v83, v42, v43\n"
      "v_pk_max_i16 v215, v215, 0\n"
      "v_mfma_f32_16x16x32_bf16 v[252:255], v[168:171], v[244:247], v[252:255]\n"
      "v_cvt_pk_bf16_f32 v79, v34, v35\n"
      "v_cvt_pk_bf16_f32 v81, v134, v135\n"
      "v_mfma_f32_16x16x32_bf16 v[40:43], v[20:23], v[240:243], 0\n"
      "v_add_f32 v224, v224, v225\n"
      "v_cvt_pk_bf16_f32 v80, v132, v133\n"
      "v_mfma_f32_16x16x32_bf16 v[40:43], v[24:27], v[244:247], v[40:43]\n"
      "v_exp_f32 v224, v224\n"
      "v_smfmac_f32_16x16x64_bf16 v[172:175], v[52:55], v[76:83], v5\n"
      "buffer_load_dwordx4 v1, s[40:43], s77 offen offset:0 lds\n"
      "v_mfma_f32_16x16x32_bf16 v[52:55], a[176:179], v[124:127], 0\n"
      "v_exp_f32 v253, v253\n"
      "v_mfma_f32_16x16x32_bf16 v[52:55], a[180:183], v[100:103], v[52:55]\n"
      "v_cvt_pk_bf16_f32 v250, v220, v221\n"
      "v_mov_b32_dpp v47, v163 row_ror:8 row_mask:0xf bank_mask:0xf\n"
      "v_mfma_f32_16x16x32_bf16 v[52:55], a[184:187], v[72:75], v[52:55]\n"
      "v_exp_f32 v252, v252\n"
      "v_mfma_f32_16x16x32_bf16 v[52:55], a[188:191], v[196:199], v[52:55]\n"
      "v_exp_f32 v41, v41\n"
      "v_mfma_f32_16x16x32_bf16 v[220:223], a[160:163], v[124:127], 0\n"
      "v_exp_f32 v254, v254\n"
      "v_mfma_f32_16x16x32_bf16 v[220:223], a[164:167], v[100:103], v[220:223]\n"
      "v_add_f32 v172, v172, v173\n"
      "v_cndmask_b32_dpp v225, v248, v224, vcc quad_perm:[0,0,2,2] row_mask:0xf bank_mask:0xf\n"
      "v_mfma_f32_16x16x32_bf16 v[220:223], a[168:171], v[72:75], v[220:223]\n"
      "v_exp_f32 v43, v43\n"
      "v_mfma_f32_16x16x32_bf16 v[220:223], a[172:175], v[196:199], v[220:223]\n"
      "v_cvt_pk_bf16_f32 v35, v54, v55\n"
      "v_mfma_f32_16x16x32_bf16 v[132:135], a[144:147], v[124:127], 0\n"
      "v_exp_f32 v172, v172\n"
      "v_mfma_f32_16x16x32_bf16 v[132:135], a[148:151], v[100:103], v[132:135]\n"
      "v_cvt_pk_bf16_f32 v34, v52, v53\n"
      "v_cndmask_b32_dpp v121, v172, v120, vcc quad_perm:[0,0,2,2] row_mask:0xf bank_mask:0xf\n"
      "v_mfma_f32_16x16x32_bf16 v[132:135], a[152:155], v[72:75], v[132:135]\n"
      "s_not_b64 vcc, vcc\n"
      "v_cndmask_b32_dpp v249, v224, v248, vcc quad_perm:[1,1,3,3] row_mask:0xf bank_mask:0xf\n"
      "v_add_f32 v224, v224, v120\n"
      "v_mfma_f32_16x16x32_bf16 v[132:135], a[156:159], v[196:199], v[132:135]\n"
      "v_add_f32 v248, v248, v172\n"
      "v_cndmask_b32_dpp v117, v152, v116, vcc quad_perm:[1,1,3,3] row_mask:0xf bank_mask:0xf\n"
      "v_mfma_f32_16x16x32_bf16 v[52:55], v[76:79], v[240:243], 0\n"
      "v_cvt_pk_bf16_f32 v153, v153, v117\n"
      "v_cvt_pk_bf16_f32 v32, v220, v221\n"
      "v_mfma_f32_16x16x32_bf16 v[52:55], v[80:83], v[244:247], v[52:55]\n"
      "buffer_store_dwordx2 v[38:39], v2, s[44:47], s34 offen offset:512 nt sc1\n"
      "v_add_f32 v152, v152, v116\n"
      "v_mfma_f32_16x16x32_bf16 v[116:119], v[192:195], a[96:99], 0\n"
      "v_add_f32 v224, v224, v248\n"
      "v_cvt_pk_bf16_f32 v225, v225, v249\n"
      "v_mfma_f32_16x16x32_bf16 v[116:119], v[200:203], a[100:103], v[116:119]\n"
      "v_add_f32 v152, v152, v231\n"
      "v_cvt_pk_bf16_f32 v30, v132, v133\n"
      "v_mfma_f32_16x16x32_bf16 v[116:119], v[104:107], a[104:107], v[116:119]\n"
      "v_cvt_pk_bf16_f32 v33, v222, v223\n"
      "v_cvt_pk_bf16_f32 v31, v134, v135\n"
      "v_mfma_f32_16x16x32_bf16 v[116:119], v[48:51], a[108:111], v[116:119]\n"
      "ds_bpermute_b32 v174, v6, v225 offset:16\n"
      "v_mfma_f32_16x16x32_bf16 v[220:223], v[20:23], v[28:31], 0\n"
      "v_exp_f32 v54, v54\n"
      "v_mfma_f32_16x16x32_bf16 v[220:223], v[24:27], v[32:35], v[220:223]\n"
      "v_cvt_pk_bf16_f32 v248, v252, v253\n"
      "v_cndmask_b32_dpp v173, v120, v172, vcc quad_perm:[1,1,3,3] row_mask:0xf bank_mask:0xf\n"
      "v_mfma_f32_16x16x32_bf16 v[36:39], v[76:79], v[28:31], 0\n"
      "v_cvt_pk_bf16_f32 v121, v121, v173\n"
      "v_add_f32 v18, v224, v152\n"
      "v_mfma_f32_16x16x32_bf16 v[36:39], v[80:83], v[32:35], v[36:39]\n"
      "ds_bpermute_b32 v173, v6, v121\n"
      "v_mfma_f32_16x16x32_bf16 v[132:135], v[192:195], a[80:83], 0\n"
      "v_exp_f32 v40, v40\n"
      "v_mfma_f32_16x16x32_bf16 v[132:135], v[200:203], a[84:87], v[132:135]\n"
      "v_exp_f32 v52, v52\n"
      "v_mfma_f32_16x16x32_bf16 v[132:135], v[104:107], a[88:91], v[132:135]\n"
      "v_add_f32_dpp v18, v18, v18 row_ror:8 row_mask:0xf bank_mask:0xf\n"
      "v_mov_b32_dpp v46, v162 row_ror:8 row_mask:0xf bank_mask:0xf\n"
      "v_mfma_f32_16x16x32_bf16 v[132:135], v[48:51], a[92:95], v[132:135]\n"
      "v_exp_f32 v42, v42\n"
      "v_smfmac_f32_16x16x64_bf16 v[144:147], v[160:163], v[240:247], v5\n"
      "ds_bpermute_b32 v158, v7, v230 offset:248\n"
      "v_smfmac_f32_16x16x64_bf16 v[144:147], v[44:47], v[28:35], v5\n"
      "ds_bpermute_b32 v175, v6, v121 offset:16\n"
      "v_mfma_f32_16x16x32_bf16 v[120:123], v[204:207], v[28:31], 0\n"
      "v_exp_f32 v39, v39\n"
      "v_mfma_f32_16x16x32_bf16 v[120:123], v[208:211], v[32:35], v[120:123]\n"
      "v_exp_f32 v223, v223\n"
      "v_mfma_f32_16x16x32_bf16 v[148:151], v[164:167], v[28:31], 0\n"
      "v_exp_f32 v255, v255\n"
      "v_mfma_f32_16x16x32_bf16 v[148:151], v[168:171], v[32:35], v[148:151]\n"
      "v_cvt_pk_bf16_f32 v187, v134, v135\n"
      "v_cvt_pk_bf16_f32 v186, v132, v133\n"
      "v_mfma_f32_16x16x32_bf16 v[244:247], v[64:67], v[28:31], 0\n"
      "buffer_load_dwordx4 v1, s[40:43], s78 offen offset:1024 lds\n"
      "v_mfma_f32_16x16x32_bf16 v[244:247], v[68:71], v[32:35], v[244:247]\n"
      "buffer_store_dwordx2 v[218:219], v2, s[44:47], s35 offen offset:768 nt sc1\n"
      "v_cvt_pk_bf16_f32 v249, v254, v255\n"
      "v_mfma_f32_16x16x32_bf16 v[132:135], v[92:95], a[64:67], 0\n"
      "v_exp_f32 v151, v151\n"
      "v_mfma_f32_16x16x32_bf16 v[132:135], v[88:91], a[68:71], v[132:135]\n"
      "v_exp_f32 v149, v149\n"
      "v_mfma_f32_16x16x32_bf16 v[132:135], v[96:99], a[72:75], v[132:135]\n"
      "v_exp_f32 v122, v122\n"
      "v_mfma_f32_16x16x32_bf16 v[132:135], v[176:179], a[76:79], v[132:135]\n"
      "v_exp_f32 v53, v53\n"
      "v_mfma_f32_16x16x32_bf16 v[240:243], v[136:139], v[28:31], 0\n"
      "v_exp_f32 v245, v245\n"
      "v_mfma_f32_16x16x32_bf16 v[240:243], v[140:143], v[32:35], v[240:243]\n"
      "v_cvt_pk_bf16_f32 v253, v42, v43\n"
      "v_cvt_pk_bf16_f32 v62, v116, v117\n"
      "v_mfma_f32_16x16x32_bf16 v[32:35], v[192:195], a[112:115], 0\n"
      "v_exp_f32 v55, v55\n"
      "v_mfma_f32_16x16x32_bf16 v[32:35], v[200:203], a[116:119], v[32:35]\n"
      "ds_bpermute_b32 v159, v7, v230 offset:8\n"
      "v_mfma_f32_16x16x32_bf16 v[32:35], v[104:107], a[120:123], v[32:35]\n"
      "v_cvt_pk_bf16_f32 v254, v52, v53\n"
      "v_cvt_pk_bf16_f32 v63, v118, v119\n"
      "v_mfma_f32_16x16x32_bf16 v[32:35], v[48:51], a[124:127], v[32:35]\n"
      "v_exp_f32 v120, v120\n"
      "v_mfma_f32_16x16x32_bf16 v[28:31], v[192:195], a[64:67], 0\n"
      "v_exp_f32 v222, v222\n"
      "v_mfma_f32_16x16x32_bf16 v[28:31], v[200:203], a[68:71], v[28:31]\n"
      "v_exp_f32 v150, v150\n"
      "v_mfma_f32_16x16x32_bf16 v[28:31], v[104:107], a[72:75], v[28:31]\n"
      "v_exp_f32 v246, v246\n"
      "v_mfma_f32_16x16x32_bf16 v[28:31], v[48:51], a[76:79], v[28:31]\n"
      "ds_bpermute_b32 v172, v6, v225\n"
      "v_mfma_f32_16x16x32_bf16 v[116:119], v[124:127], a[64:67], 0\n"
      "v_exp_f32 v38, v38\n"
      "v_mfma_f32_16x16x32_bf16 v[116:119], v[100:103], a[68:71], v[116:119]\n"
      "v_exp_f32 v123, v123\n"
      "v_mfma_f32_16x16x32_bf16 v[116:119], v[72:75], a[72:75], v[116:119]\n"
      "v_exp_f32 v148, v148\n"
      "v_mfma_f32_16x16x32_bf16 v[116:119], v[196:199], a[76:79], v[116:119]\n"
      "v_exp_f32 v121, v121\n"
      "v_mfma_f32_16x16x32_bf16 v[224:227], v[124:127], a[112:115], 0\n"
      "v_cvt_pk_bf16_f32 v115, v30, v31\n"
      "v_cvt_pk_bf16_f32 v114, v28, v29\n"
      "v_mfma_f32_16x16x32_bf16 v[224:227], v[100:103], a[116:119], v[224:227]\n"
      "v_cvt_pk_bf16_f32 v29, v150, v151\n"
      "v_cvt_pk_bf16_f32 v255, v54, v55\n"
      "v_mfma_f32_16x16x32_bf16 v[224:227], v[72:75], a[120:123], v[224:227]\n"
      "v_exp_f32 v221, v221\n"
      "v_mfma_f32_16x16x32_bf16 v[224:227], v[196:199], a[124:127], v[224:227]\n"
      "buffer_store_dwordx2 v[216:217], v2, s[44:47], s35 offen offset:256 nt sc1\n"
      "v_add_f32 v146, v146, v147\n"
      "v_mfma_f32_16x16x32_bf16 v[154:157], v[124:127], a[80:83], 0\n"
      "v_cvt_pk_bf16_f32 v28, v148, v149\n"
      "v_cvt_pk_bf16_f32 v30, v120, v121\n"
      "v_mfma_f32_16x16x32_bf16 v[154:157], v[100:103], a[84:87], v[154:157]\n"
      "v_exp_f32 v241, v241\n"
      "v_mfma_f32_16x16x32_bf16 v[154:157], v[72:75], a[88:91], v[154:157]\n"
      "v_exp_f32 v242, v242\n"
      "v_mfma_f32_16x16x32_bf16 v[154:157], v[196:199], a[92:95], v[154:157]\n"
      "v_cvt_pk_bf16_f32 v229, v226, v227\n"
      "v_cvt_pk_bf16_f32 v252, v40, v41\n"
      "v_mfma_f32_16x16x32_bf16 v[40:43], v[92:95], a[112:115], 0\n"
      "v_exp_f32 v247, v247\n"
      "v_mfma_f32_16x16x32_bf16 v[40:43], v[88:91], a[116:119], v[40:43]\n"
      "v_exp_f32 v36, v36\n"
      "v_mfma_f32_16x16x32_bf16 v[40:43], v[96:99], a[120:123], v[40:43]\n"
      "v_exp_f32 v244, v244\n"
      "v_mfma_f32_16x16x32_bf16 v[40:43], v[176:179], a[124:127], v[40:43]\n"
      "v_exp_f32 v37, v37\n"
      "v_mfma_f32_16x16x32_bf16 v[148:151], v[124:127], a[96:99], 0\n"
      "v_cvt_pk_bf16_f32 v31, v122, v123\n"
      "v_cvt_pk_bf16_f32 v52, v116, v117\n"
      "v_mfma_f32_16x16x32_bf16 v[148:151], v[100:103], a[100:103], v[148:151]\n"
      "v_cvt_pk_bf16_f32 v239, v34, v35\n"
      "v_cvt_pk_bf16_f32 v228, v224, v225\n"
      "v_mfma_f32_16x16x32_bf16 v[148:151], v[72:75], a[104:107], v[148:151]\n"
      "v_exp_f32 v243, v243\n"
      "v_mfma_f32_16x16x32_bf16 v[148:151], v[196:199], a[108:111], v[148:151]\n"
      "v_cvt_pk_bf16_f32 v55, v134, v135\n"
      "v_cvt_pk_bf16_f32 v54, v132, v133\n"
      "v_mfma_f32_16x16x32_bf16 v[216:219], a[176:179], v[192:195], 0\n"
      "v_cvt_pk_bf16_f32 v53, v118, v119\n"
      "v_cvt_pk_bf16_f32 v230, v40, v41\n"
      "v_mfma_f32_16x16x32_bf16 v[216:219], a[180:183], v[200:203], v[216:219]\n"
      "v_cvt_pk_bf16_f32 v121, v156, v157\n"
      "v_cvt_pk_bf16_f32 v120, v154, v155\n"
      "v_mfma_f32_16x16x32_bf16 v[216:219], a[184:187], v[104:107], v[216:219]\n"
      "v_exp_f32 v220, v220\n"
      "v_mfma_f32_16x16x32_bf16 v[216:219], a[188:191], v[48:51], v[216:219]\n"
      "v_exp_f32 v240, v240\n"
      "v_mfma_f32_16x16x32_bf16 v[224:227], v[92:95], a[96:99], 0\n"
      "v_cvt_pk_bf16_f32 v134, v240, v241\n"
      "v_mfma_f32_16x16x32_bf16 v[224:227], v[88:91], a[100:103], v[224:227]\n"
      "v_cvt_pk_bf16_f32 v135, v242, v243\n"
      "v_cvt_pk_bf16_f32 v231, v42, v43\n"
      "v_mfma_f32_16x16x32_bf16 v[224:227], v[96:99], a[104:107], v[224:227]\n"
      "v_cvt_pk_bf16_f32 v132, v244, v245\n"
      "v_cvt_pk_bf16_f32 v42, v216, v217\n"
      "v_mfma_f32_16x16x32_bf16 v[224:227], v[176:179], a[108:111], v[224:227]\n"
      "buffer_store_dwordx2 v[212:213], v2, s[44:47], s35 offen offset:0 nt sc1\n"
      "v_cvt_pk_bf16_f32 v133, v246, v247\n"
      "v_mfma_f32_16x16x32_bf16 v[240:243], a[144:147], v[192:195], 0\n"
      "v_cvt_pk_bf16_f32 v238, v32, v33\n"
      "v_add_f32 v144, v144, v145\n"
      "v_mfma_f32_16x16x32_bf16 v[240:243], a[148:151], v[200:203], v[240:243]\n"
      "v_exp_f32 v144, v144\n"
      "v_mfma_f32_16x16x32_bf16 v[240:243], a[152:155], v[104:107], v[240:243]\n"
      "v_exp_f32 v146, v146\n"
      "v_mfma_f32_16x16x32_bf16 v[240:243], a[156:159], v[48:51], v[240:243]\n"
      "v_cvt_pk_bf16_f32 v35, v38, v39\n"
      "v_cvt_pk_bf16_f32 v34, v36, v37\n"
      "v_mfma_f32_16x16x32_bf16 v[244:247], v[92:95], a[80:83], 0\n"
      "v_cvt_pk_bf16_f32 v43, v218, v219\n"
      "v_cvt_pk_bf16_f32 v117, v150, v151\n"
      "v_mfma_f32_16x16x32_bf16 v[244:247], v[88:91], a[84:87], v[244:247]\n"
      "ds_read_b64 v[156:157], v11 offset:0\n"
      "v_cvt_pk_bf16_f32 v116, v148, v149\n"
      "v_mfma_f32_16x16x32_bf16 v[244:247], v[96:99], a[88:91], v[244:247]\n"
      "v_cvt_pk_bf16_f32 v149, v144, v146\n"
      "v_cvt_pk_bf16_f32 v119, v226, v227\n"
      "v_mfma_f32_16x16x32_bf16 v[244:247], v[176:179], a[92:95], v[244:247]\n"
      "v_cvt_pk_bf16_f32 v39, v242, v243\n"
      "v_cvt_pk_bf16_f32 v118, v224, v225\n"
      "v_mfma_f32_16x16x32_bf16 v[224:227], a[128:131], v[192:195], 0\n"
      "v_cvt_pk_bf16_f32 v148, v144, v146\n"
      "v_cvt_pk_bf16_f32 v38, v240, v241\n"
      "v_mfma_f32_16x16x32_bf16 v[224:227], a[132:135], v[200:203], v[224:227]\n"
      "ds_bpermute_b32 v145, v7, v153 offset:4\n"
      "v_mfma_f32_16x16x32_bf16 v[224:227], a[136:139], v[104:107], v[224:227]\n"
      "v_permlane16_swap_b32 v148, v149\n"
      "v_mfma_f32_16x16x32_bf16 v[224:227], a[140:143], v[48:51], v[224:227]\n"
      "v_cvt_pk_bf16_f32 v32, v220, v221\n"
      "v_cvt_pk_bf16_f32 v33, v222, v223\n"
      "v_mfma_f32_16x16x32_bf16 v[220:223], a[160:163], v[192:195], 0\n"
      "v_cvt_pk_bf16_f32 v122, v244, v245\n"
      "v_cvt_pk_bf16_f32 v123, v246, v247\n"
      "v_mfma_f32_16x16x32_bf16 v[220:223], a[164:167], v[200:203], v[220:223]\n"
      "v_add_f32 v245, v144, v146\n"
      "v_add_f32 v244, v144, v146\n"
      "v_mfma_f32_16x16x32_bf16 v[220:223], a[168:171], v[104:107], v[220:223]\n"
      "s_nop 0\n"
      "v_permlane16_swap_b32 v244, v245\n"
      "v_mfma_f32_16x16x32_bf16 v[220:223], a[172:175], v[48:51], v[220:223]\n"
      "v_cvt_pk_bf16_f32 v36, v224, v225\n"
      "v_mov_b64 v[188:189], v[244:245]\n"
      "v_smfmac_f32_16x16x64_bf16 v[244:247], v[12:15], v[248:255], v5\n"
      "buffer_store_dwordx2 v[214:215], v2, s[44:47], s35 offen offset:512 nt sc1\n"
      "v_cvt_pk_bf16_f32 v37, v226, v227\n"
      "v_smfmac_f32_16x16x64_bf16 v[188:191], v[12:15], v[28:35], v5\n"
      "s_nop 1\n"
      "v_cvt_pk_bf16_f32 v40, v220, v221\n"
      "v_smfmac_f32_16x16x32_bf16 v[244:247], v[12:13], v[128:131], v5\n"
      "ds_bpermute_b32 v144, v7, v153\n"
      "v_cvt_pk_bf16_f32 v41, v222, v223\n"
      "s_waitcnt lgkmcnt(2)\n"
      "v_mfma_f32_16x16x4_4b_bf16 v[212:227], v[148:149], v[156:157], 0 cbsz:2 abid:0\n"
      "s_nop 3\n"
      "v_add_f32 v244, v244, v245\n"
      "v_rcp_f32 v245, v244\n"
      "v_smfmac_f32_16x16x32_bf16 v[188:191], v[12:13], v[132:135], v5\n"
      "ds_bpermute_b32 v246, v4, v245\n"
      "v_add_u32 v16, 64, v16\n"
      "s_nop 0\n"
      "v_mfma_f32_16x16x32_bf16 v[216:219], v[248:251], v[180:183], v[216:219]\n"
      "v_add_f32_dpp v18, v18, v18 row_ror:4 row_mask:0xf bank_mask:0xf\n"
      "v_mfma_f32_16x16x32_bf16 v[216:219], v[252:255], v[184:187], v[216:219]\n"
      "buffer_load_dwordx4 v1, s[40:43], s78 offen offset:3072 lds\n"
      "v_mfma_f32_16x16x32_bf16 v[216:219], v[128:131], v[120:123], v[216:219]\n"
      "v_mfma_f32_16x16x32_bf16 v[212:215], v[248:251], v[108:111], v[212:215]\n"
      "v_mfma_f32_16x16x32_bf16 v[212:215], v[252:255], v[112:115], v[212:215]\n"
      "v_add_f32 v188, v188, v189\n"
      "v_add_f32_dpp v18, v18, v18 row_ror:2 row_mask:0xf bank_mask:0xf\n"
      "v_mfma_f32_16x16x32_bf16 v[212:215], v[128:131], v[52:55], v[212:215]\n"
      "v_rcp_f32 v189, v188\n"
      "v_mfma_f32_16x16x32_bf16 v[224:227], v[248:251], v[232:235], v[224:227]\n"
      "v_mfma_f32_16x16x32_bf16 v[224:227], v[252:255], v[236:239], v[224:227]\n"
      "ds_bpermute_b32 v190, v4, v189 offset:128\n"
      "v_mfma_f32_16x16x32_bf16 v[224:227], v[128:131], v[228:231], v[224:227]\n"
      "s_waitcnt lgkmcnt(1)\n"
      "v_mul_f32_dpp v217, v246, v217 row_newbcast:4 row_mask:0xf bank_mask:0xf\n"
      "v_add_f32_dpp v18, v18, v18 row_ror:1 row_mask:0xf bank_mask:0xf\n"
      "v_mfma_f32_16x16x32_bf16 v[220:223], v[248:251], v[56:59], v[220:223]\n"
      "v_mul_f32_dpp v219, v246, v219 row_newbcast:12 row_mask:0xf bank_mask:0xf\n"
      "v_mul_f32_dpp v212, v246, v212 row_newbcast:0 row_mask:0xf bank_mask:0xf\n"
      "v_mfma_f32_16x16x32_bf16 v[220:223], v[252:255], v[60:63], v[220:223]\n"
      "v_mul_f32_dpp v213, v246, v213 row_newbcast:4 row_mask:0xf bank_mask:0xf\n"
      "v_mul_f32_dpp v218, v246, v218 row_newbcast:8 row_mask:0xf bank_mask:0xf\n"
      "v_mfma_f32_16x16x32_bf16 v[220:223], v[128:131], v[116:119], v[220:223]\n"
      "v_mul_f32_dpp v216, v246, v216 row_newbcast:0 row_mask:0xf bank_mask:0xf\n"
      "v_mfma_f32_16x16x32_bf16 v[84:87], v[64:67], v[36:39], 0\n"
      "buffer_load_dwordx4 v1, s[40:43], s77 offen offset:2048 lds\n"
      "v_mfma_f32_16x16x32_bf16 v[84:87], v[68:71], v[40:43], v[84:87]\n"
      "v_mul_f32_dpp v226, v246, v226 row_newbcast:8 row_mask:0xf bank_mask:0xf\n"
      "v_mfma_f32_16x16x32_bf16 v[216:219], v[92:95], a[208:211], v[216:219]\n"
      "v_rcp_f32 v19, v18\n"
      "v_mfma_f32_16x16x32_bf16 v[216:219], v[88:91], a[212:215], v[216:219]\n"
      "v_mul_f32_dpp v214, v246, v214 row_newbcast:8 row_mask:0xf bank_mask:0xf\n"
      "v_mfma_f32_16x16x32_bf16 v[216:219], v[96:99], a[216:219], v[216:219]\n"
      "v_mul_f32_dpp v215, v246, v215 row_newbcast:12 row_mask:0xf bank_mask:0xf\n"
      "v_mul_f32_dpp v223, v246, v223 row_newbcast:12 row_mask:0xf bank_mask:0xf\n"
      "v_mfma_f32_16x16x32_bf16 v[216:219], v[176:179], a[220:223], v[216:219]\n"
      "v_maximum3_f32 v8, v8, v18, v19\n"
      "v_mul_f32_dpp v221, v246, v221 row_newbcast:4 row_mask:0xf bank_mask:0xf\n"
      "v_mfma_f32_16x16x32_bf16 v[212:215], v[92:95], a[192:195], v[212:215]\n"
      "v_mul_f32_dpp v220, v246, v220 row_newbcast:0 row_mask:0xf bank_mask:0xf\n"
      "v_mul_f32_dpp v222, v246, v222 row_newbcast:8 row_mask:0xf bank_mask:0xf\n"
      "v_mfma_f32_16x16x32_bf16 v[212:215], v[88:91], a[196:199], v[212:215]\n"
      "ds_read_b128 v[128:131], v0 offset:8192\n"
      "v_mfma_f32_16x16x32_bf16 v[212:215], v[96:99], a[200:203], v[212:215]\n"
      "v_add_u32 v9, 64, v9\n"
      "v_maximum3_f32 v8, v8, v244, v245\n"
      "v_mfma_f32_16x16x32_bf16 v[212:215], v[176:179], a[204:207], v[212:215]\n"
      "v_mfma_f32_16x16x32_bf16 v[220:223], v[92:95], a[224:227], v[220:223]\n"
      "v_exp_f32 v87, v87\n"
      "v_mfma_f32_16x16x32_bf16 v[220:223], v[88:91], a[228:231], v[220:223]\n"
      "v_mul_f32_dpp v225, v246, v225 row_newbcast:4 row_mask:0xf bank_mask:0xf\n"
      "v_mul_f32_dpp v224, v246, v224 row_newbcast:0 row_mask:0xf bank_mask:0xf\n"
      "v_mfma_f32_16x16x32_bf16 v[220:223], v[96:99], a[232:235], v[220:223]\n"
      "v_mul_f32_dpp v227, v246, v227 row_newbcast:12 row_mask:0xf bank_mask:0xf\n"
      "v_mfma_f32_16x16x32_bf16 v[220:223], v[176:179], a[236:239], v[220:223]\n"
      "L_h4_%=:\n"
      "s_cbranch_execz L_hr_%=\n"
      "ds_read_b128 v[152:155], v0 offset:8704\n"
      "v_mfma_f32_16x16x32_bf16 v[224:227], v[92:95], a[240:243], v[224:227]\n"
      "v_exp_f32 v84, v84\n"
      "v_mfma_f32_16x16x32_bf16 v[224:227], v[88:91], a[244:247], v[224:227]\n"
      "v_cvt_pk_bf16_f32 v212, v212, v216\n"
      "v_pk_max_i16 v212, v212, 0\n"
      "v_mfma_f32_16x16x32_bf16 v[224:227], v[96:99], a[248:251], v[224:227]\n"
      "v_exp_f32 v85, v85\n"
      "v_mfma_f32_16x16x32_bf16 v[224:227], v[176:179], a[252:255], v[224:227]\n"
      "v_cvt_pk_bf16_f32 v214, v214, v218\n"
      "v_pk_max_i16 v214, v214, 0\n"
      "v_mfma_f32_16x16x4_4b_bf16 v[240:255], v[148:149], v[156:157], 0 cbsz:2 abid:2\n"
      "ds_read_b128 v[148:151], v0 offset:9216\n"
      "v_cvt_pk_bf16_f32 v216, v213, v217\n"
      "v_cvt_pk_bf16_f32 v218, v215, v219\n"
      "s_nop 1\n"
      "v_cvt_pk_bf16_f32 v215, v222, v226\n"
      "v_pk_max_i16 v215, v215, 0\n"
      "v_mfma_f32_16x16x32_bf16 v[176:179], v[136:139], v[36:39], 0\n"
      "v_cvt_pk_bf16_f32 v213, v220, v224\n"
      "v_cvt_pk_bf16_f32 v217, v221, v225\n"
      "v_mfma_f32_16x16x32_bf16 v[176:179], v[140:143], v[40:43], v[176:179]\n"
      "buffer_store_dwordx2 v[214:215], v2, s[44:47], s73 offen offset:512 nt sc1\n"
      "v_cvt_pk_bf16_f32 v219, v223, v227\n"
      "v_mfma_f32_16x16x32_bf16 v[248:251], v[28:31], v[56:59], v[248:251]\n"
      "ds_read_b128 v[224:227], v0 offset:9728\n"
      "v_pk_max_i16 v217, v217, 0\n"
      "v_mfma_f32_16x16x32_bf16 v[248:251], v[32:35], v[60:63], v[248:251]\n"
      "v_exp_f32 v86, v86\n"
      "v_mfma_f32_16x16x32_bf16 v[248:251], v[132:135], v[116:119], v[248:251]\n"
      "v_exp_f32 v178, v178\n"
      "v_mfma_f32_16x16x32_bf16 v[240:243], v[28:31], v[108:111], v[240:243]\n"
      "v_exp_f32 v177, v177\n"
      "v_mfma_f32_16x16x32_bf16 v[240:243], v[32:35], v[112:115], v[240:243]\n"
      "v_cvt_pk_bf16_f32 v221, v86, v87\n"
      "v_cvt_pk_bf16_f32 v220, v84, v85\n"
      "v_mfma_f32_16x16x32_bf16 v[240:243], v[132:135], v[52:55], v[240:243]\n"
      "v_exp_f32 v179, v179\n"
      "v_mfma_f32_16x16x32_bf16 v[244:247], v[28:31], v[180:183], v[244:247]\n"
      "s_add_u32 m0, s53, 8192\n"
      "v_exp_f32 v176, v176\n"
      "v_mfma_f32_16x16x32_bf16 v[244:247], v[32:35], v[184:187], v[244:247]\n"
      "v_pk_max_i16 v213, v213, 0\n"
      "v_pk_max_i16 v219, v219, 0\n"
      "v_mfma_f32_16x16x32_bf16 v[244:247], v[132:135], v[120:123], v[244:247]\n"
      "s_waitcnt lgkmcnt(4)\n"
      "v_mul_f32_dpp v248, v190, v248 row_newbcast:0 row_mask:0xf bank_mask:0xf\n"
      "v_cvt_pk_bf16_f32 v222, v176, v177\n"
      "v_mfma_f32_16x16x32_bf16 v[252:255], v[28:31], v[232:235], v[252:255]\n"
      "v_mul_f32_dpp v250, v190, v250 row_newbcast:8 row_mask:0xf bank_mask:0xf\n"
      "v_mfma_f32_16x16x32_bf16 v[252:255], v[32:35], v[236:239], v[252:255]\n"
      "v_mul_f32_dpp v240, v190, v240 row_newbcast:0 row_mask:0xf bank_mask:0xf\n"
      "v_mul_f32_dpp v243, v190, v243 row_newbcast:12 row_mask:0xf bank_mask:0xf\n"
      "v_mfma_f32_16x16x32_bf16 v[252:255], v[132:135], v[228:231], v[252:255]\n"
      "s_waitcnt lgkmcnt(0)\n"
      "buffer_load_dwordx4 v1, s[40:43], s91 offen offset:3072 lds\n"
      "v_mfma_f32_16x16x4_4b_bf16 v[84:99], v[158:159], v[156:157], 0 cbsz:2 abid:0\n"
      "buffer_store_dwordx2 v[212:213], v2, s[44:47], s73 offen offset:0 nt sc1\n"
      "v_mul_f32_dpp v244, v190, v244 row_newbcast:0 row_mask:0xf bank_mask:0xf\n"
      "v_mul_f32_dpp v245, v190, v245 row_newbcast:4 row_mask:0xf bank_mask:0xf\n"
      "v_pk_max_i16 v218, v218, 0\n"
      "v_mfma_f32_16x16x32_bf16 v[28:31], v[164:167], v[36:39], 0\n"
      "v_mul_f32_dpp v253, v190, v253 row_newbcast:4 row_mask:0xf bank_mask:0xf\n"
      "v_mul_f32_dpp v252, v190, v252 row_newbcast:0 row_mask:0xf bank_mask:0xf\n"
      "v_mfma_f32_16x16x32_bf16 v[28:31], v[168:171], v[40:43], v[28:31]\n"
      "v_mul_f32_dpp v249, v190, v249 row_newbcast:4 row_mask:0xf bank_mask:0xf\n"
      "s_nop 0\n"
      "v_smfmac_f32_16x16x64_bf16 v[88:91], v[172:175], v[180:187], v5\n"
      "ds_read_b64 v[132:133], v11 offset:4096\n"
      "v_mul_f32_dpp v246, v190, v246 row_newbcast:8 row_mask:0xf bank_mask:0xf\n"
      "v_smfmac_f32_16x16x64_bf16 v[84:87], v[172:175], v[108:115], v5\n"
      "buffer_load_dwordx4 v1, s[40:43], s90 offen offset:0 lds\n"
      "v_smfmac_f32_16x16x64_bf16 v[92:95], v[172:175], v[56:63], v5\n"
      "v_smfmac_f32_16x16x64_bf16 v[96:99], v[172:175], v[232:239], v5\n"
      "v_mul_f32_dpp v247, v190, v247 row_newbcast:12 row_mask:0xf bank_mask:0xf\n"
      "v_mfma_f32_16x16x32_bf16 v[32:35], v[76:79], v[36:39], 0\n"
      "v_exp_f32 v30, v30\n"
      "v_mfma_f32_16x16x32_bf16 v[32:35], v[80:83], v[40:43], v[32:35]\n"
      "v_exp_f32 v29, v29\n"
      "v_smfmac_f32_16x16x32_bf16 v[92:95], v[144:145], v[116:119], v5\n"
      "v_smfmac_f32_16x16x32_bf16 v[88:91], v[144:145], v[120:123], v5\n"
      "v_maximum3_f32 v8, v8, v188, v189\n"
      "v_smfmac_f32_16x16x32_bf16 v[96:99], v[144:145], v[228:231], v5\n"
      "v_mul_f32_dpp v254, v190, v254 row_newbcast:8 row_mask:0xf bank_mask:0xf\n"
      "v_smfmac_f32_16x16x32_bf16 v[84:87], v[144:145], v[52:55], v5\n"
      "v_mul_f32_dpp v242, v190, v242 row_newbcast:8 row_mask:0xf bank_mask:0xf\n"
      "v_cvt_pk_bf16_f32 v223, v178, v179\n"
      "v_mul_f32_dpp v251, v190, v251 row_newbcast:12 row_mask:0xf bank_mask:0xf\n"
      "v_mfma_f32_16x16x32_bf16 v[212:215], v[20:23], v[36:39], 0\n"
      "v_mul_f32_dpp v241, v190, v241 row_newbcast:4 row_mask:0xf bank_mask:0xf\n"
      "v_mul_f32_dpp v255, v190, v255 row_newbcast:12 row_mask:0xf bank_mask:0xf\n"
      "v_mfma_f32_16x16x32_bf16 v[212:215], v[24:27], v[40:43], v[212:215]\n"
      "v_exp_f32 v31, v31\n"
      "v_mfma_f32_16x16x32_bf16 v[240:243], v[124:127], a[192:195], v[240:243]\n"
      "s_waitcnt lgkmcnt(0)\n"
      "v_and_b32 v191, 0xffff0000, v133\n"
      "v_add_f32 v96, v96, v97\n"
      "v_mfma_f32_16x16x32_bf16 v[240:243], v[100:103], a[196:199], v[240:243]\n"
      "v_fma_f32 v96, v96, v19, v191\n"
      "v_lshlrev_b32 v190, 16, v133\n"
      "v_mfma_f32_16x16x32_bf16 v[240:243], v[72:75], a[200:203], v[240:243]\n"
      "v_exp_f32 v214, v214\n"
      "v_mfma_f32_16x16x32_bf16 v[240:243], v[196:199], a[204:207], v[240:243]\n"
      "v_exp_f32 v213, v213\n"
      "v_mfma_f32_16x16x32_bf16 v[244:247], v[124:127], a[208:211], v[244:247]\n"
      "v_exp_f32 v28, v28\n"
      "v_mfma_f32_16x16x32_bf16 v[244:247], v[100:103], a[212:215], v[244:247]\n"
      "v_exp_f32 v32, v32\n"
      "v_mfma_f32_16x16x32_bf16 v[244:247], v[72:75], a[216:219], v[244:247]\n"
      "v_exp_f32 v212, v212\n"
      "v_mfma_f32_16x16x32_bf16 v[244:247], v[196:199], a[220:223], v[244:247]\n"
      "buffer_store_dwordx2 v[218:219], v2, s[44:47], s73 offen offset:768 nt sc1\n"
      "v_lshlrev_b32 v188, 16, v132\n"
      "v_mfma_f32_16x16x32_bf16 v[252:255], v[124:127], a[240:243], v[252:255]\n"
      "v_add_f32 v84, v84, v85\n"
      "v_fma_f32 v84, v84, v19, v188\n"
      "v_mfma_f32_16x16x32_bf16 v[252:255], v[100:103], a[244:247], v[252:255]\n"
      "v_and_b32 v189, 0xffff0000, v132\n"
      "v_add_f32 v92, v92, v93\n"
      "v_mfma_f32_16x16x32_bf16 v[252:255], v[72:75], a[248:251], v[252:255]\n"
      "v_fma_f32 v92, v92, v19, v190\n"
      "v_cvt_pk_bf16_f32 v85, v92, v96\n"
      "v_mfma_f32_16x16x32_bf16 v[252:255], v[196:199], a[252:255], v[252:255]\n"
      "v_exp_f32 v34, v34\n"
      "v_mfma_f32_16x16x32_bf16 v[172:175], v[204:207], v[36:39], 0\n"
      "buffer_load_dwordx4 v1, s[40:43], s91 offen offset:1024 lds\n"
      "v_mfma_f32_16x16x32_bf16 v[172:175], v[208:211], v[40:43], v[172:175]\n"
      "v_exp_f32 v215, v215\n"
      "v_mfma_f32_16x16x32_bf16 v[132:135], a[144:147], v[128:131], 0\n"
      "v_exp_f32 v33, v33\n"
      "v_mfma_f32_16x16x32_bf16 v[132:135], a[148:151], v[152:155], v[132:135]\n"
      "v_exp_f32 v35, v35\n"
      "v_mfma_f32_16x16x32_bf16 v[132:135], a[152:155], v[148:151], v[132:135]\n"
      "v_add_f32 v88, v88, v89\n"
      "v_fma_f32 v88, v88, v19, v189\n"
      "v_mfma_f32_16x16x32_bf16 v[132:135], a[156:159], v[224:227], v[132:135]\n"
      "v_exp_f32 v174, v174\n"
      "v_mfma_f32_16x16x32_bf16 v[144:147], a[128:131], v[128:131], 0\n"
      "v_pk_max_i16 v216, v216, 0\n"
      "v_mfma_f32_16x16x32_bf16 v[144:147], a[132:135], v[152:155], v[144:147]\n"
      "v_exp_f32 v172, v172\n"
      "v_mfma_f32_16x16x32_bf16 v[144:147], a[136:139], v[148:151], v[144:147]\n"
      "v_cvt_pk_bf16_f32 v99, v34, v35\n"
      "v_cvt_pk_bf16_f32 v98, v32, v33\n"
      "v_mfma_f32_16x16x32_bf16 v[144:147], a[140:143], v[224:227], v[144:147]\n"
      "v_cvt_pk_bf16_f32 v93, v30, v31\n"
      "v_cvt_pk_bf16_f32 v92, v28, v29\n"
      "v_mfma_f32_16x16x32_bf16 v[188:191], a[176:179], v[128:131], 0\n"
      "v_pk_max_i16 v85, v85, 0\n"
      "v_cvt_pk_bf16_f32 v84, v84, v88\n"
      "v_mfma_f32_16x16x32_bf16 v[188:191], a[180:183], v[152:155], v[188:191]\n"
      "v_cvt_pk_bf16_f32 v96, v212, v213\n"
      "v_pk_max_i16 v84, v84, 0\n"
      "v_mfma_f32_16x16x32_bf16 v[188:191], a[184:187], v[148:151], v[188:191]\n"
      "v_cvt_pk_bf16_f32 v31, v134, v135\n"
      "v_cvt_pk_bf16_f32 v28, v144, v145\n"
      "v_mfma_f32_16x16x32_bf16 v[188:191], a[188:191], v[224:227], v[188:191]\n"
      "v_cvt_pk_bf16_f32 v30, v132, v133\n"
      "v_cvt_pk_bf16_f32 v240, v240, v244\n"
      "v_mfma_f32_16x16x32_bf16 v[132:135], a[160:163], v[128:131], 0\n"
      "v_cvt_pk_bf16_f32 v244, v241, v245\n"
      "v_cvt_pk_bf16_f32 v29, v146, v147\n"
      "v_mfma_f32_16x16x32_bf16 v[132:135], a[164:167], v[152:155], v[132:135]\n"
      "v_exp_f32 v175, v175\n"
      "v_mfma_f32_16x16x32_bf16 v[132:135], a[168:171], v[148:151], v[132:135]\n"
      "v_exp_f32 v173, v173\n"
      "v_mfma_f32_16x16x32_bf16 v[132:135], a[172:175], v[224:227], v[132:135]\n"
      "buffer_store_dwordx2 v[216:217], v2, s[44:47], s73 offen offset:256 nt sc1\n"
      "v_mov_b64 v[176:177], 0\n"
      "v_mfma_f32_16x16x32_bf16 v[248:251], v[124:127], a[224:227], v[248:251]\n"
      "v_cvt_pk_bf16_f32 v95, v174, v175\n"
      "v_cvt_pk_bf16_f32 v34, v188, v189\n"
      "v_mfma_f32_16x16x32_bf16 v[248:251], v[100:103], a[228:231], v[248:251]\n"
      "v_cvt_pk_bf16_f32 v35, v190, v191\n"
      "v_cvt_pk_bf16_f32 v242, v242, v246\n"
      "v_mfma_f32_16x16x32_bf16 v[248:251], v[72:75], a[232:235], v[248:251]\n"
      "v_cvt_pk_bf16_f32 v33, v134, v135\n"
      "v_mov_b64 v[174:175], 0\n"
      "v_mfma_f32_16x16x32_bf16 v[248:251], v[196:199], a[236:239], v[248:251]\n"
      "v_cvt_pk_bf16_f32 v32, v132, v133\n"
      "v_cvt_pk_bf16_f32 v246, v243, v247\n"
      "v_smfmac_f32_16x16x64_bf16 v[174:177], v[160:163], v[36:43], v5\n"
      "buffer_load_dwordx4 v1, s[40:43], s90 offen offset:2048 lds\n"
      "v_smfmac_f32_16x16x64_bf16 v[174:177], v[44:47], v[28:35], v5\n"
      "ds_read_b128 v[72:75], v0 offset:4608\n"
      "v_pk_max_i16 v242, v242, 0\n"
      "v_mfma_f32_16x16x32_bf16 v[88:91], v[64:67], v[28:31], 0\n"
      "v_cvt_pk_bf16_f32 v245, v249, v253\n"
      "v_cvt_pk_bf16_f32 v94, v172, v173\n"
      "v_mfma_f32_16x16x32_bf16 v[88:91], v[68:71], v[32:35], v[88:91]\n"
      "v_cvt_pk_bf16_f32 v247, v251, v255\n"
      "v_pk_max_i16 v247, v247, 0\n"
      "v_mfma_f32_16x16x32_bf16 v[144:147], v[20:23], v[28:31], 0\n"
      "v_add_f32 v174, v174, v175\n"
      "v_add_f32 v176, v176, v177\n"
      "v_mfma_f32_16x16x32_bf16 v[144:147], v[24:27], v[32:35], v[144:147]\n"
      "v_exp_f32 v176, v176\n"
      "v_mfma_f32_16x16x32_bf16 v[196:199], v[76:79], v[28:31], 0\n"
      "v_exp_f32 v174, v174\n"
      "v_mfma_f32_16x16x32_bf16 v[196:199], v[80:83], v[32:35], v[196:199]\n"
      "v_add_f32 v213, v174, v176\n"
      "v_add_f32 v212, v174, v176\n"
      "v_mfma_f32_16x16x32_bf16 v[132:135], v[204:207], v[28:31], 0\n"
      "s_nop 0\n"
      "v_permlane16_swap_b32 v212, v213\n"
      "v_mfma_f32_16x16x32_bf16 v[132:135], v[208:211], v[32:35], v[132:135]\n"
      "v_cvt_pk_bf16_f32 v217, v174, v176\n"
      "v_cvt_pk_bf16_f32 v216, v174, v176\n"
      "v_mfma_f32_16x16x32_bf16 v[172:175], v[136:139], v[28:31], 0\n"
      "v_exp_f32 v197, v197\n"
      "v_mfma_f32_16x16x32_bf16 v[172:175], v[140:143], v[32:35], v[172:175]\n"
      "v_exp_f32 v144, v144\n"
      "v_mfma_f32_16x16x32_bf16 v[188:191], v[164:167], v[28:31], 0\n"
      "v_mov_b64 v[176:177], v[212:213]\n"
      "v_cvt_pk_bf16_f32 v97, v214, v215\n"
      "v_mfma_f32_16x16x32_bf16 v[188:191], v[168:171], v[32:35], v[188:191]\n"
      "v_permlane16_swap_b32 v216, v217\n"
      "v_smfmac_f32_16x16x64_bf16 v[212:215], v[12:15], v[92:99], v5\n"
      "buffer_store_dwordx2 v[84:85], v3, s[44:47], s79 offen offset:0 nt sc1\n"
      "v_pk_max_i16 v240, v240, 0\n"
      "v_mfma_f32_16x16x4_4b_bf16 v[28:43], v[216:217], v[156:157], 0 cbsz:2 abid:0\n"
      "ds_read_b128 v[124:127], v0 offset:5632\n"
      "v_cvt_pk_bf16_f32 v243, v250, v254\n"
      "v_exp_f32 v88, v88\n"
      "v_cvt_pk_bf16_f32 v241, v248, v252\n"
      "v_pk_max_i16 v241, v241, 0\n"
      "v_smfmac_f32_16x16x32_bf16 v[212:215], v[12:13], v[220:223], v5\n"
      "s_add_u32 m0, s53, 4096\n"
      "ds_read_b128 v[100:103], v0 offset:4096\n"
      "v_pk_max_i16 v243, v243, 0\n"
      "v_exp_f32 v145, v145\n"
      "v_mfma_f32_16x16x32_bf16 v[32:35], v[92:95], v[180:183], v[32:35]\n"
      "v_exp_f32 v132, v132\n"
      "v_mfma_f32_16x16x32_bf16 v[32:35], v[96:99], v[184:187], v[32:35]\n"
      "ds_read_b128 v[84:87], v0 offset:5120\n"
      "v_add_f32 v212, v212, v213\n"
      "v_mfma_f32_16x16x32_bf16 v[32:35], v[220:223], v[120:123], v[32:35]\n"
      "v_rcp_f32 v213, v212\n"
      "v_mfma_f32_16x16x32_bf16 v[28:31], v[92:95], v[108:111], v[28:31]\n"
      "ds_bpermute_b32 v214, v4, v213\n"
      "v_mfma_f32_16x16x32_bf16 v[28:31], v[96:99], v[112:115], v[28:31]\n"
      "v_exp_f32 v147, v147\n"
      "v_mfma_f32_16x16x32_bf16 v[28:31], v[220:223], v[52:55], v[28:31]\n"
      "v_exp_f32 v146, v146\n"
      "v_mfma_f32_16x16x32_bf16 v[36:39], v[92:95], v[56:59], v[36:39]\n"
      "v_exp_f32 v91, v91\n"
      "v_mfma_f32_16x16x32_bf16 v[36:39], v[96:99], v[60:63], v[36:39]\n"
      "v_exp_f32 v172, v172\n"
      "v_mfma_f32_16x16x32_bf16 v[36:39], v[220:223], v[116:119], v[36:39]\n"
      "v_exp_f32 v196, v196\n"
      "v_mfma_f32_16x16x32_bf16 v[40:43], v[92:95], v[232:235], v[40:43]\n"
      "v_exp_f32 v173, v173\n"
      "v_mfma_f32_16x16x32_bf16 v[40:43], v[96:99], v[236:239], v[40:43]\n"
      "v_exp_f32 v198, v198\n"
      "v_mfma_f32_16x16x32_bf16 v[40:43], v[220:223], v[228:231], v[40:43]\n"
      "s_waitcnt lgkmcnt(1)\n"
      "buffer_load_dwordx4 v1, s[40:43], s92 offen offset:0 lds\n"
      "v_mfma_f32_16x16x32_bf16 v[94:97], a[176:179], v[100:103], 0\n"
      "v_exp_f32 v134, v134\n"
      "v_mfma_f32_16x16x32_bf16 v[94:97], a[180:183], v[72:75], v[94:97]\n"
      "s_waitcnt lgkmcnt(0)\n"
      "v_mul_f32_dpp v28, v214, v28 row_newbcast:0 row_mask:0xf bank_mask:0xf\n"
      "v_mul_f32_dpp v29, v214, v29 row_newbcast:4 row_mask:0xf bank_mask:0xf\n"
      "v_mfma_f32_16x16x32_bf16 v[94:97], a[184:187], v[84:87], v[94:97]\n"
      "v_cvt_pk_bf16_f32 v253, v146, v147\n"
      "v_mul_f32_dpp v34, v214, v34 row_newbcast:8 row_mask:0xf bank_mask:0xf\n"
      "v_mfma_f32_16x16x32_bf16 v[94:97], a[188:191], v[124:127], v[94:97]\n"
      "v_exp_f32 v189, v189\n"
      "v_mfma_f32_16x16x32_bf16 v[218:221], a[128:131], v[100:103], 0\n"
      "v_exp_f32 v135, v135\n"
      "v_mfma_f32_16x16x32_bf16 v[218:221], a[132:135], v[72:75], v[218:221]\n"
      "v_mul_f32_dpp v32, v214, v32 row_newbcast:0 row_mask:0xf bank_mask:0xf\n"
      "v_mul_f32_dpp v33, v214, v33 row_newbcast:4 row_mask:0xf bank_mask:0xf\n"
      "v_mfma_f32_16x16x32_bf16 v[218:221], a[136:139], v[84:87], v[218:221]\n"
      "v_mul_f32_dpp v30, v214, v30 row_newbcast:8 row_mask:0xf bank_mask:0xf\n"
      "v_mul_f32_dpp v35, v214, v35 row_newbcast:12 row_mask:0xf bank_mask:0xf\n"
      "v_mfma_f32_16x16x32_bf16 v[218:221], a[140:143], v[124:127], v[218:221]\n"
      "v_exp_f32 v89, v89\n"
      "v_mfma_f32_16x16x32_bf16 v[32:35], v[192:195], a[208:211], v[32:35]\n"
      "v_mul_f32_dpp v38, v214, v38 row_newbcast:8 row_mask:0xf bank_mask:0xf\n"
      "v_mul_f32_dpp v42, v214, v42 row_newbcast:8 row_mask:0xf bank_mask:0xf\n"
      "v_mfma_f32_16x16x32_bf16 v[32:35], v[200:203], a[212:215], v[32:35]\n"
      "v_mul_f32_dpp v41, v214, v41 row_newbcast:4 row_mask:0xf bank_mask:0xf\n"
      "v_cvt_pk_bf16_f32 v252, v144, v145\n"
      "v_mfma_f32_16x16x32_bf16 v[32:35], v[104:107], a[216:219], v[32:35]\n"
      "v_mul_f32_dpp v36, v214, v36 row_newbcast:0 row_mask:0xf bank_mask:0xf\n"
      "v_mul_f32_dpp v31, v214, v31 row_newbcast:12 row_mask:0xf bank_mask:0xf\n"
      "v_mfma_f32_16x16x32_bf16 v[32:35], v[48:51], a[220:223], v[32:35]\n"
      "v_exp_f32 v188, v188\n"
      "v_mfma_f32_16x16x32_bf16 v[28:31], v[192:195], a[192:195], v[28:31]\n"
      "v_mul_f32_dpp v40, v214, v40 row_newbcast:0 row_mask:0xf bank_mask:0xf\n"
      "v_mfma_f32_16x16x32_bf16 v[28:31], v[200:203], a[196:199], v[28:31]\n"
      "v_exp_f32 v174, v174\n"
      "v_mfma_f32_16x16x32_bf16 v[28:31], v[104:107], a[200:203], v[28:31]\n"
      "v_mul_f32_dpp v37, v214, v37 row_newbcast:4 row_mask:0xf bank_mask:0xf\n"
      "v_mul_f32_dpp v39, v214, v39 row_newbcast:12 row_mask:0xf bank_mask:0xf\n"
      "v_mfma_f32_16x16x32_bf16 v[28:31], v[48:51], a[204:207], v[28:31]\n"
      "v_maximum3_f32 v8, v8, v212, v213\n"
      "v_cvt_pk_bf16_f32 v254, v196, v197\n"
      "v_mfma_f32_16x16x32_bf16 v[36:39], v[192:195], a[224:227], v[36:39]\n"
      "v_exp_f32 v191, v191\n"
      "v_mfma_f32_16x16x32_bf16 v[36:39], v[200:203], a[228:231], v[36:39]\n"
      "buffer_load_dwordx4 v1, s[40:43], s93 offen offset:3072 lds\n"
      "v_mfma_f32_16x16x32_bf16 v[36:39], v[104:107], a[232:235], v[36:39]\n"
      "v_mul_f32_dpp v43, v214, v43 row_newbcast:12 row_mask:0xf bank_mask:0xf\n"
      "v_cvt_pk_bf16_f32 v28, v28, v32\n"
      "v_mfma_f32_16x16x32_bf16 v[36:39], v[48:51], a[236:239], v[36:39]\n"
      "ds_read_b128 v[144:147], v0 offset:0\n"
      "v_cvt_pk_bf16_f32 v214, v172, v173\n"
      "v_mfma_f32_16x16x32_bf16 v[40:43], v[192:195], a[240:243], v[40:43]\n"
      "v_exp_f32 v199, v199\n"
      "v_mfma_f32_16x16x32_bf16 v[40:43], v[200:203], a[244:247], v[40:43]\n"
      "v_exp_f32 v133, v133\n"
      "v_mfma_f32_16x16x32_bf16 v[40:43], v[104:107], a[248:251], v[40:43]\n"
      "v_cvt_pk_bf16_f32 v248, v188, v189\n"
      "v_mfma_f32_16x16x32_bf16 v[40:43], v[48:51], a[252:255], v[40:43]\n"
      "v_exp_f32 v190, v190\n"
      "v_mfma_f32_16x16x32_bf16 v[104:107], a[160:163], v[100:103], 0\n"
      "v_exp_f32 v90, v90\n"
      "v_mfma_f32_16x16x32_bf16 v[104:107], a[164:167], v[72:75], v[104:107]\n"
      "v_cvt_pk_bf16_f32 v213, v90, v91\n"
      "v_mov_b64 v[50:51], 0\n"
      "v_mfma_f32_16x16x32_bf16 v[104:107], a[168:171], v[84:87], v[104:107]\n"
      "v_cvt_pk_bf16_f32 v249, v190, v191\n"
      "v_pk_max_i16 v246, v246, 0\n"
      "v_mfma_f32_16x16x32_bf16 v[104:107], a[172:175], v[124:127], v[104:107]\n"
      "v_cvt_pk_bf16_f32 v212, v88, v89\n"
      "v_cvt_pk_bf16_f32 v250, v132, v133\n"
      "v_mfma_f32_16x16x32_bf16 v[88:91], a[144:147], v[100:103], 0\n"
      "v_cvt_pk_bf16_f32 v99, v96, v97\n"
      "v_cvt_pk_bf16_f32 v255, v198, v199\n"
      "v_mfma_f32_16x16x32_bf16 v[88:91], a[148:151], v[72:75], v[88:91]\n"
      "v_cvt_pk_bf16_f32 v251, v134, v135\n"
      "v_cvt_pk_bf16_f32 v98, v94, v95\n"
      "v_mfma_f32_16x16x32_bf16 v[88:91], a[152:155], v[84:87], v[88:91]\n"
      "v_cvt_pk_bf16_f32 v32, v29, v33\n"
      "v_cvt_pk_bf16_f32 v33, v37, v41\n"
      "v_mfma_f32_16x16x32_bf16 v[88:91], a[156:159], v[124:127], v[88:91]\n"
      "v_exp_f32 v175, v175\n"
      "v_smfmac_f32_16x16x64_bf16 v[176:179], v[12:15], v[248:255], v5\n"
      "buffer_store_dwordx2 v[246:247], v2, s[44:47], s75 offen offset:768 nt sc1\n"
      "v_cvt_pk_bf16_f32 v215, v174, v175\n"
      "v_mfma_f32_16x16x4_4b_bf16 v[188:203], v[216:217], v[156:157], 0 cbsz:2 abid:2\n"
      "v_cvt_pk_bf16_f32 v30, v30, v34\n"
      "v_pk_max_i16 v245, v245, 0\n"
      "v_cvt_pk_bf16_f32 v93, v220, v221\n"
      "v_cvt_pk_bf16_f32 v29, v36, v40\n"
      "v_cvt_pk_bf16_f32 v96, v104, v105\n"
      "v_cvt_pk_bf16_f32 v97, v106, v107\n"
      "v_smfmac_f32_16x16x32_bf16 v[176:179], v[12:13], v[212:215], v5\n"
      "ds_read_b128 v[172:175], v0 offset:1024\n"
      "v_cvt_pk_bf16_f32 v34, v31, v35\n"
      "v_cvt_pk_bf16_f32 v95, v90, v91\n"
      "v_cvt_pk_bf16_f32 v94, v88, v89\n"
      "v_mfma_f32_16x16x32_bf16 v[200:203], v[248:251], v[232:235], v[200:203]\n"
      "v_cvt_pk_bf16_f32 v92, v218, v219\n"
      "v_cvt_pk_bf16_f32 v35, v39, v43\n"
      "v_mfma_f32_16x16x32_bf16 v[200:203], v[252:255], v[236:239], v[200:203]\n"
      "buffer_load_dwordx4 v1, s[40:43], s93 offen offset:1024 lds\n"
      "v_mfma_f32_16x16x32_bf16 v[200:203], v[212:215], v[228:231], v[200:203]\n"
      "ds_read_b128 v[104:107], v0 offset:512\n"
      "v_add_f32 v176, v176, v177\n"
      "v_mfma_f32_16x16x32_bf16 v[88:91], v[76:79], v[92:95], 0\n"
      "v_cvt_pk_bf16_f32 v31, v38, v42\n"
      "v_mov_b64 v[48:49], 0\n"
      "v_mfma_f32_16x16x32_bf16 v[88:91], v[80:83], v[96:99], v[88:91]\n"
      "ds_read_b128 v[132:135], v0 offset:1536\n"
      "v_pk_max_i16 v35, v35, 0\n"
      "v_mfma_f32_16x16x32_bf16 v[40:43], v[20:23], v[92:95], 0\n"
      "v_rcp_f32 v177, v176\n"
      "v_mfma_f32_16x16x32_bf16 v[40:43], v[24:27], v[96:99], v[40:43]\n"
      "ds_bpermute_b32 v178, v4, v177 offset:128\n"
      "v_smfmac_f32_16x16x64_bf16 v[48:51], v[160:163], v[92:99], v5\n"
      "buffer_store_dwordx2 v[240:241], v2, s[44:47], s75 offen offset:0 nt sc1\n"
      "v_pk_max_i16 v29, v29, 0\n"
      "v_mfma_f32_16x16x32_bf16 v[36:39], v[204:207], v[92:95], 0\n"
      "v_exp_f32 v90, v90\n"
      "v_mfma_f32_16x16x32_bf16 v[36:39], v[208:211], v[96:99], v[36:39]\n"
      "v_exp_f32 v88, v88\n"
      "v_mfma_f32_16x16x32_bf16 v[188:191], v[248:251], v[108:111], v[188:191]\n"
      "v_exp_f32 v42, v42\n"
      "v_mfma_f32_16x16x32_bf16 v[188:191], v[252:255], v[112:115], v[188:191]\n"
      "v_exp_f32 v41, v41\n"
      "v_mfma_f32_16x16x32_bf16 v[188:191], v[212:215], v[52:55], v[188:191]\n"
      "buffer_load_dwordx4 v1, s[40:43], s92 offen offset:2048 lds\n"
      "v_mfma_f32_16x16x32_bf16 v[196:199], v[248:251], v[56:59], v[196:199]\n"
      "v_exp_f32 v91, v91\n"
      "v_mfma_f32_16x16x32_bf16 v[196:199], v[252:255], v[60:63], v[196:199]\n"
      "v_exp_f32 v39, v39\n"
      "v_mfma_f32_16x16x32_bf16 v[196:199], v[212:215], v[116:119], v[196:199]\n"
      "v_exp_f32 v89, v89\n"
      "v_mfma_f32_16x16x32_bf16 v[192:195], v[248:251], v[180:183], v[192:195]\n"
      "v_exp_f32 v36, v36\n"
      "v_mfma_f32_16x16x32_bf16 v[192:195], v[252:255], v[184:187], v[192:195]\n"
      "s_waitcnt lgkmcnt(0)\n"
      "v_mul_f32_dpp v200, v178, v200 row_newbcast:0 row_mask:0xf bank_mask:0xf\n"
      "v_mul_f32_dpp v188, v178, v188 row_newbcast:0 row_mask:0xf bank_mask:0xf\n"
      "v_mfma_f32_16x16x32_bf16 v[192:195], v[212:215], v[120:123], v[192:195]\n"
      "v_mul_f32_dpp v201, v178, v201 row_newbcast:4 row_mask:0xf bank_mask:0xf\n"
      "v_cvt_pk_bf16_f32 v218, v88, v89\n"
      "v_mfma_f32_16x16x32_bf16 v[220:223], v[64:67], v[92:95], 0\n"
      "v_exp_f32 v40, v40\n"
      "v_mfma_f32_16x16x32_bf16 v[220:223], v[68:71], v[96:99], v[220:223]\n"
      "v_exp_f32 v43, v43\n"
      "v_mfma_f32_16x16x32_bf16 v[160:163], v[136:139], v[92:95], 0\n"
      "v_cvt_pk_bf16_f32 v217, v42, v43\n"
      "v_mul_f32_dpp v192, v178, v192 row_newbcast:0 row_mask:0xf bank_mask:0xf\n"
      "v_mfma_f32_16x16x32_bf16 v[160:163], v[140:143], v[96:99], v[160:163]\n"
      "v_cvt_pk_bf16_f32 v216, v40, v41\n"
      "v_mul_f32_dpp v203, v178, v203 row_newbcast:12 row_mask:0xf bank_mask:0xf\n"
      "v_mfma_f32_16x16x32_bf16 v[40:43], v[164:167], v[92:95], 0\n"
      "v_exp_f32 v37, v37\n"
      "v_mfma_f32_16x16x32_bf16 v[40:43], v[168:171], v[96:99], v[40:43]\n"
      "v_exp_f32 v221, v221\n"
      "v_mfma_f32_16x16x32_bf16 v[92:95], a[144:147], v[144:147], 0\n"
      "s_nop 0\n"
      "v_exp_f32 v161, v161\n"
      "v_mfma_f32_16x16x32_bf16 v[92:95], a[148:151], v[104:107], v[92:95]\n"
      "v_cvt_pk_bf16_f32 v219, v90, v91\n"
      "v_mul_f32_dpp v189, v178, v189 row_newbcast:4 row_mask:0xf bank_mask:0xf\n"
      "v_mfma_f32_16x16x32_bf16 v[92:95], a[152:155], v[172:175], v[92:95]\n"
      "v_exp_f32 v38, v38\n"
      "v_mfma_f32_16x16x32_bf16 v[92:95], a[156:159], v[132:135], v[92:95]\n"
      "buffer_store_dwordx2 v[242:243], v2, s[44:47], s75 offen offset:512 nt sc1\n"
      "v_mul_f32_dpp v191, v178, v191 row_newbcast:12 row_mask:0xf bank_mask:0xf\n"
      "v_mfma_f32_16x16x32_bf16 v[88:91], a[128:131], v[144:147], 0\n"
      "v_exp_f32 v220, v220\n"
      "v_mfma_f32_16x16x32_bf16 v[88:91], a[132:135], v[104:107], v[88:91]\n"
      "v_exp_f32 v43, v43\n"
      "v_mfma_f32_16x16x32_bf16 v[88:91], a[136:139], v[172:175], v[88:91]\n"
      "s_nop 0\n"
      "v_cvt_pk_bf16_f32 v249, v94, v95\n"
      "v_mul_f32_dpp v197, v178, v197 row_newbcast:4 row_mask:0xf bank_mask:0xf\n"
      "v_mfma_f32_16x16x32_bf16 v[88:91], a[140:143], v[132:135], v[88:91]\n"
      "v_exp_f32 v223, v223\n"
      "v_mfma_f32_16x16x32_bf16 v[212:215], a[176:179], v[144:147], 0\n"
      "v_mul_f32_dpp v198, v178, v198 row_newbcast:8 row_mask:0xf bank_mask:0xf\n"
      "v_mul_f32_dpp v190, v178, v190 row_newbcast:8 row_mask:0xf bank_mask:0xf\n"
      "v_mfma_f32_16x16x32_bf16 v[212:215], a[180:183], v[104:107], v[212:215]\n"
      "v_exp_f32 v163, v163\n"
      "v_mfma_f32_16x16x32_bf16 v[212:215], a[184:187], v[172:175], v[212:215]\n"
      "v_mul_f32_dpp v195, v178, v195 row_newbcast:12 row_mask:0xf bank_mask:0xf\n"
      "v_cvt_pk_bf16_f32 v247, v90, v91\n"
      "v_mfma_f32_16x16x32_bf16 v[212:215], a[188:191], v[132:135], v[212:215]\n"
      "s_not_b64 vcc, vcc\n"
      "v_mul_f32_dpp v194, v178, v194 row_newbcast:8 row_mask:0xf bank_mask:0xf\n"
      "v_cvt_pk_bf16_f32 v248, v92, v93\n"
      "v_mfma_f32_16x16x32_bf16 v[92:95], a[160:163], v[144:147], 0\n"
      "v_exp_f32 v160, v160\n"
      "v_mfma_f32_16x16x32_bf16 v[92:95], a[164:167], v[104:107], v[92:95]\n"
      "v_cvt_pk_bf16_f32 v246, v88, v89\n"
      "v_mul_f32_dpp v202, v178, v202 row_newbcast:8 row_mask:0xf bank_mask:0xf\n"
      "v_mfma_f32_16x16x32_bf16 v[92:95], a[168:171], v[172:175], v[92:95]\n"
      "v_cvt_pk_bf16_f32 v252, v212, v213\n"
      "v_cvt_pk_bf16_f32 v253, v214, v215\n"
      "v_mfma_f32_16x16x32_bf16 v[92:95], a[172:175], v[132:135], v[92:95]\n"
      "s_add_u32 m0, s53, 0\n"
      "v_exp_f32 v162, v162\n"
      "v_mfma_f32_16x16x32_bf16 v[188:191], v[128:131], a[192:195], v[188:191]\n"
      "buffer_load_dwordx4 v1, s[40:43], s94 offen offset:2048 nt lds\n"
      "v_mfma_f32_16x16x32_bf16 v[188:191], v[152:155], a[196:199], v[188:191]\n"
      "v_exp_f32 v42, v42\n"
      "v_mfma_f32_16x16x32_bf16 v[188:191], v[148:151], a[200:203], v[188:191]\n"
      "s_nop 0\n"
      "v_cvt_pk_bf16_f32 v250, v92, v93\n"
      "v_cvt_pk_bf16_f32 v251, v94, v95\n"
      "v_mfma_f32_16x16x32_bf16 v[188:191], v[224:227], a[204:207], v[188:191]\n"
      "v_cvt_pk_bf16_f32 v213, v42, v43\n"
      "v_mul_f32_dpp v199, v178, v199 row_newbcast:12 row_mask:0xf bank_mask:0xf\n"
      "v_smfmac_f32_16x16x64_bf16 v[48:51], v[44:47], v[246:253], v5\n"
      "v_cvt_pk_bf16_f32 v215, v38, v39\n"
      "v_mfma_f32_16x16x32_bf16 v[200:203], v[128:131], a[240:243], v[200:203]\n"
      "v_mul_f32_dpp v193, v178, v193 row_newbcast:4 row_mask:0xf bank_mask:0xf\n"
      "v_mul_f32_dpp v196, v178, v196 row_newbcast:0 row_mask:0xf bank_mask:0xf\n"
      "v_mfma_f32_16x16x32_bf16 v[200:203], v[152:155], a[244:247], v[200:203]\n"
      "v_exp_f32 v40, v40\n"
      "v_mfma_f32_16x16x32_bf16 v[200:203], v[148:151], a[248:251], v[200:203]\n"
      "v_exp_f32 v41, v41\n"
      "v_mfma_f32_16x16x32_bf16 v[200:203], v[224:227], a[252:255], v[200:203]\n"
      "v_add_f32 v50, v50, v51\n"
      "v_add_f32 v48, v48, v49\n"
      "v_mfma_f32_16x16x32_bf16 v[192:195], v[128:131], a[208:211], v[192:195]\n"
      "v_exp_f32 v48, v48\n"
      "v_mfma_f32_16x16x32_bf16 v[192:195], v[152:155], a[212:215], v[192:195]\n"
      "v_exp_f32 v50, v50\n"
      "v_mfma_f32_16x16x32_bf16 v[192:195], v[148:151], a[216:219], v[192:195]\n"
      "v_cvt_pk_bf16_f32 v242, v48, v50\n"
      "v_cvt_pk_bf16_f32 v243, v48, v50\n"
      "v_mfma_f32_16x16x32_bf16 v[192:195], v[224:227], a[220:223], v[192:195]\n"
      "v_add_f32 v88, v48, v50\n"
      "v_add_f32 v89, v48, v50\n"
      "v_mfma_f32_16x16x32_bf16 v[196:199], v[128:131], a[224:227], v[196:199]\n"
      "s_nop 0\n"
      "v_permlane16_swap_b32 v88, v89\n"
      "v_mfma_f32_16x16x32_bf16 v[196:199], v[152:155], a[228:231], v[196:199]\n"
      "v_permlane16_swap_b32 v242, v243\n"
      "v_mfma_f32_16x16x32_bf16 v[196:199], v[148:151], a[232:235], v[196:199]\n"
      "v_cvt_pk_bf16_f32 v212, v40, v41\n"
      "v_cvt_pk_bf16_f32 v214, v36, v37\n"
      "v_mfma_f32_16x16x32_bf16 v[196:199], v[224:227], a[236:239], v[196:199]\n"
      "v_cvt_pk_bf16_f32 v224, v220, v221\n"
      "v_mov_b64 v[128:129], v[88:89]\n"
      "v_smfmac_f32_16x16x64_bf16 v[88:91], v[12:15], v[212:219], v5\n"
      "v_cvt_pk_bf16_f32 v226, v160, v161\n"
      "v_mfma_f32_16x16x4_4b_bf16 v[36:51], v[242:243], v[156:157], 0 cbsz:2 abid:0\n"
      "v_exp_f32 v222, v222\n"
      "v_cvt_pk_bf16_f32 v227, v162, v163\n"
      "v_cvt_pk_bf16_f32 v225, v222, v223\n"
      "v_cvt_pk_bf16_f32 v188, v188, v192\n"
      "v_cvt_pk_bf16_f32 v190, v190, v194\n"
      "v_smfmac_f32_16x16x32_bf16 v[88:91], v[12:13], v[224:227], v5\n"
      "s_nop 3\n"
      "v_mfma_f32_16x16x32_bf16 v[40:43], v[212:215], v[180:183], v[40:43]\n"
      "buffer_load_dwordx4 v1, s[40:43], s95 offen offset:1024 nt lds\n"
      "v_mfma_f32_16x16x32_bf16 v[40:43], v[216:219], v[184:187], v[40:43]\n"
      "v_pk_max_i16 v244, v244, 0\n"
      "v_add_f32 v88, v88, v89\n"
      "v_mfma_f32_16x16x32_bf16 v[40:43], v[224:227], v[120:123], v[40:43]\n"
      "v_rcp_f32 v89, v88\n"
      "v_mfma_f32_16x16x32_bf16 v[48:51], v[212:215], v[232:235], v[48:51]\n"
      "ds_bpermute_b32 v90, v4, v89\n"
      "v_mfma_f32_16x16x32_bf16 v[48:51], v[216:219], v[236:239], v[48:51]\n"
      "v_cvt_pk_bf16_f32 v192, v189, v193\n"
      "v_cvt_pk_bf16_f32 v194, v191, v195\n"
      "v_mfma_f32_16x16x32_bf16 v[48:51], v[224:227], v[228:231], v[48:51]\n"
      "v_pk_max_i16 v192, v192, 0\n"
      "v_cvt_pk_bf16_f32 v189, v196, v200\n"
      "v_mfma_f32_16x16x32_bf16 v[36:39], v[212:215], v[108:111], v[36:39]\n"
      "v_pk_max_i16 v194, v194, 0\n"
      "v_cvt_pk_bf16_f32 v195, v199, v203\n"
      "v_mfma_f32_16x16x32_bf16 v[36:39], v[216:219], v[112:115], v[36:39]\n"
      "v_cvt_pk_bf16_f32 v193, v197, v201\n"
      "v_pk_max_i16 v31, v31, 0\n"
      "v_mfma_f32_16x16x32_bf16 v[36:39], v[224:227], v[52:55], v[36:39]\n"
      "v_mfma_f32_16x16x32_bf16 v[44:47], v[212:215], v[56:59], v[44:47]\n"
      "buffer_load_dwordx4 v1, s[40:43], s94 offen offset:0 nt lds\n"
      "v_mfma_f32_16x16x32_bf16 v[44:47], v[216:219], v[60:63], v[44:47]\n"
      "v_pk_max_i16 v189, v189, 0\n"
      "v_mfma_f32_16x16x32_bf16 v[44:47], v[224:227], v[116:119], v[44:47]\n"
      "buffer_store_dwordx2 v[244:245], v2, s[44:47], s75 offen offset:256 nt sc1\n"
      "v_cvt_pk_bf16_f32 v191, v198, v202\n"
      "v_mfma_f32_16x16x4_4b_bf16 v[212:227], v[242:243], v[156:157], 0 cbsz:2 abid:2\n"
      "s_waitcnt lgkmcnt(0)\n"
      "v_mul_f32_dpp v51, v90, v51 row_newbcast:12 row_mask:0xf bank_mask:0xf\n"
      "v_mul_f32_dpp v48, v90, v48 row_newbcast:0 row_mask:0xf bank_mask:0xf\n"
      "v_mul_f32_dpp v43, v90, v43 row_newbcast:12 row_mask:0xf bank_mask:0xf\n"
      "v_mul_f32_dpp v50, v90, v50 row_newbcast:8 row_mask:0xf bank_mask:0xf\n"
      "v_mul_f32_dpp v49, v90, v49 row_newbcast:4 row_mask:0xf bank_mask:0xf\n"
      "v_mul_f32_dpp v45, v90, v45 row_newbcast:4 row_mask:0xf bank_mask:0xf\n"
      "v_mul_f32_dpp v46, v90, v46 row_newbcast:8 row_mask:0xf bank_mask:0xf\n"
      "v_pk_max_i16 v191, v191, 0\n"
      "v_mfma_f32_16x16x32_bf16 v[48:51], v[100:103], a[240:243], v[48:51]\n"
      "v_pk_max_i16 v34, v34, 0\n"
      "v_mul_f32_dpp v41, v90, v41 row_newbcast:4 row_mask:0xf bank_mask:0xf\n"
      "v_mfma_f32_16x16x32_bf16 v[48:51], v[72:75], a[244:247], v[48:51]\n"
      "v_mul_f32_dpp v37, v90, v37 row_newbcast:4 row_mask:0xf bank_mask:0xf\n"
      "v_mul_f32_dpp v39, v90, v39 row_newbcast:12 row_mask:0xf bank_mask:0xf\n"
      "v_mfma_f32_16x16x32_bf16 v[48:51], v[84:87], a[248:251], v[48:51]\n"
      "v_mul_f32_dpp v40, v90, v40 row_newbcast:0 row_mask:0xf bank_mask:0xf\n"
      "v_mul_f32_dpp v42, v90, v42 row_newbcast:8 row_mask:0xf bank_mask:0xf\n"
      "v_mfma_f32_16x16x32_bf16 v[48:51], v[124:127], a[252:255], v[48:51]\n"
      "v_pk_max_i16 v33, v33, 0\n"
      "v_mfma_f32_16x16x32_bf16 v[40:43], v[100:103], a[208:211], v[40:43]\n"
      "v_maximum3_f32 v8, v8, v176, v177\n"
      "v_pk_max_i16 v30, v30, 0\n"
      "v_mfma_f32_16x16x32_bf16 v[40:43], v[72:75], a[212:215], v[40:43]\n"
      "v_mul_f32_dpp v44, v90, v44 row_newbcast:0 row_mask:0xf bank_mask:0xf\n"
      "v_pk_max_i16 v28, v28, 0\n"
      "v_mfma_f32_16x16x32_bf16 v[40:43], v[84:87], a[216:219], v[40:43]\n"
      "v_mul_f32_dpp v47, v90, v47 row_newbcast:12 row_mask:0xf bank_mask:0xf\n"
      "v_maximum3_f32 v8, v8, v88, v89\n"
      "v_mfma_f32_16x16x32_bf16 v[40:43], v[124:127], a[220:223], v[40:43]\n"
      "v_pk_max_i16 v190, v190, 0\n"
      "v_add_u32 v10, 64, v10\n"
      "v_mfma_f32_16x16x32_bf16 v[44:47], v[100:103], a[224:227], v[44:47]\n"
      "v_mul_f32_dpp v36, v90, v36 row_newbcast:0 row_mask:0xf bank_mask:0xf\n"
      "v_mul_f32_dpp v38, v90, v38 row_newbcast:8 row_mask:0xf bank_mask:0xf\n"
      "v_mfma_f32_16x16x32_bf16 v[44:47], v[72:75], a[228:231], v[44:47]\n"
      "L_h5_%=:\n"
      "s_cbranch_execz L_hr_%=\n"
      "v_add_u32 v11, 0x200, v11\n"
      "v_mfma_f32_16x16x32_bf16 v[44:47], v[84:87], a[232:235], v[44:47]\n"
      "s_add_u32 s44, s44, 13107200\n s_addc_u32 s45, s45, 0\n"
      "v_pk_max_i16 v193, v193, 0\n"
      "v_mfma_f32_16x16x32_bf16 v[44:47], v[124:127], a[236:239], v[44:47]\n"
      "v_pk_max_i16 v195, v195, 0\n"
      "v_pk_max_i16 v32, v32, 0\n"
      "v_mfma_f32_16x16x32_bf16 v[36:39], v[100:103], a[192:195], v[36:39]\n"
      "v_pk_max_i16 v188, v188, 0\n"
      "v_mfma_f32_16x16x32_bf16 v[36:39], v[72:75], a[196:199], v[36:39]\n"
      "buffer_load_dwordx4 v1, s[40:43], s95 offen offset:3072 nt lds\n"
      "v_mfma_f32_16x16x32_bf16 v[36:39], v[84:87], a[200:203], v[36:39]\n"
      "s_add_u32 s40, s40, 13107200\n s_addc_u32 s41, s41, 0\n"
      "s_add_u32 s38, s38, s38\n"
      "v_mfma_f32_16x16x32_bf16 v[36:39], v[124:127], a[204:207], v[36:39]\n"
      "s_cbranch_scc0 L_task_%=\n"
      "s_cmp_lg_u32 s38, 0\n"
      "s_cbranch_scc1 L_e5_%=\n"
      "L_ln_%=:\n"
      "v_mfma_f32_16x16x32_bf16 v[88:91], v[204:207], v[246:249], 0\n"
      "s_waitcnt vmcnt(40)\n"
      "ds_read_b128 v[92:95], v0 offset:20480\n"
      "ds_read_b128 v[84:87], v0 offset:21504\n"
      "v_mfma_f32_16x16x32_bf16 v[88:91], v[208:211], v[250:253], v[88:91]\n"
      "s_waitcnt vmcnt(30)\n"
      "ds_read_b128 v[72:75], v0 offset:16384\n"
      "ds_read_b128 v[204:207], v0 offset:17920\n"
      "v_mfma_f32_16x16x32_bf16 v[124:127], v[164:167], v[246:249], 0\n"
      "ds_read_b128 v[148:151], v0 offset:16896\n"
      "ds_read_b128 v[164:167], v0 offset:17408\n"
      "v_mfma_f32_16x16x32_bf16 v[124:127], v[168:171], v[250:253], v[124:127]\n"
      "buffer_store_dwordx2 v[28:29], v2, s[44:47], s100 offen offset:0 nt sc1\n"
      "v_mfma_f32_16x16x32_bf16 v[208:211], v[76:79], v[246:249], 0\n"
      "v_mfma_f32_16x16x32_bf16 v[208:211], v[80:83], v[250:253], v[208:211]\n"
      "ds_read_b128 v[76:79], v0 offset:20992\n"
      "ds_read_b128 v[80:83], v0 offset:22016\n"
      "v_mfma_f32_16x16x32_bf16 v[156:159], v[64:67], v[246:249], 0\n"
      "v_cvt_pk_bf16_f32 v38, v38, v42\n"
      "v_cvt_pk_bf16_f32 v36, v36, v40\n"
      "v_mfma_f32_16x16x32_bf16 v[156:159], v[68:71], v[250:253], v[156:159]\n"
      "v_exp_f32 v89, v89\n"
      "v_mfma_f32_16x16x32_bf16 v[240:243], v[20:23], v[246:249], 0\n"
      "v_exp_f32 v124, v124\n"
      "v_mfma_f32_16x16x32_bf16 v[240:243], v[24:27], v[250:253], v[240:243]\n"
      "v_exp_f32 v91, v91\n"
      "v_mfma_f32_16x16x32_bf16 v[176:179], v[136:139], v[246:249], 0\n"
      "v_mfma_f32_16x16x32_bf16 v[176:179], v[140:143], v[250:253], v[176:179]\n"
      "buffer_store_dwordx2 v[190:191], v2, s[44:47], s101 offen offset:512 nt sc1\n"
      "s_waitcnt lgkmcnt(5)\n"
      "v_mfma_f32_16x16x32_bf16 v[140:143], a[32:35], v[72:75], 0\n"
      "s_waitcnt lgkmcnt(3)\n"
      "v_mfma_f32_16x16x32_bf16 v[140:143], a[36:39], v[148:151], v[140:143]\n"
      "s_waitcnt lgkmcnt(2)\n"
      "v_mfma_f32_16x16x32_bf16 v[140:143], a[40:43], v[164:167], v[140:143]\n"
      "v_cvt_pk_bf16_f32 v42, v39, v43\n"
      "v_mfma_f32_16x16x32_bf16 v[140:143], a[44:47], v[204:207], v[140:143]\n"
      "v_exp_f32 v240, v240\n"
      "v_mfma_f32_16x16x32_bf16 v[168:171], a[144:147], v[72:75], 0\n"
      "v_mfma_f32_16x16x32_bf16 v[168:171], a[148:151], v[148:151], v[168:171]\n"
      "v_exp_f32 v211, v211\n"
      "v_mfma_f32_16x16x32_bf16 v[168:171], a[152:155], v[164:167], v[168:171]\n"
      "v_exp_f32 v179, v179\n"
      "v_mfma_f32_16x16x32_bf16 v[168:171], a[156:159], v[204:207], v[168:171]\n"
      "v_exp_f32 v156, v156\n"
      "v_mfma_f32_16x16x32_bf16 v[160:163], a[32:35], v[92:95], 0\n"
      "v_pk_max_i16 v36, v36, 0\n"
      "s_waitcnt lgkmcnt(1)\n"
      "v_mfma_f32_16x16x32_bf16 v[160:163], a[36:39], v[76:79], v[160:163]\n"
      "v_mfma_f32_16x16x32_bf16 v[160:163], a[40:43], v[84:87], v[160:163]\n"
      "s_waitcnt lgkmcnt(0)\n"
      "v_mfma_f32_16x16x32_bf16 v[160:163], a[44:47], v[80:83], v[160:163]\n"
      "v_exp_f32 v208, v208\n"
      "v_mfma_f32_16x16x32_bf16 v[20:23], a[176:179], v[92:95], 0\n"
      "v_exp_f32 v127, v127\n"
      "v_mfma_f32_16x16x32_bf16 v[20:23], a[180:183], v[76:79], v[20:23]\n"
      "v_exp_f32 v157, v157\n"
      "v_mfma_f32_16x16x32_bf16 v[20:23], a[184:187], v[84:87], v[20:23]\n"
      "v_exp_f32 v243, v243\n"
      "v_mfma_f32_16x16x32_bf16 v[20:23], a[188:191], v[80:83], v[20:23]\n"
      "v_cvt_pk_bf16_f32 v100, v156, v157\n"
      "v_pk_max_i16 v42, v42, 0\n"
      "v_mfma_f32_16x16x32_bf16 v[196:199], a[48:51], v[72:75], 0\n"
      "v_exp_f32 v159, v159\n"
      "v_mfma_f32_16x16x32_bf16 v[196:199], a[52:55], v[148:151], v[196:199]\n"
      "v_exp_f32 v210, v210\n"
      "v_mfma_f32_16x16x32_bf16 v[196:199], a[56:59], v[164:167], v[196:199]\n"
      "v_exp_f32 v158, v158\n"
      "v_mfma_f32_16x16x32_bf16 v[196:199], a[60:63], v[204:207], v[196:199]\n"
      "v_exp_f32 v126, v126\n"
      "v_mfma_f32_16x16x32_bf16 v[26:29], a[0:3], v[92:95], 0\n"
      "v_mfma_f32_16x16x32_bf16 v[26:29], a[4:7], v[76:79], v[26:29]\n"
      "v_exp_f32 v241, v241\n"
      "v_mfma_f32_16x16x32_bf16 v[26:29], a[8:11], v[84:87], v[26:29]\n"
      "v_cvt_pk_bf16_f32 v101, v158, v159\n"
      "v_cvt_pk_bf16_f32 v40, v37, v41\n"
      "v_mfma_f32_16x16x32_bf16 v[26:29], a[12:15], v[80:83], v[26:29]\n"
      "buffer_store_dwordx2 v[192:193], v2, s[44:47], s101 offen offset:256 nt sc1\n"
      "v_cvt_pk_bf16_f32 v39, v46, v50\n"
      "v_mfma_f32_16x16x32_bf16 v[244:247], a[16:19], v[92:95], 0\n"
      "v_cvt_pk_bf16_f32 v252, v240, v241\n"
      "v_pk_max_i16 v39, v39, 0\n"
      "v_mfma_f32_16x16x32_bf16 v[244:247], a[20:23], v[76:79], v[244:247]\n"
      "v_exp_f32 v209, v209\n"
      "v_mfma_f32_16x16x32_bf16 v[244:247], a[24:27], v[84:87], v[244:247]\n"
      "v_exp_f32 v178, v178\n"
      "v_mfma_f32_16x16x32_bf16 v[244:247], a[28:31], v[80:83], v[244:247]\n"
      "v_exp_f32 v177, v177\n"
      "v_mfma_f32_16x16x32_bf16 v[152:155], a[128:131], v[92:95], 0\n"
      "v_mfma_f32_16x16x32_bf16 v[152:155], a[132:135], v[76:79], v[152:155]\n"
      "v_exp_f32 v176, v176\n"
      "v_mfma_f32_16x16x32_bf16 v[152:155], a[136:139], v[84:87], v[152:155]\n"
      "v_exp_f32 v88, v88\n"
      "v_mfma_f32_16x16x32_bf16 v[152:155], a[140:143], v[80:83], v[152:155]\n"
      "v_exp_f32 v90, v90\n"
      "v_mfma_f32_16x16x32_bf16 v[156:159], a[48:51], v[92:95], 0\n"
      "v_cvt_pk_bf16_f32 v251, v90, v91\n"
      "v_cvt_pk_bf16_f32 v254, v208, v209\n"
      "v_mfma_f32_16x16x32_bf16 v[156:159], a[52:55], v[76:79], v[156:159]\n"
      "v_mfma_f32_16x16x32_bf16 v[156:159], a[56:59], v[84:87], v[156:159]\n"
      "v_cvt_pk_bf16_f32 v68, v160, v161\n"
      "v_mfma_f32_16x16x32_bf16 v[156:159], a[60:63], v[80:83], v[156:159]\n"
      "v_cvt_pk_bf16_f32 v103, v178, v179\n"
      "v_cvt_pk_bf16_f32 v255, v210, v211\n"
      "v_mfma_f32_16x16x32_bf16 v[136:139], a[176:179], v[72:75], 0\n"
      "v_exp_f32 v242, v242\n"
      "v_mfma_f32_16x16x32_bf16 v[136:139], a[180:183], v[148:151], v[136:139]\n"
      "v_cvt_pk_bf16_f32 v241, v142, v143\n"
      "v_cvt_pk_bf16_f32 v250, v88, v89\n"
      "v_mfma_f32_16x16x32_bf16 v[136:139], a[184:187], v[164:167], v[136:139]\n"
      "v_cvt_pk_bf16_f32 v240, v140, v141\n"
      "v_cvt_pk_bf16_f32 v102, v176, v177\n"
      "v_mfma_f32_16x16x32_bf16 v[136:139], a[188:191], v[204:207], v[136:139]\n"
      "v_exp_f32 v125, v125\n"
      "v_mfma_f32_16x16x32_bf16 v[88:91], a[128:131], v[72:75], 0\n"
      "v_mfma_f32_16x16x32_bf16 v[88:91], a[132:135], v[148:151], v[88:91]\n"
      "v_cvt_pk_bf16_f32 v249, v126, v127\n"
      "v_mfma_f32_16x16x32_bf16 v[88:91], a[136:139], v[164:167], v[88:91]\n"
      "v_mfma_f32_16x16x32_bf16 v[88:91], a[140:143], v[204:207], v[88:91]\n"
      "buffer_store_dwordx2 v[30:31], v2, s[44:47], s100 offen offset:512 nt sc1\n"
      "v_cvt_pk_bf16_f32 v253, v242, v243\n"
      "v_mfma_f32_16x16x32_bf16 v[200:203], a[160:163], v[92:95], 0\n"
      "s_waitcnt vmcnt(17)\n"
      "ds_read_b128 v[190:193], v0 offset:8192\n"
      "v_mfma_f32_16x16x32_bf16 v[200:203], a[164:167], v[76:79], v[200:203]\n"
      "v_cvt_pk_bf16_f32 v69, v162, v163\n"
      "v_mfma_f32_16x16x32_bf16 v[200:203], a[168:171], v[84:87], v[200:203]\n"
      "v_cvt_pk_bf16_f32 v248, v124, v125\n"
      "v_mfma_f32_16x16x32_bf16 v[200:203], a[172:175], v[80:83], v[200:203]\n"
      "ds_read_b128 v[124:127], v9 offset:2048\n"
      "v_pk_max_i16 v40, v40, 0\n"
      "v_smfmac_f32_16x16x64_bf16 v[128:131], v[12:15], v[248:255], v5\n"
      "ds_read_b128 v[140:143], v0 offset:9216\n"
      "v_cvt_pk_bf16_f32 v243, v198, v199\n"
      "v_mfma_f32_16x16x32_bf16 v[220:223], v[248:251], v[56:59], v[220:223]\n"
      "ds_read_b128 v[208:211], v0 offset:9728\n"
      "v_mfma_f32_16x16x32_bf16 v[220:223], v[252:255], v[60:63], v[220:223]\n"
      "v_mfma_f32_16x16x32_bf16 v[220:223], v[100:103], v[116:119], v[220:223]\n"
      "v_cvt_pk_bf16_f32 v31, v22, v23\n"
      "v_smfmac_f32_16x16x32_bf16 v[128:131], v[12:13], v[100:103], v5\n"
      "buffer_store_dwordx2 v[194:195], v2, s[44:47], s101 offen offset:768 nt sc1\n"
      "v_mfma_f32_16x16x32_bf16 v[216:219], v[248:251], v[180:183], v[216:219]\n"
      "v_cvt_pk_bf16_f32 v65, v28, v29\n"
      "v_cvt_pk_bf16_f32 v43, v47, v51\n"
      "v_mfma_f32_16x16x32_bf16 v[216:219], v[252:255], v[184:187], v[216:219]\n"
      "v_cvt_pk_bf16_f32 v41, v45, v49\n"
      "v_mfma_f32_16x16x32_bf16 v[216:219], v[100:103], v[120:123], v[216:219]\n"
      "ds_read_b128 v[176:179], v0 offset:8704\n"
      "v_add_f32 v128, v128, v129\n"
      "v_mfma_f32_16x16x32_bf16 v[224:227], v[248:251], v[232:235], v[224:227]\n"
      "v_mfma_f32_16x16x32_bf16 v[224:227], v[252:255], v[236:239], v[224:227]\n"
      "v_rcp_f32 v129, v128\n"
      "v_mfma_f32_16x16x32_bf16 v[224:227], v[100:103], v[228:231], v[224:227]\n"
      "ds_bpermute_b32 v130, v4, v129 offset:128\n"
      "v_mfma_f32_16x16x32_bf16 v[232:235], a[144:147], v[92:95], 0\n"
      "v_mfma_f32_16x16x32_bf16 v[232:235], a[148:151], v[76:79], v[232:235]\n"
      "v_mfma_f32_16x16x32_bf16 v[232:235], a[152:155], v[84:87], v[232:235]\n"
      "ds_read_b128 v[116:119], v10 offset:0\n"
      "v_mfma_f32_16x16x32_bf16 v[232:235], a[156:159], v[80:83], v[232:235]\n"
      "ds_read_b128 v[120:123], v9 offset:0\n"
      "v_mfma_f32_16x16x32_bf16 v[212:215], v[248:251], v[108:111], v[212:215]\n"
      "v_cvt_pk_bf16_f32 v37, v44, v48\n"
      "v_mov_b64 v[44:45], 0\n"
      "v_mfma_f32_16x16x32_bf16 v[212:215], v[252:255], v[112:115], v[212:215]\n"
      "v_mfma_f32_16x16x32_bf16 v[212:215], v[100:103], v[52:55], v[212:215]\n"
      "v_cvt_pk_bf16_f32 v24, v152, v153\n"
      "v_mfma_f32_16x16x32_bf16 v[100:103], a[16:19], v[72:75], 0\n"
      "v_mfma_f32_16x16x32_bf16 v[100:103], a[20:23], v[148:151], v[100:103]\n"
      "v_cvt_pk_bf16_f32 v64, v26, v27\n"
      "v_cvt_pk_bf16_f32 v28, v200, v201\n"
      "v_mfma_f32_16x16x32_bf16 v[100:103], a[24:27], v[164:167], v[100:103]\n"
      "s_waitcnt lgkmcnt(2)\n"
      "v_mul_f32_dpp v216, v130, v216 row_newbcast:0 row_mask:0xf bank_mask:0xf\n"
      "v_mul_f32_dpp v217, v130, v217 row_newbcast:4 row_mask:0xf bank_mask:0xf\n"
      "v_mfma_f32_16x16x32_bf16 v[100:103], a[28:31], v[204:207], v[100:103]\n"
      "v_cvt_pk_bf16_f32 v67, v246, v247\n"
      "v_mul_f32_dpp v214, v130, v214 row_newbcast:8 row_mask:0xf bank_mask:0xf\n"
      "v_mfma_f32_16x16x32_bf16 v[252:255], a[160:163], v[72:75], 0\n"
      "v_mul_f32_dpp v212, v130, v212 row_newbcast:0 row_mask:0xf bank_mask:0xf\n"
      "v_cvt_pk_bf16_f32 v30, v20, v21\n"
      "v_mfma_f32_16x16x32_bf16 v[252:255], a[164:167], v[148:151], v[252:255]\n"
      "v_mul_f32_dpp v225, v130, v225 row_newbcast:4 row_mask:0xf bank_mask:0xf\n"
      "v_mfma_f32_16x16x32_bf16 v[252:255], a[168:171], v[164:167], v[252:255]\n"
      "v_pk_max_i16 v37, v37, 0\n"
      "v_mul_f32_dpp v223, v130, v223 row_newbcast:12 row_mask:0xf bank_mask:0xf\n"
      "v_mfma_f32_16x16x32_bf16 v[252:255], a[172:175], v[204:207], v[252:255]\n"
      "buffer_store_dwordx2 v[36:37], v2, s[44:47], s34 offen offset:0 nt sc1\n"
      "v_cvt_pk_bf16_f32 v238, v100, v101\n"
      "v_mfma_f32_16x16x32_bf16 v[48:51], a[0:3], v[72:75], 0\n"
      "v_cvt_pk_bf16_f32 v29, v202, v203\n"
      "v_mfma_f32_16x16x32_bf16 v[48:51], a[4:7], v[148:151], v[48:51]\n"
      "v_mul_f32_dpp v219, v130, v219 row_newbcast:12 row_mask:0xf bank_mask:0xf\n"
      "v_mfma_f32_16x16x32_bf16 v[48:51], a[8:11], v[164:167], v[48:51]\n"
      "v_mul_f32_dpp v213, v130, v213 row_newbcast:4 row_mask:0xf bank_mask:0xf\n"
      "v_mul_f32_dpp v227, v130, v227 row_newbcast:12 row_mask:0xf bank_mask:0xf\n"
      "v_mfma_f32_16x16x32_bf16 v[48:51], a[12:15], v[204:207], v[48:51]\n"
      "v_cvt_pk_bf16_f32 v58, v136, v137\n"
      "v_cvt_pk_bf16_f32 v27, v234, v235\n"
      "v_mfma_f32_16x16x32_bf16 v[228:231], v[190:193], a[96:99], 0\n"
      "v_mul_f32_dpp v224, v130, v224 row_newbcast:0 row_mask:0xf bank_mask:0xf\n"
      "v_mfma_f32_16x16x32_bf16 v[228:231], v[176:179], a[100:103], v[228:231]\n"
      "v_mov_b64 v[246:247], 0\n"
      "v_pk_max_i16 v38, v38, 0\n"
      "v_mfma_f32_16x16x32_bf16 v[228:231], v[140:143], a[104:107], v[228:231]\n"
      "v_pk_max_i16 v41, v41, 0\n"
      "v_mfma_f32_16x16x32_bf16 v[228:231], v[208:211], a[108:111], v[228:231]\n"
      "v_mul_f32_dpp v220, v130, v220 row_newbcast:0 row_mask:0xf bank_mask:0xf\n"
      "v_mfma_f32_16x16x32_bf16 v[184:187], v[190:193], a[80:83], 0\n"
      "v_mul_f32_dpp v222, v130, v222 row_newbcast:8 row_mask:0xf bank_mask:0xf\n"
      "v_mfma_f32_16x16x32_bf16 v[184:187], v[176:179], a[84:87], v[184:187]\n"
      "v_mfma_f32_16x16x32_bf16 v[184:187], v[140:143], a[88:91], v[184:187]\n"
      "v_mov_b64 v[152:153], 0\n"
      "v_mfma_f32_16x16x32_bf16 v[184:187], v[208:211], a[92:95], v[184:187]\n"
      "v_cvt_pk_bf16_f32 v71, v158, v159\n"
      "v_mfma_f32_16x16x32_bf16 v[20:23], v[190:193], a[112:115], 0\n"
      "v_cvt_pk_bf16_f32 v57, v254, v255\n"
      "v_mul_f32_dpp v218, v130, v218 row_newbcast:8 row_mask:0xf bank_mask:0xf\n"
      "v_mfma_f32_16x16x32_bf16 v[20:23], v[176:179], a[116:119], v[20:23]\n"
      "v_mul_f32_dpp v215, v130, v215 row_newbcast:12 row_mask:0xf bank_mask:0xf\n"
      "v_cvt_pk_bf16_f32 v136, v228, v229\n"
      "v_mfma_f32_16x16x32_bf16 v[20:23], v[140:143], a[120:123], v[20:23]\n"
      "v_cvt_pk_bf16_f32 v236, v48, v49\n"
      "v_mfma_f32_16x16x32_bf16 v[20:23], v[208:211], a[124:127], v[20:23]\n"
      "v_cvt_pk_bf16_f32 v55, v170, v171\n"
      "v_mul_f32_dpp v226, v130, v226 row_newbcast:8 row_mask:0xf bank_mask:0xf\n"
      "v_mfma_f32_16x16x32_bf16 v[212:215], v[144:147], a[192:195], v[212:215]\n"
      "v_cvt_pk_bf16_f32 v66, v244, v245\n"
      "v_cvt_pk_bf16_f32 v237, v50, v51\n"
      "v_mfma_f32_16x16x32_bf16 v[212:215], v[104:107], a[196:199], v[212:215]\n"
      "v_mfma_f32_16x16x32_bf16 v[212:215], v[172:175], a[200:203], v[212:215]\n"
      "v_cvt_pk_bf16_f32 v239, v102, v103\n"
      "v_mfma_f32_16x16x32_bf16 v[212:215], v[132:135], a[204:207], v[212:215]\n"
      "v_cvt_pk_bf16_f32 v53, v90, v91\n"
      "v_cvt_pk_bf16_f32 v242, v196, v197\n"
      "v_mfma_f32_16x16x32_bf16 v[248:251], a[32:35], v[190:193], 0\n"
      "v_cvt_pk_bf16_f32 v26, v232, v233\n"
      "v_mfma_f32_16x16x32_bf16 v[248:251], a[36:39], v[176:179], v[248:251]\n"
      "v_mfma_f32_16x16x32_bf16 v[248:251], a[40:43], v[140:143], v[248:251]\n"
      "v_mfma_f32_16x16x32_bf16 v[248:251], a[44:47], v[208:211], v[248:251]\n"
      "buffer_store_dwordx2 v[40:41], v2, s[44:47], s34 offen offset:256 nt sc1\n"
      "v_maximum3_f32 v8, v8, v128, v129\n"
      "v_mfma_f32_16x16x32_bf16 v[224:227], v[144:147], a[240:243], v[224:227]\n"
      "v_mfma_f32_16x16x32_bf16 v[224:227], v[104:107], a[244:247], v[224:227]\n"
      "v_mfma_f32_16x16x32_bf16 v[224:227], v[172:175], a[248:251], v[224:227]\n"
      "v_mfma_f32_16x16x32_bf16 v[224:227], v[132:135], a[252:255], v[224:227]\n"
      "ds_read_b128 v[232:235], v0 offset:12288\n"
      "v_cvt_pk_bf16_f32 v70, v156, v157\n"
      "v_mfma_f32_16x16x32_bf16 v[216:219], v[144:147], a[208:211], v[216:219]\n"
      "s_waitcnt vmcnt(13)\n"
      "ds_read_b128 v[156:159], v0 offset:4096\n"
      "ds_read_b128 v[48:51], v0 offset:5632\n"
      "v_mfma_f32_16x16x32_bf16 v[216:219], v[104:107], a[212:215], v[216:219]\n"
      "v_cvt_pk_bf16_f32 v108, v248, v249\n"
      "v_mul_f32_dpp v221, v130, v221 row_newbcast:4 row_mask:0xf bank_mask:0xf\n"
      "v_mfma_f32_16x16x32_bf16 v[216:219], v[172:175], a[216:219], v[216:219]\n"
      "v_mfma_f32_16x16x32_bf16 v[216:219], v[132:135], a[220:223], v[216:219]\n"
      "v_cvt_pk_bf16_f32 v25, v154, v155\n"
      "v_mfma_f32_16x16x32_bf16 v[100:103], a[0:3], v[190:193], 0\n"
      "ds_read2_b32 v[112:113], v16 offset0:0 offset1:2\n"
      "v_mfma_f32_16x16x32_bf16 v[100:103], a[4:7], v[176:179], v[100:103]\n"
      "v_mfma_f32_16x16x32_bf16 v[100:103], a[8:11], v[140:143], v[100:103]\n"
      "ds_read_b128 v[194:197], v0 offset:5120\n"
      "v_mfma_f32_16x16x32_bf16 v[100:103], a[12:15], v[208:211], v[100:103]\n"
      "s_waitcnt lgkmcnt(6)\n"
      "v_smfmac_f32_16x16x64_bf16 v[152:155], v[116:119], v[236:243], v5\n"
      "buffer_store_dwordx2 v[38:39], v2, s[44:47], s34 offen offset:512 nt sc1\n"
      "v_mfma_f32_16x16x32_bf16 v[128:131], v[236:239], v[24:27], 0\n"
      "v_mfma_f32_16x16x32_bf16 v[128:131], v[240:243], v[28:31], v[128:131]\n"
      "ds_read_b128 v[180:183], v0 offset:13824\n"
      "v_cvt_pk_bf16_f32 v52, v88, v89\n"
      "v_mfma_f32_16x16x32_bf16 v[88:91], v[64:67], v[24:27], 0\n"
      "v_cvt_pk_bf16_f32 v214, v214, v218\n"
      "v_mfma_f32_16x16x32_bf16 v[88:91], v[68:71], v[28:31], v[88:91]\n"
      "s_waitcnt lgkmcnt(6)\n"
      "v_smfmac_f32_16x16x64_bf16 v[246:249], v[116:119], v[120:127], v5\n"
      "ds_read_b128 v[120:123], v0 offset:4608\n"
      "v_mfma_f32_16x16x32_bf16 v[200:203], a[48:51], v[190:193], 0\n"
      "v_cvt_pk_bf16_f32 v229, v22, v23\n"
      "v_cvt_pk_bf16_f32 v109, v250, v251\n"
      "v_mfma_f32_16x16x32_bf16 v[200:203], a[52:55], v[176:179], v[200:203]\n"
      "v_cvt_pk_bf16_f32 v59, v138, v139\n"
      "v_cvt_pk_bf16_f32 v212, v212, v216\n"
      "v_mfma_f32_16x16x32_bf16 v[200:203], a[56:59], v[140:143], v[200:203]\n"
      "v_cvt_pk_bf16_f32 v216, v213, v217\n"
      "v_add_f32 v152, v152, v153\n"
      "v_mfma_f32_16x16x32_bf16 v[200:203], a[60:63], v[208:211], v[200:203]\n"
      "v_cvt_pk_bf16_f32 v218, v215, v219\n"
      "v_cvt_pk_bf16_f32 v41, v186, v187\n"
      "v_mfma_f32_16x16x32_bf16 v[220:223], v[144:147], a[224:227], v[220:223]\n"
      "v_mfma_f32_16x16x32_bf16 v[220:223], v[104:107], a[228:231], v[220:223]\n"
      "v_cvt_pk_bf16_f32 v40, v184, v185\n"
      "v_mfma_f32_16x16x32_bf16 v[220:223], v[172:175], a[232:235], v[220:223]\n"
      "v_exp_f32 v129, v129\n"
      "v_mfma_f32_16x16x32_bf16 v[220:223], v[132:135], a[236:239], v[220:223]\n"
      "buffer_store_dwordx2 v[188:189], v2, s[44:47], s101 offen offset:0 nt sc1\n"
      "s_waitcnt lgkmcnt(5)\n"
      "v_mfma_f32_16x16x32_bf16 v[36:39], a[0:3], v[156:159], 0\n"
      "v_cvt_pk_bf16_f32 v111, v202, v203\n"
      "s_waitcnt lgkmcnt(0)\n"
      "v_mfma_f32_16x16x32_bf16 v[36:39], a[4:7], v[120:123], v[36:39]\n"
      "v_mfma_f32_16x16x32_bf16 v[36:39], a[8:11], v[194:197], v[36:39]\n"
      "v_exp_f32 v89, v89\n"
      "v_mfma_f32_16x16x32_bf16 v[36:39], a[12:15], v[48:51], v[36:39]\n"
      "v_cvt_pk_bf16_f32 v105, v102, v103\n"
      "v_mfma_f32_16x16x32_bf16 v[124:127], v[156:159], a[80:83], 0\n"
      "v_pk_max_i16 v216, v216, 0\n"
      "v_cvt_pk_bf16_f32 v215, v222, v226\n"
      "v_mfma_f32_16x16x32_bf16 v[124:127], v[120:123], a[84:87], v[124:127]\n"
      "v_mfma_f32_16x16x32_bf16 v[124:127], v[194:197], a[88:91], v[124:127]\n"
      "v_cvt_pk_bf16_f32 v219, v223, v227\n"
      "v_mfma_f32_16x16x32_bf16 v[124:127], v[48:51], a[92:95], v[124:127]\n"
      "v_pk_max_i16 v219, v219, 0\n"
      "v_cvt_pk_bf16_f32 v54, v168, v169\n"
      "v_mfma_f32_16x16x32_bf16 v[172:175], v[190:193], a[64:67], 0\n"
      "v_cvt_pk_bf16_f32 v110, v200, v201\n"
      "v_mfma_f32_16x16x32_bf16 v[172:175], v[176:179], a[68:71], v[172:175]\n"
      "v_mfma_f32_16x16x32_bf16 v[172:175], v[140:143], a[72:75], v[172:175]\n"
      "v_mfma_f32_16x16x32_bf16 v[172:175], v[208:211], a[76:79], v[172:175]\n"
      "v_exp_f32 v130, v130\n"
      "v_mfma_f32_16x16x32_bf16 v[200:203], a[48:51], v[156:159], 0\n"
      "v_mfma_f32_16x16x32_bf16 v[200:203], a[52:55], v[120:123], v[200:203]\n"
      "v_cvt_pk_bf16_f32 v213, v220, v224\n"
      "v_cvt_pk_bf16_f32 v185, v38, v39\n"
      "v_mfma_f32_16x16x32_bf16 v[200:203], a[56:59], v[194:197], v[200:203]\n"
      "s_nop 1\n"
      "v_cvt_pk_bf16_f32 v144, v172, v173\n"
      "v_mfma_f32_16x16x32_bf16 v[200:203], a[60:63], v[48:51], v[200:203]\n"
      "v_exp_f32 v88, v88\n"
      "v_mfma_f32_16x16x32_bf16 v[168:171], a[16:19], v[190:193], 0\n"
      "ds_read_b128 v[248:251], v0 offset:13312\n"
      "v_pk_max_i16 v213, v213, 0\n"
      "v_mfma_f32_16x16x32_bf16 v[168:171], a[20:23], v[176:179], v[168:171]\n"
      "v_exp_f32 v91, v91\n"
      "v_mfma_f32_16x16x32_bf16 v[168:171], a[24:27], v[140:143], v[168:171]\n"
      "v_cvt_pk_bf16_f32 v56, v252, v253\n"
      "v_cvt_pk_bf16_f32 v39, v126, v127\n"
      "v_mfma_f32_16x16x32_bf16 v[168:171], a[28:31], v[208:211], v[168:171]\n"
      "ds_read_b128 v[252:255], v0 offset:12800\n"
      "v_mfma_f32_16x16x32_bf16 v[176:179], a[16:19], v[156:159], 0\n"
      "v_cvt_pk_bf16_f32 v191, v202, v203\n"
      "v_mfma_f32_16x16x32_bf16 v[176:179], a[20:23], v[120:123], v[176:179]\n"
      "v_mfma_f32_16x16x32_bf16 v[176:179], a[24:27], v[194:197], v[176:179]\n"
      "v_cvt_pk_bf16_f32 v38, v124, v125\n"
      "v_cvt_pk_bf16_f32 v217, v221, v225\n"
      "v_mfma_f32_16x16x32_bf16 v[176:179], a[28:31], v[48:51], v[176:179]\n"
      "buffer_store_dwordx2 v[34:35], v2, s[44:47], s100 offen offset:768 nt sc1\n"
      "v_mfma_f32_16x16x32_bf16 v[208:211], a[32:35], v[156:159], 0\n"
      "v_cvt_pk_bf16_f32 v190, v200, v201\n"
      "v_cvt_pk_bf16_f32 v106, v168, v169\n"
      "v_mfma_f32_16x16x32_bf16 v[208:211], a[36:39], v[120:123], v[208:211]\n"
      "v_mfma_f32_16x16x32_bf16 v[208:211], a[40:43], v[194:197], v[208:211]\n"
      "v_mfma_f32_16x16x32_bf16 v[208:211], a[44:47], v[48:51], v[208:211]\n"
      "s_nop 0\n"
      "v_cvt_pk_bf16_f32 v187, v178, v179\n"
      "v_mov_b64 v[138:139], 0\n"
      "v_mfma_f32_16x16x32_bf16 v[140:143], a[0:3], v[232:235], 0\n"
      "v_cvt_pk_bf16_f32 v126, v88, v89\n"
      "s_waitcnt lgkmcnt(0)\n"
      "v_mfma_f32_16x16x32_bf16 v[140:143], a[4:7], v[252:255], v[140:143]\n"
      "v_cvt_pk_bf16_f32 v186, v176, v177\n"
      "v_mfma_f32_16x16x32_bf16 v[140:143], a[8:11], v[248:251], v[140:143]\n"
      "v_exp_f32 v90, v90\n"
      "v_mfma_f32_16x16x32_bf16 v[140:143], a[12:15], v[180:183], v[140:143]\n"
      "v_pk_max_i16 v43, v43, 0\n"
      "v_pk_max_i16 v212, v212, 0\n"
      "v_mfma_f32_16x16x32_bf16 v[132:135], v[232:235], a[80:83], 0\n"
      "v_mfma_f32_16x16x32_bf16 v[132:135], v[252:255], a[84:87], v[132:135]\n"
      "v_exp_f32 v128, v128\n"
      "v_mfma_f32_16x16x32_bf16 v[132:135], v[248:251], a[88:91], v[132:135]\n"
      "v_mfma_f32_16x16x32_bf16 v[132:135], v[180:183], a[92:95], v[132:135]\n"
      "v_cvt_pk_bf16_f32 v124, v128, v129\n"
      "v_smfmac_f32_16x16x64_bf16 v[44:47], v[116:119], v[64:71], v5\n"
      "buffer_store_dwordx2 v[42:43], v2, s[44:47], s34 offen offset:768 nt sc1\n"
      "v_mfma_f32_16x16x32_bf16 v[200:203], a[16:19], v[232:235], 0\n"
      "v_exp_f32 v152, v152\n"
      "v_mfma_f32_16x16x32_bf16 v[200:203], a[20:23], v[252:255], v[200:203]\n"
      "v_cvt_pk_bf16_f32 v104, v100, v101\n"
      "v_add_f32 v246, v246, v247\n"
      "v_mfma_f32_16x16x32_bf16 v[200:203], a[24:27], v[248:251], v[200:203]\n"
      "v_cvt_pk_bf16_f32 v43, v134, v135\n"
      "v_add_f32 v44, v44, v45\n"
      "v_mfma_f32_16x16x32_bf16 v[200:203], a[28:31], v[180:183], v[200:203]\n"
      "v_pk_max_i16 v217, v217, 0\n"
      "v_cvt_pk_bf16_f32 v107, v170, v171\n"
      "v_mfma_f32_16x16x32_bf16 v[224:227], a[48:51], v[232:235], 0\n"
      "v_exp_f32 v131, v131\n"
      "v_mfma_f32_16x16x32_bf16 v[224:227], a[52:55], v[252:255], v[224:227]\n"
      "v_mov_b32 v247, 0\n"
      "v_mfma_f32_16x16x32_bf16 v[224:227], a[56:59], v[248:251], v[224:227]\n"
      "v_cvt_pk_bf16_f32 v189, v210, v211\n"
      "v_cvt_pk_bf16_f32 v188, v208, v209\n"
      "v_mfma_f32_16x16x32_bf16 v[224:227], a[60:63], v[180:183], v[224:227]\n"
      "v_cvt_pk_bf16_f32 v184, v36, v37\n"
      "v_cvt_pk_bf16_f32 v170, v200, v201\n"
      "v_mfma_f32_16x16x32_bf16 v[208:211], v[156:159], a[96:99], 0\n"
      "s_waitcnt vmcnt(11)\n"
      "ds_read_b128 v[34:37], v0 offset:0\n"
      "v_cvt_pk_bf16_f32 v127, v90, v91\n"
      "v_mfma_f32_16x16x32_bf16 v[208:211], v[120:123], a[100:103], v[208:211]\n"
      "ds_read_b128 v[176:179], v0 offset:1536\n"
      "v_cvt_pk_bf16_f32 v125, v130, v131\n"
      "v_mfma_f32_16x16x32_bf16 v[208:211], v[194:197], a[104:107], v[208:211]\n"
      "ds_read_b128 v[220:223], v0 offset:1024\n"
      "v_cvt_pk_bf16_f32 v228, v20, v21\n"
      "v_mfma_f32_16x16x32_bf16 v[208:211], v[48:51], a[108:111], v[208:211]\n"
      "ds_read2_b32 v[114:115], v16 offset0:128 offset1:130\n"
      "v_cvt_pk_bf16_f32 v171, v202, v203\n"
      "v_mfma_f32_16x16x32_bf16 v[88:91], v[156:159], a[64:67], 0\n"
      "v_cvt_pk_bf16_f32 v42, v132, v133\n"
      "v_mfma_f32_16x16x32_bf16 v[88:91], v[120:123], a[68:71], v[88:91]\n"
      "v_exp_f32_dpp v247, v246 quad_perm:[0,1,2,3] row_mask:0xf bank_mask:0x1\n"
      "v_mfma_f32_16x16x32_bf16 v[88:91], v[194:197], a[72:75], v[88:91]\n"
      "v_cvt_pk_bf16_f32 v137, v230, v231\n"
      "v_mov_b64 v[20:21], 0\n"
      "v_mfma_f32_16x16x32_bf16 v[88:91], v[48:51], a[76:79], v[88:91]\n"
      "buffer_store_dwordx2 v[212:213], v2, s[44:47], s35 offen offset:0 nt sc1\n"
      "ds_read_b128 v[128:131], v0 offset:512\n"
      "v_mfma_f32_16x16x32_bf16 v[100:103], v[156:159], a[112:115], 0\n"
      "v_mfma_f32_16x16x32_bf16 v[100:103], v[120:123], a[116:119], v[100:103]\n"
      "v_cvt_pk_bf16_f32 v168, v140, v141\n"
      "v_cvt_pk_bf16_f32 v145, v174, v175\n"
      "v_mfma_f32_16x16x32_bf16 v[100:103], v[194:197], a[120:123], v[100:103]\n"
      "v_cvt_pk_bf16_f32 v174, v224, v225\n"
      "v_mfma_f32_16x16x32_bf16 v[100:103], v[48:51], a[124:127], v[100:103]\n"
      "v_pk_max_i16 v218, v218, 0\n"
      "v_smfmac_f32_16x16x64_bf16 v[138:141], v[116:119], v[184:191], v5\n"
      "v_mfma_f32_16x16x32_bf16 v[120:123], v[104:107], v[24:27], 0\n"
      "v_pk_max_i16 v215, v215, 0\n"
      "v_cvt_pk_bf16_f32 v135, v210, v211\n"
      "v_mfma_f32_16x16x32_bf16 v[120:123], v[108:111], v[28:31], v[120:123]\n"
      "s_waitcnt lgkmcnt(1)\n"
      "v_mov_b32_dpp v194, v114 row_ror:8 row_mask:0xf bank_mask:0xf\n"
      "v_mov_b32_dpp v246, v247 quad_perm:[1,1,3,3] row_mask:0xf bank_mask:0xf\n"
      "v_smfmac_f32_16x16x64_bf16 v[20:23], v[116:119], v[104:111], v5\n"
      "v_add_f32 v138, v138, v139\n"
      "v_mfma_f32_16x16x32_bf16 v[48:51], a[32:35], v[34:37], 0\n"
      "s_waitcnt lgkmcnt(0)\n"
      "v_mfma_f32_16x16x32_bf16 v[48:51], a[36:39], v[128:131], v[48:51]\n"
      "v_mfma_f32_16x16x32_bf16 v[48:51], a[40:43], v[220:223], v[48:51]\n"
      "v_cvt_pk_bf16_f32 v246, v247, v246\n"
      "v_cvt_pk_bf16_f32 v175, v226, v227\n"
      "v_mfma_f32_16x16x32_bf16 v[48:51], a[44:47], v[176:179], v[48:51]\n"
      "v_exp_f32 v44, v44\n"
      "v_mfma_f32_16x16x32_bf16 v[200:203], a[48:51], v[34:37], 0\n"
      "v_exp_f32 v121, v121\n"
      "v_mfma_f32_16x16x32_bf16 v[200:203], a[52:55], v[128:131], v[200:203]\n"
      "v_cvt_pk_bf16_f32 v169, v142, v143\n"
      "v_cndmask_b32_dpp v153, v44, v152, vcc quad_perm:[0,0,2,2] row_mask:0xf bank_mask:0xf\n"
      "v_mfma_f32_16x16x32_bf16 v[200:203], a[56:59], v[220:223], v[200:203]\n"
      "v_exp_f32 v120, v120\n"
      "v_mfma_f32_16x16x32_bf16 v[200:203], a[60:63], v[176:179], v[200:203]\n"
      "v_cvt_pk_bf16_f32 v227, v102, v103\n"
      "v_pk_max_i16 v214, v214, 0\n"
      "v_mfma_f32_16x16x32_bf16 v[156:159], a[16:19], v[34:37], 0\n"
      "v_cvt_pk_bf16_f32 v134, v208, v209\n"
      "v_cvt_pk_bf16_f32 v226, v100, v101\n"
      "v_mfma_f32_16x16x32_bf16 v[156:159], a[20:23], v[128:131], v[156:159]\n"
      "v_mfma_f32_16x16x32_bf16 v[156:159], a[24:27], v[220:223], v[156:159]\n"
      "v_cvt_pk_bf16_f32 v143, v90, v91\n"
      "v_cvt_pk_bf16_f32 v142, v88, v89\n"
      "v_mfma_f32_16x16x32_bf16 v[156:159], a[28:31], v[176:179], v[156:159]\n"
      "buffer_store_dwordx2 v[214:215], v2, s[44:47], s35 offen offset:512 nt sc1\n"
      "v_mov_b32_dpp v192, v112 row_ror:8 row_mask:0xf bank_mask:0xf\n"
      "v_mfma_f32_16x16x32_bf16 v[88:91], a[0:3], v[34:37], 0\n"
      "v_add_f32 v20, v20, v21\n"
      "v_mfma_f32_16x16x32_bf16 v[88:91], a[4:7], v[128:131], v[88:91]\n"
      "v_cvt_pk_bf16_f32 v214, v200, v201\n"
      "v_cvt_pk_bf16_f32 v213, v50, v51\n"
      "v_mfma_f32_16x16x32_bf16 v[88:91], a[8:11], v[220:223], v[88:91]\n"
      "v_mfma_f32_16x16x32_bf16 v[88:91], a[12:15], v[176:179], v[88:91]\n"
      "v_cvt_pk_bf16_f32 v215, v202, v203\n"
      "v_cvt_pk_bf16_f32 v211, v158, v159\n"
      "v_mfma_f32_16x16x32_bf16 v[100:103], v[34:37], a[112:115], 0\n"
      "v_cvt_pk_bf16_f32 v212, v48, v49\n"
      "v_mfma_f32_16x16x32_bf16 v[100:103], v[128:131], a[116:119], v[100:103]\n"
      "v_mov_b32_dpp v195, v115 row_ror:8 row_mask:0xf bank_mask:0xf\n"
      "v_mfma_f32_16x16x32_bf16 v[100:103], v[220:223], a[120:123], v[100:103]\n"
      "ds_bpermute_b32 v158, v7, v246 offset:248\n"
      "v_mfma_f32_16x16x32_bf16 v[100:103], v[176:179], a[124:127], v[100:103]\n"
      "v_cvt_pk_bf16_f32 v208, v88, v89\n"
      "v_cvt_pk_bf16_f32 v209, v90, v91\n"
      "v_mfma_f32_16x16x32_bf16 v[200:203], a[32:35], v[232:235], 0\n"
      "v_exp_f32 v122, v122\n"
      "v_mfma_f32_16x16x32_bf16 v[200:203], a[36:39], v[252:255], v[200:203]\n"
      "v_cvt_pk_bf16_f32 v210, v156, v157\n"
      "v_mfma_f32_16x16x32_bf16 v[200:203], a[40:43], v[248:251], v[200:203]\n"
      "s_nop 0\n"
      "v_cvt_pk_bf16_f32 v225, v102, v103\n"
      "v_mfma_f32_16x16x32_bf16 v[200:203], a[44:47], v[180:183], v[200:203]\n"
      "v_cvt_pk_bf16_f32 v224, v100, v101\n"
      "v_mov_b64 v[88:89], 0\n"
      "v_mfma_f32_16x16x32_bf16 v[100:103], v[34:37], a[96:99], 0\n"
      "v_exp_f32 v123, v123\n"
      "v_mfma_f32_16x16x32_bf16 v[100:103], v[128:131], a[100:103], v[100:103]\n"
      "ds_bpermute_b32 v159, v7, v246 offset:8\n"
      "v_mfma_f32_16x16x32_bf16 v[100:103], v[220:223], a[104:107], v[100:103]\n"
      "v_mfma_f32_16x16x32_bf16 v[100:103], v[176:179], a[108:111], v[100:103]\n"
      "buffer_store_dwordx2 v[32:33], v2, s[44:47], s100 offen offset:256 nt sc1\n"
      "v_cvt_pk_bf16_f32 v173, v202, v203\n"
      "v_smfmac_f32_16x16x64_bf16 v[88:91], v[116:119], v[208:215], v5\n"
      "v_cvt_pk_bf16_f32 v172, v200, v201\n"
      "v_mfma_f32_16x16x32_bf16 v[48:51], v[232:235], a[112:115], 0\n"
      "v_exp_f32 v138, v138\n"
      "v_mfma_f32_16x16x32_bf16 v[48:51], v[252:255], a[116:119], v[48:51]\n"
      "v_mov_b32_dpp v193, v113 row_ror:8 row_mask:0xf bank_mask:0xf\n"
      "v_mov_b64 v[200:201], 0\n"
      "v_mfma_f32_16x16x32_bf16 v[48:51], v[248:251], a[120:123], v[48:51]\n"
      "v_cvt_pk_bf16_f32 v133, v102, v103\n"
      "v_cvt_pk_bf16_f32 v132, v100, v101\n"
      "v_mfma_f32_16x16x32_bf16 v[48:51], v[180:183], a[124:127], v[48:51]\n"
      "v_exp_f32 v20, v20\n"
      "v_smfmac_f32_16x16x64_bf16 v[200:203], v[116:119], v[168:175], v5\n"
      "v_add_f32 v88, v88, v89\n"
      "v_mfma_f32_16x16x32_bf16 v[100:103], v[34:37], a[80:83], 0\n"
      "v_mov_b64 v[116:117], 0\n"
      "v_mov_b64 v[118:119], 0\n"
      "v_mfma_f32_16x16x32_bf16 v[100:103], v[128:131], a[84:87], v[100:103]\n"
      "s_nop 0\n"
      "v_cvt_pk_bf16_f32 v230, v48, v49\n"
      "v_cvt_pk_bf16_f32 v231, v50, v51\n"
      "v_mfma_f32_16x16x32_bf16 v[100:103], v[220:223], a[88:91], v[100:103]\n"
      "v_exp_f32 v88, v88\n"
      "v_mfma_f32_16x16x32_bf16 v[100:103], v[176:179], a[92:95], v[100:103]\n"
      "v_add_f32 v200, v200, v201\n"
      "v_cndmask_b32_dpp v89, v20, v88, vcc quad_perm:[0,0,2,2] row_mask:0xf bank_mask:0xf\n"
      "v_mfma_f32_16x16x32_bf16 v[196:199], v[34:37], a[64:67], 0\n"
      "v_exp_f32 v200, v200\n"
      "v_mfma_f32_16x16x32_bf16 v[196:199], v[128:131], a[68:71], v[196:199]\n"
      "v_cvt_pk_bf16_f32 v49, v122, v123\n"
      "v_cndmask_b32_dpp v139, v200, v138, vcc quad_perm:[0,0,2,2] row_mask:0xf bank_mask:0xf\n"
      "v_mfma_f32_16x16x32_bf16 v[196:199], v[220:223], a[72:75], v[196:199]\n"
      "L_h6_%=:\n"
      "s_cbranch_execz L_hr_%=\n"
      "v_cvt_pk_bf16_f32 v37, v102, v103\n"
      "v_cvt_pk_bf16_f32 v48, v120, v121\n"
      "v_mfma_f32_16x16x32_bf16 v[196:199], v[176:179], a[76:79], v[196:199]\n"
      "buffer_store_dwordx2 v[216:217], v2, s[44:47], s35 offen offset:256 nt sc1\n"
      "v_cvt_pk_bf16_f32 v36, v100, v101\n"
      "v_mfma_f32_16x16x32_bf16 v[120:123], v[184:187], v[24:27], 0\n"
      "s_not_b64 vcc, vcc\n"
      "v_cndmask_b32_dpp v45, v152, v44, vcc quad_perm:[1,1,3,3] row_mask:0xf bank_mask:0xf\n"
      "v_cndmask_b32_dpp v201, v138, v200, vcc quad_perm:[1,1,3,3] row_mask:0xf bank_mask:0xf\n"
      "v_mfma_f32_16x16x32_bf16 v[120:123], v[188:191], v[28:31], v[120:123]\n"
      "v_cvt_pk_bf16_f32 v153, v153, v45\n"
      "v_cvt_pk_bf16_f32 v140, v196, v197\n"
      "v_mfma_f32_16x16x32_bf16 v[32:35], v[208:211], v[52:55], 0\n"
      "v_cndmask_b32_dpp v21, v88, v20, vcc quad_perm:[1,1,3,3] row_mask:0xf bank_mask:0xf\n"
      "v_add_f32 v88, v88, v138\n"
      "v_mfma_f32_16x16x32_bf16 v[32:35], v[212:215], v[56:59], v[32:35]\n"
      "v_add_f32 v152, v152, v44\n"
      "v_cvt_pk_bf16_f32 v139, v139, v201\n"
      "v_mfma_f32_16x16x32_bf16 v[100:103], v[168:171], v[24:27], 0\n"
      "v_cvt_pk_bf16_f32 v89, v89, v21\n"
      "v_add_f32 v20, v20, v200\n"
      "v_mfma_f32_16x16x32_bf16 v[100:103], v[172:175], v[28:31], v[100:103]\n"
      "v_add_f32 v152, v152, v247\n"
      "v_cvt_pk_bf16_f32 v141, v198, v199\n"
      "v_smfmac_f32_16x16x64_bf16 v[116:119], v[112:115], v[24:31], v5\n"
      "ds_bpermute_b32 v161, v6, v139\n"
      "v_smfmac_f32_16x16x64_bf16 v[116:119], v[192:195], v[52:59], v5\n"
      "ds_bpermute_b32 v19, v7, v153 offset:4\n"
      "v_mfma_f32_16x16x32_bf16 v[200:203], v[208:211], v[24:27], 0\n"
      "v_exp_f32 v122, v122\n"
      "v_mfma_f32_16x16x32_bf16 v[200:203], v[212:215], v[28:31], v[200:203]\n"
      "v_exp_f32 v102, v102\n"
      "v_mfma_f32_16x16x32_bf16 v[176:179], v[232:235], a[64:67], 0\n"
      "v_exp_f32 v101, v101\n"
      "v_mfma_f32_16x16x32_bf16 v[176:179], v[252:255], a[68:71], v[176:179]\n"
      "ds_bpermute_b32 v162, v6, v89 offset:16\n"
      "v_mfma_f32_16x16x32_bf16 v[176:179], v[248:251], a[72:75], v[176:179]\n"
      "v_exp_f32 v120, v120\n"
      "v_mfma_f32_16x16x32_bf16 v[176:179], v[180:183], a[76:79], v[176:179]\n"
      "v_exp_f32 v35, v35\n"
      "v_mfma_f32_16x16x32_bf16 v[244:247], v[168:171], v[52:55], 0\n"
      "v_exp_f32 v200, v200\n"
      "v_mfma_f32_16x16x32_bf16 v[244:247], v[172:175], v[56:59], v[244:247]\n"
      "v_exp_f32 v203, v203\n"
      "v_mfma_f32_16x16x32_bf16 v[24:27], v[236:239], v[52:55], 0\n"
      "v_exp_f32 v100, v100\n"
      "v_mfma_f32_16x16x32_bf16 v[24:27], v[240:243], v[56:59], v[24:27]\n"
      "v_exp_f32 v202, v202\n"
      "v_add_f32 v88, v88, v20\n"
      "v_mfma_f32_16x16x32_bf16 v[28:31], v[104:107], v[52:55], 0\n"
      "v_exp_f32 v121, v121\n"
      "v_mfma_f32_16x16x32_bf16 v[28:31], v[108:111], v[56:59], v[28:31]\n"
      "v_exp_f32 v244, v244\n"
      "v_mfma_f32_16x16x32_bf16 v[196:199], a[128:131], v[232:235], 0\n"
      "s_nop 0\n"
      "v_exp_f32 v24, v24\n"
      "v_mfma_f32_16x16x32_bf16 v[196:199], a[132:135], v[252:255], v[196:199]\n"
      "v_exp_f32 v27, v27\n"
      "v_mfma_f32_16x16x32_bf16 v[196:199], a[136:139], v[248:251], v[196:199]\n"
      "s_nop 0\n"
      "v_exp_f32 v30, v30\n"
      "v_mfma_f32_16x16x32_bf16 v[196:199], a[140:143], v[180:183], v[196:199]\n"
      "v_exp_f32 v201, v201\n"
      "v_mfma_f32_16x16x32_bf16 v[128:131], v[72:75], a[96:99], 0\n"
      "v_cvt_pk_bf16_f32 v147, v178, v179\n"
      "v_cvt_pk_bf16_f32 v50, v100, v101\n"
      "v_mfma_f32_16x16x32_bf16 v[128:131], v[148:151], a[100:103], v[128:131]\n"
      "v_cvt_pk_bf16_f32 v44, v200, v201\n"
      "v_cvt_pk_bf16_f32 v46, v120, v121\n"
      "v_mfma_f32_16x16x32_bf16 v[128:131], v[164:167], a[104:107], v[128:131]\n"
      "ds_bpermute_b32 v163, v6, v139 offset:16\n"
      "v_mfma_f32_16x16x32_bf16 v[128:131], v[204:207], a[108:111], v[128:131]\n"
      "buffer_store_dwordx2 v[218:219], v2, s[44:47], s35 offen offset:768 nt sc1\n"
      "v_exp_f32 v25, v25\n"
      "v_mfma_f32_16x16x32_bf16 v[20:23], v[184:187], v[52:55], 0\n"
      "v_exp_f32 v28, v28\n"
      "v_mfma_f32_16x16x32_bf16 v[20:23], v[188:191], v[56:59], v[20:23]\n"
      "v_exp_f32 v123, v123\n"
      "v_mfma_f32_16x16x32_bf16 v[220:223], v[64:67], v[52:55], 0\n"
      "v_add_f32 v118, v118, v119\n"
      "v_add_f32 v116, v116, v117\n"
      "v_mfma_f32_16x16x32_bf16 v[220:223], v[68:71], v[56:59], v[220:223]\n"
      "v_cvt_pk_bf16_f32 v146, v176, v177\n"
      "v_cvt_pk_bf16_f32 v45, v202, v203\n"
      "v_cvt_pk_bf16_f32 v47, v122, v123\n"
      "v_mfma_f32_16x16x32_bf16 v[52:55], v[92:95], a[96:99], 0\n"
      "v_exp_f32 v21, v21\n"
      "v_mfma_f32_16x16x32_bf16 v[52:55], v[76:79], a[100:103], v[52:55]\n"
      "ds_bpermute_b32 v160, v6, v89\n"
      "v_mfma_f32_16x16x32_bf16 v[52:55], v[84:87], a[104:107], v[52:55]\n"
      "v_exp_f32 v116, v116\n"
      "v_mfma_f32_16x16x32_bf16 v[52:55], v[80:83], a[108:111], v[52:55]\n"
      "v_exp_f32 v31, v31\n"
      "v_mfma_f32_16x16x32_bf16 v[154:157], a[176:179], v[232:235], 0\n"
      "v_exp_f32 v247, v247\n"
      "v_mfma_f32_16x16x32_bf16 v[154:157], a[180:183], v[252:255], v[154:157]\n"
      "v_exp_f32 v246, v246\n"
      "v_mfma_f32_16x16x32_bf16 v[154:157], a[184:187], v[248:251], v[154:157]\n"
      "v_exp_f32 v245, v245\n"
      "v_mfma_f32_16x16x32_bf16 v[154:157], a[188:191], v[180:183], v[154:157]\n"
      "v_exp_f32 v222, v222\n"
      "v_mfma_f32_16x16x32_bf16 v[176:179], v[92:95], a[80:83], 0\n"
      "v_exp_f32 v220, v220\n"
      "v_mfma_f32_16x16x32_bf16 v[176:179], v[76:79], a[84:87], v[176:179]\n"
      "v_cvt_pk_bf16_f32 v122, v244, v245\n"
      "v_cvt_pk_bf16_f32 v216, v196, v197\n"
      "v_mfma_f32_16x16x32_bf16 v[176:179], v[84:87], a[88:91], v[176:179]\n"
      "v_exp_f32 v103, v103\n"
      "v_mfma_f32_16x16x32_bf16 v[176:179], v[80:83], a[92:95], v[176:179]\n"
      "v_cvt_pk_bf16_f32 v51, v102, v103\n"
      "v_add_f32 v200, v88, v152\n"
      "v_mfma_f32_16x16x32_bf16 v[100:103], v[232:235], a[96:99], 0\n"
      "v_exp_f32 v118, v118\n"
      "v_mfma_f32_16x16x32_bf16 v[100:103], v[252:255], a[100:103], v[100:103]\n"
      "v_exp_f32 v26, v26\n"
      "v_mfma_f32_16x16x32_bf16 v[100:103], v[248:251], a[104:107], v[100:103]\n"
      "v_cvt_pk_bf16_f32 v123, v246, v247\n"
      "v_cvt_pk_bf16_f32 v246, v176, v177\n"
      "v_mfma_f32_16x16x32_bf16 v[100:103], v[180:183], a[108:111], v[100:103]\n"
      "v_cvt_pk_bf16_f32 v247, v178, v179\n"
      "v_cvt_pk_bf16_f32 v217, v198, v199\n"
      "v_mfma_f32_16x16x32_bf16 v[176:179], v[72:75], a[64:67], 0\n"
      "ds_bpermute_b32 v18, v7, v153\n"
      "v_mfma_f32_16x16x32_bf16 v[176:179], v[148:151], a[68:71], v[176:179]\n"
      "v_exp_f32 v34, v34\n"
      "v_mfma_f32_16x16x32_bf16 v[176:179], v[164:167], a[72:75], v[176:179]\n"
      "v_exp_f32 v22, v22\n"
      "v_mfma_f32_16x16x32_bf16 v[176:179], v[204:207], a[76:79], v[176:179]\n"
      "v_cvt_pk_bf16_f32 v138, v100, v101\n"
      "v_cvt_pk_bf16_f32 v60, v128, v129\n"
      "v_mfma_f32_16x16x32_bf16 v[196:199], v[72:75], a[80:83], 0\n"
      "v_cvt_pk_bf16_f32 v63, v54, v55\n"
      "v_cvt_pk_bf16_f32 v62, v52, v53\n"
      "v_mfma_f32_16x16x32_bf16 v[196:199], v[148:151], a[84:87], v[196:199]\n"
      "v_add_f32 v101, v116, v118\n"
      "v_cvt_pk_bf16_f32 v139, v102, v103\n"
      "v_mfma_f32_16x16x32_bf16 v[196:199], v[164:167], a[88:91], v[196:199]\n"
      "v_exp_f32 v32, v32\n"
      "v_mfma_f32_16x16x32_bf16 v[196:199], v[204:207], a[92:95], v[196:199]\n"
      "v_cvt_pk_bf16_f32 v61, v130, v131\n"
      "v_exp_f32 v223, v223\n"
      "v_mfma_f32_16x16x32_bf16 v[52:55], v[92:95], a[64:67], 0\n"
      "v_cvt_pk_bf16_f32 v89, v178, v179\n"
      "v_add_f32 v100, v116, v118\n"
      "v_mfma_f32_16x16x32_bf16 v[52:55], v[76:79], a[68:71], v[52:55]\n"
      "v_cvt_pk_bf16_f32 v203, v116, v118\n"
      "v_cvt_pk_bf16_f32 v88, v176, v177\n"
      "v_mfma_f32_16x16x32_bf16 v[52:55], v[84:87], a[72:75], v[52:55]\n"
      "v_cvt_pk_bf16_f32 v202, v116, v118\n"
      "v_cvt_pk_bf16_f32 v128, v24, v25\n"
      "v_mfma_f32_16x16x32_bf16 v[52:55], v[80:83], a[76:79], v[52:55]\n"
      "v_exp_f32 v23, v23\n"
      "v_mfma_f32_16x16x32_bf16 v[116:119], a[160:163], v[232:235], 0\n"
      "v_exp_f32 v221, v221\n"
      "v_mfma_f32_16x16x32_bf16 v[116:119], a[164:167], v[252:255], v[116:119]\n"
      "v_cvt_pk_bf16_f32 v131, v222, v223\n"
      "v_cvt_pk_bf16_f32 v130, v220, v221\n"
      "v_mfma_f32_16x16x32_bf16 v[116:119], a[168:171], v[248:251], v[116:119]\n"
      "v_cvt_pk_bf16_f32 v244, v196, v197\n"
      "v_cvt_pk_bf16_f32 v223, v156, v157\n"
      "v_mfma_f32_16x16x32_bf16 v[116:119], a[172:175], v[180:183], v[116:119]\n"
      "v_cvt_pk_bf16_f32 v91, v54, v55\n"
      "v_cvt_pk_bf16_f32 v222, v154, v155\n"
      "v_mfma_f32_16x16x32_bf16 v[176:179], a[144:147], v[232:235], 0\n"
      "v_cvt_pk_bf16_f32 v121, v30, v31\n"
      "v_cvt_pk_bf16_f32 v90, v52, v53\n"
      "v_mfma_f32_16x16x32_bf16 v[176:179], a[148:151], v[252:255], v[176:179]\n"
      "v_exp_f32 v33, v33\n"
      "v_mfma_f32_16x16x32_bf16 v[176:179], a[152:155], v[248:251], v[176:179]\n"
      "v_cvt_pk_bf16_f32 v245, v198, v199\n"
      "v_cvt_pk_bf16_f32 v220, v116, v117\n"
      "v_mfma_f32_16x16x32_bf16 v[176:179], a[156:159], v[180:183], v[176:179]\n"
      "ds_read_b64 v[156:157], v11 offset:0\n"
      "v_cvt_pk_bf16_f32 v221, v118, v119\n"
      "v_mfma_f32_16x16x32_bf16 v[52:55], v[72:75], a[112:115], 0\n"
      "v_exp_f32 v20, v20\n"
      "v_mfma_f32_16x16x32_bf16 v[52:55], v[148:151], a[116:119], v[52:55]\n"
      "v_permlane16_swap_b32 v100, v101\n"
      "v_mfma_f32_16x16x32_bf16 v[52:55], v[164:167], a[120:123], v[52:55]\n"
      "v_cvt_pk_bf16_f32 v118, v20, v21\n"
      "v_cvt_pk_bf16_f32 v119, v22, v23\n"
      "v_mfma_f32_16x16x32_bf16 v[52:55], v[204:207], a[124:127], v[52:55]\n"
      "v_permlane16_swap_b32 v202, v203\n"
      "v_mfma_f32_16x16x32_bf16 v[20:23], v[92:95], a[112:115], 0\n"
      "v_cvt_pk_bf16_f32 v116, v32, v33\n"
      "v_cvt_pk_bf16_f32 v117, v34, v35\n"
      "v_mfma_f32_16x16x32_bf16 v[20:23], v[76:79], a[116:119], v[20:23]\n"
      "v_exp_f32 v29, v29\n"
      "v_mfma_f32_16x16x32_bf16 v[20:23], v[84:87], a[120:123], v[20:23]\n"
      "v_cvt_pk_bf16_f32 v219, v178, v179\n"
      "v_cvt_pk_bf16_f32 v218, v176, v177\n"
      "v_mfma_f32_16x16x32_bf16 v[20:23], v[80:83], a[124:127], v[20:23]\n"
      "v_cvt_pk_bf16_f32 v196, v52, v53\n"
      "v_cvt_pk_bf16_f32 v120, v28, v29\n"
      "v_mov_b64 v[152:153], v[100:101]\n"
      "v_smfmac_f32_16x16x64_bf16 v[100:103], v[12:15], v[44:51], v5\n"
      "v_cvt_pk_bf16_f32 v129, v26, v27\n"
      "v_smfmac_f32_16x16x64_bf16 v[152:155], v[12:15], v[116:123], v5\n"
      "s_nop 1\n"
      "v_cvt_pk_bf16_f32 v199, v22, v23\n"
      "v_cvt_pk_bf16_f32 v198, v20, v21\n"
      "v_smfmac_f32_16x16x32_bf16 v[100:103], v[12:13], v[124:127], v5\n"
      "v_cvt_pk_bf16_f32 v197, v54, v55\n"
      "s_waitcnt lgkmcnt(0)\n"
      "v_mfma_f32_16x16x4_4b_bf16 v[20:35], v[202:203], v[156:157], 0 cbsz:2 abid:0\n"
      "s_nop 4\n"
      "v_add_f32 v100, v100, v101\n"
      "v_add_f32_dpp v200, v200, v200 row_ror:8 row_mask:0xf bank_mask:0xf\n"
      "v_smfmac_f32_16x16x32_bf16 v[152:155], v[12:13], v[128:131], v5\n"
      "s_nop 1\n"
      "v_mfma_f32_16x16x32_bf16 v[20:23], v[44:47], v[140:143], v[20:23]\n"
      "v_rcp_f32 v101, v100\n"
      "v_mfma_f32_16x16x32_bf16 v[20:23], v[48:51], v[144:147], v[20:23]\n"
      "ds_bpermute_b32 v102, v4, v101\n"
      "v_mfma_f32_16x16x32_bf16 v[20:23], v[124:127], v[88:91], v[20:23]\n"
      "v_mfma_f32_16x16x32_bf16 v[28:31], v[44:47], v[132:135], v[28:31]\n"
      "v_mfma_f32_16x16x32_bf16 v[28:31], v[48:51], v[136:139], v[28:31]\n"
      "v_mfma_f32_16x16x32_bf16 v[28:31], v[124:127], v[60:63], v[28:31]\n"
      "v_mfma_f32_16x16x32_bf16 v[24:27], v[44:47], v[36:39], v[24:27]\n"
      "v_add_f32_dpp v200, v200, v200 row_ror:4 row_mask:0xf bank_mask:0xf\n"
      "v_add_f32 v152, v152, v153\n"
      "v_mfma_f32_16x16x32_bf16 v[24:27], v[48:51], v[40:43], v[24:27]\n"
      "v_mfma_f32_16x16x32_bf16 v[24:27], v[124:127], v[244:247], v[24:27]\n"
      "v_rcp_f32 v153, v152\n"
      "v_mfma_f32_16x16x32_bf16 v[32:35], v[44:47], v[224:227], v[32:35]\n"
      "v_mfma_f32_16x16x32_bf16 v[32:35], v[48:51], v[228:231], v[32:35]\n"
      "s_waitcnt lgkmcnt(0)\n"
      "v_mul_f32_dpp v23, v102, v23 row_newbcast:12 row_mask:0xf bank_mask:0xf\n"
      "v_mfma_f32_16x16x32_bf16 v[32:35], v[124:127], v[196:199], v[32:35]\n"
      "s_nop 1\n"
      "v_mul_f32_dpp v24, v102, v24 row_newbcast:0 row_mask:0xf bank_mask:0xf\n"
      "v_mfma_f32_16x16x32_bf16 v[176:179], v[64:67], v[216:219], 0\n"
      "v_mul_f32_dpp v27, v102, v27 row_newbcast:12 row_mask:0xf bank_mask:0xf\n"
      "v_mfma_f32_16x16x32_bf16 v[176:179], v[68:71], v[220:223], v[176:179]\n"
      "v_mul_f32_dpp v21, v102, v21 row_newbcast:4 row_mask:0xf bank_mask:0xf\n"
      "v_mfma_f32_16x16x32_bf16 v[124:127], v[236:239], v[216:219], 0\n"
      "v_mfma_f32_16x16x32_bf16 v[124:127], v[240:243], v[220:223], v[124:127]\n"
      "v_mul_f32_dpp v30, v102, v30 row_newbcast:8 row_mask:0xf bank_mask:0xf\n"
      "v_mul_f32_dpp v25, v102, v25 row_newbcast:4 row_mask:0xf bank_mask:0xf\n"
      "v_mfma_f32_16x16x32_bf16 v[96:99], v[104:107], v[216:219], 0\n"
      "v_mul_f32_dpp v28, v102, v28 row_newbcast:0 row_mask:0xf bank_mask:0xf\n"
      "v_mul_f32_dpp v26, v102, v26 row_newbcast:8 row_mask:0xf bank_mask:0xf\n"
      "v_mfma_f32_16x16x32_bf16 v[96:99], v[108:111], v[220:223], v[96:99]\n"
      "v_mul_f32_dpp v32, v102, v32 row_newbcast:0 row_mask:0xf bank_mask:0xf\n"
      "v_mul_f32_dpp v22, v102, v22 row_newbcast:8 row_mask:0xf bank_mask:0xf\n"
      "v_mfma_f32_16x16x32_bf16 v[24:27], v[92:95], a[208:211], v[24:27]\n"
      "v_exp_f32 v126, v126\n"
      "v_mfma_f32_16x16x32_bf16 v[24:27], v[76:79], a[212:215], v[24:27]\n"
      "v_mul_f32_dpp v35, v102, v35 row_newbcast:12 row_mask:0xf bank_mask:0xf\n"
      "v_mul_f32_dpp v31, v102, v31 row_newbcast:12 row_mask:0xf bank_mask:0xf\n"
      "v_mfma_f32_16x16x32_bf16 v[24:27], v[84:87], a[216:219], v[24:27]\n"
      "v_mul_f32_dpp v29, v102, v29 row_newbcast:4 row_mask:0xf bank_mask:0xf\n"
      "v_mfma_f32_16x16x32_bf16 v[24:27], v[80:83], a[220:223], v[24:27]\n"
      "v_add_f32_dpp v200, v200, v200 row_ror:2 row_mask:0xf bank_mask:0xf\n"
      "v_mul_f32_dpp v33, v102, v33 row_newbcast:4 row_mask:0xf bank_mask:0xf\n"
      "v_mfma_f32_16x16x32_bf16 v[28:31], v[92:95], a[224:227], v[28:31]\n"
      "v_exp_f32 v179, v179\n"
      "v_mfma_f32_16x16x32_bf16 v[28:31], v[76:79], a[228:231], v[28:31]\n"
      "v_mul_f32_dpp v20, v102, v20 row_newbcast:0 row_mask:0xf bank_mask:0xf\n"
      "v_mul_f32_dpp v34, v102, v34 row_newbcast:8 row_mask:0xf bank_mask:0xf\n"
      "v_mfma_f32_16x16x32_bf16 v[28:31], v[84:87], a[232:235], v[28:31]\n"
      "v_exp_f32 v125, v125\n"
      "v_mfma_f32_16x16x32_bf16 v[28:31], v[80:83], a[236:239], v[28:31]\n"
      "v_add_f32_dpp v200, v200, v200 row_ror:1 row_mask:0xf bank_mask:0xf\n"
      "v_mfma_f32_16x16x32_bf16 v[20:23], v[92:95], a[192:195], v[20:23]\n"
      "v_rcp_f32 v201, v200\n"
      "v_mfma_f32_16x16x32_bf16 v[20:23], v[76:79], a[196:199], v[20:23]\n"
      "v_exp_f32 v124, v124\n"
      "v_mfma_f32_16x16x32_bf16 v[20:23], v[84:87], a[200:203], v[20:23]\n"
      "v_exp_f32 v127, v127\n"
      "v_mfma_f32_16x16x32_bf16 v[20:23], v[80:83], a[204:207], v[20:23]\n"
      "v_exp_f32 v97, v97\n"
      "v_mfma_f32_16x16x32_bf16 v[32:35], v[92:95], a[240:243], v[32:35]\n"
      "v_exp_f32 v178, v178\n"
      "v_mfma_f32_16x16x32_bf16 v[32:35], v[76:79], a[244:247], v[32:35]\n"
      "v_maximum3_f32 v8, v8, v200, v201\n"
      "v_mfma_f32_16x16x32_bf16 v[32:35], v[84:87], a[248:251], v[32:35]\n"
      "v_exp_f32 v99, v99\n"
      "v_mfma_f32_16x16x32_bf16 v[32:35], v[80:83], a[252:255], v[32:35]\n"
      "v_exp_f32 v176, v176\n"
      "v_mfma_f32_16x16x4_4b_bf16 v[44:59], v[202:203], v[156:157], 0 cbsz:2 abid:2\n"
      "ds_bpermute_b32 v154, v4, v153 offset:128\n"
      "v_cvt_pk_bf16_f32 v95, v178, v179\n"
      "v_maximum3_f32 v8, v8, v100, v101\n"
      "v_exp_f32 v177, v177\n"
      "v_cvt_pk_bf16_f32 v22, v22, v26\n"
      "v_mfma_f32_16x16x32_bf16 v[100:103], v[184:187], v[216:219], 0\n"
      "v_exp_f32 v98, v98\n"
      "v_mfma_f32_16x16x32_bf16 v[100:103], v[188:191], v[220:223], v[100:103]\n"
      "v_cvt_pk_bf16_f32 v20, v20, v24\n"
      "v_cvt_pk_bf16_f32 v24, v21, v25\n"
      "v_mfma_f32_16x16x32_bf16 v[48:51], v[116:119], v[36:39], v[48:51]\n"
      "v_cvt_pk_bf16_f32 v21, v28, v32\n"
      "v_cvt_pk_bf16_f32 v93, v126, v127\n"
      "v_mfma_f32_16x16x32_bf16 v[48:51], v[120:123], v[40:43], v[48:51]\n"
      "v_cvt_pk_bf16_f32 v26, v23, v27\n"
      "v_cvt_pk_bf16_f32 v27, v31, v35\n"
      "v_mfma_f32_16x16x32_bf16 v[48:51], v[128:131], v[244:247], v[48:51]\n"
      "v_pk_max_i16 v27, v27, 0\n"
      "v_cvt_pk_bf16_f32 v25, v29, v33\n"
      "v_mfma_f32_16x16x32_bf16 v[56:59], v[116:119], v[224:227], v[56:59]\n"
      "v_maximum3_f32 v8, v8, v152, v153\n"
      "v_mfma_f32_16x16x32_bf16 v[56:59], v[120:123], v[228:231], v[56:59]\n"
      "v_exp_f32 v102, v102\n"
      "v_mfma_f32_16x16x32_bf16 v[56:59], v[128:131], v[196:199], v[56:59]\n"
      "v_exp_f32 v100, v100\n"
      "v_mfma_f32_16x16x32_bf16 v[44:47], v[116:119], v[140:143], v[44:47]\n"
      "v_cvt_pk_bf16_f32 v94, v176, v177\n"
      "v_mfma_f32_16x16x32_bf16 v[44:47], v[120:123], v[144:147], v[44:47]\n"
      "s_waitcnt lgkmcnt(0)\n"
      "v_mul_f32_dpp v48, v154, v48 row_newbcast:0 row_mask:0xf bank_mask:0xf\n"
      "v_cvt_pk_bf16_f32 v92, v124, v125\n"
      "v_mfma_f32_16x16x32_bf16 v[44:47], v[128:131], v[88:91], v[44:47]\n"
      "ds_read_b128 v[80:83], v0 offset:8704\n"
      "v_mul_f32_dpp v59, v154, v59 row_newbcast:12 row_mask:0xf bank_mask:0xf\n"
      "v_mfma_f32_16x16x32_bf16 v[52:55], v[116:119], v[132:135], v[52:55]\n"
      "v_mfma_f32_16x16x32_bf16 v[52:55], v[120:123], v[136:139], v[52:55]\n"
      "v_cvt_pk_bf16_f32 v23, v30, v34\n"
      "v_pk_max_i16 v23, v23, 0\n"
      "v_mfma_f32_16x16x32_bf16 v[52:55], v[128:131], v[60:63], v[52:55]\n"
      "ds_read_b64 v[202:203], v11 offset:4096\n"
      "v_mul_f32_dpp v46, v154, v46 row_newbcast:8 row_mask:0xf bank_mask:0xf\n"
      "v_mfma_f32_16x16x32_bf16 v[28:31], v[168:171], v[216:219], 0\n"
      "v_mul_f32_dpp v49, v154, v49 row_newbcast:4 row_mask:0xf bank_mask:0xf\n"
      "v_mfma_f32_16x16x32_bf16 v[28:31], v[172:175], v[220:223], v[28:31]\n"
      "v_mul_f32_dpp v50, v154, v50 row_newbcast:8 row_mask:0xf bank_mask:0xf\n"
      "v_mul_f32_dpp v57, v154, v57 row_newbcast:4 row_mask:0xf bank_mask:0xf\n"
      "v_mfma_f32_16x16x4_4b_bf16 v[116:131], v[158:159], v[156:157], 0 cbsz:2 abid:0\n"
      "v_mul_f32_dpp v53, v154, v53 row_newbcast:4 row_mask:0xf bank_mask:0xf\n"
      "v_pk_max_i16 v22, v22, 0\n"
      "v_exp_f32 v96, v96\n"
      "v_mul_f32_dpp v45, v154, v45 row_newbcast:4 row_mask:0xf bank_mask:0xf\n"
      "v_mul_f32_dpp v55, v154, v55 row_newbcast:12 row_mask:0xf bank_mask:0xf\n"
      "v_mul_f32_dpp v44, v154, v44 row_newbcast:0 row_mask:0xf bank_mask:0xf\n"
      "v_pk_max_i16 v21, v21, 0\n"
      "v_mfma_f32_16x16x32_bf16 v[32:35], v[208:211], v[216:219], 0\n"
      "v_mul_f32_dpp v51, v154, v51 row_newbcast:12 row_mask:0xf bank_mask:0xf\n"
      "v_mul_f32_dpp v47, v154, v47 row_newbcast:12 row_mask:0xf bank_mask:0xf\n"
      "v_mfma_f32_16x16x32_bf16 v[32:35], v[212:215], v[220:223], v[32:35]\n"
      "v_mul_f32_dpp v54, v154, v54 row_newbcast:8 row_mask:0xf bank_mask:0xf\n"
      "v_mul_f32_dpp v58, v154, v58 row_newbcast:8 row_mask:0xf bank_mask:0xf\n"
      "v_smfmac_f32_16x16x64_bf16 v[116:119], v[160:163], v[140:147], v5\n"
      "v_mul_f32_dpp v52, v154, v52 row_newbcast:0 row_mask:0xf bank_mask:0xf\n"
      "v_mfma_f32_16x16x32_bf16 v[44:47], v[72:75], a[192:195], v[44:47]\n"
      "v_pk_max_i16 v24, v24, 0\n"
      "v_pk_max_i16 v25, v25, 0\n"
      "v_mfma_f32_16x16x32_bf16 v[44:47], v[148:151], a[196:199], v[44:47]\n"
      "v_exp_f32 v103, v103\n"
      "v_mfma_f32_16x16x32_bf16 v[44:47], v[164:167], a[200:203], v[44:47]\n"
      "v_mul_f32_dpp v56, v154, v56 row_newbcast:0 row_mask:0xf bank_mask:0xf\n"
      "v_pk_max_i16 v26, v26, 0\n"
      "v_mfma_f32_16x16x32_bf16 v[44:47], v[204:207], a[204:207], v[44:47]\n"
      "v_exp_f32 v29, v29\n"
      "v_mfma_f32_16x16x32_bf16 v[48:51], v[72:75], a[208:211], v[48:51]\n"
      "v_mov_b64 v[178:179], 0\n"
      "v_mfma_f32_16x16x32_bf16 v[48:51], v[148:151], a[212:215], v[48:51]\n"
      "v_exp_f32 v34, v34\n"
      "v_mfma_f32_16x16x32_bf16 v[48:51], v[164:167], a[216:219], v[48:51]\n"
      "v_mov_b64 v[176:177], 0\n"
      "v_mfma_f32_16x16x32_bf16 v[48:51], v[204:207], a[220:223], v[48:51]\n"
      "v_exp_f32 v28, v28\n"
      "v_smfmac_f32_16x16x64_bf16 v[176:179], v[112:115], v[216:223], v5\n"
      "buffer_store_dwordx2 v[26:27], v2, s[44:47], s73 offen offset:768 nt sc1\n"
      "ds_read_b128 v[84:87], v0 offset:9216\n"
      "v_smfmac_f32_16x16x64_bf16 v[120:123], v[160:163], v[36:43], v5\n"
      "buffer_store_dwordx2 v[24:25], v2, s[44:47], s73 offen offset:256 nt sc1\n"
      "ds_read_b128 v[76:79], v0 offset:9728\n"
      "v_smfmac_f32_16x16x64_bf16 v[128:131], v[160:163], v[224:231], v5\n"
      "ds_read_b128 v[152:155], v0 offset:8192\n"
      "v_smfmac_f32_16x16x64_bf16 v[124:127], v[160:163], v[132:139], v5\n"
      "v_cvt_pk_bf16_f32 v44, v44, v48\n"
      "v_mfma_f32_16x16x32_bf16 v[52:55], v[72:75], a[224:227], v[52:55]\n"
      "v_cvt_pk_bf16_f32 v48, v45, v49\n"
      "v_cvt_pk_bf16_f32 v219, v102, v103\n"
      "v_mfma_f32_16x16x32_bf16 v[52:55], v[148:151], a[228:231], v[52:55]\n"
      "v_exp_f32 v33, v33\n"
      "v_mfma_f32_16x16x32_bf16 v[52:55], v[164:167], a[232:235], v[52:55]\n"
      "v_cvt_pk_bf16_f32 v46, v46, v50\n"
      "v_cvt_pk_bf16_f32 v50, v47, v51\n"
      "v_mfma_f32_16x16x32_bf16 v[52:55], v[204:207], a[236:239], v[52:55]\n"
      "v_pk_max_i16 v20, v20, 0\n"
      "v_mfma_f32_16x16x32_bf16 v[56:59], v[72:75], a[240:243], v[56:59]\n"
      "v_exp_f32 v101, v101\n"
      "v_mfma_f32_16x16x32_bf16 v[56:59], v[148:151], a[244:247], v[56:59]\n"
      "v_exp_f32 v35, v35\n"
      "v_mfma_f32_16x16x32_bf16 v[56:59], v[164:167], a[248:251], v[56:59]\n"
      "v_exp_f32 v32, v32\n"
      "v_mfma_f32_16x16x32_bf16 v[56:59], v[204:207], a[252:255], v[56:59]\n"
      "v_pk_max_i16 v46, v46, 0\n"
      "v_pk_max_i16 v48, v48, 0\n"
      "v_smfmac_f32_16x16x32_bf16 v[128:131], v[18:19], v[196:199], v5\n"
      "buffer_store_dwordx2 v[22:23], v2, s[44:47], s73 offen offset:512 nt sc1\n"
      "s_waitcnt lgkmcnt(0)\n"
      "v_mfma_f32_16x16x32_bf16 v[164:167], a[144:147], v[152:155], 0\n"
      "v_mfma_f32_16x16x32_bf16 v[164:167], a[148:151], v[80:83], v[164:167]\n"
      "v_cvt_pk_bf16_f32 v218, v100, v101\n"
      "v_cvt_pk_bf16_f32 v47, v54, v58\n"
      "v_mfma_f32_16x16x32_bf16 v[164:167], a[152:155], v[84:87], v[164:167]\n"
      "v_cvt_pk_bf16_f32 v49, v53, v57\n"
      "v_pk_max_i16 v49, v49, 0\n"
      "v_mfma_f32_16x16x32_bf16 v[164:167], a[156:159], v[76:79], v[164:167]\n"
      "v_pk_max_i16 v44, v44, 0\n"
      "v_cvt_pk_bf16_f32 v51, v55, v59\n"
      "v_mfma_f32_16x16x32_bf16 v[204:207], a[128:131], v[152:155], 0\n"
      "v_exp_f32 v31, v31\n"
      "v_mfma_f32_16x16x32_bf16 v[204:207], a[132:135], v[80:83], v[204:207]\n"
      "v_pk_max_i16 v51, v51, 0\n"
      "v_and_b32 v75, 0xffff0000, v203\n"
      "v_mfma_f32_16x16x32_bf16 v[204:207], a[136:139], v[84:87], v[204:207]\n"
      "v_cvt_pk_bf16_f32 v222, v28, v29\n"
      "v_pk_max_i16 v50, v50, 0\n"
      "v_mfma_f32_16x16x32_bf16 v[204:207], a[140:143], v[76:79], v[204:207]\n"
      "buffer_store_dwordx2 v[50:51], v2, s[44:47], s75 offen offset:768 nt sc1\n"
      "v_cvt_pk_bf16_f32 v221, v98, v99\n"
      "v_mfma_f32_16x16x32_bf16 v[148:151], a[160:163], v[152:155], 0\n"
      "v_lshlrev_b32 v74, 16, v203\n"
      "v_and_b32 v73, 0xffff0000, v202\n"
      "v_mfma_f32_16x16x32_bf16 v[148:151], a[164:167], v[80:83], v[148:151]\n"
      "v_exp_f32 v30, v30\n"
      "v_mfma_f32_16x16x32_bf16 v[148:151], a[168:171], v[84:87], v[148:151]\n"
      "v_cvt_pk_bf16_f32 v220, v96, v97\n"
      "v_cvt_pk_bf16_f32 v45, v52, v56\n"
      "v_mfma_f32_16x16x32_bf16 v[148:151], a[172:175], v[76:79], v[148:151]\n"
      "v_lshlrev_b32 v72, 16, v202\n"
      "v_cvt_pk_bf16_f32 v217, v34, v35\n"
      "v_mfma_f32_16x16x32_bf16 v[96:99], a[176:179], v[152:155], 0\n"
      "v_add_f32 v128, v128, v129\n"
      "v_cvt_pk_bf16_f32 v223, v30, v31\n"
      "v_mfma_f32_16x16x32_bf16 v[96:99], a[180:183], v[80:83], v[96:99]\n"
      "v_cvt_pk_bf16_f32 v24, v204, v205\n"
      "v_cvt_pk_bf16_f32 v26, v164, v165\n"
      "v_mfma_f32_16x16x32_bf16 v[96:99], a[184:187], v[84:87], v[96:99]\n"
      "v_cvt_pk_bf16_f32 v25, v206, v207\n"
      "v_cvt_pk_bf16_f32 v27, v166, v167\n"
      "v_mfma_f32_16x16x32_bf16 v[96:99], a[188:191], v[76:79], v[96:99]\n"
      "v_cvt_pk_bf16_f32 v28, v148, v149\n"
      "v_fma_f32 v128, v128, v201, v75\n"
      "v_smfmac_f32_16x16x32_bf16 v[116:119], v[18:19], v[88:91], v5\n"
      "ds_read_b128 v[164:167], v0 offset:4608\n"
      "v_cvt_pk_bf16_f32 v216, v32, v33\n"
      "v_smfmac_f32_16x16x32_bf16 v[124:127], v[18:19], v[60:63], v5\n"
      "ds_read_b128 v[204:207], v0 offset:5120\n"
      "v_pk_max_i16 v47, v47, 0\n"
      "v_smfmac_f32_16x16x32_bf16 v[120:123], v[18:19], v[244:247], v5\n"
      "v_cvt_pk_bf16_f32 v29, v150, v151\n"
      "v_cvt_pk_bf16_f32 v31, v98, v99\n"
      "v_cvt_pk_bf16_f32 v30, v96, v97\n"
      "v_mfma_f32_16x16x32_bf16 v[52:55], v[64:67], v[24:27], 0\n"
      "s_nop 0\n"
      "v_add_f32 v124, v124, v125\n"
      "v_add_f32 v116, v116, v117\n"
      "v_mfma_f32_16x16x32_bf16 v[52:55], v[68:71], v[28:31], v[52:55]\n"
      "v_add_f32 v120, v120, v121\n"
      "v_fma_f32 v124, v124, v201, v74\n"
      "v_smfmac_f32_16x16x64_bf16 v[176:179], v[192:195], v[24:31], v5\n"
      "buffer_store_dwordx2 v[20:21], v2, s[44:47], s73 offen offset:0 nt sc1\n"
      "v_fma_f32 v120, v120, v201, v73\n"
      "v_mfma_f32_16x16x32_bf16 v[96:99], v[236:239], v[24:27], 0\n"
      "ds_read_b128 v[148:151], v0 offset:5632\n"
      "ds_read_b128 v[158:161], v0 offset:4096\n"
      "v_mfma_f32_16x16x32_bf16 v[96:99], v[240:243], v[28:31], v[96:99]\n"
      "buffer_store_dwordx2 v[48:49], v2, s[44:47], s75 offen offset:256 nt sc1\n"
      "v_fma_f32 v116, v116, v201, v72\n"
      "v_mfma_f32_16x16x32_bf16 v[56:59], v[208:211], v[24:27], 0\n"
      "v_add_f32 v178, v178, v179\n"
      "v_cvt_pk_bf16_f32 v117, v124, v128\n"
      "v_mfma_f32_16x16x32_bf16 v[56:59], v[212:215], v[28:31], v[56:59]\n"
      "v_exp_f32 v178, v178\n"
      "v_mfma_f32_16x16x32_bf16 v[124:127], v[168:171], v[24:27], 0\n"
      "v_cvt_pk_bf16_f32 v116, v116, v120\n"
      "v_add_f32 v176, v176, v177\n"
      "v_mfma_f32_16x16x32_bf16 v[124:127], v[172:175], v[28:31], v[124:127]\n"
      "v_exp_f32 v176, v176\n"
      "v_mfma_f32_16x16x32_bf16 v[48:51], v[184:187], v[24:27], 0\n"
      "v_cvt_pk_bf16_f32 v19, v176, v178\n"
      "v_cvt_pk_bf16_f32 v18, v176, v178\n"
      "v_mfma_f32_16x16x32_bf16 v[48:51], v[188:191], v[28:31], v[48:51]\n"
      "v_add_f32 v101, v176, v178\n"
      "v_pk_max_i16 v116, v116, 0\n"
      "v_mfma_f32_16x16x32_bf16 v[118:121], v[104:107], v[24:27], 0\n"
      "v_permlane16_swap_b32 v18, v19\n"
      "v_mfma_f32_16x16x32_bf16 v[118:121], v[108:111], v[28:31], v[118:121]\n"
      "v_exp_f32 v59, v59\n"
      "v_mfma_f32_16x16x4_4b_bf16 v[20:35], v[18:19], v[156:157], 0 cbsz:2 abid:0\n"
      "v_add_f32 v100, v176, v178\n"
      "v_pk_max_i16 v117, v117, 0\n"
      "v_exp_f32 v54, v54\n"
      "v_permlane16_swap_b32 v100, v101\n"
      "v_mov_b64 v[200:201], v[100:101]\n"
      "s_nop 0\n"
      "v_smfmac_f32_16x16x64_bf16 v[100:103], v[12:15], v[216:223], v5\n"
      "s_nop 2\n"
      "v_mfma_f32_16x16x32_bf16 v[28:31], v[216:219], v[132:135], v[28:31]\n"
      "v_exp_f32 v121, v121\n"
      "v_mfma_f32_16x16x32_bf16 v[28:31], v[220:223], v[136:139], v[28:31]\n"
      "v_exp_f32 v49, v49\n"
      "v_mfma_f32_16x16x32_bf16 v[28:31], v[92:95], v[60:63], v[28:31]\n"
      "v_exp_f32 v97, v97\n"
      "v_smfmac_f32_16x16x32_bf16 v[100:103], v[12:13], v[92:95], v5\n"
      "v_pk_max_i16 v45, v45, 0\n"
      "v_exp_f32 v118, v118\n"
      "v_mfma_f32_16x16x32_bf16 v[20:23], v[216:219], v[140:143], v[20:23]\n"
      "v_exp_f32 v120, v120\n"
      "v_mfma_f32_16x16x32_bf16 v[20:23], v[220:223], v[144:147], v[20:23]\n"
      "v_exp_f32 v50, v50\n"
      "v_mfma_f32_16x16x32_bf16 v[20:23], v[92:95], v[88:91], v[20:23]\n"
      "buffer_store_dwordx2 v[116:117], v3, s[44:47], s79 offen offset:0 nt sc1\n"
      "v_add_f32 v100, v100, v101\n"
      "v_mfma_f32_16x16x32_bf16 v[32:35], v[216:219], v[224:227], v[32:35]\n"
      "v_rcp_f32 v101, v100\n"
      "v_mfma_f32_16x16x32_bf16 v[32:35], v[220:223], v[228:231], v[32:35]\n"
      "ds_bpermute_b32 v102, v4, v101\n"
      "v_mfma_f32_16x16x32_bf16 v[32:35], v[92:95], v[196:199], v[32:35]\n"
      "s_waitcnt lgkmcnt(1)\n"
      "v_mfma_f32_16x16x32_bf16 v[128:131], a[160:163], v[158:161], 0\n"
      "v_exp_f32 v127, v127\n"
      "v_mfma_f32_16x16x32_bf16 v[128:131], a[164:167], v[164:167], v[128:131]\n"
      "v_exp_f32 v119, v119\n"
      "v_mfma_f32_16x16x32_bf16 v[128:131], a[168:171], v[204:207], v[128:131]\n"
      "v_exp_f32 v57, v57\n"
      "v_mfma_f32_16x16x32_bf16 v[128:131], a[172:175], v[148:151], v[128:131]\n"
      "v_exp_f32 v58, v58\n"
      "v_mfma_f32_16x16x32_bf16 v[24:27], v[216:219], v[36:39], v[24:27]\n"
      "v_exp_f32 v55, v55\n"
      "v_mfma_f32_16x16x32_bf16 v[24:27], v[220:223], v[40:43], v[24:27]\n"
      "v_exp_f32 v99, v99\n"
      "v_mfma_f32_16x16x32_bf16 v[24:27], v[92:95], v[244:247], v[24:27]\n"
      "v_exp_f32 v124, v124\n"
      "v_mfma_f32_16x16x32_bf16 v[220:223], a[176:179], v[158:161], 0\n"
      "v_exp_f32 v56, v56\n"
      "v_mfma_f32_16x16x32_bf16 v[220:223], a[180:183], v[164:167], v[220:223]\n"
      "s_waitcnt lgkmcnt(0)\n"
      "v_mul_f32_dpp v33, v102, v33 row_newbcast:4 row_mask:0xf bank_mask:0xf\n"
      "v_mul_f32_dpp v35, v102, v35 row_newbcast:12 row_mask:0xf bank_mask:0xf\n"
      "v_mfma_f32_16x16x32_bf16 v[220:223], a[184:187], v[204:207], v[220:223]\n"
      "v_mul_f32_dpp v34, v102, v34 row_newbcast:8 row_mask:0xf bank_mask:0xf\n"
      "v_mul_f32_dpp v32, v102, v32 row_newbcast:0 row_mask:0xf bank_mask:0xf\n"
      "v_mfma_f32_16x16x32_bf16 v[220:223], a[188:191], v[148:151], v[220:223]\n"
      "v_mul_f32_dpp v24, v102, v24 row_newbcast:0 row_mask:0xf bank_mask:0xf\n"
      "v_mul_f32_dpp v26, v102, v26 row_newbcast:8 row_mask:0xf bank_mask:0xf\n"
      "v_mfma_f32_16x16x32_bf16 v[92:95], a[128:131], v[158:161], 0\n"
      "v_mul_f32_dpp v30, v102, v30 row_newbcast:8 row_mask:0xf bank_mask:0xf\n"
      "v_mul_f32_dpp v29, v102, v29 row_newbcast:4 row_mask:0xf bank_mask:0xf\n"
      "v_mfma_f32_16x16x32_bf16 v[92:95], a[132:135], v[164:167], v[92:95]\n"
      "v_mul_f32_dpp v28, v102, v28 row_newbcast:0 row_mask:0xf bank_mask:0xf\n"
      "v_mul_f32_dpp v31, v102, v31 row_newbcast:12 row_mask:0xf bank_mask:0xf\n"
      "v_mfma_f32_16x16x32_bf16 v[92:95], a[136:139], v[204:207], v[92:95]\n"
      "v_exp_f32 v51, v51\n"
      "v_mfma_f32_16x16x32_bf16 v[92:95], a[140:143], v[148:151], v[92:95]\n"
      "buffer_store_dwordx2 v[46:47], v2, s[44:47], s75 offen offset:512 nt sc1\n"
      "v_cvt_pk_bf16_f32 v219, v50, v51\n"
      "v_mfma_f32_16x16x32_bf16 v[28:31], v[232:235], a[224:227], v[28:31]\n"
      "v_mul_f32_dpp v25, v102, v25 row_newbcast:4 row_mask:0xf bank_mask:0xf\n"
      "v_mul_f32_dpp v27, v102, v27 row_newbcast:12 row_mask:0xf bank_mask:0xf\n"
      "v_mfma_f32_16x16x32_bf16 v[28:31], v[252:255], a[228:231], v[28:31]\n"
      "v_exp_f32 v48, v48\n"
      "v_mfma_f32_16x16x32_bf16 v[28:31], v[248:251], a[232:235], v[28:31]\n"
      "v_mul_f32_dpp v20, v102, v20 row_newbcast:0 row_mask:0xf bank_mask:0xf\n"
      "v_cvt_pk_bf16_f32 v217, v58, v59\n"
      "v_mfma_f32_16x16x32_bf16 v[28:31], v[180:183], a[236:239], v[28:31]\n"
      "buffer_store_dwordx2 v[44:45], v2, s[44:47], s75 offen offset:0 nt sc1\n"
      "v_cvt_pk_bf16_f32 v218, v48, v49\n"
      "v_mfma_f32_16x16x32_bf16 v[32:35], v[232:235], a[240:243], v[32:35]\n"
      "v_cvt_pk_bf16_f32 v216, v56, v57\n"
      "v_mul_f32_dpp v23, v102, v23 row_newbcast:12 row_mask:0xf bank_mask:0xf\n"
      "v_mfma_f32_16x16x32_bf16 v[32:35], v[252:255], a[244:247], v[32:35]\n"
      "v_cvt_pk_bf16_f32 v122, v220, v221\n"
      "v_mfma_f32_16x16x32_bf16 v[32:35], v[248:251], a[248:251], v[32:35]\n"
      "s_add_u32 s44, s44, 13107200\n s_addc_u32 s45, s45, 0\n"
      "v_mov_b64 v[178:179], 0\n"
      "v_mfma_f32_16x16x32_bf16 v[32:35], v[180:183], a[252:255], v[32:35]\n"
      "v_mul_f32_dpp v21, v102, v21 row_newbcast:4 row_mask:0xf bank_mask:0xf\n"
      "v_maximum3_f32 v8, v8, v100, v101\n"
      "v_mfma_f32_16x16x32_bf16 v[48:51], a[144:147], v[158:161], 0\n"
      "v_mul_f32_dpp v22, v102, v22 row_newbcast:8 row_mask:0xf bank_mask:0xf\n"
      "v_cvt_pk_bf16_f32 v123, v222, v223\n"
      "v_mfma_f32_16x16x32_bf16 v[48:51], a[148:151], v[164:167], v[48:51]\n"
      "v_exp_f32 v96, v96\n"
      "v_mfma_f32_16x16x32_bf16 v[48:51], a[152:155], v[204:207], v[48:51]\n"
      "L_h7_%=:\n"
      "s_cbranch_execz L_hr_%=\n"
      "v_exp_f32 v53, v53\n"
      "v_mfma_f32_16x16x32_bf16 v[48:51], a[156:159], v[148:151], v[48:51]\n"
      "v_exp_f32 v125, v125\n"
      "v_mfma_f32_16x16x32_bf16 v[24:27], v[232:235], a[208:211], v[24:27]\n"
      "v_exp_f32 v98, v98\n"
      "v_mfma_f32_16x16x32_bf16 v[24:27], v[252:255], a[212:215], v[24:27]\n"
      "v_exp_f32 v126, v126\n"
      "v_mfma_f32_16x16x32_bf16 v[24:27], v[248:251], a[216:219], v[24:27]\n"
      "v_exp_f32 v52, v52\n"
      "v_mfma_f32_16x16x32_bf16 v[24:27], v[180:183], a[220:223], v[24:27]\n"
      "v_cvt_pk_bf16_f32 v221, v120, v121\n"
      "v_cvt_pk_bf16_f32 v117, v94, v95\n"
      "v_mfma_f32_16x16x32_bf16 v[20:23], v[232:235], a[192:195], v[20:23]\n"
      "v_cvt_pk_bf16_f32 v75, v54, v55\n"
      "v_cvt_pk_bf16_f32 v223, v126, v127\n"
      "v_mfma_f32_16x16x32_bf16 v[20:23], v[252:255], a[196:199], v[20:23]\n"
      "v_cvt_pk_bf16_f32 v222, v124, v125\n"
      "v_cvt_pk_bf16_f32 v73, v98, v99\n"
      "v_mfma_f32_16x16x32_bf16 v[20:23], v[248:251], a[200:203], v[20:23]\n"
      "v_cvt_pk_bf16_f32 v220, v118, v119\n"
      "v_cvt_pk_bf16_f32 v120, v128, v129\n"
      "v_mfma_f32_16x16x32_bf16 v[20:23], v[180:183], a[204:207], v[20:23]\n"
      "v_cvt_pk_bf16_f32 v74, v52, v53\n"
      "v_cvt_pk_bf16_f32 v119, v50, v51\n"
      "v_smfmac_f32_16x16x64_bf16 v[200:203], v[12:15], v[216:223], v5\n"
      "ds_read_b128 v[180:183], v0 offset:1024\n"
      "v_cvt_pk_bf16_f32 v118, v48, v49\n"
      "v_mfma_f32_16x16x4_4b_bf16 v[44:59], v[18:19], v[156:157], 0 cbsz:2 abid:2\n"
      "ds_read_b128 v[232:235], v0 offset:0\n"
      "v_cvt_pk_bf16_f32 v116, v92, v93\n"
      "v_cvt_pk_bf16_f32 v72, v96, v97\n"
      "v_cvt_pk_bf16_f32 v20, v20, v24\n"
      "v_cvt_pk_bf16_f32 v22, v22, v26\n"
      "v_cvt_pk_bf16_f32 v26, v23, v27\n"
      "v_smfmac_f32_16x16x32_bf16 v[200:203], v[12:13], v[72:75], v5\n"
      "ds_read_b128 v[124:127], v0 offset:1536\n"
      "v_cvt_pk_bf16_f32 v121, v130, v131\n"
      "v_pk_max_i16 v22, v22, 0\n"
      "v_cvt_pk_bf16_f32 v24, v21, v25\n"
      "v_mfma_f32_16x16x32_bf16 v[96:99], v[104:107], v[116:119], 0\n"
      "v_cvt_pk_bf16_f32 v21, v28, v32\n"
      "v_pk_max_i16 v20, v20, 0\n"
      "v_mfma_f32_16x16x32_bf16 v[96:99], v[108:111], v[120:123], v[96:99]\n"
      "v_pk_max_i16 v26, v26, 0\n"
      "v_cvt_pk_bf16_f32 v27, v31, v35\n"
      "v_mfma_f32_16x16x32_bf16 v[100:103], v[236:239], v[116:119], 0\n"
      "v_cvt_pk_bf16_f32 v25, v29, v33\n"
      "v_mov_b64 v[176:177], 0\n"
      "v_mfma_f32_16x16x32_bf16 v[100:103], v[240:243], v[120:123], v[100:103]\n"
      "v_pk_max_i16 v27, v27, 0\n"
      "v_cvt_pk_bf16_f32 v23, v30, v34\n"
      "v_mfma_f32_16x16x32_bf16 v[248:251], v[184:187], v[116:119], 0\n"
      "ds_read_b128 v[128:131], v0 offset:512\n"
      "v_add_f32 v200, v200, v201\n"
      "v_mfma_f32_16x16x32_bf16 v[248:251], v[188:191], v[120:123], v[248:251]\n"
      "v_exp_f32 v97, v97\n"
      "v_mfma_f32_16x16x32_bf16 v[52:55], v[216:219], v[132:135], v[52:55]\n"
      "v_exp_f32 v100, v100\n"
      "v_mfma_f32_16x16x32_bf16 v[52:55], v[220:223], v[136:139], v[52:55]\n"
      "v_rcp_f32 v201, v200\n"
      "v_mfma_f32_16x16x32_bf16 v[52:55], v[72:75], v[60:63], v[52:55]\n"
      "v_exp_f32 v98, v98\n"
      "v_mfma_f32_16x16x32_bf16 v[48:51], v[216:219], v[36:39], v[48:51]\n"
      "ds_bpermute_b32 v202, v4, v201 offset:128\n"
      "v_mfma_f32_16x16x32_bf16 v[48:51], v[220:223], v[40:43], v[48:51]\n"
      "v_exp_f32 v101, v101\n"
      "v_mfma_f32_16x16x32_bf16 v[48:51], v[72:75], v[244:247], v[48:51]\n"
      "v_exp_f32 v102, v102\n"
      "v_mfma_f32_16x16x32_bf16 v[28:31], v[168:171], v[116:119], 0\n"
      "v_pk_max_i16 v23, v23, 0\n"
      "v_pk_max_i16 v25, v25, 0\n"
      "v_mfma_f32_16x16x32_bf16 v[28:31], v[172:175], v[120:123], v[28:31]\n"
      "v_exp_f32 v248, v248\n"
      "v_mfma_f32_16x16x32_bf16 v[56:59], v[216:219], v[224:227], v[56:59]\n"
      "v_exp_f32 v251, v251\n"
      "v_mfma_f32_16x16x32_bf16 v[56:59], v[220:223], v[228:231], v[56:59]\n"
      "v_exp_f32 v99, v99\n"
      "v_mfma_f32_16x16x32_bf16 v[56:59], v[72:75], v[196:199], v[56:59]\n"
      "v_pk_max_i16 v24, v24, 0\n"
      "v_pk_max_i16 v21, v21, 0\n"
      "v_mfma_f32_16x16x32_bf16 v[44:47], v[216:219], v[140:143], v[44:47]\n"
      "v_exp_f32 v249, v249\n"
      "v_mfma_f32_16x16x32_bf16 v[44:47], v[220:223], v[144:147], v[44:47]\n"
      "v_exp_f32 v31, v31\n"
      "v_mfma_f32_16x16x32_bf16 v[44:47], v[72:75], v[88:91], v[44:47]\n"
      "buffer_store_dwordx2 v[20:21], v2, s[44:47], s100 offen offset:0 nt sc1\n"
      "v_cvt_pk_bf16_f32 v218, v248, v249\n"
      "v_mfma_f32_16x16x32_bf16 v[32:35], v[208:211], v[116:119], 0\n"
      "s_waitcnt lgkmcnt(0)\n"
      "v_mul_f32_dpp v51, v202, v51 row_newbcast:12 row_mask:0xf bank_mask:0xf\n"
      "v_mul_f32_dpp v53, v202, v53 row_newbcast:4 row_mask:0xf bank_mask:0xf\n"
      "v_mfma_f32_16x16x32_bf16 v[32:35], v[212:215], v[120:123], v[32:35]\n"
      "v_exp_f32 v250, v250\n"
      "v_mfma_f32_16x16x32_bf16 v[18:21], a[176:179], v[232:235], 0\n"
      "v_mul_f32_dpp v48, v202, v48 row_newbcast:0 row_mask:0xf bank_mask:0xf\n"
      "v_cvt_pk_bf16_f32 v219, v250, v251\n"
      "v_mfma_f32_16x16x32_bf16 v[18:21], a[180:183], v[128:131], v[18:21]\n"
      "v_mul_f32_dpp v49, v202, v49 row_newbcast:4 row_mask:0xf bank_mask:0xf\n"
      "v_mul_f32_dpp v50, v202, v50 row_newbcast:8 row_mask:0xf bank_mask:0xf\n"
      "v_mfma_f32_16x16x32_bf16 v[18:21], a[184:187], v[180:183], v[18:21]\n"
      "v_exp_f32 v28, v28\n"
      "v_mfma_f32_16x16x32_bf16 v[18:21], a[188:191], v[124:127], v[18:21]\n"
      "v_exp_f32 v33, v33\n"
      "v_smfmac_f32_16x16x64_bf16 v[176:179], v[112:115], v[116:123], v5\n"
      "buffer_store_dwordx2 v[22:23], v2, s[44:47], s100 offen offset:512 nt sc1\n"
      "v_mul_f32_dpp v52, v202, v52 row_newbcast:0 row_mask:0xf bank_mask:0xf\n"
      "v_mfma_f32_16x16x32_bf16 v[112:115], a[144:147], v[232:235], 0\n"
      "v_exp_f32 v32, v32\n"
      "v_mfma_f32_16x16x32_bf16 v[112:115], a[148:151], v[128:131], v[112:115]\n"
      "v_exp_f32 v29, v29\n"
      "v_mfma_f32_16x16x32_bf16 v[112:115], a[152:155], v[180:183], v[112:115]\n"
      "v_cvt_pk_bf16_f32 v255, v20, v21\n"
      "v_mul_f32_dpp v57, v202, v57 row_newbcast:4 row_mask:0xf bank_mask:0xf\n"
      "v_mfma_f32_16x16x32_bf16 v[112:115], a[156:159], v[124:127], v[112:115]\n"
      "v_exp_f32 v96, v96\n"
      "v_mfma_f32_16x16x32_bf16 v[220:223], a[128:131], v[232:235], 0\n"
      "v_exp_f32 v103, v103\n"
      "v_mfma_f32_16x16x32_bf16 v[220:223], a[132:135], v[128:131], v[220:223]\n"
      "v_mul_f32_dpp v47, v202, v47 row_newbcast:12 row_mask:0xf bank_mask:0xf\n"
      "v_cvt_pk_bf16_f32 v254, v18, v19\n"
      "v_mfma_f32_16x16x32_bf16 v[220:223], a[136:139], v[180:183], v[220:223]\n"
      "v_exp_f32 v35, v35\n"
      "v_mfma_f32_16x16x32_bf16 v[220:223], a[140:143], v[124:127], v[220:223]\n"
      "v_mul_f32_dpp v54, v202, v54 row_newbcast:8 row_mask:0xf bank_mask:0xf\n"
      "v_mul_f32_dpp v46, v202, v46 row_newbcast:8 row_mask:0xf bank_mask:0xf\n"
      "v_mfma_f32_16x16x32_bf16 v[20:23], v[64:67], v[116:119], 0\n"
      "v_mul_f32_dpp v59, v202, v59 row_newbcast:12 row_mask:0xf bank_mask:0xf\n"
      "v_mul_f32_dpp v44, v202, v44 row_newbcast:0 row_mask:0xf bank_mask:0xf\n"
      "v_mfma_f32_16x16x32_bf16 v[20:23], v[68:71], v[120:123], v[20:23]\n"
      "v_cvt_pk_bf16_f32 v251, v114, v115\n"
      "v_cvt_pk_bf16_f32 v250, v112, v113\n"
      "v_mfma_f32_16x16x32_bf16 v[120:123], a[160:163], v[232:235], 0\n"
      "v_mul_f32_dpp v45, v202, v45 row_newbcast:4 row_mask:0xf bank_mask:0xf\n"
      "v_cvt_pk_bf16_f32 v117, v102, v103\n"
      "v_mfma_f32_16x16x32_bf16 v[120:123], a[164:167], v[128:131], v[120:123]\n"
      "v_cvt_pk_bf16_f32 v249, v222, v223\n"
      "v_cvt_pk_bf16_f32 v116, v100, v101\n"
      "v_mfma_f32_16x16x32_bf16 v[120:123], a[168:171], v[180:183], v[120:123]\n"
      "v_exp_f32 v30, v30\n"
      "v_mfma_f32_16x16x32_bf16 v[120:123], a[172:175], v[124:127], v[120:123]\n"
      "v_exp_f32 v22, v22\n"
      "v_mfma_f32_16x16x32_bf16 v[48:51], v[152:155], a[208:211], v[48:51]\n"
      "v_mul_f32_dpp v55, v202, v55 row_newbcast:12 row_mask:0xf bank_mask:0xf\n"
      "v_cvt_pk_bf16_f32 v248, v220, v221\n"
      "v_mfma_f32_16x16x32_bf16 v[48:51], v[80:83], a[212:215], v[48:51]\n"
      "v_exp_f32 v34, v34\n"
      "v_mfma_f32_16x16x32_bf16 v[48:51], v[84:87], a[216:219], v[48:51]\n"
      "s_nop 0\n"
      "v_cvt_pk_bf16_f32 v252, v120, v121\n"
      "v_cvt_pk_bf16_f32 v253, v122, v123\n"
      "v_mfma_f32_16x16x32_bf16 v[48:51], v[76:79], a[220:223], v[48:51]\n"
      "v_cvt_pk_bf16_f32 v220, v96, v97\n"
      "v_mul_f32_dpp v56, v202, v56 row_newbcast:0 row_mask:0xf bank_mask:0xf\n"
      "v_mfma_f32_16x16x32_bf16 v[120:123], v[184:187], v[248:251], 0\n"
      "v_mul_f32_dpp v58, v202, v58 row_newbcast:8 row_mask:0xf bank_mask:0xf\n"
      "v_cvt_pk_bf16_f32 v221, v98, v99\n"
      "v_mfma_f32_16x16x32_bf16 v[120:123], v[188:191], v[252:255], v[120:123]\n"
      "v_cvt_pk_bf16_f32 v223, v30, v31\n"
      "v_cvt_pk_bf16_f32 v222, v28, v29\n"
      "v_smfmac_f32_16x16x64_bf16 v[176:179], v[192:195], v[248:255], v5\n"
      "buffer_store_dwordx2 v[24:25], v2, s[44:47], s100 offen offset:256 nt sc1\n"
      "v_cvt_pk_bf16_f32 v216, v32, v33\n"
      "v_mfma_f32_16x16x32_bf16 v[192:195], v[236:239], v[248:251], 0\n"
      "v_exp_f32 v20, v20\n"
      "v_mfma_f32_16x16x32_bf16 v[192:195], v[240:243], v[252:255], v[192:195]\n"
      "v_exp_f32 v23, v23\n"
      "v_mfma_f32_16x16x32_bf16 v[52:55], v[152:155], a[224:227], v[52:55]\n"
      "s_nop 0\n"
      "v_add_f32 v176, v176, v177\n"
      "v_add_f32 v178, v178, v179\n"
      "v_mfma_f32_16x16x32_bf16 v[52:55], v[80:83], a[228:231], v[52:55]\n"
      "v_exp_f32 v178, v178\n"
      "v_mfma_f32_16x16x32_bf16 v[52:55], v[84:87], a[232:235], v[52:55]\n"
      "v_exp_f32 v123, v123\n"
      "v_mfma_f32_16x16x32_bf16 v[52:55], v[76:79], a[236:239], v[52:55]\n"
      "v_exp_f32 v176, v176\n"
      "v_mfma_f32_16x16x32_bf16 v[236:239], v[64:67], v[248:251], 0\n"
      "v_cvt_pk_bf16_f32 v189, v176, v178\n"
      "v_add_f32 v240, v176, v178\n"
      "v_mfma_f32_16x16x32_bf16 v[236:239], v[68:71], v[252:255], v[236:239]\n"
      "v_cvt_pk_bf16_f32 v119, v22, v23\n"
      "v_cvt_pk_bf16_f32 v188, v176, v178\n"
      "v_mfma_f32_16x16x32_bf16 v[44:47], v[152:155], a[192:195], v[44:47]\n"
      "v_exp_f32 v121, v121\n"
      "v_mfma_f32_16x16x32_bf16 v[44:47], v[80:83], a[196:199], v[44:47]\n"
      "v_permlane16_swap_b32 v188, v189\n"
      "v_mfma_f32_16x16x32_bf16 v[44:47], v[84:87], a[200:203], v[44:47]\n"
      "v_cvt_pk_bf16_f32 v217, v34, v35\n"
      "v_add_f32 v241, v176, v178\n"
      "v_mfma_f32_16x16x32_bf16 v[44:47], v[76:79], a[204:207], v[44:47]\n"
      "v_exp_f32 v236, v236\n"
      "v_mfma_f32_16x16x32_bf16 v[92:95], v[168:171], v[248:251], 0\n"
      "v_permlane16_swap_b32 v240, v241\n"
      "v_mfma_f32_16x16x32_bf16 v[92:95], v[172:175], v[252:255], v[92:95]\n"
      "v_exp_f32 v21, v21\n"
      "v_mfma_f32_16x16x32_bf16 v[56:59], v[152:155], a[240:243], v[56:59]\n"
      "v_exp_f32 v120, v120\n"
      "v_mfma_f32_16x16x32_bf16 v[56:59], v[80:83], a[244:247], v[56:59]\n"
      "v_exp_f32 v194, v194\n"
      "v_mfma_f32_16x16x32_bf16 v[56:59], v[84:87], a[248:251], v[56:59]\n"
      "v_exp_f32 v237, v237\n"
      "v_mfma_f32_16x16x32_bf16 v[56:59], v[76:79], a[252:255], v[56:59]\n"
      "v_cvt_pk_bf16_f32 v170, v120, v121\n"
      "v_mov_b64 v[76:77], v[240:241]\n"
      "v_smfmac_f32_16x16x64_bf16 v[240:243], v[12:15], v[216:223], v5\n"
      "buffer_store_dwordx2 v[26:27], v2, s[44:47], s100 offen offset:768 nt sc1\n"
      "v_cvt_pk_bf16_f32 v118, v20, v21\n"
      "v_mfma_f32_16x16x32_bf16 v[84:87], v[208:211], v[248:251], 0\n"
      "v_exp_f32 v239, v239\n"
      "v_mfma_f32_16x16x32_bf16 v[84:87], v[212:215], v[252:255], v[84:87]\n"
      "v_cvt_pk_bf16_f32 v44, v44, v48\n"
      "v_cvt_pk_bf16_f32 v48, v45, v49\n"
      "v_cvt_pk_bf16_f32 v49, v53, v57\n"
      "v_cvt_pk_bf16_f32 v46, v46, v50\n"
      "v_smfmac_f32_16x16x32_bf16 v[240:243], v[12:13], v[116:119], v5\n"
      "v_pk_max_i16 v49, v49, 0\n"
      "v_mfma_f32_16x16x4_4b_bf16 v[20:35], v[188:189], v[156:157], 0 cbsz:2 abid:0\n"
      "v_cvt_pk_bf16_f32 v50, v47, v51\n"
      "v_cvt_pk_bf16_f32 v47, v54, v58\n"
      "v_pk_max_i16 v47, v47, 0\n"
      "v_cvt_pk_bf16_f32 v51, v55, v59\n"
      "v_cvt_pk_bf16_f32 v45, v52, v56\n"
      "v_exp_f32 v122, v122\n"
      "v_pk_max_i16 v44, v44, 0\n"
      "v_mfma_f32_16x16x32_bf16 v[212:215], v[104:107], v[248:251], 0\n"
      "v_pk_max_i16 v45, v45, 0\n"
      "v_pk_max_i16 v46, v46, 0\n"
      "v_mfma_f32_16x16x32_bf16 v[212:215], v[108:111], v[252:255], v[212:215]\n"
      "buffer_store_dwordx2 v[44:45], v2, s[44:47], s101 offen offset:0 nt sc1\n"
      "v_add_f32 v240, v240, v241\n"
      "v_mfma_f32_16x16x32_bf16 v[32:35], v[216:219], v[224:227], v[32:35]\n"
      "v_rcp_f32 v241, v240\n"
      "v_mfma_f32_16x16x32_bf16 v[32:35], v[220:223], v[228:231], v[32:35]\n"
      "ds_bpermute_b32 v242, v4, v241\n"
      "v_mfma_f32_16x16x32_bf16 v[32:35], v[116:119], v[196:199], v[32:35]\n"
      "v_exp_f32 v84, v84\n"
      "v_mfma_f32_16x16x32_bf16 v[20:23], v[216:219], v[140:143], v[20:23]\n"
      "v_exp_f32 v214, v214\n"
      "v_mfma_f32_16x16x32_bf16 v[20:23], v[220:223], v[144:147], v[20:23]\n"
      "v_exp_f32 v87, v87\n"
      "v_mfma_f32_16x16x32_bf16 v[20:23], v[116:119], v[88:91], v[20:23]\n"
      "v_exp_f32 v213, v213\n"
      "v_mfma_f32_16x16x32_bf16 v[24:27], v[216:219], v[36:39], v[24:27]\n"
      "v_exp_f32 v85, v85\n"
      "v_mfma_f32_16x16x32_bf16 v[24:27], v[220:223], v[40:43], v[24:27]\n"
      "v_exp_f32 v95, v95\n"
      "v_mfma_f32_16x16x32_bf16 v[24:27], v[116:119], v[244:247], v[24:27]\n"
      "buffer_store_dwordx2 v[46:47], v2, s[44:47], s101 offen offset:512 nt sc1\n"
      "v_pk_max_i16 v48, v48, 0\n"
      "v_mfma_f32_16x16x32_bf16 v[28:31], v[216:219], v[132:135], v[28:31]\n"
      "v_exp_f32 v86, v86\n"
      "v_mfma_f32_16x16x32_bf16 v[28:31], v[220:223], v[136:139], v[28:31]\n"
      "v_exp_f32 v215, v215\n"
      "v_mfma_f32_16x16x32_bf16 v[28:31], v[116:119], v[60:63], v[28:31]\n"
      "s_waitcnt lgkmcnt(0)\n"
      "v_mul_f32_dpp v21, v242, v21 row_newbcast:4 row_mask:0xf bank_mask:0xf\n"
      "v_mul_f32_dpp v24, v242, v24 row_newbcast:0 row_mask:0xf bank_mask:0xf\n"
      "v_mfma_f32_16x16x4_4b_bf16 v[104:119], v[188:189], v[156:157], 0 cbsz:2 abid:2\n"
      "v_mul_f32_dpp v26, v242, v26 row_newbcast:8 row_mask:0xf bank_mask:0xf\n"
      "v_cvt_pk_bf16_f32 v173, v214, v215\n"
      "v_mul_f32_dpp v27, v242, v27 row_newbcast:12 row_mask:0xf bank_mask:0xf\n"
      "v_mul_f32_dpp v32, v242, v32 row_newbcast:0 row_mask:0xf bank_mask:0xf\n"
      "v_mul_f32_dpp v23, v242, v23 row_newbcast:12 row_mask:0xf bank_mask:0xf\n"
      "v_mul_f32_dpp v31, v242, v31 row_newbcast:12 row_mask:0xf bank_mask:0xf\n"
      "v_mul_f32_dpp v25, v242, v25 row_newbcast:4 row_mask:0xf bank_mask:0xf\n"
      "v_mul_f32_dpp v35, v242, v35 row_newbcast:12 row_mask:0xf bank_mask:0xf\n"
      "v_cvt_pk_bf16_f32 v169, v86, v87\n"
      "v_mfma_f32_16x16x32_bf16 v[24:27], v[158:161], a[208:211], v[24:27]\n"
      "v_mul_f32_dpp v34, v242, v34 row_newbcast:8 row_mask:0xf bank_mask:0xf\n"
      "v_mul_f32_dpp v20, v242, v20 row_newbcast:0 row_mask:0xf bank_mask:0xf\n"
      "v_mfma_f32_16x16x32_bf16 v[24:27], v[164:167], a[212:215], v[24:27]\n"
      "v_exp_f32 v94, v94\n"
      "v_mfma_f32_16x16x32_bf16 v[24:27], v[204:207], a[216:219], v[24:27]\n"
      "v_exp_f32 v92, v92\n"
      "v_mfma_f32_16x16x32_bf16 v[24:27], v[148:151], a[220:223], v[24:27]\n"
      "buffer_store_dwordx2 v[48:49], v2, s[44:47], s101 offen offset:256 nt sc1\n"
      "v_mul_f32_dpp v22, v242, v22 row_newbcast:8 row_mask:0xf bank_mask:0xf\n"
      "v_mul_f32_dpp v29, v242, v29 row_newbcast:4 row_mask:0xf bank_mask:0xf\n"
      "v_cvt_pk_bf16_f32 v175, v94, v95\n"
      "v_mfma_f32_16x16x32_bf16 v[20:23], v[158:161], a[192:195], v[20:23]\n"
      "v_exp_f32 v212, v212\n"
      "v_mfma_f32_16x16x32_bf16 v[20:23], v[164:167], a[196:199], v[20:23]\n"
      "v_cvt_pk_bf16_f32 v172, v212, v213\n"
      "v_mul_f32_dpp v33, v242, v33 row_newbcast:4 row_mask:0xf bank_mask:0xf\n"
      "v_mfma_f32_16x16x32_bf16 v[20:23], v[204:207], a[200:203], v[20:23]\n"
      "v_exp_f32 v93, v93\n"
      "v_mfma_f32_16x16x32_bf16 v[20:23], v[148:151], a[204:207], v[20:23]\n"
      "v_cvt_pk_bf16_f32 v171, v122, v123\n"
      "v_cvt_pk_bf16_f32 v174, v92, v93\n"
      "v_pk_max_i16 v51, v51, 0\n"
      "v_mfma_f32_16x16x32_bf16 v[32:35], v[158:161], a[240:243], v[32:35]\n"
      "v_exp_f32 v192, v192\n"
      "v_mfma_f32_16x16x32_bf16 v[32:35], v[164:167], a[244:247], v[32:35]\n"
      "v_cvt_pk_bf16_f32 v168, v84, v85\n"
      "v_mul_f32_dpp v28, v242, v28 row_newbcast:0 row_mask:0xf bank_mask:0xf\n"
      "v_mfma_f32_16x16x32_bf16 v[32:35], v[204:207], a[248:251], v[32:35]\n"
      "v_exp_f32 v238, v238\n"
      "v_mfma_f32_16x16x32_bf16 v[32:35], v[148:151], a[252:255], v[32:35]\n"
      "v_mul_f32_dpp v30, v242, v30 row_newbcast:8 row_mask:0xf bank_mask:0xf\n"
      "v_pk_max_i16 v50, v50, 0\n"
      "v_smfmac_f32_16x16x64_bf16 v[76:79], v[12:15], v[168:175], v5\n"
      "buffer_store_dwordx2 v[50:51], v2, s[44:47], s101 offen offset:768 nt sc1\n"
      "v_cvt_pk_bf16_f32 v94, v236, v237\n"
      "v_mfma_f32_16x16x32_bf16 v[28:31], v[158:161], a[224:227], v[28:31]\n"
      "v_exp_f32 v193, v193\n"
      "v_mfma_f32_16x16x32_bf16 v[28:31], v[164:167], a[228:231], v[28:31]\n"
      "v_cvt_pk_bf16_f32 v92, v192, v193\n"
      "v_cvt_pk_bf16_f32 v95, v238, v239\n"
      "v_mfma_f32_16x16x32_bf16 v[28:31], v[204:207], a[232:235], v[28:31]\n"
      "v_exp_f32 v195, v195\n"
      "v_mfma_f32_16x16x32_bf16 v[28:31], v[148:151], a[236:239], v[28:31]\n"
      "v_cvt_pk_bf16_f32 v93, v194, v195\n"
      "v_cvt_pk_bf16_f32 v22, v22, v26\n"
      "v_cvt_pk_bf16_f32 v26, v23, v27\n"
      "v_pk_max_i16 v26, v26, 0\n"
      "v_smfmac_f32_16x16x32_bf16 v[76:79], v[12:13], v[92:95], v5\n"
      "s_nop 2\n"
      "v_cvt_pk_bf16_f32 v27, v31, v35\n"
      "v_pk_max_i16 v27, v27, 0\n"
      "v_mfma_f32_16x16x32_bf16 v[104:107], v[168:171], v[140:143], v[104:107]\n"
      "s_nop 1\n"
      "v_add_f32 v76, v76, v77\n"
      "v_mfma_f32_16x16x32_bf16 v[104:107], v[172:175], v[144:147], v[104:107]\n"
      "v_rcp_f32 v77, v76\n"
      "v_mfma_f32_16x16x32_bf16 v[104:107], v[92:95], v[88:91], v[104:107]\n"
      "buffer_store_dwordx2 v[26:27], v2, s[44:47], s34 offen offset:768 nt sc1\n"
      "ds_bpermute_b32 v78, v4, v77 offset:128\n"
      "v_mfma_f32_16x16x32_bf16 v[108:111], v[168:171], v[36:39], v[108:111]\n"
      "v_cvt_pk_bf16_f32 v20, v20, v24\n"
      "v_cvt_pk_bf16_f32 v24, v21, v25\n"
      "v_mfma_f32_16x16x32_bf16 v[108:111], v[172:175], v[40:43], v[108:111]\n"
      "v_mfma_f32_16x16x32_bf16 v[108:111], v[92:95], v[244:247], v[108:111]\n"
      "v_cvt_pk_bf16_f32 v25, v29, v33\n"
      "v_pk_max_i16 v20, v20, 0\n"
      "v_mfma_f32_16x16x32_bf16 v[116:119], v[168:171], v[224:227], v[116:119]\n"
      "v_pk_max_i16 v25, v25, 0\n"
      "v_pk_max_i16 v24, v24, 0\n"
      "v_mfma_f32_16x16x32_bf16 v[116:119], v[172:175], v[228:231], v[116:119]\n"
      "v_mfma_f32_16x16x32_bf16 v[116:119], v[92:95], v[196:199], v[116:119]\n"
      "buffer_store_dwordx2 v[24:25], v2, s[44:47], s34 offen offset:256 nt sc1\n"
      "v_pk_max_i16 v22, v22, 0\n"
      "v_mfma_f32_16x16x32_bf16 v[112:115], v[168:171], v[132:135], v[112:115]\n"
      "v_cvt_pk_bf16_f32 v23, v30, v34\n"
      "v_cvt_pk_bf16_f32 v21, v28, v32\n"
      "v_mfma_f32_16x16x32_bf16 v[112:115], v[172:175], v[136:139], v[112:115]\n"
      "v_mfma_f32_16x16x32_bf16 v[112:115], v[92:95], v[60:63], v[112:115]\n"
      "s_waitcnt lgkmcnt(0)\n"
      "v_mul_f32_dpp v110, v78, v110 row_newbcast:8 row_mask:0xf bank_mask:0xf\n"
      "v_mul_f32_dpp v109, v78, v109 row_newbcast:4 row_mask:0xf bank_mask:0xf\n"
      "v_mul_f32_dpp v111, v78, v111 row_newbcast:12 row_mask:0xf bank_mask:0xf\n"
      "v_mul_f32_dpp v108, v78, v108 row_newbcast:0 row_mask:0xf bank_mask:0xf\n"
      "v_mul_f32_dpp v106, v78, v106 row_newbcast:8 row_mask:0xf bank_mask:0xf\n"
      "v_pk_max_i16 v23, v23, 0\n"
      "v_mfma_f32_16x16x32_bf16 v[108:111], v[232:235], a[208:211], v[108:111]\n"
      "v_mul_f32_dpp v104, v78, v104 row_newbcast:0 row_mask:0xf bank_mask:0xf\n"
      "v_mfma_f32_16x16x32_bf16 v[108:111], v[128:131], a[212:215], v[108:111]\n"
      "v_mul_f32_dpp v105, v78, v105 row_newbcast:4 row_mask:0xf bank_mask:0xf\n"
      "v_pk_max_i16 v21, v21, 0\n"
      "v_mfma_f32_16x16x32_bf16 v[108:111], v[180:183], a[216:219], v[108:111]\n"
      "v_mul_f32_dpp v118, v78, v118 row_newbcast:8 row_mask:0xf bank_mask:0xf\n"
      "v_mul_f32_dpp v107, v78, v107 row_newbcast:12 row_mask:0xf bank_mask:0xf\n"
      "v_mfma_f32_16x16x32_bf16 v[108:111], v[124:127], a[220:223], v[108:111]\n"
      "buffer_store_dwordx2 v[22:23], v2, s[44:47], s34 offen offset:512 nt sc1\n"
      "v_mul_f32_dpp v116, v78, v116 row_newbcast:0 row_mask:0xf bank_mask:0xf\n"
      "v_mfma_f32_16x16x32_bf16 v[104:107], v[232:235], a[192:195], v[104:107]\n"
      "v_mul_f32_dpp v114, v78, v114 row_newbcast:8 row_mask:0xf bank_mask:0xf\n"
      "v_mul_f32_dpp v113, v78, v113 row_newbcast:4 row_mask:0xf bank_mask:0xf\n"
      "v_mfma_f32_16x16x32_bf16 v[104:107], v[128:131], a[196:199], v[104:107]\n"
      "v_mul_f32_dpp v119, v78, v119 row_newbcast:12 row_mask:0xf bank_mask:0xf\n"
      "v_mul_f32_dpp v117, v78, v117 row_newbcast:4 row_mask:0xf bank_mask:0xf\n"
      "v_mfma_f32_16x16x32_bf16 v[104:107], v[180:183], a[200:203], v[104:107]\n"
      "v_mfma_f32_16x16x32_bf16 v[104:107], v[124:127], a[204:207], v[104:107]\n"
      "buffer_store_dwordx2 v[20:21], v2, s[44:47], s34 offen offset:0 nt sc1\n"
      "v_mul_f32_dpp v115, v78, v115 row_newbcast:12 row_mask:0xf bank_mask:0xf\n"
      "v_mfma_f32_16x16x32_bf16 v[116:119], v[232:235], a[240:243], v[116:119]\n"
      "v_mul_f32_dpp v112, v78, v112 row_newbcast:0 row_mask:0xf bank_mask:0xf\n"
      "v_mfma_f32_16x16x32_bf16 v[116:119], v[128:131], a[244:247], v[116:119]\n"
      "v_mfma_f32_16x16x32_bf16 v[116:119], v[180:183], a[248:251], v[116:119]\n"
      "v_mfma_f32_16x16x32_bf16 v[116:119], v[124:127], a[252:255], v[116:119]\n"
      "s_nop 0\n"
      "v_cvt_pk_bf16_f32 v106, v106, v110\n"
      "v_mfma_f32_16x16x32_bf16 v[112:115], v[232:235], a[224:227], v[112:115]\n"
      "v_pk_max_i16 v106, v106, 0\n"
      "v_mfma_f32_16x16x32_bf16 v[112:115], v[128:131], a[228:231], v[112:115]\n"
      "v_cvt_pk_bf16_f32 v110, v107, v111\n"
      "v_mfma_f32_16x16x32_bf16 v[112:115], v[180:183], a[232:235], v[112:115]\n"
      "v_mfma_f32_16x16x32_bf16 v[112:115], v[124:127], a[236:239], v[112:115]\n"
      "s_nop 7\n"
      "v_cvt_pk_bf16_f32 v107, v114, v118\n"
      "v_pk_max_i16 v107, v107, 0\n"
      "buffer_store_dwordx2 v[106:107], v2, s[44:47], s35 offen offset:512 nt sc1\n"
      "v_cvt_pk_bf16_f32 v104, v104, v108\n"
      "v_cvt_pk_bf16_f32 v108, v105, v109\n"
      "v_cvt_pk_bf16_f32 v109, v113, v117\n"
      "v_pk_max_i16 v108, v108, 0\n"
      "v_cvt_pk_bf16_f32 v111, v115, v119\n"
      "v_pk_max_i16 v111, v111, 0\n"
      "v_pk_max_i16 v109, v109, 0\n"
      "v_maximum3_f32 v8, v8, v200, v201\n"
      "buffer_store_dwordx2 v[108:109], v2, s[44:47], s35 offen offset:256 nt sc1\n"
      "v_maximum3_f32 v8, v8, v240, v241\n"
      "v_pk_max_i16 v110, v110, 0\n"
      "s_not_b64 vcc, vcc\n"
      "v_cvt_pk_bf16_f32 v105, v112, v116\n"
      "v_pk_max_i16 v105, v105, 0\n"
      "buffer_store_dwordx2 v[110:111], v2, s[44:47], s35 offen offset:768 nt sc1\n"
      "v_pk_max_i16 v104, v104, 0\n"
      "v_maximum3_f32 v8, v8, v76, v77\n"
      "buffer_store_dwordx2 v[104:105], v2, s[44:47], s35 offen offset:0 nt sc1\n"
      "L_end_%=:\n"
      "s_waitcnt vmcnt(0) lgkmcnt(0)\n"
      "v_cmp_nge_f32 vcc, 0x53800000, v8\n"
      "s_nop 4\n"
      "s_cbranch_vccnz L_ai_end\n"
      "s_endpgm\n"
      "L_ai_end:\n"
      "s_mov_b32 s40, s4\n"
      "s_and_b32 s41, s5, 0xffff\n"
      "s_mov_b32 s42, 104857600\n"
      "s_mov_b32 s44, s14\n"
      "s_and_b32 s45, s15, 0xffff\n"
      "s_mov_b32 s46, 104857600\n"
      "s_mov_b32 s48, s18\n"
      "s_add_u32 s49, s18, 13107200\n"
      "s_mov_b32 s50, 8\n"
      "s_mov_b32 vcc_lo, 0x55555555\n"
      "s_mov_b32 vcc_hi, 0x55555555\n"
      "v_subrev_u32 v9, 448, v9\n"
      "v_subrev_u32 v10, 448, v10\n"
      "v_subrev_u32 v11, 3584, v11\n"
      "s_add_u32 s52, s48, 0\n"
      "s_add_u32 m0, s53, 0\n"
      "s_nop 0\n"
      "buffer_load_dwordx4 v1, s[40:43], s52 offen offset:0 lds\n"
      "buffer_load_dwordx4 v1, s[40:43], s52 offen offset:2048 lds\n"
      "s_add_u32 m0, s53, 896\n"
      "s_nop 0\n"
      "buffer_load_dwordx4 v1, s[40:43], s52 offen offset:128 lds\n"
      "buffer_load_dwordx4 v1, s[40:43], s52 offen offset:2176 lds\n"
      "s_add_u32 s52, s48, 4096\n"
      "s_add_u32 m0, s53, 4096\n"
      "s_nop 0\n"
      "buffer_load_dwordx4 v1, s[40:43], s52 offen offset:0 lds\n"
      "buffer_load_dwordx4 v1, s[40:43], s52 offen offset:2048 lds\n"
      "s_add_u32 m0, s53, 4992\n"
      "s_nop 0\n"
      "buffer_load_dwordx4 v1, s[40:43], s52 offen offset:128 lds\n"
      "buffer_load_dwordx4 v1, s[40:43], s52 offen offset:2176 lds\n"
      "s_add_u32 s52, s48, 8192\n"
      "s_add_u32 m0, s53, 8192\n"
      "s_nop 0\n"
      "buffer_load_dwordx4 v1, s[40:43], s52 offen offset:0 lds\n"
      "buffer_load_dwordx4 v1, s[40:43], s52 offen offset:2048 lds\n"
      "s_add_u32 m0, s53, 9088\n"
      "s_nop 0\n"
      "buffer_load_dwordx4 v1, s[40:43], s52 offen offset:128 lds\n"
      "buffer_load_dwordx4 v1, s[40:43], s52 offen offset:2176 lds\n"
      "s_add_u32 s52, s48, 12288\n"
      "s_add_u32 m0, s53, 12288\n"
      "s_nop 0\n"
      "buffer_load_dwordx4 v1, s[40:43], s52 offen offset:0 lds\n"
      "buffer_load_dwordx4 v1, s[40:43], s52 offen offset:2048 lds\n"
      "s_add_u32 m0, s53, 13184\n"
      "s_nop 0\n"
      "buffer_load_dwordx4 v1, s[40:43], s52 offen offset:128 lds\n"
      "buffer_load_dwordx4 v1, s[40:43], s52 offen offset:2176 lds\n"
      "s_add_u32 s52, s48, 16384\n"
      "s_add_u32 m0, s53, 16384\n"
      "s_nop 0\n"
      "buffer_load_dwordx4 v1, s[40:43], s52 offen offset:0 lds\n"
      "buffer_load_dwordx4 v1, s[40:43], s52 offen offset:2048 lds\n"
      "s_add_u32 m0, s53, 17280\n"
      "s_nop 0\n"
      "buffer_load_dwordx4 v1, s[40:43], s52 offen offset:128 lds\n"
      "buffer_load_dwordx4 v1, s[40:43], s52 offen offset:2176 lds\n"
      "s_add_u32 s52, s48, 20480\n"
      "s_add_u32 m0, s53, 20480\n"
      "s_nop 0\n"
      "buffer_load_dwordx4 v1, s[40:43], s52 offen offset:0 lds\n"
      "buffer_load_dwordx4 v1, s[40:43], s52 offen offset:2048 lds\n"
      "s_add_u32 m0, s53, 21376\n"
      "s_nop 0\n"
      "buffer_load_dwordx4 v1, s[40:43], s52 offen offset:128 lds\n"
      "buffer_load_dwordx4 v1, s[40:43], s52 offen offset:2176 lds\n"
      "s_waitcnt vmcnt(0)\n"
      "L_safe_%=:\n"
      "s_waitcnt vmcnt(45)\n"
      "ds_read_b128 v[148:151], v0 offset:12288\n"
      "ds_read_b128 v[136:139], v0 offset:12800\n"
      "ds_read_b128 v[140:143], v0 offset:13312\n"
      "ds_read_b128 v[144:147], v0 offset:13824\n"
      "s_waitcnt vmcnt(41)\n"
      "ds_read_b128 v[252:255], v0 offset:16384\n"
      "ds_read_b128 v[48:51], v0 offset:16896\n"
      "ds_read_b128 v[124:127], v0 offset:17408\n"
      "ds_read_b128 v[116:119], v0 offset:17920\n"
      "s_waitcnt lgkmcnt(7)\n"
      "v_mfma_f32_16x16x32_bf16 v[216:219], a[0:3], v[148:151], 0\n"
      "s_waitcnt lgkmcnt(6)\n"
      "v_mfma_f32_16x16x32_bf16 v[216:219], a[4:7], v[136:139], v[216:219]\n"
      "s_waitcnt lgkmcnt(5)\n"
      "v_mfma_f32_16x16x32_bf16 v[216:219], a[8:11], v[140:143], v[216:219]\n"
      "s_waitcnt lgkmcnt(4)\n"
      "v_mfma_f32_16x16x32_bf16 v[216:219], a[12:15], v[144:147], v[216:219]\n"
      "v_mfma_f32_16x16x32_bf16 v[96:99], a[16:19], v[148:151], 0\n"
      "v_mfma_f32_16x16x32_bf16 v[96:99], a[20:23], v[136:139], v[96:99]\n"
      "v_mfma_f32_16x16x32_bf16 v[96:99], a[24:27], v[140:143], v[96:99]\n"
      "v_mfma_f32_16x16x32_bf16 v[96:99], a[28:31], v[144:147], v[96:99]\n"
      "v_mfma_f32_16x16x32_bf16 v[24:27], a[32:35], v[148:151], 0\n"
      "v_mfma_f32_16x16x32_bf16 v[24:27], a[36:39], v[136:139], v[24:27]\n"
      "v_mfma_f32_16x16x32_bf16 v[24:27], a[40:43], v[140:143], v[24:27]\n"
      "v_mfma_f32_16x16x32_bf16 v[24:27], a[44:47], v[144:147], v[24:27]\n"
      "v_cvt_pk_bf16_f32 v92, v216, v217\n"
      "v_cvt_pk_bf16_f32 v93, v218, v219\n"
      "v_mfma_f32_16x16x32_bf16 v[216:219], a[48:51], v[148:151], 0\n"
      "v_mfma_f32_16x16x32_bf16 v[216:219], a[52:55], v[136:139], v[216:219]\n"
      "v_cvt_pk_bf16_f32 v94, v96, v97\n"
      "v_cvt_pk_bf16_f32 v95, v98, v99\n"
      "v_mfma_f32_16x16x32_bf16 v[216:219], a[56:59], v[140:143], v[216:219]\n"
      "v_mfma_f32_16x16x32_bf16 v[216:219], a[60:63], v[144:147], v[216:219]\n"
      "v_cvt_pk_bf16_f32 v96, v24, v25\n"
      "v_cvt_pk_bf16_f32 v97, v26, v27\n"
      "v_mfma_f32_16x16x32_bf16 v[248:251], v[148:151], a[64:67], 0\n"
      "v_mfma_f32_16x16x32_bf16 v[248:251], v[136:139], a[68:71], v[248:251]\n"
      "v_mfma_f32_16x16x32_bf16 v[248:251], v[140:143], a[72:75], v[248:251]\n"
      "v_mfma_f32_16x16x32_bf16 v[248:251], v[144:147], a[76:79], v[248:251]\n"
      "v_mfma_f32_16x16x32_bf16 v[24:27], v[148:151], a[80:83], 0\n"
      "v_mfma_f32_16x16x32_bf16 v[24:27], v[136:139], a[84:87], v[24:27]\n"
      "v_cvt_pk_bf16_f32 v98, v216, v217\n"
      "v_cvt_pk_bf16_f32 v99, v218, v219\n"
      "v_mfma_f32_16x16x32_bf16 v[24:27], v[140:143], a[88:91], v[24:27]\n"
      "v_mfma_f32_16x16x32_bf16 v[24:27], v[144:147], a[92:95], v[24:27]\n"
      "v_mfma_f32_16x16x32_bf16 v[216:219], v[148:151], a[96:99], 0\n"
      "v_mfma_f32_16x16x32_bf16 v[216:219], v[136:139], a[100:103], v[216:219]\n"
      "v_cvt_pk_bf16_f32 v58, v248, v249\n"
      "v_cvt_pk_bf16_f32 v59, v250, v251\n"
      "v_mfma_f32_16x16x32_bf16 v[216:219], v[140:143], a[104:107], v[216:219]\n"
      "v_mfma_f32_16x16x32_bf16 v[216:219], v[144:147], a[108:111], v[216:219]\n"
      "v_mfma_f32_16x16x32_bf16 v[248:251], v[148:151], a[112:115], 0\n"
      "v_mfma_f32_16x16x32_bf16 v[248:251], v[136:139], a[116:119], v[248:251]\n"
      "v_cvt_pk_bf16_f32 v134, v24, v25\n"
      "v_cvt_pk_bf16_f32 v135, v26, v27\n"
      "v_mfma_f32_16x16x32_bf16 v[248:251], v[140:143], a[120:123], v[248:251]\n"
      "v_mfma_f32_16x16x32_bf16 v[248:251], v[144:147], a[124:127], v[248:251]\n"
      "s_waitcnt vmcnt(37)\n"
      "ds_read_b128 v[24:27], v0 offset:20480\n"
      "ds_read_b128 v[152:155], v0 offset:20992\n"
      "ds_read_b128 v[20:23], v0 offset:21504\n"
      "ds_read_b128 v[156:159], v0 offset:22016\n"
      "v_cvt_pk_bf16_f32 v106, v216, v217\n"
      "v_cvt_pk_bf16_f32 v107, v218, v219\n"
      "s_waitcnt lgkmcnt(7)\n"
      "v_mfma_f32_16x16x32_bf16 v[220:223], a[0:3], v[252:255], 0\n"
      "s_waitcnt lgkmcnt(6)\n"
      "v_mfma_f32_16x16x32_bf16 v[220:223], a[4:7], v[48:51], v[220:223]\n"
      "v_cvt_pk_bf16_f32 v198, v248, v249\n"
      "v_cvt_pk_bf16_f32 v199, v250, v251\n"
      "s_waitcnt lgkmcnt(5)\n"
      "v_mfma_f32_16x16x32_bf16 v[220:223], a[8:11], v[124:127], v[220:223]\n"
      "s_waitcnt lgkmcnt(4)\n"
      "v_mfma_f32_16x16x32_bf16 v[220:223], a[12:15], v[116:119], v[220:223]\n"
      "v_mfma_f32_16x16x32_bf16 v[52:55], a[16:19], v[252:255], 0\n"
      "v_mfma_f32_16x16x32_bf16 v[52:55], a[20:23], v[48:51], v[52:55]\n"
      "v_mfma_f32_16x16x32_bf16 v[52:55], a[24:27], v[124:127], v[52:55]\n"
      "v_mfma_f32_16x16x32_bf16 v[52:55], a[28:31], v[116:119], v[52:55]\n"
      "v_mfma_f32_16x16x32_bf16 v[244:247], a[32:35], v[252:255], 0\n"
      "v_mfma_f32_16x16x32_bf16 v[244:247], a[36:39], v[48:51], v[244:247]\n"
      "v_mfma_f32_16x16x32_bf16 v[244:247], a[40:43], v[124:127], v[244:247]\n"
      "v_mfma_f32_16x16x32_bf16 v[244:247], a[44:47], v[116:119], v[244:247]\n"
      "v_cvt_pk_bf16_f32 v216, v220, v221\n"
      "v_cvt_pk_bf16_f32 v217, v222, v223\n"
      "v_mfma_f32_16x16x32_bf16 v[248:251], a[48:51], v[252:255], 0\n"
      "v_mfma_f32_16x16x32_bf16 v[248:251], a[52:55], v[48:51], v[248:251]\n"
      "v_cvt_pk_bf16_f32 v218, v52, v53\n"
      "v_cvt_pk_bf16_f32 v219, v54, v55\n"
      "v_mfma_f32_16x16x32_bf16 v[248:251], a[56:59], v[124:127], v[248:251]\n"
      "v_mfma_f32_16x16x32_bf16 v[248:251], a[60:63], v[116:119], v[248:251]\n"
      "v_cvt_pk_bf16_f32 v220, v244, v245\n"
      "v_cvt_pk_bf16_f32 v221, v246, v247\n"
      "v_mfma_f32_16x16x32_bf16 v[244:247], v[252:255], a[64:67], 0\n"
      "v_mfma_f32_16x16x32_bf16 v[244:247], v[48:51], a[68:71], v[244:247]\n"
      "v_mfma_f32_16x16x32_bf16 v[244:247], v[124:127], a[72:75], v[244:247]\n"
      "v_mfma_f32_16x16x32_bf16 v[244:247], v[116:119], a[76:79], v[244:247]\n"
      "v_mfma_f32_16x16x32_bf16 v[52:55], v[252:255], a[80:83], 0\n"
      "v_mfma_f32_16x16x32_bf16 v[52:55], v[48:51], a[84:87], v[52:55]\n"
      "v_cvt_pk_bf16_f32 v222, v248, v249\n"
      "v_cvt_pk_bf16_f32 v223, v250, v251\n"
      "v_mfma_f32_16x16x32_bf16 v[52:55], v[124:127], a[88:91], v[52:55]\n"
      "v_mfma_f32_16x16x32_bf16 v[52:55], v[116:119], a[92:95], v[52:55]\n"
      "v_mfma_f32_16x16x32_bf16 v[248:251], v[252:255], a[96:99], 0\n"
      "v_mfma_f32_16x16x32_bf16 v[248:251], v[48:51], a[100:103], v[248:251]\n"
      "v_cvt_pk_bf16_f32 v120, v244, v245\n"
      "v_cvt_pk_bf16_f32 v121, v246, v247\n"
      "v_mfma_f32_16x16x32_bf16 v[248:251], v[124:127], a[104:107], v[248:251]\n"
      "v_mfma_f32_16x16x32_bf16 v[248:251], v[116:119], a[108:111], v[248:251]\n"
      "v_mfma_f32_16x16x32_bf16 v[244:247], v[252:255], a[112:115], 0\n"
      "v_mfma_f32_16x16x32_bf16 v[244:247], v[48:51], a[116:119], v[244:247]\n"
      "v_cvt_pk_bf16_f32 v68, v52, v53\n"
      "v_cvt_pk_bf16_f32 v69, v54, v55\n"
      "v_mfma_f32_16x16x32_bf16 v[244:247], v[124:127], a[120:123], v[244:247]\n"
      "v_mfma_f32_16x16x32_bf16 v[244:247], v[116:119], a[124:127], v[244:247]\n"
      "s_waitcnt vmcnt(25)\n"
      "ds_read_b128 v[52:55], v0 offset:8192\n"
      "ds_read_b128 v[128:131], v0 offset:8704\n"
      "ds_read_b128 v[168:171], v0 offset:9216\n"
      "ds_read_b128 v[164:167], v0 offset:9728\n"
      "v_cvt_pk_bf16_f32 v36, v248, v249\n"
      "v_cvt_pk_bf16_f32 v37, v250, v251\n"
      "s_waitcnt lgkmcnt(7)\n"
      "v_mfma_f32_16x16x32_bf16 v[248:251], a[0:3], v[24:27], 0\n"
      "s_add_u32 m0, s53, 12288\n"
      "s_add_u32 s54, s49, 12288\n"
      "buffer_load_dwordx4 v1, s[40:43], s54 offen offset:0 lds\n"
      "v_cvt_pk_bf16_f32 v240, v244, v245\n"
      "v_cvt_pk_bf16_f32 v241, v246, v247\n"
      "s_waitcnt lgkmcnt(6)\n"
      "v_mfma_f32_16x16x32_bf16 v[248:251], a[4:7], v[152:155], v[248:251]\n"
      "s_waitcnt lgkmcnt(5)\n"
      "v_mfma_f32_16x16x32_bf16 v[248:251], a[8:11], v[20:23], v[248:251]\n"
      "buffer_load_dwordx4 v1, s[40:43], s54 offen offset:2048 lds\n"
      "s_add_u32 m0, s53, 13184\n"
      "s_waitcnt lgkmcnt(4)\n"
      "v_mfma_f32_16x16x32_bf16 v[248:251], a[12:15], v[156:159], v[248:251]\n"
      "v_mfma_f32_16x16x32_bf16 v[40:43], a[16:19], v[24:27], 0\n"
      "buffer_load_dwordx4 v1, s[40:43], s54 offen offset:128 lds\n"
      "v_mfma_f32_16x16x32_bf16 v[40:43], a[20:23], v[152:155], v[40:43]\n"
      "v_mfma_f32_16x16x32_bf16 v[40:43], a[24:27], v[20:23], v[40:43]\n"
      "buffer_load_dwordx4 v1, s[40:43], s54 offen offset:2176 lds\n"
      "v_mfma_f32_16x16x32_bf16 v[40:43], a[28:31], v[156:159], v[40:43]\n"
      "v_mfma_f32_16x16x32_bf16 v[100:103], a[32:35], v[24:27], 0\n"
      "v_mfma_f32_16x16x32_bf16 v[100:103], a[36:39], v[152:155], v[100:103]\n"
      "v_cvt_pk_bf16_f32 v244, v248, v249\n"
      "v_cvt_pk_bf16_f32 v245, v250, v251\n"
      "v_mfma_f32_16x16x32_bf16 v[100:103], a[40:43], v[20:23], v[100:103]\n"
      "v_mfma_f32_16x16x32_bf16 v[100:103], a[44:47], v[156:159], v[100:103]\n"
      "v_mfma_f32_16x16x32_bf16 v[44:47], a[48:51], v[24:27], 0\n"
      "v_mfma_f32_16x16x32_bf16 v[44:47], a[52:55], v[152:155], v[44:47]\n"
      "v_cvt_pk_bf16_f32 v246, v40, v41\n"
      "v_cvt_pk_bf16_f32 v247, v42, v43\n"
      "v_mfma_f32_16x16x32_bf16 v[44:47], a[56:59], v[20:23], v[44:47]\n"
      "v_mfma_f32_16x16x32_bf16 v[44:47], a[60:63], v[156:159], v[44:47]\n"
      "v_mfma_f32_16x16x32_bf16 v[40:43], v[24:27], a[64:67], 0\n"
      "v_mfma_f32_16x16x32_bf16 v[40:43], v[152:155], a[68:71], v[40:43]\n"
      "v_cvt_pk_bf16_f32 v248, v100, v101\n"
      "v_cvt_pk_bf16_f32 v249, v102, v103\n"
      "v_mfma_f32_16x16x32_bf16 v[40:43], v[20:23], a[72:75], v[40:43]\n"
      "v_mfma_f32_16x16x32_bf16 v[40:43], v[156:159], a[76:79], v[40:43]\n"
      "v_mfma_f32_16x16x32_bf16 v[100:103], v[24:27], a[80:83], 0\n"
      "v_mfma_f32_16x16x32_bf16 v[100:103], v[152:155], a[84:87], v[100:103]\n"
      "v_cvt_pk_bf16_f32 v250, v44, v45\n"
      "v_cvt_pk_bf16_f32 v251, v46, v47\n"
      "v_mfma_f32_16x16x32_bf16 v[100:103], v[20:23], a[88:91], v[100:103]\n"
      "v_mfma_f32_16x16x32_bf16 v[100:103], v[156:159], a[92:95], v[100:103]\n"
      "v_mfma_f32_16x16x32_bf16 v[44:47], v[24:27], a[96:99], 0\n"
      "v_mfma_f32_16x16x32_bf16 v[44:47], v[152:155], a[100:103], v[44:47]\n"
      "v_cvt_pk_bf16_f32 v122, v40, v41\n"
      "v_cvt_pk_bf16_f32 v123, v42, v43\n"
      "v_mfma_f32_16x16x32_bf16 v[44:47], v[20:23], a[104:107], v[44:47]\n"
      "v_mfma_f32_16x16x32_bf16 v[44:47], v[156:159], a[108:111], v[44:47]\n"
      "v_mfma_f32_16x16x32_bf16 v[40:43], v[24:27], a[112:115], 0\n"
      "v_mfma_f32_16x16x32_bf16 v[40:43], v[152:155], a[116:119], v[40:43]\n"
      "v_cvt_pk_bf16_f32 v70, v100, v101\n"
      "v_cvt_pk_bf16_f32 v71, v102, v103\n"
      "v_mfma_f32_16x16x32_bf16 v[40:43], v[20:23], a[120:123], v[40:43]\n"
      "v_mfma_f32_16x16x32_bf16 v[40:43], v[156:159], a[124:127], v[40:43]\n"
      "s_waitcnt vmcnt(21)\n"
      "ds_read_b128 v[100:103], v0 offset:4096\n"
      "ds_read_b128 v[108:111], v0 offset:4608\n"
      "ds_read_b128 v[176:179], v0 offset:5120\n"
      "ds_read_b128 v[172:175], v0 offset:5632\n"
      "v_cvt_pk_bf16_f32 v38, v44, v45\n"
      "v_cvt_pk_bf16_f32 v39, v46, v47\n"
      "s_waitcnt lgkmcnt(7)\n"
      "v_mfma_f32_16x16x32_bf16 v[44:47], a[0:3], v[52:55], 0\n"
      "s_add_u32 m0, s53, 16384\n"
      "s_add_u32 s54, s49, 16384\n"
      "buffer_load_dwordx4 v1, s[40:43], s54 offen offset:0 lds\n"
      "v_cvt_pk_bf16_f32 v242, v40, v41\n"
      "v_cvt_pk_bf16_f32 v243, v42, v43\n"
      "s_waitcnt lgkmcnt(6)\n"
      "v_mfma_f32_16x16x32_bf16 v[44:47], a[4:7], v[128:131], v[44:47]\n"
      "s_waitcnt lgkmcnt(5)\n"
      "v_mfma_f32_16x16x32_bf16 v[44:47], a[8:11], v[168:171], v[44:47]\n"
      "buffer_load_dwordx4 v1, s[40:43], s54 offen offset:2048 lds\n"
      "s_add_u32 m0, s53, 17280\n"
      "s_waitcnt lgkmcnt(4)\n"
      "v_mfma_f32_16x16x32_bf16 v[44:47], a[12:15], v[164:167], v[44:47]\n"
      "v_mfma_f32_16x16x32_bf16 v[112:115], a[16:19], v[52:55], 0\n"
      "buffer_load_dwordx4 v1, s[40:43], s54 offen offset:128 lds\n"
      "v_mfma_f32_16x16x32_bf16 v[112:115], a[20:23], v[128:131], v[112:115]\n"
      "v_mfma_f32_16x16x32_bf16 v[112:115], a[24:27], v[168:171], v[112:115]\n"
      "buffer_load_dwordx4 v1, s[40:43], s54 offen offset:2176 lds\n"
      "v_mfma_f32_16x16x32_bf16 v[112:115], a[28:31], v[164:167], v[112:115]\n"
      "v_mfma_f32_16x16x32_bf16 v[192:195], a[32:35], v[52:55], 0\n"
      "v_mfma_f32_16x16x32_bf16 v[192:195], a[36:39], v[128:131], v[192:195]\n"
      "v_cvt_pk_bf16_f32 v40, v44, v45\n"
      "v_cvt_pk_bf16_f32 v41, v46, v47\n"
      "v_mfma_f32_16x16x32_bf16 v[192:195], a[40:43], v[168:171], v[192:195]\n"
      "v_mfma_f32_16x16x32_bf16 v[192:195], a[44:47], v[164:167], v[192:195]\n"
      "v_mfma_f32_16x16x32_bf16 v[224:227], a[48:51], v[52:55], 0\n"
      "v_mfma_f32_16x16x32_bf16 v[224:227], a[52:55], v[128:131], v[224:227]\n"
      "v_cvt_pk_bf16_f32 v42, v112, v113\n"
      "v_cvt_pk_bf16_f32 v43, v114, v115\n"
      "v_mfma_f32_16x16x32_bf16 v[224:227], a[56:59], v[168:171], v[224:227]\n"
      "v_mfma_f32_16x16x32_bf16 v[224:227], a[60:63], v[164:167], v[224:227]\n"
      "v_mfma_f32_16x16x32_bf16 v[32:35], v[52:55], a[64:67], 0\n"
      "v_mfma_f32_16x16x32_bf16 v[32:35], v[128:131], a[68:71], v[32:35]\n"
      "v_cvt_pk_bf16_f32 v44, v192, v193\n"
      "v_cvt_pk_bf16_f32 v45, v194, v195\n"
      "v_mfma_f32_16x16x32_bf16 v[32:35], v[168:171], a[72:75], v[32:35]\n"
      "v_mfma_f32_16x16x32_bf16 v[32:35], v[164:167], a[76:79], v[32:35]\n"
      "v_mfma_f32_16x16x32_bf16 v[160:163], v[52:55], a[80:83], 0\n"
      "v_mfma_f32_16x16x32_bf16 v[160:163], v[128:131], a[84:87], v[160:163]\n"
      "v_cvt_pk_bf16_f32 v46, v224, v225\n"
      "v_cvt_pk_bf16_f32 v47, v226, v227\n"
      "v_mfma_f32_16x16x32_bf16 v[160:163], v[168:171], a[88:91], v[160:163]\n"
      "v_mfma_f32_16x16x32_bf16 v[160:163], v[164:167], a[92:95], v[160:163]\n"
      "v_mfma_f32_16x16x32_bf16 v[112:115], v[52:55], a[96:99], 0\n"
      "v_mfma_f32_16x16x32_bf16 v[112:115], v[128:131], a[100:103], v[112:115]\n"
      "v_cvt_pk_bf16_f32 v56, v32, v33\n"
      "v_cvt_pk_bf16_f32 v57, v34, v35\n"
      "v_mfma_f32_16x16x32_bf16 v[112:115], v[168:171], a[104:107], v[112:115]\n"
      "v_mfma_f32_16x16x32_bf16 v[112:115], v[164:167], a[108:111], v[112:115]\n"
      "v_mfma_f32_16x16x32_bf16 v[192:195], v[52:55], a[112:115], 0\n"
      "v_mfma_f32_16x16x32_bf16 v[192:195], v[128:131], a[116:119], v[192:195]\n"
      "v_cvt_pk_bf16_f32 v132, v160, v161\n"
      "v_cvt_pk_bf16_f32 v133, v162, v163\n"
      "v_mfma_f32_16x16x32_bf16 v[192:195], v[168:171], a[120:123], v[192:195]\n"
      "v_mfma_f32_16x16x32_bf16 v[192:195], v[164:167], a[124:127], v[192:195]\n"
      "s_waitcnt vmcnt(17)\n"
      "ds_read_b128 v[224:227], v0 offset:0\n"
      "ds_read_b128 v[32:35], v0 offset:512\n"
      "ds_read_b128 v[160:163], v0 offset:1024\n"
      "ds_read_b128 v[28:31], v0 offset:1536\n"
      "v_cvt_pk_bf16_f32 v104, v112, v113\n"
      "v_cvt_pk_bf16_f32 v105, v114, v115\n"
      "s_waitcnt lgkmcnt(7)\n"
      "v_mfma_f32_16x16x32_bf16 v[168:171], a[0:3], v[100:103], 0\n"
      "s_add_u32 m0, s53, 20480\n"
      "s_add_u32 s54, s49, 20480\n"
      "buffer_load_dwordx4 v1, s[40:43], s54 offen offset:0 lds\n"
      "v_cvt_pk_bf16_f32 v196, v192, v193\n"
      "v_cvt_pk_bf16_f32 v197, v194, v195\n"
      "s_waitcnt lgkmcnt(6)\n"
      "v_mfma_f32_16x16x32_bf16 v[168:171], a[4:7], v[108:111], v[168:171]\n"
      "s_waitcnt lgkmcnt(5)\n"
      "v_mfma_f32_16x16x32_bf16 v[168:171], a[8:11], v[176:179], v[168:171]\n"
      "buffer_load_dwordx4 v1, s[40:43], s54 offen offset:2048 lds\n"
      "s_add_u32 m0, s53, 21376\n"
      "s_waitcnt lgkmcnt(4)\n"
      "v_mfma_f32_16x16x32_bf16 v[168:171], a[12:15], v[172:175], v[168:171]\n"
      "v_mfma_f32_16x16x32_bf16 v[112:115], a[16:19], v[100:103], 0\n"
      "buffer_load_dwordx4 v1, s[40:43], s54 offen offset:128 lds\n"
      "v_mfma_f32_16x16x32_bf16 v[112:115], a[20:23], v[108:111], v[112:115]\n"
      "v_mfma_f32_16x16x32_bf16 v[112:115], a[24:27], v[176:179], v[112:115]\n"
      "buffer_load_dwordx4 v1, s[40:43], s54 offen offset:2176 lds\n"
      "v_mfma_f32_16x16x32_bf16 v[112:115], a[28:31], v[172:175], v[112:115]\n"
      "v_mfma_f32_16x16x32_bf16 v[192:195], a[32:35], v[100:103], 0\n"
      "v_mfma_f32_16x16x32_bf16 v[192:195], a[36:39], v[108:111], v[192:195]\n"
      "v_cvt_pk_bf16_f32 v164, v168, v169\n"
      "v_cvt_pk_bf16_f32 v165, v170, v171\n"
      "v_mfma_f32_16x16x32_bf16 v[192:195], a[40:43], v[176:179], v[192:195]\n"
      "v_mfma_f32_16x16x32_bf16 v[192:195], a[44:47], v[172:175], v[192:195]\n"
      "v_mfma_f32_16x16x32_bf16 v[52:55], a[48:51], v[100:103], 0\n"
      "v_mfma_f32_16x16x32_bf16 v[52:55], a[52:55], v[108:111], v[52:55]\n"
      "v_cvt_pk_bf16_f32 v166, v112, v113\n"
      "v_cvt_pk_bf16_f32 v167, v114, v115\n"
      "v_mfma_f32_16x16x32_bf16 v[52:55], a[56:59], v[176:179], v[52:55]\n"
      "v_mfma_f32_16x16x32_bf16 v[52:55], a[60:63], v[172:175], v[52:55]\n"
      "v_mfma_f32_16x16x32_bf16 v[112:115], v[100:103], a[64:67], 0\n"
      "v_mfma_f32_16x16x32_bf16 v[112:115], v[108:111], a[68:71], v[112:115]\n"
      "v_cvt_pk_bf16_f32 v168, v192, v193\n"
      "v_cvt_pk_bf16_f32 v169, v194, v195\n"
      "v_mfma_f32_16x16x32_bf16 v[112:115], v[176:179], a[72:75], v[112:115]\n"
      "v_mfma_f32_16x16x32_bf16 v[112:115], v[172:175], a[76:79], v[112:115]\n"
      "v_mfma_f32_16x16x32_bf16 v[192:195], v[100:103], a[80:83], 0\n"
      "v_mfma_f32_16x16x32_bf16 v[192:195], v[108:111], a[84:87], v[192:195]\n"
      "v_cvt_pk_bf16_f32 v170, v52, v53\n"
      "v_cvt_pk_bf16_f32 v171, v54, v55\n"
      "v_mfma_f32_16x16x32_bf16 v[192:195], v[176:179], a[88:91], v[192:195]\n"
      "v_mfma_f32_16x16x32_bf16 v[192:195], v[172:175], a[92:95], v[192:195]\n"
      "v_mfma_f32_16x16x32_bf16 v[88:91], v[100:103], a[96:99], 0\n"
      "v_mfma_f32_16x16x32_bf16 v[88:91], v[108:111], a[100:103], v[88:91]\n"
      "v_cvt_pk_bf16_f32 v54, v112, v113\n"
      "v_cvt_pk_bf16_f32 v55, v114, v115\n"
      "v_mfma_f32_16x16x32_bf16 v[88:91], v[176:179], a[104:107], v[88:91]\n"
      "v_mfma_f32_16x16x32_bf16 v[88:91], v[172:175], a[108:111], v[88:91]\n"
      "v_mfma_f32_16x16x32_bf16 v[112:115], v[100:103], a[112:115], 0\n"
      "v_mfma_f32_16x16x32_bf16 v[112:115], v[108:111], a[116:119], v[112:115]\n"
      "v_cvt_pk_bf16_f32 v130, v192, v193\n"
      "v_cvt_pk_bf16_f32 v131, v194, v195\n"
      "v_mfma_f32_16x16x32_bf16 v[112:115], v[176:179], a[120:123], v[112:115]\n"
      "v_mfma_f32_16x16x32_bf16 v[112:115], v[172:175], a[124:127], v[112:115]\n"
      "ds_read_b128 v[172:175], v9 offset:0\n"
      "ds_read_b128 v[176:179], v9 offset:2048\n"
      "v_cvt_pk_bf16_f32 v102, v88, v89\n"
      "v_cvt_pk_bf16_f32 v103, v90, v91\n"
      "s_waitcnt lgkmcnt(5)\n"
      "v_mfma_f32_16x16x32_bf16 v[88:91], a[0:3], v[224:227], 0\n"
      "s_waitcnt lgkmcnt(4)\n"
      "v_mfma_f32_16x16x32_bf16 v[88:91], a[4:7], v[32:35], v[88:91]\n"
      "s_waitcnt lgkmcnt(3)\n"
      "v_mfma_f32_16x16x32_bf16 v[88:91], a[8:11], v[160:163], v[88:91]\n"
      "s_waitcnt lgkmcnt(2)\n"
      "v_mfma_f32_16x16x32_bf16 v[88:91], a[12:15], v[28:31], v[88:91]\n"
      "v_cvt_pk_bf16_f32 v194, v112, v113\n"
      "v_cvt_pk_bf16_f32 v195, v114, v115\n"
      "v_mfma_f32_16x16x32_bf16 v[112:115], a[16:19], v[224:227], 0\n"
      "v_mfma_f32_16x16x32_bf16 v[112:115], a[20:23], v[32:35], v[112:115]\n"
      "v_mfma_f32_16x16x32_bf16 v[112:115], a[24:27], v[160:163], v[112:115]\n"
      "v_mfma_f32_16x16x32_bf16 v[112:115], a[28:31], v[28:31], v[112:115]\n"
      "v_mfma_f32_16x16x32_bf16 v[190:193], a[32:35], v[224:227], 0\n"
      "v_mfma_f32_16x16x32_bf16 v[190:193], a[36:39], v[32:35], v[190:193]\n"
      "v_cvt_pk_bf16_f32 v108, v88, v89\n"
      "v_cvt_pk_bf16_f32 v109, v90, v91\n"
      "v_mfma_f32_16x16x32_bf16 v[190:193], a[40:43], v[160:163], v[190:193]\n"
      "v_mfma_f32_16x16x32_bf16 v[190:193], a[44:47], v[28:31], v[190:193]\n"
      "v_mfma_f32_16x16x32_bf16 v[88:91], a[48:51], v[224:227], 0\n"
      "v_mfma_f32_16x16x32_bf16 v[88:91], a[52:55], v[32:35], v[88:91]\n"
      "v_cvt_pk_bf16_f32 v110, v112, v113\n"
      "v_cvt_pk_bf16_f32 v111, v114, v115\n"
      "v_mfma_f32_16x16x32_bf16 v[88:91], a[56:59], v[160:163], v[88:91]\n"
      "v_mfma_f32_16x16x32_bf16 v[88:91], a[60:63], v[28:31], v[88:91]\n"
      "v_add_u32 v9, 64, v9\n"
      "v_mfma_f32_16x16x32_bf16 v[228:231], v[224:227], a[64:67], 0\n"
      "v_cvt_pk_bf16_f32 v112, v190, v191\n"
      "v_cvt_pk_bf16_f32 v113, v192, v193\n"
      "v_mfma_f32_16x16x32_bf16 v[228:231], v[32:35], a[68:71], v[228:231]\n"
      "v_mfma_f32_16x16x32_bf16 v[228:231], v[160:163], a[72:75], v[228:231]\n"
      "v_mfma_f32_16x16x32_bf16 v[228:231], v[28:31], a[76:79], v[228:231]\n"
      "v_mfma_f32_16x16x32_bf16 v[190:193], v[224:227], a[80:83], 0\n"
      "v_cvt_pk_bf16_f32 v114, v88, v89\n"
      "v_cvt_pk_bf16_f32 v115, v90, v91\n"
      "v_mfma_f32_16x16x32_bf16 v[190:193], v[32:35], a[84:87], v[190:193]\n"
      "v_mfma_f32_16x16x32_bf16 v[190:193], v[160:163], a[88:91], v[190:193]\n"
      "v_mfma_f32_16x16x32_bf16 v[190:193], v[28:31], a[92:95], v[190:193]\n"
      "v_mfma_f32_16x16x32_bf16 v[88:91], v[224:227], a[96:99], 0\n"
      "v_mfma_f32_16x16x32_bf16 v[88:91], v[32:35], a[100:103], v[88:91]\n"
      "v_cvt_pk_bf16_f32 v52, v228, v229\n"
      "v_cvt_pk_bf16_f32 v53, v230, v231\n"
      "v_mfma_f32_16x16x32_bf16 v[88:91], v[160:163], a[104:107], v[88:91]\n"
      "v_mfma_f32_16x16x32_bf16 v[88:91], v[28:31], a[108:111], v[88:91]\n"
      "v_mfma_f32_16x16x32_bf16 v[228:231], v[224:227], a[112:115], 0\n"
      "v_mfma_f32_16x16x32_bf16 v[228:231], v[32:35], a[116:119], v[228:231]\n"
      "v_cvt_pk_bf16_f32 v128, v190, v191\n"
      "v_cvt_pk_bf16_f32 v129, v192, v193\n"
      "v_mfma_f32_16x16x32_bf16 v[228:231], v[160:163], a[120:123], v[228:231]\n"
      "v_mfma_f32_16x16x32_bf16 v[228:231], v[28:31], a[124:127], v[228:231]\n"
      "ds_read_b64 v[18:19], v11 offset:0\n"
      "v_mfma_f32_16x16x32_bf16 v[28:31], a[128:131], v[24:27], 0\n"
      "v_cvt_pk_bf16_f32 v100, v88, v89\n"
      "v_cvt_pk_bf16_f32 v101, v90, v91\n"
      "v_mfma_f32_16x16x32_bf16 v[28:31], a[132:135], v[152:155], v[28:31]\n"
      "v_mfma_f32_16x16x32_bf16 v[28:31], a[136:139], v[20:23], v[28:31]\n"
      "v_mfma_f32_16x16x32_bf16 v[28:31], a[140:143], v[156:159], v[28:31]\n"
      "v_mfma_f32_16x16x32_bf16 v[88:91], a[144:147], v[24:27], 0\n"
      "v_cvt_pk_bf16_f32 v192, v228, v229\n"
      "v_cvt_pk_bf16_f32 v193, v230, v231\n"
      "v_mfma_f32_16x16x32_bf16 v[88:91], a[148:151], v[152:155], v[88:91]\n"
      "v_mfma_f32_16x16x32_bf16 v[88:91], a[152:155], v[20:23], v[88:91]\n"
      "v_mfma_f32_16x16x32_bf16 v[88:91], a[156:159], v[156:159], v[88:91]\n"
      "v_mfma_f32_16x16x32_bf16 v[228:231], a[160:163], v[24:27], 0\n"
      "v_mfma_f32_16x16x32_bf16 v[228:231], a[164:167], v[152:155], v[228:231]\n"
      "v_cvt_pk_bf16_f32 v160, v28, v29\n"
      "v_cvt_pk_bf16_f32 v161, v30, v31\n"
      "v_mfma_f32_16x16x32_bf16 v[228:231], a[168:171], v[20:23], v[228:231]\n"
      "v_mfma_f32_16x16x32_bf16 v[228:231], a[172:175], v[156:159], v[228:231]\n"
      "v_mfma_f32_16x16x32_bf16 v[32:35], a[176:179], v[24:27], 0\n"
      "v_mfma_f32_16x16x32_bf16 v[32:35], a[180:183], v[152:155], v[32:35]\n"
      "v_cvt_pk_bf16_f32 v162, v88, v89\n"
      "v_cvt_pk_bf16_f32 v163, v90, v91\n"
      "v_mfma_f32_16x16x32_bf16 v[32:35], a[184:187], v[20:23], v[32:35]\n"
      "v_mfma_f32_16x16x32_bf16 v[32:35], a[188:191], v[156:159], v[32:35]\n"
      "s_waitcnt lgkmcnt(2)\n"
      "v_mfma_f32_16x16x32_bf16 v[28:31], v[172:175], v[160:163], 0\n"
      "v_cvt_pk_bf16_f32 v88, v228, v229\n"
      "v_cvt_pk_bf16_f32 v89, v230, v231\n"
      "s_nop 3\n"
      "v_cvt_pk_bf16_f32 v90, v32, v33\n"
      "v_cvt_pk_bf16_f32 v91, v34, v35\n"
      "s_waitcnt lgkmcnt(1)\n"
      "s_nop 0\n"
      "v_mfma_f32_16x16x32_bf16 v[28:31], v[176:179], v[88:91], v[28:31]\n"
      "v_mfma_f32_16x16x32_bf16 v[228:231], v[108:111], v[160:163], 0\n"
      "v_mfma_f32_16x16x32_bf16 v[228:231], v[112:115], v[88:91], v[228:231]\n"
      "v_mfma_f32_16x16x32_bf16 v[32:35], v[164:167], v[160:163], 0\n"
      "v_mfma_f32_16x16x32_bf16 v[32:35], v[168:171], v[88:91], v[32:35]\n"
      "v_mfma_f32_16x16x32_bf16 v[224:227], v[40:43], v[160:163], 0\n"
      "v_mfma_f32_16x16x32_bf16 v[224:227], v[44:47], v[88:91], v[224:227]\n"
      "v_mfma_f32_16x16x32_bf16 v[200:203], v[92:95], v[160:163], 0\n"
      "v_mfma_f32_16x16x32_bf16 v[200:203], v[96:99], v[88:91], v[200:203]\n"
      "v_max_f32 v190, v28, v29\n"
      "v_max3_f32 v190, v190, v30, v31\n"
      "v_max3_f32 v190, v190, v228, v229\n"
      "v_max3_f32 v190, v190, v230, v231\n"
      "v_max3_f32 v190, v190, v32, v33\n"
      "v_mfma_f32_16x16x32_bf16 v[180:183], v[216:219], v[160:163], 0\n"
      "v_max3_f32 v190, v190, v34, v35\n"
      "v_max3_f32 v190, v190, v224, v225\n"
      "v_max3_f32 v190, v190, v226, v227\n"
      "v_max3_f32 v190, v190, v200, v201\n"
      "v_max3_f32 v190, v190, v202, v203\n"
      "v_mfma_f32_16x16x32_bf16 v[180:183], v[220:223], v[88:91], v[180:183]\n"
      "v_mfma_f32_16x16x32_bf16 v[208:211], v[244:247], v[160:163], 0\n"
      "v_mfma_f32_16x16x32_bf16 v[208:211], v[248:251], v[88:91], v[208:211]\n"
      "v_mfma_f32_16x16x32_bf16 v[184:187], a[128:131], v[252:255], 0\n"
      "v_mfma_f32_16x16x32_bf16 v[184:187], a[132:135], v[48:51], v[184:187]\n"
      "v_mfma_f32_16x16x32_bf16 v[184:187], a[136:139], v[124:127], v[184:187]\n"
      "v_mfma_f32_16x16x32_bf16 v[184:187], a[140:143], v[116:119], v[184:187]\n"
      "v_mfma_f32_16x16x32_bf16 v[88:91], a[144:147], v[252:255], 0\n"
      "v_mfma_f32_16x16x32_bf16 v[88:91], a[148:151], v[48:51], v[88:91]\n"
      "v_max3_f32 v190, v190, v180, v181\n"
      "v_max3_f32 v190, v190, v182, v183\n"
      "v_max3_f32 v190, v190, v208, v209\n"
      "v_max3_f32 v190, v190, v210, v211\n"
      "v_mov_b32 v162, v190\n"
      "v_mfma_f32_16x16x32_bf16 v[88:91], a[152:155], v[124:127], v[88:91]\n"
      "v_cvt_pk_bf16_f32 v160, v184, v185\n"
      "v_permlane16_swap_b32 v190, v162\n"
      "v_max_f32 v190, v190, v162\n"
      "v_mov_b32 v162, v190\n"
      "v_cvt_pk_bf16_f32 v161, v186, v187\n"
      "v_mfma_f32_16x16x32_bf16 v[88:91], a[156:159], v[116:119], v[88:91]\n"
      "v_permlane32_swap_b32 v190, v162\n"
      "v_max_f32 v190, v190, v162\n"
      "v_sub_f32 v28, v28, v190\n"
      "v_sub_f32 v29, v29, v190\n"
      "v_sub_f32 v30, v30, v190\n"
      "v_sub_f32 v31, v31, v190\n"
      "v_exp_f32 v28, v28\n"
      "v_mfma_f32_16x16x32_bf16 v[184:187], a[160:163], v[252:255], 0\n"
      "v_exp_f32 v29, v29\n"
      "v_exp_f32 v30, v30\n"
      "v_exp_f32 v31, v31\n"
      "v_cvt_pk_bf16_f32 v162, v28, v29\n"
      "v_cvt_pk_bf16_f32 v163, v30, v31\n"
      "v_mfma_f32_16x16x32_bf16 v[184:187], a[164:167], v[48:51], v[184:187]\n"
      "v_sub_f32 v228, v228, v190\n"
      "v_sub_f32 v229, v229, v190\n"
      "v_sub_f32 v230, v230, v190\n"
      "v_sub_f32 v231, v231, v190\n"
      "v_sub_f32 v32, v32, v190\n"
      "v_sub_f32 v33, v33, v190\n"
      "v_sub_f32 v34, v34, v190\n"
      "v_sub_f32 v35, v35, v190\n"
      "v_mfma_f32_16x16x32_bf16 v[184:187], a[168:171], v[124:127], v[184:187]\n"
      "v_sub_f32 v224, v224, v190\n"
      "v_sub_f32 v225, v225, v190\n"
      "v_sub_f32 v226, v226, v190\n"
      "v_sub_f32 v227, v227, v190\n"
      "v_sub_f32 v200, v200, v190\n"
      "v_sub_f32 v201, v201, v190\n"
      "v_sub_f32 v202, v202, v190\n"
      "v_sub_f32 v203, v203, v190\n"
      "v_mfma_f32_16x16x32_bf16 v[184:187], a[172:175], v[116:119], v[184:187]\n"
      "v_add_f32 v188, v28, v29\n"
      "v_add_f32 v189, v30, v31\n"
      "v_exp_f32 v228, v228\n"
      "v_exp_f32 v229, v229\n"
      "v_exp_f32 v230, v230\n"
      "v_cvt_pk_bf16_f32 v28, v228, v229\n"
      "v_mfma_f32_16x16x32_bf16 v[84:87], a[176:179], v[252:255], 0\n"
      "v_exp_f32 v231, v231\n"
      "v_exp_f32 v32, v32\n"
      "v_cvt_pk_bf16_f32 v29, v230, v231\n"
      "v_exp_f32 v33, v33\n"
      "v_mfma_f32_16x16x32_bf16 v[84:87], a[180:183], v[48:51], v[84:87]\n"
      "v_exp_f32 v34, v34\n"
      "v_exp_f32 v35, v35\n"
      "v_cvt_pk_bf16_f32 v30, v32, v33\n"
      "v_cvt_pk_bf16_f32 v31, v34, v35\n"
      "v_exp_f32 v224, v224\n"
      "v_sub_f32 v180, v180, v190\n"
      "v_mfma_f32_16x16x32_bf16 v[84:87], a[184:187], v[124:127], v[84:87]\n"
      "v_exp_f32 v225, v225\n"
      "v_exp_f32 v226, v226\n"
      "v_exp_f32 v227, v227\n"
      "v_cvt_pk_bf16_f32 v32, v224, v225\n"
      "v_cvt_pk_bf16_f32 v33, v226, v227\n"
      "v_mfma_f32_16x16x32_bf16 v[84:87], a[188:191], v[116:119], v[84:87]\n"
      "v_exp_f32 v200, v200\n"
      "v_exp_f32 v201, v201\n"
      "v_exp_f32 v202, v202\n"
      "v_exp_f32 v203, v203\n"
      "s_waitcnt lgkmcnt(0)\n"
      "v_mfma_f32_16x16x4_4b_bf16 v[224:239], v[162:163], v[18:19], 0 cbsz:2 abid:0\n"
      "v_cvt_pk_bf16_f32 v34, v200, v201\n"
      "v_cvt_pk_bf16_f32 v35, v202, v203\n"
      "v_sub_f32 v181, v181, v190\n"
      "v_sub_f32 v182, v182, v190\n"
      "v_sub_f32 v183, v183, v190\n"
      "v_sub_f32 v208, v208, v190\n"
      "v_sub_f32 v209, v209, v190\n"
      "v_sub_f32 v210, v210, v190\n"
      "v_sub_f32 v211, v211, v190\n"
      "v_exp_f32 v180, v180\n"
      "v_exp_f32 v181, v181\n"
      "v_exp_f32 v182, v182\n"
      "v_exp_f32 v183, v183\n"
      "v_cvt_pk_bf16_f32 v200, v180, v181\n"
      "v_cvt_pk_bf16_f32 v201, v182, v183\n"
      "v_exp_f32 v208, v208\n"
      "v_exp_f32 v209, v209\n"
      "v_smfmac_f32_16x16x64_bf16 v[188:191], v[12:15], v[28:35], v5\n"
      "v_exp_f32 v210, v210\n"
      "v_exp_f32 v211, v211\n"
      "v_mfma_f32_16x16x32_bf16 v[224:227], v[28:31], v[52:55], v[224:227]\n"
      "v_cvt_pk_bf16_f32 v202, v208, v209\n"
      "v_cvt_pk_bf16_f32 v203, v210, v211\n"
      "v_cvt_pk_bf16_f32 v162, v88, v89\n"
      "v_cvt_pk_bf16_f32 v163, v90, v91\n"
      "v_mfma_f32_16x16x32_bf16 v[224:227], v[32:35], v[56:59], v[224:227]\n"
      "v_cvt_pk_bf16_f32 v88, v184, v185\n"
      "v_cvt_pk_bf16_f32 v89, v186, v187\n"
      "v_cvt_pk_bf16_f32 v90, v84, v85\n"
      "v_mfma_f32_16x16x32_bf16 v[224:227], v[200:203], v[120:123], v[224:227]\n"
      "v_cvt_pk_bf16_f32 v91, v86, v87\n"
      "v_smfmac_f32_16x16x32_bf16 v[188:191], v[12:13], v[200:203], v5\n"
      "v_mfma_f32_16x16x32_bf16 v[228:231], v[28:31], v[128:131], v[228:231]\n"
      "v_mfma_f32_16x16x32_bf16 v[228:231], v[32:35], v[132:135], v[228:231]\n"
      "v_mfma_f32_16x16x32_bf16 v[228:231], v[200:203], v[68:71], v[228:231]\n"
      "v_mfma_f32_16x16x32_bf16 v[232:235], v[28:31], v[100:103], v[232:235]\n"
      "v_mfma_f32_16x16x32_bf16 v[232:235], v[32:35], v[104:107], v[232:235]\n"
      "v_mfma_f32_16x16x32_bf16 v[232:235], v[200:203], v[36:39], v[232:235]\n"
      "v_mfma_f32_16x16x32_bf16 v[236:239], v[28:31], v[192:195], v[236:239]\n"
      "v_mfma_f32_16x16x32_bf16 v[236:239], v[32:35], v[196:199], v[236:239]\n"
      "v_add_f32 v188, v188, v189\n"
      "v_rcp_f32 v189, v188\n"
      "v_mfma_f32_16x16x32_bf16 v[236:239], v[200:203], v[240:243], v[236:239]\n"
      "ds_bpermute_b32 v190, v4, v189\n"
      "s_waitcnt lgkmcnt(0)\n"
      "v_mul_f32_dpp v224, v190, v224 row_newbcast:0 row_mask:0xf bank_mask:0xf\n"
      "v_mul_f32_dpp v225, v190, v225 row_newbcast:4 row_mask:0xf bank_mask:0xf\n"
      "v_mul_f32_dpp v226, v190, v226 row_newbcast:8 row_mask:0xf bank_mask:0xf\n"
      "v_mul_f32_dpp v227, v190, v227 row_newbcast:12 row_mask:0xf bank_mask:0xf\n"
      "v_mul_f32_dpp v228, v190, v228 row_newbcast:0 row_mask:0xf bank_mask:0xf\n"
      "v_mul_f32_dpp v229, v190, v229 row_newbcast:4 row_mask:0xf bank_mask:0xf\n"
      "v_mfma_f32_16x16x32_bf16 v[224:227], v[24:27], a[192:195], v[224:227]\n"
      "v_mul_f32_dpp v230, v190, v230 row_newbcast:8 row_mask:0xf bank_mask:0xf\n"
      "v_mul_f32_dpp v231, v190, v231 row_newbcast:12 row_mask:0xf bank_mask:0xf\n"
      "v_mfma_f32_16x16x32_bf16 v[224:227], v[152:155], a[196:199], v[224:227]\n"
      "v_mul_f32_dpp v232, v190, v232 row_newbcast:0 row_mask:0xf bank_mask:0xf\n"
      "v_mul_f32_dpp v233, v190, v233 row_newbcast:4 row_mask:0xf bank_mask:0xf\n"
      "v_mul_f32_dpp v234, v190, v234 row_newbcast:8 row_mask:0xf bank_mask:0xf\n"
      "v_mfma_f32_16x16x32_bf16 v[224:227], v[20:23], a[200:203], v[224:227]\n"
      "v_mul_f32_dpp v235, v190, v235 row_newbcast:12 row_mask:0xf bank_mask:0xf\n"
      "v_mul_f32_dpp v236, v190, v236 row_newbcast:0 row_mask:0xf bank_mask:0xf\n"
      "v_mul_f32_dpp v237, v190, v237 row_newbcast:4 row_mask:0xf bank_mask:0xf\n"
      "v_mfma_f32_16x16x32_bf16 v[224:227], v[156:159], a[204:207], v[224:227]\n"
      "v_mul_f32_dpp v238, v190, v238 row_newbcast:8 row_mask:0xf bank_mask:0xf\n"
      "v_mul_f32_dpp v239, v190, v239 row_newbcast:12 row_mask:0xf bank_mask:0xf\n"
      "v_mfma_f32_16x16x32_bf16 v[228:231], v[24:27], a[208:211], v[228:231]\n"
      "v_mfma_f32_16x16x32_bf16 v[228:231], v[152:155], a[212:215], v[228:231]\n"
      "v_mfma_f32_16x16x32_bf16 v[228:231], v[20:23], a[216:219], v[228:231]\n"
      "v_mfma_f32_16x16x32_bf16 v[228:231], v[156:159], a[220:223], v[228:231]\n"
      "v_mfma_f32_16x16x32_bf16 v[232:235], v[24:27], a[224:227], v[232:235]\n"
      "v_mfma_f32_16x16x32_bf16 v[232:235], v[152:155], a[228:231], v[232:235]\n"
      "v_mfma_f32_16x16x32_bf16 v[232:235], v[20:23], a[232:235], v[232:235]\n"
      "v_mfma_f32_16x16x32_bf16 v[232:235], v[156:159], a[236:239], v[232:235]\n"
      "v_mfma_f32_16x16x32_bf16 v[236:239], v[24:27], a[240:243], v[236:239]\n"
      "v_mfma_f32_16x16x32_bf16 v[236:239], v[152:155], a[244:247], v[236:239]\n"
      "v_mfma_f32_16x16x32_bf16 v[236:239], v[20:23], a[248:251], v[236:239]\n"
      "v_mfma_f32_16x16x32_bf16 v[236:239], v[156:159], a[252:255], v[236:239]\n"
      "v_cvt_pk_bf16_f32 v224, v224, v228\n"
      "v_cvt_pk_bf16_f32 v228, v225, v229\n"
      "v_pk_max_i16 v224, v224, 0\n"
      "v_pk_max_i16 v228, v228, 0\n"
      "v_mfma_f32_16x16x32_bf16 v[20:23], v[172:175], v[160:163], 0\n"
      "v_cvt_pk_bf16_f32 v226, v226, v230\n"
      "v_cvt_pk_bf16_f32 v230, v227, v231\n"
      "v_pk_max_i16 v226, v226, 0\n"
      "v_cvt_pk_bf16_f32 v225, v232, v236\n"
      "v_mfma_f32_16x16x32_bf16 v[20:23], v[176:179], v[88:91], v[20:23]\n"
      "v_cvt_pk_bf16_f32 v229, v233, v237\n"
      "v_pk_max_i16 v225, v225, 0\n"
      "v_pk_max_i16 v229, v229, 0\n"
      "v_cvt_pk_bf16_f32 v227, v234, v238\n"
      "v_mfma_f32_16x16x32_bf16 v[24:27], v[108:111], v[160:163], 0\n"
      "v_cvt_pk_bf16_f32 v231, v235, v239\n"
      "v_pk_max_i16 v227, v227, 0\n"
      "v_pk_max_i16 v230, v230, 0\n"
      "v_max_f32 v186, v20, v21\n"
      "v_mfma_f32_16x16x32_bf16 v[24:27], v[112:115], v[88:91], v[24:27]\n"
      "v_max3_f32 v186, v186, v22, v23\n"
      "v_pk_max_i16 v231, v231, 0\n"
      "v_mfma_f32_16x16x32_bf16 v[28:31], v[164:167], v[160:163], 0\n"
      "v_mfma_f32_16x16x32_bf16 v[28:31], v[168:171], v[88:91], v[28:31]\n"
      "v_mfma_f32_16x16x32_bf16 v[236:239], v[40:43], v[160:163], 0\n"
      "v_mfma_f32_16x16x32_bf16 v[236:239], v[44:47], v[88:91], v[236:239]\n"
      "v_mfma_f32_16x16x32_bf16 v[232:235], v[92:95], v[160:163], 0\n"
      "v_mfma_f32_16x16x32_bf16 v[232:235], v[96:99], v[88:91], v[232:235]\n"
      "v_max3_f32 v186, v186, v24, v25\n"
      "v_max3_f32 v186, v186, v26, v27\n"
      "v_mfma_f32_16x16x32_bf16 v[156:159], v[216:219], v[160:163], 0\n"
      "v_mfma_f32_16x16x32_bf16 v[156:159], v[220:223], v[88:91], v[156:159]\n"
      "v_max3_f32 v186, v186, v28, v29\n"
      "v_max3_f32 v186, v186, v30, v31\n"
      "v_max3_f32 v186, v186, v236, v237\n"
      "v_max3_f32 v186, v186, v238, v239\n"
      "v_max3_f32 v186, v186, v232, v233\n"
      "v_max3_f32 v186, v186, v234, v235\n"
      "v_mfma_f32_16x16x32_bf16 v[152:155], v[244:247], v[160:163], 0\n"
      "v_mfma_f32_16x16x32_bf16 v[152:155], v[248:251], v[88:91], v[152:155]\n"
      "v_max3_f32 v186, v186, v156, v157\n"
      "v_max3_f32 v186, v186, v158, v159\n"
      "v_mfma_f32_16x16x32_bf16 v[160:163], a[128:131], v[148:151], 0\n"
      "v_mfma_f32_16x16x32_bf16 v[160:163], a[132:135], v[136:139], v[160:163]\n"
      "s_add_u32 s56, s48, 20480\n"
      "buffer_store_dwordx2 v[224:225], v2, s[44:47], s56 offen offset:0 nt sc1\n"
      "buffer_store_dwordx2 v[228:229], v2, s[44:47], s56 offen offset:256 nt sc1\n"
      "v_mfma_f32_16x16x32_bf16 v[160:163], a[136:139], v[140:143], v[160:163]\n"
      "v_max3_f32 v186, v186, v152, v153\n"
      "v_max3_f32 v186, v186, v154, v155\n"
      "v_mov_b32 v88, v186\n"
      "buffer_store_dwordx2 v[226:227], v2, s[44:47], s56 offen offset:512 nt sc1\n"
      "v_mfma_f32_16x16x32_bf16 v[160:163], a[140:143], v[144:147], v[160:163]\n"
      "v_permlane16_swap_b32 v186, v88\n"
      "v_max_f32 v186, v186, v88\n"
      "v_mfma_f32_16x16x32_bf16 v[32:35], a[144:147], v[148:151], 0\n"
      "v_mov_b32 v88, v186\n"
      "buffer_store_dwordx2 v[230:231], v2, s[44:47], s56 offen offset:768 nt sc1\n"
      "v_mfma_f32_16x16x32_bf16 v[32:35], a[148:151], v[136:139], v[32:35]\n"
      "v_permlane32_swap_b32 v186, v88\n"
      "v_max_f32 v186, v186, v88\n"
      "v_sub_f32 v20, v20, v186\n"
      "v_sub_f32 v21, v21, v186\n"
      "v_sub_f32 v22, v22, v186\n"
      "v_sub_f32 v23, v23, v186\n"
      "v_sub_f32 v24, v24, v186\n"
      "v_mfma_f32_16x16x32_bf16 v[32:35], a[152:155], v[140:143], v[32:35]\n"
      "v_exp_f32 v20, v20\n"
      "v_exp_f32 v21, v21\n"
      "v_exp_f32 v22, v22\n"
      "v_cvt_pk_bf16_f32 v190, v20, v21\n"
      "v_mfma_f32_16x16x32_bf16 v[32:35], a[156:159], v[144:147], v[32:35]\n"
      "v_exp_f32 v23, v23\n"
      "v_sub_f32 v25, v25, v186\n"
      "v_cvt_pk_bf16_f32 v191, v22, v23\n"
      "v_sub_f32 v26, v26, v186\n"
      "v_sub_f32 v27, v27, v186\n"
      "v_sub_f32 v28, v28, v186\n"
      "v_mfma_f32_16x16x32_bf16 v[88:91], a[160:163], v[148:151], 0\n"
      "v_sub_f32 v29, v29, v186\n"
      "v_sub_f32 v30, v30, v186\n"
      "v_sub_f32 v31, v31, v186\n"
      "v_sub_f32 v236, v236, v186\n"
      "v_sub_f32 v237, v237, v186\n"
      "v_sub_f32 v238, v238, v186\n"
      "v_sub_f32 v239, v239, v186\n"
      "v_mfma_f32_16x16x32_bf16 v[88:91], a[164:167], v[136:139], v[88:91]\n"
      "v_sub_f32 v232, v232, v186\n"
      "v_sub_f32 v233, v233, v186\n"
      "v_sub_f32 v234, v234, v186\n"
      "v_sub_f32 v235, v235, v186\n"
      "v_add_f32 v184, v20, v21\n"
      "v_add_f32 v185, v22, v23\n"
      "v_mfma_f32_16x16x32_bf16 v[88:91], a[168:171], v[140:143], v[88:91]\n"
      "v_exp_f32 v24, v24\n"
      "v_exp_f32 v25, v25\n"
      "v_exp_f32 v26, v26\n"
      "v_exp_f32 v27, v27\n"
      "v_mfma_f32_16x16x32_bf16 v[88:91], a[172:175], v[144:147], v[88:91]\n"
      "v_cvt_pk_bf16_f32 v20, v24, v25\n"
      "v_cvt_pk_bf16_f32 v21, v26, v27\n"
      "v_exp_f32 v28, v28\n"
      "v_exp_f32 v29, v29\n"
      "v_mfma_f32_16x16x32_bf16 v[208:211], a[176:179], v[148:151], 0\n"
      "v_exp_f32 v30, v30\n"
      "v_exp_f32 v31, v31\n"
      "v_cvt_pk_bf16_f32 v22, v28, v29\n"
      "v_cvt_pk_bf16_f32 v23, v30, v31\n"
      "v_exp_f32 v236, v236\n"
      "v_mfma_f32_16x16x32_bf16 v[208:211], a[180:183], v[136:139], v[208:211]\n"
      "v_exp_f32 v237, v237\n"
      "v_exp_f32 v238, v238\n"
      "v_exp_f32 v239, v239\n"
      "v_cvt_pk_bf16_f32 v24, v236, v237\n"
      "v_mfma_f32_16x16x32_bf16 v[208:211], a[184:187], v[140:143], v[208:211]\n"
      "v_cvt_pk_bf16_f32 v25, v238, v239\n"
      "v_exp_f32 v232, v232\n"
      "v_exp_f32 v233, v233\n"
      "v_exp_f32 v234, v234\n"
      "v_mfma_f32_16x16x32_bf16 v[208:211], a[188:191], v[144:147], v[208:211]\n"
      "v_exp_f32 v235, v235\n"
      "v_cvt_pk_bf16_f32 v26, v232, v233\n"
      "v_cvt_pk_bf16_f32 v27, v234, v235\n"
      "v_sub_f32 v156, v156, v186\n"
      "v_sub_f32 v157, v157, v186\n"
      "v_sub_f32 v158, v158, v186\n"
      "v_mfma_f32_16x16x4_4b_bf16 v[224:239], v[190:191], v[18:19], 0 cbsz:2 abid:0\n"
      "v_sub_f32 v159, v159, v186\n"
      "v_sub_f32 v152, v152, v186\n"
      "v_sub_f32 v153, v153, v186\n"
      "v_sub_f32 v154, v154, v186\n"
      "v_sub_f32 v155, v155, v186\n"
      "v_exp_f32 v156, v156\n"
      "v_exp_f32 v157, v157\n"
      "v_exp_f32 v158, v158\n"
      "v_exp_f32 v159, v159\n"
      "v_cvt_pk_bf16_f32 v28, v156, v157\n"
      "v_cvt_pk_bf16_f32 v29, v158, v159\n"
      "v_exp_f32 v152, v152\n"
      "v_exp_f32 v153, v153\n"
      "v_cvt_pk_bf16_f32 v156, v160, v161\n"
      "v_smfmac_f32_16x16x64_bf16 v[184:187], v[12:15], v[20:27], v5\n"
      "v_exp_f32 v154, v154\n"
      "v_exp_f32 v155, v155\n"
      "v_mfma_f32_16x16x32_bf16 v[224:227], v[20:23], v[52:55], v[224:227]\n"
      "v_cvt_pk_bf16_f32 v30, v152, v153\n"
      "v_cvt_pk_bf16_f32 v31, v154, v155\n"
      "v_cvt_pk_bf16_f32 v157, v162, v163\n"
      "v_mfma_f32_16x16x32_bf16 v[224:227], v[24:27], v[56:59], v[224:227]\n"
      "v_cvt_pk_bf16_f32 v158, v32, v33\n"
      "v_cvt_pk_bf16_f32 v159, v34, v35\n"
      "v_cvt_pk_bf16_f32 v32, v88, v89\n"
      "v_mfma_f32_16x16x32_bf16 v[224:227], v[28:31], v[120:123], v[224:227]\n"
      "v_cvt_pk_bf16_f32 v33, v90, v91\n"
      "v_cvt_pk_bf16_f32 v34, v208, v209\n"
      "v_cvt_pk_bf16_f32 v35, v210, v211\n"
      "v_smfmac_f32_16x16x32_bf16 v[184:187], v[12:13], v[28:31], v5\n"
      "v_mfma_f32_16x16x32_bf16 v[228:231], v[20:23], v[128:131], v[228:231]\n"
      "v_mfma_f32_16x16x32_bf16 v[228:231], v[24:27], v[132:135], v[228:231]\n"
      "v_mfma_f32_16x16x32_bf16 v[228:231], v[28:31], v[68:71], v[228:231]\n"
      "v_mfma_f32_16x16x32_bf16 v[232:235], v[20:23], v[100:103], v[232:235]\n"
      "v_mfma_f32_16x16x32_bf16 v[232:235], v[24:27], v[104:107], v[232:235]\n"
      "v_mfma_f32_16x16x32_bf16 v[232:235], v[28:31], v[36:39], v[232:235]\n"
      "v_mfma_f32_16x16x32_bf16 v[236:239], v[20:23], v[192:195], v[236:239]\n"
      "v_mfma_f32_16x16x32_bf16 v[236:239], v[24:27], v[196:199], v[236:239]\n"
      "v_add_f32 v184, v184, v185\n"
      "v_rcp_f32 v185, v184\n"
      "v_mfma_f32_16x16x32_bf16 v[236:239], v[28:31], v[240:243], v[236:239]\n"
      "ds_bpermute_b32 v186, v4, v185\n"
      "s_waitcnt lgkmcnt(0)\n"
      "v_mul_f32_dpp v224, v186, v224 row_newbcast:0 row_mask:0xf bank_mask:0xf\n"
      "v_mul_f32_dpp v225, v186, v225 row_newbcast:4 row_mask:0xf bank_mask:0xf\n"
      "v_mul_f32_dpp v226, v186, v226 row_newbcast:8 row_mask:0xf bank_mask:0xf\n"
      "v_mul_f32_dpp v227, v186, v227 row_newbcast:12 row_mask:0xf bank_mask:0xf\n"
      "v_mul_f32_dpp v228, v186, v228 row_newbcast:0 row_mask:0xf bank_mask:0xf\n"
      "v_mul_f32_dpp v229, v186, v229 row_newbcast:4 row_mask:0xf bank_mask:0xf\n"
      "v_mfma_f32_16x16x32_bf16 v[224:227], v[252:255], a[192:195], v[224:227]\n"
      "v_mul_f32_dpp v230, v186, v230 row_newbcast:8 row_mask:0xf bank_mask:0xf\n"
      "v_mul_f32_dpp v231, v186, v231 row_newbcast:12 row_mask:0xf bank_mask:0xf\n"
      "v_mfma_f32_16x16x32_bf16 v[224:227], v[48:51], a[196:199], v[224:227]\n"
      "v_mul_f32_dpp v232, v186, v232 row_newbcast:0 row_mask:0xf bank_mask:0xf\n"
      "v_mul_f32_dpp v233, v186, v233 row_newbcast:4 row_mask:0xf bank_mask:0xf\n"
      "v_mul_f32_dpp v234, v186, v234 row_newbcast:8 row_mask:0xf bank_mask:0xf\n"
      "v_mfma_f32_16x16x32_bf16 v[224:227], v[124:127], a[200:203], v[224:227]\n"
      "v_mul_f32_dpp v235, v186, v235 row_newbcast:12 row_mask:0xf bank_mask:0xf\n"
      "v_mul_f32_dpp v236, v186, v236 row_newbcast:0 row_mask:0xf bank_mask:0xf\n"
      "v_mul_f32_dpp v237, v186, v237 row_newbcast:4 row_mask:0xf bank_mask:0xf\n"
      "v_mul_f32_dpp v238, v186, v238 row_newbcast:8 row_mask:0xf bank_mask:0xf\n"
      "v_mfma_f32_16x16x32_bf16 v[224:227], v[116:119], a[204:207], v[224:227]\n"
      "v_mul_f32_dpp v239, v186, v239 row_newbcast:12 row_mask:0xf bank_mask:0xf\n"
      "v_mfma_f32_16x16x32_bf16 v[228:231], v[252:255], a[208:211], v[228:231]\n"
      "v_mfma_f32_16x16x32_bf16 v[228:231], v[48:51], a[212:215], v[228:231]\n"
      "v_mfma_f32_16x16x32_bf16 v[228:231], v[124:127], a[216:219], v[228:231]\n"
      "v_mfma_f32_16x16x32_bf16 v[228:231], v[116:119], a[220:223], v[228:231]\n"
      "v_mfma_f32_16x16x32_bf16 v[232:235], v[252:255], a[224:227], v[232:235]\n"
      "v_mfma_f32_16x16x32_bf16 v[232:235], v[48:51], a[228:231], v[232:235]\n"
      "v_mfma_f32_16x16x32_bf16 v[232:235], v[124:127], a[232:235], v[232:235]\n"
      "v_mfma_f32_16x16x32_bf16 v[232:235], v[116:119], a[236:239], v[232:235]\n"
      "v_mfma_f32_16x16x32_bf16 v[236:239], v[252:255], a[240:243], v[236:239]\n"
      "v_mfma_f32_16x16x32_bf16 v[236:239], v[48:51], a[244:247], v[236:239]\n"
      "v_mfma_f32_16x16x32_bf16 v[236:239], v[124:127], a[248:251], v[236:239]\n"
      "v_mfma_f32_16x16x32_bf16 v[236:239], v[116:119], a[252:255], v[236:239]\n"
      "v_cvt_pk_bf16_f32 v224, v224, v228\n"
      "v_cvt_pk_bf16_f32 v228, v225, v229\n"
      "v_pk_max_i16 v224, v224, 0\n"
      "v_pk_max_i16 v228, v228, 0\n"
      "v_cvt_pk_bf16_f32 v226, v226, v230\n"
      "v_mfma_f32_16x16x32_bf16 v[20:23], v[172:175], v[156:159], 0\n"
      "v_cvt_pk_bf16_f32 v230, v227, v231\n"
      "v_pk_max_i16 v226, v226, 0\n"
      "v_cvt_pk_bf16_f32 v225, v232, v236\n"
      "v_cvt_pk_bf16_f32 v229, v233, v237\n"
      "v_mfma_f32_16x16x32_bf16 v[20:23], v[176:179], v[32:35], v[20:23]\n"
      "v_pk_max_i16 v225, v225, 0\n"
      "v_pk_max_i16 v229, v229, 0\n"
      "v_cvt_pk_bf16_f32 v227, v234, v238\n"
      "v_cvt_pk_bf16_f32 v231, v235, v239\n"
      "v_pk_max_i16 v227, v227, 0\n"
      "v_mfma_f32_16x16x32_bf16 v[88:91], v[108:111], v[156:159], 0\n"
      "v_pk_max_i16 v230, v230, 0\n"
      "v_pk_max_i16 v231, v231, 0\n"
      "v_max_f32 v190, v20, v21\n"
      "v_max3_f32 v190, v190, v22, v23\n"
      "v_mfma_f32_16x16x32_bf16 v[88:91], v[112:115], v[32:35], v[88:91]\n"
      "v_mfma_f32_16x16x32_bf16 v[24:27], v[164:167], v[156:159], 0\n"
      "v_mfma_f32_16x16x32_bf16 v[24:27], v[168:171], v[32:35], v[24:27]\n"
      "v_mfma_f32_16x16x32_bf16 v[236:239], v[40:43], v[156:159], 0\n"
      "v_mfma_f32_16x16x32_bf16 v[236:239], v[44:47], v[32:35], v[236:239]\n"
      "v_mfma_f32_16x16x32_bf16 v[232:235], v[92:95], v[156:159], 0\n"
      "v_mfma_f32_16x16x32_bf16 v[232:235], v[96:99], v[32:35], v[232:235]\n"
      "v_mfma_f32_16x16x32_bf16 v[160:163], v[216:219], v[156:159], 0\n"
      "v_mfma_f32_16x16x32_bf16 v[160:163], v[220:223], v[32:35], v[160:163]\n"
      "v_max3_f32 v190, v190, v88, v89\n"
      "v_max3_f32 v190, v190, v90, v91\n"
      "v_max3_f32 v190, v190, v24, v25\n"
      "v_max3_f32 v190, v190, v26, v27\n"
      "v_max3_f32 v190, v190, v236, v237\n"
      "v_max3_f32 v190, v190, v238, v239\n"
      "v_max3_f32 v190, v190, v232, v233\n"
      "v_mfma_f32_16x16x32_bf16 v[152:155], v[244:247], v[156:159], 0\n"
      "v_max3_f32 v190, v190, v234, v235\n"
      "v_max3_f32 v190, v190, v160, v161\n"
      "v_max3_f32 v190, v190, v162, v163\n"
      "v_mfma_f32_16x16x32_bf16 v[152:155], v[248:251], v[32:35], v[152:155]\n"
      "ds_read_b128 v[116:119], v0 offset:8192\n"
      "ds_read_b128 v[124:127], v0 offset:8704\n"
      "ds_read_b128 v[48:51], v0 offset:9216\n"
      "ds_read_b128 v[252:255], v0 offset:9728\n"
      "s_add_u32 s56, s48, 16384\n"
      "buffer_store_dwordx2 v[224:225], v2, s[44:47], s56 offen offset:0 nt sc1\n"
      "buffer_store_dwordx2 v[228:229], v2, s[44:47], s56 offen offset:256 nt sc1\n"
      "buffer_store_dwordx2 v[226:227], v2, s[44:47], s56 offen offset:512 nt sc1\n"
      "v_max3_f32 v190, v190, v152, v153\n"
      "v_max3_f32 v190, v190, v154, v155\n"
      "v_mov_b32 v32, v190\n"
      "buffer_store_dwordx2 v[230:231], v2, s[44:47], s56 offen offset:768 nt sc1\n"
      "s_nop 0\n"
      "v_permlane16_swap_b32 v190, v32\n"
      "v_max_f32 v190, v190, v32\n"
      "v_mov_b32 v32, v190\n"
      "s_nop 1\n"
      "v_permlane32_swap_b32 v190, v32\n"
      "v_max_f32 v190, v190, v32\n"
      "v_sub_f32 v20, v20, v190\n"
      "v_sub_f32 v21, v21, v190\n"
      "v_sub_f32 v22, v22, v190\n"
      "s_waitcnt lgkmcnt(3)\n"
      "v_mfma_f32_16x16x32_bf16 v[32:35], a[128:131], v[116:119], 0\n"
      "v_sub_f32 v23, v23, v190\n"
      "v_sub_f32 v88, v88, v190\n"
      "s_waitcnt lgkmcnt(2)\n"
      "v_mfma_f32_16x16x32_bf16 v[32:35], a[132:135], v[124:127], v[32:35]\n"
      "v_exp_f32 v20, v20\n"
      "s_waitcnt lgkmcnt(1)\n"
      "v_mfma_f32_16x16x32_bf16 v[32:35], a[136:139], v[48:51], v[32:35]\n"
      "v_exp_f32 v21, v21\n"
      "s_waitcnt lgkmcnt(0)\n"
      "v_mfma_f32_16x16x32_bf16 v[32:35], a[140:143], v[252:255], v[32:35]\n"
      "v_exp_f32 v22, v22\n"
      "v_mfma_f32_16x16x32_bf16 v[28:31], a[144:147], v[116:119], 0\n"
      "v_exp_f32 v23, v23\n"
      "v_mfma_f32_16x16x32_bf16 v[28:31], a[148:151], v[124:127], v[28:31]\n"
      "v_cvt_pk_bf16_f32 v156, v20, v21\n"
      "v_cvt_pk_bf16_f32 v157, v22, v23\n"
      "v_mfma_f32_16x16x32_bf16 v[28:31], a[152:155], v[48:51], v[28:31]\n"
      "v_sub_f32 v89, v89, v190\n"
      "v_sub_f32 v90, v90, v190\n"
      "v_sub_f32 v91, v91, v190\n"
      "v_sub_f32 v24, v24, v190\n"
      "v_mfma_f32_16x16x32_bf16 v[28:31], a[156:159], v[252:255], v[28:31]\n"
      "v_sub_f32 v25, v25, v190\n"
      "v_sub_f32 v26, v26, v190\n"
      "v_sub_f32 v27, v27, v190\n"
      "v_sub_f32 v236, v236, v190\n"
      "v_sub_f32 v237, v237, v190\n"
      "v_sub_f32 v238, v238, v190\n"
      "v_mfma_f32_16x16x32_bf16 v[184:187], a[160:163], v[116:119], 0\n"
      "v_sub_f32 v239, v239, v190\n"
      "v_sub_f32 v232, v232, v190\n"
      "v_sub_f32 v233, v233, v190\n"
      "v_sub_f32 v234, v234, v190\n"
      "v_sub_f32 v235, v235, v190\n"
      "v_add_f32 v86, v20, v21\n"
      "v_add_f32 v87, v22, v23\n"
      "v_mfma_f32_16x16x32_bf16 v[184:187], a[164:167], v[124:127], v[184:187]\n"
      "v_exp_f32 v88, v88\n"
      "v_exp_f32 v89, v89\n"
      "v_exp_f32 v90, v90\n"
      "v_mfma_f32_16x16x32_bf16 v[184:187], a[168:171], v[48:51], v[184:187]\n"
      "v_exp_f32 v91, v91\n"
      "v_cvt_pk_bf16_f32 v20, v88, v89\n"
      "v_cvt_pk_bf16_f32 v21, v90, v91\n"
      "v_exp_f32 v24, v24\n"
      "v_mfma_f32_16x16x32_bf16 v[184:187], a[172:175], v[252:255], v[184:187]\n"
      "v_exp_f32 v25, v25\n"
      "v_exp_f32 v26, v26\n"
      "v_exp_f32 v27, v27\n"
      "v_cvt_pk_bf16_f32 v22, v24, v25\n"
      "v_mfma_f32_16x16x32_bf16 v[208:211], a[176:179], v[116:119], 0\n"
      "v_cvt_pk_bf16_f32 v23, v26, v27\n"
      "v_exp_f32 v236, v236\n"
      "v_exp_f32 v237, v237\n"
      "v_exp_f32 v238, v238\n"
      "v_mfma_f32_16x16x32_bf16 v[208:211], a[180:183], v[124:127], v[208:211]\n"
      "v_exp_f32 v239, v239\n"
      "v_cvt_pk_bf16_f32 v24, v236, v237\n"
      "v_cvt_pk_bf16_f32 v25, v238, v239\n"
      "v_exp_f32 v232, v232\n"
      "v_mfma_f32_16x16x32_bf16 v[208:211], a[184:187], v[48:51], v[208:211]\n"
      "v_exp_f32 v233, v233\n"
      "v_exp_f32 v234, v234\n"
      "v_exp_f32 v235, v235\n"
      "v_cvt_pk_bf16_f32 v26, v232, v233\n"
      "v_mfma_f32_16x16x32_bf16 v[208:211], a[188:191], v[252:255], v[208:211]\n"
      "v_cvt_pk_bf16_f32 v27, v234, v235\n"
      "v_sub_f32 v160, v160, v190\n"
      "v_sub_f32 v161, v161, v190\n"
      "v_sub_f32 v162, v162, v190\n"
      "v_sub_f32 v163, v163, v190\n"
      "v_sub_f32 v152, v152, v190\n"
      "v_mfma_f32_16x16x4_4b_bf16 v[224:239], v[156:157], v[18:19], 0 cbsz:2 abid:0\n"
      "s_add_u32 m0, s53, 8192\n"
      "s_add_u32 s54, s49, 8192\n"
      "buffer_load_dwordx4 v1, s[40:43], s54 offen offset:0 lds\n"
      "v_sub_f32 v153, v153, v190\n"
      "v_sub_f32 v154, v154, v190\n"
      "v_sub_f32 v155, v155, v190\n"
      "v_exp_f32 v160, v160\n"
      "v_exp_f32 v161, v161\n"
      "v_exp_f32 v162, v162\n"
      "v_exp_f32 v163, v163\n"
      "v_cvt_pk_bf16_f32 v188, v160, v161\n"
      "v_cvt_pk_bf16_f32 v189, v162, v163\n"
      "v_exp_f32 v152, v152\n"
      "v_exp_f32 v153, v153\n"
      "v_exp_f32 v154, v154\n"
      "v_smfmac_f32_16x16x64_bf16 v[86:89], v[12:15], v[20:27], v5\n"
      "v_exp_f32 v155, v155\n"
      "v_cvt_pk_bf16_f32 v190, v152, v153\n"
      "v_mfma_f32_16x16x32_bf16 v[224:227], v[20:23], v[52:55], v[224:227]\n"
      "buffer_load_dwordx4 v1, s[40:43], s54 offen offset:2048 lds\n"
      "s_add_u32 m0, s53, 9088\n"
      "v_cvt_pk_bf16_f32 v191, v154, v155\n"
      "v_cvt_pk_bf16_f32 v156, v32, v33\n"
      "v_cvt_pk_bf16_f32 v157, v34, v35\n"
      "v_mfma_f32_16x16x32_bf16 v[224:227], v[24:27], v[56:59], v[224:227]\n"
      "v_cvt_pk_bf16_f32 v158, v28, v29\n"
      "v_cvt_pk_bf16_f32 v159, v30, v31\n"
      "v_cvt_pk_bf16_f32 v32, v184, v185\n"
      "v_mfma_f32_16x16x32_bf16 v[224:227], v[188:191], v[120:123], v[224:227]\n"
      "buffer_load_dwordx4 v1, s[40:43], s54 offen offset:128 lds\n"
      "v_cvt_pk_bf16_f32 v33, v186, v187\n"
      "v_cvt_pk_bf16_f32 v34, v208, v209\n"
      "v_cvt_pk_bf16_f32 v35, v210, v211\n"
      "v_smfmac_f32_16x16x32_bf16 v[86:89], v[12:13], v[188:191], v5\n"
      "v_mfma_f32_16x16x32_bf16 v[228:231], v[20:23], v[128:131], v[228:231]\n"
      "buffer_load_dwordx4 v1, s[40:43], s54 offen offset:2176 lds\n"
      "v_mfma_f32_16x16x32_bf16 v[228:231], v[24:27], v[132:135], v[228:231]\n"
      "v_mfma_f32_16x16x32_bf16 v[228:231], v[188:191], v[68:71], v[228:231]\n"
      "v_mfma_f32_16x16x32_bf16 v[232:235], v[20:23], v[100:103], v[232:235]\n"
      "v_mfma_f32_16x16x32_bf16 v[232:235], v[24:27], v[104:107], v[232:235]\n"
      "v_mfma_f32_16x16x32_bf16 v[232:235], v[188:191], v[36:39], v[232:235]\n"
      "v_mfma_f32_16x16x32_bf16 v[236:239], v[20:23], v[192:195], v[236:239]\n"
      "v_add_f32 v86, v86, v87\n"
      "v_rcp_f32 v87, v86\n"
      "v_mfma_f32_16x16x32_bf16 v[236:239], v[24:27], v[196:199], v[236:239]\n"
      "ds_bpermute_b32 v88, v4, v87\n"
      "v_mfma_f32_16x16x32_bf16 v[236:239], v[188:191], v[240:243], v[236:239]\n"
      "s_waitcnt lgkmcnt(0)\n"
      "v_mul_f32_dpp v224, v88, v224 row_newbcast:0 row_mask:0xf bank_mask:0xf\n"
      "v_mul_f32_dpp v225, v88, v225 row_newbcast:4 row_mask:0xf bank_mask:0xf\n"
      "v_mul_f32_dpp v226, v88, v226 row_newbcast:8 row_mask:0xf bank_mask:0xf\n"
      "v_mul_f32_dpp v227, v88, v227 row_newbcast:12 row_mask:0xf bank_mask:0xf\n"
      "v_mul_f32_dpp v228, v88, v228 row_newbcast:0 row_mask:0xf bank_mask:0xf\n"
      "v_mul_f32_dpp v229, v88, v229 row_newbcast:4 row_mask:0xf bank_mask:0xf\n"
      "v_mfma_f32_16x16x32_bf16 v[224:227], v[148:151], a[192:195], v[224:227]\n"
      "v_mul_f32_dpp v230, v88, v230 row_newbcast:8 row_mask:0xf bank_mask:0xf\n"
      "v_mul_f32_dpp v231, v88, v231 row_newbcast:12 row_mask:0xf bank_mask:0xf\n"
      "v_mfma_f32_16x16x32_bf16 v[224:227], v[136:139], a[196:199], v[224:227]\n"
      "v_mul_f32_dpp v232, v88, v232 row_newbcast:0 row_mask:0xf bank_mask:0xf\n"
      "v_mul_f32_dpp v233, v88, v233 row_newbcast:4 row_mask:0xf bank_mask:0xf\n"
      "v_mul_f32_dpp v234, v88, v234 row_newbcast:8 row_mask:0xf bank_mask:0xf\n"
      "v_mfma_f32_16x16x32_bf16 v[224:227], v[140:143], a[200:203], v[224:227]\n"
      "v_mul_f32_dpp v235, v88, v235 row_newbcast:12 row_mask:0xf bank_mask:0xf\n"
      "v_mul_f32_dpp v236, v88, v236 row_newbcast:0 row_mask:0xf bank_mask:0xf\n"
      "v_mul_f32_dpp v237, v88, v237 row_newbcast:4 row_mask:0xf bank_mask:0xf\n"
      "v_mul_f32_dpp v238, v88, v238 row_newbcast:8 row_mask:0xf bank_mask:0xf\n"
      "v_mfma_f32_16x16x32_bf16 v[224:227], v[144:147], a[204:207], v[224:227]\n"
      "v_mul_f32_dpp v239, v88, v239 row_newbcast:12 row_mask:0xf bank_mask:0xf\n"
      "v_mfma_f32_16x16x32_bf16 v[228:231], v[148:151], a[208:211], v[228:231]\n"
      "v_mfma_f32_16x16x32_bf16 v[228:231], v[136:139], a[212:215], v[228:231]\n"
      "v_mfma_f32_16x16x32_bf16 v[228:231], v[140:143], a[216:219], v[228:231]\n"
      "v_mfma_f32_16x16x32_bf16 v[228:231], v[144:147], a[220:223], v[228:231]\n"
      "v_mfma_f32_16x16x32_bf16 v[232:235], v[148:151], a[224:227], v[232:235]\n"
      "v_mfma_f32_16x16x32_bf16 v[232:235], v[136:139], a[228:231], v[232:235]\n"
      "v_mfma_f32_16x16x32_bf16 v[232:235], v[140:143], a[232:235], v[232:235]\n"
      "v_mfma_f32_16x16x32_bf16 v[232:235], v[144:147], a[236:239], v[232:235]\n"
      "v_mfma_f32_16x16x32_bf16 v[236:239], v[148:151], a[240:243], v[236:239]\n"
      "v_mfma_f32_16x16x32_bf16 v[236:239], v[136:139], a[244:247], v[236:239]\n"
      "v_mfma_f32_16x16x32_bf16 v[236:239], v[140:143], a[248:251], v[236:239]\n"
      "v_mfma_f32_16x16x32_bf16 v[236:239], v[144:147], a[252:255], v[236:239]\n"
      "v_cvt_pk_bf16_f32 v224, v224, v228\n"
      "v_cvt_pk_bf16_f32 v228, v225, v229\n"
      "v_pk_max_i16 v224, v224, 0\n"
      "v_pk_max_i16 v228, v228, 0\n"
      "v_cvt_pk_bf16_f32 v226, v226, v230\n"
      "v_mfma_f32_16x16x32_bf16 v[140:143], v[172:175], v[156:159], 0\n"
      "v_cvt_pk_bf16_f32 v230, v227, v231\n"
      "v_pk_max_i16 v226, v226, 0\n"
      "v_cvt_pk_bf16_f32 v225, v232, v236\n"
      "v_cvt_pk_bf16_f32 v229, v233, v237\n"
      "v_mfma_f32_16x16x32_bf16 v[140:143], v[176:179], v[32:35], v[140:143]\n"
      "v_pk_max_i16 v225, v225, 0\n"
      "v_pk_max_i16 v229, v229, 0\n"
      "v_cvt_pk_bf16_f32 v227, v234, v238\n"
      "v_cvt_pk_bf16_f32 v231, v235, v239\n"
      "v_pk_max_i16 v227, v227, 0\n"
      "v_mfma_f32_16x16x32_bf16 v[88:91], v[108:111], v[156:159], 0\n"
      "v_pk_max_i16 v230, v230, 0\n"
      "v_pk_max_i16 v231, v231, 0\n"
      "v_max_f32 v188, v140, v141\n"
      "v_max3_f32 v188, v188, v142, v143\n"
      "v_mfma_f32_16x16x32_bf16 v[88:91], v[112:115], v[32:35], v[88:91]\n"
      "v_mfma_f32_16x16x32_bf16 v[144:147], v[164:167], v[156:159], 0\n"
      "v_mfma_f32_16x16x32_bf16 v[144:147], v[168:171], v[32:35], v[144:147]\n"
      "v_mfma_f32_16x16x32_bf16 v[236:239], v[40:43], v[156:159], 0\n"
      "v_mfma_f32_16x16x32_bf16 v[236:239], v[44:47], v[32:35], v[236:239]\n"
      "v_mfma_f32_16x16x32_bf16 v[232:235], v[92:95], v[156:159], 0\n"
      "v_mfma_f32_16x16x32_bf16 v[232:235], v[96:99], v[32:35], v[232:235]\n"
      "v_mfma_f32_16x16x32_bf16 v[160:163], v[216:219], v[156:159], 0\n"
      "v_mfma_f32_16x16x32_bf16 v[160:163], v[220:223], v[32:35], v[160:163]\n"
      "v_max3_f32 v188, v188, v88, v89\n"
      "v_max3_f32 v188, v188, v90, v91\n"
      "v_max3_f32 v188, v188, v144, v145\n"
      "v_max3_f32 v188, v188, v146, v147\n"
      "v_max3_f32 v188, v188, v236, v237\n"
      "v_max3_f32 v188, v188, v238, v239\n"
      "v_max3_f32 v188, v188, v232, v233\n"
      "v_mfma_f32_16x16x32_bf16 v[152:155], v[244:247], v[156:159], 0\n"
      "v_max3_f32 v188, v188, v234, v235\n"
      "v_max3_f32 v188, v188, v160, v161\n"
      "v_max3_f32 v188, v188, v162, v163\n"
      "v_mfma_f32_16x16x32_bf16 v[152:155], v[248:251], v[32:35], v[152:155]\n"
      "ds_read_b128 v[136:139], v0 offset:4096\n"
      "ds_read_b128 v[148:151], v0 offset:4608\n"
      "ds_read_b128 v[24:27], v0 offset:5120\n"
      "ds_read_b128 v[20:23], v0 offset:5632\n"
      "s_add_u32 s56, s48, 12288\n"
      "buffer_store_dwordx2 v[224:225], v2, s[44:47], s56 offen offset:0 nt sc1\n"
      "buffer_store_dwordx2 v[228:229], v2, s[44:47], s56 offen offset:256 nt sc1\n"
      "buffer_store_dwordx2 v[226:227], v2, s[44:47], s56 offen offset:512 nt sc1\n"
      "v_max3_f32 v188, v188, v152, v153\n"
      "v_max3_f32 v188, v188, v154, v155\n"
      "v_mov_b32 v32, v188\n"
      "buffer_store_dwordx2 v[230:231], v2, s[44:47], s56 offen offset:768 nt sc1\n"
      "s_nop 0\n"
      "v_permlane16_swap_b32 v188, v32\n"
      "v_max_f32 v188, v188, v32\n"
      "v_mov_b32 v32, v188\n"
      "s_nop 1\n"
      "v_permlane32_swap_b32 v188, v32\n"
      "v_max_f32 v188, v188, v32\n"
      "v_sub_f32 v140, v140, v188\n"
      "v_sub_f32 v141, v141, v188\n"
      "v_sub_f32 v142, v142, v188\n"
      "s_waitcnt lgkmcnt(3)\n"
      "v_mfma_f32_16x16x32_bf16 v[32:35], a[128:131], v[136:139], 0\n"
      "v_sub_f32 v143, v143, v188\n"
      "v_sub_f32 v88, v88, v188\n"
      "s_waitcnt lgkmcnt(2)\n"
      "v_mfma_f32_16x16x32_bf16 v[32:35], a[132:135], v[148:151], v[32:35]\n"
      "v_exp_f32 v140, v140\n"
      "s_waitcnt lgkmcnt(1)\n"
      "v_mfma_f32_16x16x32_bf16 v[32:35], a[136:139], v[24:27], v[32:35]\n"
      "v_exp_f32 v141, v141\n"
      "s_waitcnt lgkmcnt(0)\n"
      "v_mfma_f32_16x16x32_bf16 v[32:35], a[140:143], v[20:23], v[32:35]\n"
      "v_exp_f32 v142, v142\n"
      "v_mfma_f32_16x16x32_bf16 v[28:31], a[144:147], v[136:139], 0\n"
      "v_exp_f32 v143, v143\n"
      "v_mfma_f32_16x16x32_bf16 v[28:31], a[148:151], v[148:151], v[28:31]\n"
      "v_cvt_pk_bf16_f32 v156, v140, v141\n"
      "v_cvt_pk_bf16_f32 v157, v142, v143\n"
      "v_mfma_f32_16x16x32_bf16 v[28:31], a[152:155], v[24:27], v[28:31]\n"
      "v_sub_f32 v89, v89, v188\n"
      "v_sub_f32 v90, v90, v188\n"
      "v_sub_f32 v91, v91, v188\n"
      "v_sub_f32 v144, v144, v188\n"
      "v_mfma_f32_16x16x32_bf16 v[28:31], a[156:159], v[20:23], v[28:31]\n"
      "v_sub_f32 v145, v145, v188\n"
      "v_sub_f32 v146, v146, v188\n"
      "v_sub_f32 v147, v147, v188\n"
      "v_sub_f32 v236, v236, v188\n"
      "v_sub_f32 v237, v237, v188\n"
      "v_sub_f32 v238, v238, v188\n"
      "v_mfma_f32_16x16x32_bf16 v[184:187], a[160:163], v[136:139], 0\n"
      "v_sub_f32 v239, v239, v188\n"
      "v_sub_f32 v232, v232, v188\n"
      "v_sub_f32 v233, v233, v188\n"
      "v_sub_f32 v234, v234, v188\n"
      "v_sub_f32 v235, v235, v188\n"
      "v_add_f32 v86, v140, v141\n"
      "v_add_f32 v87, v142, v143\n"
      "v_mfma_f32_16x16x32_bf16 v[184:187], a[164:167], v[148:151], v[184:187]\n"
      "v_exp_f32 v88, v88\n"
      "v_exp_f32 v89, v89\n"
      "v_exp_f32 v90, v90\n"
      "v_mfma_f32_16x16x32_bf16 v[184:187], a[168:171], v[24:27], v[184:187]\n"
      "v_exp_f32 v91, v91\n"
      "v_cvt_pk_bf16_f32 v140, v88, v89\n"
      "v_cvt_pk_bf16_f32 v141, v90, v91\n"
      "v_exp_f32 v144, v144\n"
      "v_mfma_f32_16x16x32_bf16 v[184:187], a[172:175], v[20:23], v[184:187]\n"
      "v_exp_f32 v145, v145\n"
      "v_exp_f32 v146, v146\n"
      "v_exp_f32 v147, v147\n"
      "v_cvt_pk_bf16_f32 v142, v144, v145\n"
      "v_mfma_f32_16x16x32_bf16 v[208:211], a[176:179], v[136:139], 0\n"
      "v_cvt_pk_bf16_f32 v143, v146, v147\n"
      "v_exp_f32 v236, v236\n"
      "v_exp_f32 v237, v237\n"
      "v_exp_f32 v238, v238\n"
      "v_mfma_f32_16x16x32_bf16 v[208:211], a[180:183], v[148:151], v[208:211]\n"
      "v_exp_f32 v239, v239\n"
      "v_cvt_pk_bf16_f32 v144, v236, v237\n"
      "v_cvt_pk_bf16_f32 v145, v238, v239\n"
      "v_exp_f32 v232, v232\n"
      "v_mfma_f32_16x16x32_bf16 v[208:211], a[184:187], v[24:27], v[208:211]\n"
      "v_exp_f32 v233, v233\n"
      "v_exp_f32 v234, v234\n"
      "v_exp_f32 v235, v235\n"
      "v_cvt_pk_bf16_f32 v146, v232, v233\n"
      "v_mfma_f32_16x16x32_bf16 v[208:211], a[188:191], v[20:23], v[208:211]\n"
      "v_cvt_pk_bf16_f32 v147, v234, v235\n"
      "v_sub_f32 v160, v160, v188\n"
      "v_sub_f32 v161, v161, v188\n"
      "v_sub_f32 v162, v162, v188\n"
      "v_sub_f32 v163, v163, v188\n"
      "v_sub_f32 v152, v152, v188\n"
      "v_mfma_f32_16x16x4_4b_bf16 v[224:239], v[156:157], v[18:19], 0 cbsz:2 abid:0\n"
      "s_add_u32 m0, s53, 4096\n"
      "s_add_u32 s54, s49, 4096\n"
      "buffer_load_dwordx4 v1, s[40:43], s54 offen offset:0 lds\n"
      "v_sub_f32 v153, v153, v188\n"
      "v_sub_f32 v154, v154, v188\n"
      "v_sub_f32 v155, v155, v188\n"
      "v_exp_f32 v160, v160\n"
      "v_exp_f32 v161, v161\n"
      "v_exp_f32 v162, v162\n"
      "v_exp_f32 v163, v163\n"
      "v_cvt_pk_bf16_f32 v188, v160, v161\n"
      "v_cvt_pk_bf16_f32 v189, v162, v163\n"
      "v_exp_f32 v152, v152\n"
      "v_exp_f32 v153, v153\n"
      "v_exp_f32 v154, v154\n"
      "v_smfmac_f32_16x16x64_bf16 v[86:89], v[12:15], v[140:147], v5\n"
      "v_exp_f32 v155, v155\n"
      "v_cvt_pk_bf16_f32 v190, v152, v153\n"
      "v_mfma_f32_16x16x32_bf16 v[224:227], v[140:143], v[52:55], v[224:227]\n"
      "buffer_load_dwordx4 v1, s[40:43], s54 offen offset:2048 lds\n"
      "s_add_u32 m0, s53, 4992\n"
      "v_cvt_pk_bf16_f32 v191, v154, v155\n"
      "v_cvt_pk_bf16_f32 v156, v32, v33\n"
      "v_cvt_pk_bf16_f32 v157, v34, v35\n"
      "v_mfma_f32_16x16x32_bf16 v[224:227], v[144:147], v[56:59], v[224:227]\n"
      "v_cvt_pk_bf16_f32 v158, v28, v29\n"
      "v_cvt_pk_bf16_f32 v159, v30, v31\n"
      "v_cvt_pk_bf16_f32 v32, v184, v185\n"
      "v_cvt_pk_bf16_f32 v33, v186, v187\n"
      "v_mfma_f32_16x16x32_bf16 v[224:227], v[188:191], v[120:123], v[224:227]\n"
      "buffer_load_dwordx4 v1, s[40:43], s54 offen offset:128 lds\n"
      "v_cvt_pk_bf16_f32 v34, v208, v209\n"
      "v_cvt_pk_bf16_f32 v35, v210, v211\n"
      "v_smfmac_f32_16x16x32_bf16 v[86:89], v[12:13], v[188:191], v5\n"
      "v_mfma_f32_16x16x32_bf16 v[228:231], v[140:143], v[128:131], v[228:231]\n"
      "buffer_load_dwordx4 v1, s[40:43], s54 offen offset:2176 lds\n"
      "v_mfma_f32_16x16x32_bf16 v[228:231], v[144:147], v[132:135], v[228:231]\n"
      "v_mfma_f32_16x16x32_bf16 v[228:231], v[188:191], v[68:71], v[228:231]\n"
      "v_mfma_f32_16x16x32_bf16 v[232:235], v[140:143], v[100:103], v[232:235]\n"
      "v_mfma_f32_16x16x32_bf16 v[232:235], v[144:147], v[104:107], v[232:235]\n"
      "v_mfma_f32_16x16x32_bf16 v[232:235], v[188:191], v[36:39], v[232:235]\n"
      "v_mfma_f32_16x16x32_bf16 v[236:239], v[140:143], v[192:195], v[236:239]\n"
      "v_add_f32 v86, v86, v87\n"
      "v_rcp_f32 v87, v86\n"
      "v_mfma_f32_16x16x32_bf16 v[236:239], v[144:147], v[196:199], v[236:239]\n"
      "ds_bpermute_b32 v88, v4, v87\n"
      "v_mfma_f32_16x16x32_bf16 v[236:239], v[188:191], v[240:243], v[236:239]\n"
      "s_waitcnt lgkmcnt(0)\n"
      "v_mul_f32_dpp v224, v88, v224 row_newbcast:0 row_mask:0xf bank_mask:0xf\n"
      "v_mul_f32_dpp v225, v88, v225 row_newbcast:4 row_mask:0xf bank_mask:0xf\n"
      "v_mul_f32_dpp v226, v88, v226 row_newbcast:8 row_mask:0xf bank_mask:0xf\n"
      "v_mul_f32_dpp v227, v88, v227 row_newbcast:12 row_mask:0xf bank_mask:0xf\n"
      "v_mul_f32_dpp v228, v88, v228 row_newbcast:0 row_mask:0xf bank_mask:0xf\n"
      "v_mul_f32_dpp v229, v88, v229 row_newbcast:4 row_mask:0xf bank_mask:0xf\n"
      "v_mfma_f32_16x16x32_bf16 v[224:227], v[116:119], a[192:195], v[224:227]\n"
      "v_mul_f32_dpp v230, v88, v230 row_newbcast:8 row_mask:0xf bank_mask:0xf\n"
      "v_mul_f32_dpp v231, v88, v231 row_newbcast:12 row_mask:0xf bank_mask:0xf\n"
      "v_mfma_f32_16x16x32_bf16 v[224:227], v[124:127], a[196:199], v[224:227]\n"
      "v_mul_f32_dpp v232, v88, v232 row_newbcast:0 row_mask:0xf bank_mask:0xf\n"
      "v_mul_f32_dpp v233, v88, v233 row_newbcast:4 row_mask:0xf bank_mask:0xf\n"
      "v_mul_f32_dpp v234, v88, v234 row_newbcast:8 row_mask:0xf bank_mask:0xf\n"
      "v_mul_f32_dpp v235, v88, v235 row_newbcast:12 row_mask:0xf bank_mask:0xf\n"
      "v_mfma_f32_16x16x32_bf16 v[224:227], v[48:51], a[200:203], v[224:227]\n"
      "v_mul_f32_dpp v236, v88, v236 row_newbcast:0 row_mask:0xf bank_mask:0xf\n"
      "v_mul_f32_dpp v237, v88, v237 row_newbcast:4 row_mask:0xf bank_mask:0xf\n"
      "v_mul_f32_dpp v238, v88, v238 row_newbcast:8 row_mask:0xf bank_mask:0xf\n"
      "v_mfma_f32_16x16x32_bf16 v[224:227], v[252:255], a[204:207], v[224:227]\n"
      "v_mul_f32_dpp v239, v88, v239 row_newbcast:12 row_mask:0xf bank_mask:0xf\n"
      "v_mfma_f32_16x16x32_bf16 v[228:231], v[116:119], a[208:211], v[228:231]\n"
      "v_mfma_f32_16x16x32_bf16 v[228:231], v[124:127], a[212:215], v[228:231]\n"
      "v_mfma_f32_16x16x32_bf16 v[228:231], v[48:51], a[216:219], v[228:231]\n"
      "v_mfma_f32_16x16x32_bf16 v[228:231], v[252:255], a[220:223], v[228:231]\n"
      "v_mfma_f32_16x16x32_bf16 v[232:235], v[116:119], a[224:227], v[232:235]\n"
      "v_mfma_f32_16x16x32_bf16 v[232:235], v[124:127], a[228:231], v[232:235]\n"
      "v_mfma_f32_16x16x32_bf16 v[232:235], v[48:51], a[232:235], v[232:235]\n"
      "v_mfma_f32_16x16x32_bf16 v[232:235], v[252:255], a[236:239], v[232:235]\n"
      "v_mfma_f32_16x16x32_bf16 v[236:239], v[116:119], a[240:243], v[236:239]\n"
      "v_mfma_f32_16x16x32_bf16 v[236:239], v[124:127], a[244:247], v[236:239]\n"
      "v_mfma_f32_16x16x32_bf16 v[236:239], v[48:51], a[248:251], v[236:239]\n"
      "v_mfma_f32_16x16x32_bf16 v[236:239], v[252:255], a[252:255], v[236:239]\n"
      "v_cvt_pk_bf16_f32 v224, v224, v228\n"
      "v_cvt_pk_bf16_f32 v228, v225, v229\n"
      "v_pk_max_i16 v224, v224, 0\n"
      "v_pk_max_i16 v228, v228, 0\n"
      "v_cvt_pk_bf16_f32 v226, v226, v230\n"
      "v_mfma_f32_16x16x32_bf16 v[140:143], v[172:175], v[156:159], 0\n"
      "v_cvt_pk_bf16_f32 v230, v227, v231\n"
      "v_pk_max_i16 v226, v226, 0\n"
      "v_cvt_pk_bf16_f32 v225, v232, v236\n"
      "v_cvt_pk_bf16_f32 v229, v233, v237\n"
      "v_mfma_f32_16x16x32_bf16 v[140:143], v[176:179], v[32:35], v[140:143]\n"
      "v_pk_max_i16 v225, v225, 0\n"
      "v_pk_max_i16 v229, v229, 0\n"
      "v_cvt_pk_bf16_f32 v227, v234, v238\n"
      "v_cvt_pk_bf16_f32 v231, v235, v239\n"
      "v_pk_max_i16 v227, v227, 0\n"
      "v_mfma_f32_16x16x32_bf16 v[88:91], v[108:111], v[156:159], 0\n"
      "v_pk_max_i16 v230, v230, 0\n"
      "v_pk_max_i16 v231, v231, 0\n"
      "v_max_f32 v188, v140, v141\n"
      "v_max3_f32 v188, v188, v142, v143\n"
      "v_mfma_f32_16x16x32_bf16 v[88:91], v[112:115], v[32:35], v[88:91]\n"
      "v_mfma_f32_16x16x32_bf16 v[144:147], v[164:167], v[156:159], 0\n"
      "v_mfma_f32_16x16x32_bf16 v[144:147], v[168:171], v[32:35], v[144:147]\n"
      "v_mfma_f32_16x16x32_bf16 v[236:239], v[40:43], v[156:159], 0\n"
      "v_mfma_f32_16x16x32_bf16 v[236:239], v[44:47], v[32:35], v[236:239]\n"
      "v_mfma_f32_16x16x32_bf16 v[232:235], v[92:95], v[156:159], 0\n"
      "v_mfma_f32_16x16x32_bf16 v[232:235], v[96:99], v[32:35], v[232:235]\n"
      "v_mfma_f32_16x16x32_bf16 v[160:163], v[216:219], v[156:159], 0\n"
      "v_mfma_f32_16x16x32_bf16 v[160:163], v[220:223], v[32:35], v[160:163]\n"
      "v_max3_f32 v188, v188, v88, v89\n"
      "v_max3_f32 v188, v188, v90, v91\n"
      "v_max3_f32 v188, v188, v144, v145\n"
      "v_max3_f32 v188, v188, v146, v147\n"
      "v_max3_f32 v188, v188, v236, v237\n"
      "v_max3_f32 v188, v188, v238, v239\n"
      "v_max3_f32 v188, v188, v232, v233\n"
      "v_mfma_f32_16x16x32_bf16 v[152:155], v[244:247], v[156:159], 0\n"
      "v_max3_f32 v188, v188, v234, v235\n"
      "v_max3_f32 v188, v188, v160, v161\n"
      "v_max3_f32 v188, v188, v162, v163\n"
      "v_mfma_f32_16x16x32_bf16 v[152:155], v[248:251], v[32:35], v[152:155]\n"
      "ds_read_b128 v[124:127], v0 offset:0\n"
      "ds_read_b128 v[48:51], v0 offset:512\n"
      "ds_read_b128 v[116:119], v0 offset:1024\n"
      "ds_read_b128 v[252:255], v0 offset:1536\n"
      "s_add_u32 s56, s48, 8192\n"
      "buffer_store_dwordx2 v[224:225], v2, s[44:47], s56 offen offset:0 nt sc1\n"
      "buffer_store_dwordx2 v[228:229], v2, s[44:47], s56 offen offset:256 nt sc1\n"
      "buffer_store_dwordx2 v[226:227], v2, s[44:47], s56 offen offset:512 nt sc1\n"
      "v_max3_f32 v188, v188, v152, v153\n"
      "v_max3_f32 v188, v188, v154, v155\n"
      "v_mov_b32 v32, v188\n"
      "buffer_store_dwordx2 v[230:231], v2, s[44:47], s56 offen offset:768 nt sc1\n"
      "s_nop 0\n"
      "v_permlane16_swap_b32 v188, v32\n"
      "v_max_f32 v188, v188, v32\n"
      "v_mov_b32 v32, v188\n"
      "s_nop 1\n"
      "v_permlane32_swap_b32 v188, v32\n"
      "v_max_f32 v188, v188, v32\n"
      "v_sub_f32 v140, v140, v188\n"
      "v_sub_f32 v141, v141, v188\n"
      "v_sub_f32 v142, v142, v188\n"
      "s_waitcnt lgkmcnt(3)\n"
      "v_mfma_f32_16x16x32_bf16 v[32:35], a[128:131], v[124:127], 0\n"
      "v_sub_f32 v143, v143, v188\n"
      "v_sub_f32 v88, v88, v188\n"
      "s_waitcnt lgkmcnt(2)\n"
      "v_mfma_f32_16x16x32_bf16 v[32:35], a[132:135], v[48:51], v[32:35]\n"
      "v_exp_f32 v140, v140\n"
      "s_waitcnt lgkmcnt(1)\n"
      "v_mfma_f32_16x16x32_bf16 v[32:35], a[136:139], v[116:119], v[32:35]\n"
      "v_exp_f32 v141, v141\n"
      "s_waitcnt lgkmcnt(0)\n"
      "v_mfma_f32_16x16x32_bf16 v[32:35], a[140:143], v[252:255], v[32:35]\n"
      "v_exp_f32 v142, v142\n"
      "v_mfma_f32_16x16x32_bf16 v[28:31], a[144:147], v[124:127], 0\n"
      "v_exp_f32 v143, v143\n"
      "v_mfma_f32_16x16x32_bf16 v[28:31], a[148:151], v[48:51], v[28:31]\n"
      "v_cvt_pk_bf16_f32 v190, v140, v141\n"
      "v_cvt_pk_bf16_f32 v191, v142, v143\n"
      "v_mfma_f32_16x16x32_bf16 v[28:31], a[152:155], v[116:119], v[28:31]\n"
      "v_sub_f32 v89, v89, v188\n"
      "v_sub_f32 v90, v90, v188\n"
      "v_sub_f32 v91, v91, v188\n"
      "v_sub_f32 v144, v144, v188\n"
      "v_mfma_f32_16x16x32_bf16 v[28:31], a[156:159], v[252:255], v[28:31]\n"
      "v_sub_f32 v145, v145, v188\n"
      "v_sub_f32 v146, v146, v188\n"
      "v_sub_f32 v147, v147, v188\n"
      "v_sub_f32 v236, v236, v188\n"
      "v_sub_f32 v237, v237, v188\n"
      "v_sub_f32 v238, v238, v188\n"
      "v_mfma_f32_16x16x32_bf16 v[184:187], a[160:163], v[124:127], 0\n"
      "v_sub_f32 v239, v239, v188\n"
      "v_sub_f32 v232, v232, v188\n"
      "v_sub_f32 v233, v233, v188\n"
      "v_sub_f32 v234, v234, v188\n"
      "v_sub_f32 v235, v235, v188\n"
      "v_add_f32 v86, v140, v141\n"
      "v_add_f32 v87, v142, v143\n"
      "v_mfma_f32_16x16x32_bf16 v[184:187], a[164:167], v[48:51], v[184:187]\n"
      "v_exp_f32 v88, v88\n"
      "v_exp_f32 v89, v89\n"
      "v_exp_f32 v90, v90\n"
      "v_mfma_f32_16x16x32_bf16 v[184:187], a[168:171], v[116:119], v[184:187]\n"
      "v_exp_f32 v91, v91\n"
      "v_cvt_pk_bf16_f32 v140, v88, v89\n"
      "v_cvt_pk_bf16_f32 v141, v90, v91\n"
      "v_exp_f32 v144, v144\n"
      "v_mfma_f32_16x16x32_bf16 v[184:187], a[172:175], v[252:255], v[184:187]\n"
      "v_exp_f32 v145, v145\n"
      "v_exp_f32 v146, v146\n"
      "v_exp_f32 v147, v147\n"
      "v_cvt_pk_bf16_f32 v142, v144, v145\n"
      "v_mfma_f32_16x16x32_bf16 v[208:211], a[176:179], v[124:127], 0\n"
      "v_cvt_pk_bf16_f32 v143, v146, v147\n"
      "v_exp_f32 v236, v236\n"
      "v_exp_f32 v237, v237\n"
      "v_exp_f32 v238, v238\n"
      "v_mfma_f32_16x16x32_bf16 v[208:211], a[180:183], v[48:51], v[208:211]\n"
      "v_exp_f32 v239, v239\n"
      "v_cvt_pk_bf16_f32 v144, v236, v237\n"
      "v_cvt_pk_bf16_f32 v145, v238, v239\n"
      "v_exp_f32 v232, v232\n"
      "v_mfma_f32_16x16x32_bf16 v[208:211], a[184:187], v[116:119], v[208:211]\n"
      "v_exp_f32 v233, v233\n"
      "v_exp_f32 v234, v234\n"
      "v_exp_f32 v235, v235\n"
      "v_cvt_pk_bf16_f32 v146, v232, v233\n"
      "v_mfma_f32_16x16x32_bf16 v[208:211], a[188:191], v[252:255], v[208:211]\n"
      "ds_read_b128 v[156:159], v10 offset:0\n"
      "v_cvt_pk_bf16_f32 v147, v234, v235\n"
      "v_sub_f32 v160, v160, v188\n"
      "v_sub_f32 v161, v161, v188\n"
      "v_sub_f32 v162, v162, v188\n"
      "v_sub_f32 v163, v163, v188\n"
      "v_sub_f32 v152, v152, v188\n"
      "v_mfma_f32_16x16x4_4b_bf16 v[224:239], v[190:191], v[18:19], 0 cbsz:2 abid:0\n"
      "s_add_u32 m0, s53, 0\n"
      "s_add_u32 s54, s49, 0\n"
      "buffer_load_dwordx4 v1, s[40:43], s54 offen offset:0 nt lds\n"
      "v_sub_f32 v153, v153, v188\n"
      "v_sub_f32 v154, v154, v188\n"
      "v_sub_f32 v155, v155, v188\n"
      "v_exp_f32 v160, v160\n"
      "v_exp_f32 v161, v161\n"
      "v_exp_f32 v162, v162\n"
      "v_exp_f32 v163, v163\n"
      "v_cvt_pk_bf16_f32 v188, v160, v161\n"
      "v_cvt_pk_bf16_f32 v189, v162, v163\n"
      "v_exp_f32 v152, v152\n"
      "v_exp_f32 v153, v153\n"
      "v_exp_f32 v154, v154\n"
      "v_smfmac_f32_16x16x64_bf16 v[86:89], v[12:15], v[140:147], v5\n"
      "v_exp_f32 v155, v155\n"
      "v_cvt_pk_bf16_f32 v190, v152, v153\n"
      "v_mfma_f32_16x16x32_bf16 v[224:227], v[140:143], v[52:55], v[224:227]\n"
      "buffer_load_dwordx4 v1, s[40:43], s54 offen offset:2048 nt lds\n"
      "s_add_u32 m0, s53, 896\n"
      "v_cvt_pk_bf16_f32 v191, v154, v155\n"
      "v_cvt_pk_bf16_f32 v152, v32, v33\n"
      "v_cvt_pk_bf16_f32 v153, v34, v35\n"
      "v_cvt_pk_bf16_f32 v154, v28, v29\n"
      "v_mfma_f32_16x16x32_bf16 v[224:227], v[144:147], v[56:59], v[224:227]\n"
      "v_cvt_pk_bf16_f32 v155, v30, v31\n"
      "v_cvt_pk_bf16_f32 v32, v184, v185\n"
      "v_cvt_pk_bf16_f32 v33, v186, v187\n"
      "v_cvt_pk_bf16_f32 v34, v208, v209\n"
      "v_mfma_f32_16x16x32_bf16 v[224:227], v[188:191], v[120:123], v[224:227]\n"
      "buffer_load_dwordx4 v1, s[40:43], s54 offen offset:128 nt lds\n"
      "v_cvt_pk_bf16_f32 v35, v210, v211\n"
      "v_smfmac_f32_16x16x32_bf16 v[86:89], v[12:13], v[188:191], v5\n"
      "v_mfma_f32_16x16x32_bf16 v[228:231], v[140:143], v[128:131], v[228:231]\n"
      "buffer_load_dwordx4 v1, s[40:43], s54 offen offset:2176 nt lds\n"
      "v_mfma_f32_16x16x32_bf16 v[228:231], v[144:147], v[132:135], v[228:231]\n"
      "v_mfma_f32_16x16x32_bf16 v[228:231], v[188:191], v[68:71], v[228:231]\n"
      "v_add_u32 v10, 64, v10\n"
      "v_mfma_f32_16x16x32_bf16 v[232:235], v[140:143], v[100:103], v[232:235]\n"
      "v_mfma_f32_16x16x32_bf16 v[232:235], v[144:147], v[104:107], v[232:235]\n"
      "v_mfma_f32_16x16x32_bf16 v[232:235], v[188:191], v[36:39], v[232:235]\n"
      "v_add_f32 v86, v86, v87\n"
      "v_rcp_f32 v87, v86\n"
      "v_mfma_f32_16x16x32_bf16 v[236:239], v[140:143], v[192:195], v[236:239]\n"
      "ds_bpermute_b32 v88, v4, v87\n"
      "v_mfma_f32_16x16x32_bf16 v[236:239], v[144:147], v[196:199], v[236:239]\n"
      "v_mfma_f32_16x16x32_bf16 v[236:239], v[188:191], v[240:243], v[236:239]\n"
      "s_waitcnt lgkmcnt(0)\n"
      "v_mul_f32_dpp v224, v88, v224 row_newbcast:0 row_mask:0xf bank_mask:0xf\n"
      "v_mul_f32_dpp v225, v88, v225 row_newbcast:4 row_mask:0xf bank_mask:0xf\n"
      "v_mul_f32_dpp v226, v88, v226 row_newbcast:8 row_mask:0xf bank_mask:0xf\n"
      "v_mul_f32_dpp v227, v88, v227 row_newbcast:12 row_mask:0xf bank_mask:0xf\n"
      "v_mul_f32_dpp v228, v88, v228 row_newbcast:0 row_mask:0xf bank_mask:0xf\n"
      "v_mul_f32_dpp v229, v88, v229 row_newbcast:4 row_mask:0xf bank_mask:0xf\n"
      "v_mfma_f32_16x16x32_bf16 v[224:227], v[136:139], a[192:195], v[224:227]\n"
      "v_mul_f32_dpp v230, v88, v230 row_newbcast:8 row_mask:0xf bank_mask:0xf\n"
      "v_mul_f32_dpp v231, v88, v231 row_newbcast:12 row_mask:0xf bank_mask:0xf\n"
      "v_mul_f32_dpp v232, v88, v232 row_newbcast:0 row_mask:0xf bank_mask:0xf\n"
      "v_mfma_f32_16x16x32_bf16 v[224:227], v[148:151], a[196:199], v[224:227]\n"
      "v_mul_f32_dpp v233, v88, v233 row_newbcast:4 row_mask:0xf bank_mask:0xf\n"
      "v_mul_f32_dpp v234, v88, v234 row_newbcast:8 row_mask:0xf bank_mask:0xf\n"
      "v_mul_f32_dpp v235, v88, v235 row_newbcast:12 row_mask:0xf bank_mask:0xf\n"
      "v_mul_f32_dpp v236, v88, v236 row_newbcast:0 row_mask:0xf bank_mask:0xf\n"
      "v_mfma_f32_16x16x32_bf16 v[224:227], v[24:27], a[200:203], v[224:227]\n"
      "v_mul_f32_dpp v237, v88, v237 row_newbcast:4 row_mask:0xf bank_mask:0xf\n"
      "v_mul_f32_dpp v238, v88, v238 row_newbcast:8 row_mask:0xf bank_mask:0xf\n"
      "v_mul_f32_dpp v239, v88, v239 row_newbcast:12 row_mask:0xf bank_mask:0xf\n"
      "v_mfma_f32_16x16x32_bf16 v[224:227], v[20:23], a[204:207], v[224:227]\n"
      "v_mfma_f32_16x16x32_bf16 v[228:231], v[136:139], a[208:211], v[228:231]\n"
      "v_mfma_f32_16x16x32_bf16 v[228:231], v[148:151], a[212:215], v[228:231]\n"
      "v_mfma_f32_16x16x32_bf16 v[228:231], v[24:27], a[216:219], v[228:231]\n"
      "v_mfma_f32_16x16x32_bf16 v[228:231], v[20:23], a[220:223], v[228:231]\n"
      "v_mfma_f32_16x16x32_bf16 v[232:235], v[136:139], a[224:227], v[232:235]\n"
      "v_mfma_f32_16x16x32_bf16 v[232:235], v[148:151], a[228:231], v[232:235]\n"
      "v_mfma_f32_16x16x32_bf16 v[232:235], v[24:27], a[232:235], v[232:235]\n"
      "v_mfma_f32_16x16x32_bf16 v[232:235], v[20:23], a[236:239], v[232:235]\n"
      "v_mfma_f32_16x16x32_bf16 v[236:239], v[136:139], a[240:243], v[236:239]\n"
      "v_mfma_f32_16x16x32_bf16 v[236:239], v[148:151], a[244:247], v[236:239]\n"
      "v_mfma_f32_16x16x32_bf16 v[236:239], v[24:27], a[248:251], v[236:239]\n"
      "v_mfma_f32_16x16x32_bf16 v[236:239], v[20:23], a[252:255], v[236:239]\n"
      "v_cvt_pk_bf16_f32 v224, v224, v228\n"
      "v_cvt_pk_bf16_f32 v228, v225, v229\n"
      "v_pk_max_i16 v224, v224, 0\n"
      "v_pk_max_i16 v228, v228, 0\n"
      "v_cvt_pk_bf16_f32 v226, v226, v230\n"
      "v_cvt_pk_bf16_f32 v230, v227, v231\n"
      "v_pk_max_i16 v226, v226, 0\n"
      "v_mfma_f32_16x16x32_bf16 v[148:151], v[172:175], v[152:155], 0\n"
      "v_cvt_pk_bf16_f32 v225, v232, v236\n"
      "v_cvt_pk_bf16_f32 v229, v233, v237\n"
      "v_pk_max_i16 v225, v225, 0\n"
      "v_pk_max_i16 v229, v229, 0\n"
      "v_cvt_pk_bf16_f32 v227, v234, v238\n"
      "v_cvt_pk_bf16_f32 v231, v235, v239\n"
      "v_pk_max_i16 v227, v227, 0\n"
      "v_mfma_f32_16x16x32_bf16 v[148:151], v[176:179], v[32:35], v[148:151]\n"
      "v_pk_max_i16 v230, v230, 0\n"
      "v_pk_max_i16 v231, v231, 0\n"
      "v_mfma_f32_16x16x32_bf16 v[136:139], v[108:111], v[152:155], 0\n"
      "v_mfma_f32_16x16x32_bf16 v[136:139], v[112:115], v[32:35], v[136:139]\n"
      "v_mfma_f32_16x16x32_bf16 v[236:239], v[164:167], v[152:155], 0\n"
      "v_mfma_f32_16x16x32_bf16 v[236:239], v[168:171], v[32:35], v[236:239]\n"
      "v_mfma_f32_16x16x32_bf16 v[160:163], v[40:43], v[152:155], 0\n"
      "v_mfma_f32_16x16x32_bf16 v[160:163], v[44:47], v[32:35], v[160:163]\n"
      "v_max_f32 v208, v148, v149\n"
      "v_max3_f32 v208, v208, v150, v151\n"
      "v_mfma_f32_16x16x32_bf16 v[28:31], v[92:95], v[152:155], 0\n"
      "v_mfma_f32_16x16x32_bf16 v[28:31], v[96:99], v[32:35], v[28:31]\n"
      "v_max3_f32 v208, v208, v136, v137\n"
      "v_max3_f32 v208, v208, v138, v139\n"
      "v_max3_f32 v208, v208, v236, v237\n"
      "v_max3_f32 v208, v208, v238, v239\n"
      "v_max3_f32 v208, v208, v160, v161\n"
      "v_max3_f32 v208, v208, v162, v163\n"
      "v_mfma_f32_16x16x32_bf16 v[24:27], v[216:219], v[152:155], 0\n"
      "v_mfma_f32_16x16x32_bf16 v[24:27], v[220:223], v[32:35], v[24:27]\n"
      "v_max3_f32 v208, v208, v28, v29\n"
      "v_max3_f32 v208, v208, v30, v31\n"
      "v_mfma_f32_16x16x32_bf16 v[20:23], v[244:247], v[152:155], 0\n"
      "v_mfma_f32_16x16x32_bf16 v[20:23], v[248:251], v[32:35], v[20:23]\n"
      "ds_read_b64 v[152:153], v11 offset:4096\n"
      "s_nop 2\n"
      "v_max3_f32 v208, v208, v24, v25\n"
      "v_max3_f32 v208, v208, v26, v27\n"
      "s_nop 1\n"
      "v_max3_f32 v208, v208, v20, v21\n"
      "v_max3_f32 v208, v208, v22, v23\n"
      "v_mov_b32 v32, v208\n"
      "s_nop 1\n"
      "v_permlane16_swap_b32 v208, v32\n"
      "v_max_f32 v208, v208, v32\n"
      "v_mov_b32 v32, v208\n"
      "s_nop 1\n"
      "v_permlane32_swap_b32 v208, v32\n"
      "v_max_f32 v208, v208, v32\n"
      "v_sub_f32 v148, v148, v208\n"
      "v_sub_f32 v149, v149, v208\n"
      "v_sub_f32 v150, v150, v208\n"
      "v_sub_f32 v151, v151, v208\n"
      "v_exp_f32 v148, v148\n"
      "v_exp_f32 v149, v149\n"
      "v_exp_f32 v150, v150\n"
      "v_exp_f32 v151, v151\n"
      "v_cvt_pk_bf16_f32 v234, v148, v149\n"
      "v_cvt_pk_bf16_f32 v235, v150, v151\n"
      "v_sub_f32 v136, v136, v208\n"
      "v_sub_f32 v137, v137, v208\n"
      "v_sub_f32 v138, v138, v208\n"
      "v_sub_f32 v139, v139, v208\n"
      "v_sub_f32 v236, v236, v208\n"
      "v_sub_f32 v237, v237, v208\n"
      "v_sub_f32 v238, v238, v208\n"
      "v_sub_f32 v239, v239, v208\n"
      "v_sub_f32 v160, v160, v208\n"
      "v_sub_f32 v161, v161, v208\n"
      "v_sub_f32 v162, v162, v208\n"
      "v_sub_f32 v163, v163, v208\n"
      "v_sub_f32 v28, v28, v208\n"
      "v_sub_f32 v29, v29, v208\n"
      "v_sub_f32 v30, v30, v208\n"
      "v_sub_f32 v31, v31, v208\n"
      "v_add_f32 v32, v148, v149\n"
      "v_add_f32 v33, v150, v151\n"
      "v_exp_f32 v136, v136\n"
      "v_exp_f32 v137, v137\n"
      "v_exp_f32 v138, v138\n"
      "v_exp_f32 v139, v139\n"
      "v_cvt_pk_bf16_f32 v232, v136, v137\n"
      "v_cvt_pk_bf16_f32 v233, v138, v139\n"
      "v_exp_f32 v236, v236\n"
      "v_mfma_f32_16x16x4_4b_bf16 v[136:151], v[234:235], v[18:19], 0 cbsz:2 abid:0\n"
      "v_exp_f32 v237, v237\n"
      "v_exp_f32 v238, v238\n"
      "v_exp_f32 v239, v239\n"
      "v_cvt_pk_bf16_f32 v234, v236, v237\n"
      "v_cvt_pk_bf16_f32 v235, v238, v239\n"
      "v_exp_f32 v160, v160\n"
      "v_exp_f32 v161, v161\n"
      "v_exp_f32 v162, v162\n"
      "v_exp_f32 v163, v163\n"
      "v_cvt_pk_bf16_f32 v236, v160, v161\n"
      "v_cvt_pk_bf16_f32 v237, v162, v163\n"
      "v_exp_f32 v28, v28\n"
      "v_exp_f32 v29, v29\n"
      "v_exp_f32 v30, v30\n"
      "v_exp_f32 v31, v31\n"
      "v_cvt_pk_bf16_f32 v238, v28, v29\n"
      "v_cvt_pk_bf16_f32 v239, v30, v31\n"
      "v_sub_f32 v24, v24, v208\n"
      "v_sub_f32 v25, v25, v208\n"
      "v_sub_f32 v26, v26, v208\n"
      "v_sub_f32 v27, v27, v208\n"
      "v_sub_f32 v20, v20, v208\n"
      "v_sub_f32 v21, v21, v208\n"
      "v_sub_f32 v22, v22, v208\n"
      "v_sub_f32 v23, v23, v208\n"
      "v_exp_f32 v24, v24\n"
      "v_exp_f32 v25, v25\n"
      "v_exp_f32 v26, v26\n"
      "v_exp_f32 v27, v27\n"
      "v_cvt_pk_bf16_f32 v208, v24, v25\n"
      "v_cvt_pk_bf16_f32 v209, v26, v27\n"
      "v_exp_f32 v20, v20\n"
      "v_exp_f32 v21, v21\n"
      "v_exp_f32 v22, v22\n"
      "v_exp_f32 v23, v23\n"
      "v_cvt_pk_bf16_f32 v210, v20, v21\n"
      "v_cvt_pk_bf16_f32 v211, v22, v23\n"
      "v_smfmac_f32_16x16x64_bf16 v[32:35], v[12:15], v[232:239], v5\n"
      "s_add_u32 s56, s48, 4096\n"
      "buffer_store_dwordx2 v[224:225], v2, s[44:47], s56 offen offset:0 nt sc1\n"
      "v_mfma_f32_16x16x32_bf16 v[136:139], v[232:235], v[52:55], v[136:139]\n"
      "buffer_store_dwordx2 v[228:229], v2, s[44:47], s56 offen offset:256 nt sc1\n"
      "v_mfma_f32_16x16x32_bf16 v[136:139], v[236:239], v[56:59], v[136:139]\n"
      "buffer_store_dwordx2 v[226:227], v2, s[44:47], s56 offen offset:512 nt sc1\n"
      "v_mfma_f32_16x16x32_bf16 v[136:139], v[208:211], v[120:123], v[136:139]\n"
      "buffer_store_dwordx2 v[230:231], v2, s[44:47], s56 offen offset:768 nt sc1\n"
      "v_smfmac_f32_16x16x32_bf16 v[32:35], v[12:13], v[208:211], v5\n"
      "s_waitcnt lgkmcnt(0)\n"
      "v_lshlrev_b32 v228, 16, v152\n"
      "v_and_b32 v229, 0xffff0000, v152\n"
      "v_mfma_f32_16x16x32_bf16 v[140:143], v[232:235], v[128:131], v[140:143]\n"
      "v_mov_b64 v[224:225], 0\n"
      "v_lshlrev_b32 v230, 16, v153\n"
      "v_and_b32 v231, 0xffff0000, v153\n"
      "v_mfma_f32_16x16x32_bf16 v[140:143], v[236:239], v[132:135], v[140:143]\n"
      "v_mov_b64 v[20:21], 0\n"
      "v_add_f32 v32, v32, v33\n"
      "v_rcp_f32 v33, v32\n"
      "v_mfma_f32_16x16x32_bf16 v[140:143], v[208:211], v[68:71], v[140:143]\n"
      "ds_bpermute_b32 v34, v4, v33\n"
      "v_mov_b64 v[28:29], 0\n"
      "v_mfma_f32_16x16x32_bf16 v[144:147], v[232:235], v[100:103], v[144:147]\n"
      "v_mov_b64 v[160:161], 0\n"
      "v_mfma_f32_16x16x32_bf16 v[144:147], v[236:239], v[104:107], v[144:147]\n"
      "v_mov_b64 v[24:25], 0\n"
      "v_mfma_f32_16x16x32_bf16 v[144:147], v[208:211], v[36:39], v[144:147]\n"
      "v_mov_b64 v[32:33], 0\n"
      "v_mfma_f32_16x16x32_bf16 v[148:151], v[232:235], v[192:195], v[148:151]\n"
      "v_mov_b64 v[232:233], 0\n"
      "v_mfma_f32_16x16x32_bf16 v[148:151], v[236:239], v[196:199], v[148:151]\n"
      "v_mfma_f32_16x16x32_bf16 v[148:151], v[208:211], v[240:243], v[148:151]\n"
      "s_waitcnt lgkmcnt(0)\n"
      "v_mul_f32_dpp v136, v34, v136 row_newbcast:0 row_mask:0xf bank_mask:0xf\n"
      "v_mul_f32_dpp v137, v34, v137 row_newbcast:4 row_mask:0xf bank_mask:0xf\n"
      "v_mul_f32_dpp v138, v34, v138 row_newbcast:8 row_mask:0xf bank_mask:0xf\n"
      "v_mul_f32_dpp v139, v34, v139 row_newbcast:12 row_mask:0xf bank_mask:0xf\n"
      "v_mul_f32_dpp v140, v34, v140 row_newbcast:0 row_mask:0xf bank_mask:0xf\n"
      "v_mul_f32_dpp v141, v34, v141 row_newbcast:4 row_mask:0xf bank_mask:0xf\n"
      "v_mfma_f32_16x16x32_bf16 v[136:139], v[124:127], a[192:195], v[136:139]\n"
      "v_mul_f32_dpp v142, v34, v142 row_newbcast:8 row_mask:0xf bank_mask:0xf\n"
      "v_mul_f32_dpp v143, v34, v143 row_newbcast:12 row_mask:0xf bank_mask:0xf\n"
      "v_mul_f32_dpp v144, v34, v144 row_newbcast:0 row_mask:0xf bank_mask:0xf\n"
      "v_mfma_f32_16x16x32_bf16 v[136:139], v[48:51], a[196:199], v[136:139]\n"
      "v_mul_f32_dpp v145, v34, v145 row_newbcast:4 row_mask:0xf bank_mask:0xf\n"
      "v_mul_f32_dpp v146, v34, v146 row_newbcast:8 row_mask:0xf bank_mask:0xf\n"
      "v_mul_f32_dpp v147, v34, v147 row_newbcast:12 row_mask:0xf bank_mask:0xf\n"
      "v_mul_f32_dpp v148, v34, v148 row_newbcast:0 row_mask:0xf bank_mask:0xf\n"
      "v_mfma_f32_16x16x32_bf16 v[136:139], v[116:119], a[200:203], v[136:139]\n"
      "v_mul_f32_dpp v149, v34, v149 row_newbcast:4 row_mask:0xf bank_mask:0xf\n"
      "v_mul_f32_dpp v150, v34, v150 row_newbcast:8 row_mask:0xf bank_mask:0xf\n"
      "v_mul_f32_dpp v151, v34, v151 row_newbcast:12 row_mask:0xf bank_mask:0xf\n"
      "v_add_u32 v11, 0x200, v11\n"
      "v_mfma_f32_16x16x32_bf16 v[136:139], v[252:255], a[204:207], v[136:139]\n"
      "v_mfma_f32_16x16x32_bf16 v[140:143], v[124:127], a[208:211], v[140:143]\n"
      "v_mfma_f32_16x16x32_bf16 v[140:143], v[48:51], a[212:215], v[140:143]\n"
      "v_mfma_f32_16x16x32_bf16 v[140:143], v[116:119], a[216:219], v[140:143]\n"
      "v_mfma_f32_16x16x32_bf16 v[140:143], v[252:255], a[220:223], v[140:143]\n"
      "s_add_u32 s56, s48, 0\n"
      "v_mfma_f32_16x16x32_bf16 v[144:147], v[124:127], a[224:227], v[144:147]\n"
      "v_mfma_f32_16x16x32_bf16 v[144:147], v[48:51], a[228:231], v[144:147]\n"
      "v_mfma_f32_16x16x32_bf16 v[144:147], v[116:119], a[232:235], v[144:147]\n"
      "v_mfma_f32_16x16x32_bf16 v[144:147], v[252:255], a[236:239], v[144:147]\n"
      "v_mfma_f32_16x16x32_bf16 v[148:151], v[124:127], a[240:243], v[148:151]\n"
      "v_mfma_f32_16x16x32_bf16 v[148:151], v[48:51], a[244:247], v[148:151]\n"
      "v_mfma_f32_16x16x32_bf16 v[148:151], v[116:119], a[248:251], v[148:151]\n"
      "v_cvt_pk_bf16_f32 v136, v136, v140\n"
      "v_cvt_pk_bf16_f32 v140, v137, v141\n"
      "v_pk_max_i16 v136, v136, 0\n"
      "v_pk_max_i16 v140, v140, 0\n"
      "v_cvt_pk_bf16_f32 v138, v138, v142\n"
      "v_cvt_pk_bf16_f32 v142, v139, v143\n"
      "v_pk_max_i16 v138, v138, 0\n"
      "v_mfma_f32_16x16x32_bf16 v[148:151], v[252:255], a[252:255], v[148:151]\n"
      "v_pk_max_i16 v142, v142, 0\n"
      "v_smfmac_f32_16x16x64_bf16 v[224:227], v[156:159], v[108:115], v5\n"
      "v_smfmac_f32_16x16x64_bf16 v[20:23], v[156:159], v[164:171], v5\n"
      "v_smfmac_f32_16x16x64_bf16 v[28:31], v[156:159], v[40:47], v5\n"
      "v_smfmac_f32_16x16x64_bf16 v[160:163], v[156:159], v[92:99], v5\n"
      "v_smfmac_f32_16x16x64_bf16 v[24:27], v[156:159], v[216:223], v5\n"
      "v_smfmac_f32_16x16x64_bf16 v[32:35], v[156:159], v[244:251], v5\n"
      "v_smfmac_f32_16x16x64_bf16 v[232:235], v[156:159], v[172:179], v5\n"
      "v_cvt_pk_bf16_f32 v137, v144, v148\n"
      "v_cvt_pk_bf16_f32 v141, v145, v149\n"
      "v_add_f32 v224, v224, v225\n"
      "v_add_f32 v20, v20, v21\n"
      "v_add_f32 v28, v28, v29\n"
      "v_add_f32 v160, v160, v161\n"
      "v_add_f32 v24, v24, v25\n"
      "v_add_f32 v32, v32, v33\n"
      "v_add_f32 v232, v232, v233\n"
      "v_max3_f32 v148, v224, v20, v28\n"
      "v_max3_f32 v148, v148, v160, v24\n"
      "v_max3_f32 v148, v148, v32, v232\n"
      "v_mov_b32 v233, 0\n"
      "v_pk_max_i16 v137, v137, 0\n"
      "v_max_f32_dpp v148, v148, v148 row_ror:8 row_mask:0xf bank_mask:0xf\n"
      "v_pk_max_i16 v141, v141, 0\n"
      "v_cvt_pk_bf16_f32 v139, v146, v150\n"
      "v_max_f32_dpp v148, v148, v148 row_ror:4 row_mask:0xf bank_mask:0xf\n"
      "v_cvt_pk_bf16_f32 v143, v147, v151\n"
      "v_pk_max_i16 v139, v139, 0\n"
      "v_max_f32_dpp v148, v148, v148 row_ror:2 row_mask:0xf bank_mask:0xf\n"
      "v_pk_max_i16 v143, v143, 0\n"
      "buffer_store_dwordx2 v[136:137], v2, s[44:47], s56 offen offset:0 nt sc1\n"
      "v_max_f32_dpp v148, v148, v148 row_ror:1 row_mask:0xf bank_mask:0xf\n"
      "v_sub_f32 v232, v232, v148\n"
      "v_sub_f32 v224, v224, v148\n"
      "v_sub_f32 v20, v20, v148\n"
      "v_exp_f32_dpp v233, v232 quad_perm:[0,1,2,3] row_mask:0xf bank_mask:0x1\n"
      "v_sub_f32 v28, v28, v148\n"
      "v_sub_f32 v160, v160, v148\n"
      "v_mov_b32_dpp v232, v233 quad_perm:[1,1,3,3] row_mask:0xf bank_mask:0xf\n"
      "v_cvt_pk_bf16_f32 v232, v233, v232\n"
      "ds_bpermute_b32 v136, v7, v232 offset:248\n"
      "ds_bpermute_b32 v137, v7, v232 offset:8\n"
      "v_sub_f32 v24, v24, v148\n"
      "v_sub_f32 v32, v32, v148\n"
      "v_exp_f32 v224, v224\n"
      "v_exp_f32 v20, v20\n"
      "v_exp_f32 v28, v28\n"
      "v_exp_f32 v160, v160\n"
      "v_exp_f32 v24, v24\n"
      "v_exp_f32 v32, v32\n"
      "v_cndmask_b32_dpp v225, v28, v224, vcc quad_perm:[0,0,2,2] row_mask:0xf bank_mask:0xf\n"
      "v_cndmask_b32_dpp v21, v160, v20, vcc quad_perm:[0,0,2,2] row_mask:0xf bank_mask:0xf\n"
      "v_cndmask_b32_dpp v25, v32, v24, vcc quad_perm:[0,0,2,2] row_mask:0xf bank_mask:0xf\n"
      "s_not_b64 vcc, vcc\n"
      "v_cndmask_b32_dpp v29, v224, v28, vcc quad_perm:[1,1,3,3] row_mask:0xf bank_mask:0xf\n"
      "v_cndmask_b32_dpp v161, v20, v160, vcc quad_perm:[1,1,3,3] row_mask:0xf bank_mask:0xf\n"
      "v_cvt_pk_bf16_f32 v225, v225, v29\n"
      "v_cvt_pk_bf16_f32 v21, v21, v161\n"
      "ds_bpermute_b32 v148, v6, v225\n"
      "ds_bpermute_b32 v149, v6, v21\n"
      "ds_bpermute_b32 v150, v6, v225 offset:16\n"
      "ds_bpermute_b32 v151, v6, v21 offset:16\n"
      "v_cndmask_b32_dpp v33, v24, v32, vcc quad_perm:[1,1,3,3] row_mask:0xf bank_mask:0xf\n"
      "v_cvt_pk_bf16_f32 v25, v25, v33\n"
      "ds_bpermute_b32 v146, v7, v25\n"
      "ds_bpermute_b32 v147, v7, v25 offset:4\n"
      "buffer_store_dwordx2 v[140:141], v2, s[44:47], s56 offen offset:256 nt sc1\n"
      "buffer_store_dwordx2 v[138:139], v2, s[44:47], s56 offen offset:512 nt sc1\n"
      "s_waitcnt lgkmcnt(6)\n"
      "v_mfma_f32_16x16x4_4b_bf16 v[164:179], v[136:137], v[18:19], 0 cbsz:2 abid:0\n"
      "buffer_store_dwordx2 v[142:143], v2, s[44:47], s56 offen offset:768 nt sc1\n"
      "s_not_b64 vcc, vcc\n"
      "v_add_f32 v224, v224, v20\n"
      "v_add_f32 v28, v28, v160\n"
      "v_add_f32 v24, v24, v32\n"
      "v_add_f32 v224, v224, v28\n"
      "v_add_f32 v24, v24, v233\n"
      "v_add_f32 v142, v224, v24\n"
      "s_add_u32 s56, s48, 24576\n"
      "s_nop 0\n"
      "v_add_f32_dpp v142, v142, v142 row_ror:8 row_mask:0xf bank_mask:0xf\n"
      "s_nop 1\n"
      "v_add_f32_dpp v142, v142, v142 row_ror:4 row_mask:0xf bank_mask:0xf\n"
      "s_nop 1\n"
      "v_add_f32_dpp v142, v142, v142 row_ror:2 row_mask:0xf bank_mask:0xf\n"
      "s_waitcnt lgkmcnt(2)\n"
      "v_smfmac_f32_16x16x64_bf16 v[164:167], v[148:151], v[52:59], v5\n"
      "v_smfmac_f32_16x16x64_bf16 v[168:171], v[148:151], v[128:135], v5\n"
      "v_add_f32_dpp v142, v142, v142 row_ror:1 row_mask:0xf bank_mask:0xf\n"
      "v_smfmac_f32_16x16x64_bf16 v[172:175], v[148:151], v[100:107], v5\n"
      "v_smfmac_f32_16x16x64_bf16 v[176:179], v[148:151], v[192:199], v5\n"
      "v_rcp_f32 v143, v142\n"
      "s_waitcnt lgkmcnt(0)\n"
      "v_smfmac_f32_16x16x32_bf16 v[164:167], v[146:147], v[120:123], v5\n"
      "v_smfmac_f32_16x16x32_bf16 v[168:171], v[146:147], v[68:71], v5\n"
      "s_nop 0\n"
      "v_smfmac_f32_16x16x32_bf16 v[172:175], v[146:147], v[36:39], v5\n"
      "v_smfmac_f32_16x16x32_bf16 v[176:179], v[146:147], v[240:243], v5\n"
      "s_nop 3\n"
      "v_add_f32 v164, v164, v165\n"
      "v_fma_f32 v164, v164, v143, v228\n"
      "v_add_f32 v168, v168, v169\n"
      "v_fma_f32 v168, v168, v143, v229\n"
      "v_cvt_pk_bf16_f32 v164, v164, v168\n"
      "v_pk_max_i16 v164, v164, 0\n"
      "v_add_f32 v172, v172, v173\n"
      "v_fma_f32 v172, v172, v143, v230\n"
      "v_add_f32 v176, v176, v177\n"
      "v_fma_f32 v176, v176, v143, v231\n"
      "v_cvt_pk_bf16_f32 v165, v172, v176\n"
      "v_pk_max_i16 v165, v165, 0\n"
      "buffer_store_dwordx2 v[164:165], v3, s[44:47], s56 offen offset:0 nt sc1\n"
      "s_mov_b32 s48, s49\n"
      "s_add_u32 s49, s49, 13107200\n"
      "s_sub_u32 s50, s50, 1\n"
      "s_cmp_eq_u32 s50, 1\n"
      "s_cselect_b32 s49, 0x80000000, s49\n"
      "s_cmp_lg_u32 s50, 0\n"
      "s_cbranch_scc1 L_safe_%=\n"
      "s_waitcnt vmcnt(0) lgkmcnt(0)\n"
      "L_done_%=:\n"
      "s_endpgm\n"
      ".p2align 12\n"
      :
      : [ka] "s"(__builtin_amdgcn_kernarg_segment_ptr()), [g] "s"(blockIdx.x), [wave] "s"(wave), [ldsb] "s"(ldsb)
      : "v0", "v1", "v2", "v3", "v4", "v5", "v6", "v7", "v8", "v9", "v10", "v11", "v12", "v13", "v14", "v15", "v16", "v17", "v18", "v19", "v20", "v21", "v22", "v23", "v24", "v25", "v26", "v27", "v28", "v29", "v30", "v31", "v32", "v33", "v34", "v35", "v36", "v37", "v38", "v39", "v40", "v41", "v42", "v43", "v44", "v45", "v46", "v47", "v48", "v49", "v50", "v51", "v52", "v53", "v54", "v55", "v56", "v57", "v58", "v59", "v60", "v61", "v62", "v63", "v64", "v65", "v66", "v67", "v68", "v69", "v70", "v71", "v72", "v73", "v74", "v75", "v76", "v77", "v78", "v79", "v80", "v81", "v82", "v83", "v84", "v85", "v86", "v87", "v88", "v89", "v90", "v91", "v92", "v93", "v94", "v95", "v96", "v97", "v98", "v99", "v100", "v101", "v102", "v103", "v104", "v105", "v106", "v107", "v108", "v109", "v110", "v111", "v112", "v113", "v114", "v115", "v116", "v117", "v118", "v119", "v120", "v121", "v122", "v123", "v124", "v125", "v126", "v127", "v128", "v129", "v130", "v131", "v132", "v133", "v134", "v135", "v136", "v137", "v138", "v139", "v140", "v141", "v142", "v143", "v144", "v145", "v146", "v147", "v148", "v149", "v150", "v151", "v152", "v153", "v154", "v155", "v156", "v157", "v158", "v159", "v160", "v161", "v162", "v163", "v164", "v165", "v166", "v167", "v168", "v169", "v170", "v171", "v172", "v173", "v174", "v175", "v176", "v177", "v178", "v179", "v180", "v181", "v182", "v183", "v184", "v185", "v186", "v187", "v188", "v189", "v190", "v191", "v192", "v193", "v194", "v195", "v196", "v197", "v198", "v199", "v200", "v201", "v202", "v203", "v204", "v205", "v206", "v207", "v208", "v209", "v210", "v211", "v212", "v213", "v214", "v215", "v216", "v217", "v218", "v219", "v220", "v221", "v222", "v223", "v224", "v225", "v226", "v227", "v228", "v229", "v230", "v231", "v232", "v233", "v234", "v235", "v236", "v237", "v238", "v239", "v240", "v241", "v242", "v243", "v244", "v245", "v246", "v247", "v248", "v249", "v250", "v251", "v252", "v253", "v254", "v255", "a0", "a1", "a2", "a3", "a4", "a5", "a6", "a7", "a8", "a9", "a10", "a11", "a12", "a13", "a14", "a15", "a16", "a17", "a18", "a19", "a20", "a21", "a22", "a23", "a24", "a25", "a26", "a27", "a28", "a29", "a30", "a31", "a32", "a33", "a34", "a35", "a36", "a37", "a38", "a39", "a40", "a41", "a42", "a43", "a44", "a45", "a46", "a47", "a48", "a49", "a50", "a51", "a52", "a53", "a54", "a55", "a56", "a57", "a58", "a59", "a60", "a61", "a62", "a63", "a64", "a65", "a66", "a67", "a68", "a69", "a70", "a71", "a72", "a73", "a74", "a75", "a76", "a77", "a78", "a79", "a80", "a81", "a82", "a83", "a84", "a85", "a86", "a87", "a88", "a89", "a90", "a91", "a92", "a93", "a94", "a95", "a96", "a97", "a98", "a99", "a100", "a101", "a102", "a103", "a104", "a105", "a106", "a107", "a108", "a109", "a110", "a111", "a112", "a113", "a114", "a115", "a116", "a117", "a118", "a119", "a120", "a121", "a122", "a123", "a124", "a125", "a126", "a127", "a128", "a129", "a130", "a131", "a132", "a133", "a134", "a135", "a136", "a137", "a138", "a139", "a140", "a141", "a142", "a143", "a144", "a145", "a146", "a147", "a148", "a149", "a150", "a151", "a152", "a153", "a154", "a155", "a156", "a157", "a158", "a159", "a160", "a161", "a162", "a163", "a164", "a165", "a166", "a167", "a168", "a169", "a170", "a171", "a172", "a173", "a174", "a175", "a176", "a177", "a178", "a179", "a180", "a181", "a182", "a183", "a184", "a185", "a186", "a187", "a188", "a189", "a190", "a191", "a192", "a193", "a194", "a195", "a196", "a197", "a198", "a199", "a200", "a201", "a202", "a203", "a204", "a205", "a206", "a207", "a208", "a209", "a210", "a211", "a212", "a213", "a214", "a215", "a216", "a217", "a218", "a219", "a220", "a221", "a222", "a223", "a224", "a225", "a226", "a227", "a228", "a229", "a230", "a231", "a232", "a233", "a234", "a235", "a236", "a237", "a238", "a239", "a240", "a241", "a242", "a243", "a244", "a245", "a246", "a247", "a248", "a249", "a250", "a251", "a252", "a253", "a254", "a255", "s4", "s5", "s6", "s7", "s8", "s9", "s10", "s11", "s12", "s13", "s14", "s15", "s16", "s17", "s18", "s19", "s20", "s21", "s22", "s23", "s24", "s25", "s26", "s27", "s28", "s29", "s30", "s31", "s32", "s33", "s38", "s39", "s40", "s41", "s42", "s43", "s44", "s45", "s46", "s47", "s48", "s49", "s50", "s51", "s52", "s53", "s54", "s55", "s56", "s57", "s73", "s74", "s75", "s76", "s77", "s78", "s79", "s90", "s91", "s92", "s93", "s94", "s95", "s100", "s101", "s34", "s35", "vcc", "scc", "m0", "memory");
}

extern "C" void kernel_launch(void* const* d_in, const int* in_sizes, int n_in,
                              void* d_out, int out_size, void* d_ws, size_t ws_size,
                              hipStream_t stream) {
  const uint16_t* e = (const uint16_t*)d_in[0];
  const uint16_t* wq = (const uint16_t*)d_in[1];
  const uint16_t* wk = (const uint16_t*)d_in[2];
  const uint16_t* wv = (const uint16_t*)d_in[3];
  const uint16_t* wres = (const uint16_t*)d_in[4];
  uint16_t* out = (uint16_t*)d_out;
  autoint_kernel<<<256, 256, 0, stream>>>(e, wq, wk, wv, wres, out, (uint8_t*)d_ws);
}
```
